```python
import torch

def _softmax(x, axis=-1):
    return torch.softmax(x, dim=axis)

def _gelu(x):
    return torch.nn.functional.gelu(x, approximate='tanh')

def _silu(x):
    return torch.nn.functional.silu(x)

def _sigmoid(x):
    return torch.sigmoid(x)

def _layernorm(x, axis=-1, eps=1e-05):
    m = x.mean(dim=axis, keepdim=True)
    v = x.var(dim=axis, keepdim=True, unbiased=False)
    return (x - m) / torch.sqrt(v + eps)

def _scatter_add(tgt, idx, src, axis=0):
    return tgt.clone().index_add_(axis, idx, src)

def _segment_sum(src, seg, n):
    out = torch.zeros((n,) + src.shape[1:], dtype=src.dtype)
    return out.index_add_(0, seg, src)

def _pad(x, pw):
    flat = tuple((v for pair in reversed(pw) for v in pair))
    return torch.nn.functional.pad(x, flat)
kernel_name = 'adaLN_cond__memory_aug_recurrent_mem_tr__moe_arch_mod__prompt'

def setup_inputs():

    _w = lambda a: (lambda t: t.to(torch.bfloat16) if t.is_floating_point() else t)(torch.as_tensor(a))

    def _A_setup():
        B, S, Dm = (1, 2048, 640)
        H, Dh = (32, 192)
        M, seg = (8, 32)
        seg = min(seg, S)
        S = S // seg * seg
        return {'x': _w((lambda *s: torch.randn(*s, dtype=torch.bfloat16) * 0.05)(B, S, Dm)), 'mem0': _w((lambda *s: torch.randn(*s, dtype=torch.bfloat16) * 0.4)(B, M, Dm)), 'Wq': _w((lambda *s: torch.randn(*s, dtype=torch.bfloat16) * 3.2)(Dm, H, Dh) * Dm ** (-0.5)), 'Wk': _w((lambda *s: torch.randn(*s, dtype=torch.bfloat16) * 3.2)(Dm, H, Dh) * Dm ** (-0.5)), 'Wv': _w((lambda *s: torch.randn(*s, dtype=torch.bfloat16) * 0.4)(Dm, H, Dh) * Dm ** (-0.5)), 'Wo': _w((lambda *s: torch.randn(*s, dtype=torch.bfloat16) * 0.4)(H, Dh, Dm) * (H * Dh) ** (-0.5)), 'seg_len': seg}
    _A_in = _A_setup()

    def _B_setup():
        B, S, D, F, C = (1, 2048, 640, 1536, 8)
        return {'x': _w((lambda *s: torch.randn(*s, dtype=torch.bfloat16) * 0.05)(B, S, D)), 'w_r': _w((lambda *s: torch.randn(*s, dtype=torch.bfloat16) * 0.05)(D)), 'w_gate': _w((lambda *s: torch.randn(*s, dtype=torch.bfloat16) * 0.05)(D, F)), 'w_up': _w((lambda *s: torch.randn(*s, dtype=torch.bfloat16) * 0.05)(D, F)), 'w_down': _w((lambda *s: torch.randn(*s, dtype=torch.bfloat16) * 0.05)(F, D))}
    _B_in = _B_setup()
    return {'A_x': _A_in['x'], 'A_mem0': _A_in['mem0'], 'A_Wq': _A_in['Wq'], 'A_Wk': _A_in['Wk'], 'A_Wv': _A_in['Wv'], 'A_Wo': _A_in['Wo'], 'A_seg_len': _A_in['seg_len'], 'B_x': _B_in['x'], 'B_w_r': _B_in['w_r'], 'B_w_gate': _B_in['w_gate'], 'B_w_up': _B_in['w_up'], 'B_w_down': _B_in['w_down'], 'c': _w((lambda *s: torch.randn(*s, dtype=torch.bfloat16) * 0.05)(1, 640) * 8.0), 'w_mod': _w((lambda *s: torch.randn(*s, dtype=torch.bfloat16) * 0.05)(640, 6 * 640) * 640 ** (-0.5) * 8.0)}

def reference(A_x, A_mem0, A_Wq, A_Wk, A_Wv, A_Wo, A_seg_len, B_x, B_w_r, B_w_gate, B_w_up, B_w_down, c, w_mod):

    def _A_ref(x, mem0, Wq, Wk, Wv, Wo, seg_len):
        B, S, Dm = x.shape
        H, Dh = (Wq.shape[1], Wq.shape[2])
        M = mem0.shape[1]
        scale = Dh ** (-0.5)
        n_seg = S // seg_len
        L = M + seg_len
        idx = torch.arange(L)
        mask = (idx[:, None] < M) | (idx[:, None] >= idx[None, :])
        mem = mem0
        outs = []
        for s in range(n_seg):
            xs = x[:, s * seg_len:(s + 1) * seg_len, :].clone()
            h = torch.concatenate([mem, xs], axis=1)
            q = torch.einsum('btd,dhe->bhte', h, Wq)
            k = torch.einsum('btd,dhe->bhte', h, Wk)
            v = torch.einsum('btd,dhe->bhte', h, Wv)
            scores = torch.einsum('bhqe,bhke->bhqk', q, k) * scale
            scores = torch.where(mask[None, None], scores, -1000000000.0)
            p = _softmax(scores, axis=-1)
            o = torch.einsum('bhqk,bhke->bhqe', p, v)
            o = torch.einsum('bhte,hed->btd', o, Wo)
            mem = o[:, :M, :].clone()
            outs.append(o[:, M:, :])
        return torch.concatenate(outs, axis=1)

    def _B_ref(x, w_r, w_gate, w_up, w_down):
        B, S, D = x.shape
        C = 8
        r = x @ w_r
        idx = torch.argsort(-r, axis=-1)[:, :C]
        idx_e = (lambda x, n, axis: torch.repeat_interleave(x, n, dim=axis))(idx.reshape(B, C, 1), D, axis=2)
        x_sel = (lambda x, i, axis: torch.take_along_dim(x, i, dim=axis))(x, idx_e, axis=1)
        g = _silu(x_sel @ w_gate)
        u = x_sel @ w_up
        y = g * u @ w_down
        r_sel = _sigmoid((lambda x, i, axis: torch.take_along_dim(x, i, dim=axis))(r, idx, axis=1))
        y = y * r_sel.reshape(B, C, 1)
        b_off = (torch.arange(B) * S).reshape(B, 1)
        flat_idx = (idx + b_off).reshape(B * C)
        zero = (x * 0.0).reshape(B * S, D)
        upd = _scatter_add(zero, flat_idx, y.reshape(B * C, D), axis=0)
        return x + upd.reshape(B, S, D)
    x = A_x
    mod = (_silu(c) @ w_mod).reshape(c.shape[0], 6, 1, -1)
    sa, ca, ga, sb, cb, gb = (mod[:, 0].clone(), mod[:, 1].clone(), mod[:, 2].clone(), mod[:, 3].clone(), mod[:, 4].clone(), mod[:, 5].clone())
    h = _layernorm(x, axis=-1) * (1 + ca) + sa
    x = x + ga * _A_ref(h, A_mem0, A_Wq, A_Wk, A_Wv, A_Wo, A_seg_len)
    h = _layernorm(x, axis=-1) * (1 + cb) + sb
    x = x + gb * _B_ref(h, B_w_r, B_w_gate, B_w_up, B_w_down)
    return x
```

```cpp
#include <hip/hip_runtime.h>
#include <hip/hip_bf16.h>
#include <stdint.h>
#include <type_traits>

#define GAS __attribute__((address_space(1)))
#define LAS __attribute__((address_space(3)))
#define RLX_AGENT __ATOMIC_RELAXED, __HIP_MEMORY_SCOPE_AGENT
typedef unsigned short u16;
typedef unsigned v4u __attribute__((ext_vector_type(4)));
typedef unsigned v2u __attribute__((ext_vector_type(2)));
typedef unsigned v8u __attribute__((ext_vector_type(8)));
typedef float f4 __attribute__((ext_vector_type(4)));
typedef __bf16 bf2 __attribute__((ext_vector_type(2)));
typedef __bf16 bf8 __attribute__((ext_vector_type(8)));
typedef __bf16 bf16v __attribute__((ext_vector_type(16)));
typedef short s4 __attribute__((ext_vector_type(4)));
typedef GAS unsigned gu32;
typedef __amdgpu_buffer_rsrc_t rsrc_t;

constexpr int S = 2048, DM = 640, NH = 32, DH = 192, HD = NH * DH, NSEG = 64, FF = 1536, TOPK = 8;

constexpr size_t MB = 1u << 20;
constexpr int RING = 32, HRING = 18, USLOT = 12, HB = 64, PB = 67, LB = 73;
__host__ __device__ constexpr int slot_blk(int s) { return 2 * (s % RING); }
constexpr int NCAL = LB, NBLK = LB + 6;
constexpr unsigned WS_BYTES = (unsigned)NBLK << 21;
constexpr int CTRL_OFF = LB << 21;
constexpr int CTRL_BYTES = 131072;
constexpr int MODB_OFF = CTRL_OFF + CTRL_BYTES;
constexpr int HF_OFF = MODB_OFF + 8192;
constexpr int HF_BYTES = S * DM * 2;
constexpr int X1_OFF = HF_OFF + HF_BYTES;
constexpr int R_OFF = X1_OFF + 2 * HF_BYTES;
constexpr int ACT_OFF = R_OFF + S * 4;
constexpr int WS_END = ACT_OFF + TOPK * FF * 2;
constexpr int TRACE_OFF = (LB + 5) << 21;
static_assert(WS_END <= TRACE_OFF, "workspace");
constexpr int H_COPY = 40960, H_STEP = 4 * H_COPY, H_FLAG = 10240, H_NOTE = 14336, H_PLAN = 15360, H_E = 16384, H_P = 32768, KQ_OFF = 262144, PT_SEG = 81920;

constexpr int C_TICK = 256;
constexpr int C_ANCH = 768;
constexpr int C_ERR = 1088;
constexpr int C_FIN = 1152;
constexpr int C_IDX = 1280;
constexpr int C_U = 2048;
constexpr int C_LN = 3072;
constexpr int C_STAT = 3584;
constexpr int C_PT = 8192;
constexpr int C_Z = 8448;
constexpr int C_G = 8704;
constexpr int C_G0 = 9600;
constexpr int C_Q = 9728;
constexpr int C_AF = 12288;
constexpr int C_RD = 14336;
constexpr int C_VF = 16384;
constexpr int C_ZERO = 17408;
constexpr int C_DBG = 24576;
__host__ __device__ constexpr int lmap(int off) { return ((off >> 13) << 14) | (off & 8191); }
constexpr int OOB = 0x40000000;

__device__ __forceinline__ float u2f(unsigned v) { return __builtin_bit_cast(float, v); }
__device__ __forceinline__ unsigned f2u(float v) { return __builtin_bit_cast(unsigned, v); }
__device__ __forceinline__ float bf2f(u16 v) { return __builtin_bit_cast(float, (unsigned)v << 16); }
__device__ __forceinline__ float lo2f(unsigned v) { return __builtin_bit_cast(float, v << 16); }
__device__ __forceinline__ float hi2f(unsigned v) { return __builtin_bit_cast(float, v & 0xffff0000u); }
__device__ __forceinline__ unsigned pack2(float a, float b) { bf2 v = {(__bf16)a, (__bf16)b}; return __builtin_bit_cast(unsigned, v); }
__device__ __forceinline__ float rbf(float a) { return (float)(__bf16)a; }
__device__ __forceinline__ unsigned long long uni64(unsigned long long a) {
  return (unsigned)__builtin_amdgcn_readfirstlane((unsigned)a) | (unsigned long long)(unsigned)__builtin_amdgcn_readfirstlane((unsigned)(a >> 32)) << 32;
}
__device__ __forceinline__ rsrc_t mkrsrc(const void* p, unsigned bytes) { return __builtin_amdgcn_make_buffer_rsrc((void*)uni64((unsigned long long)p), 0, bytes, 0x00020000); }
__device__ __forceinline__ unsigned xcc_id() { return (unsigned)__builtin_amdgcn_s_getreg(20 | (3 << 11)) & 0xfu; }
__device__ __forceinline__ int rfl(int v) { return __builtin_amdgcn_readfirstlane(v); }
__device__ __forceinline__ f4 mfma32(v4u a, v4u b, f4 c) { return __builtin_amdgcn_mfma_f32_16x16x32_bf16(__builtin_bit_cast(bf8, a), __builtin_bit_cast(bf8, b), c, 0, 0, 0); }
__device__ __forceinline__ f4 mfma16(v2u a, v2u b, f4 c) { return mfma32(v4u{a[0], a[1], 0u, 0u}, v4u{b[0], b[1], 0u, 0u}, c); }
__device__ __forceinline__ f4 smfma(v4u a, v8u b, f4 c, int idx) { return __builtin_amdgcn_smfmac_f32_16x16x64_bf16(__builtin_bit_cast(bf8, a), __builtin_bit_cast(bf16v, b), c, idx, 0, 0); }
__device__ __forceinline__ v2u trread(unsigned addr) { return __builtin_bit_cast(v2u, __builtin_amdgcn_ds_read_tr16_b64_v4i16((LAS s4*)addr)); }
__device__ __forceinline__ v4u lds128(unsigned addr) { return *(LAS v4u*)addr; }
__device__ __forceinline__ v2u lds64(unsigned addr) { return *(LAS v2u*)addr; }
__device__ __forceinline__ void dma16(rsrc_t r, int voff, int soff, unsigned ldsaddr) {
  asm volatile("s_mov_b32 m0, %0\n\ts_nop 3\n\tbuffer_load_dwordx4 %1, %2, %3 offen lds" ::"s"(ldsaddr), "v"(voff), "s"(r), "s"(soff) : "memory");
}
__device__ __forceinline__ void dma16_sc1(rsrc_t r, int voff, int soff, unsigned ldsaddr) {
  asm volatile("s_mov_b32 m0, %0\n\ts_nop 3\n\tbuffer_load_dwordx4 %1, %2, %3 offen sc1 lds" ::"s"(ldsaddr), "v"(voff), "s"(r), "s"(soff) : "memory");
}
#define VMCNT(n) asm volatile("s_waitcnt vmcnt(" #n ")" ::: "memory")
#define LGKM0() asm volatile("s_waitcnt lgkmcnt(0)" ::: "memory")
#define BAR() asm volatile("s_barrier" ::: "memory")
__device__ __forceinline__ float swapsum32(float a, float b) {
  auto r = __builtin_amdgcn_permlane32_swap(__builtin_bit_cast(unsigned, a), __builtin_bit_cast(unsigned, b), false, false);
  return u2f(r[0]) + u2f(r[1]);
}
__device__ __forceinline__ float swapsum16(float a, float b) {
  auto r = __builtin_amdgcn_permlane16_swap(__builtin_bit_cast(unsigned, a), __builtin_bit_cast(unsigned, b), false, false);
  return u2f(r[0]) + u2f(r[1]);
}
__device__ __forceinline__ float max_x32(float a) {
  auto r = __builtin_amdgcn_permlane32_swap(__builtin_bit_cast(unsigned, a), __builtin_bit_cast(unsigned, a), false, false);
  return fmaxf(u2f(r[0]), u2f(r[1]));
}
__device__ __forceinline__ float max_x16(float a) {
  auto r = __builtin_amdgcn_permlane16_swap(__builtin_bit_cast(unsigned, a), __builtin_bit_cast(unsigned, a), false, false);
  return fmaxf(u2f(r[0]), u2f(r[1]));
}
__device__ __forceinline__ unsigned ror8u(unsigned v) { return (unsigned)__builtin_amdgcn_update_dpp(0, (int)v, 0x128, 0xf, 0xf, true); }
__device__ __forceinline__ float ror8f(float v) { return __builtin_bit_cast(float, ror8u(__builtin_bit_cast(unsigned, v))); }
__device__ __forceinline__ float wave_sum(float v) {
  v += u2f(__builtin_amdgcn_update_dpp(0, (int)f2u(v), 0xb1, 0xf, 0xf, true)); v += u2f(__builtin_amdgcn_update_dpp(0, (int)f2u(v), 0x4e, 0xf, 0xf, true));
  v += u2f(__builtin_amdgcn_update_dpp(0, (int)f2u(v), 0x124, 0xf, 0xf, true)); v += ror8f(v);
  v = swapsum16(v, v);
  return swapsum32(v, v);
}
typedef unsigned short us2 __attribute__((ext_vector_type(2)));
__device__ __forceinline__ unsigned unsent(unsigned w) { return __builtin_bit_cast(unsigned, __builtin_elementwise_min(__builtin_bit_cast(us2, w), us2{0xfffe, 0xfffe})); }

struct Params {
  const u16 *x, *mem0, *wq, *wk, *wv, *wo, *wr, *wg, *wu, *wd, *c, *wmod;
  u16* out;
  unsigned char* ws;
  int dE, dL, dM, dZ, dS, lead;
};

constexpr int PRE_FILL = 20;
constexpr int PRE_GRID = NCAL + PRE_FILL + 120;
__global__ void __launch_bounds__(256) k_pre(Params p) {
  const int tid = threadIdx.x, b = blockIdx.x;
  if (b < NCAL) {
    if (tid < 64) {
      rsrc_t r = mkrsrc(p.ws, WS_BYTES);
      unsigned best[2] = {1u << 30, 1u << 30};
#pragma unroll 1
      for (int rep = 0; rep < 6; ++rep) {
        const int off = tid < 8 ? (b << 21) + 0x1FC000 + (rep & 1) * 8192 + tid * 16 : OOB;
        const unsigned long long t1 = __builtin_amdgcn_s_memtime();
        LGKM0();
        __builtin_amdgcn_raw_buffer_store_b128(v4u{1u, 2u, 3u, 4u}, r, off, 0, 16);
        VMCNT(0);
        const unsigned d = (unsigned)(__builtin_amdgcn_s_memtime() - t1);
        if (rep & 1) best[1] = min(best[1], d); else best[0] = min(best[0], d);
      }
      if (tid == 0) p.ws[CTRL_OFF + b] = (best[1] < best[0] ? 1u : 0u) ^ (xcc_id() >> 2 & 1);
    }
    return;
  }
  if (b < NCAL + PRE_FILL) {
    const int st = (b - NCAL) >> 2, i = (b - NCAL) * 256 + tid;
    const v4u z = {0u, 0u, 0u, 0u}, m = {~0u, ~0u, ~0u, ~0u};
    if (i >= 16 && i < C_ZERO / 4) *(v4u*)(p.ws + CTRL_OFF + i * 16) = z;
    if (i < S / 4 + TOPK * FF / 8) *(v4u*)(p.ws + R_OFF + i * 16) = m;
    const rsrc_t r = mkrsrc(p.ws, WS_BYTES);
#pragma unroll
    for (int k = 0; k < 10; ++k) {
      const int o = k * 256 + tid;
      v4u val = m;
      if (st == 0 && o < 640) {
        const v4u x = *(const v4u*)(p.mem0 + (o & 7) * DM + (o >> 3) * 8);
        val = v4u{unsent(x[0]), unsent(x[1]), unsent(x[2]), unsent(x[3])};
      }
      if (o >= 640 && o < 896) val = z;
      if (st > 0) continue;
      const int mo = ((HB + st / 6) << 21) + lmap((st % 6) * H_STEP + (b - NCAL & 3) * H_COPY + o * 16);
      __builtin_amdgcn_raw_buffer_store_b128(val, r, mo, 0, 16); __builtin_amdgcn_raw_buffer_store_b128(val, r, mo + 8192, 0, 16);
    }
    return;
  }
  __shared__ float red[64][33];
  const int col0 = (b - NCAL - PRE_FILL) * 32, cg = tid & 3, rg = tid >> 2;
  v4u w[10]; u16 cv[10];
#pragma unroll
  for (int i = 0; i < 10; ++i) { w[i] = *(const v4u*)(p.wmod + (size_t)(rg + 64 * i) * 3840 + col0 + cg * 8); cv[i] = p.c[rg + 64 * i]; }
  __builtin_amdgcn_sched_barrier(0);
  float acc[8] = {};
#pragma unroll
  for (int i = 0; i < 10; ++i) {
    const float c = bf2f(cv[i]), sv = rbf(c / (1.f + __expf(-c)));
#pragma unroll
    for (int k = 0; k < 4; ++k) { acc[2 * k] += sv * lo2f(w[i][k]); acc[2 * k + 1] += sv * hi2f(w[i][k]); }
  }
#pragma unroll
  for (int k = 0; k < 8; ++k) red[rg][cg * 8 + k] = acc[k];
  __syncthreads();
  const int c = tid & 31, part = tid >> 5;
  float sum = 0.f;
#pragma unroll
  for (int i = 0; i < 8; ++i) sum += red[part * 8 + i][c];
  __syncthreads();
  red[part][c] = sum;
  __syncthreads();
  if (tid < 32) {
    sum = 0.f;
#pragma unroll
    for (int i = 0; i < 8; ++i) sum += red[i][tid];
    ((u16*)(p.ws + MODB_OFF))[col0 + tid] = (u16)pack2((col0 / DM) % 3 == 1 ? 1.f + rbf(sum) : sum, 0.f);
  }
}

#define XSTART 64
#define MEET 3
#define AHEAD 8
static_assert(AHEAD + 9 <= HRING, "hand-off ring");
#define SDELAY 0
#define KDELAY 0
#define GPOLL 8
#define KQ_DELAY -100
constexpr int MAIN_LDS = 163840;
constexpr int SPIN_CAP = 1 << 18;
typedef unsigned v12u __attribute__((ext_vector_type(12)));
typedef unsigned v16u __attribute__((ext_vector_type(16)));
typedef unsigned v32u __attribute__((ext_vector_type(32)));

#define SROLE_CLOBBERS \
  "v32", "v33", "v34", "v35", "v36", "v37", "v38", "v39", "v40", "v41", "v42", "v43", "v44", "v45", "v46", "v47", "v48", "v49", "v50", "v51", "v52", \
  "v53", "v54", "v55", "v56", "v57", "v58", "v59", "v60", "v61", "v62", "v63", "v64", "v65", "v66", "v67", "v68", "v69", "v70", "v71", "v72", "v73", \
  "v74", "v75", "v76", "v77", "v78", "v79", "v80", "v81", "v82", "v83", "v84", "v85", "v86", "v87", "v88", "v89", "v90", "v91", "v92", "v93", "v94", \
  "v95", "v96", "v97", "v98", "v99", "v100", "v101", "v102", "v103", "v104", "v105", "v106", "v107", "v108", "v109", "v110", "v111", "v112", "v113", \
  "v114", "v115", "v116", "v117", "v118", "v119", "v120", "v121", "v122", "v123", "v124", "v125", "v126", "v127", "v128", "v129", "v130", "v131", \
  "v132", "v133", "v134", "v135", "v136", "v137", "v138", "v139", "v140", "v141", "v142", "v143", "v144", "v145", "v146", "v147", "v148", "v149", \
  "v150", "v151", "v152", "v153", "v154", "v155", "v156", "v157", "v158", "v159", "v160", "v161", "v162", "v163", "v164", "v165", "v166", "v167", \
  "v168", "v169", "v170", "v171", "v172", "v173", "v174", "v175", "v176", "v177", "v178", "v179", "v180", "v181", "v182", "v183", "v184", "v185", \
  "v186", "v187", "v188", "v189", "v190", "v191", "v192", "v193", "v194", "v195", "v196", "v197", "v198", "v199", "v200", "v201", "v202", "v203", \
  "v204", "v205", "v206", "v207", "v208", "v209", "v210", "v211", "v212", "v213", "v214", "v215", "v216", "v217", "v218", "v219", "v220", "v221", \
  "v222", "v223", "v224", "v225", "v226", "v227", "v228", "v229", "v230", "v231", "v232", "v233", "v234", "v235", "v236", "v237", "v238", "v239", \
  "v240", "v241", "v242", "v243", "v244", "v245", "v246", "v247", "v248", "v249", "v250", "v251", "v252", "v253", "v254", "v255", "s34", "s35", "s48", \
  "s49", "s50", "s51", "s68", "s69", "s70", "s71", "s72", "s73", "s74", "s75", "s76", "s77", "s78", "s79", "s80", "s81", "s82", "s83", "s84", "s85", \
  "s86", "s87", "s88", "s89", "s90", "s93", "s94", "s95", "s96", "a0", "a1", "a2", "a3", "a4", "a5", "a6", "a7", "a8", "a9", "a10", "a11", "a12", "a13", \
  "a14", "a15", "a16", "a17", "a18", "a19", "a20", "a21", "a22", "a23", "a24", "a25", "a26", "a27", "a28", "a29", "a30", "a31", "a32", "a33", "a34", \
  "a35", "a36", "a37", "a38", "a39", "a40", "a41", "a42", "a43", "a44", "a45", "a46", "a47", "a48", "a49", "a50", "a51", "a52", "a53", "a54", "a55", \
  "a56", "a57", "a58", "a59", "a60", "a61", "a62", "a63", "a64", "a65", "a66", "a67", "a68", "a69", "a70", "a71", "a72", "a73", "a74", "a75", "a76", \
  "a77", "a78", "a79", "a80", "a81", "a82", "a83", "a84", "a85", "a86", "a87", "a88", "a89", "a90", "a91", "a92", "a93", "a94", "a95", "a96", "a97", \
  "a98", "a99", "a100", "a101", "a102", "a103", "a104", "a105", "a106", "a107", "a108", "a109", "a110", "a111", "a112", "a113", "a114", "a115", "a116", \
  "a117", "a118", "a119", "a120", "a121", "a122", "a123", "a124", "a125", "a126", "a127", "a128", "a129", "a130", "a131", "a132", "a133", "a134", \
  "a135", "a136", "a137", "a138", "a139", "a140", "a141", "a142", "a143", "a144", "a145", "a146", "a147", "a148", "a149", "a150", "a151", "a152", \
  "a153", "a154", "a155", "a156", "a157", "a158", "a159", "a160", "a161", "a162", "a163", "a164", "a165", "a166", "a167", "a168", "a169", "a170", \
  "a171", "a172", "a173", "a174", "a175", "a176", "a177", "a178", "a179", "a180", "a181", "a182", "a183", "a184", "a185", "a186", "a187", "a188", \
  "a189", "a190", "a191", "a192", "a193", "a194", "a195", "a196", "a197", "a198", "a199", "a200", "a201", "a202", "a203", "a204", "a205", "a206", \
  "a207", "a208", "a209", "a210", "a211", "a212", "a213", "a214", "a215", "a216", "a217", "a218", "a219", "a220", "a221", "a222", "a223", "a224", \
  "a225", "a226", "a227", "a228", "a229", "a230", "a231", "a232", "a233", "a234", "a235", "a236", "a237", "a238", "a239", "a240", "a241", "a242", \
  "a243", "a244", "a245", "a246", "a247", "a248", "a249", "a250", "a251", "a252", "a253", "a254", "a255"
#define SROLE_ASM \
  "s_mov_b32 s34, 0x00ff00ff\n" \
  "s_mov_b32 s35, 0x00ff00ff\n" \
  "s_mov_b32 s68, 0\n" \
  "s_mov_b32 s73, 0\n" \
  "s_mov_b32 s56, 0\n" \
  "s_mov_b32 s57, 0\n" \
  "s_mov_b32 s51, 0\n" \
  "s_mov_b32 s71, 0\n" \
  "s_mov_b32 s50, 5900\n" \
  "v_mov_b32 v4, 0x3dd53b94\n" \
  "s_mov_b32 s93, s58\n" \
  "s_mov_b32 s94, s59\n" \
  "s_mov_b32 s95, s60\n" \
  "s_mov_b32 s96, s61\n" \
  "s_mov_b32 s58, 0\n" \
  "s_mov_b32 s59, 0\n" \
  "s_mov_b32 s60, 0\n" \
  "s_mov_b32 s61, 0\n" \
  "s_mov_b32 s62, 0\n" \
  "s_mov_b32 s63, 0\n" \
  "s_mov_b32 s64, 0\n" \
  "s_mov_b32 s65, 0\n" \
  "s_mov_b32 s66, 0\n" \
  "s_mov_b32 s67, 0\n" \
  "s_memrealtime s[76:77]\n" \
  "s_memtime s[78:79]\n" \
  "s_waitcnt lgkmcnt(0)\n" \
  "s_mov_b32 s88, s76\n" \
  "s_mov_b32 s89, s78\n" \
  "s_mul_i32 s72, s52, 6144\n" \
  "s_add_u32 m0, s72, 0\n" \
  "s_mov_b32 s74, 0\n" \
  "buffer_load_dwordx4 v24, s[40:43], s74 offen offset:0 lds\n" \
  "buffer_load_dwordx4 v25, s[40:43], s74 offen offset:1024 lds\n" \
  "buffer_load_dwordx4 v26, s[40:43], s74 offen offset:2048 lds\n" \
  "s_add_u32 m0, s72, 3072\n" \
  "s_mov_b32 s74, 98304\n" \
  "buffer_load_dwordx4 v24, s[40:43], s74 offen offset:0 lds\n" \
  "buffer_load_dwordx4 v25, s[40:43], s74 offen offset:1024 lds\n" \
  "buffer_load_dwordx4 v26, s[40:43], s74 offen offset:2048 lds\n" \
  "s_add_u32 m0, s72, 24576\n" \
  "s_mov_b32 s74, 0\n" \
  "buffer_load_dwordx4 v24, s[44:47], s74 offen offset:0 lds\n" \
  "buffer_load_dwordx4 v25, s[44:47], s74 offen offset:1024 lds\n" \
  "buffer_load_dwordx4 v26, s[44:47], s74 offen offset:2048 lds\n" \
  "s_add_u32 m0, s72, 27648\n" \
  "s_mov_b32 s74, 98304\n" \
  "buffer_load_dwordx4 v24, s[44:47], s74 offen offset:0 lds\n" \
  "buffer_load_dwordx4 v25, s[44:47], s74 offen offset:1024 lds\n" \
  "buffer_load_dwordx4 v26, s[44:47], s74 offen offset:2048 lds\n" \
  "s_add_u32 m0, s72, 49152\n" \
  "s_mov_b32 s74, 786432\n" \
  "buffer_load_dwordx4 v24, s[40:43], s74 offen offset:0 lds\n" \
  "buffer_load_dwordx4 v25, s[40:43], s74 offen offset:1024 lds\n" \
  "buffer_load_dwordx4 v26, s[40:43], s74 offen offset:2048 lds\n" \
  "s_add_u32 m0, s72, 52224\n" \
  "s_mov_b32 s74, 884736\n" \
  "buffer_load_dwordx4 v24, s[40:43], s74 offen offset:0 lds\n" \
  "buffer_load_dwordx4 v25, s[40:43], s74 offen offset:1024 lds\n" \
  "buffer_load_dwordx4 v26, s[40:43], s74 offen offset:2048 lds\n" \
  "s_add_u32 m0, s72, 73728\n" \
  "s_mov_b32 s74, 786432\n" \
  "buffer_load_dwordx4 v24, s[44:47], s74 offen offset:0 lds\n" \
  "buffer_load_dwordx4 v25, s[44:47], s74 offen offset:1024 lds\n" \
  "buffer_load_dwordx4 v26, s[44:47], s74 offen offset:2048 lds\n" \
  "s_add_u32 m0, s72, 76800\n" \
  "s_mov_b32 s74, 884736\n" \
  "buffer_load_dwordx4 v24, s[44:47], s74 offen offset:0 lds\n" \
  "buffer_load_dwordx4 v25, s[44:47], s74 offen offset:1024 lds\n" \
  "buffer_load_dwordx4 v26, s[44:47], s74 offen offset:2048 lds\n" \
  "s_add_u32 m0, s72, 98304\n" \
  "s_mov_b32 s74, 1572864\n" \
  "buffer_load_dwordx4 v24, s[40:43], s74 offen offset:0 lds\n" \
  "buffer_load_dwordx4 v25, s[40:43], s74 offen offset:1024 lds\n" \
  "buffer_load_dwordx4 v26, s[40:43], s74 offen offset:2048 lds\n" \
  "s_add_u32 m0, s72, 101376\n" \
  "s_mov_b32 s74, 1671168\n" \
  "buffer_load_dwordx4 v24, s[40:43], s74 offen offset:0 lds\n" \
  "buffer_load_dwordx4 v25, s[40:43], s74 offen offset:1024 lds\n" \
  "buffer_load_dwordx4 v26, s[40:43], s74 offen offset:2048 lds\n" \
  "v_add_u32 v31, 73728, v27\n" \
  "v_add_u32 v23, 73728, v30\n" \
  "s_waitcnt vmcnt(24) lgkmcnt(0)\n" \
  "s_barrier\n" \
  "s_add_u32 m0, s72, 122880\n" \
  "s_mov_b32 s74, 1572864\n" \
  "buffer_load_dwordx4 v24, s[44:47], s74 offen offset:0 lds\n" \
  "buffer_load_dwordx4 v25, s[44:47], s74 offen offset:1024 lds\n" \
  "buffer_load_dwordx4 v26, s[44:47], s74 offen offset:2048 lds\n" \
  "s_add_u32 m0, s72, 125952\n" \
  "s_mov_b32 s74, 1671168\n" \
  "buffer_load_dwordx4 v24, s[44:47], s74 offen offset:0 lds\n" \
  "buffer_load_dwordx4 v25, s[44:47], s74 offen offset:1024 lds\n" \
  "buffer_load_dwordx4 v26, s[44:47], s74 offen offset:2048 lds\n" \
  "ds_read_b64_tr_b16 v[32:33], v27 offset:0\n" \
  "ds_read_b64_tr_b16 v[34:35], v27 offset:768\n" \
  "ds_read_b64_tr_b16 v[36:37], v27 offset:12288\n" \
  "ds_read_b64_tr_b16 v[38:39], v27 offset:13056\n" \
  "ds_read_b64_tr_b16 v[40:41], v27 offset:8\n" \
  "ds_read_b64_tr_b16 v[42:43], v27 offset:776\n" \
  "ds_read_b64_tr_b16 v[44:45], v27 offset:12296\n" \
  "ds_read_b64_tr_b16 v[46:47], v27 offset:13064\n" \
  "ds_read_b64_tr_b16 v[48:49], v30 offset:0\n" \
  "ds_read_b64_tr_b16 v[50:51], v30 offset:768\n" \
  "ds_read_b64_tr_b16 v[52:53], v30 offset:12288\n" \
  "ds_read_b64_tr_b16 v[54:55], v30 offset:13056\n" \
  "s_waitcnt vmcnt(24) lgkmcnt(0)\n" \
  "s_barrier\n" \
  "s_add_u32 m0, s72, 0\n" \
  "s_mov_b32 s74, 2359296\n" \
  "buffer_load_dwordx4 v24, s[40:43], s74 offen offset:0 lds\n" \
  "buffer_load_dwordx4 v25, s[40:43], s74 offen offset:1024 lds\n" \
  "buffer_load_dwordx4 v26, s[40:43], s74 offen offset:2048 lds\n" \
  "s_add_u32 m0, s72, 3072\n" \
  "s_mov_b32 s74, 2457600\n" \
  "buffer_load_dwordx4 v24, s[40:43], s74 offen offset:0 lds\n" \
  "buffer_load_dwordx4 v25, s[40:43], s74 offen offset:1024 lds\n" \
  "buffer_load_dwordx4 v26, s[40:43], s74 offen offset:2048 lds\n" \
  "ds_read_b64_tr_b16 v[56:57], v27 offset:24576\n" \
  "ds_read_b64_tr_b16 v[58:59], v27 offset:25344\n" \
  "ds_read_b64_tr_b16 v[60:61], v27 offset:36864\n" \
  "ds_read_b64_tr_b16 v[62:63], v27 offset:37632\n" \
  "ds_read_b64_tr_b16 a[0:1], v27 offset:24584\n" \
  "ds_read_b64_tr_b16 a[2:3], v27 offset:25352\n" \
  "ds_read_b64_tr_b16 a[4:5], v27 offset:36872\n" \
  "ds_read_b64_tr_b16 a[6:7], v27 offset:37640\n" \
  "ds_read_b64_tr_b16 a[8:9], v30 offset:24576\n" \
  "ds_read_b64_tr_b16 a[10:11], v30 offset:25344\n" \
  "ds_read_b64_tr_b16 a[12:13], v30 offset:36864\n" \
  "ds_read_b64_tr_b16 a[14:15], v30 offset:37632\n" \
  "s_waitcnt vmcnt(24) lgkmcnt(0)\n" \
  "s_barrier\n" \
  "s_add_u32 m0, s72, 24576\n" \
  "s_mov_b32 s74, 2359296\n" \
  "buffer_load_dwordx4 v24, s[44:47], s74 offen offset:0 lds\n" \
  "buffer_load_dwordx4 v25, s[44:47], s74 offen offset:1024 lds\n" \
  "buffer_load_dwordx4 v26, s[44:47], s74 offen offset:2048 lds\n" \
  "s_add_u32 m0, s72, 27648\n" \
  "s_mov_b32 s74, 2457600\n" \
  "buffer_load_dwordx4 v24, s[44:47], s74 offen offset:0 lds\n" \
  "buffer_load_dwordx4 v25, s[44:47], s74 offen offset:1024 lds\n" \
  "buffer_load_dwordx4 v26, s[44:47], s74 offen offset:2048 lds\n" \
  "ds_read_b64_tr_b16 a[16:17], v27 offset:49152\n" \
  "ds_read_b64_tr_b16 a[18:19], v27 offset:49920\n" \
  "ds_read_b64_tr_b16 a[20:21], v27 offset:61440\n" \
  "ds_read_b64_tr_b16 a[22:23], v27 offset:62208\n" \
  "ds_read_b64_tr_b16 a[24:25], v27 offset:49160\n" \
  "ds_read_b64_tr_b16 a[26:27], v27 offset:49928\n" \
  "ds_read_b64_tr_b16 a[28:29], v27 offset:61448\n" \
  "ds_read_b64_tr_b16 a[30:31], v27 offset:62216\n" \
  "ds_read_b64_tr_b16 v[64:65], v30 offset:49152\n" \
  "ds_read_b64_tr_b16 v[66:67], v30 offset:49920\n" \
  "ds_read_b64_tr_b16 v[68:69], v30 offset:61440\n" \
  "ds_read_b64_tr_b16 v[70:71], v30 offset:62208\n" \
  "s_waitcnt vmcnt(24) lgkmcnt(0)\n" \
  "s_barrier\n" \
  "s_add_u32 m0, s72, 49152\n" \
  "s_mov_b32 s74, 3145728\n" \
  "buffer_load_dwordx4 v24, s[40:43], s74 offen offset:0 lds\n" \
  "buffer_load_dwordx4 v25, s[40:43], s74 offen offset:1024 lds\n" \
  "buffer_load_dwordx4 v26, s[40:43], s74 offen offset:2048 lds\n" \
  "s_add_u32 m0, s72, 52224\n" \
  "s_mov_b32 s74, 3244032\n" \
  "buffer_load_dwordx4 v24, s[40:43], s74 offen offset:0 lds\n" \
  "buffer_load_dwordx4 v25, s[40:43], s74 offen offset:1024 lds\n" \
  "buffer_load_dwordx4 v26, s[40:43], s74 offen offset:2048 lds\n" \
  "ds_read_b64_tr_b16 a[32:33], v31 offset:0\n" \
  "ds_read_b64_tr_b16 a[34:35], v31 offset:768\n" \
  "ds_read_b64_tr_b16 a[36:37], v31 offset:12288\n" \
  "ds_read_b64_tr_b16 a[38:39], v31 offset:13056\n" \
  "ds_read_b64_tr_b16 a[40:41], v31 offset:8\n" \
  "ds_read_b64_tr_b16 a[42:43], v31 offset:776\n" \
  "ds_read_b64_tr_b16 a[44:45], v31 offset:12296\n" \
  "ds_read_b64_tr_b16 a[46:47], v31 offset:13064\n" \
  "ds_read_b64_tr_b16 a[48:49], v23 offset:0\n" \
  "ds_read_b64_tr_b16 a[50:51], v23 offset:768\n" \
  "ds_read_b64_tr_b16 a[52:53], v23 offset:12288\n" \
  "ds_read_b64_tr_b16 a[54:55], v23 offset:13056\n" \
  "s_waitcnt vmcnt(24) lgkmcnt(0)\n" \
  "s_barrier\n" \
  "s_add_u32 m0, s72, 73728\n" \
  "s_mov_b32 s74, 3145728\n" \
  "buffer_load_dwordx4 v24, s[44:47], s74 offen offset:0 lds\n" \
  "buffer_load_dwordx4 v25, s[44:47], s74 offen offset:1024 lds\n" \
  "buffer_load_dwordx4 v26, s[44:47], s74 offen offset:2048 lds\n" \
  "s_add_u32 m0, s72, 76800\n" \
  "s_mov_b32 s74, 3244032\n" \
  "buffer_load_dwordx4 v24, s[44:47], s74 offen offset:0 lds\n" \
  "buffer_load_dwordx4 v25, s[44:47], s74 offen offset:1024 lds\n" \
  "buffer_load_dwordx4 v26, s[44:47], s74 offen offset:2048 lds\n" \
  "ds_read_b64_tr_b16 a[56:57], v31 offset:24576\n" \
  "ds_read_b64_tr_b16 a[58:59], v31 offset:25344\n" \
  "ds_read_b64_tr_b16 a[60:61], v31 offset:36864\n" \
  "ds_read_b64_tr_b16 a[62:63], v31 offset:37632\n" \
  "ds_read_b64_tr_b16 v[72:73], v31 offset:24584\n" \
  "ds_read_b64_tr_b16 v[74:75], v31 offset:25352\n" \
  "ds_read_b64_tr_b16 v[76:77], v31 offset:36872\n" \
  "ds_read_b64_tr_b16 v[78:79], v31 offset:37640\n" \
  "ds_read_b64_tr_b16 a[64:65], v23 offset:24576\n" \
  "ds_read_b64_tr_b16 a[66:67], v23 offset:25344\n" \
  "ds_read_b64_tr_b16 a[68:69], v23 offset:36864\n" \
  "ds_read_b64_tr_b16 a[70:71], v23 offset:37632\n" \
  "s_waitcnt vmcnt(24) lgkmcnt(0)\n" \
  "s_barrier\n" \
  "s_add_u32 m0, s72, 98304\n" \
  "s_mov_b32 s74, 3932160\n" \
  "buffer_load_dwordx4 v24, s[40:43], s74 offen offset:0 lds\n" \
  "buffer_load_dwordx4 v25, s[40:43], s74 offen offset:1024 lds\n" \
  "buffer_load_dwordx4 v26, s[40:43], s74 offen offset:2048 lds\n" \
  "s_add_u32 m0, s72, 101376\n" \
  "s_mov_b32 s74, 4030464\n" \
  "buffer_load_dwordx4 v24, s[40:43], s74 offen offset:0 lds\n" \
  "buffer_load_dwordx4 v25, s[40:43], s74 offen offset:1024 lds\n" \
  "buffer_load_dwordx4 v26, s[40:43], s74 offen offset:2048 lds\n" \
  "ds_read_b64_tr_b16 a[200:201], v31 offset:49152\n" \
  "ds_read_b64_tr_b16 a[202:203], v31 offset:49920\n" \
  "ds_read_b64_tr_b16 a[204:205], v31 offset:61440\n" \
  "ds_read_b64_tr_b16 a[206:207], v31 offset:62208\n" \
  "ds_read_b64_tr_b16 v[128:129], v31 offset:49160\n" \
  "ds_read_b64_tr_b16 v[130:131], v31 offset:49928\n" \
  "ds_read_b64_tr_b16 v[132:133], v31 offset:61448\n" \
  "ds_read_b64_tr_b16 v[134:135], v31 offset:62216\n" \
  "ds_read_b64_tr_b16 v[192:193], v23 offset:49152\n" \
  "ds_read_b64_tr_b16 v[194:195], v23 offset:49920\n" \
  "ds_read_b64_tr_b16 v[196:197], v23 offset:61440\n" \
  "ds_read_b64_tr_b16 v[198:199], v23 offset:62208\n" \
  "s_waitcnt vmcnt(24) lgkmcnt(0)\n" \
  "s_barrier\n" \
  "s_add_u32 m0, s72, 122880\n" \
  "s_mov_b32 s74, 3932160\n" \
  "buffer_load_dwordx4 v24, s[44:47], s74 offen offset:0 lds\n" \
  "buffer_load_dwordx4 v25, s[44:47], s74 offen offset:1024 lds\n" \
  "buffer_load_dwordx4 v26, s[44:47], s74 offen offset:2048 lds\n" \
  "s_add_u32 m0, s72, 125952\n" \
  "s_mov_b32 s74, 4030464\n" \
  "buffer_load_dwordx4 v24, s[44:47], s74 offen offset:0 lds\n" \
  "buffer_load_dwordx4 v25, s[44:47], s74 offen offset:1024 lds\n" \
  "buffer_load_dwordx4 v26, s[44:47], s74 offen offset:2048 lds\n" \
  "ds_read_b64_tr_b16 a[72:73], v27 offset:0\n" \
  "ds_read_b64_tr_b16 a[74:75], v27 offset:768\n" \
  "ds_read_b64_tr_b16 a[76:77], v27 offset:12288\n" \
  "ds_read_b64_tr_b16 a[78:79], v27 offset:13056\n" \
  "ds_read_b64_tr_b16 a[80:81], v27 offset:8\n" \
  "ds_read_b64_tr_b16 a[82:83], v27 offset:776\n" \
  "ds_read_b64_tr_b16 a[84:85], v27 offset:12296\n" \
  "ds_read_b64_tr_b16 a[86:87], v27 offset:13064\n" \
  "ds_read_b64_tr_b16 a[88:89], v30 offset:0\n" \
  "ds_read_b64_tr_b16 a[90:91], v30 offset:768\n" \
  "ds_read_b64_tr_b16 a[92:93], v30 offset:12288\n" \
  "ds_read_b64_tr_b16 a[94:95], v30 offset:13056\n" \
  "s_waitcnt vmcnt(24) lgkmcnt(0)\n" \
  "s_barrier\n" \
  "s_add_u32 m0, s72, 0\n" \
  "s_mov_b32 s74, 4718592\n" \
  "buffer_load_dwordx4 v24, s[40:43], s74 offen offset:0 lds\n" \
  "buffer_load_dwordx4 v25, s[40:43], s74 offen offset:1024 lds\n" \
  "buffer_load_dwordx4 v26, s[40:43], s74 offen offset:2048 lds\n" \
  "s_add_u32 m0, s72, 3072\n" \
  "s_mov_b32 s74, 4816896\n" \
  "buffer_load_dwordx4 v24, s[40:43], s74 offen offset:0 lds\n" \
  "buffer_load_dwordx4 v25, s[40:43], s74 offen offset:1024 lds\n" \
  "buffer_load_dwordx4 v26, s[40:43], s74 offen offset:2048 lds\n" \
  "ds_read_b64_tr_b16 a[208:209], v27 offset:24576\n" \
  "ds_read_b64_tr_b16 a[210:211], v27 offset:25344\n" \
  "ds_read_b64_tr_b16 a[212:213], v27 offset:36864\n" \
  "ds_read_b64_tr_b16 a[214:215], v27 offset:37632\n" \
  "ds_read_b64_tr_b16 v[136:137], v27 offset:24584\n" \
  "ds_read_b64_tr_b16 v[138:139], v27 offset:25352\n" \
  "ds_read_b64_tr_b16 v[140:141], v27 offset:36872\n" \
  "ds_read_b64_tr_b16 v[142:143], v27 offset:37640\n" \
  "ds_read_b64_tr_b16 v[200:201], v30 offset:24576\n" \
  "ds_read_b64_tr_b16 v[202:203], v30 offset:25344\n" \
  "ds_read_b64_tr_b16 v[204:205], v30 offset:36864\n" \
  "ds_read_b64_tr_b16 v[206:207], v30 offset:37632\n" \
  "s_waitcnt vmcnt(24) lgkmcnt(0)\n" \
  "s_barrier\n" \
  "s_add_u32 m0, s72, 24576\n" \
  "s_mov_b32 s74, 4718592\n" \
  "buffer_load_dwordx4 v24, s[44:47], s74 offen offset:0 lds\n" \
  "buffer_load_dwordx4 v25, s[44:47], s74 offen offset:1024 lds\n" \
  "buffer_load_dwordx4 v26, s[44:47], s74 offen offset:2048 lds\n" \
  "s_add_u32 m0, s72, 27648\n" \
  "s_mov_b32 s74, 4816896\n" \
  "buffer_load_dwordx4 v24, s[44:47], s74 offen offset:0 lds\n" \
  "buffer_load_dwordx4 v25, s[44:47], s74 offen offset:1024 lds\n" \
  "buffer_load_dwordx4 v26, s[44:47], s74 offen offset:2048 lds\n" \
  "ds_read_b64_tr_b16 v[80:81], v27 offset:49152\n" \
  "ds_read_b64_tr_b16 v[82:83], v27 offset:49920\n" \
  "ds_read_b64_tr_b16 v[84:85], v27 offset:61440\n" \
  "ds_read_b64_tr_b16 v[86:87], v27 offset:62208\n" \
  "ds_read_b64_tr_b16 a[96:97], v27 offset:49160\n" \
  "ds_read_b64_tr_b16 a[98:99], v27 offset:49928\n" \
  "ds_read_b64_tr_b16 a[100:101], v27 offset:61448\n" \
  "ds_read_b64_tr_b16 a[102:103], v27 offset:62216\n" \
  "ds_read_b64_tr_b16 v[88:89], v30 offset:49152\n" \
  "ds_read_b64_tr_b16 v[90:91], v30 offset:49920\n" \
  "ds_read_b64_tr_b16 v[92:93], v30 offset:61440\n" \
  "ds_read_b64_tr_b16 v[94:95], v30 offset:62208\n" \
  "s_waitcnt vmcnt(24) lgkmcnt(0)\n" \
  "s_barrier\n" \
  "s_add_u32 m0, s72, 49152\n" \
  "s_mov_b32 s74, 5505024\n" \
  "buffer_load_dwordx4 v24, s[40:43], s74 offen offset:0 lds\n" \
  "buffer_load_dwordx4 v25, s[40:43], s74 offen offset:1024 lds\n" \
  "buffer_load_dwordx4 v26, s[40:43], s74 offen offset:2048 lds\n" \
  "s_add_u32 m0, s72, 52224\n" \
  "s_mov_b32 s74, 5603328\n" \
  "buffer_load_dwordx4 v24, s[40:43], s74 offen offset:0 lds\n" \
  "buffer_load_dwordx4 v25, s[40:43], s74 offen offset:1024 lds\n" \
  "buffer_load_dwordx4 v26, s[40:43], s74 offen offset:2048 lds\n" \
  "ds_read_b64_tr_b16 a[216:217], v31 offset:0\n" \
  "ds_read_b64_tr_b16 a[218:219], v31 offset:768\n" \
  "ds_read_b64_tr_b16 a[220:221], v31 offset:12288\n" \
  "ds_read_b64_tr_b16 a[222:223], v31 offset:13056\n" \
  "ds_read_b64_tr_b16 v[144:145], v31 offset:8\n" \
  "ds_read_b64_tr_b16 v[146:147], v31 offset:776\n" \
  "ds_read_b64_tr_b16 v[148:149], v31 offset:12296\n" \
  "ds_read_b64_tr_b16 v[150:151], v31 offset:13064\n" \
  "ds_read_b64_tr_b16 v[208:209], v23 offset:0\n" \
  "ds_read_b64_tr_b16 v[210:211], v23 offset:768\n" \
  "ds_read_b64_tr_b16 v[212:213], v23 offset:12288\n" \
  "ds_read_b64_tr_b16 v[214:215], v23 offset:13056\n" \
  "s_waitcnt vmcnt(24) lgkmcnt(0)\n" \
  "s_barrier\n" \
  "s_add_u32 m0, s72, 73728\n" \
  "s_mov_b32 s74, 5505024\n" \
  "buffer_load_dwordx4 v24, s[44:47], s74 offen offset:0 lds\n" \
  "buffer_load_dwordx4 v25, s[44:47], s74 offen offset:1024 lds\n" \
  "buffer_load_dwordx4 v26, s[44:47], s74 offen offset:2048 lds\n" \
  "s_add_u32 m0, s72, 76800\n" \
  "s_mov_b32 s74, 5603328\n" \
  "buffer_load_dwordx4 v24, s[44:47], s74 offen offset:0 lds\n" \
  "buffer_load_dwordx4 v25, s[44:47], s74 offen offset:1024 lds\n" \
  "buffer_load_dwordx4 v26, s[44:47], s74 offen offset:2048 lds\n" \
  "ds_read_b64_tr_b16 a[104:105], v31 offset:24576\n" \
  "ds_read_b64_tr_b16 a[106:107], v31 offset:25344\n" \
  "ds_read_b64_tr_b16 a[108:109], v31 offset:36864\n" \
  "ds_read_b64_tr_b16 a[110:111], v31 offset:37632\n" \
  "ds_read_b64_tr_b16 v[96:97], v31 offset:24584\n" \
  "ds_read_b64_tr_b16 v[98:99], v31 offset:25352\n" \
  "ds_read_b64_tr_b16 v[100:101], v31 offset:36872\n" \
  "ds_read_b64_tr_b16 v[102:103], v31 offset:37640\n" \
  "ds_read_b64_tr_b16 a[112:113], v23 offset:24576\n" \
  "ds_read_b64_tr_b16 a[114:115], v23 offset:25344\n" \
  "ds_read_b64_tr_b16 a[116:117], v23 offset:36864\n" \
  "ds_read_b64_tr_b16 a[118:119], v23 offset:37632\n" \
  "s_waitcnt vmcnt(24) lgkmcnt(0)\n" \
  "s_barrier\n" \
  "s_add_u32 m0, s72, 98304\n" \
  "s_mov_b32 s74, 6291456\n" \
  "buffer_load_dwordx4 v24, s[40:43], s74 offen offset:0 lds\n" \
  "buffer_load_dwordx4 v25, s[40:43], s74 offen offset:1024 lds\n" \
  "buffer_load_dwordx4 v26, s[40:43], s74 offen offset:2048 lds\n" \
  "s_add_u32 m0, s72, 101376\n" \
  "s_mov_b32 s74, 6389760\n" \
  "buffer_load_dwordx4 v24, s[40:43], s74 offen offset:0 lds\n" \
  "buffer_load_dwordx4 v25, s[40:43], s74 offen offset:1024 lds\n" \
  "buffer_load_dwordx4 v26, s[40:43], s74 offen offset:2048 lds\n" \
  "ds_read_b64_tr_b16 a[224:225], v31 offset:49152\n" \
  "ds_read_b64_tr_b16 a[226:227], v31 offset:49920\n" \
  "ds_read_b64_tr_b16 a[228:229], v31 offset:61440\n" \
  "ds_read_b64_tr_b16 a[230:231], v31 offset:62208\n" \
  "ds_read_b64_tr_b16 v[152:153], v31 offset:49160\n" \
  "ds_read_b64_tr_b16 v[154:155], v31 offset:49928\n" \
  "ds_read_b64_tr_b16 v[156:157], v31 offset:61448\n" \
  "ds_read_b64_tr_b16 v[158:159], v31 offset:62216\n" \
  "ds_read_b64_tr_b16 v[216:217], v23 offset:49152\n" \
  "ds_read_b64_tr_b16 v[218:219], v23 offset:49920\n" \
  "ds_read_b64_tr_b16 v[220:221], v23 offset:61440\n" \
  "ds_read_b64_tr_b16 v[222:223], v23 offset:62208\n" \
  "s_waitcnt vmcnt(24) lgkmcnt(0)\n" \
  "s_barrier\n" \
  "s_add_u32 m0, s72, 122880\n" \
  "s_mov_b32 s74, 6291456\n" \
  "buffer_load_dwordx4 v24, s[44:47], s74 offen offset:0 lds\n" \
  "buffer_load_dwordx4 v25, s[44:47], s74 offen offset:1024 lds\n" \
  "buffer_load_dwordx4 v26, s[44:47], s74 offen offset:2048 lds\n" \
  "s_add_u32 m0, s72, 125952\n" \
  "s_mov_b32 s74, 6389760\n" \
  "buffer_load_dwordx4 v24, s[44:47], s74 offen offset:0 lds\n" \
  "buffer_load_dwordx4 v25, s[44:47], s74 offen offset:1024 lds\n" \
  "buffer_load_dwordx4 v26, s[44:47], s74 offen offset:2048 lds\n" \
  "ds_read_b64_tr_b16 a[120:121], v27 offset:0\n" \
  "ds_read_b64_tr_b16 a[122:123], v27 offset:768\n" \
  "ds_read_b64_tr_b16 a[124:125], v27 offset:12288\n" \
  "ds_read_b64_tr_b16 a[126:127], v27 offset:13056\n" \
  "ds_read_b64_tr_b16 v[104:105], v27 offset:8\n" \
  "ds_read_b64_tr_b16 v[106:107], v27 offset:776\n" \
  "ds_read_b64_tr_b16 v[108:109], v27 offset:12296\n" \
  "ds_read_b64_tr_b16 v[110:111], v27 offset:13064\n" \
  "ds_read_b64_tr_b16 a[168:169], v30 offset:0\n" \
  "ds_read_b64_tr_b16 a[170:171], v30 offset:768\n" \
  "ds_read_b64_tr_b16 a[172:173], v30 offset:12288\n" \
  "ds_read_b64_tr_b16 a[174:175], v30 offset:13056\n" \
  "s_waitcnt vmcnt(24) lgkmcnt(0)\n" \
  "s_barrier\n" \
  "s_add_u32 m0, s72, 0\n" \
  "s_mov_b32 s74, 7077888\n" \
  "buffer_load_dwordx4 v24, s[40:43], s74 offen offset:0 lds\n" \
  "buffer_load_dwordx4 v25, s[40:43], s74 offen offset:1024 lds\n" \
  "buffer_load_dwordx4 v26, s[40:43], s74 offen offset:2048 lds\n" \
  "s_add_u32 m0, s72, 3072\n" \
  "s_mov_b32 s74, 7176192\n" \
  "buffer_load_dwordx4 v24, s[40:43], s74 offen offset:0 lds\n" \
  "buffer_load_dwordx4 v25, s[40:43], s74 offen offset:1024 lds\n" \
  "buffer_load_dwordx4 v26, s[40:43], s74 offen offset:2048 lds\n" \
  "ds_read_b64_tr_b16 a[232:233], v27 offset:24576\n" \
  "ds_read_b64_tr_b16 a[234:235], v27 offset:25344\n" \
  "ds_read_b64_tr_b16 a[236:237], v27 offset:36864\n" \
  "ds_read_b64_tr_b16 a[238:239], v27 offset:37632\n" \
  "ds_read_b64_tr_b16 v[160:161], v27 offset:24584\n" \
  "ds_read_b64_tr_b16 v[162:163], v27 offset:25352\n" \
  "ds_read_b64_tr_b16 v[164:165], v27 offset:36872\n" \
  "ds_read_b64_tr_b16 v[166:167], v27 offset:37640\n" \
  "ds_read_b64_tr_b16 v[224:225], v30 offset:24576\n" \
  "ds_read_b64_tr_b16 v[226:227], v30 offset:25344\n" \
  "ds_read_b64_tr_b16 v[228:229], v30 offset:36864\n" \
  "ds_read_b64_tr_b16 v[230:231], v30 offset:37632\n" \
  "s_waitcnt vmcnt(24) lgkmcnt(0)\n" \
  "s_barrier\n" \
  "s_add_u32 m0, s72, 24576\n" \
  "s_mov_b32 s74, 7077888\n" \
  "buffer_load_dwordx4 v24, s[44:47], s74 offen offset:0 lds\n" \
  "buffer_load_dwordx4 v25, s[44:47], s74 offen offset:1024 lds\n" \
  "buffer_load_dwordx4 v26, s[44:47], s74 offen offset:2048 lds\n" \
  "s_add_u32 m0, s72, 27648\n" \
  "s_mov_b32 s74, 7176192\n" \
  "buffer_load_dwordx4 v24, s[44:47], s74 offen offset:0 lds\n" \
  "buffer_load_dwordx4 v25, s[44:47], s74 offen offset:1024 lds\n" \
  "buffer_load_dwordx4 v26, s[44:47], s74 offen offset:2048 lds\n" \
  "ds_read_b64_tr_b16 a[128:129], v27 offset:49152\n" \
  "ds_read_b64_tr_b16 a[130:131], v27 offset:49920\n" \
  "ds_read_b64_tr_b16 a[132:133], v27 offset:61440\n" \
  "ds_read_b64_tr_b16 a[134:135], v27 offset:62208\n" \
  "ds_read_b64_tr_b16 a[152:153], v27 offset:49160\n" \
  "ds_read_b64_tr_b16 a[154:155], v27 offset:49928\n" \
  "ds_read_b64_tr_b16 a[156:157], v27 offset:61448\n" \
  "ds_read_b64_tr_b16 a[158:159], v27 offset:62216\n" \
  "ds_read_b64_tr_b16 a[176:177], v30 offset:49152\n" \
  "ds_read_b64_tr_b16 a[178:179], v30 offset:49920\n" \
  "ds_read_b64_tr_b16 a[180:181], v30 offset:61440\n" \
  "ds_read_b64_tr_b16 a[182:183], v30 offset:62208\n" \
  "s_waitcnt vmcnt(24) lgkmcnt(0)\n" \
  "s_barrier\n" \
  "ds_read_b64_tr_b16 a[240:241], v31 offset:0\n" \
  "ds_read_b64_tr_b16 a[242:243], v31 offset:768\n" \
  "ds_read_b64_tr_b16 a[244:245], v31 offset:12288\n" \
  "ds_read_b64_tr_b16 a[246:247], v31 offset:13056\n" \
  "ds_read_b64_tr_b16 v[168:169], v31 offset:8\n" \
  "ds_read_b64_tr_b16 v[170:171], v31 offset:776\n" \
  "ds_read_b64_tr_b16 v[172:173], v31 offset:12296\n" \
  "ds_read_b64_tr_b16 v[174:175], v31 offset:13064\n" \
  "ds_read_b64_tr_b16 v[232:233], v23 offset:0\n" \
  "ds_read_b64_tr_b16 v[234:235], v23 offset:768\n" \
  "ds_read_b64_tr_b16 v[236:237], v23 offset:12288\n" \
  "ds_read_b64_tr_b16 v[238:239], v23 offset:13056\n" \
  "s_waitcnt vmcnt(18) lgkmcnt(0)\n" \
  "s_barrier\n" \
  "ds_read_b64_tr_b16 a[136:137], v31 offset:24576\n" \
  "ds_read_b64_tr_b16 a[138:139], v31 offset:25344\n" \
  "ds_read_b64_tr_b16 a[140:141], v31 offset:36864\n" \
  "ds_read_b64_tr_b16 a[142:143], v31 offset:37632\n" \
  "ds_read_b64_tr_b16 v[112:113], v31 offset:24584\n" \
  "ds_read_b64_tr_b16 v[114:115], v31 offset:25352\n" \
  "ds_read_b64_tr_b16 v[116:117], v31 offset:36872\n" \
  "ds_read_b64_tr_b16 v[118:119], v31 offset:37640\n" \
  "ds_read_b64_tr_b16 a[184:185], v23 offset:24576\n" \
  "ds_read_b64_tr_b16 a[186:187], v23 offset:25344\n" \
  "ds_read_b64_tr_b16 a[188:189], v23 offset:36864\n" \
  "ds_read_b64_tr_b16 a[190:191], v23 offset:37632\n" \
  "s_waitcnt vmcnt(12) lgkmcnt(0)\n" \
  "s_barrier\n" \
  "ds_read_b64_tr_b16 a[248:249], v31 offset:49152\n" \
  "ds_read_b64_tr_b16 a[250:251], v31 offset:49920\n" \
  "ds_read_b64_tr_b16 a[252:253], v31 offset:61440\n" \
  "ds_read_b64_tr_b16 a[254:255], v31 offset:62208\n" \
  "ds_read_b64_tr_b16 v[176:177], v31 offset:49160\n" \
  "ds_read_b64_tr_b16 v[178:179], v31 offset:49928\n" \
  "ds_read_b64_tr_b16 v[180:181], v31 offset:61448\n" \
  "ds_read_b64_tr_b16 v[182:183], v31 offset:62216\n" \
  "ds_read_b64_tr_b16 v[240:241], v23 offset:49152\n" \
  "ds_read_b64_tr_b16 v[242:243], v23 offset:49920\n" \
  "ds_read_b64_tr_b16 v[244:245], v23 offset:61440\n" \
  "ds_read_b64_tr_b16 v[246:247], v23 offset:62208\n" \
  "s_waitcnt vmcnt(6) lgkmcnt(0)\n" \
  "s_barrier\n" \
  "ds_read_b64_tr_b16 a[144:145], v27 offset:0\n" \
  "ds_read_b64_tr_b16 a[146:147], v27 offset:768\n" \
  "ds_read_b64_tr_b16 a[148:149], v27 offset:12288\n" \
  "ds_read_b64_tr_b16 a[150:151], v27 offset:13056\n" \
  "ds_read_b64_tr_b16 a[160:161], v27 offset:8\n" \
  "ds_read_b64_tr_b16 a[162:163], v27 offset:776\n" \
  "ds_read_b64_tr_b16 a[164:165], v27 offset:12296\n" \
  "ds_read_b64_tr_b16 a[166:167], v27 offset:13064\n" \
  "ds_read_b64_tr_b16 a[192:193], v30 offset:0\n" \
  "ds_read_b64_tr_b16 a[194:195], v30 offset:768\n" \
  "ds_read_b64_tr_b16 a[196:197], v30 offset:12288\n" \
  "ds_read_b64_tr_b16 a[198:199], v30 offset:13056\n" \
  "s_waitcnt vmcnt(0) lgkmcnt(0)\n" \
  "s_barrier\n" \
  "ds_read_b64_tr_b16 v[120:121], v27 offset:24576\n" \
  "ds_read_b64_tr_b16 v[122:123], v27 offset:25344\n" \
  "ds_read_b64_tr_b16 v[124:125], v27 offset:36864\n" \
  "ds_read_b64_tr_b16 v[126:127], v27 offset:37632\n" \
  "ds_read_b64_tr_b16 v[184:185], v27 offset:24584\n" \
  "ds_read_b64_tr_b16 v[186:187], v27 offset:25352\n" \
  "ds_read_b64_tr_b16 v[188:189], v27 offset:36872\n" \
  "ds_read_b64_tr_b16 v[190:191], v27 offset:37640\n" \
  "ds_read_b64_tr_b16 v[248:249], v30 offset:24576\n" \
  "ds_read_b64_tr_b16 v[250:251], v30 offset:25344\n" \
  "ds_read_b64_tr_b16 v[252:253], v30 offset:36864\n" \
  "ds_read_b64_tr_b16 v[254:255], v30 offset:37632\n" \
  "s_waitcnt lgkmcnt(0)\n" \
  "s_barrier\n" \
  "ds_write_b128 v3, v[32:35] offset:0\n" \
  "ds_write_b128 v3, v[36:39] offset:1024\n" \
  "ds_write_b128 v3, v[40:43] offset:2048\n" \
  "ds_write_b128 v3, v[44:47] offset:3072\n" \
  "ds_write_b128 v3, v[48:51] offset:4096\n" \
  "ds_write_b128 v3, v[52:55] offset:5120\n" \
  "ds_write_b128 v3, v[56:59] offset:6144\n" \
  "ds_write_b128 v3, v[60:63] offset:7168\n" \
  "ds_write_b128 v3, v[64:67] offset:8192\n" \
  "ds_write_b128 v3, v[68:71] offset:9216\n" \
  "ds_write_b128 v3, v[72:75] offset:10240\n" \
  "ds_write_b128 v3, v[76:79] offset:11264\n" \
  "ds_write_b128 v3, v[80:83] offset:12288\n" \
  "ds_write_b128 v3, v[84:87] offset:13312\n" \
  "ds_write_b128 v3, v[88:91] offset:14336\n" \
  "s_waitcnt lgkmcnt(8)\n" \
  "ds_write_b128 v3, v[92:95] offset:15360\n" \
  "ds_write_b128 v3, v[96:99] offset:16384\n" \
  "ds_write_b128 v3, v[100:103] offset:17408\n" \
  "ds_write_b128 v3, v[104:107] offset:18432\n" \
  "ds_write_b128 v3, v[108:111] offset:19456\n" \
  "ds_write_b128 v3, v[112:115] offset:20480\n" \
  "ds_write_b128 v3, v[116:119] offset:21504\n" \
  "s_waitcnt lgkmcnt(0)\n" \
  "s_cmp_eq_u32 s52, 1\n" \
  "s_cbranch_scc1 LSTARTs1_%=\n" \
  "s_cmp_eq_u32 s52, 2\n" \
  "s_cbranch_scc1 LSTARTs2_%=\n" \
  "s_cmp_eq_u32 s52, 3\n" \
  "s_cbranch_scc1 LSTARTs3_%=\n" \
  "LSTARTs0_%=:\n" \
  "v_mov_b32 v17, v28\n" \
  "s_mov_b32 s47, s38\n" \
  "s_mov_b32 s38, 0\n" \
  "s_mov_b32 s68, 63\n" \
  "s_mov_b32 s73, 20001\n" \
  "s_mov_b32 s56, 19998\n" \
  "LPASSs0_%=:\n" \
  "s_mov_b32 s40, 0\n" \
  "s_mov_b32 s42, 0x40000000\n" \
  "v_and_b32 v20, 0x7fff, v20\n" \
  "v_and_b32 v21, 0x7fff, v21\n" \
  "v_mov_b32 v64, 0\n" \
  "ds_write_b32 v12, v64 offset:512\n" \
  "s_waitcnt lgkmcnt(0)\n" \
  "LQGs0_%=:\n" \
  "buffer_load_dword v19, v18, s[36:39], 0 offen sc1\n" \
  "s_waitcnt vmcnt(0)\n" \
  "v_cmp_ge_u32 vcc, 1, v19\n" \
  "s_nop 3\n" \
  "s_cbranch_vccz LRGs0_%=\n" \
  "s_add_u32 s73, s73, 1\n" \
  "s_cmp_gt_u32 s73, 20000\n" \
  "s_cbranch_scc1 LRGs0_%=\n" \
  "s_sleep 4\n" \
  "s_branch LQGs0_%=\n" \
  "LRGs0_%=:\n" \
  "v_mov_b32 v64, 1\n" \
  "buffer_atomic_add v64, v17, s[36:39], 0 offen sc1\n" \
  "s_barrier\n" \
  "s_add_u32 m0, s40, 0\n" \
  "s_add_u32 s75, s55, 0\n" \
  "buffer_load_dwordx4 v8, s[36:39], s75 offen offset:0 sc1 lds\n" \
  "buffer_load_dwordx4 v8, s[36:39], s75 offen offset:1024 sc1 lds\n" \
  "buffer_load_dwordx4 v8, s[36:39], s75 offen offset:2048 sc1 lds\n" \
  "buffer_load_dwordx4 v8, s[36:39], s75 offen offset:3072 sc1 lds\n" \
  "s_add_u32 m0, s40, 4096\n" \
  "s_add_u32 s75, s55, 4096\n" \
  "buffer_load_dwordx4 v8, s[36:39], s75 offen offset:0 sc1 lds\n" \
  "buffer_load_dwordx4 v8, s[36:39], s75 offen offset:1024 sc1 lds\n" \
  "s_xor_b32 s40, s40, 0x8000\n" \
  "v_readlane_b32 s72, v11, 0\n" \
  "s_nop 0\n" \
  "s_add_u32 s69, s72, 2048\n" \
  "s_add_u32 s70, s72, 6144\n" \
  "s_add_u32 s44, s72, s93\n" \
  "s_add_u32 s45, s72, s94\n" \
  "s_add_u32 s46, s72, s95\n" \
  "v_readlane_b32 s41, v7, 0\n" \
  "s_nop 1\n" \
  "ds_read_b128 v[88:91], v3 offset:0\n" \
  "ds_read_b128 v[92:95], v3 offset:1024\n" \
  "ds_read_b128 v[96:99], v3 offset:2048\n" \
  "ds_read_b128 v[100:103], v3 offset:3072\n" \
  "ds_read_b128 v[104:107], v3 offset:4096\n" \
  "ds_read_b128 v[108:111], v3 offset:5120\n" \
  "ds_read_b128 v[112:115], v3 offset:6144\n" \
  "ds_read_b128 v[116:119], v3 offset:7168\n" \
  "v_mov_b32 v60, -1\n" \
  "v_mov_b32 v61, -1\n" \
  "ds_write_b64 v6, v[60:61] offset:32768\n" \
  "ds_write_b64 v6, v[60:61] offset:33280\n" \
  "ds_write_b64 v6, v[60:61] offset:36864\n" \
  "v_mov_b32 v64, 0\n" \
  "v_mov_b32 v65, 0\n" \
  "v_mov_b32 v66, 0\n" \
  "v_mov_b32 v67, 0\n" \
  "v_mov_b32 v68, 0\n" \
  "v_mov_b32 v69, 0\n" \
  "v_mov_b32 v70, 0\n" \
  "v_mov_b32 v71, 0\n" \
  "v_mov_b32 v72, 0\n" \
  "v_mov_b32 v73, 0\n" \
  "v_mov_b32 v74, 0\n" \
  "v_mov_b32 v75, 0\n" \
  "v_mov_b32 v76, 0\n" \
  "v_mov_b32 v77, 0\n" \
  "v_mov_b32 v78, 0\n" \
  "v_mov_b32 v79, 0\n" \
  "v_mov_b32 v80, 0\n" \
  "v_mov_b32 v81, 0\n" \
  "v_mov_b32 v82, 0\n" \
  "v_mov_b32 v83, 0\n" \
  "v_mov_b32 v84, 0\n" \
  "v_mov_b32 v85, 0\n" \
  "v_mov_b32 v86, 0\n" \
  "v_mov_b32 v87, 0\n" \
  "s_waitcnt lgkmcnt(0)\n" \
  "LOOPs0_%=:\n" \
  "s_barrier\n" \
  "buffer_load_dwordx4 v[24:27], v1, s[36:39], s72 offen offset:0 sc1\n" \
  "s_mov_b32 m0, 65536\n" \
  "s_nop 0\n" \
  "buffer_load_dwordx4 v1, s[36:39], s69 offen sc1 lds\n" \
  "buffer_load_dwordx4 v[28:31], v1, s[36:39], s72 offen offset:1024 sc1\n" \
  "s_mov_b32 m0, 69632\n" \
  "s_nop 0\n" \
  "buffer_load_dwordx4 v1, s[36:39], s70 offen sc1 lds\n" \
  "ds_write_b64 v6, v[60:61] offset:0\n" \
  "ds_write_b64 v6, v[60:61] offset:512\n" \
  "ds_write_b64 v6, v[60:61] offset:4096\n" \
  "s_add_u32 s74, s68, 2\n" \
  "s_min_u32 s43, s74, 63\n" \
  "s_add_u32 s68, s68, 1\n" \
  "LMs0_%=:\n" \
  "s_memtime s[76:77]\n" \
  "s_waitcnt vmcnt(3)\n" \
  "v_smfmac_f32_16x16x64_bf16 v[64:67], v[24:27], v[88:95], v0\n" \
  "ds_read_b128 v[88:91], v3 offset:8192\n" \
  "v_smfmac_f32_16x16x64_bf16 v[68:71], v[24:27], v[96:103], v0\n" \
  "ds_read_b128 v[92:95], v3 offset:9216\n" \
  "v_smfmac_f32_16x16x64_bf16 v[72:75], v[24:27], v[104:111], v0\n" \
  "ds_read_b128 v[96:99], v3 offset:10240\n" \
  "v_smfmac_f32_16x16x64_bf16 v[76:79], v[24:27], v[112:119], v0\n" \
  "ds_read_b128 v[100:103], v3 offset:11264\n" \
  "v_smfmac_f32_16x16x64_bf16 v[80:83], v[24:27], a[0:7], v0\n" \
  "ds_read_b128 v[104:107], v3 offset:12288\n" \
  "v_smfmac_f32_16x16x64_bf16 v[84:87], v[24:27], a[8:15], v0\n" \
  "s_waitcnt vmcnt(2)\n" \
  "s_waitcnt lgkmcnt(5)\n" \
  "s_barrier\n" \
  "ds_read_b128 v[32:35], v2 offset:2048\n" \
  "s_waitcnt vmcnt(1)\n" \
  "v_smfmac_f32_16x16x64_bf16 v[64:67], v[28:31], a[16:23], v0\n" \
  "ds_read_b128 v[36:39], v2 offset:3072\n" \
  "v_smfmac_f32_16x16x64_bf16 v[68:71], v[28:31], a[24:31], v0\n" \
  "ds_read_b128 v[40:43], v2 offset:4096\n" \
  "s_waitcnt lgkmcnt(6)\n" \
  "v_smfmac_f32_16x16x64_bf16 v[72:75], v[28:31], v[88:95], v0\n" \
  "ds_read_b128 v[44:47], v2 offset:5120\n" \
  "v_smfmac_f32_16x16x64_bf16 v[76:79], v[28:31], a[32:39], v0\n" \
  "ds_read_b128 v[108:111], v3 offset:13312\n" \
  "v_smfmac_f32_16x16x64_bf16 v[80:83], v[28:31], a[40:47], v0\n" \
  "ds_read_b128 v[112:115], v3 offset:14336\n" \
  "v_smfmac_f32_16x16x64_bf16 v[84:87], v[28:31], a[48:55], v0\n" \
  "ds_read_b128 v[116:119], v3 offset:15360\n" \
  "s_waitcnt lgkmcnt(6)\n" \
  "v_smfmac_f32_16x16x64_bf16 v[64:67], v[32:35], a[56:63], v0\n" \
  "ds_read_b128 v[88:91], v3 offset:16384\n" \
  "v_smfmac_f32_16x16x64_bf16 v[68:71], v[32:35], v[96:103], v0\n" \
  "ds_read_b128 v[92:95], v3 offset:17408\n" \
  "v_smfmac_f32_16x16x64_bf16 v[72:75], v[32:35], a[64:71], v0\n" \
  "ds_read_b128 v[96:99], v3 offset:18432\n" \
  "s_waitcnt lgkmcnt(8)\n" \
  "v_smfmac_f32_16x16x64_bf16 v[64:67], v[36:39], a[72:79], v0\n" \
  "s_waitcnt vmcnt(0)\n" \
  "s_barrier\n" \
  "ds_read_b128 v[100:103], v3 offset:19456\n" \
  "v_smfmac_f32_16x16x64_bf16 v[68:71], v[36:39], a[80:87], v0\n" \
  "ds_read_b128 v[48:51], v2 offset:6144\n" \
  "v_smfmac_f32_16x16x64_bf16 v[72:75], v[36:39], a[88:95], v0\n" \
  "ds_read_b128 v[52:55], v2 offset:7168\n" \
  "s_waitcnt lgkmcnt(8)\n" \
  "v_smfmac_f32_16x16x64_bf16 v[64:67], v[40:43], v[104:111], v0\n" \
  "ds_read_b128 v[56:59], v2 offset:8192\n" \
  "v_smfmac_f32_16x16x64_bf16 v[68:71], v[40:43], a[96:103], v0\n" \
  "ds_read_b128 v[60:63], v2 offset:9216\n" \
  "s_waitcnt lgkmcnt(8)\n" \
  "v_smfmac_f32_16x16x64_bf16 v[72:75], v[40:43], v[112:119], v0\n" \
  "ds_read_b128 v[104:107], v3 offset:20480\n" \
  "v_smfmac_f32_16x16x64_bf16 v[64:67], v[44:47], a[104:111], v0\n" \
  "ds_read_b128 v[108:111], v3 offset:21504\n" \
  "s_waitcnt lgkmcnt(8)\n" \
  "v_smfmac_f32_16x16x64_bf16 v[68:71], v[44:47], v[88:95], v0\n" \
  "v_smfmac_f32_16x16x64_bf16 v[72:75], v[44:47], a[112:119], v0\n" \
  "s_waitcnt lgkmcnt(5)\n" \
  "v_smfmac_f32_16x16x64_bf16 v[64:67], v[48:51], a[120:127], v0\n" \
  "s_waitcnt lgkmcnt(4)\n" \
  "v_smfmac_f32_16x16x64_bf16 v[64:67], v[52:55], a[128:135], v0\n" \
  "s_waitcnt lgkmcnt(3)\n" \
  "v_smfmac_f32_16x16x64_bf16 v[64:67], v[56:59], a[136:143], v0\n" \
  "s_waitcnt lgkmcnt(2)\n" \
  "v_smfmac_f32_16x16x64_bf16 v[64:67], v[60:63], a[144:151], v0\n" \
  "v_smfmac_f32_16x16x64_bf16 v[68:71], v[48:51], v[96:103], v0\n" \
  "v_smfmac_f32_16x16x64_bf16 v[68:71], v[52:55], a[152:159], v0\n" \
  "buffer_load_dword v19, v18, s[36:39], 0 offen sc1\n" \
  "s_waitcnt lgkmcnt(0)\n" \
  "v_cmp_u_f32 vcc, v64, v65\n" \
  "v_cmp_u_f32 s[82:83], v66, v67\n" \
  "v_smfmac_f32_16x16x64_bf16 v[68:71], v[56:59], v[104:111], v0\n" \
  "s_or_b64 vcc, vcc, s[82:83]\n" \
  "s_cbranch_scc1 LMQs0_%=\n" \
  "v_cvt_pk_bf16_f32 v64, v64, v65\n" \
  "v_smfmac_f32_16x16x64_bf16 v[68:71], v[60:63], a[160:167], v0\n" \
  "v_cvt_pk_bf16_f32 v65, v66, v67\n" \
  "v_smfmac_f32_16x16x64_bf16 v[72:75], v[48:51], a[168:175], v0\n" \
  "ds_write_b64 v6, v[64:65] offset:32768\n" \
  "v_smfmac_f32_16x16x64_bf16 v[72:75], v[52:55], a[176:183], v0\n" \
  "v_cvt_pk_bf16_f32 v68, v68, v69\n" \
  "v_smfmac_f32_16x16x64_bf16 v[72:75], v[56:59], a[184:191], v0\n" \
  "v_cvt_pk_bf16_f32 v69, v70, v71\n" \
  "v_smfmac_f32_16x16x64_bf16 v[72:75], v[60:63], a[192:199], v0\n" \
  "ds_write_b64 v6, v[68:69] offset:33280\n" \
  "v_smfmac_f32_16x16x64_bf16 v[76:79], v[32:35], a[200:207], v0\n" \
  "ds_read_b64_tr_b16 v[24:25], v9 offset:32768\n" \
  "v_smfmac_f32_16x16x64_bf16 v[76:79], v[36:39], a[208:215], v0\n" \
  "ds_read_b64_tr_b16 v[28:29], v9 offset:33792\n" \
  "v_cvt_pk_bf16_f32 v72, v72, v73\n" \
  "v_smfmac_f32_16x16x64_bf16 v[76:79], v[40:43], a[216:223], v0\n" \
  "ds_read_b64_tr_b16 v[112:113], v9 offset:34816\n" \
  "v_cvt_pk_bf16_f32 v73, v74, v75\n" \
  "v_smfmac_f32_16x16x64_bf16 v[76:79], v[44:47], a[224:231], v0\n" \
  "ds_write_b64 v6, v[72:73] offset:36864\n" \
  "v_smfmac_f32_16x16x64_bf16 v[76:79], v[48:51], a[232:239], v0\n" \
  "ds_read_b64_tr_b16 v[64:65], v9 offset:35840\n" \
  "v_smfmac_f32_16x16x64_bf16 v[76:79], v[52:55], a[240:247], v0\n" \
  "ds_read_b64_tr_b16 v[26:27], v9 offset:33280\n" \
  "v_smfmac_f32_16x16x64_bf16 v[76:79], v[56:59], a[248:255], v0\n" \
  "ds_read_b64_tr_b16 v[30:31], v9 offset:34304\n" \
  "v_smfmac_f32_16x16x64_bf16 v[76:79], v[60:63], v[120:127], v0\n" \
  "ds_read_b64_tr_b16 v[114:115], v9 offset:35328\n" \
  "v_smfmac_f32_16x16x64_bf16 v[80:83], v[32:35], v[128:135], v0\n" \
  "ds_read_b64_tr_b16 v[66:67], v9 offset:36352\n" \
  "v_smfmac_f32_16x16x64_bf16 v[80:83], v[36:39], v[136:143], v0\n" \
  "ds_read_b64_tr_b16 v[68:69], v9 offset:36864\n" \
  "v_cvt_pk_bf16_f32 v76, v76, v77\n" \
  "v_smfmac_f32_16x16x64_bf16 v[80:83], v[40:43], v[144:151], v0\n" \
  "ds_read_b64_tr_b16 v[70:71], v9 offset:37376\n" \
  "v_cvt_pk_bf16_f32 v77, v78, v79\n" \
  "v_smfmac_f32_16x16x64_bf16 v[80:83], v[44:47], v[152:159], v0\n" \
  "ds_write_b64 v6, v[76:77] offset:0\n" \
  "v_smfmac_f32_16x16x64_bf16 v[80:83], v[48:51], v[160:167], v0\n" \
  "ds_read_b64_tr_b16 v[72:73], v9 offset:37888\n" \
  "v_smfmac_f32_16x16x64_bf16 v[80:83], v[52:55], v[168:175], v0\n" \
  "s_waitcnt lgkmcnt(8)\n" \
  "ds_read_b64_tr_b16 v[74:75], v9 offset:38400\n" \
  "v_smfmac_f32_16x16x64_bf16 v[80:83], v[56:59], v[176:183], v0\n" \
  "v_smfmac_f32_16x16x64_bf16 v[80:83], v[60:63], v[184:191], v0\n" \
  "ds_read_b64_tr_b16 v[88:89], v9 offset:0\n" \
  "v_smfmac_f32_16x16x64_bf16 v[84:87], v[32:35], v[192:199], v0\n" \
  "ds_read_b64_tr_b16 v[92:93], v9 offset:1024\n" \
  "v_smfmac_f32_16x16x64_bf16 v[84:87], v[36:39], v[200:207], v0\n" \
  "ds_read_b64_tr_b16 v[96:97], v9 offset:2048\n" \
  "v_cvt_pk_bf16_f32 v80, v80, v81\n" \
  "v_smfmac_f32_16x16x64_bf16 v[84:87], v[40:43], v[208:215], v0\n" \
  "ds_read_b64_tr_b16 v[100:101], v9 offset:3072\n" \
  "v_cvt_pk_bf16_f32 v81, v82, v83\n" \
  "v_smfmac_f32_16x16x64_bf16 v[84:87], v[44:47], v[216:223], v0\n" \
  "ds_write_b64 v6, v[80:81] offset:512\n" \
  "v_smfmac_f32_16x16x64_bf16 v[84:87], v[48:51], v[224:231], v0\n" \
  "v_smfmac_f32_16x16x64_bf16 v[84:87], v[52:55], v[232:239], v0\n" \
  "v_smfmac_f32_16x16x64_bf16 v[84:87], v[56:59], v[240:247], v0\n" \
  "v_smfmac_f32_16x16x64_bf16 v[84:87], v[60:63], v[248:255], v0\n" \
  "s_waitcnt lgkmcnt(4)\n" \
  "s_nop 6\n" \
  "v_cvt_pk_bf16_f32 v84, v84, v85\n" \
  "v_cvt_pk_bf16_f32 v85, v86, v87\n" \
  "ds_write_b64 v6, v[84:85] offset:4096\n" \
  "ds_read_b64_tr_b16 v[90:91], v9 offset:512\n" \
  "ds_read_b64_tr_b16 v[102:103], v9 offset:3584\n" \
  "ds_read_b64_tr_b16 v[94:95], v9 offset:1536\n" \
  "ds_read_b64_tr_b16 v[98:99], v9 offset:2560\n" \
  "s_barrier ; timing only\n" \
  "ds_read_b64_tr_b16 v[104:105], v9 offset:4096\n" \
  "ds_read_b64_tr_b16 v[106:107], v9 offset:4608\n" \
  "ds_read_b64_tr_b16 v[108:109], v9 offset:5120\n" \
  "ds_read_b64_tr_b16 v[110:111], v9 offset:5632\n" \
  "ds_read2_b32 v[34:35], v12 offset0:0 offset1:64\n" \
  "s_waitcnt lgkmcnt(8)\n" \
  "v_mfma_f32_16x16x32_bf16 v[116:119], v[88:91], v[24:27], 0\n" \
  "s_waitcnt lgkmcnt(6)\n" \
  "v_mfma_f32_16x16x32_bf16 v[116:119], v[92:95], v[28:31], v[116:119]\n" \
  "s_waitcnt lgkmcnt(5)\n" \
  "v_mfma_f32_16x16x32_bf16 v[116:119], v[96:99], v[112:115], v[116:119]\n" \
  "v_mfma_f32_16x16x32_bf16 v[116:119], v[100:103], v[64:67], v[116:119]\n" \
  "s_waitcnt lgkmcnt(3)\n" \
  "v_mfma_f32_16x16x32_bf16 v[116:119], v[104:107], v[68:71], v[116:119]\n" \
  "s_waitcnt lgkmcnt(1)\n" \
  "v_mfma_f32_16x16x32_bf16 v[116:119], v[108:111], v[72:75], v[116:119]\n" \
  "s_waitcnt lgkmcnt(0)\n" \
  "v_add_f32 v34, v34, v35\n" \
  "s_nop 1\n" \
  "v_add_f32_dpp v34, v34, v34 row_ror:8 row_mask:0xf bank_mask:0xf\n" \
  "s_nop 2\n" \
  "v_permlane32_swap_b32 v116, v118\n" \
  "v_permlane32_swap_b32 v117, v119\n" \
  "v_add_f32 v118, v116, v118\n" \
  "v_add_f32 v119, v117, v119\n" \
  "v_mul_f32 v116, v4, v118\n" \
  "v_mul_f32 v117, v4, v119\n" \
  "v_fmac_f32_dpp v116, v118, v4 row_ror:8 row_mask:0xf bank_mask:0xf\n" \
  "v_fmac_f32_dpp v117, v119, v4 row_ror:8 row_mask:0xf bank_mask:0xf\n" \
  "v_exp_f32 v32, v116\n" \
  "v_exp_f32 v33, v117\n" \
  "v_add_f32 v34, v34, v32\n" \
  "v_add_f32 v36, v34, v33\n" \
  "v_add_f32 v37, v34, v33\n" \
  "v_cndmask_b32 v32, v14, v32, s[34:35]\n" \
  "v_cndmask_b32 v33, v15, v33, s[34:35]\n" \
  "v_permlane16_swap_b32 v36, v37\n" \
  "v_add_f32 v34, v36, v37\n" \
  "v_add_f32 v35, v36, v37\n" \
  "s_nop 1\n" \
  "v_permlane32_swap_b32 v34, v35\n" \
  "v_add_f32 v34, v34, v35\n" \
  "v_rcp_f32 v38, v34\n" \
  "v_subrev_u32 v34, 0x1f800000, v34\n" \
  "v_cmp_lt_u32 vcc, 0x40000000, v34\n" \
  "v_mul_f32 v32, v32, v38\n" \
  "v_mul_f32 v33, v33, v38\n" \
  "v_cvt_pk_bf16_f32 v32, v32, v33\n" \
  "s_cbranch_vccnz LMXs0_%=\n" \
  "buffer_store_dword v32, v13, s[36:39], s72 offen offset:0 sc1\n" \
  "buffer_store_dword v32, v13, s[36:39], s44 offen offset:0 sc1\n" \
  "buffer_store_dword v32, v13, s[36:39], s45 offen offset:0 sc1\n" \
  "buffer_store_dword v32, v13, s[36:39], s46 offen offset:0 sc1\n" \
  "s_barrier\n" \
  "LMYs0_%=:\n" \
  "s_waitcnt vmcnt(4)\n" \
  "v_cmp_ge_u32 vcc, s43, v19\n" \
  "s_nop 3\n" \
  "s_cbranch_vccnz LFFs0_%=\n" \
  "LFOKs0_%=:\n" \
  "v_xor_b32 v20, 0x8000, v20\n" \
  "v_xor_b32 v21, 0x8000, v21\n" \
  "ds_read_b128 v[88:91], v3 offset:0\n" \
  "ds_read_b128 v[92:95], v3 offset:1024\n" \
  "ds_read_b128 v[96:99], v3 offset:2048\n" \
  "ds_read_b128 v[100:103], v3 offset:3072\n" \
  "ds_read_b128 v[104:107], v3 offset:4096\n" \
  "ds_read_b128 v[108:111], v3 offset:5120\n" \
  "ds_read_b128 v[112:115], v3 offset:6144\n" \
  "ds_read_b128 v[116:119], v3 offset:7168\n" \
  "v_mov_b32 v60, -1\n" \
  "v_mov_b32 v61, -1\n" \
  "ds_write_b64 v6, v[60:61] offset:32768\n" \
  "ds_write_b64 v6, v[60:61] offset:33280\n" \
  "ds_write_b64 v6, v[60:61] offset:36864\n" \
  "v_mov_b32 v64, 0\n" \
  "v_mov_b32 v65, 0\n" \
  "v_mov_b32 v66, 0\n" \
  "v_mov_b32 v67, 0\n" \
  "v_mov_b32 v68, 0\n" \
  "v_mov_b32 v69, 0\n" \
  "v_mov_b32 v70, 0\n" \
  "v_mov_b32 v71, 0\n" \
  "v_mov_b32 v72, 0\n" \
  "v_mov_b32 v73, 0\n" \
  "v_mov_b32 v74, 0\n" \
  "v_mov_b32 v75, 0\n" \
  "v_mov_b32 v76, 0\n" \
  "v_mov_b32 v77, 0\n" \
  "v_mov_b32 v78, 0\n" \
  "v_mov_b32 v79, 0\n" \
  "v_mov_b32 v80, 0\n" \
  "v_mov_b32 v81, 0\n" \
  "v_mov_b32 v82, 0\n" \
  "v_mov_b32 v83, 0\n" \
  "v_mov_b32 v84, 0\n" \
  "v_mov_b32 v85, 0\n" \
  "v_mov_b32 v86, 0\n" \
  "v_mov_b32 v87, 0\n" \
  "s_waitcnt lgkmcnt(0)\n" \
  "s_add_u32 s49, s76, s96\n" \
  "LUKs0_%=:\n" \
  "s_memtime s[78:79]\n" \
  "s_waitcnt lgkmcnt(0)\n" \
  "s_sub_i32 s74, s49, s78\n" \
  "s_cmp_gt_i32 s74, 0\n" \
  "s_cbranch_scc1 LUKs0_%=\n" \
  "s_add_u32 m0, s40, 0\n" \
  "s_add_u32 s75, s41, 0\n" \
  "buffer_load_dwordx4 v8, s[36:39], s75 offen offset:0 sc1 lds\n" \
  "buffer_load_dwordx4 v8, s[36:39], s75 offen offset:1024 sc1 lds\n" \
  "buffer_load_dwordx4 v8, s[36:39], s75 offen offset:2048 sc1 lds\n" \
  "buffer_load_dwordx4 v8, s[36:39], s75 offen offset:3072 sc1 lds\n" \
  "s_add_u32 m0, s40, 4096\n" \
  "s_add_u32 s75, s41, 4096\n" \
  "buffer_load_dwordx4 v8, s[36:39], s75 offen offset:0 sc1 lds\n" \
  "buffer_load_dwordx4 v8, s[36:39], s75 offen offset:1024 sc1 lds\n" \
  "s_add_u32 m0, s40, 4096\n" \
  "s_add_u32 s75, s41, 4096\n" \
  "buffer_load_dwordx4 v8, s[36:39], s75 offen offset:2048 sc1 lds\n" \
  "buffer_load_dwordx4 v8, s[36:39], s75 offen offset:3072 sc1 lds\n" \
  "s_add_u32 m0, s40, 8192\n" \
  "s_add_u32 s75, s41, 16384\n" \
  "buffer_load_dwordx4 v8, s[36:39], s75 offen offset:0 sc1 lds\n" \
  "buffer_load_dwordx4 v8, s[36:39], s75 offen offset:1024 sc1 lds\n" \
  "buffer_load_dwordx4 v8, s[36:39], s75 offen offset:2048 sc1 lds\n" \
  "buffer_load_dwordx4 v8, s[36:39], s75 offen offset:3072 sc1 lds\n" \
  "s_add_u32 m0, s40, 12288\n" \
  "s_add_u32 s75, s41, 20480\n" \
  "buffer_load_dwordx4 v8, s[36:39], s75 offen offset:0 sc1 lds\n" \
  "buffer_load_dwordx4 v8, s[36:39], s75 offen offset:1024 sc1 lds\n" \
  "buffer_load_dwordx4 v8, s[36:39], s75 offen offset:2048 sc1 lds\n" \
  "buffer_load_dwordx4 v8, s[36:39], s75 offen offset:3072 sc1 lds\n" \
  "s_add_u32 m0, s40, 16384\n" \
  "s_add_u32 s75, s41, 32768\n" \
  "buffer_load_dwordx4 v8, s[36:39], s75 offen offset:0 sc1 lds\n" \
  "buffer_load_dwordx4 v8, s[36:39], s75 offen offset:1024 sc1 lds\n" \
  "s_add_u32 m0, s40, 16384\n" \
  "s_add_u32 s75, s41, 32768\n" \
  "buffer_load_dwordx4 v8, s[36:39], s75 offen offset:2048 sc1 lds\n" \
  "buffer_load_dwordx4 v8, s[36:39], s75 offen offset:3072 sc1 lds\n" \
  "s_add_u32 m0, s40, 20480\n" \
  "s_add_u32 s75, s41, 36864\n" \
  "buffer_load_dwordx4 v8, s[36:39], s75 offen offset:0 sc1 lds\n" \
  "buffer_load_dwordx4 v8, s[36:39], s75 offen offset:1024 sc1 lds\n" \
  "buffer_load_dwordx4 v8, s[36:39], s75 offen offset:2048 sc1 lds\n" \
  "buffer_load_dwordx4 v8, s[36:39], s75 offen offset:3072 sc1 lds\n" \
  "s_xor_b32 s40, s40, 0x8000\n" \
  "s_cmp_eq_u32 s68, 64\n" \
  "s_cbranch_scc1 LENDs0_%=\n" \
  "v_readlane_b32 s72, v11, s68\n" \
  "s_nop 0\n" \
  "s_add_u32 s69, s72, 2048\n" \
  "s_add_u32 s70, s72, 6144\n" \
  "s_add_u32 s44, s72, s93\n" \
  "s_add_u32 s45, s72, s94\n" \
  "s_add_u32 s46, s72, s95\n" \
  "v_readlane_b32 s41, v7, s68\n" \
  "s_nop 1\n" \
  "s_branch LOOPs0_%=\n" \
  "LFFs0_%=:\n" \
  "LQFs0_%=:\n" \
  "buffer_load_dword v19, v18, s[36:39], 0 offen sc1\n" \
  "s_waitcnt vmcnt(0)\n" \
  "v_cmp_ge_u32 vcc, s43, v19\n" \
  "s_nop 3\n" \
  "s_cbranch_vccz LRFs0_%=\n" \
  "s_add_u32 s73, s73, 1\n" \
  "s_cmp_gt_u32 s73, 20000\n" \
  "s_cbranch_scc1 LRFs0_%=\n" \
  "s_sleep 16\n" \
  "s_branch LQFs0_%=\n" \
  "LRFs0_%=:\n" \
  "s_branch LFOKs0_%=\n" \
  "LMFs0_%=:\n" \
  "ds_read_b128 v[88:91], v3 offset:0\n" \
  "ds_read_b128 v[92:95], v3 offset:1024\n" \
  "ds_read_b128 v[96:99], v3 offset:2048\n" \
  "ds_read_b128 v[100:103], v3 offset:3072\n" \
  "ds_read_b128 v[104:107], v3 offset:4096\n" \
  "ds_read_b128 v[108:111], v3 offset:5120\n" \
  "ds_read_b128 v[112:115], v3 offset:6144\n" \
  "ds_read_b128 v[116:119], v3 offset:7168\n" \
  "v_mov_b32 v60, -1\n" \
  "v_mov_b32 v61, -1\n" \
  "ds_write_b64 v6, v[60:61] offset:32768\n" \
  "ds_write_b64 v6, v[60:61] offset:33280\n" \
  "ds_write_b64 v6, v[60:61] offset:36864\n" \
  "v_mov_b32 v64, 0\n" \
  "v_mov_b32 v65, 0\n" \
  "v_mov_b32 v66, 0\n" \
  "v_mov_b32 v67, 0\n" \
  "v_mov_b32 v68, 0\n" \
  "v_mov_b32 v69, 0\n" \
  "v_mov_b32 v70, 0\n" \
  "v_mov_b32 v71, 0\n" \
  "v_mov_b32 v72, 0\n" \
  "v_mov_b32 v73, 0\n" \
  "v_mov_b32 v74, 0\n" \
  "v_mov_b32 v75, 0\n" \
  "v_mov_b32 v76, 0\n" \
  "v_mov_b32 v77, 0\n" \
  "v_mov_b32 v78, 0\n" \
  "v_mov_b32 v79, 0\n" \
  "v_mov_b32 v80, 0\n" \
  "v_mov_b32 v81, 0\n" \
  "v_mov_b32 v82, 0\n" \
  "v_mov_b32 v83, 0\n" \
  "v_mov_b32 v84, 0\n" \
  "v_mov_b32 v85, 0\n" \
  "v_mov_b32 v86, 0\n" \
  "v_mov_b32 v87, 0\n" \
  "s_waitcnt lgkmcnt(0)\n" \
  "s_barrier\n" \
  "buffer_load_dwordx4 v[24:27], v1, s[36:39], s72 offen offset:0 sc1\n" \
  "s_mov_b32 m0, 65536\n" \
  "s_nop 0\n" \
  "buffer_load_dwordx4 v1, s[36:39], s69 offen sc1 lds\n" \
  "buffer_load_dwordx4 v[28:31], v1, s[36:39], s72 offen offset:1024 sc1\n" \
  "s_mov_b32 m0, 69632\n" \
  "s_nop 0\n" \
  "buffer_load_dwordx4 v1, s[36:39], s70 offen sc1 lds\n" \
  "ds_write_b64 v6, v[60:61] offset:0\n" \
  "ds_write_b64 v6, v[60:61] offset:512\n" \
  "ds_write_b64 v6, v[60:61] offset:4096\n" \
  "s_branch LMs0_%=\n" \
  "LMQs0_%=:\n" \
  "s_waitcnt lgkmcnt(0)\n" \
  "s_barrier\n" \
  "v_mov_b32 v38, -1\n" \
  "LMXs0_%=:\n" \
  "v_cmp_u_f32 vcc, v38, v38\n" \
  "s_nop 3\n" \
  "s_cmp_lg_u64 vcc, 0\n" \
  "s_cselect_b32 s74, 2, 1\n" \
  "s_cmp_gt_u32 s56, 20000\n" \
  "s_cselect_b32 s74, 1, s74\n" \
  "v_mov_b32 v34, s74\n" \
  "ds_write_b32 v12, v34 offset:512\n" \
  "s_waitcnt lgkmcnt(0)\n" \
  "s_barrier\n" \
  "s_waitcnt vmcnt(0)\n" \
  "s_barrier\n" \
  "v_mov_b32 v34, 0\n" \
  "ds_write_b32 v12, v34 offset:512\n" \
  "s_lshr_b32 s74, s74, 1\n" \
  "s_add_u32 s56, s56, s74\n" \
  "s_cmp_lg_u32 s74, 0\n" \
  "s_cbranch_scc1 LMFs0_%=\n" \
  "s_add_u32 s57, s57, 1\n" \
  "v_mov_b32 v46, v116\n" \
  "v_mov_b32 v47, v117\n" \
  "ds_read_b128 v[88:91], v20 offset:0\n" \
  "ds_read_b128 v[92:95], v20 offset:2048\n" \
  "ds_read_b128 v[96:99], v20 offset:4096\n" \
  "ds_read_b128 v[100:103], v20 offset:6144\n" \
  "ds_read_b64 v[104:105], v21 offset:0\n" \
  "ds_read_b64 v[106:107], v21 offset:1024\n" \
  "ds_read_b64 v[108:109], v21 offset:2048\n" \
  "ds_read_b64 v[110:111], v21 offset:3072\n" \
  "s_waitcnt lgkmcnt(7)\n" \
  "v_mfma_f32_16x16x32_bf16 v[48:51], v[88:91], v[24:27], 0\n" \
  "s_waitcnt lgkmcnt(6)\n" \
  "v_mfma_f32_16x16x32_bf16 v[48:51], v[92:95], v[28:31], v[48:51]\n" \
  "s_waitcnt lgkmcnt(5)\n" \
  "v_mfma_f32_16x16x32_bf16 v[48:51], v[96:99], v[112:115], v[48:51]\n" \
  "s_waitcnt lgkmcnt(4)\n" \
  "v_mfma_f32_16x16x32_bf16 v[48:51], v[100:103], v[64:67], v[48:51]\n" \
  "s_waitcnt lgkmcnt(2)\n" \
  "v_mfma_f32_16x16x32_bf16 v[48:51], v[104:107], v[68:71], v[48:51]\n" \
  "s_waitcnt lgkmcnt(0)\n" \
  "v_mfma_f32_16x16x32_bf16 v[48:51], v[108:111], v[72:75], v[48:51]\n" \
  "s_nop 7\n" \
  "ds_read_b128 v[88:91], v20 offset:128\n" \
  "ds_read_b128 v[92:95], v20 offset:2176\n" \
  "ds_read_b128 v[96:99], v20 offset:4224\n" \
  "ds_read_b128 v[100:103], v20 offset:6272\n" \
  "ds_read_b64 v[104:105], v21 offset:128\n" \
  "ds_read_b64 v[106:107], v21 offset:1152\n" \
  "ds_read_b64 v[108:109], v21 offset:2176\n" \
  "ds_read_b64 v[110:111], v21 offset:3200\n" \
  "s_waitcnt lgkmcnt(7)\n" \
  "v_mfma_f32_16x16x32_bf16 v[52:55], v[88:91], v[24:27], 0\n" \
  "s_waitcnt lgkmcnt(6)\n" \
  "v_mfma_f32_16x16x32_bf16 v[52:55], v[92:95], v[28:31], v[52:55]\n" \
  "s_waitcnt lgkmcnt(5)\n" \
  "v_mfma_f32_16x16x32_bf16 v[52:55], v[96:99], v[112:115], v[52:55]\n" \
  "s_waitcnt lgkmcnt(4)\n" \
  "v_mfma_f32_16x16x32_bf16 v[52:55], v[100:103], v[64:67], v[52:55]\n" \
  "s_waitcnt lgkmcnt(2)\n" \
  "v_mfma_f32_16x16x32_bf16 v[52:55], v[104:107], v[68:71], v[52:55]\n" \
  "s_waitcnt lgkmcnt(0)\n" \
  "v_mfma_f32_16x16x32_bf16 v[52:55], v[108:111], v[72:75], v[52:55]\n" \
  "s_nop 7\n" \
  "v_add_f32_dpp v48, v48, v48 row_ror:8 row_mask:0xf bank_mask:0xf\n" \
  "v_mul_f32 v48, v4, v48\n" \
  "v_add_f32_dpp v49, v49, v49 row_ror:8 row_mask:0xf bank_mask:0xf\n" \
  "v_mul_f32 v49, v4, v49\n" \
  "v_add_f32_dpp v50, v50, v50 row_ror:8 row_mask:0xf bank_mask:0xf\n" \
  "v_mul_f32 v50, v4, v50\n" \
  "v_add_f32_dpp v51, v51, v51 row_ror:8 row_mask:0xf bank_mask:0xf\n" \
  "v_mul_f32 v51, v4, v51\n" \
  "v_add_f32_dpp v52, v52, v52 row_ror:8 row_mask:0xf bank_mask:0xf\n" \
  "v_mul_f32 v52, v4, v52\n" \
  "v_add_f32_dpp v53, v53, v53 row_ror:8 row_mask:0xf bank_mask:0xf\n" \
  "v_mul_f32 v53, v4, v53\n" \
  "v_add_f32_dpp v54, v54, v54 row_ror:8 row_mask:0xf bank_mask:0xf\n" \
  "v_mul_f32 v54, v4, v54\n" \
  "v_add_f32_dpp v55, v55, v55 row_ror:8 row_mask:0xf bank_mask:0xf\n" \
  "v_mul_f32 v55, v4, v55\n" \
  "v_max_f32 v32, v46, v47\n" \
  "v_max3_f32 v32, v32, v48, v49\n" \
  "v_max3_f32 v32, v32, v50, v51\n" \
  "v_max3_f32 v32, v32, v52, v53\n" \
  "v_max3_f32 v32, v32, v54, v55\n" \
  "v_mov_b32 v33, v32\n" \
  "s_nop 1\n" \
  "v_permlane16_swap_b32 v32, v33\n" \
  "v_max_f32 v32, v32, v33\n" \
  "v_mov_b32 v33, v32\n" \
  "s_nop 1\n" \
  "v_permlane32_swap_b32 v32, v33\n" \
  "v_max_f32 v32, v32, v33\n" \
  "v_sub_f32 v46, v46, v32\n" \
  "v_sub_f32 v47, v47, v32\n" \
  "v_sub_f32 v48, v48, v32\n" \
  "v_sub_f32 v49, v49, v32\n" \
  "v_sub_f32 v50, v50, v32\n" \
  "v_sub_f32 v51, v51, v32\n" \
  "v_sub_f32 v52, v52, v32\n" \
  "v_sub_f32 v53, v53, v32\n" \
  "v_sub_f32 v54, v54, v32\n" \
  "v_sub_f32 v55, v55, v32\n" \
  "v_exp_f32 v46, v46\n" \
  "v_exp_f32 v47, v47\n" \
  "v_exp_f32 v48, v48\n" \
  "v_exp_f32 v49, v49\n" \
  "v_exp_f32 v50, v50\n" \
  "v_exp_f32 v51, v51\n" \
  "v_exp_f32 v52, v52\n" \
  "v_exp_f32 v53, v53\n" \
  "v_exp_f32 v54, v54\n" \
  "v_exp_f32 v55, v55\n" \
  "v_add_f32 v32, v46, v47\n" \
  "v_add_f32 v32, v32, v48\n" \
  "v_add_f32 v32, v32, v49\n" \
  "v_add_f32 v32, v32, v50\n" \
  "v_add_f32 v32, v32, v51\n" \
  "v_add_f32 v32, v32, v52\n" \
  "v_add_f32 v32, v32, v53\n" \
  "v_add_f32 v32, v32, v54\n" \
  "v_add_f32 v32, v32, v55\n" \
  "v_mov_b32 v33, v32\n" \
  "s_nop 1\n" \
  "v_permlane16_swap_b32 v32, v33\n" \
  "v_add_f32 v32, v32, v33\n" \
  "v_mov_b32 v33, v32\n" \
  "s_nop 1\n" \
  "v_permlane32_swap_b32 v32, v33\n" \
  "v_add_f32 v32, v32, v33\n" \
  "v_rcp_f32 v32, v32\n" \
  "v_cvt_pk_bf16_f32 v34, v48, v49\n" \
  "v_pk_min_u16 v34, v34, -2 op_sel_hi:[1,0]\n" \
  "v_cvt_pk_bf16_f32 v35, v50, v51\n" \
  "v_pk_min_u16 v35, v35, -2 op_sel_hi:[1,0]\n" \
  "v_cvt_pk_bf16_f32 v36, v52, v53\n" \
  "v_pk_min_u16 v36, v36, -2 op_sel_hi:[1,0]\n" \
  "v_cvt_pk_bf16_f32 v37, v54, v55\n" \
  "v_pk_min_u16 v37, v37, -2 op_sel_hi:[1,0]\n" \
  "v_cndmask_b32 v34, v35, v34, s[34:35]\n" \
  "buffer_store_dword v34, v22, s[36:39], s72 offen offset:0 sc1\n" \
  "buffer_store_dword v34, v22, s[36:39], s44 offen offset:0 sc1\n" \
  "buffer_store_dword v34, v22, s[36:39], s45 offen offset:0 sc1\n" \
  "buffer_store_dword v34, v22, s[36:39], s46 offen offset:0 sc1\n" \
  "v_cndmask_b32 v36, v37, v36, s[34:35]\n" \
  "buffer_store_dword v36, v22, s[36:39], s72 offen offset:256 sc1\n" \
  "buffer_store_dword v36, v22, s[36:39], s44 offen offset:256 sc1\n" \
  "buffer_store_dword v36, v22, s[36:39], s45 offen offset:256 sc1\n" \
  "buffer_store_dword v36, v22, s[36:39], s46 offen offset:256 sc1\n" \
  "v_cndmask_b32 v46, v14, v46, s[34:35]\n" \
  "v_cndmask_b32 v47, v15, v47, s[34:35]\n" \
  "v_mul_f32 v46, v46, v32\n" \
  "v_mul_f32 v47, v47, v32\n" \
  "v_cvt_pk_bf16_f32 v46, v46, v47\n" \
  "v_pk_min_u16 v46, v46, -2 op_sel_hi:[1,0]\n" \
  "v_or_b32 v33, v14, v15\n" \
  "v_cmp_eq_u32 vcc, 0, v33\n" \
  "v_mov_b32 v33, 0xfffe\n" \
  "s_nop 3\n" \
  "s_andn2_b64 vcc, vcc, s[34:35]\n" \
  "s_nop 3\n" \
  "v_cndmask_b32 v46, v46, v33, vcc\n" \
  "s_waitcnt vmcnt(0) lgkmcnt(0)\n" \
  "buffer_store_dword v46, v13, s[36:39], s72 offen offset:0 sc1\n" \
  "buffer_store_dword v46, v13, s[36:39], s44 offen offset:0 sc1\n" \
  "buffer_store_dword v46, v13, s[36:39], s45 offen offset:0 sc1\n" \
  "buffer_store_dword v46, v13, s[36:39], s46 offen offset:0 sc1\n" \
  "s_branch LMYs0_%=\n" \
  "LENDs0_%=:\n" \
  "s_cmp_lg_u32 s38, 0\n" \
  "s_cbranch_scc1 LFINs0_%=\n" \
  "s_waitcnt vmcnt(0) lgkmcnt(0)\n" \
  "s_mov_b32 s38, s47\n" \
  "s_mov_b32 s68, 0\n" \
  "s_mov_b32 s73, 0\n" \
  "s_mov_b32 s56, 0\n" \
  "s_mov_b32 s57, 0\n" \
  "s_branch LPASSs0_%=\n" \
  "LFINs0_%=:\n" \
  "s_branch LDONE_%=\n" \
  "LSTARTs1_%=:\n" \
  "s_mov_b32 s47, s38\n" \
  "s_mov_b32 s38, 0\n" \
  "s_mov_b32 s68, 63\n" \
  "s_mov_b32 s73, 20001\n" \
  "s_mov_b32 s56, 19998\n" \
  "LPASSs1_%=:\n" \
  "s_mov_b32 s40, 0\n" \
  "s_mov_b32 s42, 0x40000000\n" \
  "v_and_b32 v20, 0x7fff, v20\n" \
  "v_and_b32 v21, 0x7fff, v21\n" \
  "v_and_b32 v18, 0x7fff, v18\n" \
  "s_barrier\n" \
  "s_add_u32 m0, s40, 4096\n" \
  "s_add_u32 s75, s55, 4096\n" \
  "buffer_load_dwordx4 v8, s[36:39], s75 offen offset:2048 sc1 lds\n" \
  "buffer_load_dwordx4 v8, s[36:39], s75 offen offset:3072 sc1 lds\n" \
  "s_add_u32 m0, s40, 8192\n" \
  "s_add_u32 s75, s55, 16384\n" \
  "buffer_load_dwordx4 v8, s[36:39], s75 offen offset:0 sc1 lds\n" \
  "buffer_load_dwordx4 v8, s[36:39], s75 offen offset:1024 sc1 lds\n" \
  "buffer_load_dwordx4 v8, s[36:39], s75 offen offset:2048 sc1 lds\n" \
  "buffer_load_dwordx4 v8, s[36:39], s75 offen offset:3072 sc1 lds\n" \
  "s_xor_b32 s40, s40, 0x8000\n" \
  "v_readlane_b32 s72, v11, 0\n" \
  "s_nop 0\n" \
  "s_add_u32 s69, s72, 3072\n" \
  "s_add_u32 s70, s72, 7168\n" \
  "s_add_u32 s44, s72, s93\n" \
  "s_add_u32 s45, s72, s94\n" \
  "s_add_u32 s46, s72, s95\n" \
  "v_readlane_b32 s41, v7, 0\n" \
  "s_nop 1\n" \
  "LOOPs1_%=:\n" \
  "ds_read_b128 v[88:91], v3 offset:0\n" \
  "ds_read_b128 v[92:95], v3 offset:1024\n" \
  "ds_read_b128 v[96:99], v3 offset:2048\n" \
  "ds_read_b128 v[100:103], v3 offset:3072\n" \
  "ds_read_b128 v[104:107], v3 offset:4096\n" \
  "ds_read_b128 v[108:111], v3 offset:5120\n" \
  "ds_read_b128 v[112:115], v3 offset:6144\n" \
  "ds_read_b128 v[116:119], v3 offset:7168\n" \
  "v_mov_b32 v60, -1\n" \
  "v_mov_b32 v61, -1\n" \
  "ds_write_b64 v6, v[60:61] offset:33792\n" \
  "ds_write_b64 v6, v[60:61] offset:34304\n" \
  "ds_write_b64 v6, v[60:61] offset:37376\n" \
  "v_mov_b32 v64, 0\n" \
  "v_mov_b32 v65, 0\n" \
  "v_mov_b32 v66, 0\n" \
  "v_mov_b32 v67, 0\n" \
  "v_mov_b32 v68, 0\n" \
  "v_mov_b32 v69, 0\n" \
  "v_mov_b32 v70, 0\n" \
  "v_mov_b32 v71, 0\n" \
  "v_mov_b32 v72, 0\n" \
  "v_mov_b32 v73, 0\n" \
  "v_mov_b32 v74, 0\n" \
  "v_mov_b32 v75, 0\n" \
  "v_mov_b32 v76, 0\n" \
  "v_mov_b32 v77, 0\n" \
  "v_mov_b32 v78, 0\n" \
  "v_mov_b32 v79, 0\n" \
  "v_mov_b32 v80, 0\n" \
  "v_mov_b32 v81, 0\n" \
  "v_mov_b32 v82, 0\n" \
  "v_mov_b32 v83, 0\n" \
  "v_mov_b32 v84, 0\n" \
  "v_mov_b32 v85, 0\n" \
  "v_mov_b32 v86, 0\n" \
  "v_mov_b32 v87, 0\n" \
  "s_waitcnt lgkmcnt(0)\n" \
  "s_barrier\n" \
  "buffer_load_dwordx4 v[24:27], v1, s[36:39], s72 offen offset:0 sc1\n" \
  "s_mov_b32 m0, 66560\n" \
  "s_nop 0\n" \
  "buffer_load_dwordx4 v1, s[36:39], s69 offen sc1 lds\n" \
  "buffer_load_dwordx4 v[28:31], v1, s[36:39], s72 offen offset:1024 sc1\n" \
  "s_mov_b32 m0, 70656\n" \
  "s_nop 0\n" \
  "buffer_load_dwordx4 v1, s[36:39], s70 offen sc1 lds\n" \
  "ds_write_b64 v6, v[60:61] offset:1024\n" \
  "ds_write_b64 v6, v[60:61] offset:1536\n" \
  "ds_write_b64 v6, v[60:61] offset:4608\n" \
  "ds_write_b32 v12, v60 offset:0\n" \
  "s_add_u32 s68, s68, 1\n" \
  "LMs1_%=:\n" \
  "buffer_store_dwordx4 v[52:55], v16, s[36:39], s42 offen sc1\n" \
  "buffer_store_dword v56, v17, s[36:39], s42 offen sc1\n" \
  "s_mov_b32 s42, 0x40000000\n" \
  "s_waitcnt vmcnt(5)\n" \
  "v_smfmac_f32_16x16x64_bf16 v[64:67], v[24:27], v[88:95], v0\n" \
  "ds_read_b128 v[88:91], v3 offset:8192\n" \
  "v_smfmac_f32_16x16x64_bf16 v[68:71], v[24:27], v[96:103], v0\n" \
  "ds_read_b128 v[92:95], v3 offset:9216\n" \
  "v_smfmac_f32_16x16x64_bf16 v[72:75], v[24:27], v[104:111], v0\n" \
  "ds_read_b128 v[96:99], v3 offset:10240\n" \
  "v_smfmac_f32_16x16x64_bf16 v[76:79], v[24:27], v[112:119], v0\n" \
  "ds_read_b128 v[100:103], v3 offset:11264\n" \
  "v_smfmac_f32_16x16x64_bf16 v[80:83], v[24:27], a[0:7], v0\n" \
  "ds_read_b128 v[104:107], v3 offset:12288\n" \
  "v_smfmac_f32_16x16x64_bf16 v[84:87], v[24:27], a[8:15], v0\n" \
  "s_waitcnt vmcnt(4)\n" \
  "s_waitcnt lgkmcnt(5)\n" \
  "s_barrier\n" \
  "ds_read_b128 v[32:35], v2 offset:2048\n" \
  "s_waitcnt vmcnt(3)\n" \
  "v_smfmac_f32_16x16x64_bf16 v[64:67], v[28:31], a[16:23], v0\n" \
  "ds_read_b128 v[36:39], v2 offset:3072\n" \
  "v_smfmac_f32_16x16x64_bf16 v[68:71], v[28:31], a[24:31], v0\n" \
  "ds_read_b128 v[40:43], v2 offset:4096\n" \
  "s_waitcnt lgkmcnt(6)\n" \
  "v_smfmac_f32_16x16x64_bf16 v[72:75], v[28:31], v[88:95], v0\n" \
  "ds_read_b128 v[44:47], v2 offset:5120\n" \
  "v_smfmac_f32_16x16x64_bf16 v[76:79], v[28:31], a[32:39], v0\n" \
  "ds_read_b128 v[108:111], v3 offset:13312\n" \
  "v_smfmac_f32_16x16x64_bf16 v[80:83], v[28:31], a[40:47], v0\n" \
  "ds_read_b128 v[112:115], v3 offset:14336\n" \
  "v_smfmac_f32_16x16x64_bf16 v[84:87], v[28:31], a[48:55], v0\n" \
  "ds_read_b128 v[116:119], v3 offset:15360\n" \
  "s_waitcnt lgkmcnt(6)\n" \
  "v_smfmac_f32_16x16x64_bf16 v[64:67], v[32:35], a[56:63], v0\n" \
  "ds_read_b128 v[88:91], v3 offset:16384\n" \
  "v_smfmac_f32_16x16x64_bf16 v[68:71], v[32:35], v[96:103], v0\n" \
  "ds_read_b128 v[92:95], v3 offset:17408\n" \
  "v_smfmac_f32_16x16x64_bf16 v[72:75], v[32:35], a[64:71], v0\n" \
  "ds_read_b128 v[96:99], v3 offset:18432\n" \
  "s_waitcnt lgkmcnt(8)\n" \
  "v_smfmac_f32_16x16x64_bf16 v[64:67], v[36:39], a[72:79], v0\n" \
  "s_waitcnt vmcnt(2)\n" \
  "s_barrier\n" \
  "ds_read_b128 v[100:103], v3 offset:19456\n" \
  "v_smfmac_f32_16x16x64_bf16 v[68:71], v[36:39], a[80:87], v0\n" \
  "ds_read_b128 v[48:51], v2 offset:6144\n" \
  "v_smfmac_f32_16x16x64_bf16 v[72:75], v[36:39], a[88:95], v0\n" \
  "ds_read_b128 v[52:55], v2 offset:7168\n" \
  "s_waitcnt lgkmcnt(8)\n" \
  "v_smfmac_f32_16x16x64_bf16 v[64:67], v[40:43], v[104:111], v0\n" \
  "ds_read_b128 v[56:59], v2 offset:8192\n" \
  "v_smfmac_f32_16x16x64_bf16 v[68:71], v[40:43], a[96:103], v0\n" \
  "ds_read_b128 v[60:63], v2 offset:9216\n" \
  "s_waitcnt lgkmcnt(8)\n" \
  "v_smfmac_f32_16x16x64_bf16 v[72:75], v[40:43], v[112:119], v0\n" \
  "ds_read_b128 v[104:107], v3 offset:20480\n" \
  "v_smfmac_f32_16x16x64_bf16 v[64:67], v[44:47], a[104:111], v0\n" \
  "ds_read_b128 v[108:111], v3 offset:21504\n" \
  "s_waitcnt lgkmcnt(8)\n" \
  "v_smfmac_f32_16x16x64_bf16 v[68:71], v[44:47], v[88:95], v0\n" \
  "ds_read_b128 v[88:91], v20 offset:0\n" \
  "v_smfmac_f32_16x16x64_bf16 v[72:75], v[44:47], a[112:119], v0\n" \
  "ds_read_b128 v[92:95], v20 offset:2048\n" \
  "s_waitcnt lgkmcnt(7)\n" \
  "v_smfmac_f32_16x16x64_bf16 v[64:67], v[48:51], a[120:127], v0\n" \
  "s_waitcnt lgkmcnt(6)\n" \
  "v_smfmac_f32_16x16x64_bf16 v[64:67], v[52:55], a[128:135], v0\n" \
  "s_waitcnt lgkmcnt(5)\n" \
  "v_smfmac_f32_16x16x64_bf16 v[64:67], v[56:59], a[136:143], v0\n" \
  "s_waitcnt lgkmcnt(4)\n" \
  "v_smfmac_f32_16x16x64_bf16 v[64:67], v[60:63], a[144:151], v0\n" \
  "v_smfmac_f32_16x16x64_bf16 v[68:71], v[48:51], v[96:103], v0\n" \
  "ds_read_b128 v[96:99], v20 offset:4096\n" \
  "v_smfmac_f32_16x16x64_bf16 v[68:71], v[52:55], a[152:159], v0\n" \
  "ds_read_b128 v[100:103], v20 offset:6144\n" \
  "s_waitcnt lgkmcnt(4)\n" \
  "v_cmp_u_f32 vcc, v64, v65\n" \
  "v_cmp_u_f32 s[82:83], v66, v67\n" \
  "v_smfmac_f32_16x16x64_bf16 v[68:71], v[56:59], v[104:111], v0\n" \
  "s_or_b64 vcc, vcc, s[82:83]\n" \
  "s_cbranch_scc1 LMQs1_%=\n" \
  "ds_read_b64 v[104:105], v21 offset:0\n" \
  "v_cvt_pk_bf16_f32 v64, v64, v65\n" \
  "v_smfmac_f32_16x16x64_bf16 v[68:71], v[60:63], a[160:167], v0\n" \
  "ds_read_b64 v[106:107], v21 offset:1024\n" \
  "v_cvt_pk_bf16_f32 v65, v66, v67\n" \
  "v_smfmac_f32_16x16x64_bf16 v[72:75], v[48:51], a[168:175], v0\n" \
  "ds_write_b64 v6, v[64:65] offset:33792\n" \
  "v_smfmac_f32_16x16x64_bf16 v[72:75], v[52:55], a[176:183], v0\n" \
  "ds_read_b64 v[108:109], v21 offset:2048\n" \
  "v_cvt_pk_bf16_f32 v68, v68, v69\n" \
  "v_smfmac_f32_16x16x64_bf16 v[72:75], v[56:59], a[184:191], v0\n" \
  "ds_read_b64 v[110:111], v21 offset:3072\n" \
  "v_cvt_pk_bf16_f32 v69, v70, v71\n" \
  "v_smfmac_f32_16x16x64_bf16 v[72:75], v[60:63], a[192:199], v0\n" \
  "ds_write_b64 v6, v[68:69] offset:34304\n" \
  "v_smfmac_f32_16x16x64_bf16 v[76:79], v[32:35], a[200:207], v0\n" \
  "ds_read_b64_tr_b16 v[24:25], v9 offset:32768\n" \
  "v_smfmac_f32_16x16x64_bf16 v[76:79], v[36:39], a[208:215], v0\n" \
  "ds_read_b64_tr_b16 v[28:29], v9 offset:33792\n" \
  "v_cvt_pk_bf16_f32 v72, v72, v73\n" \
  "v_smfmac_f32_16x16x64_bf16 v[76:79], v[40:43], a[216:223], v0\n" \
  "ds_read_b64_tr_b16 v[112:113], v9 offset:34816\n" \
  "v_cvt_pk_bf16_f32 v73, v74, v75\n" \
  "v_smfmac_f32_16x16x64_bf16 v[76:79], v[44:47], a[224:231], v0\n" \
  "ds_write_b64 v6, v[72:73] offset:37376\n" \
  "v_smfmac_f32_16x16x64_bf16 v[76:79], v[48:51], a[232:239], v0\n" \
  "ds_read_b64_tr_b16 v[64:65], v9 offset:35840\n" \
  "v_smfmac_f32_16x16x64_bf16 v[76:79], v[52:55], a[240:247], v0\n" \
  "s_waitcnt lgkmcnt(8)\n" \
  "ds_read_b64_tr_b16 v[26:27], v9 offset:33280\n" \
  "v_smfmac_f32_16x16x64_bf16 v[76:79], v[56:59], a[248:255], v0\n" \
  "ds_read_b64_tr_b16 v[30:31], v9 offset:34304\n" \
  "v_smfmac_f32_16x16x64_bf16 v[76:79], v[60:63], v[120:127], v0\n" \
  "ds_read_b64_tr_b16 v[114:115], v9 offset:35328\n" \
  "v_smfmac_f32_16x16x64_bf16 v[80:83], v[32:35], v[128:135], v0\n" \
  "ds_read_b64_tr_b16 v[66:67], v9 offset:36352\n" \
  "v_smfmac_f32_16x16x64_bf16 v[80:83], v[36:39], v[136:143], v0\n" \
  "ds_read_b64_tr_b16 v[68:69], v9 offset:36864\n" \
  "v_cvt_pk_bf16_f32 v76, v76, v77\n" \
  "v_smfmac_f32_16x16x64_bf16 v[80:83], v[40:43], v[144:151], v0\n" \
  "ds_read_b64_tr_b16 v[70:71], v9 offset:37376\n" \
  "v_cvt_pk_bf16_f32 v77, v78, v79\n" \
  "v_smfmac_f32_16x16x64_bf16 v[80:83], v[44:47], v[152:159], v0\n" \
  "ds_write_b64 v6, v[76:77] offset:1024\n" \
  "v_smfmac_f32_16x16x64_bf16 v[80:83], v[48:51], v[160:167], v0\n" \
  "s_waitcnt lgkmcnt(8)\n" \
  "ds_read_b64_tr_b16 v[72:73], v9 offset:37888\n" \
  "v_smfmac_f32_16x16x64_bf16 v[80:83], v[52:55], v[168:175], v0\n" \
  "ds_read_b64_tr_b16 v[74:75], v9 offset:38400\n" \
  "v_smfmac_f32_16x16x64_bf16 v[80:83], v[56:59], v[176:183], v0\n" \
  "v_smfmac_f32_16x16x64_bf16 v[80:83], v[60:63], v[184:191], v0\n" \
  "s_waitcnt lgkmcnt(8)\n" \
  "v_mfma_f32_16x16x32_bf16 v[116:119], v[88:91], v[24:27], 0\n" \
  "s_waitcnt lgkmcnt(7)\n" \
  "v_mfma_f32_16x16x32_bf16 v[116:119], v[92:95], v[28:31], v[116:119]\n" \
  "s_waitcnt lgkmcnt(6)\n" \
  "v_cvt_pk_bf16_f32 v80, v80, v81\n" \
  "v_mfma_f32_16x16x32_bf16 v[116:119], v[96:99], v[112:115], v[116:119]\n" \
  "s_waitcnt lgkmcnt(5)\n" \
  "v_cvt_pk_bf16_f32 v81, v82, v83\n" \
  "v_mfma_f32_16x16x32_bf16 v[116:119], v[100:103], v[64:67], v[116:119]\n" \
  "ds_write_b64 v6, v[80:81] offset:1536\n" \
  "s_waitcnt lgkmcnt(4)\n" \
  "v_mfma_f32_16x16x32_bf16 v[116:119], v[104:107], v[68:71], v[116:119]\n" \
  "s_waitcnt lgkmcnt(1)\n" \
  "v_mfma_f32_16x16x32_bf16 v[116:119], v[108:111], v[72:75], v[116:119]\n" \
  "v_smfmac_f32_16x16x64_bf16 v[84:87], v[32:35], v[192:199], v0\n" \
  "v_smfmac_f32_16x16x64_bf16 v[84:87], v[36:39], v[200:207], v0\n" \
  "v_add_f32_dpp v88, v116, v116 row_ror:8 row_mask:0xf bank_mask:0x3\n" \
  "v_add_f32_dpp v89, v117, v117 row_ror:8 row_mask:0xf bank_mask:0x3\n" \
  "v_smfmac_f32_16x16x64_bf16 v[84:87], v[40:43], v[208:215], v0\n" \
  "v_add_f32_dpp v88, v118, v118 row_ror:8 row_mask:0xf bank_mask:0xc\n" \
  "v_add_f32_dpp v89, v119, v119 row_ror:8 row_mask:0xf bank_mask:0xc\n" \
  "v_smfmac_f32_16x16x64_bf16 v[84:87], v[44:47], v[216:223], v0\n" \
  "v_mul_f32 v88, v4, v88\n" \
  "v_mul_f32 v89, v4, v89\n" \
  "v_smfmac_f32_16x16x64_bf16 v[84:87], v[48:51], v[224:231], v0\n" \
  "v_exp_f32 v88, v88\n" \
  "v_smfmac_f32_16x16x64_bf16 v[84:87], v[52:55], v[232:239], v0\n" \
  "v_exp_f32 v89, v89\n" \
  "v_smfmac_f32_16x16x64_bf16 v[84:87], v[56:59], v[240:247], v0\n" \
  "v_cmp_o_f32 vcc, v88, v89\n" \
  "v_cvt_pk_bf16_f32 v91, v88, v89\n" \
  "v_smfmac_f32_16x16x64_bf16 v[84:87], v[60:63], v[248:255], v0\n" \
  "s_mov_b64 exec, vcc\n" \
  "v_add_f32 v90, v88, v89\n" \
  "buffer_store_dword v91, v22, s[36:39], s72 offen offset:0 sc1\n" \
  "buffer_store_dword v91, v22, s[36:39], s44 offen offset:0 sc1\n" \
  "buffer_store_dword v91, v22, s[36:39], s45 offen offset:0 sc1\n" \
  "buffer_store_dword v91, v22, s[36:39], s46 offen offset:0 sc1\n" \
  "ds_write_b32 v12, v90 offset:0\n" \
  "s_mov_b64 exec, -1\n" \
  "v_cvt_pk_bf16_f32 v84, v84, v85\n" \
  "v_cvt_pk_bf16_f32 v85, v86, v87\n" \
  "ds_write_b64 v6, v[84:85] offset:4608\n" \
  "s_barrier ; timing only\n" \
  "v_mov_b32 v56, 0xf149f2ca\n" \
  "v_cmp_gt_i32 vcc, 0, v13\n" \
  "s_nop 1\n" \
  "v_cndmask_b32 v112, 0, v56, vcc\n" \
  "v_cmp_gt_i32 vcc, 1, v13\n" \
  "s_nop 1\n" \
  "v_cndmask_b32 v113, 0, v56, vcc\n" \
  "v_cmp_gt_i32 vcc, 2, v13\n" \
  "s_nop 1\n" \
  "v_cndmask_b32 v114, 0, v56, vcc\n" \
  "v_cmp_gt_i32 vcc, 3, v13\n" \
  "s_nop 1\n" \
  "v_cndmask_b32 v115, 0, v56, vcc\n" \
  "ds_read_b128 v[24:27], v18 offset:8192\n" \
  "ds_read_b128 v[64:67], v18 offset:0\n" \
  "ds_read_b128 v[28:31], v18 offset:10240\n" \
  "ds_read_b128 v[68:71], v18 offset:2048\n" \
  "ds_read_b128 v[32:35], v18 offset:12288\n" \
  "ds_read_b128 v[72:75], v18 offset:4096\n" \
  "ds_read_b128 v[36:39], v18 offset:14336\n" \
  "ds_read_b128 v[76:79], v18 offset:6144\n" \
  "s_waitcnt lgkmcnt(6)\n" \
  "v_mfma_f32_16x16x32_bf16 v[112:115], v[64:67], v[24:27], v[112:115]\n" \
  "ds_read_b64 v[40:41], v18 offset:20480\n" \
  "ds_read_b64 v[42:43], v18 offset:21504\n" \
  "ds_read_b64 v[80:81], v18 offset:16384\n" \
  "ds_read_b64 v[82:83], v18 offset:17408\n" \
  "s_waitcnt lgkmcnt(8)\n" \
  "v_mfma_f32_16x16x32_bf16 v[112:115], v[68:71], v[28:31], v[112:115]\n" \
  "ds_read_b64 v[44:45], v18 offset:22528\n" \
  "ds_read_b64 v[46:47], v18 offset:23552\n" \
  "ds_read_b64 v[84:85], v18 offset:18432\n" \
  "ds_read_b64 v[86:87], v18 offset:19456\n" \
  "s_waitcnt lgkmcnt(10)\n" \
  "v_mfma_f32_16x16x32_bf16 v[112:115], v[72:75], v[32:35], v[112:115]\n" \
  "s_waitcnt lgkmcnt(8)\n" \
  "v_mfma_f32_16x16x32_bf16 v[112:115], v[76:79], v[36:39], v[112:115]\n" \
  "s_waitcnt lgkmcnt(4)\n" \
  "v_mfma_f32_16x16x32_bf16 v[112:115], v[80:83], v[40:43], v[112:115]\n" \
  "s_waitcnt lgkmcnt(0)\n" \
  "v_mfma_f32_16x16x32_bf16 v[112:115], v[84:87], v[44:47], v[112:115]\n" \
  "s_barrier\n" \
  "ds_read_b32 v60, v12 offset:512\n" \
  "ds_read_b64_tr_b16 v[64:65], v9 offset:0\n" \
  "ds_read_b64_tr_b16 v[66:67], v9 offset:512\n" \
  "ds_read_b64_tr_b16 v[68:69], v9 offset:1024\n" \
  "ds_read_b64_tr_b16 v[70:71], v9 offset:1536\n" \
  "ds_read_b64_tr_b16 v[72:73], v9 offset:2048\n" \
  "ds_read_b64_tr_b16 v[74:75], v9 offset:2560\n" \
  "ds_read_b64_tr_b16 v[76:77], v9 offset:3072\n" \
  "ds_read_b64_tr_b16 v[78:79], v9 offset:3584\n" \
  "ds_read_b64_tr_b16 v[80:81], v9 offset:4096\n" \
  "ds_read_b64_tr_b16 v[82:83], v9 offset:4608\n" \
  "ds_read_b64_tr_b16 v[84:85], v9 offset:5120\n" \
  "ds_read_b64_tr_b16 v[86:87], v9 offset:5632\n" \
  "s_waitcnt lgkmcnt(12)\n" \
  "v_cmp_ne_u32 vcc, 0, v60\n" \
  "s_nop 3\n" \
  "s_cbranch_vccnz LMXs1_%=\n" \
  "LMYs1_%=:\n" \
  "s_waitcnt lgkmcnt(10)\n" \
  "v_mfma_f32_16x16x32_bf16 v[108:111], v[64:67], v[24:27], 0\n" \
  "s_waitcnt lgkmcnt(8)\n" \
  "v_mfma_f32_16x16x32_bf16 v[108:111], v[68:71], v[28:31], v[108:111]\n" \
  "s_waitcnt lgkmcnt(6)\n" \
  "v_mfma_f32_16x16x32_bf16 v[108:111], v[72:75], v[32:35], v[108:111]\n" \
  "s_waitcnt lgkmcnt(4)\n" \
  "v_mfma_f32_16x16x32_bf16 v[108:111], v[76:79], v[36:39], v[108:111]\n" \
  "s_waitcnt lgkmcnt(2)\n" \
  "v_mfma_f32_16x16x32_bf16 v[108:111], v[80:83], v[40:43], v[108:111]\n" \
  "s_waitcnt lgkmcnt(0)\n" \
  "v_mfma_f32_16x16x32_bf16 v[108:111], v[84:87], v[44:47], v[108:111]\n" \
  "s_nop 7\n" \
  "v_permlane32_swap_b32 v108, v110\n" \
  "v_permlane32_swap_b32 v109, v111\n" \
  "v_pk_add_f32 v[110:111], v[108:109], v[110:111]\n" \
  "v_mov_b32 v32, 0x3d93cd3a\n" \
  "v_mov_b32 v34, 0x3fb8aa3b\n" \
  "v_cvt_pk_bf16_f32 v24, v110, v111\n" \
  "v_cvt_pk_bf16_f32 v25, v112, v113\n" \
  "v_cvt_pk_bf16_f32 v26, v114, v115\n" \
  "v_lshlrev_b32 v110, 16, v24\n" \
  "v_and_b32 v111, 0xffff0000, v24\n" \
  "v_pk_mul_f32 v[110:111], v[110:111], v[32:33] op_sel_hi:[1,0]\n" \
  "v_cvt_pk_bf16_f32 v24, v110, v111\n" \
  "v_lshlrev_b32 v110, 16, v24\n" \
  "v_and_b32 v111, 0xffff0000, v24\n" \
  "v_lshlrev_b32 v112, 16, v25\n" \
  "v_and_b32 v113, 0xffff0000, v25\n" \
  "v_pk_mul_f32 v[112:113], v[112:113], v[32:33] op_sel_hi:[1,0]\n" \
  "v_cvt_pk_bf16_f32 v25, v112, v113\n" \
  "v_lshlrev_b32 v112, 16, v25\n" \
  "v_and_b32 v113, 0xffff0000, v25\n" \
  "v_lshlrev_b32 v114, 16, v26\n" \
  "v_and_b32 v115, 0xffff0000, v26\n" \
  "v_pk_mul_f32 v[114:115], v[114:115], v[32:33] op_sel_hi:[1,0]\n" \
  "v_cvt_pk_bf16_f32 v26, v114, v115\n" \
  "v_lshlrev_b32 v114, 16, v26\n" \
  "v_and_b32 v115, 0xffff0000, v26\n" \
  "v_max_f32 v24, v110, v111\n" \
  "v_max3_f32 v24, v24, v112, v113\n" \
  "v_max3_f32 v24, v24, v114, v115\n" \
  "v_mov_b32 v25, v24\n" \
  "s_nop 1\n" \
  "v_permlane16_swap_b32 v24, v25\n" \
  "v_max_f32 v24, v24, v25\n" \
  "v_mov_b32 v25, v24\n" \
  "s_nop 1\n" \
  "v_permlane32_swap_b32 v24, v25\n" \
  "v_max_f32 v24, v24, v25\n" \
  "v_pk_add_f32 v[110:111], v[110:111], v[24:25] op_sel_hi:[1,0] neg_lo:[0,1] neg_hi:[0,1]\n" \
  "v_pk_mul_f32 v[110:111], v[110:111], v[34:35] op_sel_hi:[1,0]\n" \
  "v_pk_add_f32 v[112:113], v[112:113], v[24:25] op_sel_hi:[1,0] neg_lo:[0,1] neg_hi:[0,1]\n" \
  "v_pk_mul_f32 v[112:113], v[112:113], v[34:35] op_sel_hi:[1,0]\n" \
  "v_pk_add_f32 v[114:115], v[114:115], v[24:25] op_sel_hi:[1,0] neg_lo:[0,1] neg_hi:[0,1]\n" \
  "v_pk_mul_f32 v[114:115], v[114:115], v[34:35] op_sel_hi:[1,0]\n" \
  "v_exp_f32 v110, v110\n" \
  "v_exp_f32 v111, v111\n" \
  "v_exp_f32 v112, v112\n" \
  "v_exp_f32 v113, v113\n" \
  "v_exp_f32 v114, v114\n" \
  "v_exp_f32 v115, v115\n" \
  "v_pk_add_f32 v[24:25], v[110:111], v[112:113]\n" \
  "v_pk_add_f32 v[24:25], v[24:25], v[114:115]\n" \
  "v_add_f32 v24, v24, v25\n" \
  "v_mov_b32 v25, v24\n" \
  "s_nop 1\n" \
  "v_permlane16_swap_b32 v24, v25\n" \
  "v_add_f32 v24, v24, v25\n" \
  "v_mov_b32 v25, v24\n" \
  "s_nop 1\n" \
  "v_permlane32_swap_b32 v24, v25\n" \
  "v_add_f32 v24, v24, v25\n" \
  "v_rcp_f32 v24, v24\n" \
  "v_mov_b32 v54, 0\n" \
  "v_mov_b32 v55, 0\n" \
  "v_pk_mul_f32 v[110:111], v[110:111], v[24:25] op_sel_hi:[1,0]\n" \
  "v_pk_mul_f32 v[112:113], v[112:113], v[24:25] op_sel_hi:[1,0]\n" \
  "v_pk_mul_f32 v[114:115], v[114:115], v[24:25] op_sel_hi:[1,0]\n" \
  "v_cvt_pk_bf16_f32 v52, v112, v113\n" \
  "v_cvt_pk_bf16_f32 v53, v114, v115\n" \
  "v_cvt_pk_bf16_f32 v56, v110, v111\n" \
  "s_sub_u32 s74, s68, 1\n" \
  "v_readlane_b32 s42, v15, s74\n" \
  "v_xor_b32 v20, 0x8000, v20\n" \
  "v_xor_b32 v21, 0x8000, v21\n" \
  "v_xor_b32 v18, 0x8000, v18\n" \
  "s_cmp_eq_u32 s68, 64\n" \
  "s_cbranch_scc1 LENDs1_%=\n" \
  "v_readlane_b32 s72, v11, s68\n" \
  "s_nop 0\n" \
  "s_add_u32 s69, s72, 3072\n" \
  "s_add_u32 s70, s72, 7168\n" \
  "s_add_u32 s44, s72, s93\n" \
  "s_add_u32 s45, s72, s94\n" \
  "s_add_u32 s46, s72, s95\n" \
  "v_readlane_b32 s41, v7, s68\n" \
  "s_nop 1\n" \
  "s_branch LOOPs1_%=\n" \
  "LMFs1_%=:\n" \
  "ds_read_b128 v[88:91], v3 offset:0\n" \
  "ds_read_b128 v[92:95], v3 offset:1024\n" \
  "ds_read_b128 v[96:99], v3 offset:2048\n" \
  "ds_read_b128 v[100:103], v3 offset:3072\n" \
  "ds_read_b128 v[104:107], v3 offset:4096\n" \
  "ds_read_b128 v[108:111], v3 offset:5120\n" \
  "ds_read_b128 v[112:115], v3 offset:6144\n" \
  "ds_read_b128 v[116:119], v3 offset:7168\n" \
  "v_mov_b32 v60, -1\n" \
  "v_mov_b32 v61, -1\n" \
  "ds_write_b64 v6, v[60:61] offset:33792\n" \
  "ds_write_b64 v6, v[60:61] offset:34304\n" \
  "ds_write_b64 v6, v[60:61] offset:37376\n" \
  "v_mov_b32 v64, 0\n" \
  "v_mov_b32 v65, 0\n" \
  "v_mov_b32 v66, 0\n" \
  "v_mov_b32 v67, 0\n" \
  "v_mov_b32 v68, 0\n" \
  "v_mov_b32 v69, 0\n" \
  "v_mov_b32 v70, 0\n" \
  "v_mov_b32 v71, 0\n" \
  "v_mov_b32 v72, 0\n" \
  "v_mov_b32 v73, 0\n" \
  "v_mov_b32 v74, 0\n" \
  "v_mov_b32 v75, 0\n" \
  "v_mov_b32 v76, 0\n" \
  "v_mov_b32 v77, 0\n" \
  "v_mov_b32 v78, 0\n" \
  "v_mov_b32 v79, 0\n" \
  "v_mov_b32 v80, 0\n" \
  "v_mov_b32 v81, 0\n" \
  "v_mov_b32 v82, 0\n" \
  "v_mov_b32 v83, 0\n" \
  "v_mov_b32 v84, 0\n" \
  "v_mov_b32 v85, 0\n" \
  "v_mov_b32 v86, 0\n" \
  "v_mov_b32 v87, 0\n" \
  "s_waitcnt lgkmcnt(0)\n" \
  "s_barrier\n" \
  "buffer_load_dwordx4 v[24:27], v1, s[36:39], s72 offen offset:0 sc1\n" \
  "s_mov_b32 m0, 66560\n" \
  "s_nop 0\n" \
  "buffer_load_dwordx4 v1, s[36:39], s69 offen sc1 lds\n" \
  "buffer_load_dwordx4 v[28:31], v1, s[36:39], s72 offen offset:1024 sc1\n" \
  "s_mov_b32 m0, 70656\n" \
  "s_nop 0\n" \
  "buffer_load_dwordx4 v1, s[36:39], s70 offen sc1 lds\n" \
  "ds_write_b64 v6, v[60:61] offset:1024\n" \
  "ds_write_b64 v6, v[60:61] offset:1536\n" \
  "ds_write_b64 v6, v[60:61] offset:4608\n" \
  "ds_write_b32 v12, v60 offset:0\n" \
  "s_branch LMs1_%=\n" \
  "LMQs1_%=:\n" \
  "s_waitcnt lgkmcnt(0)\n" \
  "s_barrier\n" \
  "s_barrier\n" \
  "ds_read_b32 v60, v12 offset:512\n" \
  "s_waitcnt lgkmcnt(0)\n" \
  "LMXs1_%=:\n" \
  "v_cmp_eq_u32 vcc, 2, v60\n" \
  "s_waitcnt vmcnt(0) lgkmcnt(0)\n" \
  "s_barrier\n" \
  "s_cbranch_vccnz LMFs1_%=\n" \
  "s_branch LMYs1_%=\n" \
  "LENDs1_%=:\n" \
  "s_cmp_lg_u32 s38, 0\n" \
  "s_cbranch_scc1 LFINs1_%=\n" \
  "s_waitcnt vmcnt(0) lgkmcnt(0)\n" \
  "s_mov_b32 s38, s47\n" \
  "s_mov_b32 s68, 0\n" \
  "s_mov_b32 s73, 0\n" \
  "s_mov_b32 s56, 0\n" \
  "s_mov_b32 s57, 0\n" \
  "s_branch LPASSs1_%=\n" \
  "LFINs1_%=:\n" \
  "s_nop 1\n" \
  "buffer_store_dwordx4 v[52:55], v16, s[36:39], s42 offen sc1\n" \
  "buffer_store_dword v56, v17, s[36:39], s42 offen sc1\n" \
  "s_waitcnt vmcnt(0)\n" \
  "v_mov_b32 v19, 1\n" \
  "buffer_atomic_add v19, v14, s[36:39], 0 offen sc1\n" \
  "s_branch LDONE_%=\n" \
  "LSTARTs2_%=:\n" \
  "s_mov_b32 s47, s38\n" \
  "s_mov_b32 s38, 0\n" \
  "s_mov_b32 s68, 63\n" \
  "s_mov_b32 s73, 20001\n" \
  "s_mov_b32 s56, 19998\n" \
  "LPASSs2_%=:\n" \
  "s_mov_b32 s40, 0\n" \
  "s_mov_b32 s42, 0x40000000\n" \
  "v_and_b32 v20, 0x7fff, v20\n" \
  "v_and_b32 v21, 0x7fff, v21\n" \
  "v_and_b32 v18, 0x7fff, v18\n" \
  "s_barrier\n" \
  "s_add_u32 m0, s40, 12288\n" \
  "s_add_u32 s75, s55, 20480\n" \
  "buffer_load_dwordx4 v8, s[36:39], s75 offen offset:0 sc1 lds\n" \
  "buffer_load_dwordx4 v8, s[36:39], s75 offen offset:1024 sc1 lds\n" \
  "buffer_load_dwordx4 v8, s[36:39], s75 offen offset:2048 sc1 lds\n" \
  "buffer_load_dwordx4 v8, s[36:39], s75 offen offset:3072 sc1 lds\n" \
  "s_add_u32 m0, s40, 16384\n" \
  "s_add_u32 s75, s55, 32768\n" \
  "buffer_load_dwordx4 v8, s[36:39], s75 offen offset:0 sc1 lds\n" \
  "buffer_load_dwordx4 v8, s[36:39], s75 offen offset:1024 sc1 lds\n" \
  "s_xor_b32 s40, s40, 0x8000\n" \
  "v_readlane_b32 s72, v11, 0\n" \
  "s_nop 0\n" \
  "s_add_u32 s69, s72, 4096\n" \
  "s_add_u32 s70, s72, 16384\n" \
  "s_add_u32 s44, s72, s93\n" \
  "s_add_u32 s45, s72, s94\n" \
  "s_add_u32 s46, s72, s95\n" \
  "v_readlane_b32 s41, v7, 0\n" \
  "s_nop 1\n" \
  "LOOPs2_%=:\n" \
  "ds_read_b128 v[88:91], v3 offset:0\n" \
  "ds_read_b128 v[92:95], v3 offset:1024\n" \
  "ds_read_b128 v[96:99], v3 offset:2048\n" \
  "ds_read_b128 v[100:103], v3 offset:3072\n" \
  "ds_read_b128 v[104:107], v3 offset:4096\n" \
  "ds_read_b128 v[108:111], v3 offset:5120\n" \
  "ds_read_b128 v[112:115], v3 offset:6144\n" \
  "ds_read_b128 v[116:119], v3 offset:7168\n" \
  "v_mov_b32 v60, -1\n" \
  "v_mov_b32 v61, -1\n" \
  "ds_write_b64 v6, v[60:61] offset:34816\n" \
  "ds_write_b64 v6, v[60:61] offset:35328\n" \
  "ds_write_b64 v6, v[60:61] offset:37888\n" \
  "v_mov_b32 v64, 0\n" \
  "v_mov_b32 v65, 0\n" \
  "v_mov_b32 v66, 0\n" \
  "v_mov_b32 v67, 0\n" \
  "v_mov_b32 v68, 0\n" \
  "v_mov_b32 v69, 0\n" \
  "v_mov_b32 v70, 0\n" \
  "v_mov_b32 v71, 0\n" \
  "v_mov_b32 v72, 0\n" \
  "v_mov_b32 v73, 0\n" \
  "v_mov_b32 v74, 0\n" \
  "v_mov_b32 v75, 0\n" \
  "v_mov_b32 v76, 0\n" \
  "v_mov_b32 v77, 0\n" \
  "v_mov_b32 v78, 0\n" \
  "v_mov_b32 v79, 0\n" \
  "v_mov_b32 v80, 0\n" \
  "v_mov_b32 v81, 0\n" \
  "v_mov_b32 v82, 0\n" \
  "v_mov_b32 v83, 0\n" \
  "v_mov_b32 v84, 0\n" \
  "v_mov_b32 v85, 0\n" \
  "v_mov_b32 v86, 0\n" \
  "v_mov_b32 v87, 0\n" \
  "s_waitcnt lgkmcnt(0)\n" \
  "s_barrier\n" \
  "buffer_load_dwordx4 v[24:27], v1, s[36:39], s72 offen offset:0 sc1\n" \
  "s_mov_b32 m0, 67584\n" \
  "s_nop 0\n" \
  "buffer_load_dwordx4 v1, s[36:39], s69 offen sc1 lds\n" \
  "buffer_load_dwordx4 v[28:31], v1, s[36:39], s72 offen offset:1024 sc1\n" \
  "s_mov_b32 m0, 71680\n" \
  "s_nop 0\n" \
  "buffer_load_dwordx4 v1, s[36:39], s70 offen sc1 lds\n" \
  "ds_write_b64 v6, v[60:61] offset:2048\n" \
  "ds_write_b64 v6, v[60:61] offset:2560\n" \
  "ds_write_b64 v6, v[60:61] offset:5120\n" \
  "ds_write_b32 v12, v60 offset:256\n" \
  "s_add_u32 s68, s68, 1\n" \
  "LMs2_%=:\n" \
  "buffer_store_dwordx4 v[52:55], v16, s[36:39], s42 offen sc1\n" \
  "buffer_store_dword v56, v17, s[36:39], s42 offen sc1\n" \
  "s_mov_b32 s42, 0x40000000\n" \
  "s_waitcnt vmcnt(5)\n" \
  "v_smfmac_f32_16x16x64_bf16 v[64:67], v[24:27], v[88:95], v0\n" \
  "ds_read_b128 v[88:91], v3 offset:8192\n" \
  "v_smfmac_f32_16x16x64_bf16 v[68:71], v[24:27], v[96:103], v0\n" \
  "ds_read_b128 v[92:95], v3 offset:9216\n" \
  "v_smfmac_f32_16x16x64_bf16 v[72:75], v[24:27], v[104:111], v0\n" \
  "ds_read_b128 v[96:99], v3 offset:10240\n" \
  "v_smfmac_f32_16x16x64_bf16 v[76:79], v[24:27], v[112:119], v0\n" \
  "ds_read_b128 v[100:103], v3 offset:11264\n" \
  "v_smfmac_f32_16x16x64_bf16 v[80:83], v[24:27], a[0:7], v0\n" \
  "ds_read_b128 v[104:107], v3 offset:12288\n" \
  "v_smfmac_f32_16x16x64_bf16 v[84:87], v[24:27], a[8:15], v0\n" \
  "s_waitcnt vmcnt(4)\n" \
  "s_waitcnt lgkmcnt(5)\n" \
  "s_barrier\n" \
  "ds_read_b128 v[32:35], v2 offset:2048\n" \
  "s_waitcnt vmcnt(3)\n" \
  "v_smfmac_f32_16x16x64_bf16 v[64:67], v[28:31], a[16:23], v0\n" \
  "ds_read_b128 v[36:39], v2 offset:3072\n" \
  "v_smfmac_f32_16x16x64_bf16 v[68:71], v[28:31], a[24:31], v0\n" \
  "ds_read_b128 v[40:43], v2 offset:4096\n" \
  "s_waitcnt lgkmcnt(6)\n" \
  "v_smfmac_f32_16x16x64_bf16 v[72:75], v[28:31], v[88:95], v0\n" \
  "ds_read_b128 v[44:47], v2 offset:5120\n" \
  "v_smfmac_f32_16x16x64_bf16 v[76:79], v[28:31], a[32:39], v0\n" \
  "ds_read_b128 v[108:111], v3 offset:13312\n" \
  "v_smfmac_f32_16x16x64_bf16 v[80:83], v[28:31], a[40:47], v0\n" \
  "ds_read_b128 v[112:115], v3 offset:14336\n" \
  "v_smfmac_f32_16x16x64_bf16 v[84:87], v[28:31], a[48:55], v0\n" \
  "ds_read_b128 v[116:119], v3 offset:15360\n" \
  "s_waitcnt lgkmcnt(6)\n" \
  "v_smfmac_f32_16x16x64_bf16 v[64:67], v[32:35], a[56:63], v0\n" \
  "ds_read_b128 v[88:91], v3 offset:16384\n" \
  "v_smfmac_f32_16x16x64_bf16 v[68:71], v[32:35], v[96:103], v0\n" \
  "ds_read_b128 v[92:95], v3 offset:17408\n" \
  "v_smfmac_f32_16x16x64_bf16 v[72:75], v[32:35], a[64:71], v0\n" \
  "ds_read_b128 v[96:99], v3 offset:18432\n" \
  "s_waitcnt lgkmcnt(8)\n" \
  "v_smfmac_f32_16x16x64_bf16 v[64:67], v[36:39], a[72:79], v0\n" \
  "s_waitcnt vmcnt(2)\n" \
  "s_barrier\n" \
  "ds_read_b128 v[100:103], v3 offset:19456\n" \
  "v_smfmac_f32_16x16x64_bf16 v[68:71], v[36:39], a[80:87], v0\n" \
  "ds_read_b128 v[48:51], v2 offset:6144\n" \
  "v_smfmac_f32_16x16x64_bf16 v[72:75], v[36:39], a[88:95], v0\n" \
  "ds_read_b128 v[52:55], v2 offset:7168\n" \
  "s_waitcnt lgkmcnt(8)\n" \
  "v_smfmac_f32_16x16x64_bf16 v[64:67], v[40:43], v[104:111], v0\n" \
  "ds_read_b128 v[56:59], v2 offset:8192\n" \
  "v_smfmac_f32_16x16x64_bf16 v[68:71], v[40:43], a[96:103], v0\n" \
  "ds_read_b128 v[60:63], v2 offset:9216\n" \
  "s_waitcnt lgkmcnt(8)\n" \
  "v_smfmac_f32_16x16x64_bf16 v[72:75], v[40:43], v[112:119], v0\n" \
  "ds_read_b128 v[104:107], v3 offset:20480\n" \
  "v_smfmac_f32_16x16x64_bf16 v[64:67], v[44:47], a[104:111], v0\n" \
  "ds_read_b128 v[108:111], v3 offset:21504\n" \
  "s_waitcnt lgkmcnt(8)\n" \
  "v_smfmac_f32_16x16x64_bf16 v[68:71], v[44:47], v[88:95], v0\n" \
  "ds_read_b128 v[88:91], v20 offset:0\n" \
  "v_smfmac_f32_16x16x64_bf16 v[72:75], v[44:47], a[112:119], v0\n" \
  "ds_read_b128 v[92:95], v20 offset:2048\n" \
  "s_waitcnt lgkmcnt(7)\n" \
  "v_smfmac_f32_16x16x64_bf16 v[64:67], v[48:51], a[120:127], v0\n" \
  "s_waitcnt lgkmcnt(6)\n" \
  "v_smfmac_f32_16x16x64_bf16 v[64:67], v[52:55], a[128:135], v0\n" \
  "s_waitcnt lgkmcnt(5)\n" \
  "v_smfmac_f32_16x16x64_bf16 v[64:67], v[56:59], a[136:143], v0\n" \
  "s_waitcnt lgkmcnt(4)\n" \
  "v_smfmac_f32_16x16x64_bf16 v[64:67], v[60:63], a[144:151], v0\n" \
  "v_smfmac_f32_16x16x64_bf16 v[68:71], v[48:51], v[96:103], v0\n" \
  "ds_read_b128 v[96:99], v20 offset:4096\n" \
  "v_smfmac_f32_16x16x64_bf16 v[68:71], v[52:55], a[152:159], v0\n" \
  "ds_read_b128 v[100:103], v20 offset:6144\n" \
  "s_waitcnt lgkmcnt(4)\n" \
  "v_cmp_u_f32 vcc, v64, v65\n" \
  "v_cmp_u_f32 s[82:83], v66, v67\n" \
  "v_smfmac_f32_16x16x64_bf16 v[68:71], v[56:59], v[104:111], v0\n" \
  "s_or_b64 vcc, vcc, s[82:83]\n" \
  "s_cbranch_scc1 LMQs2_%=\n" \
  "ds_read_b64 v[104:105], v21 offset:0\n" \
  "v_cvt_pk_bf16_f32 v64, v64, v65\n" \
  "v_smfmac_f32_16x16x64_bf16 v[68:71], v[60:63], a[160:167], v0\n" \
  "ds_read_b64 v[106:107], v21 offset:1024\n" \
  "v_cvt_pk_bf16_f32 v65, v66, v67\n" \
  "v_smfmac_f32_16x16x64_bf16 v[72:75], v[48:51], a[168:175], v0\n" \
  "ds_write_b64 v6, v[64:65] offset:34816\n" \
  "v_smfmac_f32_16x16x64_bf16 v[72:75], v[52:55], a[176:183], v0\n" \
  "ds_read_b64 v[108:109], v21 offset:2048\n" \
  "v_cvt_pk_bf16_f32 v68, v68, v69\n" \
  "v_smfmac_f32_16x16x64_bf16 v[72:75], v[56:59], a[184:191], v0\n" \
  "ds_read_b64 v[110:111], v21 offset:3072\n" \
  "v_cvt_pk_bf16_f32 v69, v70, v71\n" \
  "v_smfmac_f32_16x16x64_bf16 v[72:75], v[60:63], a[192:199], v0\n" \
  "ds_write_b64 v6, v[68:69] offset:35328\n" \
  "v_smfmac_f32_16x16x64_bf16 v[76:79], v[32:35], a[200:207], v0\n" \
  "ds_read_b64_tr_b16 v[24:25], v9 offset:32768\n" \
  "v_smfmac_f32_16x16x64_bf16 v[76:79], v[36:39], a[208:215], v0\n" \
  "ds_read_b64_tr_b16 v[28:29], v9 offset:33792\n" \
  "v_cvt_pk_bf16_f32 v72, v72, v73\n" \
  "v_smfmac_f32_16x16x64_bf16 v[76:79], v[40:43], a[216:223], v0\n" \
  "ds_read_b64_tr_b16 v[112:113], v9 offset:34816\n" \
  "v_cvt_pk_bf16_f32 v73, v74, v75\n" \
  "v_smfmac_f32_16x16x64_bf16 v[76:79], v[44:47], a[224:231], v0\n" \
  "ds_write_b64 v6, v[72:73] offset:37888\n" \
  "v_smfmac_f32_16x16x64_bf16 v[76:79], v[48:51], a[232:239], v0\n" \
  "ds_read_b64_tr_b16 v[64:65], v9 offset:35840\n" \
  "v_smfmac_f32_16x16x64_bf16 v[76:79], v[52:55], a[240:247], v0\n" \
  "s_waitcnt lgkmcnt(8)\n" \
  "ds_read_b64_tr_b16 v[26:27], v9 offset:33280\n" \
  "v_smfmac_f32_16x16x64_bf16 v[76:79], v[56:59], a[248:255], v0\n" \
  "ds_read_b64_tr_b16 v[30:31], v9 offset:34304\n" \
  "v_smfmac_f32_16x16x64_bf16 v[76:79], v[60:63], v[120:127], v0\n" \
  "ds_read_b64_tr_b16 v[114:115], v9 offset:35328\n" \
  "v_smfmac_f32_16x16x64_bf16 v[80:83], v[32:35], v[128:135], v0\n" \
  "ds_read_b64_tr_b16 v[66:67], v9 offset:36352\n" \
  "v_smfmac_f32_16x16x64_bf16 v[80:83], v[36:39], v[136:143], v0\n" \
  "ds_read_b64_tr_b16 v[68:69], v9 offset:36864\n" \
  "v_cvt_pk_bf16_f32 v76, v76, v77\n" \
  "v_smfmac_f32_16x16x64_bf16 v[80:83], v[40:43], v[144:151], v0\n" \
  "ds_read_b64_tr_b16 v[70:71], v9 offset:37376\n" \
  "v_cvt_pk_bf16_f32 v77, v78, v79\n" \
  "v_smfmac_f32_16x16x64_bf16 v[80:83], v[44:47], v[152:159], v0\n" \
  "ds_write_b64 v6, v[76:77] offset:2048\n" \
  "v_smfmac_f32_16x16x64_bf16 v[80:83], v[48:51], v[160:167], v0\n" \
  "s_waitcnt lgkmcnt(8)\n" \
  "ds_read_b64_tr_b16 v[72:73], v9 offset:37888\n" \
  "v_smfmac_f32_16x16x64_bf16 v[80:83], v[52:55], v[168:175], v0\n" \
  "ds_read_b64_tr_b16 v[74:75], v9 offset:38400\n" \
  "v_smfmac_f32_16x16x64_bf16 v[80:83], v[56:59], v[176:183], v0\n" \
  "v_smfmac_f32_16x16x64_bf16 v[80:83], v[60:63], v[184:191], v0\n" \
  "s_waitcnt lgkmcnt(8)\n" \
  "v_mfma_f32_16x16x32_bf16 v[116:119], v[88:91], v[24:27], 0\n" \
  "s_waitcnt lgkmcnt(7)\n" \
  "v_mfma_f32_16x16x32_bf16 v[116:119], v[92:95], v[28:31], v[116:119]\n" \
  "s_waitcnt lgkmcnt(6)\n" \
  "v_cvt_pk_bf16_f32 v80, v80, v81\n" \
  "v_mfma_f32_16x16x32_bf16 v[116:119], v[96:99], v[112:115], v[116:119]\n" \
  "s_waitcnt lgkmcnt(5)\n" \
  "v_cvt_pk_bf16_f32 v81, v82, v83\n" \
  "v_mfma_f32_16x16x32_bf16 v[116:119], v[100:103], v[64:67], v[116:119]\n" \
  "ds_write_b64 v6, v[80:81] offset:2560\n" \
  "s_waitcnt lgkmcnt(4)\n" \
  "v_mfma_f32_16x16x32_bf16 v[116:119], v[104:107], v[68:71], v[116:119]\n" \
  "s_waitcnt lgkmcnt(1)\n" \
  "v_mfma_f32_16x16x32_bf16 v[116:119], v[108:111], v[72:75], v[116:119]\n" \
  "v_smfmac_f32_16x16x64_bf16 v[84:87], v[32:35], v[192:199], v0\n" \
  "v_smfmac_f32_16x16x64_bf16 v[84:87], v[36:39], v[200:207], v0\n" \
  "v_add_f32_dpp v88, v116, v116 row_ror:8 row_mask:0xf bank_mask:0x3\n" \
  "v_add_f32_dpp v89, v117, v117 row_ror:8 row_mask:0xf bank_mask:0x3\n" \
  "v_smfmac_f32_16x16x64_bf16 v[84:87], v[40:43], v[208:215], v0\n" \
  "v_add_f32_dpp v88, v118, v118 row_ror:8 row_mask:0xf bank_mask:0xc\n" \
  "v_add_f32_dpp v89, v119, v119 row_ror:8 row_mask:0xf bank_mask:0xc\n" \
  "v_smfmac_f32_16x16x64_bf16 v[84:87], v[44:47], v[216:223], v0\n" \
  "v_mul_f32 v88, v4, v88\n" \
  "v_mul_f32 v89, v4, v89\n" \
  "v_smfmac_f32_16x16x64_bf16 v[84:87], v[48:51], v[224:231], v0\n" \
  "v_exp_f32 v88, v88\n" \
  "v_smfmac_f32_16x16x64_bf16 v[84:87], v[52:55], v[232:239], v0\n" \
  "v_exp_f32 v89, v89\n" \
  "v_smfmac_f32_16x16x64_bf16 v[84:87], v[56:59], v[240:247], v0\n" \
  "v_cmp_o_f32 vcc, v88, v89\n" \
  "v_cvt_pk_bf16_f32 v91, v88, v89\n" \
  "v_smfmac_f32_16x16x64_bf16 v[84:87], v[60:63], v[248:255], v0\n" \
  "s_mov_b64 exec, vcc\n" \
  "v_add_f32 v90, v88, v89\n" \
  "buffer_store_dword v91, v22, s[36:39], s72 offen offset:0 sc1\n" \
  "buffer_store_dword v91, v22, s[36:39], s44 offen offset:0 sc1\n" \
  "buffer_store_dword v91, v22, s[36:39], s45 offen offset:0 sc1\n" \
  "buffer_store_dword v91, v22, s[36:39], s46 offen offset:0 sc1\n" \
  "ds_write_b32 v12, v90 offset:256\n" \
  "s_mov_b64 exec, -1\n" \
  "v_cvt_pk_bf16_f32 v84, v84, v85\n" \
  "v_cvt_pk_bf16_f32 v85, v86, v87\n" \
  "ds_write_b64 v6, v[84:85] offset:5120\n" \
  "s_barrier ; timing only\n" \
  "v_mov_b32 v56, 0xf149f2ca\n" \
  "v_cmp_gt_i32 vcc, 0, v13\n" \
  "s_nop 1\n" \
  "v_cndmask_b32 v116, 0, v56, vcc\n" \
  "v_cmp_gt_i32 vcc, 1, v13\n" \
  "s_nop 1\n" \
  "v_cndmask_b32 v117, 0, v56, vcc\n" \
  "v_cmp_gt_i32 vcc, 2, v13\n" \
  "s_nop 1\n" \
  "v_cndmask_b32 v118, 0, v56, vcc\n" \
  "v_cmp_gt_i32 vcc, 3, v13\n" \
  "s_nop 1\n" \
  "v_cndmask_b32 v119, 0, v56, vcc\n" \
  "ds_read_b128 v[24:27], v18 offset:9216\n" \
  "ds_read_b128 v[64:67], v18 offset:0\n" \
  "ds_read_b128 v[88:91], v18 offset:1024\n" \
  "ds_read_b128 v[28:31], v18 offset:11264\n" \
  "ds_read_b128 v[68:71], v18 offset:2048\n" \
  "ds_read_b128 v[92:95], v18 offset:3072\n" \
  "ds_read_b128 v[32:35], v18 offset:13312\n" \
  "ds_read_b128 v[72:75], v18 offset:4096\n" \
  "ds_read_b128 v[96:99], v18 offset:5120\n" \
  "ds_read_b128 v[36:39], v18 offset:15360\n" \
  "ds_read_b128 v[76:79], v18 offset:6144\n" \
  "ds_read_b128 v[100:103], v18 offset:7168\n" \
  "s_waitcnt lgkmcnt(10)\n" \
  "v_mfma_f32_16x16x32_bf16 v[112:115], v[64:67], v[24:27], 0\n" \
  "s_waitcnt lgkmcnt(9)\n" \
  "v_mfma_f32_16x16x32_bf16 v[116:119], v[88:91], v[24:27], v[116:119]\n" \
  "ds_read_b64 v[40:41], v18 offset:20488\n" \
  "ds_read_b64 v[42:43], v18 offset:21512\n" \
  "ds_read_b64 v[80:81], v18 offset:16384\n" \
  "ds_read_b64 v[82:83], v18 offset:17408\n" \
  "ds_read_b64 v[104:105], v18 offset:16392\n" \
  "ds_read_b64 v[106:107], v18 offset:17416\n" \
  "s_waitcnt lgkmcnt(13)\n" \
  "v_mfma_f32_16x16x32_bf16 v[112:115], v[68:71], v[28:31], v[112:115]\n" \
  "s_waitcnt lgkmcnt(12)\n" \
  "v_mfma_f32_16x16x32_bf16 v[116:119], v[92:95], v[28:31], v[116:119]\n" \
  "ds_read_b64 v[44:45], v18 offset:22536\n" \
  "ds_read_b64 v[46:47], v18 offset:23560\n" \
  "ds_read_b64 v[84:85], v18 offset:18432\n" \
  "s_waitcnt lgkmcnt(8)\n" \
  "ds_read_b64 v[86:87], v18 offset:19456\n" \
  "ds_read_b64 v[88:89], v18 offset:18440\n" \
  "ds_read_b64 v[90:91], v18 offset:19464\n" \
  "v_mfma_f32_16x16x32_bf16 v[112:115], v[72:75], v[32:35], v[112:115]\n" \
  "v_mfma_f32_16x16x32_bf16 v[116:119], v[96:99], v[32:35], v[116:119]\n" \
  "v_mfma_f32_16x16x32_bf16 v[112:115], v[76:79], v[36:39], v[112:115]\n" \
  "v_mfma_f32_16x16x32_bf16 v[116:119], v[100:103], v[36:39], v[116:119]\n" \
  "s_waitcnt lgkmcnt(8)\n" \
  "v_mfma_f32_16x16x32_bf16 v[112:115], v[80:83], v[40:43], v[112:115]\n" \
  "s_waitcnt lgkmcnt(6)\n" \
  "v_mfma_f32_16x16x32_bf16 v[116:119], v[104:107], v[40:43], v[116:119]\n" \
  "s_waitcnt lgkmcnt(2)\n" \
  "v_mfma_f32_16x16x32_bf16 v[112:115], v[84:87], v[44:47], v[112:115]\n" \
  "s_waitcnt lgkmcnt(0)\n" \
  "v_mfma_f32_16x16x32_bf16 v[116:119], v[88:91], v[44:47], v[116:119]\n" \
  "s_barrier\n" \
  "ds_read_b32 v60, v12 offset:512\n" \
  "ds_read_b64_tr_b16 v[64:65], v9 offset:0\n" \
  "ds_read_b64_tr_b16 v[66:67], v9 offset:512\n" \
  "ds_read_b64_tr_b16 v[68:69], v9 offset:1024\n" \
  "ds_read_b64_tr_b16 v[70:71], v9 offset:1536\n" \
  "ds_read_b64_tr_b16 v[72:73], v9 offset:2048\n" \
  "ds_read_b64_tr_b16 v[74:75], v9 offset:2560\n" \
  "ds_read_b64_tr_b16 v[76:77], v9 offset:3072\n" \
  "ds_read_b64_tr_b16 v[78:79], v9 offset:3584\n" \
  "ds_read_b64_tr_b16 v[80:81], v9 offset:4096\n" \
  "ds_read_b64_tr_b16 v[82:83], v9 offset:4608\n" \
  "ds_read_b64_tr_b16 v[84:85], v9 offset:5120\n" \
  "ds_read_b64_tr_b16 v[86:87], v9 offset:5632\n" \
  "s_waitcnt lgkmcnt(12)\n" \
  "v_cmp_ne_u32 vcc, 0, v60\n" \
  "s_nop 3\n" \
  "s_cbranch_vccnz LMXs2_%=\n" \
  "LMYs2_%=:\n" \
  "s_waitcnt lgkmcnt(10)\n" \
  "v_mfma_f32_16x16x32_bf16 v[108:111], v[64:67], v[24:27], 0\n" \
  "s_waitcnt lgkmcnt(8)\n" \
  "v_mfma_f32_16x16x32_bf16 v[108:111], v[68:71], v[28:31], v[108:111]\n" \
  "s_waitcnt lgkmcnt(6)\n" \
  "v_mfma_f32_16x16x32_bf16 v[108:111], v[72:75], v[32:35], v[108:111]\n" \
  "s_waitcnt lgkmcnt(4)\n" \
  "v_mfma_f32_16x16x32_bf16 v[108:111], v[76:79], v[36:39], v[108:111]\n" \
  "s_waitcnt lgkmcnt(2)\n" \
  "v_mfma_f32_16x16x32_bf16 v[108:111], v[80:83], v[40:43], v[108:111]\n" \
  "s_waitcnt lgkmcnt(0)\n" \
  "v_mfma_f32_16x16x32_bf16 v[108:111], v[84:87], v[44:47], v[108:111]\n" \
  "s_nop 7\n" \
  "v_permlane32_swap_b32 v108, v110\n" \
  "v_permlane32_swap_b32 v109, v111\n" \
  "v_pk_add_f32 v[110:111], v[108:109], v[110:111]\n" \
  "v_mov_b32 v32, 0x3d93cd3a\n" \
  "v_mov_b32 v34, 0x3fb8aa3b\n" \
  "v_cvt_pk_bf16_f32 v24, v110, v111\n" \
  "v_cvt_pk_bf16_f32 v25, v112, v113\n" \
  "v_cvt_pk_bf16_f32 v26, v114, v115\n" \
  "v_cvt_pk_bf16_f32 v27, v116, v117\n" \
  "v_cvt_pk_bf16_f32 v28, v118, v119\n" \
  "v_lshlrev_b32 v110, 16, v24\n" \
  "v_and_b32 v111, 0xffff0000, v24\n" \
  "v_pk_mul_f32 v[110:111], v[110:111], v[32:33] op_sel_hi:[1,0]\n" \
  "v_cvt_pk_bf16_f32 v24, v110, v111\n" \
  "v_lshlrev_b32 v110, 16, v24\n" \
  "v_and_b32 v111, 0xffff0000, v24\n" \
  "v_lshlrev_b32 v112, 16, v25\n" \
  "v_and_b32 v113, 0xffff0000, v25\n" \
  "v_pk_mul_f32 v[112:113], v[112:113], v[32:33] op_sel_hi:[1,0]\n" \
  "v_cvt_pk_bf16_f32 v25, v112, v113\n" \
  "v_lshlrev_b32 v112, 16, v25\n" \
  "v_and_b32 v113, 0xffff0000, v25\n" \
  "v_lshlrev_b32 v114, 16, v26\n" \
  "v_and_b32 v115, 0xffff0000, v26\n" \
  "v_pk_mul_f32 v[114:115], v[114:115], v[32:33] op_sel_hi:[1,0]\n" \
  "v_cvt_pk_bf16_f32 v26, v114, v115\n" \
  "v_lshlrev_b32 v114, 16, v26\n" \
  "v_and_b32 v115, 0xffff0000, v26\n" \
  "v_lshlrev_b32 v116, 16, v27\n" \
  "v_and_b32 v117, 0xffff0000, v27\n" \
  "v_pk_mul_f32 v[116:117], v[116:117], v[32:33] op_sel_hi:[1,0]\n" \
  "v_cvt_pk_bf16_f32 v27, v116, v117\n" \
  "v_lshlrev_b32 v116, 16, v27\n" \
  "v_and_b32 v117, 0xffff0000, v27\n" \
  "v_lshlrev_b32 v118, 16, v28\n" \
  "v_and_b32 v119, 0xffff0000, v28\n" \
  "v_pk_mul_f32 v[118:119], v[118:119], v[32:33] op_sel_hi:[1,0]\n" \
  "v_cvt_pk_bf16_f32 v28, v118, v119\n" \
  "v_lshlrev_b32 v118, 16, v28\n" \
  "v_and_b32 v119, 0xffff0000, v28\n" \
  "v_max_f32 v24, v110, v111\n" \
  "v_max3_f32 v24, v24, v112, v113\n" \
  "v_max3_f32 v24, v24, v114, v115\n" \
  "v_max3_f32 v24, v24, v116, v117\n" \
  "v_max3_f32 v24, v24, v118, v119\n" \
  "v_mov_b32 v25, v24\n" \
  "s_nop 1\n" \
  "v_permlane16_swap_b32 v24, v25\n" \
  "v_max_f32 v24, v24, v25\n" \
  "v_mov_b32 v25, v24\n" \
  "s_nop 1\n" \
  "v_permlane32_swap_b32 v24, v25\n" \
  "v_max_f32 v24, v24, v25\n" \
  "v_pk_add_f32 v[110:111], v[110:111], v[24:25] op_sel_hi:[1,0] neg_lo:[0,1] neg_hi:[0,1]\n" \
  "v_pk_mul_f32 v[110:111], v[110:111], v[34:35] op_sel_hi:[1,0]\n" \
  "v_pk_add_f32 v[112:113], v[112:113], v[24:25] op_sel_hi:[1,0] neg_lo:[0,1] neg_hi:[0,1]\n" \
  "v_pk_mul_f32 v[112:113], v[112:113], v[34:35] op_sel_hi:[1,0]\n" \
  "v_pk_add_f32 v[114:115], v[114:115], v[24:25] op_sel_hi:[1,0] neg_lo:[0,1] neg_hi:[0,1]\n" \
  "v_pk_mul_f32 v[114:115], v[114:115], v[34:35] op_sel_hi:[1,0]\n" \
  "v_pk_add_f32 v[116:117], v[116:117], v[24:25] op_sel_hi:[1,0] neg_lo:[0,1] neg_hi:[0,1]\n" \
  "v_pk_mul_f32 v[116:117], v[116:117], v[34:35] op_sel_hi:[1,0]\n" \
  "v_pk_add_f32 v[118:119], v[118:119], v[24:25] op_sel_hi:[1,0] neg_lo:[0,1] neg_hi:[0,1]\n" \
  "v_pk_mul_f32 v[118:119], v[118:119], v[34:35] op_sel_hi:[1,0]\n" \
  "v_exp_f32 v110, v110\n" \
  "v_exp_f32 v111, v111\n" \
  "v_exp_f32 v112, v112\n" \
  "v_exp_f32 v113, v113\n" \
  "v_exp_f32 v114, v114\n" \
  "v_exp_f32 v115, v115\n" \
  "v_exp_f32 v116, v116\n" \
  "v_exp_f32 v117, v117\n" \
  "v_exp_f32 v118, v118\n" \
  "v_exp_f32 v119, v119\n" \
  "v_pk_add_f32 v[24:25], v[110:111], v[112:113]\n" \
  "v_pk_add_f32 v[24:25], v[24:25], v[114:115]\n" \
  "v_pk_add_f32 v[24:25], v[24:25], v[116:117]\n" \
  "v_pk_add_f32 v[24:25], v[24:25], v[118:119]\n" \
  "v_add_f32 v24, v24, v25\n" \
  "v_mov_b32 v25, v24\n" \
  "s_nop 1\n" \
  "v_permlane16_swap_b32 v24, v25\n" \
  "v_add_f32 v24, v24, v25\n" \
  "v_mov_b32 v25, v24\n" \
  "s_nop 1\n" \
  "v_permlane32_swap_b32 v24, v25\n" \
  "v_add_f32 v24, v24, v25\n" \
  "v_rcp_f32 v24, v24\n" \
  "s_nop 0\n" \
  "v_pk_mul_f32 v[110:111], v[110:111], v[24:25] op_sel_hi:[1,0]\n" \
  "v_pk_mul_f32 v[112:113], v[112:113], v[24:25] op_sel_hi:[1,0]\n" \
  "v_pk_mul_f32 v[114:115], v[114:115], v[24:25] op_sel_hi:[1,0]\n" \
  "v_pk_mul_f32 v[116:117], v[116:117], v[24:25] op_sel_hi:[1,0]\n" \
  "v_pk_mul_f32 v[118:119], v[118:119], v[24:25] op_sel_hi:[1,0]\n" \
  "v_cvt_pk_bf16_f32 v52, v112, v113\n" \
  "v_cvt_pk_bf16_f32 v53, v114, v115\n" \
  "v_cvt_pk_bf16_f32 v54, v116, v117\n" \
  "v_cvt_pk_bf16_f32 v55, v118, v119\n" \
  "v_cvt_pk_bf16_f32 v56, v110, v111\n" \
  "s_sub_u32 s74, s68, 1\n" \
  "v_readlane_b32 s42, v15, s74\n" \
  "v_xor_b32 v20, 0x8000, v20\n" \
  "v_xor_b32 v21, 0x8000, v21\n" \
  "v_xor_b32 v18, 0x8000, v18\n" \
  "s_cmp_eq_u32 s68, 64\n" \
  "s_cbranch_scc1 LENDs2_%=\n" \
  "v_readlane_b32 s72, v11, s68\n" \
  "s_nop 0\n" \
  "s_add_u32 s69, s72, 4096\n" \
  "s_add_u32 s70, s72, 16384\n" \
  "s_add_u32 s44, s72, s93\n" \
  "s_add_u32 s45, s72, s94\n" \
  "s_add_u32 s46, s72, s95\n" \
  "v_readlane_b32 s41, v7, s68\n" \
  "s_nop 1\n" \
  "s_branch LOOPs2_%=\n" \
  "LMFs2_%=:\n" \
  "ds_read_b128 v[88:91], v3 offset:0\n" \
  "ds_read_b128 v[92:95], v3 offset:1024\n" \
  "ds_read_b128 v[96:99], v3 offset:2048\n" \
  "ds_read_b128 v[100:103], v3 offset:3072\n" \
  "ds_read_b128 v[104:107], v3 offset:4096\n" \
  "ds_read_b128 v[108:111], v3 offset:5120\n" \
  "ds_read_b128 v[112:115], v3 offset:6144\n" \
  "ds_read_b128 v[116:119], v3 offset:7168\n" \
  "v_mov_b32 v60, -1\n" \
  "v_mov_b32 v61, -1\n" \
  "ds_write_b64 v6, v[60:61] offset:34816\n" \
  "ds_write_b64 v6, v[60:61] offset:35328\n" \
  "ds_write_b64 v6, v[60:61] offset:37888\n" \
  "v_mov_b32 v64, 0\n" \
  "v_mov_b32 v65, 0\n" \
  "v_mov_b32 v66, 0\n" \
  "v_mov_b32 v67, 0\n" \
  "v_mov_b32 v68, 0\n" \
  "v_mov_b32 v69, 0\n" \
  "v_mov_b32 v70, 0\n" \
  "v_mov_b32 v71, 0\n" \
  "v_mov_b32 v72, 0\n" \
  "v_mov_b32 v73, 0\n" \
  "v_mov_b32 v74, 0\n" \
  "v_mov_b32 v75, 0\n" \
  "v_mov_b32 v76, 0\n" \
  "v_mov_b32 v77, 0\n" \
  "v_mov_b32 v78, 0\n" \
  "v_mov_b32 v79, 0\n" \
  "v_mov_b32 v80, 0\n" \
  "v_mov_b32 v81, 0\n" \
  "v_mov_b32 v82, 0\n" \
  "v_mov_b32 v83, 0\n" \
  "v_mov_b32 v84, 0\n" \
  "v_mov_b32 v85, 0\n" \
  "v_mov_b32 v86, 0\n" \
  "v_mov_b32 v87, 0\n" \
  "s_waitcnt lgkmcnt(0)\n" \
  "s_barrier\n" \
  "buffer_load_dwordx4 v[24:27], v1, s[36:39], s72 offen offset:0 sc1\n" \
  "s_mov_b32 m0, 67584\n" \
  "s_nop 0\n" \
  "buffer_load_dwordx4 v1, s[36:39], s69 offen sc1 lds\n" \
  "buffer_load_dwordx4 v[28:31], v1, s[36:39], s72 offen offset:1024 sc1\n" \
  "s_mov_b32 m0, 71680\n" \
  "s_nop 0\n" \
  "buffer_load_dwordx4 v1, s[36:39], s70 offen sc1 lds\n" \
  "ds_write_b64 v6, v[60:61] offset:2048\n" \
  "ds_write_b64 v6, v[60:61] offset:2560\n" \
  "ds_write_b64 v6, v[60:61] offset:5120\n" \
  "ds_write_b32 v12, v60 offset:256\n" \
  "s_branch LMs2_%=\n" \
  "LMQs2_%=:\n" \
  "s_waitcnt lgkmcnt(0)\n" \
  "s_barrier\n" \
  "s_barrier\n" \
  "ds_read_b32 v60, v12 offset:512\n" \
  "s_waitcnt lgkmcnt(0)\n" \
  "LMXs2_%=:\n" \
  "v_cmp_eq_u32 vcc, 2, v60\n" \
  "s_waitcnt vmcnt(0) lgkmcnt(0)\n" \
  "s_barrier\n" \
  "s_cbranch_vccnz LMFs2_%=\n" \
  "s_branch LMYs2_%=\n" \
  "LENDs2_%=:\n" \
  "s_cmp_lg_u32 s38, 0\n" \
  "s_cbranch_scc1 LFINs2_%=\n" \
  "s_waitcnt vmcnt(0) lgkmcnt(0)\n" \
  "s_mov_b32 s38, s47\n" \
  "s_mov_b32 s68, 0\n" \
  "s_mov_b32 s73, 0\n" \
  "s_mov_b32 s56, 0\n" \
  "s_mov_b32 s57, 0\n" \
  "s_branch LPASSs2_%=\n" \
  "LFINs2_%=:\n" \
  "s_nop 1\n" \
  "buffer_store_dwordx4 v[52:55], v16, s[36:39], s42 offen sc1\n" \
  "buffer_store_dword v56, v17, s[36:39], s42 offen sc1\n" \
  "s_waitcnt vmcnt(0)\n" \
  "v_mov_b32 v19, 1\n" \
  "buffer_atomic_add v19, v14, s[36:39], 0 offen sc1\n" \
  "s_branch LDONE_%=\n" \
  "LSTARTs3_%=:\n" \
  "v_mov_b32 v17, v29\n" \
  "s_mov_b32 s47, s38\n" \
  "s_mov_b32 s38, 0\n" \
  "s_mov_b32 s68, 63\n" \
  "s_mov_b32 s73, 20001\n" \
  "s_mov_b32 s56, 19998\n" \
  "s_getpc_b64 s[82:83]\n" \
  "LPCAs3_%=:\n" \
  "s_add_u32 s84, s82, LEAs3_%=-LPCAs3_%=+24\n" \
  "s_addc_u32 s85, s83, 0\n" \
  "LPASSs3_%=:\n" \
  "s_mov_b32 s40, 0\n" \
  "s_mov_b32 s42, 0x40000000\n" \
  "s_barrier\n" \
  "s_add_u32 m0, s40, 16384\n" \
  "s_add_u32 s75, s55, 32768\n" \
  "buffer_load_dwordx4 v8, s[36:39], s75 offen offset:2048 sc1 lds\n" \
  "buffer_load_dwordx4 v8, s[36:39], s75 offen offset:3072 sc1 lds\n" \
  "s_add_u32 m0, s40, 20480\n" \
  "s_add_u32 s75, s55, 36864\n" \
  "buffer_load_dwordx4 v8, s[36:39], s75 offen offset:0 sc1 lds\n" \
  "buffer_load_dwordx4 v8, s[36:39], s75 offen offset:1024 sc1 lds\n" \
  "buffer_load_dwordx4 v8, s[36:39], s75 offen offset:2048 sc1 lds\n" \
  "buffer_load_dwordx4 v8, s[36:39], s75 offen offset:3072 sc1 lds\n" \
  "s_xor_b32 s40, s40, 0x8000\n" \
  "LQAs3_%=:\n" \
  "buffer_load_dword v64, v17, s[36:39], 0 offen sc1\n" \
  "s_waitcnt vmcnt(0)\n" \
  "v_cmp_eq_u32 vcc, 0, v64\n" \
  "s_nop 3\n" \
  "s_cbranch_vccz LRAs3_%=\n" \
  "s_add_u32 s73, s73, 1\n" \
  "s_cmp_gt_u32 s73, 20000\n" \
  "s_cbranch_scc1 LRAs3_%=\n" \
  "s_sleep 2\n" \
  "s_branch LQAs3_%=\n" \
  "LRAs3_%=:\n" \
  "s_memrealtime s[76:77]\n" \
  "s_memtime s[78:79]\n" \
  "s_waitcnt lgkmcnt(0)\n" \
  "s_lshl_b32 s48, s76, 2\n" \
  "v_readlane_b32 s90, v64, 0\n" \
  "s_nop 1\n" \
  "s_sub_u32 s74, s90, s48\n" \
  "s_add_u32 s74, s74, 40000\n" \
  "s_cmp_gt_u32 s74, 80000\n" \
  "s_cselect_b32 s90, s48, s90\n" \
  "s_sub_u32 s74, s78, s89\n" \
  "s_sub_u32 s75, s76, s88\n" \
  "v_cvt_f32_u32 v24, s74\n" \
  "v_cvt_f32_u32 v25, s75\n" \
  "v_rcp_f32 v25, v25\n" \
  "v_mul_f32 v24, 0x43820000, v24\n" \
  "v_mul_f32 v24, v24, v25\n" \
  "v_cvt_u32_f32 v24, v24\n" \
  "s_nop 0\n" \
  "v_readfirstlane_b32 s74, v24\n" \
  "s_min_u32 s74, s74, 6400\n" \
  "s_cmp_gt_u32 s75, 150\n" \
  "s_cselect_b32 s50, s74, s50\n" \
  "s_cselect_b32 s88, s76, s88\n" \
  "s_cselect_b32 s89, s78, s89\n" \
  "s_add_u32 s49, s78, 0x1000000\n" \
  "s_mul_i32 s86, s54, 6\n" \
  "s_mov_b32 s87, -200\n" \
  "s_mov_b32 s51, 0\n" \
  "s_mul_i32 s74, s51, s54\n" \
  "s_add_u32 s74, s74, s90\n" \
  "s_sub_i32 s75, s74, s48\n" \
  "s_mov_b32 s48, s74\n" \
  "s_mul_i32 s75, s75, s50\n" \
  "s_ashr_i32 s75, s75, 10\n" \
  "s_add_u32 s49, s49, s75\n" \
  "v_readlane_b32 s72, v11, 0\n" \
  "s_nop 0\n" \
  "s_add_u32 s69, s72, 5120\n" \
  "s_add_u32 s70, s72, 17408\n" \
  "s_add_u32 s44, s72, s93\n" \
  "s_add_u32 s45, s72, s94\n" \
  "s_add_u32 s46, s72, s95\n" \
  "v_readlane_b32 s41, v7, 0\n" \
  "s_nop 1\n" \
  "ds_read_b128 v[88:91], v3 offset:0\n" \
  "ds_read_b128 v[92:95], v3 offset:1024\n" \
  "ds_read_b128 v[96:99], v3 offset:2048\n" \
  "ds_read_b128 v[100:103], v3 offset:3072\n" \
  "ds_read_b128 v[104:107], v3 offset:4096\n" \
  "ds_read_b128 v[108:111], v3 offset:5120\n" \
  "ds_read_b128 v[112:115], v3 offset:6144\n" \
  "ds_read_b128 v[116:119], v3 offset:7168\n" \
  "v_mov_b32 v60, -1\n" \
  "v_mov_b32 v61, -1\n" \
  "ds_write_b64 v6, v[60:61] offset:35840\n" \
  "ds_write_b64 v6, v[60:61] offset:36352\n" \
  "ds_write_b64 v6, v[60:61] offset:38400\n" \
  "v_mov_b32 v64, 0\n" \
  "v_mov_b32 v65, 0\n" \
  "v_mov_b32 v66, 0\n" \
  "v_mov_b32 v67, 0\n" \
  "v_mov_b32 v68, 0\n" \
  "v_mov_b32 v69, 0\n" \
  "v_mov_b32 v70, 0\n" \
  "v_mov_b32 v71, 0\n" \
  "v_mov_b32 v72, 0\n" \
  "v_mov_b32 v73, 0\n" \
  "v_mov_b32 v74, 0\n" \
  "v_mov_b32 v75, 0\n" \
  "v_mov_b32 v76, 0\n" \
  "v_mov_b32 v77, 0\n" \
  "v_mov_b32 v78, 0\n" \
  "v_mov_b32 v79, 0\n" \
  "v_mov_b32 v80, 0\n" \
  "v_mov_b32 v81, 0\n" \
  "v_mov_b32 v82, 0\n" \
  "v_mov_b32 v83, 0\n" \
  "v_mov_b32 v84, 0\n" \
  "v_mov_b32 v85, 0\n" \
  "v_mov_b32 v86, 0\n" \
  "v_mov_b32 v87, 0\n" \
  "s_waitcnt lgkmcnt(0)\n" \
  "LOOPs3_%=:\n" \
  "LWAs3_%=:\n" \
  "s_memrealtime s[76:77]\n" \
  "s_memtime s[78:79]\n" \
  "s_waitcnt lgkmcnt(0)\n" \
  "s_lshl_b32 s75, s76, 2\n" \
  "s_sub_i32 s75, s48, s75\n" \
  "s_mul_i32 s75, s75, s50\n" \
  "s_ashr_i32 s75, s75, 10\n" \
  "s_sub_i32 s74, s49, s78\n" \
  "s_min_i32 s74, s74, s75\n" \
  "s_sub_i32 s75, s75, 92\n" \
  "s_max_i32 s74, s74, s75\n" \
  "s_add_u32 s49, s74, s78\n" \
  "s_cmp_gt_i32 s74, 168\n" \
  "s_cbranch_scc1 LWAs3_%=\n" \
  "s_cmp_lt_i32 s74, s87\n" \
  "s_cbranch_scc1 LSAs3_%=\n" \
  "s_max_i32 s74, s74, 48\n" \
  "s_lshr_b32 s74, s74, 3\n" \
  "s_lshl_b32 s74, s74, 2\n" \
  "s_sub_u32 s80, s84, s74\n" \
  "s_subb_u32 s81, s85, 0\n" \
  "s_setpc_b64 s[80:81]\n" \
  "s_nop 1\n" \
  "s_nop 1\n" \
  "s_nop 1\n" \
  "s_nop 1\n" \
  "s_nop 1\n" \
  "s_nop 1\n" \
  "s_nop 1\n" \
  "s_nop 1\n" \
  "s_nop 1\n" \
  "s_nop 1\n" \
  "s_nop 1\n" \
  "s_nop 1\n" \
  "s_nop 1\n" \
  "s_nop 1\n" \
  "s_nop 1\n" \
  "s_nop 1\n" \
  "LEAs3_%=:\n" \
  "s_barrier\n" \
  "buffer_load_dwordx4 v[24:27], v1, s[36:39], s72 offen offset:0 sc1\n" \
  "s_mov_b32 m0, 68608\n" \
  "s_nop 0\n" \
  "buffer_load_dwordx4 v1, s[36:39], s69 offen sc1 lds\n" \
  "buffer_load_dwordx4 v[28:31], v1, s[36:39], s72 offen offset:1024 sc1\n" \
  "s_mov_b32 m0, 72704\n" \
  "s_nop 0\n" \
  "buffer_load_dwordx4 v1, s[36:39], s70 offen sc1 lds\n" \
  "ds_write_b64 v6, v[60:61] offset:3072\n" \
  "ds_write_b64 v6, v[60:61] offset:3584\n" \
  "ds_write_b64 v6, v[60:61] offset:5632\n" \
  "s_add_u32 s68, s68, 1\n" \
  "LMs3_%=:\n" \
  "s_waitcnt vmcnt(3)\n" \
  "v_smfmac_f32_16x16x64_bf16 v[64:67], v[24:27], v[88:95], v0\n" \
  "ds_read_b128 v[88:91], v3 offset:8192\n" \
  "v_smfmac_f32_16x16x64_bf16 v[68:71], v[24:27], v[96:103], v0\n" \
  "ds_read_b128 v[92:95], v3 offset:9216\n" \
  "v_smfmac_f32_16x16x64_bf16 v[72:75], v[24:27], v[104:111], v0\n" \
  "ds_read_b128 v[96:99], v3 offset:10240\n" \
  "v_smfmac_f32_16x16x64_bf16 v[76:79], v[24:27], v[112:119], v0\n" \
  "ds_read_b128 v[100:103], v3 offset:11264\n" \
  "v_smfmac_f32_16x16x64_bf16 v[80:83], v[24:27], a[0:7], v0\n" \
  "ds_read_b128 v[104:107], v3 offset:12288\n" \
  "v_smfmac_f32_16x16x64_bf16 v[84:87], v[24:27], a[8:15], v0\n" \
  "s_waitcnt vmcnt(2)\n" \
  "s_waitcnt lgkmcnt(5)\n" \
  "s_barrier\n" \
  "ds_read_b128 v[32:35], v2 offset:2048\n" \
  "s_waitcnt vmcnt(1)\n" \
  "v_smfmac_f32_16x16x64_bf16 v[64:67], v[28:31], a[16:23], v0\n" \
  "ds_read_b128 v[36:39], v2 offset:3072\n" \
  "v_smfmac_f32_16x16x64_bf16 v[68:71], v[28:31], a[24:31], v0\n" \
  "ds_read_b128 v[40:43], v2 offset:4096\n" \
  "s_waitcnt lgkmcnt(6)\n" \
  "v_smfmac_f32_16x16x64_bf16 v[72:75], v[28:31], v[88:95], v0\n" \
  "ds_read_b128 v[44:47], v2 offset:5120\n" \
  "v_smfmac_f32_16x16x64_bf16 v[76:79], v[28:31], a[32:39], v0\n" \
  "ds_read_b128 v[108:111], v3 offset:13312\n" \
  "v_smfmac_f32_16x16x64_bf16 v[80:83], v[28:31], a[40:47], v0\n" \
  "ds_read_b128 v[112:115], v3 offset:14336\n" \
  "v_smfmac_f32_16x16x64_bf16 v[84:87], v[28:31], a[48:55], v0\n" \
  "ds_read_b128 v[116:119], v3 offset:15360\n" \
  "s_waitcnt lgkmcnt(6)\n" \
  "v_smfmac_f32_16x16x64_bf16 v[64:67], v[32:35], a[56:63], v0\n" \
  "ds_read_b128 v[88:91], v3 offset:16384\n" \
  "v_smfmac_f32_16x16x64_bf16 v[68:71], v[32:35], v[96:103], v0\n" \
  "ds_read_b128 v[92:95], v3 offset:17408\n" \
  "v_smfmac_f32_16x16x64_bf16 v[72:75], v[32:35], a[64:71], v0\n" \
  "ds_read_b128 v[96:99], v3 offset:18432\n" \
  "s_waitcnt lgkmcnt(8)\n" \
  "v_smfmac_f32_16x16x64_bf16 v[64:67], v[36:39], a[72:79], v0\n" \
  "s_waitcnt vmcnt(0)\n" \
  "s_barrier\n" \
  "ds_read_b128 v[100:103], v3 offset:19456\n" \
  "v_smfmac_f32_16x16x64_bf16 v[68:71], v[36:39], a[80:87], v0\n" \
  "ds_read_b128 v[48:51], v2 offset:6144\n" \
  "v_smfmac_f32_16x16x64_bf16 v[72:75], v[36:39], a[88:95], v0\n" \
  "ds_read_b128 v[52:55], v2 offset:7168\n" \
  "s_waitcnt lgkmcnt(8)\n" \
  "v_smfmac_f32_16x16x64_bf16 v[64:67], v[40:43], v[104:111], v0\n" \
  "ds_read_b128 v[56:59], v2 offset:8192\n" \
  "v_smfmac_f32_16x16x64_bf16 v[68:71], v[40:43], a[96:103], v0\n" \
  "ds_read_b128 v[60:63], v2 offset:9216\n" \
  "s_waitcnt lgkmcnt(8)\n" \
  "v_smfmac_f32_16x16x64_bf16 v[72:75], v[40:43], v[112:119], v0\n" \
  "ds_read_b128 v[104:107], v3 offset:20480\n" \
  "v_smfmac_f32_16x16x64_bf16 v[64:67], v[44:47], a[104:111], v0\n" \
  "ds_read_b128 v[108:111], v3 offset:21504\n" \
  "s_waitcnt lgkmcnt(8)\n" \
  "v_smfmac_f32_16x16x64_bf16 v[68:71], v[44:47], v[88:95], v0\n" \
  "v_smfmac_f32_16x16x64_bf16 v[72:75], v[44:47], a[112:119], v0\n" \
  "s_waitcnt lgkmcnt(5)\n" \
  "v_smfmac_f32_16x16x64_bf16 v[64:67], v[48:51], a[120:127], v0\n" \
  "s_waitcnt lgkmcnt(4)\n" \
  "v_smfmac_f32_16x16x64_bf16 v[64:67], v[52:55], a[128:135], v0\n" \
  "s_waitcnt lgkmcnt(3)\n" \
  "v_smfmac_f32_16x16x64_bf16 v[64:67], v[56:59], a[136:143], v0\n" \
  "s_waitcnt lgkmcnt(2)\n" \
  "v_smfmac_f32_16x16x64_bf16 v[64:67], v[60:63], a[144:151], v0\n" \
  "v_smfmac_f32_16x16x64_bf16 v[68:71], v[48:51], v[96:103], v0\n" \
  "v_smfmac_f32_16x16x64_bf16 v[68:71], v[52:55], a[152:159], v0\n" \
  "s_waitcnt lgkmcnt(0)\n" \
  "v_cmp_u_f32 vcc, v64, v65\n" \
  "v_cmp_u_f32 s[82:83], v66, v67\n" \
  "v_smfmac_f32_16x16x64_bf16 v[68:71], v[56:59], v[104:111], v0\n" \
  "s_or_b64 vcc, vcc, s[82:83]\n" \
  "s_cbranch_scc1 LMQs3_%=\n" \
  "v_cvt_pk_bf16_f32 v64, v64, v65\n" \
  "v_smfmac_f32_16x16x64_bf16 v[68:71], v[60:63], a[160:167], v0\n" \
  "v_cvt_pk_bf16_f32 v65, v66, v67\n" \
  "v_smfmac_f32_16x16x64_bf16 v[72:75], v[48:51], a[168:175], v0\n" \
  "ds_write_b64 v6, v[64:65] offset:35840\n" \
  "v_smfmac_f32_16x16x64_bf16 v[72:75], v[52:55], a[176:183], v0\n" \
  "v_cvt_pk_bf16_f32 v68, v68, v69\n" \
  "v_smfmac_f32_16x16x64_bf16 v[72:75], v[56:59], a[184:191], v0\n" \
  "v_cvt_pk_bf16_f32 v69, v70, v71\n" \
  "v_smfmac_f32_16x16x64_bf16 v[72:75], v[60:63], a[192:199], v0\n" \
  "ds_write_b64 v6, v[68:69] offset:36352\n" \
  "v_smfmac_f32_16x16x64_bf16 v[76:79], v[32:35], a[200:207], v0\n" \
  "v_smfmac_f32_16x16x64_bf16 v[76:79], v[36:39], a[208:215], v0\n" \
  "v_cvt_pk_bf16_f32 v72, v72, v73\n" \
  "v_smfmac_f32_16x16x64_bf16 v[76:79], v[40:43], a[216:223], v0\n" \
  "v_cvt_pk_bf16_f32 v73, v74, v75\n" \
  "v_smfmac_f32_16x16x64_bf16 v[76:79], v[44:47], a[224:231], v0\n" \
  "ds_write_b64 v6, v[72:73] offset:38400\n" \
  "v_smfmac_f32_16x16x64_bf16 v[76:79], v[48:51], a[232:239], v0\n" \
  "v_smfmac_f32_16x16x64_bf16 v[76:79], v[52:55], a[240:247], v0\n" \
  "v_smfmac_f32_16x16x64_bf16 v[76:79], v[56:59], a[248:255], v0\n" \
  "v_smfmac_f32_16x16x64_bf16 v[76:79], v[60:63], v[120:127], v0\n" \
  "v_smfmac_f32_16x16x64_bf16 v[80:83], v[32:35], v[128:135], v0\n" \
  "v_smfmac_f32_16x16x64_bf16 v[80:83], v[36:39], v[136:143], v0\n" \
  "v_cvt_pk_bf16_f32 v76, v76, v77\n" \
  "v_smfmac_f32_16x16x64_bf16 v[80:83], v[40:43], v[144:151], v0\n" \
  "v_cvt_pk_bf16_f32 v77, v78, v79\n" \
  "v_smfmac_f32_16x16x64_bf16 v[80:83], v[44:47], v[152:159], v0\n" \
  "ds_write_b64 v6, v[76:77] offset:3072\n" \
  "v_smfmac_f32_16x16x64_bf16 v[80:83], v[48:51], v[160:167], v0\n" \
  "v_smfmac_f32_16x16x64_bf16 v[80:83], v[52:55], v[168:175], v0\n" \
  "v_smfmac_f32_16x16x64_bf16 v[80:83], v[56:59], v[176:183], v0\n" \
  "v_smfmac_f32_16x16x64_bf16 v[80:83], v[60:63], v[184:191], v0\n" \
  "v_smfmac_f32_16x16x64_bf16 v[84:87], v[32:35], v[192:199], v0\n" \
  "v_smfmac_f32_16x16x64_bf16 v[84:87], v[36:39], v[200:207], v0\n" \
  "v_cvt_pk_bf16_f32 v80, v80, v81\n" \
  "v_smfmac_f32_16x16x64_bf16 v[84:87], v[40:43], v[208:215], v0\n" \
  "v_cvt_pk_bf16_f32 v81, v82, v83\n" \
  "v_smfmac_f32_16x16x64_bf16 v[84:87], v[44:47], v[216:223], v0\n" \
  "ds_write_b64 v6, v[80:81] offset:3584\n" \
  "v_smfmac_f32_16x16x64_bf16 v[84:87], v[48:51], v[224:231], v0\n" \
  "v_smfmac_f32_16x16x64_bf16 v[84:87], v[52:55], v[232:239], v0\n" \
  "v_smfmac_f32_16x16x64_bf16 v[84:87], v[56:59], v[240:247], v0\n" \
  "v_smfmac_f32_16x16x64_bf16 v[84:87], v[60:63], v[248:255], v0\n" \
  "s_nop 7\n" \
  "v_cvt_pk_bf16_f32 v84, v84, v85\n" \
  "v_cvt_pk_bf16_f32 v85, v86, v87\n" \
  "ds_write_b64 v6, v[84:85] offset:5632\n" \
  "s_barrier ; timing only\n" \
  "s_waitcnt lgkmcnt(0)\n" \
  "s_barrier\n" \
  "ds_read_b32 v60, v12 offset:512\n" \
  "s_waitcnt lgkmcnt(0)\n" \
  "v_cmp_ne_u32 vcc, 0, v60\n" \
  "s_nop 3\n" \
  "s_cbranch_vccnz LMXs3_%=\n" \
  "LMYs3_%=:\n" \
  "buffer_load_dword v15, v14, s[36:39], s72 offen sc1\n" \
  "s_lshl_b32 s74, s68, 16\n" \
  "s_or_b32 s74, s74, s51\n" \
  "v_mov_b32 v60, s74\n" \
  "buffer_store_dword v60, v16, s[36:39], 0 offen sc1\n" \
  "ds_read_b128 v[88:91], v3 offset:0\n" \
  "ds_read_b128 v[92:95], v3 offset:1024\n" \
  "ds_read_b128 v[96:99], v3 offset:2048\n" \
  "ds_read_b128 v[100:103], v3 offset:3072\n" \
  "ds_read_b128 v[104:107], v3 offset:4096\n" \
  "ds_read_b128 v[108:111], v3 offset:5120\n" \
  "ds_read_b128 v[112:115], v3 offset:6144\n" \
  "ds_read_b128 v[116:119], v3 offset:7168\n" \
  "v_mov_b32 v60, -1\n" \
  "v_mov_b32 v61, -1\n" \
  "ds_write_b64 v6, v[60:61] offset:35840\n" \
  "ds_write_b64 v6, v[60:61] offset:36352\n" \
  "ds_write_b64 v6, v[60:61] offset:38400\n" \
  "v_mov_b32 v64, 0\n" \
  "v_mov_b32 v65, 0\n" \
  "v_mov_b32 v66, 0\n" \
  "v_mov_b32 v67, 0\n" \
  "v_mov_b32 v68, 0\n" \
  "v_mov_b32 v69, 0\n" \
  "v_mov_b32 v70, 0\n" \
  "v_mov_b32 v71, 0\n" \
  "v_mov_b32 v72, 0\n" \
  "v_mov_b32 v73, 0\n" \
  "v_mov_b32 v74, 0\n" \
  "v_mov_b32 v75, 0\n" \
  "v_mov_b32 v76, 0\n" \
  "v_mov_b32 v77, 0\n" \
  "v_mov_b32 v78, 0\n" \
  "v_mov_b32 v79, 0\n" \
  "v_mov_b32 v80, 0\n" \
  "v_mov_b32 v81, 0\n" \
  "v_mov_b32 v82, 0\n" \
  "v_mov_b32 v83, 0\n" \
  "v_mov_b32 v84, 0\n" \
  "v_mov_b32 v85, 0\n" \
  "v_mov_b32 v86, 0\n" \
  "v_mov_b32 v87, 0\n" \
  "s_waitcnt lgkmcnt(0)\n" \
  "s_memrealtime s[76:77]\n" \
  "s_memtime s[78:79]\n" \
  "s_waitcnt lgkmcnt(0)\n" \
  "s_lshl_b32 s75, s76, 2\n" \
  "s_sub_i32 s75, s48, s75\n" \
  "s_mul_i32 s75, s75, s50\n" \
  "s_ashr_i32 s75, s75, 10\n" \
  "s_sub_i32 s74, s49, s78\n" \
  "s_min_i32 s74, s74, s75\n" \
  "s_sub_i32 s75, s75, 92\n" \
  "s_max_i32 s74, s74, s75\n" \
  "s_add_u32 s49, s74, s78\n" \
  "s_sub_u32 s74, s78, s89\n" \
  "s_sub_u32 s75, s76, s88\n" \
  "v_cvt_f32_u32 v24, s74\n" \
  "v_cvt_f32_u32 v25, s75\n" \
  "v_rcp_f32 v25, v25\n" \
  "v_mul_f32 v24, 0x43820000, v24\n" \
  "v_mul_f32 v24, v24, v25\n" \
  "v_cvt_u32_f32 v24, v24\n" \
  "s_nop 0\n" \
  "v_readfirstlane_b32 s74, v24\n" \
  "s_min_u32 s74, s74, 6400\n" \
  "s_cmp_gt_u32 s75, 900\n" \
  "s_cselect_b32 s50, s74, s50\n" \
  "s_cselect_b32 s88, s76, s88\n" \
  "s_cselect_b32 s89, s78, s89\n" \
  "s_waitcnt vmcnt(1)\n" \
  "v_readlane_b32 s74, v15, 3\n" \
  "s_nop 0\n" \
  "s_sub_u32 s75, s74, s51\n" \
  "s_cmp_lt_u32 s75, 16\n" \
  "s_cselect_b32 s51, s74, s51\n" \
  "v_readlane_b32 s74, v15, 0\n" \
  "s_add_u32 s90, s90, s53\n" \
  "s_sub_u32 s75, s74, s90\n" \
  "s_add_u32 s75, s75, 2000\n" \
  "s_cmp_lt_u32 s75, 4000\n" \
  "s_cselect_b32 s90, s74, s90\n" \
  "s_mul_i32 s74, s51, s54\n" \
  "s_add_u32 s74, s74, s90\n" \
  "s_sub_i32 s75, s74, s48\n" \
  "s_mov_b32 s48, s74\n" \
  "s_mul_i32 s75, s75, s50\n" \
  "s_ashr_i32 s75, s75, 10\n" \
  "s_add_u32 s49, s49, s75\n" \
  "s_cmp_eq_u32 s68, 64\n" \
  "s_cbranch_scc1 LENDs3_%=\n" \
  "v_readlane_b32 s72, v11, s68\n" \
  "s_nop 0\n" \
  "s_add_u32 s69, s72, 5120\n" \
  "s_add_u32 s70, s72, 17408\n" \
  "s_add_u32 s44, s72, s93\n" \
  "s_add_u32 s45, s72, s94\n" \
  "s_add_u32 s46, s72, s95\n" \
  "v_readlane_b32 s41, v7, s68\n" \
  "s_nop 1\n" \
  "s_branch LOOPs3_%=\n" \
  "LSAs3_%=:\n" \
  "s_add_u32 s48, s48, s54\n" \
  "s_add_u32 s49, s49, s86\n" \
  "s_add_u32 s51, s51, 1\n" \
  "s_branch LWAs3_%=\n" \
  "LMFs3_%=:\n" \
  "ds_read_b128 v[88:91], v3 offset:0\n" \
  "ds_read_b128 v[92:95], v3 offset:1024\n" \
  "ds_read_b128 v[96:99], v3 offset:2048\n" \
  "ds_read_b128 v[100:103], v3 offset:3072\n" \
  "ds_read_b128 v[104:107], v3 offset:4096\n" \
  "ds_read_b128 v[108:111], v3 offset:5120\n" \
  "ds_read_b128 v[112:115], v3 offset:6144\n" \
  "ds_read_b128 v[116:119], v3 offset:7168\n" \
  "v_mov_b32 v60, -1\n" \
  "v_mov_b32 v61, -1\n" \
  "ds_write_b64 v6, v[60:61] offset:35840\n" \
  "ds_write_b64 v6, v[60:61] offset:36352\n" \
  "ds_write_b64 v6, v[60:61] offset:38400\n" \
  "v_mov_b32 v64, 0\n" \
  "v_mov_b32 v65, 0\n" \
  "v_mov_b32 v66, 0\n" \
  "v_mov_b32 v67, 0\n" \
  "v_mov_b32 v68, 0\n" \
  "v_mov_b32 v69, 0\n" \
  "v_mov_b32 v70, 0\n" \
  "v_mov_b32 v71, 0\n" \
  "v_mov_b32 v72, 0\n" \
  "v_mov_b32 v73, 0\n" \
  "v_mov_b32 v74, 0\n" \
  "v_mov_b32 v75, 0\n" \
  "v_mov_b32 v76, 0\n" \
  "v_mov_b32 v77, 0\n" \
  "v_mov_b32 v78, 0\n" \
  "v_mov_b32 v79, 0\n" \
  "v_mov_b32 v80, 0\n" \
  "v_mov_b32 v81, 0\n" \
  "v_mov_b32 v82, 0\n" \
  "v_mov_b32 v83, 0\n" \
  "v_mov_b32 v84, 0\n" \
  "v_mov_b32 v85, 0\n" \
  "v_mov_b32 v86, 0\n" \
  "v_mov_b32 v87, 0\n" \
  "s_waitcnt lgkmcnt(0)\n" \
  "LHMs3_%=:\n" \
  "s_add_u32 s48, s48, s54\n" \
  "s_add_u32 s49, s49, s86\n" \
  "s_add_u32 s51, s51, 1\n" \
  "LGMs3_%=:\n" \
  "s_memrealtime s[76:77]\n" \
  "s_memtime s[78:79]\n" \
  "s_waitcnt lgkmcnt(0)\n" \
  "s_lshl_b32 s75, s76, 2\n" \
  "s_sub_i32 s75, s48, s75\n" \
  "s_mul_i32 s75, s75, s50\n" \
  "s_ashr_i32 s75, s75, 10\n" \
  "s_sub_i32 s74, s49, s78\n" \
  "s_min_i32 s74, s74, s75\n" \
  "s_sub_i32 s75, s75, 92\n" \
  "s_max_i32 s74, s74, s75\n" \
  "s_add_u32 s49, s74, s78\n" \
  "s_cmp_gt_i32 s74, 60\n" \
  "s_cbranch_scc1 LGMs3_%=\n" \
  "s_cmp_lt_i32 s74, s87\n" \
  "s_cbranch_scc1 LHMs3_%=\n" \
  "s_barrier\n" \
  "buffer_load_dwordx4 v[24:27], v1, s[36:39], s72 offen offset:0 sc1\n" \
  "s_mov_b32 m0, 68608\n" \
  "s_nop 0\n" \
  "buffer_load_dwordx4 v1, s[36:39], s69 offen sc1 lds\n" \
  "buffer_load_dwordx4 v[28:31], v1, s[36:39], s72 offen offset:1024 sc1\n" \
  "s_mov_b32 m0, 72704\n" \
  "s_nop 0\n" \
  "buffer_load_dwordx4 v1, s[36:39], s70 offen sc1 lds\n" \
  "ds_write_b64 v6, v[60:61] offset:3072\n" \
  "ds_write_b64 v6, v[60:61] offset:3584\n" \
  "ds_write_b64 v6, v[60:61] offset:5632\n" \
  "s_branch LMs3_%=\n" \
  "LMQs3_%=:\n" \
  "s_waitcnt lgkmcnt(0)\n" \
  "s_barrier\n" \
  "s_barrier\n" \
  "ds_read_b32 v60, v12 offset:512\n" \
  "s_waitcnt lgkmcnt(0)\n" \
  "LMXs3_%=:\n" \
  "v_cmp_eq_u32 vcc, 2, v60\n" \
  "s_waitcnt lgkmcnt(0)\n" \
  "s_barrier\n" \
  "s_cbranch_vccnz LMFs3_%=\n" \
  "s_branch LMYs3_%=\n" \
  "LENDs3_%=:\n" \
  "s_cmp_lg_u32 s38, 0\n" \
  "s_cbranch_scc1 LFINs3_%=\n" \
  "s_waitcnt vmcnt(0) lgkmcnt(0)\n" \
  "s_mov_b32 s38, s47\n" \
  "s_mov_b32 s68, 0\n" \
  "s_mov_b32 s73, 0\n" \
  "s_mov_b32 s56, 0\n" \
  "s_mov_b32 s57, 0\n" \
  "s_branch LPASSs3_%=\n" \
  "LFINs3_%=:\n" \
  "s_branch LDONE_%=\n" \
  "LDONE_%=:\n" \
  "s_waitcnt vmcnt(0) lgkmcnt(0)\n" \
  "s_mov_b32 s53, s73\n" \
  "s_mov_b32 s54, s51\n" \
  "s_mov_b32 s55, s50\n"
#define CROLE_CLOBBERS \
  "v32", "v33", "v34", "v35", "v36", "v37", "v38", "v39", "v40", "v41", "v42", "v43", "v44", "v45", "v46", "v47", "v48", "v49", "v50", "v51", "v52", \
  "v53", "v54", "v55", "v56", "v57", "v58", "v59", "v60", "v61", "v62", "v63", "v64", "v65", "v66", "v67", "v68", "v69", "v70", "v71", "v72", "v73", \
  "v74", "v75", "v76", "v77", "v78", "v79", "v80", "v81", "v82", "v83", "v84", "v85", "v86", "v87", "v88", "v89", "v90", "v91", "v92", "v93", "v94", \
  "v95", "v96", "v97", "v98", "v99", "v100", "v101", "v102", "v103", "v104", "v105", "v106", "v107", "v108", "v109", "v110", "v111", "v112", "v113", \
  "v114", "v115", "v116", "v117", "v118", "v119", "v120", "v121", "v122", "v123", "v124", "v125", "v126", "v127", "v128", "v129", "v130", "v131", \
  "v132", "v133", "v134", "v135", "v136", "v137", "v138", "v139", "v140", "v141", "v142", "v143", "v144", "v145", "v146", "v147", "v148", "v149", \
  "v150", "v151", "v152", "v153", "v154", "v155", "v156", "v157", "v158", "v159", "v160", "v161", "v162", "v163", "v164", "v165", "v166", "v167", \
  "v168", "v169", "v170", "v171", "v172", "v173", "v174", "v175", "v176", "v177", "v178", "v179", "v180", "v181", "v182", "v183", "v184", "v185", \
  "v186", "v187", "v188", "v189", "v190", "v191", "v192", "v193", "v194", "v195", "v196", "v197", "v198", "v199", "v200", "v201", "v202", "v203", \
  "v204", "v205", "v206", "v207", "v208", "v209", "v210", "v211", "v212", "v213", "v214", "v215", "v216", "v217", "v218", "v219", "v220", "v221", \
  "v222", "v223", "v224", "v225", "v226", "v227", "v228", "v229", "v230", "v231", "v232", "v233", "v234", "v235", "v236", "v237", "v238", "v239", \
  "v240", "v241", "v242", "v243", "v244", "v245", "v246", "v247", "v248", "v249", "v250", "v251", "v252", "v253", "v254", "v255", "s34", "s35", "s40", \
  "s41", "s42", "s43", "s44", "s45", "s46", "s47", "s48", "s49", "s50", "s51", "s68", "s69", "s70", "s71", "s72", "s73", "s74", "s75", "s76", "s77", \
  "s78", "s79", "s80", "s81", "s82", "s83", "s84", "s85", "s86", "s87", "s88", "s89", "s90", "s91", "s93", "s94", "s95", "a0", "a1", "a2", "a3", "a4", \
  "a5", "a6", "a7", "a8", "a9", "a10", "a11", "a12", "a13", "a14", "a15", "a16", "a17", "a18", "a19", "a20", "a21", "a22", "a23", "a24", "a25", "a26", \
  "a27", "a28", "a29", "a30", "a31", "a32", "a33", "a34", "a35", "a36", "a37", "a38", "a39", "a40", "a41", "a42", "a43", "a44", "a45", "a46", "a47", \
  "a48", "a49", "a50", "a51", "a52", "a53", "a54", "a55", "a56", "a57", "a58", "a59", "a60", "a61", "a62", "a63", "a64", "a65", "a66", "a67", "a68", \
  "a69", "a70", "a71", "a72", "a73", "a74", "a75", "a76", "a77", "a78", "a79", "a80", "a81", "a82", "a83", "a84", "a85", "a86", "a87", "a88", "a89", \
  "a90", "a91", "a92", "a93", "a94", "a95", "a96", "a97", "a98", "a99", "a100", "a101", "a102", "a103", "a104", "a105", "a106", "a107", "a108", "a109", \
  "a110", "a111", "a112", "a113", "a114", "a115", "a116", "a117", "a118", "a119", "a120", "a121", "a122", "a123", "a124", "a125", "a126", "a127", \
  "a128", "a129", "a130", "a131", "a132", "a133", "a134", "a135", "a136", "a137", "a138", "a139", "a140", "a141", "a142", "a143", "a144", "a145", \
  "a146", "a147", "a148", "a149", "a150", "a151", "a152", "a153", "a154", "a155", "a156", "a157", "a158", "a159", "a160", "a161", "a162", "a163", \
  "a164", "a165", "a166", "a167", "a168", "a169", "a170", "a171", "a172", "a173", "a174", "a175", "a176", "a177", "a178", "a179", "a180", "a181", \
  "a182", "a183", "a184", "a185", "a186", "a187", "a188", "a189", "a190", "a191", "a192", "a193", "a194", "a195", "a196", "a197", "a198", "a199", \
  "a200", "a201", "a202", "a203", "a204", "a205", "a206", "a207", "a208", "a209", "a210", "a211", "a212", "a213", "a214", "a215", "a216", "a217", \
  "a218", "a219", "a220", "a221", "a222", "a223", "a224", "a225", "a226", "a227", "a228", "a229", "a230", "a231", "a232", "a233", "a234", "a235", \
  "a236", "a237", "a238", "a239", "a240", "a241", "a242", "a243", "a244", "a245", "a246", "a247", "a248", "a249", "a250", "a251", "a252", "a253", \
  "a254", "a255"
#define CROLE_ASM \
  "s_mov_b32 s34, 0x00ff00ff\n" \
  "s_mov_b32 s35, 0x00ff00ff\n" \
  "s_mov_b32 s40, 0x00ff00ff\n" \
  "s_mov_b32 s41, 0xff00ff00\n" \
  "s_mov_b32 s68, 0\n" \
  "s_mov_b32 s73, 0\n" \
  "s_mov_b32 s58, 0\n" \
  "s_mov_b32 s59, 0\n" \
  "s_mov_b32 s51, 0\n" \
  "s_mov_b32 s71, 0\n" \
  "s_mov_b32 s44, 0\n" \
  "s_mov_b32 s50, 5900\n" \
  "s_mov_b32 s42, 0x40000000\n" \
  "s_mov_b32 s93, s60\n" \
  "s_mov_b32 s60, 0\n" \
  "s_mov_b32 s61, 0\n" \
  "s_mov_b32 s62, 0\n" \
  "s_mov_b32 s63, 0\n" \
  "s_mov_b32 s64, 0\n" \
  "s_mov_b32 s65, 0\n" \
  "s_mov_b32 s66, 0\n" \
  "s_mov_b32 s67, 0\n" \
  "s_cmp_eq_u32 s52, 3\n" \
  "s_cbranch_scc1 LSTARTc3_%=\n" \
  "LSTARTc0_%=:\n" \
  "s_barrier\n" \
  "v_mov_b32 v15, 1\n" \
  "v_readlane_b32 s46, v6, 0\n" \
  "s_nop 3\n" \
  "v_readlane_b32 s47, v6, 1\n" \
  "buffer_load_dwordx4 a[0:3], v18, s[36:39], s46 offen sc1\n" \
  "buffer_load_dwordx4 a[80:83], v19, s[36:39], s46 offen sc1\n" \
  "buffer_load_dwordx4 a[160:163], v20, s[36:39], s46 offen sc1\n" \
  "buffer_load_dwordx4 a[240:243], v21, s[36:39], s46 offen sc1\n" \
  "buffer_load_dwordx4 v[176:179], v22, s[36:39], s46 offen sc1\n" \
  "s_barrier\n" \
  "v_readlane_b32 s46, v6, 2\n" \
  "buffer_load_dwordx4 a[4:7], v18, s[36:39], s47 offen sc1\n" \
  "buffer_load_dwordx4 a[84:87], v19, s[36:39], s47 offen sc1\n" \
  "buffer_load_dwordx4 a[164:167], v20, s[36:39], s47 offen sc1\n" \
  "buffer_load_dwordx4 a[244:247], v21, s[36:39], s47 offen sc1\n" \
  "buffer_load_dwordx4 v[180:183], v22, s[36:39], s47 offen sc1\n" \
  "v_readlane_b32 s47, v6, 3\n" \
  "buffer_load_dwordx4 a[8:11], v18, s[36:39], s46 offen sc1\n" \
  "buffer_load_dwordx4 a[88:91], v19, s[36:39], s46 offen sc1\n" \
  "buffer_load_dwordx4 a[168:171], v20, s[36:39], s46 offen sc1\n" \
  "buffer_load_dwordx4 a[248:251], v21, s[36:39], s46 offen sc1\n" \
  "buffer_load_dwordx4 v[184:187], v22, s[36:39], s46 offen sc1\n" \
  "s_barrier\n" \
  "v_readlane_b32 s46, v6, 4\n" \
  "buffer_load_dwordx4 a[12:15], v18, s[36:39], s47 offen sc1\n" \
  "buffer_load_dwordx4 a[92:95], v19, s[36:39], s47 offen sc1\n" \
  "buffer_load_dwordx4 a[172:175], v20, s[36:39], s47 offen sc1\n" \
  "buffer_load_dwordx4 a[252:255], v21, s[36:39], s47 offen sc1\n" \
  "buffer_load_dwordx4 v[188:191], v22, s[36:39], s47 offen sc1\n" \
  "v_readlane_b32 s47, v6, 5\n" \
  "buffer_load_dwordx4 a[16:19], v18, s[36:39], s46 offen sc1\n" \
  "buffer_load_dwordx4 a[96:99], v19, s[36:39], s46 offen sc1\n" \
  "buffer_load_dwordx4 a[176:179], v20, s[36:39], s46 offen sc1\n" \
  "buffer_load_dwordx4 v[112:115], v21, s[36:39], s46 offen sc1\n" \
  "buffer_load_dwordx4 v[192:195], v22, s[36:39], s46 offen sc1\n" \
  "v_readlane_b32 s46, v6, 6\n" \
  "buffer_load_dwordx4 a[20:23], v18, s[36:39], s47 offen sc1\n" \
  "buffer_load_dwordx4 a[100:103], v19, s[36:39], s47 offen sc1\n" \
  "buffer_load_dwordx4 a[180:183], v20, s[36:39], s47 offen sc1\n" \
  "buffer_load_dwordx4 v[116:119], v21, s[36:39], s47 offen sc1\n" \
  "buffer_load_dwordx4 v[196:199], v22, s[36:39], s47 offen sc1\n" \
  "v_readlane_b32 s47, v6, 7\n" \
  "buffer_load_dwordx4 a[24:27], v18, s[36:39], s46 offen sc1\n" \
  "buffer_load_dwordx4 a[104:107], v19, s[36:39], s46 offen sc1\n" \
  "buffer_load_dwordx4 a[184:187], v20, s[36:39], s46 offen sc1\n" \
  "buffer_load_dwordx4 v[120:123], v21, s[36:39], s46 offen sc1\n" \
  "buffer_load_dwordx4 v[200:203], v22, s[36:39], s46 offen sc1\n" \
  "v_readlane_b32 s46, v6, 8\n" \
  "buffer_load_dwordx4 a[28:31], v18, s[36:39], s47 offen sc1\n" \
  "buffer_load_dwordx4 a[108:111], v19, s[36:39], s47 offen sc1\n" \
  "buffer_load_dwordx4 a[188:191], v20, s[36:39], s47 offen sc1\n" \
  "buffer_load_dwordx4 v[124:127], v21, s[36:39], s47 offen sc1\n" \
  "buffer_load_dwordx4 v[204:207], v22, s[36:39], s47 offen sc1\n" \
  "s_barrier\n" \
  "v_readlane_b32 s47, v6, 9\n" \
  "buffer_load_dwordx4 a[32:35], v18, s[36:39], s46 offen sc1\n" \
  "buffer_load_dwordx4 a[112:115], v19, s[36:39], s46 offen sc1\n" \
  "buffer_load_dwordx4 a[192:195], v20, s[36:39], s46 offen sc1\n" \
  "buffer_load_dwordx4 v[128:131], v21, s[36:39], s46 offen sc1\n" \
  "buffer_load_dwordx4 v[208:211], v22, s[36:39], s46 offen sc1\n" \
  "v_readlane_b32 s46, v6, 10\n" \
  "buffer_load_dwordx4 a[36:39], v18, s[36:39], s47 offen sc1\n" \
  "buffer_load_dwordx4 a[116:119], v19, s[36:39], s47 offen sc1\n" \
  "buffer_load_dwordx4 a[196:199], v20, s[36:39], s47 offen sc1\n" \
  "buffer_load_dwordx4 v[132:135], v21, s[36:39], s47 offen sc1\n" \
  "buffer_load_dwordx4 v[212:215], v22, s[36:39], s47 offen sc1\n" \
  "v_readlane_b32 s47, v6, 11\n" \
  "buffer_load_dwordx4 a[40:43], v18, s[36:39], s46 offen sc1\n" \
  "buffer_load_dwordx4 a[120:123], v19, s[36:39], s46 offen sc1\n" \
  "buffer_load_dwordx4 a[200:203], v20, s[36:39], s46 offen sc1\n" \
  "buffer_load_dwordx4 v[136:139], v21, s[36:39], s46 offen sc1\n" \
  "buffer_load_dwordx4 v[216:219], v22, s[36:39], s46 offen sc1\n" \
  "v_readlane_b32 s46, v6, 12\n" \
  "buffer_load_dwordx4 a[44:47], v18, s[36:39], s47 offen sc1\n" \
  "buffer_load_dwordx4 a[124:127], v19, s[36:39], s47 offen sc1\n" \
  "buffer_load_dwordx4 a[204:207], v20, s[36:39], s47 offen sc1\n" \
  "buffer_load_dwordx4 v[140:143], v21, s[36:39], s47 offen sc1\n" \
  "buffer_load_dwordx4 v[220:223], v22, s[36:39], s47 offen sc1\n" \
  "v_readlane_b32 s47, v6, 13\n" \
  "buffer_load_dwordx4 a[48:51], v18, s[36:39], s46 offen sc1\n" \
  "buffer_load_dwordx4 a[128:131], v19, s[36:39], s46 offen sc1\n" \
  "buffer_load_dwordx4 a[208:211], v20, s[36:39], s46 offen sc1\n" \
  "buffer_load_dwordx4 v[144:147], v21, s[36:39], s46 offen sc1\n" \
  "buffer_load_dwordx4 v[224:227], v22, s[36:39], s46 offen sc1\n" \
  "s_barrier\n" \
  "v_readlane_b32 s46, v6, 14\n" \
  "buffer_load_dwordx4 a[52:55], v18, s[36:39], s47 offen sc1\n" \
  "buffer_load_dwordx4 a[132:135], v19, s[36:39], s47 offen sc1\n" \
  "buffer_load_dwordx4 a[212:215], v20, s[36:39], s47 offen sc1\n" \
  "buffer_load_dwordx4 v[148:151], v21, s[36:39], s47 offen sc1\n" \
  "buffer_load_dwordx4 v[228:231], v22, s[36:39], s47 offen sc1\n" \
  "v_readlane_b32 s47, v6, 15\n" \
  "buffer_load_dwordx4 a[56:59], v18, s[36:39], s46 offen sc1\n" \
  "buffer_load_dwordx4 a[136:139], v19, s[36:39], s46 offen sc1\n" \
  "buffer_load_dwordx4 a[216:219], v20, s[36:39], s46 offen sc1\n" \
  "buffer_load_dwordx4 v[152:155], v21, s[36:39], s46 offen sc1\n" \
  "buffer_load_dwordx4 v[232:235], v22, s[36:39], s46 offen sc1\n" \
  "v_readlane_b32 s46, v6, 16\n" \
  "buffer_load_dwordx4 a[60:63], v18, s[36:39], s47 offen sc1\n" \
  "buffer_load_dwordx4 a[140:143], v19, s[36:39], s47 offen sc1\n" \
  "buffer_load_dwordx4 a[220:223], v20, s[36:39], s47 offen sc1\n" \
  "buffer_load_dwordx4 v[156:159], v21, s[36:39], s47 offen sc1\n" \
  "buffer_load_dwordx4 v[236:239], v22, s[36:39], s47 offen sc1\n" \
  "v_readlane_b32 s47, v6, 17\n" \
  "buffer_load_dwordx4 a[64:67], v18, s[36:39], s46 offen sc1\n" \
  "buffer_load_dwordx4 a[144:147], v19, s[36:39], s46 offen sc1\n" \
  "buffer_load_dwordx4 a[224:227], v20, s[36:39], s46 offen sc1\n" \
  "buffer_load_dwordx4 v[160:163], v21, s[36:39], s46 offen sc1\n" \
  "buffer_load_dwordx4 v[240:243], v22, s[36:39], s46 offen sc1\n" \
  "v_readlane_b32 s46, v6, 18\n" \
  "buffer_load_dwordx4 a[68:71], v18, s[36:39], s47 offen sc1\n" \
  "buffer_load_dwordx4 a[148:151], v19, s[36:39], s47 offen sc1\n" \
  "buffer_load_dwordx4 a[228:231], v20, s[36:39], s47 offen sc1\n" \
  "buffer_load_dwordx4 v[164:167], v21, s[36:39], s47 offen sc1\n" \
  "buffer_load_dwordx4 v[244:247], v22, s[36:39], s47 offen sc1\n" \
  "buffer_atomic_add v15, v7, s[36:39], 0 offen sc1\n" \
  "v_readlane_b32 s47, v6, 19\n" \
  "buffer_load_dwordx4 a[72:75], v18, s[36:39], s46 offen sc1\n" \
  "buffer_load_dwordx4 a[152:155], v19, s[36:39], s46 offen sc1\n" \
  "buffer_load_dwordx4 a[232:235], v20, s[36:39], s46 offen sc1\n" \
  "buffer_load_dwordx4 v[168:171], v21, s[36:39], s46 offen sc1\n" \
  "buffer_load_dwordx4 v[248:251], v22, s[36:39], s46 offen sc1\n" \
  "buffer_load_dwordx4 a[76:79], v18, s[36:39], s47 offen sc1\n" \
  "buffer_load_dwordx4 a[156:159], v19, s[36:39], s47 offen sc1\n" \
  "buffer_load_dwordx4 a[236:239], v20, s[36:39], s47 offen sc1\n" \
  "buffer_load_dwordx4 v[172:175], v21, s[36:39], s47 offen sc1\n" \
  "buffer_load_dwordx4 v[252:255], v22, s[36:39], s47 offen sc1\n" \
  "s_cmp_eq_u32 s52, 1\n" \
  "s_cbranch_scc1 LSTARTc1_%=\n" \
  "s_cmp_eq_u32 s52, 2\n" \
  "s_cbranch_scc1 LSTARTc2_%=\n" \
  "s_mov_b32 s57, 0\n" \
  "v_readlane_b32 s44, v4, 0\n" \
  "s_nop 3\n" \
  "LOOPc0_%=:\n" \
  "s_mov_b32 m0, 0\n" \
  "s_add_u32 s75, s44, 0\n" \
  "s_barrier\n" \
  "buffer_load_dwordx4 v1, s[36:39], s75 offen offset:0 sc1 lds\n" \
  "buffer_load_dwordx4 v1, s[36:39], s75 offen offset:1024 sc1 lds\n" \
  "buffer_load_dwordx4 v1, s[36:39], s75 offen offset:2048 sc1 lds\n" \
  "buffer_load_dwordx4 v1, s[36:39], s75 offen offset:3072 sc1 lds\n" \
  "v_mov_b32 v56, 0\n" \
  "v_mov_b32 v57, 0\n" \
  "v_mov_b32 v58, 0\n" \
  "v_mov_b32 v59, 0\n" \
  "v_mov_b32 v60, 0\n" \
  "v_mov_b32 v61, 0\n" \
  "v_mov_b32 v62, 0\n" \
  "v_mov_b32 v63, 0\n" \
  "v_mov_b32 v64, 0\n" \
  "v_mov_b32 v65, 0\n" \
  "v_mov_b32 v66, 0\n" \
  "v_mov_b32 v67, 0\n" \
  "v_mov_b32 v68, 0\n" \
  "v_mov_b32 v69, 0\n" \
  "v_mov_b32 v70, 0\n" \
  "v_mov_b32 v71, 0\n" \
  "v_mov_b32 v72, 0\n" \
  "v_mov_b32 v73, 0\n" \
  "v_mov_b32 v74, 0\n" \
  "v_mov_b32 v75, 0\n" \
  "s_add_u32 s68, s68, 1\n" \
  "s_min_u32 s74, s68, 63\n" \
  "v_readlane_b32 s45, v4, s74\n" \
  "s_waitcnt vmcnt(0)\n" \
  "s_barrier\n" \
  "buffer_store_dwordx4 v[76:79], v10, s[36:39], s42 offen offset:0 sc1\n" \
  "buffer_store_dwordx4 v[80:83], v10, s[36:39], s42 offen offset:1024 sc1\n" \
  "buffer_store_dwordx4 v[84:87], v10, s[36:39], s42 offen offset:2048 sc1\n" \
  "buffer_store_dwordx4 v[88:91], v10, s[36:39], s42 offen offset:3072 sc1\n" \
  "LMc0_%=:\n" \
  "ds_read_b128 v[16:19], v1 offset:0\n" \
  "ds_read_b128 v[20:23], v1 offset:1024\n" \
  "ds_read_b128 v[24:27], v1 offset:2048\n" \
  "ds_read_b128 v[28:31], v1 offset:3072\n" \
  "ds_read_b128 v[32:35], v1 offset:4096\n" \
  "ds_read_b128 v[36:39], v1 offset:5120\n" \
  "ds_read_b128 v[40:43], v1 offset:6144\n" \
  "ds_read_b128 v[44:47], v1 offset:7168\n" \
  "ds_read_b128 v[48:51], v1 offset:8192\n" \
  "ds_read_b128 v[52:55], v1 offset:9216\n" \
  "s_waitcnt lgkmcnt(9)\n" \
  "v_smfmac_f32_16x16x64_bf16 v[56:59], v[16:19], a[0:7], v0\n" \
  "s_waitcnt lgkmcnt(8)\n" \
  "v_smfmac_f32_16x16x64_bf16 v[56:59], v[20:23], a[8:15], v0\n" \
  "s_waitcnt lgkmcnt(7)\n" \
  "v_smfmac_f32_16x16x64_bf16 v[56:59], v[24:27], a[16:23], v0\n" \
  "s_waitcnt lgkmcnt(6)\n" \
  "v_smfmac_f32_16x16x64_bf16 v[56:59], v[28:31], a[24:31], v0\n" \
  "s_waitcnt lgkmcnt(5)\n" \
  "v_smfmac_f32_16x16x64_bf16 v[56:59], v[32:35], a[32:39], v0\n" \
  "s_waitcnt lgkmcnt(4)\n" \
  "v_smfmac_f32_16x16x64_bf16 v[56:59], v[36:39], a[40:47], v0\n" \
  "s_waitcnt lgkmcnt(3)\n" \
  "v_smfmac_f32_16x16x64_bf16 v[56:59], v[40:43], a[48:55], v0\n" \
  "s_waitcnt lgkmcnt(2)\n" \
  "v_smfmac_f32_16x16x64_bf16 v[56:59], v[44:47], a[56:63], v0\n" \
  "s_waitcnt lgkmcnt(1)\n" \
  "v_smfmac_f32_16x16x64_bf16 v[56:59], v[48:51], a[64:71], v0\n" \
  "s_waitcnt lgkmcnt(0)\n" \
  "v_smfmac_f32_16x16x64_bf16 v[56:59], v[52:55], a[72:79], v0\n" \
  "v_smfmac_f32_16x16x64_bf16 v[60:63], v[16:19], a[80:87], v0\n" \
  "v_smfmac_f32_16x16x64_bf16 v[64:67], v[16:19], a[160:167], v0\n" \
  "v_cmp_u_f32 vcc, v56, v57\n" \
  "v_cmp_u_f32 s[82:83], v58, v59\n" \
  "s_nop 1\n" \
  "s_or_b64 vcc, vcc, s[82:83]\n" \
  "s_cbranch_scc1 LMFc0_%=\n" \
  "LMOKc0_%=:\n" \
  "v_smfmac_f32_16x16x64_bf16 v[68:71], v[16:19], a[240:247], v0\n" \
  "v_smfmac_f32_16x16x64_bf16 v[72:75], v[16:19], v[176:183], v0\n" \
  "v_smfmac_f32_16x16x64_bf16 v[60:63], v[20:23], a[88:95], v0\n" \
  "v_smfmac_f32_16x16x64_bf16 v[64:67], v[20:23], a[168:175], v0\n" \
  "v_smfmac_f32_16x16x64_bf16 v[68:71], v[20:23], a[248:255], v0\n" \
  "v_smfmac_f32_16x16x64_bf16 v[72:75], v[20:23], v[184:191], v0\n" \
  "v_smfmac_f32_16x16x64_bf16 v[60:63], v[24:27], a[96:103], v0\n" \
  "v_smfmac_f32_16x16x64_bf16 v[64:67], v[24:27], a[176:183], v0\n" \
  "v_smfmac_f32_16x16x64_bf16 v[68:71], v[24:27], v[112:119], v0\n" \
  "v_smfmac_f32_16x16x64_bf16 v[72:75], v[24:27], v[192:199], v0\n" \
  "v_smfmac_f32_16x16x64_bf16 v[60:63], v[28:31], a[104:111], v0\n" \
  "v_smfmac_f32_16x16x64_bf16 v[64:67], v[28:31], a[184:191], v0\n" \
  "v_smfmac_f32_16x16x64_bf16 v[68:71], v[28:31], v[120:127], v0\n" \
  "v_smfmac_f32_16x16x64_bf16 v[72:75], v[28:31], v[200:207], v0\n" \
  "v_smfmac_f32_16x16x64_bf16 v[60:63], v[32:35], a[112:119], v0\n" \
  "v_smfmac_f32_16x16x64_bf16 v[64:67], v[32:35], a[192:199], v0\n" \
  "v_smfmac_f32_16x16x64_bf16 v[68:71], v[32:35], v[128:135], v0\n" \
  "v_smfmac_f32_16x16x64_bf16 v[72:75], v[32:35], v[208:215], v0\n" \
  "v_smfmac_f32_16x16x64_bf16 v[60:63], v[36:39], a[120:127], v0\n" \
  "v_smfmac_f32_16x16x64_bf16 v[64:67], v[36:39], a[200:207], v0\n" \
  "v_smfmac_f32_16x16x64_bf16 v[68:71], v[36:39], v[136:143], v0\n" \
  "v_smfmac_f32_16x16x64_bf16 v[72:75], v[36:39], v[216:223], v0\n" \
  "v_smfmac_f32_16x16x64_bf16 v[60:63], v[40:43], a[128:135], v0\n" \
  "v_smfmac_f32_16x16x64_bf16 v[64:67], v[40:43], a[208:215], v0\n" \
  "v_smfmac_f32_16x16x64_bf16 v[68:71], v[40:43], v[144:151], v0\n" \
  "v_smfmac_f32_16x16x64_bf16 v[72:75], v[40:43], v[224:231], v0\n" \
  "v_smfmac_f32_16x16x64_bf16 v[60:63], v[44:47], a[136:143], v0\n" \
  "v_smfmac_f32_16x16x64_bf16 v[64:67], v[44:47], a[216:223], v0\n" \
  "v_smfmac_f32_16x16x64_bf16 v[68:71], v[44:47], v[152:159], v0\n" \
  "v_smfmac_f32_16x16x64_bf16 v[72:75], v[44:47], v[232:239], v0\n" \
  "v_smfmac_f32_16x16x64_bf16 v[60:63], v[48:51], a[144:151], v0\n" \
  "v_smfmac_f32_16x16x64_bf16 v[64:67], v[48:51], a[224:231], v0\n" \
  "v_smfmac_f32_16x16x64_bf16 v[68:71], v[48:51], v[160:167], v0\n" \
  "v_smfmac_f32_16x16x64_bf16 v[72:75], v[48:51], v[240:247], v0\n" \
  "v_smfmac_f32_16x16x64_bf16 v[60:63], v[52:55], a[152:159], v0\n" \
  "v_smfmac_f32_16x16x64_bf16 v[64:67], v[52:55], a[232:239], v0\n" \
  "v_smfmac_f32_16x16x64_bf16 v[68:71], v[52:55], v[168:175], v0\n" \
  "v_smfmac_f32_16x16x64_bf16 v[72:75], v[52:55], v[248:255], v0\n" \
  "v_permlane32_swap_b32 v56, v60\n" \
  "v_permlane32_swap_b32 v57, v61\n" \
  "v_permlane32_swap_b32 v58, v62\n" \
  "v_permlane32_swap_b32 v59, v63\n" \
  "v_pk_add_f32 v[56:57], v[56:57], v[60:61]\n" \
  "v_pk_add_f32 v[58:59], v[58:59], v[62:63]\n" \
  "v_cvt_pk_bf16_f32 v56, v56, v57\n" \
  "v_cvt_pk_bf16_f32 v57, v58, v59\n" \
  "ds_write_b64 v2, v[56:57] offset:0\n" \
  "v_permlane32_swap_b32 v64, v68\n" \
  "v_permlane32_swap_b32 v65, v69\n" \
  "v_permlane32_swap_b32 v66, v70\n" \
  "v_permlane32_swap_b32 v67, v71\n" \
  "v_pk_add_f32 v[64:65], v[64:65], v[68:69]\n" \
  "v_pk_add_f32 v[66:67], v[66:67], v[70:71]\n" \
  "v_cvt_pk_bf16_f32 v64, v64, v65\n" \
  "v_cvt_pk_bf16_f32 v65, v66, v67\n" \
  "ds_write_b64 v2, v[64:65] offset:512\n" \
  "v_permlane32_swap_b32 v72, v74\n" \
  "v_permlane32_swap_b32 v73, v75\n" \
  "v_pk_add_f32 v[72:73], v[72:73], v[74:75]\n" \
  "v_cvt_pk_bf16_f32 v72, v72, v73\n" \
  "ds_write_b32 v3, v72 offset:1024\n" \
  "s_waitcnt lgkmcnt(0)\n" \
  "s_waitcnt vmcnt(0)\n" \
  "s_barrier\n" \
  "ds_read_b128 v[76:79], v1 offset:10240\n" \
  "ds_read_b128 v[80:83], v1 offset:11264\n" \
  "ds_read_b128 v[84:87], v1 offset:12288\n" \
  "ds_read_b128 v[88:91], v1 offset:13312\n" \
  "s_sub_u32 s74, s68, 1\n" \
  "v_readlane_b32 s42, v9, s74\n" \
  "s_waitcnt lgkmcnt(0)\n" \
  "s_mov_b32 s44, s45\n" \
  "s_barrier\n" \
  "s_cmp_eq_u32 s68, 64\n" \
  "s_cbranch_scc0 LOOPc0_%=\n" \
  "buffer_store_dwordx4 v[76:79], v10, s[36:39], s42 offen offset:0 sc1\n" \
  "buffer_store_dwordx4 v[80:83], v10, s[36:39], s42 offen offset:1024 sc1\n" \
  "buffer_store_dwordx4 v[84:87], v10, s[36:39], s42 offen offset:2048 sc1\n" \
  "buffer_store_dwordx4 v[88:91], v10, s[36:39], s42 offen offset:3072 sc1\n" \
  "s_waitcnt vmcnt(0)\n" \
  "buffer_atomic_add v15, v11, s[36:39], 0 offen sc1\n" \
  "s_branch LDONE_%=\n" \
  "LMFc0_%=:\n" \
  "v_max_u32 v92, v16, v17\n" \
  "v_max3_u32 v92, v92, v18, v19\n" \
  "v_max3_u32 v92, v92, v20, v21\n" \
  "v_max3_u32 v92, v92, v22, v23\n" \
  "v_max3_u32 v92, v92, v24, v25\n" \
  "v_max3_u32 v92, v92, v26, v27\n" \
  "v_max3_u32 v92, v92, v28, v29\n" \
  "v_max3_u32 v92, v92, v30, v31\n" \
  "v_max3_u32 v92, v92, v32, v33\n" \
  "v_max3_u32 v92, v92, v34, v35\n" \
  "v_max3_u32 v92, v92, v36, v37\n" \
  "v_max3_u32 v92, v92, v38, v39\n" \
  "v_max3_u32 v92, v92, v40, v41\n" \
  "v_max3_u32 v92, v92, v42, v43\n" \
  "v_max3_u32 v92, v92, v44, v45\n" \
  "v_max3_u32 v92, v92, v46, v47\n" \
  "v_max3_u32 v92, v92, v48, v49\n" \
  "v_max3_u32 v92, v92, v50, v51\n" \
  "v_max3_u32 v92, v92, v52, v53\n" \
  "v_max3_u32 v92, v92, v54, v55\n" \
  "v_cmp_eq_u32 vcc, -1, v92\n" \
  "s_nop 3\n" \
  "s_cbranch_vccz LMOKc0_%=\n" \
  "s_cmp_gt_u32 s57, 20000\n" \
  "s_cbranch_scc1 LMOKc0_%=\n" \
  "s_add_u32 s57, s57, 1\n" \
  "s_barrier\n" \
  "buffer_load_dwordx4 v1, s[36:39], s75 offen offset:0 sc1 lds\n" \
  "buffer_load_dwordx4 v1, s[36:39], s75 offen offset:1024 sc1 lds\n" \
  "buffer_load_dwordx4 v1, s[36:39], s75 offen offset:2048 sc1 lds\n" \
  "buffer_load_dwordx4 v1, s[36:39], s75 offen offset:3072 sc1 lds\n" \
  "v_mov_b32 v56, 0\n" \
  "v_mov_b32 v57, 0\n" \
  "v_mov_b32 v58, 0\n" \
  "v_mov_b32 v59, 0\n" \
  "v_mov_b32 v60, 0\n" \
  "v_mov_b32 v61, 0\n" \
  "v_mov_b32 v62, 0\n" \
  "v_mov_b32 v63, 0\n" \
  "v_mov_b32 v64, 0\n" \
  "v_mov_b32 v65, 0\n" \
  "v_mov_b32 v66, 0\n" \
  "v_mov_b32 v67, 0\n" \
  "v_mov_b32 v68, 0\n" \
  "v_mov_b32 v69, 0\n" \
  "v_mov_b32 v70, 0\n" \
  "v_mov_b32 v71, 0\n" \
  "v_mov_b32 v72, 0\n" \
  "v_mov_b32 v73, 0\n" \
  "v_mov_b32 v74, 0\n" \
  "v_mov_b32 v75, 0\n" \
  "s_waitcnt vmcnt(0)\n" \
  "s_barrier\n" \
  "s_branch LMc0_%=\n" \
  "LSTARTc1_%=:\n" \
  "s_mov_b32 s57, 0\n" \
  "v_readlane_b32 s44, v4, 0\n" \
  "s_nop 3\n" \
  "LOOPc1_%=:\n" \
  "s_mov_b32 m0, 4096\n" \
  "s_add_u32 s75, s44, 4096\n" \
  "s_barrier\n" \
  "buffer_load_dwordx4 v1, s[36:39], s75 offen offset:0 sc1 lds\n" \
  "buffer_load_dwordx4 v1, s[36:39], s75 offen offset:1024 sc1 lds\n" \
  "buffer_load_dwordx4 v1, s[36:39], s75 offen offset:2048 sc1 lds\n" \
  "buffer_load_dwordx4 v1, s[36:39], s75 offen offset:3072 sc1 lds\n" \
  "v_mov_b32 v56, 0\n" \
  "v_mov_b32 v57, 0\n" \
  "v_mov_b32 v58, 0\n" \
  "v_mov_b32 v59, 0\n" \
  "v_mov_b32 v60, 0\n" \
  "v_mov_b32 v61, 0\n" \
  "v_mov_b32 v62, 0\n" \
  "v_mov_b32 v63, 0\n" \
  "v_mov_b32 v64, 0\n" \
  "v_mov_b32 v65, 0\n" \
  "v_mov_b32 v66, 0\n" \
  "v_mov_b32 v67, 0\n" \
  "v_mov_b32 v68, 0\n" \
  "v_mov_b32 v69, 0\n" \
  "v_mov_b32 v70, 0\n" \
  "v_mov_b32 v71, 0\n" \
  "v_mov_b32 v72, 0\n" \
  "v_mov_b32 v73, 0\n" \
  "v_mov_b32 v74, 0\n" \
  "v_mov_b32 v75, 0\n" \
  "s_add_u32 s68, s68, 1\n" \
  "s_min_u32 s74, s68, 63\n" \
  "v_readlane_b32 s45, v4, s74\n" \
  "s_waitcnt vmcnt(0)\n" \
  "s_barrier\n" \
  "LMc1_%=:\n" \
  "ds_read_b128 v[16:19], v1 offset:0\n" \
  "ds_read_b128 v[20:23], v1 offset:1024\n" \
  "ds_read_b128 v[24:27], v1 offset:2048\n" \
  "ds_read_b128 v[28:31], v1 offset:3072\n" \
  "ds_read_b128 v[32:35], v1 offset:4096\n" \
  "ds_read_b128 v[36:39], v1 offset:5120\n" \
  "ds_read_b128 v[40:43], v1 offset:6144\n" \
  "ds_read_b128 v[44:47], v1 offset:7168\n" \
  "ds_read_b128 v[48:51], v1 offset:8192\n" \
  "ds_read_b128 v[52:55], v1 offset:9216\n" \
  "s_waitcnt lgkmcnt(9)\n" \
  "v_smfmac_f32_16x16x64_bf16 v[56:59], v[16:19], a[0:7], v0\n" \
  "s_waitcnt lgkmcnt(8)\n" \
  "v_smfmac_f32_16x16x64_bf16 v[56:59], v[20:23], a[8:15], v0\n" \
  "s_waitcnt lgkmcnt(7)\n" \
  "v_smfmac_f32_16x16x64_bf16 v[56:59], v[24:27], a[16:23], v0\n" \
  "s_waitcnt lgkmcnt(6)\n" \
  "v_smfmac_f32_16x16x64_bf16 v[56:59], v[28:31], a[24:31], v0\n" \
  "s_waitcnt lgkmcnt(5)\n" \
  "v_smfmac_f32_16x16x64_bf16 v[56:59], v[32:35], a[32:39], v0\n" \
  "s_waitcnt lgkmcnt(4)\n" \
  "v_smfmac_f32_16x16x64_bf16 v[56:59], v[36:39], a[40:47], v0\n" \
  "s_waitcnt lgkmcnt(3)\n" \
  "v_smfmac_f32_16x16x64_bf16 v[56:59], v[40:43], a[48:55], v0\n" \
  "s_waitcnt lgkmcnt(2)\n" \
  "v_smfmac_f32_16x16x64_bf16 v[56:59], v[44:47], a[56:63], v0\n" \
  "s_waitcnt lgkmcnt(1)\n" \
  "v_smfmac_f32_16x16x64_bf16 v[56:59], v[48:51], a[64:71], v0\n" \
  "s_waitcnt lgkmcnt(0)\n" \
  "v_smfmac_f32_16x16x64_bf16 v[56:59], v[52:55], a[72:79], v0\n" \
  "v_smfmac_f32_16x16x64_bf16 v[60:63], v[16:19], a[80:87], v0\n" \
  "v_smfmac_f32_16x16x64_bf16 v[64:67], v[16:19], a[160:167], v0\n" \
  "v_cmp_u_f32 vcc, v56, v57\n" \
  "v_cmp_u_f32 s[82:83], v58, v59\n" \
  "s_nop 1\n" \
  "s_or_b64 vcc, vcc, s[82:83]\n" \
  "s_cbranch_scc1 LMFc1_%=\n" \
  "LMOKc1_%=:\n" \
  "v_smfmac_f32_16x16x64_bf16 v[68:71], v[16:19], a[240:247], v0\n" \
  "v_smfmac_f32_16x16x64_bf16 v[72:75], v[16:19], v[176:183], v0\n" \
  "v_smfmac_f32_16x16x64_bf16 v[60:63], v[20:23], a[88:95], v0\n" \
  "v_smfmac_f32_16x16x64_bf16 v[64:67], v[20:23], a[168:175], v0\n" \
  "v_smfmac_f32_16x16x64_bf16 v[68:71], v[20:23], a[248:255], v0\n" \
  "v_smfmac_f32_16x16x64_bf16 v[72:75], v[20:23], v[184:191], v0\n" \
  "v_smfmac_f32_16x16x64_bf16 v[60:63], v[24:27], a[96:103], v0\n" \
  "v_smfmac_f32_16x16x64_bf16 v[64:67], v[24:27], a[176:183], v0\n" \
  "v_smfmac_f32_16x16x64_bf16 v[68:71], v[24:27], v[112:119], v0\n" \
  "v_smfmac_f32_16x16x64_bf16 v[72:75], v[24:27], v[192:199], v0\n" \
  "v_smfmac_f32_16x16x64_bf16 v[60:63], v[28:31], a[104:111], v0\n" \
  "v_smfmac_f32_16x16x64_bf16 v[64:67], v[28:31], a[184:191], v0\n" \
  "v_smfmac_f32_16x16x64_bf16 v[68:71], v[28:31], v[120:127], v0\n" \
  "v_smfmac_f32_16x16x64_bf16 v[72:75], v[28:31], v[200:207], v0\n" \
  "v_smfmac_f32_16x16x64_bf16 v[60:63], v[32:35], a[112:119], v0\n" \
  "v_smfmac_f32_16x16x64_bf16 v[64:67], v[32:35], a[192:199], v0\n" \
  "v_smfmac_f32_16x16x64_bf16 v[68:71], v[32:35], v[128:135], v0\n" \
  "v_smfmac_f32_16x16x64_bf16 v[72:75], v[32:35], v[208:215], v0\n" \
  "v_smfmac_f32_16x16x64_bf16 v[60:63], v[36:39], a[120:127], v0\n" \
  "v_smfmac_f32_16x16x64_bf16 v[64:67], v[36:39], a[200:207], v0\n" \
  "v_smfmac_f32_16x16x64_bf16 v[68:71], v[36:39], v[136:143], v0\n" \
  "v_smfmac_f32_16x16x64_bf16 v[72:75], v[36:39], v[216:223], v0\n" \
  "v_smfmac_f32_16x16x64_bf16 v[60:63], v[40:43], a[128:135], v0\n" \
  "v_smfmac_f32_16x16x64_bf16 v[64:67], v[40:43], a[208:215], v0\n" \
  "v_smfmac_f32_16x16x64_bf16 v[68:71], v[40:43], v[144:151], v0\n" \
  "v_smfmac_f32_16x16x64_bf16 v[72:75], v[40:43], v[224:231], v0\n" \
  "v_smfmac_f32_16x16x64_bf16 v[60:63], v[44:47], a[136:143], v0\n" \
  "v_smfmac_f32_16x16x64_bf16 v[64:67], v[44:47], a[216:223], v0\n" \
  "v_smfmac_f32_16x16x64_bf16 v[68:71], v[44:47], v[152:159], v0\n" \
  "v_smfmac_f32_16x16x64_bf16 v[72:75], v[44:47], v[232:239], v0\n" \
  "v_smfmac_f32_16x16x64_bf16 v[60:63], v[48:51], a[144:151], v0\n" \
  "v_smfmac_f32_16x16x64_bf16 v[64:67], v[48:51], a[224:231], v0\n" \
  "v_smfmac_f32_16x16x64_bf16 v[68:71], v[48:51], v[160:167], v0\n" \
  "v_smfmac_f32_16x16x64_bf16 v[72:75], v[48:51], v[240:247], v0\n" \
  "v_smfmac_f32_16x16x64_bf16 v[60:63], v[52:55], a[152:159], v0\n" \
  "v_smfmac_f32_16x16x64_bf16 v[64:67], v[52:55], a[232:239], v0\n" \
  "v_smfmac_f32_16x16x64_bf16 v[68:71], v[52:55], v[168:175], v0\n" \
  "v_smfmac_f32_16x16x64_bf16 v[72:75], v[52:55], v[248:255], v0\n" \
  "v_permlane32_swap_b32 v56, v60\n" \
  "v_permlane32_swap_b32 v57, v61\n" \
  "v_permlane32_swap_b32 v58, v62\n" \
  "v_permlane32_swap_b32 v59, v63\n" \
  "v_pk_add_f32 v[56:57], v[56:57], v[60:61]\n" \
  "v_pk_add_f32 v[58:59], v[58:59], v[62:63]\n" \
  "v_cvt_pk_bf16_f32 v56, v56, v57\n" \
  "v_cvt_pk_bf16_f32 v57, v58, v59\n" \
  "ds_write_b64 v2, v[56:57] offset:1280\n" \
  "v_permlane32_swap_b32 v64, v68\n" \
  "v_permlane32_swap_b32 v65, v69\n" \
  "v_permlane32_swap_b32 v66, v70\n" \
  "v_permlane32_swap_b32 v67, v71\n" \
  "v_pk_add_f32 v[64:65], v[64:65], v[68:69]\n" \
  "v_pk_add_f32 v[66:67], v[66:67], v[70:71]\n" \
  "v_cvt_pk_bf16_f32 v64, v64, v65\n" \
  "v_cvt_pk_bf16_f32 v65, v66, v67\n" \
  "ds_write_b64 v2, v[64:65] offset:1792\n" \
  "v_permlane32_swap_b32 v72, v74\n" \
  "v_permlane32_swap_b32 v73, v75\n" \
  "v_pk_add_f32 v[72:73], v[72:73], v[74:75]\n" \
  "v_cvt_pk_bf16_f32 v72, v72, v73\n" \
  "ds_write_b32 v3, v72 offset:2304\n" \
  "s_waitcnt lgkmcnt(0)\n" \
  "s_barrier\n" \
  "s_mov_b32 s44, s45\n" \
  "s_barrier\n" \
  "s_cmp_eq_u32 s68, 64\n" \
  "s_cbranch_scc0 LOOPc1_%=\n" \
  "s_branch LDONE_%=\n" \
  "LMFc1_%=:\n" \
  "v_max_u32 v92, v16, v17\n" \
  "v_max3_u32 v92, v92, v18, v19\n" \
  "v_max3_u32 v92, v92, v20, v21\n" \
  "v_max3_u32 v92, v92, v22, v23\n" \
  "v_max3_u32 v92, v92, v24, v25\n" \
  "v_max3_u32 v92, v92, v26, v27\n" \
  "v_max3_u32 v92, v92, v28, v29\n" \
  "v_max3_u32 v92, v92, v30, v31\n" \
  "v_max3_u32 v92, v92, v32, v33\n" \
  "v_max3_u32 v92, v92, v34, v35\n" \
  "v_max3_u32 v92, v92, v36, v37\n" \
  "v_max3_u32 v92, v92, v38, v39\n" \
  "v_max3_u32 v92, v92, v40, v41\n" \
  "v_max3_u32 v92, v92, v42, v43\n" \
  "v_max3_u32 v92, v92, v44, v45\n" \
  "v_max3_u32 v92, v92, v46, v47\n" \
  "v_max3_u32 v92, v92, v48, v49\n" \
  "v_max3_u32 v92, v92, v50, v51\n" \
  "v_max3_u32 v92, v92, v52, v53\n" \
  "v_max3_u32 v92, v92, v54, v55\n" \
  "v_cmp_eq_u32 vcc, -1, v92\n" \
  "s_nop 3\n" \
  "s_cbranch_vccz LMOKc1_%=\n" \
  "s_cmp_gt_u32 s57, 20000\n" \
  "s_cbranch_scc1 LMOKc1_%=\n" \
  "s_add_u32 s57, s57, 1\n" \
  "s_barrier\n" \
  "buffer_load_dwordx4 v1, s[36:39], s75 offen offset:0 sc1 lds\n" \
  "buffer_load_dwordx4 v1, s[36:39], s75 offen offset:1024 sc1 lds\n" \
  "buffer_load_dwordx4 v1, s[36:39], s75 offen offset:2048 sc1 lds\n" \
  "buffer_load_dwordx4 v1, s[36:39], s75 offen offset:3072 sc1 lds\n" \
  "v_mov_b32 v56, 0\n" \
  "v_mov_b32 v57, 0\n" \
  "v_mov_b32 v58, 0\n" \
  "v_mov_b32 v59, 0\n" \
  "v_mov_b32 v60, 0\n" \
  "v_mov_b32 v61, 0\n" \
  "v_mov_b32 v62, 0\n" \
  "v_mov_b32 v63, 0\n" \
  "v_mov_b32 v64, 0\n" \
  "v_mov_b32 v65, 0\n" \
  "v_mov_b32 v66, 0\n" \
  "v_mov_b32 v67, 0\n" \
  "v_mov_b32 v68, 0\n" \
  "v_mov_b32 v69, 0\n" \
  "v_mov_b32 v70, 0\n" \
  "v_mov_b32 v71, 0\n" \
  "v_mov_b32 v72, 0\n" \
  "v_mov_b32 v73, 0\n" \
  "v_mov_b32 v74, 0\n" \
  "v_mov_b32 v75, 0\n" \
  "s_waitcnt vmcnt(0)\n" \
  "s_barrier\n" \
  "s_branch LMc1_%=\n" \
  "LSTARTc2_%=:\n" \
  "s_mov_b32 s57, 0\n" \
  "v_readlane_b32 s44, v4, 0\n" \
  "s_nop 3\n" \
  "LOOPc2_%=:\n" \
  "s_mov_b32 m0, 8192\n" \
  "s_add_u32 s75, s44, 16384\n" \
  "s_barrier\n" \
  "buffer_load_dwordx4 v1, s[36:39], s75 offen offset:0 sc1 lds\n" \
  "buffer_load_dwordx4 v1, s[36:39], s75 offen offset:1024 sc1 lds\n" \
  "v_mov_b32 v56, 0\n" \
  "v_mov_b32 v57, 0\n" \
  "v_mov_b32 v58, 0\n" \
  "v_mov_b32 v59, 0\n" \
  "v_mov_b32 v60, 0\n" \
  "v_mov_b32 v61, 0\n" \
  "v_mov_b32 v62, 0\n" \
  "v_mov_b32 v63, 0\n" \
  "v_mov_b32 v64, 0\n" \
  "v_mov_b32 v65, 0\n" \
  "v_mov_b32 v66, 0\n" \
  "v_mov_b32 v67, 0\n" \
  "v_mov_b32 v68, 0\n" \
  "v_mov_b32 v69, 0\n" \
  "v_mov_b32 v70, 0\n" \
  "v_mov_b32 v71, 0\n" \
  "v_mov_b32 v72, 0\n" \
  "v_mov_b32 v73, 0\n" \
  "v_mov_b32 v74, 0\n" \
  "v_mov_b32 v75, 0\n" \
  "s_add_u32 s68, s68, 1\n" \
  "s_min_u32 s74, s68, 63\n" \
  "v_readlane_b32 s45, v4, s74\n" \
  "s_waitcnt vmcnt(0)\n" \
  "s_barrier\n" \
  "LMc2_%=:\n" \
  "ds_read_b128 v[16:19], v1 offset:0\n" \
  "ds_read_b128 v[20:23], v1 offset:1024\n" \
  "ds_read_b128 v[24:27], v1 offset:2048\n" \
  "ds_read_b128 v[28:31], v1 offset:3072\n" \
  "ds_read_b128 v[32:35], v1 offset:4096\n" \
  "ds_read_b128 v[36:39], v1 offset:5120\n" \
  "ds_read_b128 v[40:43], v1 offset:6144\n" \
  "ds_read_b128 v[44:47], v1 offset:7168\n" \
  "ds_read_b128 v[48:51], v1 offset:8192\n" \
  "ds_read_b128 v[52:55], v1 offset:9216\n" \
  "s_waitcnt lgkmcnt(9)\n" \
  "v_smfmac_f32_16x16x64_bf16 v[56:59], v[16:19], a[0:7], v0\n" \
  "s_waitcnt lgkmcnt(8)\n" \
  "v_smfmac_f32_16x16x64_bf16 v[56:59], v[20:23], a[8:15], v0\n" \
  "s_waitcnt lgkmcnt(7)\n" \
  "v_smfmac_f32_16x16x64_bf16 v[56:59], v[24:27], a[16:23], v0\n" \
  "s_waitcnt lgkmcnt(6)\n" \
  "v_smfmac_f32_16x16x64_bf16 v[56:59], v[28:31], a[24:31], v0\n" \
  "s_waitcnt lgkmcnt(5)\n" \
  "v_smfmac_f32_16x16x64_bf16 v[56:59], v[32:35], a[32:39], v0\n" \
  "s_waitcnt lgkmcnt(4)\n" \
  "v_smfmac_f32_16x16x64_bf16 v[56:59], v[36:39], a[40:47], v0\n" \
  "s_waitcnt lgkmcnt(3)\n" \
  "v_smfmac_f32_16x16x64_bf16 v[56:59], v[40:43], a[48:55], v0\n" \
  "s_waitcnt lgkmcnt(2)\n" \
  "v_smfmac_f32_16x16x64_bf16 v[56:59], v[44:47], a[56:63], v0\n" \
  "s_waitcnt lgkmcnt(1)\n" \
  "v_smfmac_f32_16x16x64_bf16 v[56:59], v[48:51], a[64:71], v0\n" \
  "s_waitcnt lgkmcnt(0)\n" \
  "v_smfmac_f32_16x16x64_bf16 v[56:59], v[52:55], a[72:79], v0\n" \
  "v_smfmac_f32_16x16x64_bf16 v[60:63], v[16:19], a[80:87], v0\n" \
  "v_smfmac_f32_16x16x64_bf16 v[64:67], v[16:19], a[160:167], v0\n" \
  "v_cmp_u_f32 vcc, v56, v57\n" \
  "v_cmp_u_f32 s[82:83], v58, v59\n" \
  "s_nop 1\n" \
  "s_or_b64 vcc, vcc, s[82:83]\n" \
  "s_cbranch_scc1 LMFc2_%=\n" \
  "LMOKc2_%=:\n" \
  "v_smfmac_f32_16x16x64_bf16 v[68:71], v[16:19], a[240:247], v0\n" \
  "v_smfmac_f32_16x16x64_bf16 v[72:75], v[16:19], v[176:183], v0\n" \
  "v_smfmac_f32_16x16x64_bf16 v[60:63], v[20:23], a[88:95], v0\n" \
  "v_smfmac_f32_16x16x64_bf16 v[64:67], v[20:23], a[168:175], v0\n" \
  "v_smfmac_f32_16x16x64_bf16 v[68:71], v[20:23], a[248:255], v0\n" \
  "v_smfmac_f32_16x16x64_bf16 v[72:75], v[20:23], v[184:191], v0\n" \
  "v_smfmac_f32_16x16x64_bf16 v[60:63], v[24:27], a[96:103], v0\n" \
  "v_smfmac_f32_16x16x64_bf16 v[64:67], v[24:27], a[176:183], v0\n" \
  "v_smfmac_f32_16x16x64_bf16 v[68:71], v[24:27], v[112:119], v0\n" \
  "v_smfmac_f32_16x16x64_bf16 v[72:75], v[24:27], v[192:199], v0\n" \
  "v_smfmac_f32_16x16x64_bf16 v[60:63], v[28:31], a[104:111], v0\n" \
  "v_smfmac_f32_16x16x64_bf16 v[64:67], v[28:31], a[184:191], v0\n" \
  "v_smfmac_f32_16x16x64_bf16 v[68:71], v[28:31], v[120:127], v0\n" \
  "v_smfmac_f32_16x16x64_bf16 v[72:75], v[28:31], v[200:207], v0\n" \
  "v_smfmac_f32_16x16x64_bf16 v[60:63], v[32:35], a[112:119], v0\n" \
  "v_smfmac_f32_16x16x64_bf16 v[64:67], v[32:35], a[192:199], v0\n" \
  "v_smfmac_f32_16x16x64_bf16 v[68:71], v[32:35], v[128:135], v0\n" \
  "v_smfmac_f32_16x16x64_bf16 v[72:75], v[32:35], v[208:215], v0\n" \
  "v_smfmac_f32_16x16x64_bf16 v[60:63], v[36:39], a[120:127], v0\n" \
  "v_smfmac_f32_16x16x64_bf16 v[64:67], v[36:39], a[200:207], v0\n" \
  "v_smfmac_f32_16x16x64_bf16 v[68:71], v[36:39], v[136:143], v0\n" \
  "v_smfmac_f32_16x16x64_bf16 v[72:75], v[36:39], v[216:223], v0\n" \
  "v_smfmac_f32_16x16x64_bf16 v[60:63], v[40:43], a[128:135], v0\n" \
  "v_smfmac_f32_16x16x64_bf16 v[64:67], v[40:43], a[208:215], v0\n" \
  "v_smfmac_f32_16x16x64_bf16 v[68:71], v[40:43], v[144:151], v0\n" \
  "v_smfmac_f32_16x16x64_bf16 v[72:75], v[40:43], v[224:231], v0\n" \
  "v_smfmac_f32_16x16x64_bf16 v[60:63], v[44:47], a[136:143], v0\n" \
  "v_smfmac_f32_16x16x64_bf16 v[64:67], v[44:47], a[216:223], v0\n" \
  "v_smfmac_f32_16x16x64_bf16 v[68:71], v[44:47], v[152:159], v0\n" \
  "v_smfmac_f32_16x16x64_bf16 v[72:75], v[44:47], v[232:239], v0\n" \
  "v_smfmac_f32_16x16x64_bf16 v[60:63], v[48:51], a[144:151], v0\n" \
  "v_smfmac_f32_16x16x64_bf16 v[64:67], v[48:51], a[224:231], v0\n" \
  "v_smfmac_f32_16x16x64_bf16 v[68:71], v[48:51], v[160:167], v0\n" \
  "v_smfmac_f32_16x16x64_bf16 v[72:75], v[48:51], v[240:247], v0\n" \
  "v_smfmac_f32_16x16x64_bf16 v[60:63], v[52:55], a[152:159], v0\n" \
  "v_smfmac_f32_16x16x64_bf16 v[64:67], v[52:55], a[232:239], v0\n" \
  "v_smfmac_f32_16x16x64_bf16 v[68:71], v[52:55], v[168:175], v0\n" \
  "v_smfmac_f32_16x16x64_bf16 v[72:75], v[52:55], v[248:255], v0\n" \
  "v_permlane32_swap_b32 v56, v60\n" \
  "v_permlane32_swap_b32 v57, v61\n" \
  "v_permlane32_swap_b32 v58, v62\n" \
  "v_permlane32_swap_b32 v59, v63\n" \
  "v_pk_add_f32 v[56:57], v[56:57], v[60:61]\n" \
  "v_pk_add_f32 v[58:59], v[58:59], v[62:63]\n" \
  "v_cvt_pk_bf16_f32 v56, v56, v57\n" \
  "v_cvt_pk_bf16_f32 v57, v58, v59\n" \
  "ds_write_b64 v2, v[56:57] offset:2560\n" \
  "v_permlane32_swap_b32 v64, v68\n" \
  "v_permlane32_swap_b32 v65, v69\n" \
  "v_permlane32_swap_b32 v66, v70\n" \
  "v_permlane32_swap_b32 v67, v71\n" \
  "v_pk_add_f32 v[64:65], v[64:65], v[68:69]\n" \
  "v_pk_add_f32 v[66:67], v[66:67], v[70:71]\n" \
  "v_cvt_pk_bf16_f32 v64, v64, v65\n" \
  "v_cvt_pk_bf16_f32 v65, v66, v67\n" \
  "ds_write_b64 v2, v[64:65] offset:3072\n" \
  "v_permlane32_swap_b32 v72, v74\n" \
  "v_permlane32_swap_b32 v73, v75\n" \
  "v_pk_add_f32 v[72:73], v[72:73], v[74:75]\n" \
  "v_cvt_pk_bf16_f32 v72, v72, v73\n" \
  "ds_write_b32 v3, v72 offset:3584\n" \
  "s_waitcnt lgkmcnt(0)\n" \
  "s_barrier\n" \
  "s_mov_b32 s44, s45\n" \
  "s_barrier\n" \
  "s_cmp_eq_u32 s68, 64\n" \
  "s_cbranch_scc0 LOOPc2_%=\n" \
  "s_branch LDONE_%=\n" \
  "LMFc2_%=:\n" \
  "v_max_u32 v92, v16, v17\n" \
  "v_max3_u32 v92, v92, v18, v19\n" \
  "v_max3_u32 v92, v92, v20, v21\n" \
  "v_max3_u32 v92, v92, v22, v23\n" \
  "v_max3_u32 v92, v92, v24, v25\n" \
  "v_max3_u32 v92, v92, v26, v27\n" \
  "v_max3_u32 v92, v92, v28, v29\n" \
  "v_max3_u32 v92, v92, v30, v31\n" \
  "v_max3_u32 v92, v92, v32, v33\n" \
  "v_max3_u32 v92, v92, v34, v35\n" \
  "v_max3_u32 v92, v92, v36, v37\n" \
  "v_max3_u32 v92, v92, v38, v39\n" \
  "v_max3_u32 v92, v92, v40, v41\n" \
  "v_max3_u32 v92, v92, v42, v43\n" \
  "v_max3_u32 v92, v92, v44, v45\n" \
  "v_max3_u32 v92, v92, v46, v47\n" \
  "v_max3_u32 v92, v92, v48, v49\n" \
  "v_max3_u32 v92, v92, v50, v51\n" \
  "v_max3_u32 v92, v92, v52, v53\n" \
  "v_max3_u32 v92, v92, v54, v55\n" \
  "v_cmp_eq_u32 vcc, -1, v92\n" \
  "s_nop 3\n" \
  "s_cbranch_vccz LMOKc2_%=\n" \
  "s_cmp_gt_u32 s57, 20000\n" \
  "s_cbranch_scc1 LMOKc2_%=\n" \
  "s_add_u32 s57, s57, 1\n" \
  "s_barrier\n" \
  "buffer_load_dwordx4 v1, s[36:39], s75 offen offset:0 sc1 lds\n" \
  "buffer_load_dwordx4 v1, s[36:39], s75 offen offset:1024 sc1 lds\n" \
  "v_mov_b32 v56, 0\n" \
  "v_mov_b32 v57, 0\n" \
  "v_mov_b32 v58, 0\n" \
  "v_mov_b32 v59, 0\n" \
  "v_mov_b32 v60, 0\n" \
  "v_mov_b32 v61, 0\n" \
  "v_mov_b32 v62, 0\n" \
  "v_mov_b32 v63, 0\n" \
  "v_mov_b32 v64, 0\n" \
  "v_mov_b32 v65, 0\n" \
  "v_mov_b32 v66, 0\n" \
  "v_mov_b32 v67, 0\n" \
  "v_mov_b32 v68, 0\n" \
  "v_mov_b32 v69, 0\n" \
  "v_mov_b32 v70, 0\n" \
  "v_mov_b32 v71, 0\n" \
  "v_mov_b32 v72, 0\n" \
  "v_mov_b32 v73, 0\n" \
  "v_mov_b32 v74, 0\n" \
  "v_mov_b32 v75, 0\n" \
  "s_waitcnt vmcnt(0)\n" \
  "s_barrier\n" \
  "s_branch LMc2_%=\n" \
  "LSTARTc3_%=:\n" \
  "v_mov_b32 v96, v16\n" \
  "v_mov_b32 v97, v17\n" \
  "v_mov_b32 v100, v18\n" \
  "s_memrealtime s[76:77]\n" \
  "s_memtime s[78:79]\n" \
  "s_waitcnt lgkmcnt(0)\n" \
  "s_mov_b32 s88, s76\n" \
  "s_mov_b32 s89, s78\n" \
  "LQUc3_%=:\n" \
  "buffer_load_dword v92, v14, s[36:39], 0 offen sc1\n" \
  "s_waitcnt vmcnt(0)\n" \
  "v_cmp_gt_u32 vcc, v15, v92\n" \
  "s_nop 3\n" \
  "s_cbranch_vccz LRUc3_%=\n" \
  "s_add_u32 s73, s73, 1\n" \
  "s_cmp_gt_u32 s73, 20000\n" \
  "s_cbranch_scc1 LRUc3_%=\n" \
  "s_sleep 8\n" \
  "s_branch LQUc3_%=\n" \
  "LRUc3_%=:\n" \
  "s_barrier\n" \
  "v_mov_b32 v15, 1\n" \
  "s_mov_b32 s56, s38\n" \
  "s_mov_b32 s38, 0\n" \
  "s_mov_b32 s73, 20001\n" \
  "s_mov_b32 s57, 20001\n" \
  "s_getpc_b64 s[82:83]\n" \
  "LPCEc3_%=:\n" \
  "s_add_u32 s84, s82, LEEc3_%=-LPCEc3_%=+24\n" \
  "s_addc_u32 s85, s83, 0\n" \
  "s_getpc_b64 s[82:83]\n" \
  "LPCLc3_%=:\n" \
  "s_add_u32 s90, s82, LELc3_%=-LPCLc3_%=+24\n" \
  "s_addc_u32 s91, s83, 0\n" \
  "v_mov_b32 v92, 0\n" \
  "s_memrealtime s[76:77]\n" \
  "s_memtime s[78:79]\n" \
  "s_waitcnt lgkmcnt(0)\n" \
  "s_lshl_b32 s48, s76, 2\n" \
  "v_readlane_b32 s72, v92, 0\n" \
  "s_nop 1\n" \
  "s_sub_u32 s74, s72, s48\n" \
  "s_add_u32 s74, s74, 40000\n" \
  "s_cmp_gt_u32 s74, 80000\n" \
  "s_cselect_b32 s72, s48, s72\n" \
  "v_readlane_b32 s94, v92, 1\n" \
  "s_nop 1\n" \
  "s_sub_u32 s74, s94, s48\n" \
  "s_add_u32 s74, s74, 40000\n" \
  "s_cmp_gt_u32 s74, 80000\n" \
  "s_cselect_b32 s94, s48, s94\n" \
  "v_readlane_b32 s95, v92, 2\n" \
  "s_nop 1\n" \
  "s_sub_u32 s74, s95, s48\n" \
  "s_add_u32 s74, s74, 40000\n" \
  "s_cmp_gt_u32 s74, 80000\n" \
  "s_cselect_b32 s95, s48, s95\n" \
  "s_sub_u32 s74, s78, s89\n" \
  "s_sub_u32 s75, s76, s88\n" \
  "v_cvt_f32_u32 v92, s74\n" \
  "v_cvt_f32_u32 v93, s75\n" \
  "v_rcp_f32 v93, v93\n" \
  "v_mul_f32 v92, 0x43820000, v92\n" \
  "v_mul_f32 v92, v92, v93\n" \
  "v_cvt_u32_f32 v92, v92\n" \
  "s_nop 0\n" \
  "v_readfirstlane_b32 s74, v92\n" \
  "s_min_u32 s74, s74, 6400\n" \
  "s_cmp_gt_u32 s75, 150\n" \
  "s_cselect_b32 s50, s74, s50\n" \
  "s_cselect_b32 s88, s76, s88\n" \
  "s_cselect_b32 s89, s78, s89\n" \
  "s_add_u32 s49, s78, 0x1000000\n" \
  "s_mul_i32 s86, s54, 6\n" \
  "s_mov_b32 s87, -200\n" \
  "s_mov_b32 s51, 0\n" \
  "s_mul_i32 s74, s51, s54\n" \
  "s_add_u32 s74, s74, s72\n" \
  "s_sub_i32 s75, s74, s48\n" \
  "s_mov_b32 s48, s74\n" \
  "s_mul_i32 s75, s75, s50\n" \
  "s_ashr_i32 s75, s75, 10\n" \
  "s_add_u32 s49, s49, s75\n" \
  "s_mov_b32 s87, 0x80000000\n" \
  "s_branch LOOPc3_%=\n" \
  "LPASSc3_%=:\n" \
  "s_waitcnt vmcnt(0)\n" \
  "s_mov_b32 s38, s56\n" \
  "s_mov_b32 s68, 0\n" \
  "s_mov_b32 s73, 0\n" \
  "s_mov_b32 s58, 0\n" \
  "s_mov_b32 s59, 0\n" \
  "v_readlane_b32 s46, v6, 0\n" \
  "s_nop 3\n" \
  "v_readlane_b32 s47, v6, 1\n" \
  "buffer_load_dwordx4 a[0:3], v100, s[36:39], s46 offen sc1\n" \
  "s_nop 3\n" \
  "v_readlane_b32 s46, v6, 2\n" \
  "buffer_load_dwordx4 a[4:7], v100, s[36:39], s47 offen sc1\n" \
  "s_nop 3\n" \
  "v_readlane_b32 s47, v6, 3\n" \
  "buffer_load_dwordx4 a[8:11], v100, s[36:39], s46 offen sc1\n" \
  "s_nop 3\n" \
  "v_readlane_b32 s46, v6, 4\n" \
  "buffer_load_dwordx4 a[12:15], v100, s[36:39], s47 offen sc1\n" \
  "s_nop 3\n" \
  "v_readlane_b32 s47, v6, 5\n" \
  "buffer_load_dwordx4 a[16:19], v100, s[36:39], s46 offen sc1\n" \
  "s_nop 3\n" \
  "v_readlane_b32 s46, v6, 6\n" \
  "buffer_load_dwordx4 a[20:23], v100, s[36:39], s47 offen sc1\n" \
  "s_nop 3\n" \
  "v_readlane_b32 s47, v6, 7\n" \
  "buffer_load_dwordx4 a[24:27], v100, s[36:39], s46 offen sc1\n" \
  "s_nop 3\n" \
  "v_readlane_b32 s46, v6, 8\n" \
  "buffer_load_dwordx4 a[28:31], v100, s[36:39], s47 offen sc1\n" \
  "s_nop 3\n" \
  "v_readlane_b32 s47, v6, 9\n" \
  "buffer_load_dwordx4 a[32:35], v100, s[36:39], s46 offen sc1\n" \
  "s_nop 3\n" \
  "v_readlane_b32 s46, v6, 10\n" \
  "buffer_load_dwordx4 a[36:39], v100, s[36:39], s47 offen sc1\n" \
  "s_nop 3\n" \
  "v_readlane_b32 s47, v6, 11\n" \
  "buffer_load_dwordx4 a[40:43], v100, s[36:39], s46 offen sc1\n" \
  "s_nop 3\n" \
  "v_readlane_b32 s46, v6, 12\n" \
  "buffer_load_dwordx4 a[44:47], v100, s[36:39], s47 offen sc1\n" \
  "s_nop 3\n" \
  "v_readlane_b32 s47, v6, 13\n" \
  "buffer_load_dwordx4 a[48:51], v100, s[36:39], s46 offen sc1\n" \
  "s_nop 3\n" \
  "v_readlane_b32 s46, v6, 14\n" \
  "buffer_load_dwordx4 a[52:55], v100, s[36:39], s47 offen sc1\n" \
  "s_nop 3\n" \
  "v_readlane_b32 s47, v6, 15\n" \
  "buffer_load_dwordx4 a[56:59], v100, s[36:39], s46 offen sc1\n" \
  "s_nop 3\n" \
  "v_readlane_b32 s46, v6, 16\n" \
  "buffer_load_dwordx4 a[60:63], v100, s[36:39], s47 offen sc1\n" \
  "s_nop 3\n" \
  "v_readlane_b32 s47, v6, 17\n" \
  "buffer_load_dwordx4 a[64:67], v100, s[36:39], s46 offen sc1\n" \
  "s_nop 3\n" \
  "v_readlane_b32 s46, v6, 18\n" \
  "buffer_load_dwordx4 a[68:71], v100, s[36:39], s47 offen sc1\n" \
  "s_nop 3\n" \
  "v_readlane_b32 s47, v6, 19\n" \
  "buffer_load_dwordx4 a[72:75], v100, s[36:39], s46 offen sc1\n" \
  "s_nop 3\n" \
  "buffer_load_dwordx4 a[76:79], v100, s[36:39], s47 offen sc1\n" \
  "s_nop 3\n" \
  "LQAc3_%=:\n" \
  "buffer_load_dword v92, v8, s[36:39], 0 offen sc1\n" \
  "s_waitcnt vmcnt(0)\n" \
  "v_cmp_eq_u32 vcc, 0, v92\n" \
  "s_nop 3\n" \
  "s_cbranch_vccz LRAc3_%=\n" \
  "s_add_u32 s73, s73, 1\n" \
  "s_cmp_gt_u32 s73, 20000\n" \
  "s_cbranch_scc1 LRAc3_%=\n" \
  "s_sleep 2\n" \
  "s_branch LQAc3_%=\n" \
  "LRAc3_%=:\n" \
  "s_memrealtime s[76:77]\n" \
  "s_memtime s[78:79]\n" \
  "s_waitcnt lgkmcnt(0)\n" \
  "s_lshl_b32 s48, s76, 2\n" \
  "v_readlane_b32 s72, v92, 0\n" \
  "s_nop 1\n" \
  "s_sub_u32 s74, s72, s48\n" \
  "s_add_u32 s74, s74, 40000\n" \
  "s_cmp_gt_u32 s74, 80000\n" \
  "s_cselect_b32 s72, s48, s72\n" \
  "v_readlane_b32 s94, v92, 1\n" \
  "s_nop 1\n" \
  "s_sub_u32 s74, s94, s48\n" \
  "s_add_u32 s74, s74, 40000\n" \
  "s_cmp_gt_u32 s74, 80000\n" \
  "s_cselect_b32 s94, s48, s94\n" \
  "v_readlane_b32 s95, v92, 2\n" \
  "s_nop 1\n" \
  "s_sub_u32 s74, s95, s48\n" \
  "s_add_u32 s74, s74, 40000\n" \
  "s_cmp_gt_u32 s74, 80000\n" \
  "s_cselect_b32 s95, s48, s95\n" \
  "s_sub_u32 s74, s78, s89\n" \
  "s_sub_u32 s75, s76, s88\n" \
  "v_cvt_f32_u32 v92, s74\n" \
  "v_cvt_f32_u32 v93, s75\n" \
  "v_rcp_f32 v93, v93\n" \
  "v_mul_f32 v92, 0x43820000, v92\n" \
  "v_mul_f32 v92, v92, v93\n" \
  "v_cvt_u32_f32 v92, v92\n" \
  "s_nop 0\n" \
  "v_readfirstlane_b32 s74, v92\n" \
  "s_min_u32 s74, s74, 6400\n" \
  "s_cmp_gt_u32 s75, 150\n" \
  "s_cselect_b32 s50, s74, s50\n" \
  "s_cselect_b32 s88, s76, s88\n" \
  "s_cselect_b32 s89, s78, s89\n" \
  "s_add_u32 s49, s78, 0x1000000\n" \
  "s_mul_i32 s86, s54, 6\n" \
  "s_mov_b32 s87, -200\n" \
  "s_mov_b32 s51, 0\n" \
  "s_mov_b32 s57, 0\n" \
  "v_readlane_b32 s44, v4, 0\n" \
  "s_nop 3\n" \
  "LOOPc3_%=:\n" \
  "LZWc3_%=:\n" \
  "s_memrealtime s[76:77]\n" \
  "s_memtime s[78:79]\n" \
  "s_waitcnt lgkmcnt(0)\n" \
  "s_lshl_b32 s75, s76, 2\n" \
  "s_sub_i32 s75, s48, s75\n" \
  "s_mul_i32 s75, s75, s50\n" \
  "s_ashr_i32 s75, s75, 10\n" \
  "s_sub_i32 s74, s49, s78\n" \
  "s_min_i32 s74, s74, s75\n" \
  "s_sub_i32 s75, s75, 92\n" \
  "s_max_i32 s74, s74, s75\n" \
  "s_add_u32 s49, s74, s78\n" \
  "s_cmp_gt_i32 s74, 60\n" \
  "s_cbranch_scc1 LZWc3_%=\n" \
  "s_barrier\n" \
  "v_mov_b32 v56, 0\n" \
  "v_mov_b32 v57, 0\n" \
  "v_mov_b32 v58, 0\n" \
  "v_mov_b32 v59, 0\n" \
  "v_readlane_b32 s74, v9, s68\n" \
  "s_nop 0\n" \
  "s_add_u32 s46, s74, 0\n" \
  "s_add_u32 s47, s74, 4096\n" \
  "s_add_u32 s69, s74, 16384\n" \
  "s_add_u32 s70, s74, 20480\n" \
  "s_add_u32 s68, s68, 1\n" \
  "s_min_u32 s74, s68, 63\n" \
  "v_readlane_b32 s45, v4, s74\n" \
  "s_nop 15\n" \
  "buffer_load_dwordx4 a[80:83], v5, s[36:39], s46 offen offset:0 sc1\n" \
  "buffer_load_dwordx4 a[84:87], v5, s[36:39], s46 offen offset:1024 sc1\n" \
  "buffer_load_dwordx4 a[88:91], v5, s[36:39], s46 offen offset:2048 sc1\n" \
  "buffer_load_dwordx4 a[92:95], v5, s[36:39], s46 offen offset:3072 sc1\n" \
  "buffer_load_dwordx4 a[96:99], v5, s[36:39], s47 offen offset:0 sc1\n" \
  "buffer_load_dwordx4 a[100:103], v5, s[36:39], s47 offen offset:1024 sc1\n" \
  "buffer_load_dwordx4 a[104:107], v5, s[36:39], s47 offen offset:2048 sc1\n" \
  "buffer_load_dwordx4 a[108:111], v5, s[36:39], s47 offen offset:3072 sc1\n" \
  "buffer_load_dwordx4 a[112:115], v5, s[36:39], s69 offen offset:0 sc1\n" \
  "buffer_load_dwordx4 a[116:119], v5, s[36:39], s69 offen offset:1024 sc1\n" \
  "buffer_load_dwordx4 a[120:123], v5, s[36:39], s69 offen offset:2048 sc1\n" \
  "buffer_load_dwordx4 a[124:127], v5, s[36:39], s69 offen offset:3072 sc1\n" \
  "buffer_load_dwordx4 a[128:131], v5, s[36:39], s70 offen offset:0 sc1\n" \
  "buffer_load_dwordx4 a[132:135], v5, s[36:39], s70 offen offset:1024 sc1\n" \
  "buffer_load_dwordx4 a[136:139], v5, s[36:39], s70 offen offset:2048 sc1\n" \
  "buffer_load_dwordx4 a[140:143], v5, s[36:39], s70 offen offset:3072 sc1\n" \
  "buffer_load_dword v98, v97, s[36:39], s44 offen sc1\n" \
  "s_barrier\n" \
  "LMc3_%=:\n" \
  "ds_read_b128 v[16:19], v1 offset:0\n" \
  "ds_read_b128 v[20:23], v1 offset:1024\n" \
  "ds_read_b128 v[24:27], v1 offset:2048\n" \
  "ds_read_b128 v[28:31], v1 offset:3072\n" \
  "ds_read_b128 v[32:35], v1 offset:4096\n" \
  "ds_read_b128 v[36:39], v1 offset:5120\n" \
  "ds_read_b128 v[40:43], v1 offset:6144\n" \
  "ds_read_b128 v[44:47], v1 offset:7168\n" \
  "ds_read_b128 v[48:51], v1 offset:8192\n" \
  "ds_read_b128 v[52:55], v1 offset:9216\n" \
  "s_waitcnt lgkmcnt(9)\n" \
  "v_smfmac_f32_16x16x64_bf16 v[56:59], v[16:19], a[0:7], v0\n" \
  "s_waitcnt lgkmcnt(8)\n" \
  "v_smfmac_f32_16x16x64_bf16 v[56:59], v[20:23], a[8:15], v0\n" \
  "s_waitcnt lgkmcnt(7)\n" \
  "v_smfmac_f32_16x16x64_bf16 v[56:59], v[24:27], a[16:23], v0\n" \
  "s_waitcnt lgkmcnt(6)\n" \
  "v_smfmac_f32_16x16x64_bf16 v[56:59], v[28:31], a[24:31], v0\n" \
  "s_waitcnt lgkmcnt(5)\n" \
  "v_smfmac_f32_16x16x64_bf16 v[56:59], v[32:35], a[32:39], v0\n" \
  "s_waitcnt lgkmcnt(4)\n" \
  "v_smfmac_f32_16x16x64_bf16 v[56:59], v[36:39], a[40:47], v0\n" \
  "s_waitcnt lgkmcnt(3)\n" \
  "v_smfmac_f32_16x16x64_bf16 v[56:59], v[40:43], a[48:55], v0\n" \
  "s_waitcnt lgkmcnt(2)\n" \
  "v_smfmac_f32_16x16x64_bf16 v[56:59], v[44:47], a[56:63], v0\n" \
  "s_waitcnt lgkmcnt(1)\n" \
  "v_smfmac_f32_16x16x64_bf16 v[56:59], v[48:51], a[64:71], v0\n" \
  "s_waitcnt lgkmcnt(0)\n" \
  "v_smfmac_f32_16x16x64_bf16 v[56:59], v[52:55], a[72:79], v0\n" \
  "s_nop 7\n" \
  "v_cmp_u_f32 vcc, v56, v57\n" \
  "v_cmp_u_f32 s[82:83], v58, v59\n" \
  "s_nop 1\n" \
  "s_or_b64 vcc, vcc, s[82:83]\n" \
  "s_cbranch_scc1 LMFc3_%=\n" \
  "LMOKc3_%=:\n" \
  "v_permlane32_swap_b32 v56, v58\n" \
  "v_permlane32_swap_b32 v57, v59\n" \
  "v_pk_add_f32 v[56:57], v[56:57], v[58:59]\n" \
  "v_cvt_pk_bf16_f32 v56, v56, v57\n" \
  "ds_write_b32 v3, v56 offset:3840\n" \
  "s_waitcnt lgkmcnt(0)\n" \
  "s_cmp_eq_u32 s68, 64\n" \
  "s_cbranch_scc1 LENDc3_%=\n" \
  "s_add_u32 s46, s44, 32768\n" \
  "s_add_u32 s47, s44, 36864\n" \
  "s_add_u32 s69, s44, 49152\n" \
  "s_add_u32 s70, s44, 53248\n" \
  "s_sub_u32 s43, s45, s93\n" \
  "s_mul_i32 s74, s51, s54\n" \
  "s_add_u32 s74, s74, s94\n" \
  "s_sub_i32 s75, s74, s48\n" \
  "s_mov_b32 s48, s74\n" \
  "s_mul_i32 s75, s75, s50\n" \
  "s_ashr_i32 s75, s75, 10\n" \
  "s_add_u32 s49, s49, s75\n" \
  "s_memrealtime s[76:77]\n" \
  "s_memtime s[78:79]\n" \
  "s_waitcnt lgkmcnt(0)\n" \
  "s_lshl_b32 s75, s76, 2\n" \
  "s_sub_i32 s75, s48, s75\n" \
  "s_mul_i32 s75, s75, s50\n" \
  "s_ashr_i32 s75, s75, 10\n" \
  "s_sub_i32 s74, s49, s78\n" \
  "s_min_i32 s74, s74, s75\n" \
  "s_sub_i32 s75, s75, 92\n" \
  "s_max_i32 s74, s74, s75\n" \
  "s_add_u32 s49, s74, s78\n" \
  "s_sub_u32 s74, s78, s89\n" \
  "s_sub_u32 s75, s76, s88\n" \
  "v_cvt_f32_u32 v92, s74\n" \
  "v_cvt_f32_u32 v93, s75\n" \
  "v_rcp_f32 v93, v93\n" \
  "v_mul_f32 v92, 0x43820000, v92\n" \
  "v_mul_f32 v92, v92, v93\n" \
  "v_cvt_u32_f32 v92, v92\n" \
  "s_nop 0\n" \
  "v_readfirstlane_b32 s74, v92\n" \
  "s_min_u32 s74, s74, 6400\n" \
  "s_cmp_gt_u32 s75, 900\n" \
  "s_cselect_b32 s50, s74, s50\n" \
  "s_cselect_b32 s88, s76, s88\n" \
  "s_cselect_b32 s89, s78, s89\n" \
  "LWEc3_%=:\n" \
  "s_memrealtime s[76:77]\n" \
  "s_memtime s[78:79]\n" \
  "s_waitcnt lgkmcnt(0)\n" \
  "s_lshl_b32 s75, s76, 2\n" \
  "s_sub_i32 s75, s48, s75\n" \
  "s_mul_i32 s75, s75, s50\n" \
  "s_ashr_i32 s75, s75, 10\n" \
  "s_sub_i32 s74, s49, s78\n" \
  "s_min_i32 s74, s74, s75\n" \
  "s_sub_i32 s75, s75, 92\n" \
  "s_max_i32 s74, s74, s75\n" \
  "s_add_u32 s49, s74, s78\n" \
  "s_cmp_gt_i32 s74, 168\n" \
  "s_cbranch_scc1 LWEc3_%=\n" \
  "s_cmp_lt_i32 s74, s87\n" \
  "s_cbranch_scc1 LSEc3_%=\n" \
  "s_max_i32 s74, s74, 48\n" \
  "s_lshr_b32 s74, s74, 3\n" \
  "s_lshl_b32 s74, s74, 2\n" \
  "s_sub_u32 s80, s84, s74\n" \
  "s_subb_u32 s81, s85, 0\n" \
  "s_setpc_b64 s[80:81]\n" \
  "s_nop 1\n" \
  "s_nop 1\n" \
  "s_nop 1\n" \
  "s_nop 1\n" \
  "s_nop 1\n" \
  "s_nop 1\n" \
  "s_nop 1\n" \
  "s_nop 1\n" \
  "s_nop 1\n" \
  "s_nop 1\n" \
  "s_nop 1\n" \
  "s_nop 1\n" \
  "s_nop 1\n" \
  "s_nop 1\n" \
  "s_nop 1\n" \
  "s_nop 1\n" \
  "LEEc3_%=:\n" \
  "buffer_load_dwordx4 v[112:115], v10, s[36:39], s46 offen offset:0 sc1\n" \
  "buffer_load_dwordx4 v[120:123], v10, s[36:39], s46 offen offset:2048 sc1\n" \
  "buffer_load_dwordx4 v[116:119], v11, s[36:39], s46 offen offset:1024 sc1\n" \
  "buffer_load_dwordx4 v[124:127], v11, s[36:39], s46 offen offset:3072 sc1\n" \
  "buffer_load_dwordx4 v[132:135], v11, s[36:39], s47 offen offset:1024 sc1\n" \
  "buffer_load_dwordx4 v[140:143], v11, s[36:39], s47 offen offset:3072 sc1\n" \
  "buffer_load_dwordx4 v[128:131], v10, s[36:39], s47 offen offset:0 sc1\n" \
  "buffer_load_dwordx4 v[136:139], v10, s[36:39], s47 offen offset:2048 sc1\n" \
  "buffer_load_dwordx4 v[144:147], v10, s[36:39], s69 offen offset:0 sc1\n" \
  "buffer_load_dwordx4 v[152:155], v10, s[36:39], s69 offen offset:2048 sc1\n" \
  "buffer_load_dwordx4 v[148:151], v11, s[36:39], s69 offen offset:1024 sc1\n" \
  "buffer_load_dwordx4 v[156:159], v11, s[36:39], s69 offen offset:3072 sc1\n" \
  "buffer_load_dwordx4 v[164:167], v11, s[36:39], s70 offen offset:1024 sc1\n" \
  "buffer_load_dwordx4 v[172:175], v11, s[36:39], s70 offen offset:3072 sc1\n" \
  "buffer_load_dwordx4 v[160:163], v10, s[36:39], s70 offen offset:0 sc1\n" \
  "buffer_load_dwordx4 v[168:171], v10, s[36:39], s70 offen offset:2048 sc1\n" \
  "s_add_u32 s46, s44, 65536\n" \
  "s_add_u32 s47, s44, 69632\n" \
  "s_mul_i32 s74, s51, s54\n" \
  "s_add_u32 s74, s74, s95\n" \
  "s_sub_i32 s75, s74, s48\n" \
  "s_mov_b32 s48, s74\n" \
  "s_mul_i32 s75, s75, s50\n" \
  "s_ashr_i32 s75, s75, 10\n" \
  "s_add_u32 s49, s49, s75\n" \
  "LULc3_%=:\n" \
  "s_memtime s[78:79]\n" \
  "s_waitcnt lgkmcnt(0)\n" \
  "s_sub_i32 s74, s49, s78\n" \
  "s_cmp_gt_i32 s74, 168\n" \
  "s_cbranch_scc1 LULc3_%=\n" \
  "s_max_i32 s74, s74, 48\n" \
  "s_lshr_b32 s74, s74, 3\n" \
  "s_lshl_b32 s74, s74, 2\n" \
  "s_sub_u32 s80, s90, s74\n" \
  "s_subb_u32 s81, s91, 0\n" \
  "s_setpc_b64 s[80:81]\n" \
  "s_nop 1\n" \
  "s_nop 1\n" \
  "s_nop 1\n" \
  "s_nop 1\n" \
  "s_nop 1\n" \
  "s_nop 1\n" \
  "s_nop 1\n" \
  "s_nop 1\n" \
  "s_nop 1\n" \
  "s_nop 1\n" \
  "s_nop 1\n" \
  "s_nop 1\n" \
  "s_nop 1\n" \
  "s_nop 1\n" \
  "s_nop 1\n" \
  "s_nop 1\n" \
  "LELc3_%=:\n" \
  "buffer_load_dwordx4 v[16:19], v1, s[36:39], s46 offen offset:0 sc1\n" \
  "buffer_load_dwordx4 v[20:23], v1, s[36:39], s46 offen offset:1024 sc1\n" \
  "buffer_load_dwordx4 v[24:27], v1, s[36:39], s46 offen offset:2048 sc1\n" \
  "buffer_load_dwordx4 v[28:31], v1, s[36:39], s46 offen offset:3072 sc1\n" \
  "buffer_load_dwordx4 v[32:35], v1, s[36:39], s47 offen offset:0 sc1\n" \
  "buffer_load_dwordx4 v[36:39], v1, s[36:39], s47 offen offset:1024 sc1\n" \
  "buffer_load_dwordx4 v[40:43], v1, s[36:39], s47 offen offset:2048 sc1\n" \
  "buffer_load_dwordx4 v[44:47], v1, s[36:39], s47 offen offset:3072 sc1\n" \
  "s_cmp_lg_u32 s71, 0\n" \
  "s_cbranch_scc1 LNBc3_%=\n" \
  "s_barrier\n" \
  "LNBc3_%=:\n" \
  "s_mov_b32 s71, 0\n" \
  "ds_read_b128 v[224:227], v1 offset:10240\n" \
  "ds_read_b128 v[228:231], v1 offset:11264\n" \
  "ds_read_b128 v[232:235], v1 offset:12288\n" \
  "ds_read_b128 v[236:239], v1 offset:13312\n" \
  "s_mov_b64 exec, s[40:41]\n" \
  "s_waitcnt vmcnt(23)\n" \
  "v_mfma_f32_16x16x32_bf16 v[176:179], v[112:115], a[80:83], 0\n" \
  "s_waitcnt vmcnt(22)\n" \
  "v_mfma_f32_16x16x32_bf16 v[184:187], v[120:123], a[88:91], 0\n" \
  "s_waitcnt vmcnt(20)\n" \
  "v_mfma_f32_16x16x32_bf16 v[180:183], v[116:119], a[84:87], 0\n" \
  "v_cvt_pk_bf16_f32 v208, v176, v177\n" \
  "v_cvt_pk_bf16_f32 v209, v178, v179\n" \
  "v_mfma_f32_16x16x32_bf16 v[188:191], v[124:127], a[92:95], 0\n" \
  "s_waitcnt vmcnt(18)\n" \
  "v_cvt_pk_bf16_f32 v210, v184, v185\n" \
  "v_cvt_pk_bf16_f32 v211, v186, v187\n" \
  "v_mfma_f32_16x16x32_bf16 v[196:199], v[132:135], a[100:103], 0\n" \
  "s_not_b64 exec, exec\n" \
  "v_cvt_pk_bf16_f32 v208, v180, v181\n" \
  "v_cvt_pk_bf16_f32 v209, v182, v183\n" \
  "v_mfma_f32_16x16x32_bf16 v[204:207], v[140:143], a[108:111], 0\n" \
  "s_waitcnt vmcnt(16)\n" \
  "v_cvt_pk_bf16_f32 v210, v188, v189\n" \
  "v_cvt_pk_bf16_f32 v211, v190, v191\n" \
  "v_mfma_f32_16x16x32_bf16 v[192:195], v[128:131], a[96:99], 0\n" \
  "v_cvt_pk_bf16_f32 v212, v196, v197\n" \
  "v_cvt_pk_bf16_f32 v213, v198, v199\n" \
  "v_mfma_f32_16x16x32_bf16 v[200:203], v[136:139], a[104:107], 0\n" \
  "s_waitcnt vmcnt(14)\n" \
  "v_cvt_pk_bf16_f32 v214, v204, v205\n" \
  "v_cvt_pk_bf16_f32 v215, v206, v207\n" \
  "v_mfma_f32_16x16x32_bf16 v[176:179], v[144:147], a[112:115], 0\n" \
  "s_not_b64 exec, exec\n" \
  "v_cvt_pk_bf16_f32 v212, v192, v193\n" \
  "v_cvt_pk_bf16_f32 v213, v194, v195\n" \
  "v_mfma_f32_16x16x32_bf16 v[184:187], v[152:155], a[120:123], 0\n" \
  "s_waitcnt vmcnt(12)\n" \
  "v_cvt_pk_bf16_f32 v214, v200, v201\n" \
  "v_cvt_pk_bf16_f32 v215, v202, v203\n" \
  "v_mfma_f32_16x16x32_bf16 v[180:183], v[148:151], a[116:119], 0\n" \
  "v_cvt_pk_bf16_f32 v216, v176, v177\n" \
  "v_cvt_pk_bf16_f32 v217, v178, v179\n" \
  "v_mfma_f32_16x16x32_bf16 v[188:191], v[156:159], a[124:127], 0\n" \
  "s_waitcnt vmcnt(10)\n" \
  "v_cvt_pk_bf16_f32 v218, v184, v185\n" \
  "v_cvt_pk_bf16_f32 v219, v186, v187\n" \
  "v_mfma_f32_16x16x32_bf16 v[196:199], v[164:167], a[132:135], 0\n" \
  "s_not_b64 exec, exec\n" \
  "v_cvt_pk_bf16_f32 v216, v180, v181\n" \
  "v_cvt_pk_bf16_f32 v217, v182, v183\n" \
  "v_mfma_f32_16x16x32_bf16 v[204:207], v[172:175], a[140:143], 0\n" \
  "s_waitcnt vmcnt(9)\n" \
  "v_cvt_pk_bf16_f32 v218, v188, v189\n" \
  "v_cvt_pk_bf16_f32 v219, v190, v191\n" \
  "v_mfma_f32_16x16x32_bf16 v[192:195], v[160:163], a[128:131], 0\n" \
  "s_waitcnt vmcnt(8)\n" \
  "v_cvt_pk_bf16_f32 v220, v196, v197\n" \
  "v_cvt_pk_bf16_f32 v221, v198, v199\n" \
  "v_mfma_f32_16x16x32_bf16 v[200:203], v[168:171], a[136:139], 0\n" \
  "v_cvt_pk_bf16_f32 v222, v204, v205\n" \
  "v_cvt_pk_bf16_f32 v223, v206, v207\n" \
  "s_waitcnt vmcnt(7)\n" \
  "s_waitcnt lgkmcnt(3)\n" \
  "v_mfma_f32_16x16x32_bf16 v[240:243], v[224:227], v[16:19], 0\n" \
  "s_not_b64 exec, exec\n" \
  "v_cvt_pk_bf16_f32 v220, v192, v193\n" \
  "v_cvt_pk_bf16_f32 v221, v194, v195\n" \
  "s_waitcnt vmcnt(6)\n" \
  "s_waitcnt lgkmcnt(2)\n" \
  "v_mfma_f32_16x16x32_bf16 v[240:243], v[228:231], v[20:23], v[240:243]\n" \
  "v_cvt_pk_bf16_f32 v222, v200, v201\n" \
  "v_cvt_pk_bf16_f32 v223, v202, v203\n" \
  "s_waitcnt vmcnt(5)\n" \
  "s_waitcnt lgkmcnt(1)\n" \
  "v_mfma_f32_16x16x32_bf16 v[240:243], v[232:235], v[24:27], v[240:243]\n" \
  "s_mov_b64 exec, -1\n" \
  "s_waitcnt vmcnt(4)\n" \
  "s_waitcnt lgkmcnt(0)\n" \
  "v_mfma_f32_16x16x32_bf16 v[240:243], v[236:239], v[28:31], v[240:243]\n" \
  "s_waitcnt vmcnt(3)\n" \
  "v_mfma_f32_16x16x32_bf16 v[240:243], v[208:211], v[32:35], v[240:243]\n" \
  "s_waitcnt vmcnt(2)\n" \
  "v_mfma_f32_16x16x32_bf16 v[240:243], v[212:215], v[36:39], v[240:243]\n" \
  "s_waitcnt vmcnt(1)\n" \
  "v_mfma_f32_16x16x32_bf16 v[240:243], v[216:219], v[40:43], v[240:243]\n" \
  "s_waitcnt vmcnt(0)\n" \
  "v_mfma_f32_16x16x32_bf16 v[240:243], v[220:223], v[44:47], v[240:243]\n" \
  "s_nop 7\n" \
  "v_permlane32_swap_b32 v240, v242\n" \
  "v_permlane32_swap_b32 v241, v243\n" \
  "s_nop 0\n" \
  "v_add_f32_dpp v240, v242, v240 row_ror:8 row_mask:0xf bank_mask:0xf\n" \
  "v_add_f32_dpp v241, v243, v241 row_ror:8 row_mask:0xf bank_mask:0xf\n" \
  "v_cmp_u_f32 vcc, v240, v241\n" \
  "v_cvt_pk_bf16_f32 v240, v240, v241\n" \
  "s_and_b64 vcc, vcc, s[34:35]\n" \
  "s_cbranch_scc1 LPFc3_%=\n" \
  "LPOKc3_%=:\n" \
  "v_mov_b32_dpp v240, v240 row_ror:8 row_mask:0xf bank_mask:0xc\n" \
  "buffer_store_dword v240, v13, s[36:39], s43 offen sc1\n" \
  "buffer_store_dword v240, v2, s[36:39], s43 offen sc1\n" \
  "v_readlane_b32 s74, v98, 3\n" \
  "s_nop 0\n" \
  "s_sub_u32 s75, s74, s51\n" \
  "s_cmp_lt_u32 s75, 16\n" \
  "s_cselect_b32 s51, s74, s51\n" \
  "v_readlane_b32 s74, v98, 0\n" \
  "s_add_u32 s72, s72, s53\n" \
  "s_sub_u32 s75, s74, s72\n" \
  "s_add_u32 s75, s75, 2000\n" \
  "s_cmp_lt_u32 s75, 4000\n" \
  "s_cselect_b32 s72, s74, s72\n" \
  "v_readlane_b32 s74, v98, 1\n" \
  "s_add_u32 s94, s94, s53\n" \
  "s_sub_u32 s75, s74, s94\n" \
  "s_add_u32 s75, s75, 2000\n" \
  "s_cmp_lt_u32 s75, 4000\n" \
  "s_cselect_b32 s94, s74, s94\n" \
  "v_readlane_b32 s74, v98, 2\n" \
  "s_add_u32 s95, s95, s53\n" \
  "s_sub_u32 s75, s74, s95\n" \
  "s_add_u32 s75, s75, 2000\n" \
  "s_cmp_lt_u32 s75, 4000\n" \
  "s_cselect_b32 s95, s74, s95\n" \
  "s_mul_i32 s74, s51, s54\n" \
  "s_add_u32 s74, s74, s72\n" \
  "s_add_u32 s74, s74, s55\n" \
  "s_sub_i32 s75, s74, s48\n" \
  "s_mov_b32 s48, s74\n" \
  "s_mul_i32 s75, s75, s50\n" \
  "s_ashr_i32 s75, s75, 10\n" \
  "s_add_u32 s49, s49, s75\n" \
  "s_barrier\n" \
  "s_mov_b32 s44, s45\n" \
  "s_cmp_lg_u32 s38, 0\n" \
  "s_cbranch_scc1 LOOPc3_%=\n" \
  "s_branch LPASSc3_%=\n" \
  "LSEc3_%=:\n" \
  "s_add_u32 s48, s48, s54\n" \
  "s_add_u32 s49, s49, s86\n" \
  "s_add_u32 s51, s51, 1\n" \
  "s_branch LWEc3_%=\n" \
  "LENDc3_%=:\n" \
  "s_barrier\n" \
  "s_barrier\n" \
  "s_branch LDONE_%=\n" \
  "LPFc3_%=:\n" \
  "s_add_u32 s46, s44, 32768\n" \
  "s_add_u32 s47, s44, 36864\n" \
  "s_add_u32 s69, s44, 49152\n" \
  "s_add_u32 s70, s44, 53248\n" \
  "v_max_u32 v92, v16, v17\n" \
  "v_max3_u32 v92, v92, v18, v19\n" \
  "v_max3_u32 v92, v92, v20, v21\n" \
  "v_max3_u32 v92, v92, v22, v23\n" \
  "v_max3_u32 v92, v92, v24, v25\n" \
  "v_max3_u32 v92, v92, v26, v27\n" \
  "v_max3_u32 v92, v92, v28, v29\n" \
  "v_max3_u32 v92, v92, v30, v31\n" \
  "v_max3_u32 v92, v92, v32, v33\n" \
  "v_max3_u32 v92, v92, v34, v35\n" \
  "v_max3_u32 v92, v92, v36, v37\n" \
  "v_max3_u32 v92, v92, v38, v39\n" \
  "v_max3_u32 v92, v92, v40, v41\n" \
  "v_max3_u32 v92, v92, v42, v43\n" \
  "v_max3_u32 v92, v92, v44, v45\n" \
  "v_max3_u32 v92, v92, v46, v47\n" \
  "v_cmp_eq_u32 vcc, -1, v92\n" \
  "s_nop 3\n" \
  "s_cbranch_vccnz LPRc3_%=\n" \
  "s_add_u32 s59, s59, 1\n" \
  "v_max_u32 v92, v112, v113\n" \
  "v_max3_u32 v92, v92, v114, v115\n" \
  "v_max3_u32 v92, v92, v116, v117\n" \
  "v_max3_u32 v92, v92, v118, v119\n" \
  "v_max3_u32 v92, v92, v120, v121\n" \
  "v_max3_u32 v92, v92, v122, v123\n" \
  "v_max3_u32 v92, v92, v124, v125\n" \
  "v_max3_u32 v92, v92, v126, v127\n" \
  "v_max3_u32 v92, v92, v128, v129\n" \
  "v_max3_u32 v92, v92, v130, v131\n" \
  "v_max3_u32 v92, v92, v132, v133\n" \
  "v_max3_u32 v92, v92, v134, v135\n" \
  "v_max3_u32 v92, v92, v136, v137\n" \
  "v_max3_u32 v92, v92, v138, v139\n" \
  "v_max3_u32 v92, v92, v140, v141\n" \
  "v_max3_u32 v92, v92, v142, v143\n" \
  "v_max3_u32 v92, v92, v144, v145\n" \
  "v_max3_u32 v92, v92, v146, v147\n" \
  "v_max3_u32 v92, v92, v148, v149\n" \
  "v_max3_u32 v92, v92, v150, v151\n" \
  "v_max3_u32 v92, v92, v152, v153\n" \
  "v_max3_u32 v92, v92, v154, v155\n" \
  "v_max3_u32 v92, v92, v156, v157\n" \
  "v_max3_u32 v92, v92, v158, v159\n" \
  "v_max3_u32 v92, v92, v160, v161\n" \
  "v_max3_u32 v92, v92, v162, v163\n" \
  "v_max3_u32 v92, v92, v164, v165\n" \
  "v_max3_u32 v92, v92, v166, v167\n" \
  "v_max3_u32 v92, v92, v168, v169\n" \
  "v_max3_u32 v92, v92, v170, v171\n" \
  "v_max3_u32 v92, v92, v172, v173\n" \
  "v_max3_u32 v92, v92, v174, v175\n" \
  "v_cmp_eq_u32 vcc, -1, v92\n" \
  "s_nop 3\n" \
  "s_cbranch_vccz LPEc3_%=\n" \
  "LPRc3_%=:\n" \
  "s_add_u32 s73, s73, 1\n" \
  "s_cmp_gt_u32 s73, 20000\n" \
  "s_cbranch_scc1 LPEc3_%=\n" \
  "s_add_u32 s58, s58, 1\n" \
  "s_mov_b32 s71, 1\n" \
  "s_mul_i32 s74, s51, s54\n" \
  "s_add_u32 s74, s74, s94\n" \
  "s_sub_i32 s75, s74, s48\n" \
  "s_mov_b32 s48, s74\n" \
  "s_mul_i32 s75, s75, s50\n" \
  "s_ashr_i32 s75, s75, 10\n" \
  "s_add_u32 s49, s49, s75\n" \
  "s_branch LSEc3_%=\n" \
  "LPEc3_%=:\n" \
  "buffer_load_dwordx4 v[112:115], v10, s[36:39], s46 offen offset:0 sc1\n" \
  "buffer_load_dwordx4 v[120:123], v10, s[36:39], s46 offen offset:2048 sc1\n" \
  "buffer_load_dwordx4 v[116:119], v11, s[36:39], s46 offen offset:1024 sc1\n" \
  "buffer_load_dwordx4 v[124:127], v11, s[36:39], s46 offen offset:3072 sc1\n" \
  "buffer_load_dwordx4 v[132:135], v11, s[36:39], s47 offen offset:1024 sc1\n" \
  "buffer_load_dwordx4 v[140:143], v11, s[36:39], s47 offen offset:3072 sc1\n" \
  "buffer_load_dwordx4 v[128:131], v10, s[36:39], s47 offen offset:0 sc1\n" \
  "buffer_load_dwordx4 v[136:139], v10, s[36:39], s47 offen offset:2048 sc1\n" \
  "buffer_load_dwordx4 v[144:147], v10, s[36:39], s69 offen offset:0 sc1\n" \
  "buffer_load_dwordx4 v[152:155], v10, s[36:39], s69 offen offset:2048 sc1\n" \
  "buffer_load_dwordx4 v[148:151], v11, s[36:39], s69 offen offset:1024 sc1\n" \
  "buffer_load_dwordx4 v[156:159], v11, s[36:39], s69 offen offset:3072 sc1\n" \
  "buffer_load_dwordx4 v[164:167], v11, s[36:39], s70 offen offset:1024 sc1\n" \
  "buffer_load_dwordx4 v[172:175], v11, s[36:39], s70 offen offset:3072 sc1\n" \
  "buffer_load_dwordx4 v[160:163], v10, s[36:39], s70 offen offset:0 sc1\n" \
  "buffer_load_dwordx4 v[168:171], v10, s[36:39], s70 offen offset:2048 sc1\n" \
  "s_waitcnt vmcnt(0)\n" \
  "LPGc3_%=:\n" \
  "v_cmp_eq_u32 vcc, 0, v12\n" \
  "s_nop 1\n" \
  "v_cndmask_b32 v32, 0, v32, vcc\n" \
  "s_nop 1\n" \
  "v_cndmask_b32 v36, 0, v36, vcc\n" \
  "s_nop 1\n" \
  "v_cndmask_b32 v40, 0, v40, vcc\n" \
  "s_nop 1\n" \
  "v_cndmask_b32 v44, 0, v44, vcc\n" \
  "v_cmp_eq_u32 vcc, 1, v12\n" \
  "s_nop 1\n" \
  "v_cndmask_b32 v33, 0, v33, vcc\n" \
  "s_nop 1\n" \
  "v_cndmask_b32 v37, 0, v37, vcc\n" \
  "s_nop 1\n" \
  "v_cndmask_b32 v41, 0, v41, vcc\n" \
  "s_nop 1\n" \
  "v_cndmask_b32 v45, 0, v45, vcc\n" \
  "v_cmp_eq_u32 vcc, 0, v12\n" \
  "s_nop 1\n" \
  "v_cndmask_b32 v34, 0, v34, vcc\n" \
  "s_nop 1\n" \
  "v_cndmask_b32 v38, 0, v38, vcc\n" \
  "s_nop 1\n" \
  "v_cndmask_b32 v42, 0, v42, vcc\n" \
  "s_nop 1\n" \
  "v_cndmask_b32 v46, 0, v46, vcc\n" \
  "v_cmp_eq_u32 vcc, 1, v12\n" \
  "s_nop 1\n" \
  "v_cndmask_b32 v35, 0, v35, vcc\n" \
  "s_nop 1\n" \
  "v_cndmask_b32 v39, 0, v39, vcc\n" \
  "s_nop 1\n" \
  "v_cndmask_b32 v43, 0, v43, vcc\n" \
  "s_nop 1\n" \
  "v_cndmask_b32 v47, 0, v47, vcc\n" \
  "v_mfma_f32_16x16x32_bf16 v[176:179], v[112:115], a[80:83], 0\n" \
  "v_mfma_f32_16x16x32_bf16 v[180:183], v[116:119], a[84:87], 0\n" \
  "v_mfma_f32_16x16x32_bf16 v[184:187], v[120:123], a[88:91], 0\n" \
  "v_mfma_f32_16x16x32_bf16 v[188:191], v[124:127], a[92:95], 0\n" \
  "v_mfma_f32_16x16x32_bf16 v[192:195], v[128:131], a[96:99], 0\n" \
  "v_mfma_f32_16x16x32_bf16 v[196:199], v[132:135], a[100:103], 0\n" \
  "v_mfma_f32_16x16x32_bf16 v[200:203], v[136:139], a[104:107], 0\n" \
  "v_mfma_f32_16x16x32_bf16 v[204:207], v[140:143], a[108:111], 0\n" \
  "s_mov_b64 exec, s[40:41]\n" \
  "v_cvt_pk_bf16_f32 v208, v176, v177\n" \
  "v_cvt_pk_bf16_f32 v209, v178, v179\n" \
  "v_cvt_pk_bf16_f32 v210, v184, v185\n" \
  "v_cvt_pk_bf16_f32 v211, v186, v187\n" \
  "v_cvt_pk_bf16_f32 v212, v192, v193\n" \
  "v_cvt_pk_bf16_f32 v213, v194, v195\n" \
  "v_cvt_pk_bf16_f32 v214, v200, v201\n" \
  "v_cvt_pk_bf16_f32 v215, v202, v203\n" \
  "s_not_b64 exec, exec\n" \
  "v_cvt_pk_bf16_f32 v208, v180, v181\n" \
  "v_cvt_pk_bf16_f32 v209, v182, v183\n" \
  "v_cvt_pk_bf16_f32 v210, v188, v189\n" \
  "v_cvt_pk_bf16_f32 v211, v190, v191\n" \
  "v_cvt_pk_bf16_f32 v212, v196, v197\n" \
  "v_cvt_pk_bf16_f32 v213, v198, v199\n" \
  "v_cvt_pk_bf16_f32 v214, v204, v205\n" \
  "v_cvt_pk_bf16_f32 v215, v206, v207\n" \
  "s_mov_b64 exec, -1\n" \
  "v_mfma_f32_16x16x32_bf16 v[176:179], v[144:147], a[112:115], 0\n" \
  "v_mfma_f32_16x16x32_bf16 v[180:183], v[148:151], a[116:119], 0\n" \
  "v_mfma_f32_16x16x32_bf16 v[184:187], v[152:155], a[120:123], 0\n" \
  "v_mfma_f32_16x16x32_bf16 v[188:191], v[156:159], a[124:127], 0\n" \
  "v_mfma_f32_16x16x32_bf16 v[192:195], v[160:163], a[128:131], 0\n" \
  "v_mfma_f32_16x16x32_bf16 v[196:199], v[164:167], a[132:135], 0\n" \
  "v_mfma_f32_16x16x32_bf16 v[200:203], v[168:171], a[136:139], 0\n" \
  "v_mfma_f32_16x16x32_bf16 v[204:207], v[172:175], a[140:143], 0\n" \
  "s_mov_b64 exec, s[40:41]\n" \
  "v_cvt_pk_bf16_f32 v216, v176, v177\n" \
  "v_cvt_pk_bf16_f32 v217, v178, v179\n" \
  "v_cvt_pk_bf16_f32 v218, v184, v185\n" \
  "v_cvt_pk_bf16_f32 v219, v186, v187\n" \
  "v_cvt_pk_bf16_f32 v220, v192, v193\n" \
  "v_cvt_pk_bf16_f32 v221, v194, v195\n" \
  "v_cvt_pk_bf16_f32 v222, v200, v201\n" \
  "v_cvt_pk_bf16_f32 v223, v202, v203\n" \
  "s_not_b64 exec, exec\n" \
  "v_cvt_pk_bf16_f32 v216, v180, v181\n" \
  "v_cvt_pk_bf16_f32 v217, v182, v183\n" \
  "v_cvt_pk_bf16_f32 v218, v188, v189\n" \
  "v_cvt_pk_bf16_f32 v219, v190, v191\n" \
  "v_cvt_pk_bf16_f32 v220, v196, v197\n" \
  "v_cvt_pk_bf16_f32 v221, v198, v199\n" \
  "v_cvt_pk_bf16_f32 v222, v204, v205\n" \
  "v_cvt_pk_bf16_f32 v223, v206, v207\n" \
  "s_mov_b64 exec, -1\n" \
  "v_mfma_f32_16x16x32_bf16 v[240:243], v[224:227], v[16:19], 0\n" \
  "v_mfma_f32_16x16x32_bf16 v[240:243], v[228:231], v[20:23], v[240:243]\n" \
  "v_mfma_f32_16x16x32_bf16 v[240:243], v[232:235], v[24:27], v[240:243]\n" \
  "v_mfma_f32_16x16x32_bf16 v[240:243], v[236:239], v[28:31], v[240:243]\n" \
  "v_mfma_f32_16x16x32_bf16 v[240:243], v[208:211], v[32:35], v[240:243]\n" \
  "v_mfma_f32_16x16x32_bf16 v[240:243], v[212:215], v[36:39], v[240:243]\n" \
  "v_mfma_f32_16x16x32_bf16 v[240:243], v[216:219], v[40:43], v[240:243]\n" \
  "v_mfma_f32_16x16x32_bf16 v[240:243], v[220:223], v[44:47], v[240:243]\n" \
  "s_nop 7\n" \
  "v_permlane32_swap_b32 v240, v242\n" \
  "v_permlane32_swap_b32 v241, v243\n" \
  "s_nop 0\n" \
  "v_add_f32_dpp v240, v242, v240 row_ror:8 row_mask:0xf bank_mask:0xf\n" \
  "v_add_f32_dpp v241, v243, v241 row_ror:8 row_mask:0xf bank_mask:0xf\n" \
  "v_cvt_pk_bf16_f32 v240, v240, v241\n" \
  "v_pk_min_u16 v240, v240, -2 op_sel_hi:[1,0]\n" \
  "s_nop 1\n" \
  "s_branch LPOKc3_%=\n" \
  "LMFc3_%=:\n" \
  "v_max_u32 v92, v16, v17\n" \
  "v_max3_u32 v92, v92, v18, v19\n" \
  "v_max3_u32 v92, v92, v20, v21\n" \
  "v_max3_u32 v92, v92, v22, v23\n" \
  "v_max3_u32 v92, v92, v24, v25\n" \
  "v_max3_u32 v92, v92, v26, v27\n" \
  "v_max3_u32 v92, v92, v28, v29\n" \
  "v_max3_u32 v92, v92, v30, v31\n" \
  "v_max3_u32 v92, v92, v32, v33\n" \
  "v_max3_u32 v92, v92, v34, v35\n" \
  "v_max3_u32 v92, v92, v36, v37\n" \
  "v_max3_u32 v92, v92, v38, v39\n" \
  "v_max3_u32 v92, v92, v40, v41\n" \
  "v_max3_u32 v92, v92, v42, v43\n" \
  "v_max3_u32 v92, v92, v44, v45\n" \
  "v_max3_u32 v92, v92, v46, v47\n" \
  "v_max3_u32 v92, v92, v48, v49\n" \
  "v_max3_u32 v92, v92, v50, v51\n" \
  "v_max3_u32 v92, v92, v52, v53\n" \
  "v_max3_u32 v92, v92, v54, v55\n" \
  "v_cmp_eq_u32 vcc, -1, v92\n" \
  "s_nop 3\n" \
  "s_cbranch_vccz LMOKc3_%=\n" \
  "s_cmp_gt_u32 s57, 20000\n" \
  "s_cbranch_scc1 LMOKc3_%=\n" \
  "s_add_u32 s57, s57, 1\n" \
  "LHMc3_%=:\n" \
  "s_add_u32 s48, s48, s54\n" \
  "s_add_u32 s49, s49, s86\n" \
  "s_add_u32 s51, s51, 1\n" \
  "LGMc3_%=:\n" \
  "s_memrealtime s[76:77]\n" \
  "s_memtime s[78:79]\n" \
  "s_waitcnt lgkmcnt(0)\n" \
  "s_lshl_b32 s75, s76, 2\n" \
  "s_sub_i32 s75, s48, s75\n" \
  "s_mul_i32 s75, s75, s50\n" \
  "s_ashr_i32 s75, s75, 10\n" \
  "s_sub_i32 s74, s49, s78\n" \
  "s_min_i32 s74, s74, s75\n" \
  "s_sub_i32 s75, s75, 92\n" \
  "s_max_i32 s74, s74, s75\n" \
  "s_add_u32 s49, s74, s78\n" \
  "s_cmp_gt_i32 s74, 60\n" \
  "s_cbranch_scc1 LGMc3_%=\n" \
  "s_cmp_lt_i32 s74, s87\n" \
  "s_cbranch_scc1 LHMc3_%=\n" \
  "s_barrier\n" \
  "v_mov_b32 v56, 0\n" \
  "v_mov_b32 v57, 0\n" \
  "v_mov_b32 v58, 0\n" \
  "v_mov_b32 v59, 0\n" \
  "s_waitcnt vmcnt(0)\n" \
  "s_barrier\n" \
  "s_branch LMc3_%=\n" \
  "LDONE_%=:\n" \
  "s_waitcnt vmcnt(0) lgkmcnt(0)\n" \
  "s_mov_b32 s53, s73\n" \
  "s_mov_b32 s54, s51\n" \
  "s_mov_b32 s55, s50\n"
#define PROJ_CLOBBERS \
  "v12", "v13", "v14", "v15", "v16", "v17", "v18", "v19", "v20", "v21", "v22", "v23", "v24", "v25", "v26", "v27", "v28", "v29", "v30", "v31", "v32", \
  "v33", "v34", "v35", "v36", "v37", "v38", "v39", "v40", "v41", "v42", "v43", "v44", "v45", "v46", "v47", "v48", "v49", "v50", "v51", "v52", "v53", \
  "v54", "v55", "v56", "v57", "v58", "v59", "v60", "v61", "v62", "v63", "v64", "v65", "v66", "v67", "v68", "v69", "v70", "v71", "v72", "v73", "v74", \
  "v75", "v76", "v77", "v78", "v79", "v100", "v101", "v102", "v103", "v104", "v105", "v106", "v107", "v108", "v109", "v110", "v111", "v112", "v113", \
  "v114", "v115", "v116", "v117", "v118", "v119", "v120", "v121", "v122", "v123", "s68", "s69", "s70", "s71", "s72", "s73", "s74", "s75", "s76", "s77"
#define PROJ_ASM \
  "s_waitcnt vmcnt(0)\n" \
  "s_barrier\n" \
  "ds_read_b128 v[32:35], v0 offset:0\n" \
  "ds_read_b128 v[36:39], v0 offset:20480\n" \
  "ds_read_b128 v[40:43], v0 offset:1024\n" \
  "ds_read_b128 v[44:47], v0 offset:21504\n" \
  "v_add_u32 v9, 40960, v0\n" \
  "v_add_u32 v10, 81920, v0\n" \
  "s_mul_i32 s68, s52, 40960\n" \
  "s_add_u32 s68, s68, 81920\n" \
  "s_mov_b32 s69, 0x40000000\n" \
  "s_mov_b32 s71, 0x40000000\n" \
  "v_mov_b32 v12, -1\n" \
  "v_mov_b32 v13, -1\n" \
  "v_mov_b32 v14, -1\n" \
  "v_mov_b32 v15, -1\n" \
  "LPL_%=:\n" \
  "s_waitcnt lgkmcnt(2)\n" \
  "v_mfma_f32_16x16x32_bf16 v[56:59], a[0:3], v[32:35], 0\n" \
  "ds_read_b128 v[48:51], v0 offset:2048\n" \
  "v_mfma_f32_16x16x32_bf16 v[68:71], a[0:3], v[36:39], 0\n" \
  "ds_read_b128 v[52:55], v0 offset:22528\n" \
  "v_mfma_f32_16x16x32_bf16 v[60:63], a[80:83], v[32:35], 0\n" \
  "buffer_store_dwordx4 v[12:15], v7, s[40:43], s71 offen sc1\n" \
  "v_mfma_f32_16x16x32_bf16 v[72:75], a[80:83], v[36:39], 0\n" \
  "v_cvt_pk_bf16_f32 v16, v100, v101\n" \
  "v_mfma_f32_16x16x32_bf16 v[64:67], a[160:163], v[32:35], 0\n" \
  "v_cvt_pk_bf16_f32 v17, v102, v103\n" \
  "v_mfma_f32_16x16x32_bf16 v[76:79], a[160:163], v[36:39], 0\n" \
  "v_cvt_pk_bf16_f32 v18, v104, v105\n" \
  "s_waitcnt lgkmcnt(2)\n" \
  "v_mfma_f32_16x16x32_bf16 v[56:59], a[4:7], v[40:43], v[56:59]\n" \
  "ds_read_b128 v[32:35], v0 offset:3072\n" \
  "v_mfma_f32_16x16x32_bf16 v[68:71], a[4:7], v[44:47], v[68:71]\n" \
  "ds_read_b128 v[36:39], v0 offset:23552\n" \
  "v_mfma_f32_16x16x32_bf16 v[60:63], a[84:87], v[40:43], v[60:63]\n" \
  "v_cvt_pk_bf16_f32 v19, v106, v107\n" \
  "v_mfma_f32_16x16x32_bf16 v[72:75], a[84:87], v[44:47], v[72:75]\n" \
  "buffer_store_dwordx4 v[16:19], v2, s[40:43], s69 offen sc1\n" \
  "v_mfma_f32_16x16x32_bf16 v[64:67], a[164:167], v[40:43], v[64:67]\n" \
  "v_cvt_pk_bf16_f32 v20, v112, v113\n" \
  "v_mfma_f32_16x16x32_bf16 v[76:79], a[164:167], v[44:47], v[76:79]\n" \
  "v_cvt_pk_bf16_f32 v21, v114, v115\n" \
  "s_waitcnt lgkmcnt(2)\n" \
  "v_mfma_f32_16x16x32_bf16 v[56:59], a[8:11], v[48:51], v[56:59]\n" \
  "ds_read_b128 v[40:43], v0 offset:4096\n" \
  "v_mfma_f32_16x16x32_bf16 v[68:71], a[8:11], v[52:55], v[68:71]\n" \
  "ds_read_b128 v[44:47], v0 offset:24576\n" \
  "v_mfma_f32_16x16x32_bf16 v[60:63], a[88:91], v[48:51], v[60:63]\n" \
  "v_cvt_pk_bf16_f32 v22, v116, v117\n" \
  "v_mfma_f32_16x16x32_bf16 v[72:75], a[88:91], v[52:55], v[72:75]\n" \
  "v_cvt_pk_bf16_f32 v23, v118, v119\n" \
  "v_mfma_f32_16x16x32_bf16 v[64:67], a[168:171], v[48:51], v[64:67]\n" \
  "buffer_store_dwordx4 v[20:23], v2, s[40:43], s69 offen offset:1024 sc1\n" \
  "v_mfma_f32_16x16x32_bf16 v[76:79], a[168:171], v[52:55], v[76:79]\n" \
  "v_cvt_pk_bf16_f32 v24, v108, v109\n" \
  "s_waitcnt lgkmcnt(2)\n" \
  "v_mfma_f32_16x16x32_bf16 v[56:59], a[12:15], v[32:35], v[56:59]\n" \
  "ds_read_b128 v[48:51], v0 offset:5120\n" \
  "v_mfma_f32_16x16x32_bf16 v[68:71], a[12:15], v[36:39], v[68:71]\n" \
  "ds_read_b128 v[52:55], v0 offset:25600\n" \
  "v_mfma_f32_16x16x32_bf16 v[60:63], a[92:95], v[32:35], v[60:63]\n" \
  "v_cvt_pk_bf16_f32 v25, v110, v111\n" \
  "v_mfma_f32_16x16x32_bf16 v[72:75], a[92:95], v[36:39], v[72:75]\n" \
  "v_cvt_pk_bf16_f32 v26, v120, v121\n" \
  "v_mfma_f32_16x16x32_bf16 v[64:67], a[172:175], v[32:35], v[64:67]\n" \
  "v_cvt_pk_bf16_f32 v27, v122, v123\n" \
  "v_mfma_f32_16x16x32_bf16 v[76:79], a[172:175], v[36:39], v[76:79]\n" \
  "buffer_store_dwordx4 v[24:27], v3, s[40:43], s69 offen sc1\n" \
  "s_waitcnt lgkmcnt(2)\n" \
  "v_mfma_f32_16x16x32_bf16 v[56:59], a[16:19], v[40:43], v[56:59]\n" \
  "ds_read_b128 v[32:35], v0 offset:6144\n" \
  "v_mfma_f32_16x16x32_bf16 v[68:71], a[16:19], v[44:47], v[68:71]\n" \
  "ds_read_b128 v[36:39], v0 offset:26624\n" \
  "v_mfma_f32_16x16x32_bf16 v[60:63], a[96:99], v[40:43], v[60:63]\n" \
  "s_add_u32 s72, s68, 4096\n" \
  "v_mfma_f32_16x16x32_bf16 v[72:75], a[96:99], v[44:47], v[72:75]\n" \
  "s_add_u32 s73, s68, 8192\n" \
  "v_mfma_f32_16x16x32_bf16 v[64:67], a[176:179], v[40:43], v[64:67]\n" \
  "s_add_u32 m0, s54, 81920\n" \
  "v_mfma_f32_16x16x32_bf16 v[76:79], a[176:179], v[44:47], v[76:79]\n" \
  "buffer_load_dwordx4 v1, s[36:39], s68 offen offset:0 lds\n" \
  "s_waitcnt lgkmcnt(2)\n" \
  "v_mfma_f32_16x16x32_bf16 v[56:59], a[20:23], v[48:51], v[56:59]\n" \
  "ds_read_b128 v[40:43], v0 offset:7168\n" \
  "v_mfma_f32_16x16x32_bf16 v[68:71], a[20:23], v[52:55], v[68:71]\n" \
  "ds_read_b128 v[44:47], v0 offset:27648\n" \
  "v_mfma_f32_16x16x32_bf16 v[60:63], a[100:103], v[48:51], v[60:63]\n" \
  "buffer_load_dwordx4 v1, s[36:39], s68 offen offset:1024 lds\n" \
  "v_mfma_f32_16x16x32_bf16 v[72:75], a[100:103], v[52:55], v[72:75]\n" \
  "v_mfma_f32_16x16x32_bf16 v[64:67], a[180:183], v[48:51], v[64:67]\n" \
  "v_mfma_f32_16x16x32_bf16 v[76:79], a[180:183], v[52:55], v[76:79]\n" \
  "s_waitcnt lgkmcnt(2)\n" \
  "v_mfma_f32_16x16x32_bf16 v[56:59], a[24:27], v[32:35], v[56:59]\n" \
  "ds_read_b128 v[48:51], v0 offset:8192\n" \
  "v_mfma_f32_16x16x32_bf16 v[68:71], a[24:27], v[36:39], v[68:71]\n" \
  "ds_read_b128 v[52:55], v0 offset:28672\n" \
  "v_mfma_f32_16x16x32_bf16 v[60:63], a[104:107], v[32:35], v[60:63]\n" \
  "buffer_load_dwordx4 v1, s[36:39], s68 offen offset:2048 lds\n" \
  "v_mfma_f32_16x16x32_bf16 v[72:75], a[104:107], v[36:39], v[72:75]\n" \
  "v_mfma_f32_16x16x32_bf16 v[64:67], a[184:187], v[32:35], v[64:67]\n" \
  "v_mfma_f32_16x16x32_bf16 v[76:79], a[184:187], v[36:39], v[76:79]\n" \
  "s_waitcnt lgkmcnt(2)\n" \
  "v_mfma_f32_16x16x32_bf16 v[56:59], a[28:31], v[40:43], v[56:59]\n" \
  "ds_read_b128 v[32:35], v0 offset:9216\n" \
  "v_mfma_f32_16x16x32_bf16 v[68:71], a[28:31], v[44:47], v[68:71]\n" \
  "ds_read_b128 v[36:39], v0 offset:29696\n" \
  "v_mfma_f32_16x16x32_bf16 v[60:63], a[108:111], v[40:43], v[60:63]\n" \
  "buffer_load_dwordx4 v1, s[36:39], s68 offen offset:3072 lds\n" \
  "v_mfma_f32_16x16x32_bf16 v[72:75], a[108:111], v[44:47], v[72:75]\n" \
  "v_mfma_f32_16x16x32_bf16 v[64:67], a[188:191], v[40:43], v[64:67]\n" \
  "v_mfma_f32_16x16x32_bf16 v[76:79], a[188:191], v[44:47], v[76:79]\n" \
  "s_waitcnt lgkmcnt(2)\n" \
  "v_mfma_f32_16x16x32_bf16 v[56:59], a[32:35], v[48:51], v[56:59]\n" \
  "ds_read_b128 v[40:43], v0 offset:10240\n" \
  "v_mfma_f32_16x16x32_bf16 v[68:71], a[32:35], v[52:55], v[68:71]\n" \
  "ds_read_b128 v[44:47], v0 offset:30720\n" \
  "v_mfma_f32_16x16x32_bf16 v[60:63], a[112:115], v[48:51], v[60:63]\n" \
  "s_add_u32 m0, s54, 86016\n" \
  "v_mfma_f32_16x16x32_bf16 v[72:75], a[112:115], v[52:55], v[72:75]\n" \
  "buffer_load_dwordx4 v1, s[36:39], s72 offen offset:0 lds\n" \
  "v_mfma_f32_16x16x32_bf16 v[64:67], a[192:195], v[48:51], v[64:67]\n" \
  "v_mfma_f32_16x16x32_bf16 v[76:79], a[192:195], v[52:55], v[76:79]\n" \
  "s_waitcnt lgkmcnt(2)\n" \
  "v_mfma_f32_16x16x32_bf16 v[56:59], a[36:39], v[32:35], v[56:59]\n" \
  "ds_read_b128 v[48:51], v0 offset:11264\n" \
  "v_mfma_f32_16x16x32_bf16 v[68:71], a[36:39], v[36:39], v[68:71]\n" \
  "ds_read_b128 v[52:55], v0 offset:31744\n" \
  "v_mfma_f32_16x16x32_bf16 v[60:63], a[116:119], v[32:35], v[60:63]\n" \
  "buffer_load_dwordx4 v1, s[36:39], s72 offen offset:1024 lds\n" \
  "v_mfma_f32_16x16x32_bf16 v[72:75], a[116:119], v[36:39], v[72:75]\n" \
  "v_mfma_f32_16x16x32_bf16 v[64:67], a[196:199], v[32:35], v[64:67]\n" \
  "v_mfma_f32_16x16x32_bf16 v[76:79], a[196:199], v[36:39], v[76:79]\n" \
  "s_waitcnt lgkmcnt(2)\n" \
  "v_mfma_f32_16x16x32_bf16 v[56:59], a[40:43], v[40:43], v[56:59]\n" \
  "ds_read_b128 v[32:35], v0 offset:12288\n" \
  "v_mfma_f32_16x16x32_bf16 v[68:71], a[40:43], v[44:47], v[68:71]\n" \
  "ds_read_b128 v[36:39], v0 offset:32768\n" \
  "v_mfma_f32_16x16x32_bf16 v[60:63], a[120:123], v[40:43], v[60:63]\n" \
  "buffer_load_dwordx4 v1, s[36:39], s72 offen offset:2048 lds\n" \
  "v_mfma_f32_16x16x32_bf16 v[72:75], a[120:123], v[44:47], v[72:75]\n" \
  "v_mfma_f32_16x16x32_bf16 v[64:67], a[200:203], v[40:43], v[64:67]\n" \
  "v_mfma_f32_16x16x32_bf16 v[76:79], a[200:203], v[44:47], v[76:79]\n" \
  "s_waitcnt lgkmcnt(2)\n" \
  "v_mfma_f32_16x16x32_bf16 v[56:59], a[44:47], v[48:51], v[56:59]\n" \
  "ds_read_b128 v[40:43], v0 offset:13312\n" \
  "v_mfma_f32_16x16x32_bf16 v[68:71], a[44:47], v[52:55], v[68:71]\n" \
  "ds_read_b128 v[44:47], v0 offset:33792\n" \
  "v_mfma_f32_16x16x32_bf16 v[60:63], a[124:127], v[48:51], v[60:63]\n" \
  "buffer_load_dwordx4 v1, s[36:39], s72 offen offset:3072 lds\n" \
  "v_mfma_f32_16x16x32_bf16 v[72:75], a[124:127], v[52:55], v[72:75]\n" \
  "v_mfma_f32_16x16x32_bf16 v[64:67], a[204:207], v[48:51], v[64:67]\n" \
  "v_mfma_f32_16x16x32_bf16 v[76:79], a[204:207], v[52:55], v[76:79]\n" \
  "s_waitcnt lgkmcnt(2)\n" \
  "v_mfma_f32_16x16x32_bf16 v[56:59], a[48:51], v[32:35], v[56:59]\n" \
  "ds_read_b128 v[48:51], v0 offset:14336\n" \
  "v_mfma_f32_16x16x32_bf16 v[68:71], a[48:51], v[36:39], v[68:71]\n" \
  "ds_read_b128 v[52:55], v0 offset:34816\n" \
  "v_mfma_f32_16x16x32_bf16 v[60:63], a[128:131], v[32:35], v[60:63]\n" \
  "s_add_u32 m0, s54, 90112\n" \
  "v_mfma_f32_16x16x32_bf16 v[72:75], a[128:131], v[36:39], v[72:75]\n" \
  "buffer_load_dwordx4 v1, s[36:39], s73 offen offset:0 lds\n" \
  "v_mfma_f32_16x16x32_bf16 v[64:67], a[208:211], v[32:35], v[64:67]\n" \
  "v_mfma_f32_16x16x32_bf16 v[76:79], a[208:211], v[36:39], v[76:79]\n" \
  "s_waitcnt lgkmcnt(2)\n" \
  "v_mfma_f32_16x16x32_bf16 v[56:59], a[52:55], v[40:43], v[56:59]\n" \
  "ds_read_b128 v[32:35], v0 offset:15360\n" \
  "v_mfma_f32_16x16x32_bf16 v[68:71], a[52:55], v[44:47], v[68:71]\n" \
  "ds_read_b128 v[36:39], v0 offset:35840\n" \
  "v_mfma_f32_16x16x32_bf16 v[60:63], a[132:135], v[40:43], v[60:63]\n" \
  "buffer_load_dwordx4 v1, s[36:39], s73 offen offset:1024 lds\n" \
  "v_mfma_f32_16x16x32_bf16 v[72:75], a[132:135], v[44:47], v[72:75]\n" \
  "v_mfma_f32_16x16x32_bf16 v[64:67], a[212:215], v[40:43], v[64:67]\n" \
  "v_mfma_f32_16x16x32_bf16 v[76:79], a[212:215], v[44:47], v[76:79]\n" \
  "s_waitcnt lgkmcnt(2)\n" \
  "v_mfma_f32_16x16x32_bf16 v[56:59], a[56:59], v[48:51], v[56:59]\n" \
  "ds_read_b128 v[40:43], v0 offset:16384\n" \
  "v_mfma_f32_16x16x32_bf16 v[68:71], a[56:59], v[52:55], v[68:71]\n" \
  "ds_read_b128 v[44:47], v0 offset:36864\n" \
  "v_mfma_f32_16x16x32_bf16 v[60:63], a[136:139], v[48:51], v[60:63]\n" \
  "s_add_u32 s68, s68, 40960\n" \
  "v_mfma_f32_16x16x32_bf16 v[72:75], a[136:139], v[52:55], v[72:75]\n" \
  "v_mfma_f32_16x16x32_bf16 v[64:67], a[216:219], v[48:51], v[64:67]\n" \
  "v_mfma_f32_16x16x32_bf16 v[76:79], a[216:219], v[52:55], v[76:79]\n" \
  "s_waitcnt lgkmcnt(2)\n" \
  "v_mfma_f32_16x16x32_bf16 v[56:59], a[60:63], v[32:35], v[56:59]\n" \
  "ds_read_b128 v[48:51], v0 offset:17408\n" \
  "v_mfma_f32_16x16x32_bf16 v[68:71], a[60:63], v[36:39], v[68:71]\n" \
  "ds_read_b128 v[52:55], v0 offset:37888\n" \
  "v_mfma_f32_16x16x32_bf16 v[60:63], a[140:143], v[32:35], v[60:63]\n" \
  "v_mov_b32 v11, s52\n" \
  "v_mfma_f32_16x16x32_bf16 v[72:75], a[140:143], v[36:39], v[72:75]\n" \
  "s_waitcnt vmcnt(10)\n" \
  "v_mfma_f32_16x16x32_bf16 v[64:67], a[220:223], v[32:35], v[64:67]\n" \
  "buffer_store_dword v11, v5, s[40:43], 0 offen sc1\n" \
  "v_mfma_f32_16x16x32_bf16 v[76:79], a[220:223], v[36:39], v[76:79]\n" \
  "s_waitcnt lgkmcnt(2)\n" \
  "v_mfma_f32_16x16x32_bf16 v[56:59], a[64:67], v[40:43], v[56:59]\n" \
  "ds_read_b128 v[32:35], v0 offset:18432\n" \
  "v_mfma_f32_16x16x32_bf16 v[68:71], a[64:67], v[44:47], v[68:71]\n" \
  "ds_read_b128 v[36:39], v0 offset:38912\n" \
  "v_mfma_f32_16x16x32_bf16 v[60:63], a[144:147], v[40:43], v[60:63]\n" \
  "v_mfma_f32_16x16x32_bf16 v[72:75], a[144:147], v[44:47], v[72:75]\n" \
  "v_mfma_f32_16x16x32_bf16 v[64:67], a[224:227], v[40:43], v[64:67]\n" \
  "v_mfma_f32_16x16x32_bf16 v[76:79], a[224:227], v[44:47], v[76:79]\n" \
  "s_waitcnt lgkmcnt(2)\n" \
  "v_mfma_f32_16x16x32_bf16 v[56:59], a[68:71], v[48:51], v[56:59]\n" \
  "ds_read_b128 v[40:43], v0 offset:19456\n" \
  "v_mfma_f32_16x16x32_bf16 v[68:71], a[68:71], v[52:55], v[68:71]\n" \
  "ds_read_b128 v[44:47], v0 offset:39936\n" \
  "v_mfma_f32_16x16x32_bf16 v[60:63], a[148:151], v[48:51], v[60:63]\n" \
  "v_readlane_b32 s69, v4, s52\n" \
  "v_mfma_f32_16x16x32_bf16 v[72:75], a[148:151], v[52:55], v[72:75]\n" \
  "v_readlane_b32 s71, v8, s52\n" \
  "v_mfma_f32_16x16x32_bf16 v[64:67], a[228:231], v[48:51], v[64:67]\n" \
  "s_add_u32 s52, s52, 1\n" \
  "v_mfma_f32_16x16x32_bf16 v[76:79], a[228:231], v[52:55], v[76:79]\n" \
  "s_waitcnt lgkmcnt(0)\n" \
  "s_barrier\n" \
  "v_mfma_f32_16x16x32_bf16 v[56:59], a[72:75], v[32:35], v[56:59]\n" \
  "ds_read_b128 v[48:51], v9 offset:0\n" \
  "v_mfma_f32_16x16x32_bf16 v[68:71], a[72:75], v[36:39], v[68:71]\n" \
  "ds_read_b128 v[52:55], v9 offset:20480\n" \
  "v_mfma_f32_16x16x32_bf16 v[60:63], a[152:155], v[32:35], v[60:63]\n" \
  "v_mfma_f32_16x16x32_bf16 v[72:75], a[152:155], v[36:39], v[72:75]\n" \
  "v_mfma_f32_16x16x32_bf16 v[64:67], a[232:235], v[32:35], v[64:67]\n" \
  "v_mfma_f32_16x16x32_bf16 v[76:79], a[232:235], v[36:39], v[76:79]\n" \
  "v_mfma_f32_16x16x32_bf16 v[56:59], a[76:79], v[40:43], v[56:59]\n" \
  "ds_read_b128 v[32:35], v9 offset:1024\n" \
  "v_mfma_f32_16x16x32_bf16 v[68:71], a[76:79], v[44:47], v[68:71]\n" \
  "ds_read_b128 v[36:39], v9 offset:21504\n" \
  "v_mfma_f32_16x16x32_bf16 v[60:63], a[156:159], v[40:43], v[60:63]\n" \
  "s_cmp_eq_u32 s52, s53\n" \
  "v_mfma_f32_16x16x32_bf16 v[72:75], a[156:159], v[44:47], v[72:75]\n" \
  "v_mfma_f32_16x16x32_bf16 v[64:67], a[236:239], v[40:43], v[64:67]\n" \
  "v_mfma_f32_16x16x32_bf16 v[76:79], a[236:239], v[44:47], v[76:79]\n" \
  "s_waitcnt lgkmcnt(2)\n" \
  "v_mfma_f32_16x16x32_bf16 v[100:103], a[0:3], v[48:51], 0\n" \
  "ds_read_b128 v[40:43], v9 offset:2048\n" \
  "v_mfma_f32_16x16x32_bf16 v[112:115], a[0:3], v[52:55], 0\n" \
  "ds_read_b128 v[44:47], v9 offset:22528\n" \
  "v_mfma_f32_16x16x32_bf16 v[104:107], a[80:83], v[48:51], 0\n" \
  "buffer_store_dwordx4 v[12:15], v7, s[40:43], s71 offen sc1\n" \
  "v_mfma_f32_16x16x32_bf16 v[116:119], a[80:83], v[52:55], 0\n" \
  "v_cvt_pk_bf16_f32 v16, v56, v57\n" \
  "v_mfma_f32_16x16x32_bf16 v[108:111], a[160:163], v[48:51], 0\n" \
  "v_cvt_pk_bf16_f32 v17, v58, v59\n" \
  "v_mfma_f32_16x16x32_bf16 v[120:123], a[160:163], v[52:55], 0\n" \
  "v_cvt_pk_bf16_f32 v18, v60, v61\n" \
  "s_waitcnt lgkmcnt(2)\n" \
  "v_mfma_f32_16x16x32_bf16 v[100:103], a[4:7], v[32:35], v[100:103]\n" \
  "ds_read_b128 v[48:51], v9 offset:3072\n" \
  "v_mfma_f32_16x16x32_bf16 v[112:115], a[4:7], v[36:39], v[112:115]\n" \
  "ds_read_b128 v[52:55], v9 offset:23552\n" \
  "v_mfma_f32_16x16x32_bf16 v[104:107], a[84:87], v[32:35], v[104:107]\n" \
  "v_cvt_pk_bf16_f32 v19, v62, v63\n" \
  "v_mfma_f32_16x16x32_bf16 v[116:119], a[84:87], v[36:39], v[116:119]\n" \
  "buffer_store_dwordx4 v[16:19], v2, s[40:43], s69 offen sc1\n" \
  "v_mfma_f32_16x16x32_bf16 v[108:111], a[164:167], v[32:35], v[108:111]\n" \
  "v_cvt_pk_bf16_f32 v20, v68, v69\n" \
  "v_mfma_f32_16x16x32_bf16 v[120:123], a[164:167], v[36:39], v[120:123]\n" \
  "v_cvt_pk_bf16_f32 v21, v70, v71\n" \
  "s_waitcnt lgkmcnt(2)\n" \
  "v_mfma_f32_16x16x32_bf16 v[100:103], a[8:11], v[40:43], v[100:103]\n" \
  "ds_read_b128 v[32:35], v9 offset:4096\n" \
  "v_mfma_f32_16x16x32_bf16 v[112:115], a[8:11], v[44:47], v[112:115]\n" \
  "ds_read_b128 v[36:39], v9 offset:24576\n" \
  "v_mfma_f32_16x16x32_bf16 v[104:107], a[88:91], v[40:43], v[104:107]\n" \
  "v_cvt_pk_bf16_f32 v22, v72, v73\n" \
  "v_mfma_f32_16x16x32_bf16 v[116:119], a[88:91], v[44:47], v[116:119]\n" \
  "v_cvt_pk_bf16_f32 v23, v74, v75\n" \
  "v_mfma_f32_16x16x32_bf16 v[108:111], a[168:171], v[40:43], v[108:111]\n" \
  "buffer_store_dwordx4 v[20:23], v2, s[40:43], s69 offen offset:1024 sc1\n" \
  "v_mfma_f32_16x16x32_bf16 v[120:123], a[168:171], v[44:47], v[120:123]\n" \
  "v_cvt_pk_bf16_f32 v24, v64, v65\n" \
  "s_waitcnt lgkmcnt(2)\n" \
  "v_mfma_f32_16x16x32_bf16 v[100:103], a[12:15], v[48:51], v[100:103]\n" \
  "ds_read_b128 v[40:43], v9 offset:5120\n" \
  "v_mfma_f32_16x16x32_bf16 v[112:115], a[12:15], v[52:55], v[112:115]\n" \
  "ds_read_b128 v[44:47], v9 offset:25600\n" \
  "v_mfma_f32_16x16x32_bf16 v[104:107], a[92:95], v[48:51], v[104:107]\n" \
  "v_cvt_pk_bf16_f32 v25, v66, v67\n" \
  "v_mfma_f32_16x16x32_bf16 v[116:119], a[92:95], v[52:55], v[116:119]\n" \
  "v_cvt_pk_bf16_f32 v26, v76, v77\n" \
  "v_mfma_f32_16x16x32_bf16 v[108:111], a[172:175], v[48:51], v[108:111]\n" \
  "v_cvt_pk_bf16_f32 v27, v78, v79\n" \
  "v_mfma_f32_16x16x32_bf16 v[120:123], a[172:175], v[52:55], v[120:123]\n" \
  "buffer_store_dwordx4 v[24:27], v3, s[40:43], s69 offen sc1\n" \
  "s_waitcnt lgkmcnt(2)\n" \
  "v_mfma_f32_16x16x32_bf16 v[100:103], a[16:19], v[32:35], v[100:103]\n" \
  "ds_read_b128 v[48:51], v9 offset:6144\n" \
  "v_mfma_f32_16x16x32_bf16 v[112:115], a[16:19], v[36:39], v[112:115]\n" \
  "ds_read_b128 v[52:55], v9 offset:26624\n" \
  "v_mfma_f32_16x16x32_bf16 v[104:107], a[96:99], v[32:35], v[104:107]\n" \
  "s_add_u32 s72, s68, 4096\n" \
  "v_mfma_f32_16x16x32_bf16 v[116:119], a[96:99], v[36:39], v[116:119]\n" \
  "s_add_u32 s73, s68, 8192\n" \
  "v_mfma_f32_16x16x32_bf16 v[108:111], a[176:179], v[32:35], v[108:111]\n" \
  "s_add_u32 m0, s54, 0\n" \
  "v_mfma_f32_16x16x32_bf16 v[120:123], a[176:179], v[36:39], v[120:123]\n" \
  "buffer_load_dwordx4 v1, s[36:39], s68 offen offset:0 lds\n" \
  "s_waitcnt lgkmcnt(2)\n" \
  "v_mfma_f32_16x16x32_bf16 v[100:103], a[20:23], v[40:43], v[100:103]\n" \
  "ds_read_b128 v[32:35], v9 offset:7168\n" \
  "v_mfma_f32_16x16x32_bf16 v[112:115], a[20:23], v[44:47], v[112:115]\n" \
  "ds_read_b128 v[36:39], v9 offset:27648\n" \
  "v_mfma_f32_16x16x32_bf16 v[104:107], a[100:103], v[40:43], v[104:107]\n" \
  "buffer_load_dwordx4 v1, s[36:39], s68 offen offset:1024 lds\n" \
  "v_mfma_f32_16x16x32_bf16 v[116:119], a[100:103], v[44:47], v[116:119]\n" \
  "v_mfma_f32_16x16x32_bf16 v[108:111], a[180:183], v[40:43], v[108:111]\n" \
  "v_mfma_f32_16x16x32_bf16 v[120:123], a[180:183], v[44:47], v[120:123]\n" \
  "s_waitcnt lgkmcnt(2)\n" \
  "v_mfma_f32_16x16x32_bf16 v[100:103], a[24:27], v[48:51], v[100:103]\n" \
  "ds_read_b128 v[40:43], v9 offset:8192\n" \
  "v_mfma_f32_16x16x32_bf16 v[112:115], a[24:27], v[52:55], v[112:115]\n" \
  "ds_read_b128 v[44:47], v9 offset:28672\n" \
  "v_mfma_f32_16x16x32_bf16 v[104:107], a[104:107], v[48:51], v[104:107]\n" \
  "buffer_load_dwordx4 v1, s[36:39], s68 offen offset:2048 lds\n" \
  "v_mfma_f32_16x16x32_bf16 v[116:119], a[104:107], v[52:55], v[116:119]\n" \
  "v_mfma_f32_16x16x32_bf16 v[108:111], a[184:187], v[48:51], v[108:111]\n" \
  "v_mfma_f32_16x16x32_bf16 v[120:123], a[184:187], v[52:55], v[120:123]\n" \
  "s_waitcnt lgkmcnt(2)\n" \
  "v_mfma_f32_16x16x32_bf16 v[100:103], a[28:31], v[32:35], v[100:103]\n" \
  "ds_read_b128 v[48:51], v9 offset:9216\n" \
  "v_mfma_f32_16x16x32_bf16 v[112:115], a[28:31], v[36:39], v[112:115]\n" \
  "ds_read_b128 v[52:55], v9 offset:29696\n" \
  "v_mfma_f32_16x16x32_bf16 v[104:107], a[108:111], v[32:35], v[104:107]\n" \
  "buffer_load_dwordx4 v1, s[36:39], s68 offen offset:3072 lds\n" \
  "v_mfma_f32_16x16x32_bf16 v[116:119], a[108:111], v[36:39], v[116:119]\n" \
  "v_mfma_f32_16x16x32_bf16 v[108:111], a[188:191], v[32:35], v[108:111]\n" \
  "v_mfma_f32_16x16x32_bf16 v[120:123], a[188:191], v[36:39], v[120:123]\n" \
  "s_waitcnt lgkmcnt(2)\n" \
  "v_mfma_f32_16x16x32_bf16 v[100:103], a[32:35], v[40:43], v[100:103]\n" \
  "ds_read_b128 v[32:35], v9 offset:10240\n" \
  "v_mfma_f32_16x16x32_bf16 v[112:115], a[32:35], v[44:47], v[112:115]\n" \
  "ds_read_b128 v[36:39], v9 offset:30720\n" \
  "v_mfma_f32_16x16x32_bf16 v[104:107], a[112:115], v[40:43], v[104:107]\n" \
  "s_add_u32 m0, s54, 4096\n" \
  "v_mfma_f32_16x16x32_bf16 v[116:119], a[112:115], v[44:47], v[116:119]\n" \
  "buffer_load_dwordx4 v1, s[36:39], s72 offen offset:0 lds\n" \
  "v_mfma_f32_16x16x32_bf16 v[108:111], a[192:195], v[40:43], v[108:111]\n" \
  "v_mfma_f32_16x16x32_bf16 v[120:123], a[192:195], v[44:47], v[120:123]\n" \
  "s_waitcnt lgkmcnt(2)\n" \
  "v_mfma_f32_16x16x32_bf16 v[100:103], a[36:39], v[48:51], v[100:103]\n" \
  "ds_read_b128 v[40:43], v9 offset:11264\n" \
  "v_mfma_f32_16x16x32_bf16 v[112:115], a[36:39], v[52:55], v[112:115]\n" \
  "ds_read_b128 v[44:47], v9 offset:31744\n" \
  "v_mfma_f32_16x16x32_bf16 v[104:107], a[116:119], v[48:51], v[104:107]\n" \
  "buffer_load_dwordx4 v1, s[36:39], s72 offen offset:1024 lds\n" \
  "v_mfma_f32_16x16x32_bf16 v[116:119], a[116:119], v[52:55], v[116:119]\n" \
  "v_mfma_f32_16x16x32_bf16 v[108:111], a[196:199], v[48:51], v[108:111]\n" \
  "v_mfma_f32_16x16x32_bf16 v[120:123], a[196:199], v[52:55], v[120:123]\n" \
  "s_waitcnt lgkmcnt(2)\n" \
  "v_mfma_f32_16x16x32_bf16 v[100:103], a[40:43], v[32:35], v[100:103]\n" \
  "ds_read_b128 v[48:51], v9 offset:12288\n" \
  "v_mfma_f32_16x16x32_bf16 v[112:115], a[40:43], v[36:39], v[112:115]\n" \
  "ds_read_b128 v[52:55], v9 offset:32768\n" \
  "v_mfma_f32_16x16x32_bf16 v[104:107], a[120:123], v[32:35], v[104:107]\n" \
  "buffer_load_dwordx4 v1, s[36:39], s72 offen offset:2048 lds\n" \
  "v_mfma_f32_16x16x32_bf16 v[116:119], a[120:123], v[36:39], v[116:119]\n" \
  "v_mfma_f32_16x16x32_bf16 v[108:111], a[200:203], v[32:35], v[108:111]\n" \
  "v_mfma_f32_16x16x32_bf16 v[120:123], a[200:203], v[36:39], v[120:123]\n" \
  "s_waitcnt lgkmcnt(2)\n" \
  "v_mfma_f32_16x16x32_bf16 v[100:103], a[44:47], v[40:43], v[100:103]\n" \
  "ds_read_b128 v[32:35], v9 offset:13312\n" \
  "v_mfma_f32_16x16x32_bf16 v[112:115], a[44:47], v[44:47], v[112:115]\n" \
  "ds_read_b128 v[36:39], v9 offset:33792\n" \
  "v_mfma_f32_16x16x32_bf16 v[104:107], a[124:127], v[40:43], v[104:107]\n" \
  "buffer_load_dwordx4 v1, s[36:39], s72 offen offset:3072 lds\n" \
  "v_mfma_f32_16x16x32_bf16 v[116:119], a[124:127], v[44:47], v[116:119]\n" \
  "v_mfma_f32_16x16x32_bf16 v[108:111], a[204:207], v[40:43], v[108:111]\n" \
  "v_mfma_f32_16x16x32_bf16 v[120:123], a[204:207], v[44:47], v[120:123]\n" \
  "s_waitcnt lgkmcnt(2)\n" \
  "v_mfma_f32_16x16x32_bf16 v[100:103], a[48:51], v[48:51], v[100:103]\n" \
  "ds_read_b128 v[40:43], v9 offset:14336\n" \
  "v_mfma_f32_16x16x32_bf16 v[112:115], a[48:51], v[52:55], v[112:115]\n" \
  "ds_read_b128 v[44:47], v9 offset:34816\n" \
  "v_mfma_f32_16x16x32_bf16 v[104:107], a[128:131], v[48:51], v[104:107]\n" \
  "s_add_u32 m0, s54, 8192\n" \
  "v_mfma_f32_16x16x32_bf16 v[116:119], a[128:131], v[52:55], v[116:119]\n" \
  "buffer_load_dwordx4 v1, s[36:39], s73 offen offset:0 lds\n" \
  "v_mfma_f32_16x16x32_bf16 v[108:111], a[208:211], v[48:51], v[108:111]\n" \
  "v_mfma_f32_16x16x32_bf16 v[120:123], a[208:211], v[52:55], v[120:123]\n" \
  "s_waitcnt lgkmcnt(2)\n" \
  "v_mfma_f32_16x16x32_bf16 v[100:103], a[52:55], v[32:35], v[100:103]\n" \
  "ds_read_b128 v[48:51], v9 offset:15360\n" \
  "v_mfma_f32_16x16x32_bf16 v[112:115], a[52:55], v[36:39], v[112:115]\n" \
  "ds_read_b128 v[52:55], v9 offset:35840\n" \
  "v_mfma_f32_16x16x32_bf16 v[104:107], a[132:135], v[32:35], v[104:107]\n" \
  "buffer_load_dwordx4 v1, s[36:39], s73 offen offset:1024 lds\n" \
  "v_mfma_f32_16x16x32_bf16 v[116:119], a[132:135], v[36:39], v[116:119]\n" \
  "v_mfma_f32_16x16x32_bf16 v[108:111], a[212:215], v[32:35], v[108:111]\n" \
  "v_mfma_f32_16x16x32_bf16 v[120:123], a[212:215], v[36:39], v[120:123]\n" \
  "s_waitcnt lgkmcnt(2)\n" \
  "v_mfma_f32_16x16x32_bf16 v[100:103], a[56:59], v[40:43], v[100:103]\n" \
  "ds_read_b128 v[32:35], v9 offset:16384\n" \
  "v_mfma_f32_16x16x32_bf16 v[112:115], a[56:59], v[44:47], v[112:115]\n" \
  "ds_read_b128 v[36:39], v9 offset:36864\n" \
  "v_mfma_f32_16x16x32_bf16 v[104:107], a[136:139], v[40:43], v[104:107]\n" \
  "s_add_u32 s68, s68, 40960\n" \
  "v_mfma_f32_16x16x32_bf16 v[116:119], a[136:139], v[44:47], v[116:119]\n" \
  "v_mfma_f32_16x16x32_bf16 v[108:111], a[216:219], v[40:43], v[108:111]\n" \
  "v_mfma_f32_16x16x32_bf16 v[120:123], a[216:219], v[44:47], v[120:123]\n" \
  "s_waitcnt lgkmcnt(2)\n" \
  "v_mfma_f32_16x16x32_bf16 v[100:103], a[60:63], v[48:51], v[100:103]\n" \
  "ds_read_b128 v[40:43], v9 offset:17408\n" \
  "v_mfma_f32_16x16x32_bf16 v[112:115], a[60:63], v[52:55], v[112:115]\n" \
  "ds_read_b128 v[44:47], v9 offset:37888\n" \
  "v_mfma_f32_16x16x32_bf16 v[104:107], a[140:143], v[48:51], v[104:107]\n" \
  "v_mov_b32 v11, s52\n" \
  "v_mfma_f32_16x16x32_bf16 v[116:119], a[140:143], v[52:55], v[116:119]\n" \
  "s_waitcnt vmcnt(10)\n" \
  "v_mfma_f32_16x16x32_bf16 v[108:111], a[220:223], v[48:51], v[108:111]\n" \
  "buffer_store_dword v11, v5, s[40:43], 0 offen sc1\n" \
  "v_mfma_f32_16x16x32_bf16 v[120:123], a[220:223], v[52:55], v[120:123]\n" \
  "s_waitcnt lgkmcnt(2)\n" \
  "v_mfma_f32_16x16x32_bf16 v[100:103], a[64:67], v[32:35], v[100:103]\n" \
  "ds_read_b128 v[48:51], v9 offset:18432\n" \
  "v_mfma_f32_16x16x32_bf16 v[112:115], a[64:67], v[36:39], v[112:115]\n" \
  "ds_read_b128 v[52:55], v9 offset:38912\n" \
  "v_mfma_f32_16x16x32_bf16 v[104:107], a[144:147], v[32:35], v[104:107]\n" \
  "v_mfma_f32_16x16x32_bf16 v[116:119], a[144:147], v[36:39], v[116:119]\n" \
  "v_mfma_f32_16x16x32_bf16 v[108:111], a[224:227], v[32:35], v[108:111]\n" \
  "v_mfma_f32_16x16x32_bf16 v[120:123], a[224:227], v[36:39], v[120:123]\n" \
  "s_waitcnt lgkmcnt(2)\n" \
  "v_mfma_f32_16x16x32_bf16 v[100:103], a[68:71], v[40:43], v[100:103]\n" \
  "ds_read_b128 v[32:35], v9 offset:19456\n" \
  "v_mfma_f32_16x16x32_bf16 v[112:115], a[68:71], v[44:47], v[112:115]\n" \
  "ds_read_b128 v[36:39], v9 offset:39936\n" \
  "v_mfma_f32_16x16x32_bf16 v[104:107], a[148:151], v[40:43], v[104:107]\n" \
  "v_readlane_b32 s69, v4, s52\n" \
  "v_mfma_f32_16x16x32_bf16 v[116:119], a[148:151], v[44:47], v[116:119]\n" \
  "v_readlane_b32 s71, v8, s52\n" \
  "v_mfma_f32_16x16x32_bf16 v[108:111], a[228:231], v[40:43], v[108:111]\n" \
  "s_add_u32 s52, s52, 1\n" \
  "v_mfma_f32_16x16x32_bf16 v[120:123], a[228:231], v[44:47], v[120:123]\n" \
  "s_waitcnt lgkmcnt(0)\n" \
  "s_barrier\n" \
  "v_mfma_f32_16x16x32_bf16 v[100:103], a[72:75], v[48:51], v[100:103]\n" \
  "ds_read_b128 v[40:43], v10 offset:0\n" \
  "v_mfma_f32_16x16x32_bf16 v[112:115], a[72:75], v[52:55], v[112:115]\n" \
  "ds_read_b128 v[44:47], v10 offset:20480\n" \
  "v_mfma_f32_16x16x32_bf16 v[104:107], a[152:155], v[48:51], v[104:107]\n" \
  "v_mfma_f32_16x16x32_bf16 v[116:119], a[152:155], v[52:55], v[116:119]\n" \
  "v_mfma_f32_16x16x32_bf16 v[108:111], a[232:235], v[48:51], v[108:111]\n" \
  "v_mfma_f32_16x16x32_bf16 v[120:123], a[232:235], v[52:55], v[120:123]\n" \
  "v_mfma_f32_16x16x32_bf16 v[100:103], a[76:79], v[32:35], v[100:103]\n" \
  "ds_read_b128 v[48:51], v10 offset:1024\n" \
  "v_mfma_f32_16x16x32_bf16 v[112:115], a[76:79], v[36:39], v[112:115]\n" \
  "ds_read_b128 v[52:55], v10 offset:21504\n" \
  "v_mfma_f32_16x16x32_bf16 v[104:107], a[156:159], v[32:35], v[104:107]\n" \
  "s_cmp_eq_u32 s52, s53\n" \
  "v_mfma_f32_16x16x32_bf16 v[116:119], a[156:159], v[36:39], v[116:119]\n" \
  "v_mfma_f32_16x16x32_bf16 v[108:111], a[236:239], v[32:35], v[108:111]\n" \
  "v_mfma_f32_16x16x32_bf16 v[120:123], a[236:239], v[36:39], v[120:123]\n" \
  "s_waitcnt lgkmcnt(2)\n" \
  "v_mfma_f32_16x16x32_bf16 v[56:59], a[0:3], v[40:43], 0\n" \
  "ds_read_b128 v[32:35], v10 offset:2048\n" \
  "v_mfma_f32_16x16x32_bf16 v[68:71], a[0:3], v[44:47], 0\n" \
  "ds_read_b128 v[36:39], v10 offset:22528\n" \
  "v_mfma_f32_16x16x32_bf16 v[60:63], a[80:83], v[40:43], 0\n" \
  "buffer_store_dwordx4 v[12:15], v7, s[40:43], s71 offen sc1\n" \
  "v_mfma_f32_16x16x32_bf16 v[72:75], a[80:83], v[44:47], 0\n" \
  "v_cvt_pk_bf16_f32 v16, v100, v101\n" \
  "v_mfma_f32_16x16x32_bf16 v[64:67], a[160:163], v[40:43], 0\n" \
  "v_cvt_pk_bf16_f32 v17, v102, v103\n" \
  "v_mfma_f32_16x16x32_bf16 v[76:79], a[160:163], v[44:47], 0\n" \
  "v_cvt_pk_bf16_f32 v18, v104, v105\n" \
  "s_waitcnt lgkmcnt(2)\n" \
  "v_mfma_f32_16x16x32_bf16 v[56:59], a[4:7], v[48:51], v[56:59]\n" \
  "ds_read_b128 v[40:43], v10 offset:3072\n" \
  "v_mfma_f32_16x16x32_bf16 v[68:71], a[4:7], v[52:55], v[68:71]\n" \
  "ds_read_b128 v[44:47], v10 offset:23552\n" \
  "v_mfma_f32_16x16x32_bf16 v[60:63], a[84:87], v[48:51], v[60:63]\n" \
  "v_cvt_pk_bf16_f32 v19, v106, v107\n" \
  "v_mfma_f32_16x16x32_bf16 v[72:75], a[84:87], v[52:55], v[72:75]\n" \
  "buffer_store_dwordx4 v[16:19], v2, s[40:43], s69 offen sc1\n" \
  "v_mfma_f32_16x16x32_bf16 v[64:67], a[164:167], v[48:51], v[64:67]\n" \
  "v_cvt_pk_bf16_f32 v20, v112, v113\n" \
  "v_mfma_f32_16x16x32_bf16 v[76:79], a[164:167], v[52:55], v[76:79]\n" \
  "v_cvt_pk_bf16_f32 v21, v114, v115\n" \
  "s_waitcnt lgkmcnt(2)\n" \
  "v_mfma_f32_16x16x32_bf16 v[56:59], a[8:11], v[32:35], v[56:59]\n" \
  "ds_read_b128 v[48:51], v10 offset:4096\n" \
  "v_mfma_f32_16x16x32_bf16 v[68:71], a[8:11], v[36:39], v[68:71]\n" \
  "ds_read_b128 v[52:55], v10 offset:24576\n" \
  "v_mfma_f32_16x16x32_bf16 v[60:63], a[88:91], v[32:35], v[60:63]\n" \
  "v_cvt_pk_bf16_f32 v22, v116, v117\n" \
  "v_mfma_f32_16x16x32_bf16 v[72:75], a[88:91], v[36:39], v[72:75]\n" \
  "v_cvt_pk_bf16_f32 v23, v118, v119\n" \
  "v_mfma_f32_16x16x32_bf16 v[64:67], a[168:171], v[32:35], v[64:67]\n" \
  "buffer_store_dwordx4 v[20:23], v2, s[40:43], s69 offen offset:1024 sc1\n" \
  "v_mfma_f32_16x16x32_bf16 v[76:79], a[168:171], v[36:39], v[76:79]\n" \
  "v_cvt_pk_bf16_f32 v24, v108, v109\n" \
  "s_waitcnt lgkmcnt(2)\n" \
  "v_mfma_f32_16x16x32_bf16 v[56:59], a[12:15], v[40:43], v[56:59]\n" \
  "ds_read_b128 v[32:35], v10 offset:5120\n" \
  "v_mfma_f32_16x16x32_bf16 v[68:71], a[12:15], v[44:47], v[68:71]\n" \
  "ds_read_b128 v[36:39], v10 offset:25600\n" \
  "v_mfma_f32_16x16x32_bf16 v[60:63], a[92:95], v[40:43], v[60:63]\n" \
  "v_cvt_pk_bf16_f32 v25, v110, v111\n" \
  "v_mfma_f32_16x16x32_bf16 v[72:75], a[92:95], v[44:47], v[72:75]\n" \
  "v_cvt_pk_bf16_f32 v26, v120, v121\n" \
  "v_mfma_f32_16x16x32_bf16 v[64:67], a[172:175], v[40:43], v[64:67]\n" \
  "v_cvt_pk_bf16_f32 v27, v122, v123\n" \
  "v_mfma_f32_16x16x32_bf16 v[76:79], a[172:175], v[44:47], v[76:79]\n" \
  "buffer_store_dwordx4 v[24:27], v3, s[40:43], s69 offen sc1\n" \
  "s_waitcnt lgkmcnt(2)\n" \
  "v_mfma_f32_16x16x32_bf16 v[56:59], a[16:19], v[48:51], v[56:59]\n" \
  "ds_read_b128 v[40:43], v10 offset:6144\n" \
  "v_mfma_f32_16x16x32_bf16 v[68:71], a[16:19], v[52:55], v[68:71]\n" \
  "ds_read_b128 v[44:47], v10 offset:26624\n" \
  "v_mfma_f32_16x16x32_bf16 v[60:63], a[96:99], v[48:51], v[60:63]\n" \
  "s_add_u32 s72, s68, 4096\n" \
  "v_mfma_f32_16x16x32_bf16 v[72:75], a[96:99], v[52:55], v[72:75]\n" \
  "s_add_u32 s73, s68, 8192\n" \
  "v_mfma_f32_16x16x32_bf16 v[64:67], a[176:179], v[48:51], v[64:67]\n" \
  "s_add_u32 m0, s54, 40960\n" \
  "v_mfma_f32_16x16x32_bf16 v[76:79], a[176:179], v[52:55], v[76:79]\n" \
  "buffer_load_dwordx4 v1, s[36:39], s68 offen offset:0 lds\n" \
  "s_waitcnt lgkmcnt(2)\n" \
  "v_mfma_f32_16x16x32_bf16 v[56:59], a[20:23], v[32:35], v[56:59]\n" \
  "ds_read_b128 v[48:51], v10 offset:7168\n" \
  "v_mfma_f32_16x16x32_bf16 v[68:71], a[20:23], v[36:39], v[68:71]\n" \
  "ds_read_b128 v[52:55], v10 offset:27648\n" \
  "v_mfma_f32_16x16x32_bf16 v[60:63], a[100:103], v[32:35], v[60:63]\n" \
  "buffer_load_dwordx4 v1, s[36:39], s68 offen offset:1024 lds\n" \
  "v_mfma_f32_16x16x32_bf16 v[72:75], a[100:103], v[36:39], v[72:75]\n" \
  "v_mfma_f32_16x16x32_bf16 v[64:67], a[180:183], v[32:35], v[64:67]\n" \
  "v_mfma_f32_16x16x32_bf16 v[76:79], a[180:183], v[36:39], v[76:79]\n" \
  "s_waitcnt lgkmcnt(2)\n" \
  "v_mfma_f32_16x16x32_bf16 v[56:59], a[24:27], v[40:43], v[56:59]\n" \
  "ds_read_b128 v[32:35], v10 offset:8192\n" \
  "v_mfma_f32_16x16x32_bf16 v[68:71], a[24:27], v[44:47], v[68:71]\n" \
  "ds_read_b128 v[36:39], v10 offset:28672\n" \
  "v_mfma_f32_16x16x32_bf16 v[60:63], a[104:107], v[40:43], v[60:63]\n" \
  "buffer_load_dwordx4 v1, s[36:39], s68 offen offset:2048 lds\n" \
  "v_mfma_f32_16x16x32_bf16 v[72:75], a[104:107], v[44:47], v[72:75]\n" \
  "v_mfma_f32_16x16x32_bf16 v[64:67], a[184:187], v[40:43], v[64:67]\n" \
  "v_mfma_f32_16x16x32_bf16 v[76:79], a[184:187], v[44:47], v[76:79]\n" \
  "s_waitcnt lgkmcnt(2)\n" \
  "v_mfma_f32_16x16x32_bf16 v[56:59], a[28:31], v[48:51], v[56:59]\n" \
  "ds_read_b128 v[40:43], v10 offset:9216\n" \
  "v_mfma_f32_16x16x32_bf16 v[68:71], a[28:31], v[52:55], v[68:71]\n" \
  "ds_read_b128 v[44:47], v10 offset:29696\n" \
  "v_mfma_f32_16x16x32_bf16 v[60:63], a[108:111], v[48:51], v[60:63]\n" \
  "buffer_load_dwordx4 v1, s[36:39], s68 offen offset:3072 lds\n" \
  "v_mfma_f32_16x16x32_bf16 v[72:75], a[108:111], v[52:55], v[72:75]\n" \
  "v_mfma_f32_16x16x32_bf16 v[64:67], a[188:191], v[48:51], v[64:67]\n" \
  "v_mfma_f32_16x16x32_bf16 v[76:79], a[188:191], v[52:55], v[76:79]\n" \
  "s_waitcnt lgkmcnt(2)\n" \
  "v_mfma_f32_16x16x32_bf16 v[56:59], a[32:35], v[32:35], v[56:59]\n" \
  "ds_read_b128 v[48:51], v10 offset:10240\n" \
  "v_mfma_f32_16x16x32_bf16 v[68:71], a[32:35], v[36:39], v[68:71]\n" \
  "ds_read_b128 v[52:55], v10 offset:30720\n" \
  "v_mfma_f32_16x16x32_bf16 v[60:63], a[112:115], v[32:35], v[60:63]\n" \
  "s_add_u32 m0, s54, 45056\n" \
  "v_mfma_f32_16x16x32_bf16 v[72:75], a[112:115], v[36:39], v[72:75]\n" \
  "buffer_load_dwordx4 v1, s[36:39], s72 offen offset:0 lds\n" \
  "v_mfma_f32_16x16x32_bf16 v[64:67], a[192:195], v[32:35], v[64:67]\n" \
  "v_mfma_f32_16x16x32_bf16 v[76:79], a[192:195], v[36:39], v[76:79]\n" \
  "s_waitcnt lgkmcnt(2)\n" \
  "v_mfma_f32_16x16x32_bf16 v[56:59], a[36:39], v[40:43], v[56:59]\n" \
  "ds_read_b128 v[32:35], v10 offset:11264\n" \
  "v_mfma_f32_16x16x32_bf16 v[68:71], a[36:39], v[44:47], v[68:71]\n" \
  "ds_read_b128 v[36:39], v10 offset:31744\n" \
  "v_mfma_f32_16x16x32_bf16 v[60:63], a[116:119], v[40:43], v[60:63]\n" \
  "buffer_load_dwordx4 v1, s[36:39], s72 offen offset:1024 lds\n" \
  "v_mfma_f32_16x16x32_bf16 v[72:75], a[116:119], v[44:47], v[72:75]\n" \
  "v_mfma_f32_16x16x32_bf16 v[64:67], a[196:199], v[40:43], v[64:67]\n" \
  "v_mfma_f32_16x16x32_bf16 v[76:79], a[196:199], v[44:47], v[76:79]\n" \
  "s_waitcnt lgkmcnt(2)\n" \
  "v_mfma_f32_16x16x32_bf16 v[56:59], a[40:43], v[48:51], v[56:59]\n" \
  "ds_read_b128 v[40:43], v10 offset:12288\n" \
  "v_mfma_f32_16x16x32_bf16 v[68:71], a[40:43], v[52:55], v[68:71]\n" \
  "ds_read_b128 v[44:47], v10 offset:32768\n" \
  "v_mfma_f32_16x16x32_bf16 v[60:63], a[120:123], v[48:51], v[60:63]\n" \
  "buffer_load_dwordx4 v1, s[36:39], s72 offen offset:2048 lds\n" \
  "v_mfma_f32_16x16x32_bf16 v[72:75], a[120:123], v[52:55], v[72:75]\n" \
  "v_mfma_f32_16x16x32_bf16 v[64:67], a[200:203], v[48:51], v[64:67]\n" \
  "v_mfma_f32_16x16x32_bf16 v[76:79], a[200:203], v[52:55], v[76:79]\n" \
  "s_waitcnt lgkmcnt(2)\n" \
  "v_mfma_f32_16x16x32_bf16 v[56:59], a[44:47], v[32:35], v[56:59]\n" \
  "ds_read_b128 v[48:51], v10 offset:13312\n" \
  "v_mfma_f32_16x16x32_bf16 v[68:71], a[44:47], v[36:39], v[68:71]\n" \
  "ds_read_b128 v[52:55], v10 offset:33792\n" \
  "v_mfma_f32_16x16x32_bf16 v[60:63], a[124:127], v[32:35], v[60:63]\n" \
  "buffer_load_dwordx4 v1, s[36:39], s72 offen offset:3072 lds\n" \
  "v_mfma_f32_16x16x32_bf16 v[72:75], a[124:127], v[36:39], v[72:75]\n" \
  "v_mfma_f32_16x16x32_bf16 v[64:67], a[204:207], v[32:35], v[64:67]\n" \
  "v_mfma_f32_16x16x32_bf16 v[76:79], a[204:207], v[36:39], v[76:79]\n" \
  "s_waitcnt lgkmcnt(2)\n" \
  "v_mfma_f32_16x16x32_bf16 v[56:59], a[48:51], v[40:43], v[56:59]\n" \
  "ds_read_b128 v[32:35], v10 offset:14336\n" \
  "v_mfma_f32_16x16x32_bf16 v[68:71], a[48:51], v[44:47], v[68:71]\n" \
  "ds_read_b128 v[36:39], v10 offset:34816\n" \
  "v_mfma_f32_16x16x32_bf16 v[60:63], a[128:131], v[40:43], v[60:63]\n" \
  "s_add_u32 m0, s54, 49152\n" \
  "v_mfma_f32_16x16x32_bf16 v[72:75], a[128:131], v[44:47], v[72:75]\n" \
  "buffer_load_dwordx4 v1, s[36:39], s73 offen offset:0 lds\n" \
  "v_mfma_f32_16x16x32_bf16 v[64:67], a[208:211], v[40:43], v[64:67]\n" \
  "v_mfma_f32_16x16x32_bf16 v[76:79], a[208:211], v[44:47], v[76:79]\n" \
  "s_waitcnt lgkmcnt(2)\n" \
  "v_mfma_f32_16x16x32_bf16 v[56:59], a[52:55], v[48:51], v[56:59]\n" \
  "ds_read_b128 v[40:43], v10 offset:15360\n" \
  "v_mfma_f32_16x16x32_bf16 v[68:71], a[52:55], v[52:55], v[68:71]\n" \
  "ds_read_b128 v[44:47], v10 offset:35840\n" \
  "v_mfma_f32_16x16x32_bf16 v[60:63], a[132:135], v[48:51], v[60:63]\n" \
  "buffer_load_dwordx4 v1, s[36:39], s73 offen offset:1024 lds\n" \
  "v_mfma_f32_16x16x32_bf16 v[72:75], a[132:135], v[52:55], v[72:75]\n" \
  "v_mfma_f32_16x16x32_bf16 v[64:67], a[212:215], v[48:51], v[64:67]\n" \
  "v_mfma_f32_16x16x32_bf16 v[76:79], a[212:215], v[52:55], v[76:79]\n" \
  "s_waitcnt lgkmcnt(2)\n" \
  "v_mfma_f32_16x16x32_bf16 v[56:59], a[56:59], v[32:35], v[56:59]\n" \
  "ds_read_b128 v[48:51], v10 offset:16384\n" \
  "v_mfma_f32_16x16x32_bf16 v[68:71], a[56:59], v[36:39], v[68:71]\n" \
  "ds_read_b128 v[52:55], v10 offset:36864\n" \
  "v_mfma_f32_16x16x32_bf16 v[60:63], a[136:139], v[32:35], v[60:63]\n" \
  "s_add_u32 s68, s68, 40960\n" \
  "v_mfma_f32_16x16x32_bf16 v[72:75], a[136:139], v[36:39], v[72:75]\n" \
  "v_mfma_f32_16x16x32_bf16 v[64:67], a[216:219], v[32:35], v[64:67]\n" \
  "v_mfma_f32_16x16x32_bf16 v[76:79], a[216:219], v[36:39], v[76:79]\n" \
  "s_waitcnt lgkmcnt(2)\n" \
  "v_mfma_f32_16x16x32_bf16 v[56:59], a[60:63], v[40:43], v[56:59]\n" \
  "ds_read_b128 v[32:35], v10 offset:17408\n" \
  "v_mfma_f32_16x16x32_bf16 v[68:71], a[60:63], v[44:47], v[68:71]\n" \
  "ds_read_b128 v[36:39], v10 offset:37888\n" \
  "v_mfma_f32_16x16x32_bf16 v[60:63], a[140:143], v[40:43], v[60:63]\n" \
  "v_mov_b32 v11, s52\n" \
  "v_mfma_f32_16x16x32_bf16 v[72:75], a[140:143], v[44:47], v[72:75]\n" \
  "s_waitcnt vmcnt(10)\n" \
  "v_mfma_f32_16x16x32_bf16 v[64:67], a[220:223], v[40:43], v[64:67]\n" \
  "buffer_store_dword v11, v5, s[40:43], 0 offen sc1\n" \
  "v_mfma_f32_16x16x32_bf16 v[76:79], a[220:223], v[44:47], v[76:79]\n" \
  "s_waitcnt lgkmcnt(2)\n" \
  "v_mfma_f32_16x16x32_bf16 v[56:59], a[64:67], v[48:51], v[56:59]\n" \
  "ds_read_b128 v[40:43], v10 offset:18432\n" \
  "v_mfma_f32_16x16x32_bf16 v[68:71], a[64:67], v[52:55], v[68:71]\n" \
  "ds_read_b128 v[44:47], v10 offset:38912\n" \
  "v_mfma_f32_16x16x32_bf16 v[60:63], a[144:147], v[48:51], v[60:63]\n" \
  "v_mfma_f32_16x16x32_bf16 v[72:75], a[144:147], v[52:55], v[72:75]\n" \
  "v_mfma_f32_16x16x32_bf16 v[64:67], a[224:227], v[48:51], v[64:67]\n" \
  "v_mfma_f32_16x16x32_bf16 v[76:79], a[224:227], v[52:55], v[76:79]\n" \
  "s_waitcnt lgkmcnt(2)\n" \
  "v_mfma_f32_16x16x32_bf16 v[56:59], a[68:71], v[32:35], v[56:59]\n" \
  "ds_read_b128 v[48:51], v10 offset:19456\n" \
  "v_mfma_f32_16x16x32_bf16 v[68:71], a[68:71], v[36:39], v[68:71]\n" \
  "ds_read_b128 v[52:55], v10 offset:39936\n" \
  "v_mfma_f32_16x16x32_bf16 v[60:63], a[148:151], v[32:35], v[60:63]\n" \
  "v_readlane_b32 s69, v4, s52\n" \
  "v_mfma_f32_16x16x32_bf16 v[72:75], a[148:151], v[36:39], v[72:75]\n" \
  "v_readlane_b32 s71, v8, s52\n" \
  "v_mfma_f32_16x16x32_bf16 v[64:67], a[228:231], v[32:35], v[64:67]\n" \
  "s_add_u32 s52, s52, 1\n" \
  "v_mfma_f32_16x16x32_bf16 v[76:79], a[228:231], v[36:39], v[76:79]\n" \
  "s_waitcnt lgkmcnt(0)\n" \
  "s_barrier\n" \
  "v_mfma_f32_16x16x32_bf16 v[56:59], a[72:75], v[40:43], v[56:59]\n" \
  "ds_read_b128 v[32:35], v0 offset:0\n" \
  "v_mfma_f32_16x16x32_bf16 v[68:71], a[72:75], v[44:47], v[68:71]\n" \
  "ds_read_b128 v[36:39], v0 offset:20480\n" \
  "v_mfma_f32_16x16x32_bf16 v[60:63], a[152:155], v[40:43], v[60:63]\n" \
  "v_mfma_f32_16x16x32_bf16 v[72:75], a[152:155], v[44:47], v[72:75]\n" \
  "v_mfma_f32_16x16x32_bf16 v[64:67], a[232:235], v[40:43], v[64:67]\n" \
  "v_mfma_f32_16x16x32_bf16 v[76:79], a[232:235], v[44:47], v[76:79]\n" \
  "v_mfma_f32_16x16x32_bf16 v[56:59], a[76:79], v[48:51], v[56:59]\n" \
  "ds_read_b128 v[40:43], v0 offset:1024\n" \
  "v_mfma_f32_16x16x32_bf16 v[68:71], a[76:79], v[52:55], v[68:71]\n" \
  "ds_read_b128 v[44:47], v0 offset:21504\n" \
  "v_mfma_f32_16x16x32_bf16 v[60:63], a[156:159], v[48:51], v[60:63]\n" \
  "s_cmp_eq_u32 s52, s53\n" \
  "v_mfma_f32_16x16x32_bf16 v[72:75], a[156:159], v[52:55], v[72:75]\n" \
  "v_mfma_f32_16x16x32_bf16 v[64:67], a[236:239], v[48:51], v[64:67]\n" \
  "v_mfma_f32_16x16x32_bf16 v[76:79], a[236:239], v[52:55], v[76:79]\n" \
  "s_waitcnt lgkmcnt(2)\n" \
  "v_mfma_f32_16x16x32_bf16 v[100:103], a[0:3], v[32:35], 0\n" \
  "ds_read_b128 v[48:51], v0 offset:2048\n" \
  "v_mfma_f32_16x16x32_bf16 v[112:115], a[0:3], v[36:39], 0\n" \
  "ds_read_b128 v[52:55], v0 offset:22528\n" \
  "v_mfma_f32_16x16x32_bf16 v[104:107], a[80:83], v[32:35], 0\n" \
  "buffer_store_dwordx4 v[12:15], v7, s[40:43], s71 offen sc1\n" \
  "v_mfma_f32_16x16x32_bf16 v[116:119], a[80:83], v[36:39], 0\n" \
  "v_cvt_pk_bf16_f32 v16, v56, v57\n" \
  "v_mfma_f32_16x16x32_bf16 v[108:111], a[160:163], v[32:35], 0\n" \
  "v_cvt_pk_bf16_f32 v17, v58, v59\n" \
  "v_mfma_f32_16x16x32_bf16 v[120:123], a[160:163], v[36:39], 0\n" \
  "v_cvt_pk_bf16_f32 v18, v60, v61\n" \
  "s_waitcnt lgkmcnt(2)\n" \
  "v_mfma_f32_16x16x32_bf16 v[100:103], a[4:7], v[40:43], v[100:103]\n" \
  "ds_read_b128 v[32:35], v0 offset:3072\n" \
  "v_mfma_f32_16x16x32_bf16 v[112:115], a[4:7], v[44:47], v[112:115]\n" \
  "ds_read_b128 v[36:39], v0 offset:23552\n" \
  "v_mfma_f32_16x16x32_bf16 v[104:107], a[84:87], v[40:43], v[104:107]\n" \
  "v_cvt_pk_bf16_f32 v19, v62, v63\n" \
  "v_mfma_f32_16x16x32_bf16 v[116:119], a[84:87], v[44:47], v[116:119]\n" \
  "buffer_store_dwordx4 v[16:19], v2, s[40:43], s69 offen sc1\n" \
  "v_mfma_f32_16x16x32_bf16 v[108:111], a[164:167], v[40:43], v[108:111]\n" \
  "v_cvt_pk_bf16_f32 v20, v68, v69\n" \
  "v_mfma_f32_16x16x32_bf16 v[120:123], a[164:167], v[44:47], v[120:123]\n" \
  "v_cvt_pk_bf16_f32 v21, v70, v71\n" \
  "s_waitcnt lgkmcnt(2)\n" \
  "v_mfma_f32_16x16x32_bf16 v[100:103], a[8:11], v[48:51], v[100:103]\n" \
  "ds_read_b128 v[40:43], v0 offset:4096\n" \
  "v_mfma_f32_16x16x32_bf16 v[112:115], a[8:11], v[52:55], v[112:115]\n" \
  "ds_read_b128 v[44:47], v0 offset:24576\n" \
  "v_mfma_f32_16x16x32_bf16 v[104:107], a[88:91], v[48:51], v[104:107]\n" \
  "v_cvt_pk_bf16_f32 v22, v72, v73\n" \
  "v_mfma_f32_16x16x32_bf16 v[116:119], a[88:91], v[52:55], v[116:119]\n" \
  "v_cvt_pk_bf16_f32 v23, v74, v75\n" \
  "v_mfma_f32_16x16x32_bf16 v[108:111], a[168:171], v[48:51], v[108:111]\n" \
  "buffer_store_dwordx4 v[20:23], v2, s[40:43], s69 offen offset:1024 sc1\n" \
  "v_mfma_f32_16x16x32_bf16 v[120:123], a[168:171], v[52:55], v[120:123]\n" \
  "v_cvt_pk_bf16_f32 v24, v64, v65\n" \
  "s_waitcnt lgkmcnt(2)\n" \
  "v_mfma_f32_16x16x32_bf16 v[100:103], a[12:15], v[32:35], v[100:103]\n" \
  "ds_read_b128 v[48:51], v0 offset:5120\n" \
  "v_mfma_f32_16x16x32_bf16 v[112:115], a[12:15], v[36:39], v[112:115]\n" \
  "ds_read_b128 v[52:55], v0 offset:25600\n" \
  "v_mfma_f32_16x16x32_bf16 v[104:107], a[92:95], v[32:35], v[104:107]\n" \
  "v_cvt_pk_bf16_f32 v25, v66, v67\n" \
  "v_mfma_f32_16x16x32_bf16 v[116:119], a[92:95], v[36:39], v[116:119]\n" \
  "v_cvt_pk_bf16_f32 v26, v76, v77\n" \
  "v_mfma_f32_16x16x32_bf16 v[108:111], a[172:175], v[32:35], v[108:111]\n" \
  "v_cvt_pk_bf16_f32 v27, v78, v79\n" \
  "v_mfma_f32_16x16x32_bf16 v[120:123], a[172:175], v[36:39], v[120:123]\n" \
  "buffer_store_dwordx4 v[24:27], v3, s[40:43], s69 offen sc1\n" \
  "s_waitcnt lgkmcnt(2)\n" \
  "v_mfma_f32_16x16x32_bf16 v[100:103], a[16:19], v[40:43], v[100:103]\n" \
  "ds_read_b128 v[32:35], v0 offset:6144\n" \
  "v_mfma_f32_16x16x32_bf16 v[112:115], a[16:19], v[44:47], v[112:115]\n" \
  "ds_read_b128 v[36:39], v0 offset:26624\n" \
  "v_mfma_f32_16x16x32_bf16 v[104:107], a[96:99], v[40:43], v[104:107]\n" \
  "s_add_u32 s72, s68, 4096\n" \
  "v_mfma_f32_16x16x32_bf16 v[116:119], a[96:99], v[44:47], v[116:119]\n" \
  "s_add_u32 s73, s68, 8192\n" \
  "v_mfma_f32_16x16x32_bf16 v[108:111], a[176:179], v[40:43], v[108:111]\n" \
  "s_add_u32 m0, s54, 81920\n" \
  "v_mfma_f32_16x16x32_bf16 v[120:123], a[176:179], v[44:47], v[120:123]\n" \
  "buffer_load_dwordx4 v1, s[36:39], s68 offen offset:0 lds\n" \
  "s_waitcnt lgkmcnt(2)\n" \
  "v_mfma_f32_16x16x32_bf16 v[100:103], a[20:23], v[48:51], v[100:103]\n" \
  "ds_read_b128 v[40:43], v0 offset:7168\n" \
  "v_mfma_f32_16x16x32_bf16 v[112:115], a[20:23], v[52:55], v[112:115]\n" \
  "ds_read_b128 v[44:47], v0 offset:27648\n" \
  "v_mfma_f32_16x16x32_bf16 v[104:107], a[100:103], v[48:51], v[104:107]\n" \
  "buffer_load_dwordx4 v1, s[36:39], s68 offen offset:1024 lds\n" \
  "v_mfma_f32_16x16x32_bf16 v[116:119], a[100:103], v[52:55], v[116:119]\n" \
  "v_mfma_f32_16x16x32_bf16 v[108:111], a[180:183], v[48:51], v[108:111]\n" \
  "v_mfma_f32_16x16x32_bf16 v[120:123], a[180:183], v[52:55], v[120:123]\n" \
  "s_waitcnt lgkmcnt(2)\n" \
  "v_mfma_f32_16x16x32_bf16 v[100:103], a[24:27], v[32:35], v[100:103]\n" \
  "ds_read_b128 v[48:51], v0 offset:8192\n" \
  "v_mfma_f32_16x16x32_bf16 v[112:115], a[24:27], v[36:39], v[112:115]\n" \
  "ds_read_b128 v[52:55], v0 offset:28672\n" \
  "v_mfma_f32_16x16x32_bf16 v[104:107], a[104:107], v[32:35], v[104:107]\n" \
  "buffer_load_dwordx4 v1, s[36:39], s68 offen offset:2048 lds\n" \
  "v_mfma_f32_16x16x32_bf16 v[116:119], a[104:107], v[36:39], v[116:119]\n" \
  "v_mfma_f32_16x16x32_bf16 v[108:111], a[184:187], v[32:35], v[108:111]\n" \
  "v_mfma_f32_16x16x32_bf16 v[120:123], a[184:187], v[36:39], v[120:123]\n" \
  "s_waitcnt lgkmcnt(2)\n" \
  "v_mfma_f32_16x16x32_bf16 v[100:103], a[28:31], v[40:43], v[100:103]\n" \
  "ds_read_b128 v[32:35], v0 offset:9216\n" \
  "v_mfma_f32_16x16x32_bf16 v[112:115], a[28:31], v[44:47], v[112:115]\n" \
  "ds_read_b128 v[36:39], v0 offset:29696\n" \
  "v_mfma_f32_16x16x32_bf16 v[104:107], a[108:111], v[40:43], v[104:107]\n" \
  "buffer_load_dwordx4 v1, s[36:39], s68 offen offset:3072 lds\n" \
  "v_mfma_f32_16x16x32_bf16 v[116:119], a[108:111], v[44:47], v[116:119]\n" \
  "v_mfma_f32_16x16x32_bf16 v[108:111], a[188:191], v[40:43], v[108:111]\n" \
  "v_mfma_f32_16x16x32_bf16 v[120:123], a[188:191], v[44:47], v[120:123]\n" \
  "s_waitcnt lgkmcnt(2)\n" \
  "v_mfma_f32_16x16x32_bf16 v[100:103], a[32:35], v[48:51], v[100:103]\n" \
  "ds_read_b128 v[40:43], v0 offset:10240\n" \
  "v_mfma_f32_16x16x32_bf16 v[112:115], a[32:35], v[52:55], v[112:115]\n" \
  "ds_read_b128 v[44:47], v0 offset:30720\n" \
  "v_mfma_f32_16x16x32_bf16 v[104:107], a[112:115], v[48:51], v[104:107]\n" \
  "s_add_u32 m0, s54, 86016\n" \
  "v_mfma_f32_16x16x32_bf16 v[116:119], a[112:115], v[52:55], v[116:119]\n" \
  "buffer_load_dwordx4 v1, s[36:39], s72 offen offset:0 lds\n" \
  "v_mfma_f32_16x16x32_bf16 v[108:111], a[192:195], v[48:51], v[108:111]\n" \
  "v_mfma_f32_16x16x32_bf16 v[120:123], a[192:195], v[52:55], v[120:123]\n" \
  "s_waitcnt lgkmcnt(2)\n" \
  "v_mfma_f32_16x16x32_bf16 v[100:103], a[36:39], v[32:35], v[100:103]\n" \
  "ds_read_b128 v[48:51], v0 offset:11264\n" \
  "v_mfma_f32_16x16x32_bf16 v[112:115], a[36:39], v[36:39], v[112:115]\n" \
  "ds_read_b128 v[52:55], v0 offset:31744\n" \
  "v_mfma_f32_16x16x32_bf16 v[104:107], a[116:119], v[32:35], v[104:107]\n" \
  "buffer_load_dwordx4 v1, s[36:39], s72 offen offset:1024 lds\n" \
  "v_mfma_f32_16x16x32_bf16 v[116:119], a[116:119], v[36:39], v[116:119]\n" \
  "v_mfma_f32_16x16x32_bf16 v[108:111], a[196:199], v[32:35], v[108:111]\n" \
  "v_mfma_f32_16x16x32_bf16 v[120:123], a[196:199], v[36:39], v[120:123]\n" \
  "s_waitcnt lgkmcnt(2)\n" \
  "v_mfma_f32_16x16x32_bf16 v[100:103], a[40:43], v[40:43], v[100:103]\n" \
  "ds_read_b128 v[32:35], v0 offset:12288\n" \
  "v_mfma_f32_16x16x32_bf16 v[112:115], a[40:43], v[44:47], v[112:115]\n" \
  "ds_read_b128 v[36:39], v0 offset:32768\n" \
  "v_mfma_f32_16x16x32_bf16 v[104:107], a[120:123], v[40:43], v[104:107]\n" \
  "buffer_load_dwordx4 v1, s[36:39], s72 offen offset:2048 lds\n" \
  "v_mfma_f32_16x16x32_bf16 v[116:119], a[120:123], v[44:47], v[116:119]\n" \
  "v_mfma_f32_16x16x32_bf16 v[108:111], a[200:203], v[40:43], v[108:111]\n" \
  "v_mfma_f32_16x16x32_bf16 v[120:123], a[200:203], v[44:47], v[120:123]\n" \
  "s_waitcnt lgkmcnt(2)\n" \
  "v_mfma_f32_16x16x32_bf16 v[100:103], a[44:47], v[48:51], v[100:103]\n" \
  "ds_read_b128 v[40:43], v0 offset:13312\n" \
  "v_mfma_f32_16x16x32_bf16 v[112:115], a[44:47], v[52:55], v[112:115]\n" \
  "ds_read_b128 v[44:47], v0 offset:33792\n" \
  "v_mfma_f32_16x16x32_bf16 v[104:107], a[124:127], v[48:51], v[104:107]\n" \
  "buffer_load_dwordx4 v1, s[36:39], s72 offen offset:3072 lds\n" \
  "v_mfma_f32_16x16x32_bf16 v[116:119], a[124:127], v[52:55], v[116:119]\n" \
  "v_mfma_f32_16x16x32_bf16 v[108:111], a[204:207], v[48:51], v[108:111]\n" \
  "v_mfma_f32_16x16x32_bf16 v[120:123], a[204:207], v[52:55], v[120:123]\n" \
  "s_waitcnt lgkmcnt(2)\n" \
  "v_mfma_f32_16x16x32_bf16 v[100:103], a[48:51], v[32:35], v[100:103]\n" \
  "ds_read_b128 v[48:51], v0 offset:14336\n" \
  "v_mfma_f32_16x16x32_bf16 v[112:115], a[48:51], v[36:39], v[112:115]\n" \
  "ds_read_b128 v[52:55], v0 offset:34816\n" \
  "v_mfma_f32_16x16x32_bf16 v[104:107], a[128:131], v[32:35], v[104:107]\n" \
  "s_add_u32 m0, s54, 90112\n" \
  "v_mfma_f32_16x16x32_bf16 v[116:119], a[128:131], v[36:39], v[116:119]\n" \
  "buffer_load_dwordx4 v1, s[36:39], s73 offen offset:0 lds\n" \
  "v_mfma_f32_16x16x32_bf16 v[108:111], a[208:211], v[32:35], v[108:111]\n" \
  "v_mfma_f32_16x16x32_bf16 v[120:123], a[208:211], v[36:39], v[120:123]\n" \
  "s_waitcnt lgkmcnt(2)\n" \
  "v_mfma_f32_16x16x32_bf16 v[100:103], a[52:55], v[40:43], v[100:103]\n" \
  "ds_read_b128 v[32:35], v0 offset:15360\n" \
  "v_mfma_f32_16x16x32_bf16 v[112:115], a[52:55], v[44:47], v[112:115]\n" \
  "ds_read_b128 v[36:39], v0 offset:35840\n" \
  "v_mfma_f32_16x16x32_bf16 v[104:107], a[132:135], v[40:43], v[104:107]\n" \
  "buffer_load_dwordx4 v1, s[36:39], s73 offen offset:1024 lds\n" \
  "v_mfma_f32_16x16x32_bf16 v[116:119], a[132:135], v[44:47], v[116:119]\n" \
  "v_mfma_f32_16x16x32_bf16 v[108:111], a[212:215], v[40:43], v[108:111]\n" \
  "v_mfma_f32_16x16x32_bf16 v[120:123], a[212:215], v[44:47], v[120:123]\n" \
  "s_waitcnt lgkmcnt(2)\n" \
  "v_mfma_f32_16x16x32_bf16 v[100:103], a[56:59], v[48:51], v[100:103]\n" \
  "ds_read_b128 v[40:43], v0 offset:16384\n" \
  "v_mfma_f32_16x16x32_bf16 v[112:115], a[56:59], v[52:55], v[112:115]\n" \
  "ds_read_b128 v[44:47], v0 offset:36864\n" \
  "v_mfma_f32_16x16x32_bf16 v[104:107], a[136:139], v[48:51], v[104:107]\n" \
  "s_add_u32 s68, s68, 40960\n" \
  "v_mfma_f32_16x16x32_bf16 v[116:119], a[136:139], v[52:55], v[116:119]\n" \
  "v_mfma_f32_16x16x32_bf16 v[108:111], a[216:219], v[48:51], v[108:111]\n" \
  "v_mfma_f32_16x16x32_bf16 v[120:123], a[216:219], v[52:55], v[120:123]\n" \
  "s_waitcnt lgkmcnt(2)\n" \
  "v_mfma_f32_16x16x32_bf16 v[100:103], a[60:63], v[32:35], v[100:103]\n" \
  "ds_read_b128 v[48:51], v0 offset:17408\n" \
  "v_mfma_f32_16x16x32_bf16 v[112:115], a[60:63], v[36:39], v[112:115]\n" \
  "ds_read_b128 v[52:55], v0 offset:37888\n" \
  "v_mfma_f32_16x16x32_bf16 v[104:107], a[140:143], v[32:35], v[104:107]\n" \
  "v_mov_b32 v11, s52\n" \
  "v_mfma_f32_16x16x32_bf16 v[116:119], a[140:143], v[36:39], v[116:119]\n" \
  "s_waitcnt vmcnt(10)\n" \
  "v_mfma_f32_16x16x32_bf16 v[108:111], a[220:223], v[32:35], v[108:111]\n" \
  "buffer_store_dword v11, v5, s[40:43], 0 offen sc1\n" \
  "v_mfma_f32_16x16x32_bf16 v[120:123], a[220:223], v[36:39], v[120:123]\n" \
  "s_waitcnt lgkmcnt(2)\n" \
  "v_mfma_f32_16x16x32_bf16 v[100:103], a[64:67], v[40:43], v[100:103]\n" \
  "ds_read_b128 v[32:35], v0 offset:18432\n" \
  "v_mfma_f32_16x16x32_bf16 v[112:115], a[64:67], v[44:47], v[112:115]\n" \
  "ds_read_b128 v[36:39], v0 offset:38912\n" \
  "v_mfma_f32_16x16x32_bf16 v[104:107], a[144:147], v[40:43], v[104:107]\n" \
  "v_mfma_f32_16x16x32_bf16 v[116:119], a[144:147], v[44:47], v[116:119]\n" \
  "v_mfma_f32_16x16x32_bf16 v[108:111], a[224:227], v[40:43], v[108:111]\n" \
  "v_mfma_f32_16x16x32_bf16 v[120:123], a[224:227], v[44:47], v[120:123]\n" \
  "s_waitcnt lgkmcnt(2)\n" \
  "v_mfma_f32_16x16x32_bf16 v[100:103], a[68:71], v[48:51], v[100:103]\n" \
  "ds_read_b128 v[40:43], v0 offset:19456\n" \
  "v_mfma_f32_16x16x32_bf16 v[112:115], a[68:71], v[52:55], v[112:115]\n" \
  "ds_read_b128 v[44:47], v0 offset:39936\n" \
  "v_mfma_f32_16x16x32_bf16 v[104:107], a[148:151], v[48:51], v[104:107]\n" \
  "v_readlane_b32 s69, v4, s52\n" \
  "v_mfma_f32_16x16x32_bf16 v[116:119], a[148:151], v[52:55], v[116:119]\n" \
  "v_readlane_b32 s71, v8, s52\n" \
  "v_mfma_f32_16x16x32_bf16 v[108:111], a[228:231], v[48:51], v[108:111]\n" \
  "s_add_u32 s52, s52, 1\n" \
  "v_mfma_f32_16x16x32_bf16 v[120:123], a[228:231], v[52:55], v[120:123]\n" \
  "s_waitcnt lgkmcnt(0)\n" \
  "s_barrier\n" \
  "v_mfma_f32_16x16x32_bf16 v[100:103], a[72:75], v[32:35], v[100:103]\n" \
  "ds_read_b128 v[48:51], v9 offset:0\n" \
  "v_mfma_f32_16x16x32_bf16 v[112:115], a[72:75], v[36:39], v[112:115]\n" \
  "ds_read_b128 v[52:55], v9 offset:20480\n" \
  "v_mfma_f32_16x16x32_bf16 v[104:107], a[152:155], v[32:35], v[104:107]\n" \
  "v_mfma_f32_16x16x32_bf16 v[116:119], a[152:155], v[36:39], v[116:119]\n" \
  "v_mfma_f32_16x16x32_bf16 v[108:111], a[232:235], v[32:35], v[108:111]\n" \
  "v_mfma_f32_16x16x32_bf16 v[120:123], a[232:235], v[36:39], v[120:123]\n" \
  "v_mfma_f32_16x16x32_bf16 v[100:103], a[76:79], v[40:43], v[100:103]\n" \
  "ds_read_b128 v[32:35], v9 offset:1024\n" \
  "v_mfma_f32_16x16x32_bf16 v[112:115], a[76:79], v[44:47], v[112:115]\n" \
  "ds_read_b128 v[36:39], v9 offset:21504\n" \
  "v_mfma_f32_16x16x32_bf16 v[104:107], a[156:159], v[40:43], v[104:107]\n" \
  "s_cmp_eq_u32 s52, s53\n" \
  "v_mfma_f32_16x16x32_bf16 v[116:119], a[156:159], v[44:47], v[116:119]\n" \
  "v_mfma_f32_16x16x32_bf16 v[108:111], a[236:239], v[40:43], v[108:111]\n" \
  "v_mfma_f32_16x16x32_bf16 v[120:123], a[236:239], v[44:47], v[120:123]\n" \
  "s_cbranch_scc1 LPE_%=\n" \
  "s_waitcnt lgkmcnt(2)\n" \
  "v_mfma_f32_16x16x32_bf16 v[56:59], a[0:3], v[48:51], 0\n" \
  "ds_read_b128 v[40:43], v9 offset:2048\n" \
  "v_mfma_f32_16x16x32_bf16 v[68:71], a[0:3], v[52:55], 0\n" \
  "ds_read_b128 v[44:47], v9 offset:22528\n" \
  "v_mfma_f32_16x16x32_bf16 v[60:63], a[80:83], v[48:51], 0\n" \
  "buffer_store_dwordx4 v[12:15], v7, s[40:43], s71 offen sc1\n" \
  "v_mfma_f32_16x16x32_bf16 v[72:75], a[80:83], v[52:55], 0\n" \
  "v_cvt_pk_bf16_f32 v16, v100, v101\n" \
  "v_mfma_f32_16x16x32_bf16 v[64:67], a[160:163], v[48:51], 0\n" \
  "v_cvt_pk_bf16_f32 v17, v102, v103\n" \
  "v_mfma_f32_16x16x32_bf16 v[76:79], a[160:163], v[52:55], 0\n" \
  "v_cvt_pk_bf16_f32 v18, v104, v105\n" \
  "s_waitcnt lgkmcnt(2)\n" \
  "v_mfma_f32_16x16x32_bf16 v[56:59], a[4:7], v[32:35], v[56:59]\n" \
  "ds_read_b128 v[48:51], v9 offset:3072\n" \
  "v_mfma_f32_16x16x32_bf16 v[68:71], a[4:7], v[36:39], v[68:71]\n" \
  "ds_read_b128 v[52:55], v9 offset:23552\n" \
  "v_mfma_f32_16x16x32_bf16 v[60:63], a[84:87], v[32:35], v[60:63]\n" \
  "v_cvt_pk_bf16_f32 v19, v106, v107\n" \
  "v_mfma_f32_16x16x32_bf16 v[72:75], a[84:87], v[36:39], v[72:75]\n" \
  "buffer_store_dwordx4 v[16:19], v2, s[40:43], s69 offen sc1\n" \
  "v_mfma_f32_16x16x32_bf16 v[64:67], a[164:167], v[32:35], v[64:67]\n" \
  "v_cvt_pk_bf16_f32 v20, v112, v113\n" \
  "v_mfma_f32_16x16x32_bf16 v[76:79], a[164:167], v[36:39], v[76:79]\n" \
  "v_cvt_pk_bf16_f32 v21, v114, v115\n" \
  "s_waitcnt lgkmcnt(2)\n" \
  "v_mfma_f32_16x16x32_bf16 v[56:59], a[8:11], v[40:43], v[56:59]\n" \
  "ds_read_b128 v[32:35], v9 offset:4096\n" \
  "v_mfma_f32_16x16x32_bf16 v[68:71], a[8:11], v[44:47], v[68:71]\n" \
  "ds_read_b128 v[36:39], v9 offset:24576\n" \
  "v_mfma_f32_16x16x32_bf16 v[60:63], a[88:91], v[40:43], v[60:63]\n" \
  "v_cvt_pk_bf16_f32 v22, v116, v117\n" \
  "v_mfma_f32_16x16x32_bf16 v[72:75], a[88:91], v[44:47], v[72:75]\n" \
  "v_cvt_pk_bf16_f32 v23, v118, v119\n" \
  "v_mfma_f32_16x16x32_bf16 v[64:67], a[168:171], v[40:43], v[64:67]\n" \
  "buffer_store_dwordx4 v[20:23], v2, s[40:43], s69 offen offset:1024 sc1\n" \
  "v_mfma_f32_16x16x32_bf16 v[76:79], a[168:171], v[44:47], v[76:79]\n" \
  "v_cvt_pk_bf16_f32 v24, v108, v109\n" \
  "s_waitcnt lgkmcnt(2)\n" \
  "v_mfma_f32_16x16x32_bf16 v[56:59], a[12:15], v[48:51], v[56:59]\n" \
  "ds_read_b128 v[40:43], v9 offset:5120\n" \
  "v_mfma_f32_16x16x32_bf16 v[68:71], a[12:15], v[52:55], v[68:71]\n" \
  "ds_read_b128 v[44:47], v9 offset:25600\n" \
  "v_mfma_f32_16x16x32_bf16 v[60:63], a[92:95], v[48:51], v[60:63]\n" \
  "v_cvt_pk_bf16_f32 v25, v110, v111\n" \
  "v_mfma_f32_16x16x32_bf16 v[72:75], a[92:95], v[52:55], v[72:75]\n" \
  "v_cvt_pk_bf16_f32 v26, v120, v121\n" \
  "v_mfma_f32_16x16x32_bf16 v[64:67], a[172:175], v[48:51], v[64:67]\n" \
  "v_cvt_pk_bf16_f32 v27, v122, v123\n" \
  "v_mfma_f32_16x16x32_bf16 v[76:79], a[172:175], v[52:55], v[76:79]\n" \
  "buffer_store_dwordx4 v[24:27], v3, s[40:43], s69 offen sc1\n" \
  "s_waitcnt lgkmcnt(2)\n" \
  "v_mfma_f32_16x16x32_bf16 v[56:59], a[16:19], v[32:35], v[56:59]\n" \
  "ds_read_b128 v[48:51], v9 offset:6144\n" \
  "v_mfma_f32_16x16x32_bf16 v[68:71], a[16:19], v[36:39], v[68:71]\n" \
  "ds_read_b128 v[52:55], v9 offset:26624\n" \
  "v_mfma_f32_16x16x32_bf16 v[60:63], a[96:99], v[32:35], v[60:63]\n" \
  "s_add_u32 s72, s68, 4096\n" \
  "v_mfma_f32_16x16x32_bf16 v[72:75], a[96:99], v[36:39], v[72:75]\n" \
  "s_add_u32 s73, s68, 8192\n" \
  "v_mfma_f32_16x16x32_bf16 v[64:67], a[176:179], v[32:35], v[64:67]\n" \
  "s_add_u32 m0, s54, 0\n" \
  "v_mfma_f32_16x16x32_bf16 v[76:79], a[176:179], v[36:39], v[76:79]\n" \
  "buffer_load_dwordx4 v1, s[36:39], s68 offen offset:0 lds\n" \
  "s_waitcnt lgkmcnt(2)\n" \
  "v_mfma_f32_16x16x32_bf16 v[56:59], a[20:23], v[40:43], v[56:59]\n" \
  "ds_read_b128 v[32:35], v9 offset:7168\n" \
  "v_mfma_f32_16x16x32_bf16 v[68:71], a[20:23], v[44:47], v[68:71]\n" \
  "ds_read_b128 v[36:39], v9 offset:27648\n" \
  "v_mfma_f32_16x16x32_bf16 v[60:63], a[100:103], v[40:43], v[60:63]\n" \
  "buffer_load_dwordx4 v1, s[36:39], s68 offen offset:1024 lds\n" \
  "v_mfma_f32_16x16x32_bf16 v[72:75], a[100:103], v[44:47], v[72:75]\n" \
  "v_mfma_f32_16x16x32_bf16 v[64:67], a[180:183], v[40:43], v[64:67]\n" \
  "v_mfma_f32_16x16x32_bf16 v[76:79], a[180:183], v[44:47], v[76:79]\n" \
  "s_waitcnt lgkmcnt(2)\n" \
  "v_mfma_f32_16x16x32_bf16 v[56:59], a[24:27], v[48:51], v[56:59]\n" \
  "ds_read_b128 v[40:43], v9 offset:8192\n" \
  "v_mfma_f32_16x16x32_bf16 v[68:71], a[24:27], v[52:55], v[68:71]\n" \
  "ds_read_b128 v[44:47], v9 offset:28672\n" \
  "v_mfma_f32_16x16x32_bf16 v[60:63], a[104:107], v[48:51], v[60:63]\n" \
  "buffer_load_dwordx4 v1, s[36:39], s68 offen offset:2048 lds\n" \
  "v_mfma_f32_16x16x32_bf16 v[72:75], a[104:107], v[52:55], v[72:75]\n" \
  "v_mfma_f32_16x16x32_bf16 v[64:67], a[184:187], v[48:51], v[64:67]\n" \
  "v_mfma_f32_16x16x32_bf16 v[76:79], a[184:187], v[52:55], v[76:79]\n" \
  "s_waitcnt lgkmcnt(2)\n" \
  "v_mfma_f32_16x16x32_bf16 v[56:59], a[28:31], v[32:35], v[56:59]\n" \
  "ds_read_b128 v[48:51], v9 offset:9216\n" \
  "v_mfma_f32_16x16x32_bf16 v[68:71], a[28:31], v[36:39], v[68:71]\n" \
  "ds_read_b128 v[52:55], v9 offset:29696\n" \
  "v_mfma_f32_16x16x32_bf16 v[60:63], a[108:111], v[32:35], v[60:63]\n" \
  "buffer_load_dwordx4 v1, s[36:39], s68 offen offset:3072 lds\n" \
  "v_mfma_f32_16x16x32_bf16 v[72:75], a[108:111], v[36:39], v[72:75]\n" \
  "v_mfma_f32_16x16x32_bf16 v[64:67], a[188:191], v[32:35], v[64:67]\n" \
  "v_mfma_f32_16x16x32_bf16 v[76:79], a[188:191], v[36:39], v[76:79]\n" \
  "s_waitcnt lgkmcnt(2)\n" \
  "v_mfma_f32_16x16x32_bf16 v[56:59], a[32:35], v[40:43], v[56:59]\n" \
  "ds_read_b128 v[32:35], v9 offset:10240\n" \
  "v_mfma_f32_16x16x32_bf16 v[68:71], a[32:35], v[44:47], v[68:71]\n" \
  "ds_read_b128 v[36:39], v9 offset:30720\n" \
  "v_mfma_f32_16x16x32_bf16 v[60:63], a[112:115], v[40:43], v[60:63]\n" \
  "s_add_u32 m0, s54, 4096\n" \
  "v_mfma_f32_16x16x32_bf16 v[72:75], a[112:115], v[44:47], v[72:75]\n" \
  "buffer_load_dwordx4 v1, s[36:39], s72 offen offset:0 lds\n" \
  "v_mfma_f32_16x16x32_bf16 v[64:67], a[192:195], v[40:43], v[64:67]\n" \
  "v_mfma_f32_16x16x32_bf16 v[76:79], a[192:195], v[44:47], v[76:79]\n" \
  "s_waitcnt lgkmcnt(2)\n" \
  "v_mfma_f32_16x16x32_bf16 v[56:59], a[36:39], v[48:51], v[56:59]\n" \
  "ds_read_b128 v[40:43], v9 offset:11264\n" \
  "v_mfma_f32_16x16x32_bf16 v[68:71], a[36:39], v[52:55], v[68:71]\n" \
  "ds_read_b128 v[44:47], v9 offset:31744\n" \
  "v_mfma_f32_16x16x32_bf16 v[60:63], a[116:119], v[48:51], v[60:63]\n" \
  "buffer_load_dwordx4 v1, s[36:39], s72 offen offset:1024 lds\n" \
  "v_mfma_f32_16x16x32_bf16 v[72:75], a[116:119], v[52:55], v[72:75]\n" \
  "v_mfma_f32_16x16x32_bf16 v[64:67], a[196:199], v[48:51], v[64:67]\n" \
  "v_mfma_f32_16x16x32_bf16 v[76:79], a[196:199], v[52:55], v[76:79]\n" \
  "s_waitcnt lgkmcnt(2)\n" \
  "v_mfma_f32_16x16x32_bf16 v[56:59], a[40:43], v[32:35], v[56:59]\n" \
  "ds_read_b128 v[48:51], v9 offset:12288\n" \
  "v_mfma_f32_16x16x32_bf16 v[68:71], a[40:43], v[36:39], v[68:71]\n" \
  "ds_read_b128 v[52:55], v9 offset:32768\n" \
  "v_mfma_f32_16x16x32_bf16 v[60:63], a[120:123], v[32:35], v[60:63]\n" \
  "buffer_load_dwordx4 v1, s[36:39], s72 offen offset:2048 lds\n" \
  "v_mfma_f32_16x16x32_bf16 v[72:75], a[120:123], v[36:39], v[72:75]\n" \
  "v_mfma_f32_16x16x32_bf16 v[64:67], a[200:203], v[32:35], v[64:67]\n" \
  "v_mfma_f32_16x16x32_bf16 v[76:79], a[200:203], v[36:39], v[76:79]\n" \
  "s_waitcnt lgkmcnt(2)\n" \
  "v_mfma_f32_16x16x32_bf16 v[56:59], a[44:47], v[40:43], v[56:59]\n" \
  "ds_read_b128 v[32:35], v9 offset:13312\n" \
  "v_mfma_f32_16x16x32_bf16 v[68:71], a[44:47], v[44:47], v[68:71]\n" \
  "ds_read_b128 v[36:39], v9 offset:33792\n" \
  "v_mfma_f32_16x16x32_bf16 v[60:63], a[124:127], v[40:43], v[60:63]\n" \
  "buffer_load_dwordx4 v1, s[36:39], s72 offen offset:3072 lds\n" \
  "v_mfma_f32_16x16x32_bf16 v[72:75], a[124:127], v[44:47], v[72:75]\n" \
  "v_mfma_f32_16x16x32_bf16 v[64:67], a[204:207], v[40:43], v[64:67]\n" \
  "v_mfma_f32_16x16x32_bf16 v[76:79], a[204:207], v[44:47], v[76:79]\n" \
  "s_waitcnt lgkmcnt(2)\n" \
  "v_mfma_f32_16x16x32_bf16 v[56:59], a[48:51], v[48:51], v[56:59]\n" \
  "ds_read_b128 v[40:43], v9 offset:14336\n" \
  "v_mfma_f32_16x16x32_bf16 v[68:71], a[48:51], v[52:55], v[68:71]\n" \
  "ds_read_b128 v[44:47], v9 offset:34816\n" \
  "v_mfma_f32_16x16x32_bf16 v[60:63], a[128:131], v[48:51], v[60:63]\n" \
  "s_add_u32 m0, s54, 8192\n" \
  "v_mfma_f32_16x16x32_bf16 v[72:75], a[128:131], v[52:55], v[72:75]\n" \
  "buffer_load_dwordx4 v1, s[36:39], s73 offen offset:0 lds\n" \
  "v_mfma_f32_16x16x32_bf16 v[64:67], a[208:211], v[48:51], v[64:67]\n" \
  "v_mfma_f32_16x16x32_bf16 v[76:79], a[208:211], v[52:55], v[76:79]\n" \
  "s_waitcnt lgkmcnt(2)\n" \
  "v_mfma_f32_16x16x32_bf16 v[56:59], a[52:55], v[32:35], v[56:59]\n" \
  "ds_read_b128 v[48:51], v9 offset:15360\n" \
  "v_mfma_f32_16x16x32_bf16 v[68:71], a[52:55], v[36:39], v[68:71]\n" \
  "ds_read_b128 v[52:55], v9 offset:35840\n" \
  "v_mfma_f32_16x16x32_bf16 v[60:63], a[132:135], v[32:35], v[60:63]\n" \
  "buffer_load_dwordx4 v1, s[36:39], s73 offen offset:1024 lds\n" \
  "v_mfma_f32_16x16x32_bf16 v[72:75], a[132:135], v[36:39], v[72:75]\n" \
  "v_mfma_f32_16x16x32_bf16 v[64:67], a[212:215], v[32:35], v[64:67]\n" \
  "v_mfma_f32_16x16x32_bf16 v[76:79], a[212:215], v[36:39], v[76:79]\n" \
  "s_waitcnt lgkmcnt(2)\n" \
  "v_mfma_f32_16x16x32_bf16 v[56:59], a[56:59], v[40:43], v[56:59]\n" \
  "ds_read_b128 v[32:35], v9 offset:16384\n" \
  "v_mfma_f32_16x16x32_bf16 v[68:71], a[56:59], v[44:47], v[68:71]\n" \
  "ds_read_b128 v[36:39], v9 offset:36864\n" \
  "v_mfma_f32_16x16x32_bf16 v[60:63], a[136:139], v[40:43], v[60:63]\n" \
  "s_add_u32 s68, s68, 40960\n" \
  "v_mfma_f32_16x16x32_bf16 v[72:75], a[136:139], v[44:47], v[72:75]\n" \
  "v_mfma_f32_16x16x32_bf16 v[64:67], a[216:219], v[40:43], v[64:67]\n" \
  "v_mfma_f32_16x16x32_bf16 v[76:79], a[216:219], v[44:47], v[76:79]\n" \
  "s_waitcnt lgkmcnt(2)\n" \
  "v_mfma_f32_16x16x32_bf16 v[56:59], a[60:63], v[48:51], v[56:59]\n" \
  "ds_read_b128 v[40:43], v9 offset:17408\n" \
  "v_mfma_f32_16x16x32_bf16 v[68:71], a[60:63], v[52:55], v[68:71]\n" \
  "ds_read_b128 v[44:47], v9 offset:37888\n" \
  "v_mfma_f32_16x16x32_bf16 v[60:63], a[140:143], v[48:51], v[60:63]\n" \
  "v_mov_b32 v11, s52\n" \
  "v_mfma_f32_16x16x32_bf16 v[72:75], a[140:143], v[52:55], v[72:75]\n" \
  "s_waitcnt vmcnt(10)\n" \
  "v_mfma_f32_16x16x32_bf16 v[64:67], a[220:223], v[48:51], v[64:67]\n" \
  "buffer_store_dword v11, v5, s[40:43], 0 offen sc1\n" \
  "v_mfma_f32_16x16x32_bf16 v[76:79], a[220:223], v[52:55], v[76:79]\n" \
  "s_waitcnt lgkmcnt(2)\n" \
  "v_mfma_f32_16x16x32_bf16 v[56:59], a[64:67], v[32:35], v[56:59]\n" \
  "ds_read_b128 v[48:51], v9 offset:18432\n" \
  "v_mfma_f32_16x16x32_bf16 v[68:71], a[64:67], v[36:39], v[68:71]\n" \
  "ds_read_b128 v[52:55], v9 offset:38912\n" \
  "v_mfma_f32_16x16x32_bf16 v[60:63], a[144:147], v[32:35], v[60:63]\n" \
  "v_mfma_f32_16x16x32_bf16 v[72:75], a[144:147], v[36:39], v[72:75]\n" \
  "v_mfma_f32_16x16x32_bf16 v[64:67], a[224:227], v[32:35], v[64:67]\n" \
  "v_mfma_f32_16x16x32_bf16 v[76:79], a[224:227], v[36:39], v[76:79]\n" \
  "s_waitcnt lgkmcnt(2)\n" \
  "v_mfma_f32_16x16x32_bf16 v[56:59], a[68:71], v[40:43], v[56:59]\n" \
  "ds_read_b128 v[32:35], v9 offset:19456\n" \
  "v_mfma_f32_16x16x32_bf16 v[68:71], a[68:71], v[44:47], v[68:71]\n" \
  "ds_read_b128 v[36:39], v9 offset:39936\n" \
  "v_mfma_f32_16x16x32_bf16 v[60:63], a[148:151], v[40:43], v[60:63]\n" \
  "v_readlane_b32 s69, v4, s52\n" \
  "v_mfma_f32_16x16x32_bf16 v[72:75], a[148:151], v[44:47], v[72:75]\n" \
  "v_readlane_b32 s71, v8, s52\n" \
  "v_mfma_f32_16x16x32_bf16 v[64:67], a[228:231], v[40:43], v[64:67]\n" \
  "s_add_u32 s52, s52, 1\n" \
  "v_mfma_f32_16x16x32_bf16 v[76:79], a[228:231], v[44:47], v[76:79]\n" \
  "s_waitcnt lgkmcnt(0)\n" \
  "s_barrier\n" \
  "v_mfma_f32_16x16x32_bf16 v[56:59], a[72:75], v[48:51], v[56:59]\n" \
  "ds_read_b128 v[40:43], v10 offset:0\n" \
  "v_mfma_f32_16x16x32_bf16 v[68:71], a[72:75], v[52:55], v[68:71]\n" \
  "ds_read_b128 v[44:47], v10 offset:20480\n" \
  "v_mfma_f32_16x16x32_bf16 v[60:63], a[152:155], v[48:51], v[60:63]\n" \
  "v_mfma_f32_16x16x32_bf16 v[72:75], a[152:155], v[52:55], v[72:75]\n" \
  "v_mfma_f32_16x16x32_bf16 v[64:67], a[232:235], v[48:51], v[64:67]\n" \
  "v_mfma_f32_16x16x32_bf16 v[76:79], a[232:235], v[52:55], v[76:79]\n" \
  "v_mfma_f32_16x16x32_bf16 v[56:59], a[76:79], v[32:35], v[56:59]\n" \
  "ds_read_b128 v[48:51], v10 offset:1024\n" \
  "v_mfma_f32_16x16x32_bf16 v[68:71], a[76:79], v[36:39], v[68:71]\n" \
  "ds_read_b128 v[52:55], v10 offset:21504\n" \
  "v_mfma_f32_16x16x32_bf16 v[60:63], a[156:159], v[32:35], v[60:63]\n" \
  "s_cmp_eq_u32 s52, s53\n" \
  "v_mfma_f32_16x16x32_bf16 v[72:75], a[156:159], v[36:39], v[72:75]\n" \
  "v_mfma_f32_16x16x32_bf16 v[64:67], a[236:239], v[32:35], v[64:67]\n" \
  "v_mfma_f32_16x16x32_bf16 v[76:79], a[236:239], v[36:39], v[76:79]\n" \
  "s_waitcnt lgkmcnt(2)\n" \
  "v_mfma_f32_16x16x32_bf16 v[100:103], a[0:3], v[40:43], 0\n" \
  "ds_read_b128 v[32:35], v10 offset:2048\n" \
  "v_mfma_f32_16x16x32_bf16 v[112:115], a[0:3], v[44:47], 0\n" \
  "ds_read_b128 v[36:39], v10 offset:22528\n" \
  "v_mfma_f32_16x16x32_bf16 v[104:107], a[80:83], v[40:43], 0\n" \
  "buffer_store_dwordx4 v[12:15], v7, s[40:43], s71 offen sc1\n" \
  "v_mfma_f32_16x16x32_bf16 v[116:119], a[80:83], v[44:47], 0\n" \
  "v_cvt_pk_bf16_f32 v16, v56, v57\n" \
  "v_mfma_f32_16x16x32_bf16 v[108:111], a[160:163], v[40:43], 0\n" \
  "v_cvt_pk_bf16_f32 v17, v58, v59\n" \
  "v_mfma_f32_16x16x32_bf16 v[120:123], a[160:163], v[44:47], 0\n" \
  "v_cvt_pk_bf16_f32 v18, v60, v61\n" \
  "s_waitcnt lgkmcnt(2)\n" \
  "v_mfma_f32_16x16x32_bf16 v[100:103], a[4:7], v[48:51], v[100:103]\n" \
  "ds_read_b128 v[40:43], v10 offset:3072\n" \
  "v_mfma_f32_16x16x32_bf16 v[112:115], a[4:7], v[52:55], v[112:115]\n" \
  "ds_read_b128 v[44:47], v10 offset:23552\n" \
  "v_mfma_f32_16x16x32_bf16 v[104:107], a[84:87], v[48:51], v[104:107]\n" \
  "v_cvt_pk_bf16_f32 v19, v62, v63\n" \
  "v_mfma_f32_16x16x32_bf16 v[116:119], a[84:87], v[52:55], v[116:119]\n" \
  "buffer_store_dwordx4 v[16:19], v2, s[40:43], s69 offen sc1\n" \
  "v_mfma_f32_16x16x32_bf16 v[108:111], a[164:167], v[48:51], v[108:111]\n" \
  "v_cvt_pk_bf16_f32 v20, v68, v69\n" \
  "v_mfma_f32_16x16x32_bf16 v[120:123], a[164:167], v[52:55], v[120:123]\n" \
  "v_cvt_pk_bf16_f32 v21, v70, v71\n" \
  "s_waitcnt lgkmcnt(2)\n" \
  "v_mfma_f32_16x16x32_bf16 v[100:103], a[8:11], v[32:35], v[100:103]\n" \
  "ds_read_b128 v[48:51], v10 offset:4096\n" \
  "v_mfma_f32_16x16x32_bf16 v[112:115], a[8:11], v[36:39], v[112:115]\n" \
  "ds_read_b128 v[52:55], v10 offset:24576\n" \
  "v_mfma_f32_16x16x32_bf16 v[104:107], a[88:91], v[32:35], v[104:107]\n" \
  "v_cvt_pk_bf16_f32 v22, v72, v73\n" \
  "v_mfma_f32_16x16x32_bf16 v[116:119], a[88:91], v[36:39], v[116:119]\n" \
  "v_cvt_pk_bf16_f32 v23, v74, v75\n" \
  "v_mfma_f32_16x16x32_bf16 v[108:111], a[168:171], v[32:35], v[108:111]\n" \
  "buffer_store_dwordx4 v[20:23], v2, s[40:43], s69 offen offset:1024 sc1\n" \
  "v_mfma_f32_16x16x32_bf16 v[120:123], a[168:171], v[36:39], v[120:123]\n" \
  "v_cvt_pk_bf16_f32 v24, v64, v65\n" \
  "s_waitcnt lgkmcnt(2)\n" \
  "v_mfma_f32_16x16x32_bf16 v[100:103], a[12:15], v[40:43], v[100:103]\n" \
  "ds_read_b128 v[32:35], v10 offset:5120\n" \
  "v_mfma_f32_16x16x32_bf16 v[112:115], a[12:15], v[44:47], v[112:115]\n" \
  "ds_read_b128 v[36:39], v10 offset:25600\n" \
  "v_mfma_f32_16x16x32_bf16 v[104:107], a[92:95], v[40:43], v[104:107]\n" \
  "v_cvt_pk_bf16_f32 v25, v66, v67\n" \
  "v_mfma_f32_16x16x32_bf16 v[116:119], a[92:95], v[44:47], v[116:119]\n" \
  "v_cvt_pk_bf16_f32 v26, v76, v77\n" \
  "v_mfma_f32_16x16x32_bf16 v[108:111], a[172:175], v[40:43], v[108:111]\n" \
  "v_cvt_pk_bf16_f32 v27, v78, v79\n" \
  "v_mfma_f32_16x16x32_bf16 v[120:123], a[172:175], v[44:47], v[120:123]\n" \
  "buffer_store_dwordx4 v[24:27], v3, s[40:43], s69 offen sc1\n" \
  "s_waitcnt lgkmcnt(2)\n" \
  "v_mfma_f32_16x16x32_bf16 v[100:103], a[16:19], v[48:51], v[100:103]\n" \
  "ds_read_b128 v[40:43], v10 offset:6144\n" \
  "v_mfma_f32_16x16x32_bf16 v[112:115], a[16:19], v[52:55], v[112:115]\n" \
  "ds_read_b128 v[44:47], v10 offset:26624\n" \
  "v_mfma_f32_16x16x32_bf16 v[104:107], a[96:99], v[48:51], v[104:107]\n" \
  "s_add_u32 s72, s68, 4096\n" \
  "v_mfma_f32_16x16x32_bf16 v[116:119], a[96:99], v[52:55], v[116:119]\n" \
  "s_add_u32 s73, s68, 8192\n" \
  "v_mfma_f32_16x16x32_bf16 v[108:111], a[176:179], v[48:51], v[108:111]\n" \
  "s_add_u32 m0, s54, 40960\n" \
  "v_mfma_f32_16x16x32_bf16 v[120:123], a[176:179], v[52:55], v[120:123]\n" \
  "buffer_load_dwordx4 v1, s[36:39], s68 offen offset:0 lds\n" \
  "s_waitcnt lgkmcnt(2)\n" \
  "v_mfma_f32_16x16x32_bf16 v[100:103], a[20:23], v[32:35], v[100:103]\n" \
  "ds_read_b128 v[48:51], v10 offset:7168\n" \
  "v_mfma_f32_16x16x32_bf16 v[112:115], a[20:23], v[36:39], v[112:115]\n" \
  "ds_read_b128 v[52:55], v10 offset:27648\n" \
  "v_mfma_f32_16x16x32_bf16 v[104:107], a[100:103], v[32:35], v[104:107]\n" \
  "buffer_load_dwordx4 v1, s[36:39], s68 offen offset:1024 lds\n" \
  "v_mfma_f32_16x16x32_bf16 v[116:119], a[100:103], v[36:39], v[116:119]\n" \
  "v_mfma_f32_16x16x32_bf16 v[108:111], a[180:183], v[32:35], v[108:111]\n" \
  "v_mfma_f32_16x16x32_bf16 v[120:123], a[180:183], v[36:39], v[120:123]\n" \
  "s_waitcnt lgkmcnt(2)\n" \
  "v_mfma_f32_16x16x32_bf16 v[100:103], a[24:27], v[40:43], v[100:103]\n" \
  "ds_read_b128 v[32:35], v10 offset:8192\n" \
  "v_mfma_f32_16x16x32_bf16 v[112:115], a[24:27], v[44:47], v[112:115]\n" \
  "ds_read_b128 v[36:39], v10 offset:28672\n" \
  "v_mfma_f32_16x16x32_bf16 v[104:107], a[104:107], v[40:43], v[104:107]\n" \
  "buffer_load_dwordx4 v1, s[36:39], s68 offen offset:2048 lds\n" \
  "v_mfma_f32_16x16x32_bf16 v[116:119], a[104:107], v[44:47], v[116:119]\n" \
  "v_mfma_f32_16x16x32_bf16 v[108:111], a[184:187], v[40:43], v[108:111]\n" \
  "v_mfma_f32_16x16x32_bf16 v[120:123], a[184:187], v[44:47], v[120:123]\n" \
  "s_waitcnt lgkmcnt(2)\n" \
  "v_mfma_f32_16x16x32_bf16 v[100:103], a[28:31], v[48:51], v[100:103]\n" \
  "ds_read_b128 v[40:43], v10 offset:9216\n" \
  "v_mfma_f32_16x16x32_bf16 v[112:115], a[28:31], v[52:55], v[112:115]\n" \
  "ds_read_b128 v[44:47], v10 offset:29696\n" \
  "v_mfma_f32_16x16x32_bf16 v[104:107], a[108:111], v[48:51], v[104:107]\n" \
  "buffer_load_dwordx4 v1, s[36:39], s68 offen offset:3072 lds\n" \
  "v_mfma_f32_16x16x32_bf16 v[116:119], a[108:111], v[52:55], v[116:119]\n" \
  "v_mfma_f32_16x16x32_bf16 v[108:111], a[188:191], v[48:51], v[108:111]\n" \
  "v_mfma_f32_16x16x32_bf16 v[120:123], a[188:191], v[52:55], v[120:123]\n" \
  "s_waitcnt lgkmcnt(2)\n" \
  "v_mfma_f32_16x16x32_bf16 v[100:103], a[32:35], v[32:35], v[100:103]\n" \
  "ds_read_b128 v[48:51], v10 offset:10240\n" \
  "v_mfma_f32_16x16x32_bf16 v[112:115], a[32:35], v[36:39], v[112:115]\n" \
  "ds_read_b128 v[52:55], v10 offset:30720\n" \
  "v_mfma_f32_16x16x32_bf16 v[104:107], a[112:115], v[32:35], v[104:107]\n" \
  "s_add_u32 m0, s54, 45056\n" \
  "v_mfma_f32_16x16x32_bf16 v[116:119], a[112:115], v[36:39], v[116:119]\n" \
  "buffer_load_dwordx4 v1, s[36:39], s72 offen offset:0 lds\n" \
  "v_mfma_f32_16x16x32_bf16 v[108:111], a[192:195], v[32:35], v[108:111]\n" \
  "v_mfma_f32_16x16x32_bf16 v[120:123], a[192:195], v[36:39], v[120:123]\n" \
  "s_waitcnt lgkmcnt(2)\n" \
  "v_mfma_f32_16x16x32_bf16 v[100:103], a[36:39], v[40:43], v[100:103]\n" \
  "ds_read_b128 v[32:35], v10 offset:11264\n" \
  "v_mfma_f32_16x16x32_bf16 v[112:115], a[36:39], v[44:47], v[112:115]\n" \
  "ds_read_b128 v[36:39], v10 offset:31744\n" \
  "v_mfma_f32_16x16x32_bf16 v[104:107], a[116:119], v[40:43], v[104:107]\n" \
  "buffer_load_dwordx4 v1, s[36:39], s72 offen offset:1024 lds\n" \
  "v_mfma_f32_16x16x32_bf16 v[116:119], a[116:119], v[44:47], v[116:119]\n" \
  "v_mfma_f32_16x16x32_bf16 v[108:111], a[196:199], v[40:43], v[108:111]\n" \
  "v_mfma_f32_16x16x32_bf16 v[120:123], a[196:199], v[44:47], v[120:123]\n" \
  "s_waitcnt lgkmcnt(2)\n" \
  "v_mfma_f32_16x16x32_bf16 v[100:103], a[40:43], v[48:51], v[100:103]\n" \
  "ds_read_b128 v[40:43], v10 offset:12288\n" \
  "v_mfma_f32_16x16x32_bf16 v[112:115], a[40:43], v[52:55], v[112:115]\n" \
  "ds_read_b128 v[44:47], v10 offset:32768\n" \
  "v_mfma_f32_16x16x32_bf16 v[104:107], a[120:123], v[48:51], v[104:107]\n" \
  "buffer_load_dwordx4 v1, s[36:39], s72 offen offset:2048 lds\n" \
  "v_mfma_f32_16x16x32_bf16 v[116:119], a[120:123], v[52:55], v[116:119]\n" \
  "v_mfma_f32_16x16x32_bf16 v[108:111], a[200:203], v[48:51], v[108:111]\n" \
  "v_mfma_f32_16x16x32_bf16 v[120:123], a[200:203], v[52:55], v[120:123]\n" \
  "s_waitcnt lgkmcnt(2)\n" \
  "v_mfma_f32_16x16x32_bf16 v[100:103], a[44:47], v[32:35], v[100:103]\n" \
  "ds_read_b128 v[48:51], v10 offset:13312\n" \
  "v_mfma_f32_16x16x32_bf16 v[112:115], a[44:47], v[36:39], v[112:115]\n" \
  "ds_read_b128 v[52:55], v10 offset:33792\n" \
  "v_mfma_f32_16x16x32_bf16 v[104:107], a[124:127], v[32:35], v[104:107]\n" \
  "buffer_load_dwordx4 v1, s[36:39], s72 offen offset:3072 lds\n" \
  "v_mfma_f32_16x16x32_bf16 v[116:119], a[124:127], v[36:39], v[116:119]\n" \
  "v_mfma_f32_16x16x32_bf16 v[108:111], a[204:207], v[32:35], v[108:111]\n" \
  "v_mfma_f32_16x16x32_bf16 v[120:123], a[204:207], v[36:39], v[120:123]\n" \
  "s_waitcnt lgkmcnt(2)\n" \
  "v_mfma_f32_16x16x32_bf16 v[100:103], a[48:51], v[40:43], v[100:103]\n" \
  "ds_read_b128 v[32:35], v10 offset:14336\n" \
  "v_mfma_f32_16x16x32_bf16 v[112:115], a[48:51], v[44:47], v[112:115]\n" \
  "ds_read_b128 v[36:39], v10 offset:34816\n" \
  "v_mfma_f32_16x16x32_bf16 v[104:107], a[128:131], v[40:43], v[104:107]\n" \
  "s_add_u32 m0, s54, 49152\n" \
  "v_mfma_f32_16x16x32_bf16 v[116:119], a[128:131], v[44:47], v[116:119]\n" \
  "buffer_load_dwordx4 v1, s[36:39], s73 offen offset:0 lds\n" \
  "v_mfma_f32_16x16x32_bf16 v[108:111], a[208:211], v[40:43], v[108:111]\n" \
  "v_mfma_f32_16x16x32_bf16 v[120:123], a[208:211], v[44:47], v[120:123]\n" \
  "s_waitcnt lgkmcnt(2)\n" \
  "v_mfma_f32_16x16x32_bf16 v[100:103], a[52:55], v[48:51], v[100:103]\n" \
  "ds_read_b128 v[40:43], v10 offset:15360\n" \
  "v_mfma_f32_16x16x32_bf16 v[112:115], a[52:55], v[52:55], v[112:115]\n" \
  "ds_read_b128 v[44:47], v10 offset:35840\n" \
  "v_mfma_f32_16x16x32_bf16 v[104:107], a[132:135], v[48:51], v[104:107]\n" \
  "buffer_load_dwordx4 v1, s[36:39], s73 offen offset:1024 lds\n" \
  "v_mfma_f32_16x16x32_bf16 v[116:119], a[132:135], v[52:55], v[116:119]\n" \
  "v_mfma_f32_16x16x32_bf16 v[108:111], a[212:215], v[48:51], v[108:111]\n" \
  "v_mfma_f32_16x16x32_bf16 v[120:123], a[212:215], v[52:55], v[120:123]\n" \
  "s_waitcnt lgkmcnt(2)\n" \
  "v_mfma_f32_16x16x32_bf16 v[100:103], a[56:59], v[32:35], v[100:103]\n" \
  "ds_read_b128 v[48:51], v10 offset:16384\n" \
  "v_mfma_f32_16x16x32_bf16 v[112:115], a[56:59], v[36:39], v[112:115]\n" \
  "ds_read_b128 v[52:55], v10 offset:36864\n" \
  "v_mfma_f32_16x16x32_bf16 v[104:107], a[136:139], v[32:35], v[104:107]\n" \
  "s_add_u32 s68, s68, 40960\n" \
  "v_mfma_f32_16x16x32_bf16 v[116:119], a[136:139], v[36:39], v[116:119]\n" \
  "v_mfma_f32_16x16x32_bf16 v[108:111], a[216:219], v[32:35], v[108:111]\n" \
  "v_mfma_f32_16x16x32_bf16 v[120:123], a[216:219], v[36:39], v[120:123]\n" \
  "s_waitcnt lgkmcnt(2)\n" \
  "v_mfma_f32_16x16x32_bf16 v[100:103], a[60:63], v[40:43], v[100:103]\n" \
  "ds_read_b128 v[32:35], v10 offset:17408\n" \
  "v_mfma_f32_16x16x32_bf16 v[112:115], a[60:63], v[44:47], v[112:115]\n" \
  "ds_read_b128 v[36:39], v10 offset:37888\n" \
  "v_mfma_f32_16x16x32_bf16 v[104:107], a[140:143], v[40:43], v[104:107]\n" \
  "v_mov_b32 v11, s52\n" \
  "v_mfma_f32_16x16x32_bf16 v[116:119], a[140:143], v[44:47], v[116:119]\n" \
  "s_waitcnt vmcnt(10)\n" \
  "v_mfma_f32_16x16x32_bf16 v[108:111], a[220:223], v[40:43], v[108:111]\n" \
  "buffer_store_dword v11, v5, s[40:43], 0 offen sc1\n" \
  "v_mfma_f32_16x16x32_bf16 v[120:123], a[220:223], v[44:47], v[120:123]\n" \
  "s_waitcnt lgkmcnt(2)\n" \
  "v_mfma_f32_16x16x32_bf16 v[100:103], a[64:67], v[48:51], v[100:103]\n" \
  "ds_read_b128 v[40:43], v10 offset:18432\n" \
  "v_mfma_f32_16x16x32_bf16 v[112:115], a[64:67], v[52:55], v[112:115]\n" \
  "ds_read_b128 v[44:47], v10 offset:38912\n" \
  "v_mfma_f32_16x16x32_bf16 v[104:107], a[144:147], v[48:51], v[104:107]\n" \
  "v_mfma_f32_16x16x32_bf16 v[116:119], a[144:147], v[52:55], v[116:119]\n" \
  "v_mfma_f32_16x16x32_bf16 v[108:111], a[224:227], v[48:51], v[108:111]\n" \
  "v_mfma_f32_16x16x32_bf16 v[120:123], a[224:227], v[52:55], v[120:123]\n" \
  "s_waitcnt lgkmcnt(2)\n" \
  "v_mfma_f32_16x16x32_bf16 v[100:103], a[68:71], v[32:35], v[100:103]\n" \
  "ds_read_b128 v[48:51], v10 offset:19456\n" \
  "v_mfma_f32_16x16x32_bf16 v[112:115], a[68:71], v[36:39], v[112:115]\n" \
  "ds_read_b128 v[52:55], v10 offset:39936\n" \
  "v_mfma_f32_16x16x32_bf16 v[104:107], a[148:151], v[32:35], v[104:107]\n" \
  "v_readlane_b32 s69, v4, s52\n" \
  "v_mfma_f32_16x16x32_bf16 v[116:119], a[148:151], v[36:39], v[116:119]\n" \
  "v_readlane_b32 s71, v8, s52\n" \
  "v_mfma_f32_16x16x32_bf16 v[108:111], a[228:231], v[32:35], v[108:111]\n" \
  "s_add_u32 s52, s52, 1\n" \
  "v_mfma_f32_16x16x32_bf16 v[120:123], a[228:231], v[36:39], v[120:123]\n" \
  "s_waitcnt lgkmcnt(0)\n" \
  "s_barrier\n" \
  "v_mfma_f32_16x16x32_bf16 v[100:103], a[72:75], v[40:43], v[100:103]\n" \
  "ds_read_b128 v[32:35], v0 offset:0\n" \
  "v_mfma_f32_16x16x32_bf16 v[112:115], a[72:75], v[44:47], v[112:115]\n" \
  "ds_read_b128 v[36:39], v0 offset:20480\n" \
  "v_mfma_f32_16x16x32_bf16 v[104:107], a[152:155], v[40:43], v[104:107]\n" \
  "v_mfma_f32_16x16x32_bf16 v[116:119], a[152:155], v[44:47], v[116:119]\n" \
  "v_mfma_f32_16x16x32_bf16 v[108:111], a[232:235], v[40:43], v[108:111]\n" \
  "v_mfma_f32_16x16x32_bf16 v[120:123], a[232:235], v[44:47], v[120:123]\n" \
  "v_mfma_f32_16x16x32_bf16 v[100:103], a[76:79], v[48:51], v[100:103]\n" \
  "ds_read_b128 v[40:43], v0 offset:1024\n" \
  "v_mfma_f32_16x16x32_bf16 v[112:115], a[76:79], v[52:55], v[112:115]\n" \
  "ds_read_b128 v[44:47], v0 offset:21504\n" \
  "v_mfma_f32_16x16x32_bf16 v[104:107], a[156:159], v[48:51], v[104:107]\n" \
  "s_cmp_eq_u32 s52, s53\n" \
  "v_mfma_f32_16x16x32_bf16 v[116:119], a[156:159], v[52:55], v[116:119]\n" \
  "v_mfma_f32_16x16x32_bf16 v[108:111], a[236:239], v[48:51], v[108:111]\n" \
  "v_mfma_f32_16x16x32_bf16 v[120:123], a[236:239], v[52:55], v[120:123]\n" \
  "s_cbranch_scc0 LPL_%=\n" \
  "LPE_%=:\n" \
  "buffer_store_dwordx4 v[12:15], v7, s[40:43], s71 offen sc1\n" \
  "s_nop 6\n" \
  "v_cvt_pk_bf16_f32 v16, v100, v101\n" \
  "v_cvt_pk_bf16_f32 v17, v102, v103\n" \
  "v_cvt_pk_bf16_f32 v18, v104, v105\n" \
  "v_cvt_pk_bf16_f32 v19, v106, v107\n" \
  "v_cvt_pk_bf16_f32 v20, v112, v113\n" \
  "v_cvt_pk_bf16_f32 v21, v114, v115\n" \
  "v_cvt_pk_bf16_f32 v22, v116, v117\n" \
  "v_cvt_pk_bf16_f32 v23, v118, v119\n" \
  "v_cvt_pk_bf16_f32 v24, v108, v109\n" \
  "v_cvt_pk_bf16_f32 v25, v110, v111\n" \
  "v_cvt_pk_bf16_f32 v26, v120, v121\n" \
  "v_cvt_pk_bf16_f32 v27, v122, v123\n" \
  "buffer_store_dwordx4 v[16:19], v2, s[40:43], s69 offen sc1\n" \
  "buffer_store_dwordx4 v[20:23], v2, s[40:43], s69 offen offset:1024 sc1\n" \
  "buffer_store_dwordx4 v[24:27], v3, s[40:43], s69 offen sc1\n" \
  "v_mov_b32 v11, s52\n" \
  "s_waitcnt vmcnt(0) lgkmcnt(0)\n" \
  "buffer_store_dword v11, v5, s[40:43], 0 offen sc1\n"
#define VO_CLOBBERS \
  "v16", "v17", "v18", "v19", "v20", "v21", "v22", "v23", "v24", "v25", "v26", "v27", "v28", "v29", "v30", "v31", "v32", "v33", "v34", "v35", "v36", \
  "v37", "v38", "v39", "v40", "v41", "v42", "v43", "v44", "v45", "v46", "v47", "v48", "v49", "v50", "v51", "v52", "v53", "v54", "v55", "v56", "v57", \
  "v58", "v59", "v60", "v61", "v62", "v63", "v64", "v65", "v66", "v67", "v68", "v69", "v70", "v71", "v72", "v73", "v74", "v75", "v76", "v77", "v78", \
  "v79", "v80", "v81", "v82", "v83", "v84", "v85", "v86", "v87", "v88", "v89", "v90", "v91", "v92", "v93", "v94", "v95", "v96", "v97", "v98", "v99", \
  "v100", "v101", "v102", "v103", "v104", "v105", "v106", "v107", "v108", "v109", "v110", "v111", "v112", "v113", "v114", "v115", "v116", "v117", \
  "v118", "v119", "v120", "v121", "v122", "v123", "v124", "v125", "v126", "v127", "v224", "v225", "v226", "v227", "v228", "v229", "v230", "v231", \
  "v232", "v233", "v234", "v235", "v236", "v237", "v238", "v239", "v240", "v241", "v242", "v243", "v244", "v245", "v246", "v247", "v248", "v249", \
  "v250", "v251", "v252", "v253", "v254", "v255", "s68", "s69", "s70", "s71", "s72", "s73", "s74", "s75", "s76", "s77"
#define VO_ASM \
  "s_lshl_b32 s73, s54, 11\n" \
  "s_lshl_b32 s74, s54, 10\n" \
  "s_add_u32 s74, s74, 8192\n" \
  "s_mov_b32 s75, 0\n" \
  "s_mov_b32 s76, s52\n" \
  "s_mov_b32 s69, 0x40000000\n" \
  "s_barrier\n" \
  "v_mov_b32 v12, v0\n" \
  "LVPS0_%=:\n" \
  "buffer_load_dword v11, v9, s[36:39], 0 offen sc1\n" \
  "s_waitcnt vmcnt(0)\n" \
  "v_cmp_ge_u32 vcc, s76, v11\n" \
  "s_nop 3\n" \
  "s_cbranch_vccz LVS0_%=\n" \
  "s_add_u32 s75, s75, 1\n" \
  "s_cmp_gt_u32 s75, 200000\n" \
  "s_cbranch_scc1 LVS0_%=\n" \
  "s_sleep 2\n" \
  "s_branch LVPS0_%=\n" \
  "LVS0_%=:\n" \
  "v_readlane_b32 s71, v6, s76\n" \
  "s_add_u32 m0, s73, 0\n" \
  "s_nop 4\n" \
  "buffer_load_dwordx4 v1, s[36:39], s71 offen offset:0 sc1 lds\n" \
  "buffer_load_dwordx4 v1, s[36:39], s71 offen offset:1024 sc1 lds\n" \
  "s_add_u32 m0, s74, 0\n" \
  "s_add_u32 s76, s76, 1\n" \
  "buffer_load_dwordx4 v2, s[36:39], s71 offen sc1 lds\n" \
  "s_min_u32 s76, s76, 63\n" \
  "LVPS1_%=:\n" \
  "buffer_load_dword v11, v9, s[36:39], 0 offen sc1\n" \
  "s_waitcnt vmcnt(0)\n" \
  "v_cmp_ge_u32 vcc, s76, v11\n" \
  "s_nop 3\n" \
  "s_cbranch_vccz LVS1_%=\n" \
  "s_add_u32 s75, s75, 1\n" \
  "s_cmp_gt_u32 s75, 200000\n" \
  "s_cbranch_scc1 LVS1_%=\n" \
  "s_sleep 2\n" \
  "s_branch LVPS1_%=\n" \
  "LVS1_%=:\n" \
  "v_readlane_b32 s71, v6, s76\n" \
  "s_add_u32 m0, s73, 16384\n" \
  "s_nop 4\n" \
  "buffer_load_dwordx4 v1, s[36:39], s71 offen offset:0 sc1 lds\n" \
  "buffer_load_dwordx4 v1, s[36:39], s71 offen offset:1024 sc1 lds\n" \
  "s_add_u32 m0, s74, 16384\n" \
  "s_add_u32 s76, s76, 1\n" \
  "buffer_load_dwordx4 v2, s[36:39], s71 offen sc1 lds\n" \
  "s_min_u32 s76, s76, 63\n" \
  "s_xor_b32 s73, s73, 0x8000\n" \
  "s_xor_b32 s74, s74, 0x8000\n" \
  "buffer_load_dword v11, v9, s[36:39], 0 offen sc1\n" \
  "s_waitcnt vmcnt(0)\n" \
  "s_barrier\n" \
  "ds_read_b128 v[32:35], v12 offset:0\n" \
  "ds_read_b128 v[56:59], v12 offset:1024\n" \
  "ds_read_b128 v[36:39], v12 offset:2048\n" \
  "ds_read_b128 v[60:63], v12 offset:3072\n" \
  "ds_read_b128 v[40:43], v12 offset:4096\n" \
  "ds_read_b128 v[64:67], v12 offset:5120\n" \
  "ds_read_b128 v[44:47], v12 offset:6144\n" \
  "ds_read_b128 v[68:71], v12 offset:7168\n" \
  "s_waitcnt lgkmcnt(4)\n" \
  "ds_read_b64 v[48:49], v12 offset:8192\n" \
  "ds_read_b64 v[72:73], v12 offset:8200\n" \
  "ds_read_b64 v[50:51], v12 offset:9216\n" \
  "ds_read_b64 v[74:75], v12 offset:9224\n" \
  "ds_read_b64 v[52:53], v12 offset:10240\n" \
  "ds_read_b64 v[76:77], v12 offset:10248\n" \
  "ds_read_b64 v[54:55], v12 offset:11264\n" \
  "ds_read_b64 v[78:79], v12 offset:11272\n" \
  "s_waitcnt lgkmcnt(0)\n" \
  "LVL_%=:\n" \
  "s_mov_b32 s77, s69\n" \
  "v_readlane_b32 s68, v4, s52\n" \
  "v_readlane_b32 s69, v5, s52\n" \
  "v_mfma_f32_16x16x32_bf16 v[224:227], v[32:35], a[0:3], 0\n" \
  "v_mfma_f32_16x16x32_bf16 v[228:231], v[56:59], a[0:3], 0\n" \
  "v_mfma_f32_16x16x32_bf16 v[224:227], v[36:39], a[4:7], v[224:227]\n" \
  "v_cvt_pk_bf16_f32 v252, v232, v233\n" \
  "v_mfma_f32_16x16x32_bf16 v[228:231], v[60:63], a[4:7], v[228:231]\n" \
  "v_cvt_pk_bf16_f32 v253, v234, v235\n" \
  "v_mfma_f32_16x16x32_bf16 v[224:227], v[40:43], a[8:11], v[224:227]\n" \
  "v_cvt_pk_bf16_f32 v254, v236, v237\n" \
  "v_mfma_f32_16x16x32_bf16 v[228:231], v[64:67], a[8:11], v[228:231]\n" \
  "v_cvt_pk_bf16_f32 v255, v238, v239\n" \
  "v_mfma_f32_16x16x32_bf16 v[224:227], v[44:47], a[12:15], v[224:227]\n" \
  "s_add_u32 s72, s77, 589824\n" \
  "v_mfma_f32_16x16x32_bf16 v[228:231], v[68:71], a[12:15], v[228:231]\n" \
  "buffer_store_dwordx4 v[252:255], v3, s[36:39], s72 offen sc1\n" \
  "v_mfma_f32_16x16x32_bf16 v[224:227], v[48:51], a[16:19], v[224:227]\n" \
  "s_waitcnt vmcnt(7)\n" \
  "v_mfma_f32_16x16x32_bf16 v[228:231], v[72:75], a[16:19], v[228:231]\n" \
  "v_cmp_ge_u32 vcc, s76, v11\n" \
  "v_mfma_f32_16x16x32_bf16 v[224:227], v[52:55], a[20:23], v[224:227]\n" \
  "v_mfma_f32_16x16x32_bf16 v[228:231], v[76:79], a[20:23], v[228:231]\n" \
  "s_cbranch_vccnz LVP0_%=\n" \
  "LVK0_%=:\n" \
  "v_mfma_f32_16x16x32_bf16 v[232:235], v[32:35], a[24:27], 0\n" \
  "v_mfma_f32_16x16x32_bf16 v[236:239], v[56:59], a[24:27], 0\n" \
  "v_cvt_pk_bf16_f32 v248, v224, v225\n" \
  "v_mfma_f32_16x16x32_bf16 v[232:235], v[36:39], a[28:31], v[232:235]\n" \
  "v_cvt_pk_bf16_f32 v249, v226, v227\n" \
  "v_mfma_f32_16x16x32_bf16 v[236:239], v[60:63], a[28:31], v[236:239]\n" \
  "v_cvt_pk_bf16_f32 v250, v228, v229\n" \
  "v_mfma_f32_16x16x32_bf16 v[232:235], v[40:43], a[32:35], v[232:235]\n" \
  "v_cvt_pk_bf16_f32 v251, v230, v231\n" \
  "v_mfma_f32_16x16x32_bf16 v[236:239], v[64:67], a[32:35], v[236:239]\n" \
  "s_add_u32 s72, s68, 0\n" \
  "v_mfma_f32_16x16x32_bf16 v[232:235], v[44:47], a[36:39], v[232:235]\n" \
  "buffer_store_dwordx4 v[248:251], v3, s[36:39], s72 offen sc1\n" \
  "v_mfma_f32_16x16x32_bf16 v[236:239], v[68:71], a[36:39], v[236:239]\n" \
  "v_readlane_b32 s71, v6, s76\n" \
  "v_mfma_f32_16x16x32_bf16 v[232:235], v[48:51], a[40:43], v[232:235]\n" \
  "s_add_u32 m0, s73, 0\n" \
  "v_mfma_f32_16x16x32_bf16 v[236:239], v[72:75], a[40:43], v[236:239]\n" \
  "s_nop 2\n" \
  "v_mfma_f32_16x16x32_bf16 v[232:235], v[52:55], a[44:47], v[232:235]\n" \
  "buffer_load_dwordx4 v1, s[36:39], s71 offen offset:0 sc1 lds\n" \
  "v_mfma_f32_16x16x32_bf16 v[236:239], v[76:79], a[44:47], v[236:239]\n" \
  "buffer_load_dwordx4 v1, s[36:39], s71 offen offset:1024 sc1 lds\n" \
  "v_mfma_f32_16x16x32_bf16 v[224:227], v[32:35], a[48:51], 0\n" \
  "v_mfma_f32_16x16x32_bf16 v[228:231], v[56:59], a[48:51], 0\n" \
  "v_mfma_f32_16x16x32_bf16 v[224:227], v[36:39], a[52:55], v[224:227]\n" \
  "v_cvt_pk_bf16_f32 v252, v232, v233\n" \
  "v_mfma_f32_16x16x32_bf16 v[228:231], v[60:63], a[52:55], v[228:231]\n" \
  "v_cvt_pk_bf16_f32 v253, v234, v235\n" \
  "v_mfma_f32_16x16x32_bf16 v[224:227], v[40:43], a[56:59], v[224:227]\n" \
  "v_cvt_pk_bf16_f32 v254, v236, v237\n" \
  "v_mfma_f32_16x16x32_bf16 v[228:231], v[64:67], a[56:59], v[228:231]\n" \
  "v_cvt_pk_bf16_f32 v255, v238, v239\n" \
  "v_mfma_f32_16x16x32_bf16 v[224:227], v[44:47], a[60:63], v[224:227]\n" \
  "s_add_u32 s72, s68, 65536\n" \
  "v_mfma_f32_16x16x32_bf16 v[228:231], v[68:71], a[60:63], v[228:231]\n" \
  "buffer_store_dwordx4 v[252:255], v3, s[36:39], s72 offen sc1\n" \
  "v_mfma_f32_16x16x32_bf16 v[224:227], v[48:51], a[64:67], v[224:227]\n" \
  "s_add_u32 m0, s74, 0\n" \
  "v_mfma_f32_16x16x32_bf16 v[228:231], v[72:75], a[64:67], v[228:231]\n" \
  "s_add_u32 s76, s76, 1\n" \
  "v_mfma_f32_16x16x32_bf16 v[224:227], v[52:55], a[68:71], v[224:227]\n" \
  "buffer_load_dwordx4 v2, s[36:39], s71 offen sc1 lds\n" \
  "v_mfma_f32_16x16x32_bf16 v[228:231], v[76:79], a[68:71], v[228:231]\n" \
  "s_min_u32 s76, s76, 63\n" \
  "v_mfma_f32_16x16x32_bf16 v[232:235], v[32:35], a[72:75], 0\n" \
  "v_mfma_f32_16x16x32_bf16 v[236:239], v[56:59], a[72:75], 0\n" \
  "v_mfma_f32_16x16x32_bf16 v[232:235], v[36:39], a[76:79], v[232:235]\n" \
  "v_cvt_pk_bf16_f32 v248, v224, v225\n" \
  "v_mfma_f32_16x16x32_bf16 v[236:239], v[60:63], a[76:79], v[236:239]\n" \
  "v_cvt_pk_bf16_f32 v249, v226, v227\n" \
  "v_mfma_f32_16x16x32_bf16 v[232:235], v[40:43], a[80:83], v[232:235]\n" \
  "v_cvt_pk_bf16_f32 v250, v228, v229\n" \
  "v_mfma_f32_16x16x32_bf16 v[236:239], v[64:67], a[80:83], v[236:239]\n" \
  "v_cvt_pk_bf16_f32 v251, v230, v231\n" \
  "v_mfma_f32_16x16x32_bf16 v[232:235], v[44:47], a[84:87], v[232:235]\n" \
  "s_add_u32 s72, s69, 131072\n" \
  "v_mfma_f32_16x16x32_bf16 v[236:239], v[68:71], a[84:87], v[236:239]\n" \
  "buffer_store_dwordx4 v[248:251], v3, s[36:39], s72 offen sc1\n" \
  "v_mfma_f32_16x16x32_bf16 v[232:235], v[48:51], a[88:91], v[232:235]\n" \
  "v_mfma_f32_16x16x32_bf16 v[236:239], v[72:75], a[88:91], v[236:239]\n" \
  "v_mfma_f32_16x16x32_bf16 v[232:235], v[52:55], a[92:95], v[232:235]\n" \
  "v_mfma_f32_16x16x32_bf16 v[236:239], v[76:79], a[92:95], v[236:239]\n" \
  "v_mfma_f32_16x16x32_bf16 v[224:227], v[32:35], a[96:99], 0\n" \
  "v_mfma_f32_16x16x32_bf16 v[228:231], v[56:59], a[96:99], 0\n" \
  "v_mfma_f32_16x16x32_bf16 v[224:227], v[36:39], a[100:103], v[224:227]\n" \
  "v_cvt_pk_bf16_f32 v252, v232, v233\n" \
  "v_mfma_f32_16x16x32_bf16 v[228:231], v[60:63], a[100:103], v[228:231]\n" \
  "v_cvt_pk_bf16_f32 v253, v234, v235\n" \
  "v_mfma_f32_16x16x32_bf16 v[224:227], v[40:43], a[104:107], v[224:227]\n" \
  "v_cvt_pk_bf16_f32 v254, v236, v237\n" \
  "v_mfma_f32_16x16x32_bf16 v[228:231], v[64:67], a[104:107], v[228:231]\n" \
  "v_cvt_pk_bf16_f32 v255, v238, v239\n" \
  "v_mfma_f32_16x16x32_bf16 v[224:227], v[44:47], a[108:111], v[224:227]\n" \
  "s_add_u32 s72, s69, 196608\n" \
  "v_mfma_f32_16x16x32_bf16 v[228:231], v[68:71], a[108:111], v[228:231]\n" \
  "buffer_store_dwordx4 v[252:255], v3, s[36:39], s72 offen sc1\n" \
  "v_mfma_f32_16x16x32_bf16 v[224:227], v[48:51], a[112:115], v[224:227]\n" \
  "buffer_load_dword v11, v9, s[36:39], 0 offen sc1\n" \
  "v_mfma_f32_16x16x32_bf16 v[228:231], v[72:75], a[112:115], v[228:231]\n" \
  "v_mfma_f32_16x16x32_bf16 v[224:227], v[52:55], a[116:119], v[224:227]\n" \
  "v_mfma_f32_16x16x32_bf16 v[228:231], v[76:79], a[116:119], v[228:231]\n" \
  "v_mfma_f32_16x16x32_bf16 v[232:235], v[32:35], a[120:123], 0\n" \
  "v_mfma_f32_16x16x32_bf16 v[236:239], v[56:59], a[120:123], 0\n" \
  "v_mfma_f32_16x16x32_bf16 v[232:235], v[36:39], a[124:127], v[232:235]\n" \
  "v_cvt_pk_bf16_f32 v248, v224, v225\n" \
  "v_mfma_f32_16x16x32_bf16 v[236:239], v[60:63], a[124:127], v[236:239]\n" \
  "v_cvt_pk_bf16_f32 v249, v226, v227\n" \
  "v_mfma_f32_16x16x32_bf16 v[232:235], v[40:43], a[128:131], v[232:235]\n" \
  "v_cvt_pk_bf16_f32 v250, v228, v229\n" \
  "v_mfma_f32_16x16x32_bf16 v[236:239], v[64:67], a[128:131], v[236:239]\n" \
  "v_cvt_pk_bf16_f32 v251, v230, v231\n" \
  "v_mfma_f32_16x16x32_bf16 v[232:235], v[44:47], a[132:135], v[232:235]\n" \
  "s_add_u32 s72, s69, 262144\n" \
  "v_mfma_f32_16x16x32_bf16 v[236:239], v[68:71], a[132:135], v[236:239]\n" \
  "buffer_store_dwordx4 v[248:251], v3, s[36:39], s72 offen sc1\n" \
  "v_mfma_f32_16x16x32_bf16 v[232:235], v[48:51], a[136:139], v[232:235]\n" \
  "v_mfma_f32_16x16x32_bf16 v[236:239], v[72:75], a[136:139], v[236:239]\n" \
  "v_mfma_f32_16x16x32_bf16 v[232:235], v[52:55], a[140:143], v[232:235]\n" \
  "v_mfma_f32_16x16x32_bf16 v[236:239], v[76:79], a[140:143], v[236:239]\n" \
  "v_mfma_f32_16x16x32_bf16 v[224:227], v[32:35], a[144:147], 0\n" \
  "v_mfma_f32_16x16x32_bf16 v[228:231], v[56:59], a[144:147], 0\n" \
  "v_mfma_f32_16x16x32_bf16 v[224:227], v[36:39], a[148:151], v[224:227]\n" \
  "v_cvt_pk_bf16_f32 v252, v232, v233\n" \
  "v_mfma_f32_16x16x32_bf16 v[228:231], v[60:63], a[148:151], v[228:231]\n" \
  "v_cvt_pk_bf16_f32 v253, v234, v235\n" \
  "v_mfma_f32_16x16x32_bf16 v[224:227], v[40:43], a[152:155], v[224:227]\n" \
  "v_cvt_pk_bf16_f32 v254, v236, v237\n" \
  "v_mfma_f32_16x16x32_bf16 v[228:231], v[64:67], a[152:155], v[228:231]\n" \
  "v_cvt_pk_bf16_f32 v255, v238, v239\n" \
  "v_mfma_f32_16x16x32_bf16 v[224:227], v[44:47], a[156:159], v[224:227]\n" \
  "s_add_u32 s72, s69, 327680\n" \
  "v_mfma_f32_16x16x32_bf16 v[228:231], v[68:71], a[156:159], v[228:231]\n" \
  "buffer_store_dwordx4 v[252:255], v3, s[36:39], s72 offen sc1\n" \
  "v_mfma_f32_16x16x32_bf16 v[224:227], v[48:51], a[160:163], v[224:227]\n" \
  "v_mov_b32 v10, s52\n" \
  "v_mfma_f32_16x16x32_bf16 v[228:231], v[72:75], a[160:163], v[228:231]\n" \
  "s_waitcnt vmcnt(10)\n" \
  "v_mfma_f32_16x16x32_bf16 v[224:227], v[52:55], a[164:167], v[224:227]\n" \
  "buffer_store_dword v10, v7, s[36:39], 0 offen sc1\n" \
  "v_mfma_f32_16x16x32_bf16 v[228:231], v[76:79], a[164:167], v[228:231]\n" \
  "s_barrier\n" \
  "v_mfma_f32_16x16x32_bf16 v[232:235], v[32:35], a[168:171], 0\n" \
  "v_mfma_f32_16x16x32_bf16 v[236:239], v[56:59], a[168:171], 0\n" \
  "v_cvt_pk_bf16_f32 v248, v224, v225\n" \
  "v_cvt_pk_bf16_f32 v249, v226, v227\n" \
  "v_mfma_f32_16x16x32_bf16 v[232:235], v[36:39], a[172:175], v[232:235]\n" \
  "v_cvt_pk_bf16_f32 v250, v228, v229\n" \
  "v_cvt_pk_bf16_f32 v251, v230, v231\n" \
  "v_mfma_f32_16x16x32_bf16 v[236:239], v[60:63], a[172:175], v[236:239]\n" \
  "s_add_u32 s72, s69, 393216\n" \
  "v_mfma_f32_16x16x32_bf16 v[232:235], v[40:43], a[176:179], v[232:235]\n" \
  "buffer_store_dwordx4 v[248:251], v3, s[36:39], s72 offen sc1\n" \
  "v_mfma_f32_16x16x32_bf16 v[236:239], v[64:67], a[176:179], v[236:239]\n" \
  "ds_read_b128 v[80:83], v12 offset:16384\n" \
  "v_mfma_f32_16x16x32_bf16 v[232:235], v[44:47], a[180:183], v[232:235]\n" \
  "ds_read_b128 v[104:107], v12 offset:17408\n" \
  "v_mfma_f32_16x16x32_bf16 v[236:239], v[68:71], a[180:183], v[236:239]\n" \
  "ds_read_b128 v[84:87], v12 offset:18432\n" \
  "v_mfma_f32_16x16x32_bf16 v[232:235], v[48:51], a[184:187], v[232:235]\n" \
  "ds_read_b128 v[108:111], v12 offset:19456\n" \
  "v_mfma_f32_16x16x32_bf16 v[236:239], v[72:75], a[184:187], v[236:239]\n" \
  "ds_read_b128 v[88:91], v12 offset:20480\n" \
  "v_mfma_f32_16x16x32_bf16 v[232:235], v[52:55], a[188:191], v[232:235]\n" \
  "ds_read_b128 v[112:115], v12 offset:21504\n" \
  "v_mfma_f32_16x16x32_bf16 v[236:239], v[76:79], a[188:191], v[236:239]\n" \
  "ds_read_b128 v[92:95], v12 offset:22528\n" \
  "v_mfma_f32_16x16x32_bf16 v[224:227], v[32:35], a[192:195], 0\n" \
  "v_mfma_f32_16x16x32_bf16 v[228:231], v[56:59], a[192:195], 0\n" \
  "v_cvt_pk_bf16_f32 v252, v232, v233\n" \
  "v_cvt_pk_bf16_f32 v253, v234, v235\n" \
  "v_mfma_f32_16x16x32_bf16 v[224:227], v[36:39], a[196:199], v[224:227]\n" \
  "v_cvt_pk_bf16_f32 v254, v236, v237\n" \
  "v_cvt_pk_bf16_f32 v255, v238, v239\n" \
  "v_mfma_f32_16x16x32_bf16 v[228:231], v[60:63], a[196:199], v[228:231]\n" \
  "s_add_u32 s72, s69, 458752\n" \
  "v_mfma_f32_16x16x32_bf16 v[224:227], v[40:43], a[200:203], v[224:227]\n" \
  "buffer_store_dwordx4 v[252:255], v3, s[36:39], s72 offen sc1\n" \
  "v_mfma_f32_16x16x32_bf16 v[228:231], v[64:67], a[200:203], v[228:231]\n" \
  "ds_read_b128 v[116:119], v12 offset:23552\n" \
  "v_mfma_f32_16x16x32_bf16 v[224:227], v[44:47], a[204:207], v[224:227]\n" \
  "ds_read_b64 v[96:97], v12 offset:24576\n" \
  "v_mfma_f32_16x16x32_bf16 v[228:231], v[68:71], a[204:207], v[228:231]\n" \
  "ds_read_b64 v[120:121], v12 offset:24584\n" \
  "v_mfma_f32_16x16x32_bf16 v[224:227], v[48:51], a[208:211], v[224:227]\n" \
  "ds_read_b64 v[98:99], v12 offset:25600\n" \
  "v_mfma_f32_16x16x32_bf16 v[228:231], v[72:75], a[208:211], v[228:231]\n" \
  "ds_read_b64 v[122:123], v12 offset:25608\n" \
  "v_mfma_f32_16x16x32_bf16 v[224:227], v[52:55], a[212:215], v[224:227]\n" \
  "ds_read_b64 v[100:101], v12 offset:26624\n" \
  "v_mfma_f32_16x16x32_bf16 v[228:231], v[76:79], a[212:215], v[228:231]\n" \
  "ds_read_b64 v[124:125], v12 offset:26632\n" \
  "v_mfma_f32_16x16x32_bf16 v[232:235], v[32:35], a[216:219], 0\n" \
  "v_mfma_f32_16x16x32_bf16 v[236:239], v[56:59], a[216:219], 0\n" \
  "v_mfma_f32_16x16x32_bf16 v[232:235], v[36:39], a[220:223], v[232:235]\n" \
  "v_cvt_pk_bf16_f32 v248, v224, v225\n" \
  "v_mfma_f32_16x16x32_bf16 v[236:239], v[60:63], a[220:223], v[236:239]\n" \
  "v_cvt_pk_bf16_f32 v249, v226, v227\n" \
  "v_mfma_f32_16x16x32_bf16 v[232:235], v[40:43], a[224:227], v[232:235]\n" \
  "v_cvt_pk_bf16_f32 v250, v228, v229\n" \
  "v_mfma_f32_16x16x32_bf16 v[236:239], v[64:67], a[224:227], v[236:239]\n" \
  "v_cvt_pk_bf16_f32 v251, v230, v231\n" \
  "v_mfma_f32_16x16x32_bf16 v[232:235], v[44:47], a[228:231], v[232:235]\n" \
  "s_add_u32 s72, s69, 524288\n" \
  "v_mfma_f32_16x16x32_bf16 v[236:239], v[68:71], a[228:231], v[236:239]\n" \
  "buffer_store_dwordx4 v[248:251], v3, s[36:39], s72 offen sc1\n" \
  "v_mfma_f32_16x16x32_bf16 v[232:235], v[48:51], a[232:235], v[232:235]\n" \
  "ds_read_b64 v[102:103], v12 offset:27648\n" \
  "v_mfma_f32_16x16x32_bf16 v[236:239], v[72:75], a[232:235], v[236:239]\n" \
  "ds_read_b64 v[126:127], v12 offset:27656\n" \
  "v_mfma_f32_16x16x32_bf16 v[232:235], v[52:55], a[236:239], v[232:235]\n" \
  "s_add_u32 s52, s52, 1\n" \
  "v_mfma_f32_16x16x32_bf16 v[236:239], v[76:79], a[236:239], v[236:239]\n" \
  "s_cmp_eq_u32 s52, s53\n" \
  "s_waitcnt lgkmcnt(0)\n" \
  "s_mov_b32 s77, s69\n" \
  "v_readlane_b32 s68, v4, s52\n" \
  "v_readlane_b32 s69, v5, s52\n" \
  "v_mfma_f32_16x16x32_bf16 v[224:227], v[80:83], a[0:3], 0\n" \
  "v_mfma_f32_16x16x32_bf16 v[228:231], v[104:107], a[0:3], 0\n" \
  "v_mfma_f32_16x16x32_bf16 v[224:227], v[84:87], a[4:7], v[224:227]\n" \
  "v_cvt_pk_bf16_f32 v252, v232, v233\n" \
  "v_mfma_f32_16x16x32_bf16 v[228:231], v[108:111], a[4:7], v[228:231]\n" \
  "v_cvt_pk_bf16_f32 v253, v234, v235\n" \
  "v_mfma_f32_16x16x32_bf16 v[224:227], v[88:91], a[8:11], v[224:227]\n" \
  "v_cvt_pk_bf16_f32 v254, v236, v237\n" \
  "v_mfma_f32_16x16x32_bf16 v[228:231], v[112:115], a[8:11], v[228:231]\n" \
  "v_cvt_pk_bf16_f32 v255, v238, v239\n" \
  "v_mfma_f32_16x16x32_bf16 v[224:227], v[92:95], a[12:15], v[224:227]\n" \
  "s_add_u32 s72, s77, 589824\n" \
  "v_mfma_f32_16x16x32_bf16 v[228:231], v[116:119], a[12:15], v[228:231]\n" \
  "buffer_store_dwordx4 v[252:255], v3, s[36:39], s72 offen sc1\n" \
  "v_mfma_f32_16x16x32_bf16 v[224:227], v[96:99], a[16:19], v[224:227]\n" \
  "s_waitcnt vmcnt(7)\n" \
  "v_mfma_f32_16x16x32_bf16 v[228:231], v[120:123], a[16:19], v[228:231]\n" \
  "v_cmp_ge_u32 vcc, s76, v11\n" \
  "v_mfma_f32_16x16x32_bf16 v[224:227], v[100:103], a[20:23], v[224:227]\n" \
  "v_mfma_f32_16x16x32_bf16 v[228:231], v[124:127], a[20:23], v[228:231]\n" \
  "s_cbranch_vccnz LVP1_%=\n" \
  "LVK1_%=:\n" \
  "v_mfma_f32_16x16x32_bf16 v[232:235], v[80:83], a[24:27], 0\n" \
  "v_mfma_f32_16x16x32_bf16 v[236:239], v[104:107], a[24:27], 0\n" \
  "v_cvt_pk_bf16_f32 v248, v224, v225\n" \
  "v_mfma_f32_16x16x32_bf16 v[232:235], v[84:87], a[28:31], v[232:235]\n" \
  "v_cvt_pk_bf16_f32 v249, v226, v227\n" \
  "v_mfma_f32_16x16x32_bf16 v[236:239], v[108:111], a[28:31], v[236:239]\n" \
  "v_cvt_pk_bf16_f32 v250, v228, v229\n" \
  "v_mfma_f32_16x16x32_bf16 v[232:235], v[88:91], a[32:35], v[232:235]\n" \
  "v_cvt_pk_bf16_f32 v251, v230, v231\n" \
  "v_mfma_f32_16x16x32_bf16 v[236:239], v[112:115], a[32:35], v[236:239]\n" \
  "s_add_u32 s72, s68, 0\n" \
  "v_mfma_f32_16x16x32_bf16 v[232:235], v[92:95], a[36:39], v[232:235]\n" \
  "buffer_store_dwordx4 v[248:251], v3, s[36:39], s72 offen sc1\n" \
  "v_mfma_f32_16x16x32_bf16 v[236:239], v[116:119], a[36:39], v[236:239]\n" \
  "v_readlane_b32 s71, v6, s76\n" \
  "v_mfma_f32_16x16x32_bf16 v[232:235], v[96:99], a[40:43], v[232:235]\n" \
  "s_add_u32 m0, s73, 16384\n" \
  "v_mfma_f32_16x16x32_bf16 v[236:239], v[120:123], a[40:43], v[236:239]\n" \
  "s_nop 2\n" \
  "v_mfma_f32_16x16x32_bf16 v[232:235], v[100:103], a[44:47], v[232:235]\n" \
  "buffer_load_dwordx4 v1, s[36:39], s71 offen offset:0 sc1 lds\n" \
  "v_mfma_f32_16x16x32_bf16 v[236:239], v[124:127], a[44:47], v[236:239]\n" \
  "buffer_load_dwordx4 v1, s[36:39], s71 offen offset:1024 sc1 lds\n" \
  "v_mfma_f32_16x16x32_bf16 v[224:227], v[80:83], a[48:51], 0\n" \
  "v_mfma_f32_16x16x32_bf16 v[228:231], v[104:107], a[48:51], 0\n" \
  "v_mfma_f32_16x16x32_bf16 v[224:227], v[84:87], a[52:55], v[224:227]\n" \
  "v_cvt_pk_bf16_f32 v252, v232, v233\n" \
  "v_mfma_f32_16x16x32_bf16 v[228:231], v[108:111], a[52:55], v[228:231]\n" \
  "v_cvt_pk_bf16_f32 v253, v234, v235\n" \
  "v_mfma_f32_16x16x32_bf16 v[224:227], v[88:91], a[56:59], v[224:227]\n" \
  "v_cvt_pk_bf16_f32 v254, v236, v237\n" \
  "v_mfma_f32_16x16x32_bf16 v[228:231], v[112:115], a[56:59], v[228:231]\n" \
  "v_cvt_pk_bf16_f32 v255, v238, v239\n" \
  "v_mfma_f32_16x16x32_bf16 v[224:227], v[92:95], a[60:63], v[224:227]\n" \
  "s_add_u32 s72, s68, 65536\n" \
  "v_mfma_f32_16x16x32_bf16 v[228:231], v[116:119], a[60:63], v[228:231]\n" \
  "buffer_store_dwordx4 v[252:255], v3, s[36:39], s72 offen sc1\n" \
  "v_mfma_f32_16x16x32_bf16 v[224:227], v[96:99], a[64:67], v[224:227]\n" \
  "s_add_u32 m0, s74, 16384\n" \
  "v_mfma_f32_16x16x32_bf16 v[228:231], v[120:123], a[64:67], v[228:231]\n" \
  "s_add_u32 s76, s76, 1\n" \
  "v_mfma_f32_16x16x32_bf16 v[224:227], v[100:103], a[68:71], v[224:227]\n" \
  "buffer_load_dwordx4 v2, s[36:39], s71 offen sc1 lds\n" \
  "v_mfma_f32_16x16x32_bf16 v[228:231], v[124:127], a[68:71], v[228:231]\n" \
  "s_min_u32 s76, s76, 63\n" \
  "v_mfma_f32_16x16x32_bf16 v[232:235], v[80:83], a[72:75], 0\n" \
  "v_mfma_f32_16x16x32_bf16 v[236:239], v[104:107], a[72:75], 0\n" \
  "v_mfma_f32_16x16x32_bf16 v[232:235], v[84:87], a[76:79], v[232:235]\n" \
  "v_cvt_pk_bf16_f32 v248, v224, v225\n" \
  "v_mfma_f32_16x16x32_bf16 v[236:239], v[108:111], a[76:79], v[236:239]\n" \
  "v_cvt_pk_bf16_f32 v249, v226, v227\n" \
  "v_mfma_f32_16x16x32_bf16 v[232:235], v[88:91], a[80:83], v[232:235]\n" \
  "v_cvt_pk_bf16_f32 v250, v228, v229\n" \
  "v_mfma_f32_16x16x32_bf16 v[236:239], v[112:115], a[80:83], v[236:239]\n" \
  "v_cvt_pk_bf16_f32 v251, v230, v231\n" \
  "v_mfma_f32_16x16x32_bf16 v[232:235], v[92:95], a[84:87], v[232:235]\n" \
  "s_add_u32 s72, s69, 131072\n" \
  "v_mfma_f32_16x16x32_bf16 v[236:239], v[116:119], a[84:87], v[236:239]\n" \
  "buffer_store_dwordx4 v[248:251], v3, s[36:39], s72 offen sc1\n" \
  "v_mfma_f32_16x16x32_bf16 v[232:235], v[96:99], a[88:91], v[232:235]\n" \
  "v_mfma_f32_16x16x32_bf16 v[236:239], v[120:123], a[88:91], v[236:239]\n" \
  "v_mfma_f32_16x16x32_bf16 v[232:235], v[100:103], a[92:95], v[232:235]\n" \
  "v_mfma_f32_16x16x32_bf16 v[236:239], v[124:127], a[92:95], v[236:239]\n" \
  "v_mfma_f32_16x16x32_bf16 v[224:227], v[80:83], a[96:99], 0\n" \
  "v_mfma_f32_16x16x32_bf16 v[228:231], v[104:107], a[96:99], 0\n" \
  "v_mfma_f32_16x16x32_bf16 v[224:227], v[84:87], a[100:103], v[224:227]\n" \
  "v_cvt_pk_bf16_f32 v252, v232, v233\n" \
  "v_mfma_f32_16x16x32_bf16 v[228:231], v[108:111], a[100:103], v[228:231]\n" \
  "v_cvt_pk_bf16_f32 v253, v234, v235\n" \
  "v_mfma_f32_16x16x32_bf16 v[224:227], v[88:91], a[104:107], v[224:227]\n" \
  "v_cvt_pk_bf16_f32 v254, v236, v237\n" \
  "v_mfma_f32_16x16x32_bf16 v[228:231], v[112:115], a[104:107], v[228:231]\n" \
  "v_cvt_pk_bf16_f32 v255, v238, v239\n" \
  "v_mfma_f32_16x16x32_bf16 v[224:227], v[92:95], a[108:111], v[224:227]\n" \
  "s_add_u32 s72, s69, 196608\n" \
  "v_mfma_f32_16x16x32_bf16 v[228:231], v[116:119], a[108:111], v[228:231]\n" \
  "buffer_store_dwordx4 v[252:255], v3, s[36:39], s72 offen sc1\n" \
  "v_mfma_f32_16x16x32_bf16 v[224:227], v[96:99], a[112:115], v[224:227]\n" \
  "buffer_load_dword v11, v9, s[36:39], 0 offen sc1\n" \
  "v_mfma_f32_16x16x32_bf16 v[228:231], v[120:123], a[112:115], v[228:231]\n" \
  "v_mfma_f32_16x16x32_bf16 v[224:227], v[100:103], a[116:119], v[224:227]\n" \
  "v_mfma_f32_16x16x32_bf16 v[228:231], v[124:127], a[116:119], v[228:231]\n" \
  "v_mfma_f32_16x16x32_bf16 v[232:235], v[80:83], a[120:123], 0\n" \
  "v_mfma_f32_16x16x32_bf16 v[236:239], v[104:107], a[120:123], 0\n" \
  "v_mfma_f32_16x16x32_bf16 v[232:235], v[84:87], a[124:127], v[232:235]\n" \
  "v_cvt_pk_bf16_f32 v248, v224, v225\n" \
  "v_mfma_f32_16x16x32_bf16 v[236:239], v[108:111], a[124:127], v[236:239]\n" \
  "v_cvt_pk_bf16_f32 v249, v226, v227\n" \
  "v_mfma_f32_16x16x32_bf16 v[232:235], v[88:91], a[128:131], v[232:235]\n" \
  "v_cvt_pk_bf16_f32 v250, v228, v229\n" \
  "v_mfma_f32_16x16x32_bf16 v[236:239], v[112:115], a[128:131], v[236:239]\n" \
  "v_cvt_pk_bf16_f32 v251, v230, v231\n" \
  "v_mfma_f32_16x16x32_bf16 v[232:235], v[92:95], a[132:135], v[232:235]\n" \
  "s_add_u32 s72, s69, 262144\n" \
  "v_mfma_f32_16x16x32_bf16 v[236:239], v[116:119], a[132:135], v[236:239]\n" \
  "buffer_store_dwordx4 v[248:251], v3, s[36:39], s72 offen sc1\n" \
  "v_mfma_f32_16x16x32_bf16 v[232:235], v[96:99], a[136:139], v[232:235]\n" \
  "s_xor_b32 s73, s73, 0x8000\n" \
  "v_mfma_f32_16x16x32_bf16 v[236:239], v[120:123], a[136:139], v[236:239]\n" \
  "s_xor_b32 s74, s74, 0x8000\n" \
  "v_mfma_f32_16x16x32_bf16 v[232:235], v[100:103], a[140:143], v[232:235]\n" \
  "v_xor_b32 v12, 0x8000, v12\n" \
  "v_mfma_f32_16x16x32_bf16 v[236:239], v[124:127], a[140:143], v[236:239]\n" \
  "v_mfma_f32_16x16x32_bf16 v[224:227], v[80:83], a[144:147], 0\n" \
  "v_mfma_f32_16x16x32_bf16 v[228:231], v[104:107], a[144:147], 0\n" \
  "v_mfma_f32_16x16x32_bf16 v[224:227], v[84:87], a[148:151], v[224:227]\n" \
  "v_cvt_pk_bf16_f32 v252, v232, v233\n" \
  "v_mfma_f32_16x16x32_bf16 v[228:231], v[108:111], a[148:151], v[228:231]\n" \
  "v_cvt_pk_bf16_f32 v253, v234, v235\n" \
  "v_mfma_f32_16x16x32_bf16 v[224:227], v[88:91], a[152:155], v[224:227]\n" \
  "v_cvt_pk_bf16_f32 v254, v236, v237\n" \
  "v_mfma_f32_16x16x32_bf16 v[228:231], v[112:115], a[152:155], v[228:231]\n" \
  "v_cvt_pk_bf16_f32 v255, v238, v239\n" \
  "v_mfma_f32_16x16x32_bf16 v[224:227], v[92:95], a[156:159], v[224:227]\n" \
  "s_add_u32 s72, s69, 327680\n" \
  "v_mfma_f32_16x16x32_bf16 v[228:231], v[116:119], a[156:159], v[228:231]\n" \
  "buffer_store_dwordx4 v[252:255], v3, s[36:39], s72 offen sc1\n" \
  "v_mfma_f32_16x16x32_bf16 v[224:227], v[96:99], a[160:163], v[224:227]\n" \
  "v_mov_b32 v10, s52\n" \
  "v_mfma_f32_16x16x32_bf16 v[228:231], v[120:123], a[160:163], v[228:231]\n" \
  "s_waitcnt vmcnt(10)\n" \
  "v_mfma_f32_16x16x32_bf16 v[224:227], v[100:103], a[164:167], v[224:227]\n" \
  "buffer_store_dword v10, v7, s[36:39], 0 offen sc1\n" \
  "v_mfma_f32_16x16x32_bf16 v[228:231], v[124:127], a[164:167], v[228:231]\n" \
  "s_barrier\n" \
  "v_mfma_f32_16x16x32_bf16 v[232:235], v[80:83], a[168:171], 0\n" \
  "v_mfma_f32_16x16x32_bf16 v[236:239], v[104:107], a[168:171], 0\n" \
  "v_cvt_pk_bf16_f32 v248, v224, v225\n" \
  "v_cvt_pk_bf16_f32 v249, v226, v227\n" \
  "v_mfma_f32_16x16x32_bf16 v[232:235], v[84:87], a[172:175], v[232:235]\n" \
  "v_cvt_pk_bf16_f32 v250, v228, v229\n" \
  "v_cvt_pk_bf16_f32 v251, v230, v231\n" \
  "v_mfma_f32_16x16x32_bf16 v[236:239], v[108:111], a[172:175], v[236:239]\n" \
  "s_add_u32 s72, s69, 393216\n" \
  "v_mfma_f32_16x16x32_bf16 v[232:235], v[88:91], a[176:179], v[232:235]\n" \
  "buffer_store_dwordx4 v[248:251], v3, s[36:39], s72 offen sc1\n" \
  "v_mfma_f32_16x16x32_bf16 v[236:239], v[112:115], a[176:179], v[236:239]\n" \
  "ds_read_b128 v[32:35], v12 offset:0\n" \
  "v_mfma_f32_16x16x32_bf16 v[232:235], v[92:95], a[180:183], v[232:235]\n" \
  "ds_read_b128 v[56:59], v12 offset:1024\n" \
  "v_mfma_f32_16x16x32_bf16 v[236:239], v[116:119], a[180:183], v[236:239]\n" \
  "ds_read_b128 v[36:39], v12 offset:2048\n" \
  "v_mfma_f32_16x16x32_bf16 v[232:235], v[96:99], a[184:187], v[232:235]\n" \
  "ds_read_b128 v[60:63], v12 offset:3072\n" \
  "v_mfma_f32_16x16x32_bf16 v[236:239], v[120:123], a[184:187], v[236:239]\n" \
  "ds_read_b128 v[40:43], v12 offset:4096\n" \
  "v_mfma_f32_16x16x32_bf16 v[232:235], v[100:103], a[188:191], v[232:235]\n" \
  "ds_read_b128 v[64:67], v12 offset:5120\n" \
  "v_mfma_f32_16x16x32_bf16 v[236:239], v[124:127], a[188:191], v[236:239]\n" \
  "ds_read_b128 v[44:47], v12 offset:6144\n" \
  "v_mfma_f32_16x16x32_bf16 v[224:227], v[80:83], a[192:195], 0\n" \
  "v_mfma_f32_16x16x32_bf16 v[228:231], v[104:107], a[192:195], 0\n" \
  "v_cvt_pk_bf16_f32 v252, v232, v233\n" \
  "v_cvt_pk_bf16_f32 v253, v234, v235\n" \
  "v_mfma_f32_16x16x32_bf16 v[224:227], v[84:87], a[196:199], v[224:227]\n" \
  "v_cvt_pk_bf16_f32 v254, v236, v237\n" \
  "v_cvt_pk_bf16_f32 v255, v238, v239\n" \
  "v_mfma_f32_16x16x32_bf16 v[228:231], v[108:111], a[196:199], v[228:231]\n" \
  "s_add_u32 s72, s69, 458752\n" \
  "v_mfma_f32_16x16x32_bf16 v[224:227], v[88:91], a[200:203], v[224:227]\n" \
  "buffer_store_dwordx4 v[252:255], v3, s[36:39], s72 offen sc1\n" \
  "v_mfma_f32_16x16x32_bf16 v[228:231], v[112:115], a[200:203], v[228:231]\n" \
  "ds_read_b128 v[68:71], v12 offset:7168\n" \
  "v_mfma_f32_16x16x32_bf16 v[224:227], v[92:95], a[204:207], v[224:227]\n" \
  "ds_read_b64 v[48:49], v12 offset:8192\n" \
  "v_mfma_f32_16x16x32_bf16 v[228:231], v[116:119], a[204:207], v[228:231]\n" \
  "ds_read_b64 v[72:73], v12 offset:8200\n" \
  "v_mfma_f32_16x16x32_bf16 v[224:227], v[96:99], a[208:211], v[224:227]\n" \
  "ds_read_b64 v[50:51], v12 offset:9216\n" \
  "v_mfma_f32_16x16x32_bf16 v[228:231], v[120:123], a[208:211], v[228:231]\n" \
  "ds_read_b64 v[74:75], v12 offset:9224\n" \
  "v_mfma_f32_16x16x32_bf16 v[224:227], v[100:103], a[212:215], v[224:227]\n" \
  "ds_read_b64 v[52:53], v12 offset:10240\n" \
  "v_mfma_f32_16x16x32_bf16 v[228:231], v[124:127], a[212:215], v[228:231]\n" \
  "ds_read_b64 v[76:77], v12 offset:10248\n" \
  "v_mfma_f32_16x16x32_bf16 v[232:235], v[80:83], a[216:219], 0\n" \
  "v_mfma_f32_16x16x32_bf16 v[236:239], v[104:107], a[216:219], 0\n" \
  "v_mfma_f32_16x16x32_bf16 v[232:235], v[84:87], a[220:223], v[232:235]\n" \
  "v_cvt_pk_bf16_f32 v248, v224, v225\n" \
  "v_mfma_f32_16x16x32_bf16 v[236:239], v[108:111], a[220:223], v[236:239]\n" \
  "v_cvt_pk_bf16_f32 v249, v226, v227\n" \
  "v_mfma_f32_16x16x32_bf16 v[232:235], v[88:91], a[224:227], v[232:235]\n" \
  "v_cvt_pk_bf16_f32 v250, v228, v229\n" \
  "v_mfma_f32_16x16x32_bf16 v[236:239], v[112:115], a[224:227], v[236:239]\n" \
  "v_cvt_pk_bf16_f32 v251, v230, v231\n" \
  "v_mfma_f32_16x16x32_bf16 v[232:235], v[92:95], a[228:231], v[232:235]\n" \
  "s_add_u32 s72, s69, 524288\n" \
  "v_mfma_f32_16x16x32_bf16 v[236:239], v[116:119], a[228:231], v[236:239]\n" \
  "buffer_store_dwordx4 v[248:251], v3, s[36:39], s72 offen sc1\n" \
  "v_mfma_f32_16x16x32_bf16 v[232:235], v[96:99], a[232:235], v[232:235]\n" \
  "ds_read_b64 v[54:55], v12 offset:11264\n" \
  "v_mfma_f32_16x16x32_bf16 v[236:239], v[120:123], a[232:235], v[236:239]\n" \
  "ds_read_b64 v[78:79], v12 offset:11272\n" \
  "v_mfma_f32_16x16x32_bf16 v[232:235], v[100:103], a[236:239], v[232:235]\n" \
  "s_add_u32 s52, s52, 1\n" \
  "v_mfma_f32_16x16x32_bf16 v[236:239], v[124:127], a[236:239], v[236:239]\n" \
  "s_cmp_eq_u32 s52, s53\n" \
  "s_waitcnt lgkmcnt(0)\n" \
  "s_cbranch_scc0 LVL_%=\n" \
  "s_mov_b32 s77, s69\n" \
  "s_nop 7\n" \
  "v_cvt_pk_bf16_f32 v252, v232, v233\n" \
  "v_cvt_pk_bf16_f32 v253, v234, v235\n" \
  "v_cvt_pk_bf16_f32 v254, v236, v237\n" \
  "v_cvt_pk_bf16_f32 v255, v238, v239\n" \
  "s_add_u32 s72, s77, 589824\n" \
  "buffer_store_dwordx4 v[252:255], v3, s[36:39], s72 offen sc1\n" \
  "v_mov_b32 v10, s52\n" \
  "s_waitcnt vmcnt(0)\n" \
  "buffer_store_dword v10, v7, s[36:39], 0 offen sc1\n" \
  "s_branch LVD_%=\n" \
  "LVP0_%=:\n" \
  "buffer_load_dword v11, v9, s[36:39], 0 offen sc1\n" \
  "s_waitcnt vmcnt(0)\n" \
  "v_cmp_ge_u32 vcc, s76, v11\n" \
  "s_nop 3\n" \
  "s_cbranch_vccz LVK0_%=\n" \
  "s_add_u32 s75, s75, 1\n" \
  "s_cmp_gt_u32 s75, 200000\n" \
  "s_cbranch_scc1 LVK0_%=\n" \
  "s_sleep 2\n" \
  "s_branch LVP0_%=\n" \
  "LVP1_%=:\n" \
  "buffer_load_dword v11, v9, s[36:39], 0 offen sc1\n" \
  "s_waitcnt vmcnt(0)\n" \
  "v_cmp_ge_u32 vcc, s76, v11\n" \
  "s_nop 3\n" \
  "s_cbranch_vccz LVK1_%=\n" \
  "s_add_u32 s75, s75, 1\n" \
  "s_cmp_gt_u32 s75, 200000\n" \
  "s_cbranch_scc1 LVK1_%=\n" \
  "s_sleep 2\n" \
  "s_branch LVP1_%=\n" \
  "LVD_%=:\n"
#define UG_CLOBBERS \
  "v32", "v33", "v34", "v35", "v36", "v37", "v38", "v39", "v40", "v41", "v42", "v43", "v44", "v45", "v46", "v47", "v48", "v49", "v50", "v51", "v52", \
  "v53", "v54", "v55", "v56", "v57", "v58", "v59", "v60", "v61", "v62", "v63", "v64", "v65", "v66", "v67", "v68", "v69", "v70", "v71", "v72", "v73", \
  "v74", "v75", "v76", "v77", "v78", "v79", "v80", "v81", "v82", "v83", "v84", "v85", "v86", "v87", "v88", "v89", "v90", "v91", "v92", "v93", "v94", \
  "v95", "v96", "v97", "v98", "v99", "v100", "v101", "v102", "v103", "v104", "v105", "v106", "v107", "v108", "v109", "v110", "v111", "v112", "v113", \
  "v114", "v115", "v116", "v117", "v118", "v119", "v120", "v121", "v122", "v123", "v124", "v125", "v126", "v127", "v128", "v129", "v130", "v131", \
  "v132", "v133", "v134", "v135", "v136", "v137", "v138", "v139", "v140", "v141", "v142", "v143", "v144", "v145", "v146", "v147", "v148", "v149", \
  "v150", "v151", "v152", "v153", "v154", "v155", "v156", "v157", "v158", "v159", "v160", "v161", "v162", "v163", "v164", "v165", "v166", "v167", \
  "v168", "v169", "v170", "v171", "v172", "v173", "v174", "v175", "v176", "v177", "v178", "v179", "v180", "v181", "v182", "v183", "v184", "v185", \
  "v186", "v187", "v188", "v189", "v190", "v191", "v192", "v193", "v194", "v195", "v196", "v197", "v198", "v199", "v200", "v201", "v202", "v203", \
  "v204", "v205", "v206", "v207", "s68", "s69", "s70", "s71", "s72", "s73", "s74", "s75", "s76", "s77"
#define UG_ASM \
  "s_mul_i32 s74, s52, 21504\n" \
  "s_mul_i32 s73, s52, 61440\n" \
  "s_add_u32 s77, s73, 61440\n" \
  "LUW_%=:\n" \
  "s_add_u32 m0, s74, 0\n" \
  "s_nop 0\n" \
  "buffer_load_dwordx4 v0, s[44:47], s73 offen offset:0 lds\n" \
  "buffer_load_dwordx4 v1, s[44:47], s73 offen offset:1024 lds\n" \
  "buffer_load_dwordx4 v2, s[44:47], s73 offen offset:2048 lds\n" \
  "buffer_load_dwordx4 v3, s[44:47], s73 offen offset:3072 lds\n" \
  "s_add_u32 m0, s74, 4096\n" \
  "s_nop 0\n" \
  "buffer_load_dwordx4 v4, s[44:47], s73 offen offset:0 lds\n" \
  "buffer_load_dwordx4 v5, s[44:47], s73 offen offset:1024 lds\n" \
  "buffer_load_dwordx4 v6, s[44:47], s73 offen offset:2048 lds\n" \
  "s_add_u32 s74, s74, 7168\n" \
  "s_add_u32 s73, s73, 20480\n" \
  "s_cmp_lg_u32 s73, s77\n" \
  "s_cbranch_scc1 LUW_%=\n" \
  "s_and_b32 s73, s52, 1\n" \
  "s_lshr_b32 s74, s52, 1\n" \
  "s_mul_i32 s68, s73, 10\n" \
  "s_add_u32 s77, s68, 10\n" \
  "s_add_u32 s75, s68, s74\n" \
  "s_mul_i32 s75, s75, 393216\n" \
  "s_lshl_b32 s73, s73, 1\n" \
  "s_add_u32 s73, s73, s74\n" \
  "s_mul_i32 s76, s73, 12288\n" \
  "s_add_u32 s76, s76, 86016\n" \
  "s_add_u32 m0, s76, 0\n" \
  "s_nop 0\n" \
  "buffer_load_dwordx4 v8, s[40:43], s75 offen offset:0 lds\n" \
  "buffer_load_dwordx4 v9, s[40:43], s75 offen offset:1024 lds\n" \
  "buffer_load_dwordx4 v10, s[40:43], s75 offen offset:2048 lds\n" \
  "buffer_load_dwordx4 v11, s[40:43], s75 offen offset:3072 lds\n" \
  "s_add_u32 m0, s76, 4096\n" \
  "s_nop 0\n" \
  "buffer_load_dwordx4 v12, s[40:43], s75 offen offset:0 lds\n" \
  "buffer_load_dwordx4 v13, s[40:43], s75 offen offset:1024 lds\n" \
  "buffer_load_dwordx4 v14, s[40:43], s75 offen offset:2048 lds\n" \
  "buffer_load_dwordx4 v15, s[40:43], s75 offen offset:3072 lds\n" \
  "s_add_u32 m0, s76, 8192\n" \
  "s_nop 0\n" \
  "buffer_load_dwordx4 v16, s[40:43], s75 offen offset:0 lds\n" \
  "buffer_load_dwordx4 v17, s[40:43], s75 offen offset:1024 lds\n" \
  "buffer_load_dwordx4 v18, s[40:43], s75 offen offset:2048 lds\n" \
  "buffer_load_dwordx4 v19, s[40:43], s75 offen offset:3072 lds\n" \
  "s_add_u32 s75, s75, 786432\n" \
  "v_add_u32 v27, 43008, v7\n" \
  "v_add_u32 v28, 43008, v26\n" \
  "v_mov_b32 v0, 1\n" \
  "s_waitcnt vmcnt(0)\n" \
  "s_barrier\n" \
  "ds_read_b128 v[32:35], v20 offset:0\n" \
  "ds_read_b128 v[56:59], v20 offset:768\n" \
  "ds_read_b128 v[80:83], v20 offset:12288\n" \
  "ds_read_b128 v[104:107], v20 offset:13056\n" \
  "ds_read_b64_tr_b16 v[128:129], v7 offset:0\n" \
  "ds_read_b64_tr_b16 v[130:131], v7 offset:1792\n" \
  "ds_read_b128 v[36:39], v21 offset:0\n" \
  "ds_read_b128 v[60:63], v21 offset:768\n" \
  "ds_read_b128 v[84:87], v21 offset:12288\n" \
  "ds_read_b128 v[108:111], v21 offset:13056\n" \
  "ds_read_b64_tr_b16 v[132:133], v7 offset:14336\n" \
  "ds_read_b64_tr_b16 v[134:135], v7 offset:16128\n" \
  "s_waitcnt lgkmcnt(2)\n" \
  "ds_read_b128 v[40:43], v20 offset:128\n" \
  "ds_read_b128 v[64:67], v20 offset:896\n" \
  "ds_read_b128 v[88:91], v20 offset:12416\n" \
  "ds_read_b128 v[112:115], v20 offset:13184\n" \
  "ds_read_b64_tr_b16 v[136:137], v7 offset:28672\n" \
  "ds_read_b64_tr_b16 v[138:139], v7 offset:30464\n" \
  "ds_read_b128 v[44:47], v21 offset:128\n" \
  "ds_read_b128 v[68:71], v21 offset:896\n" \
  "ds_read_b128 v[92:95], v21 offset:12416\n" \
  "ds_read_b128 v[116:119], v21 offset:13184\n" \
  "ds_read_b64_tr_b16 v[140:141], v27 offset:0\n" \
  "ds_read_b64_tr_b16 v[142:143], v27 offset:1792\n" \
  "s_waitcnt lgkmcnt(2)\n" \
  "ds_read_b128 v[48:51], v20 offset:256\n" \
  "ds_read_b128 v[72:75], v20 offset:1024\n" \
  "ds_read_b128 v[96:99], v20 offset:12544\n" \
  "ds_read_b128 v[120:123], v20 offset:13312\n" \
  "ds_read_b64_tr_b16 v[144:145], v27 offset:14336\n" \
  "ds_read_b64_tr_b16 v[146:147], v27 offset:16128\n" \
  "ds_read_b128 v[52:55], v21 offset:256\n" \
  "ds_read_b128 v[76:79], v21 offset:1024\n" \
  "ds_read_b128 v[100:103], v21 offset:12544\n" \
  "ds_read_b128 v[124:127], v21 offset:13312\n" \
  "s_waitcnt lgkmcnt(2)\n" \
  "s_mov_b32 s71, 0x40000000\n" \
  "s_mov_b32 s72, 0x40000000\n" \
  "s_waitcnt lgkmcnt(0)\n" \
  "LUR_%=:\n" \
  "v_mfma_f32_16x16x32_bf16 v[152:155], v[32:35], v[128:131], 0\n" \
  "ds_read_b64_tr_b16 v[148:149], v27 offset:28672\n" \
  "v_mfma_f32_16x16x32_bf16 v[156:159], v[56:59], v[128:131], 0\n" \
  "s_waitcnt lgkmcnt(8)\n" \
  "ds_read_b64_tr_b16 v[150:151], v27 offset:30464\n" \
  "v_mfma_f32_16x16x32_bf16 v[160:163], v[80:83], v[128:131], 0\n" \
  "v_cvt_pk_bf16_f32 v200, v184, v185\n" \
  "v_cvt_pk_bf16_f32 v201, v186, v187\n" \
  "v_mfma_f32_16x16x32_bf16 v[164:167], v[104:107], v[128:131], 0\n" \
  "v_cvt_pk_bf16_f32 v202, v188, v189\n" \
  "v_cvt_pk_bf16_f32 v203, v190, v191\n" \
  "v_mfma_f32_16x16x32_bf16 v[152:155], v[36:39], v[132:135], v[152:155]\n" \
  "ds_read_b64_tr_b16 v[128:129], v26 offset:0\n" \
  "v_mfma_f32_16x16x32_bf16 v[156:159], v[60:63], v[132:135], v[156:159]\n" \
  "ds_read_b64_tr_b16 v[130:131], v26 offset:1792\n" \
  "v_mfma_f32_16x16x32_bf16 v[160:163], v[84:87], v[132:135], v[160:163]\n" \
  "s_add_u32 s73, s71, 393216\n" \
  "v_mfma_f32_16x16x32_bf16 v[164:167], v[108:111], v[132:135], v[164:167]\n" \
  "buffer_store_dwordx4 v[200:203], v22, s[36:39], s73 offen sc1\n" \
  "v_mfma_f32_16x16x32_bf16 v[152:155], v[40:43], v[136:139], v[152:155]\n" \
  "ds_read_b64_tr_b16 v[132:133], v26 offset:14336\n" \
  "v_mfma_f32_16x16x32_bf16 v[156:159], v[64:67], v[136:139], v[156:159]\n" \
  "ds_read_b64_tr_b16 v[134:135], v26 offset:16128\n" \
  "v_mfma_f32_16x16x32_bf16 v[160:163], v[88:91], v[136:139], v[160:163]\n" \
  "v_cvt_pk_bf16_f32 v204, v192, v193\n" \
  "v_cvt_pk_bf16_f32 v205, v194, v195\n" \
  "v_mfma_f32_16x16x32_bf16 v[164:167], v[112:115], v[136:139], v[164:167]\n" \
  "v_cvt_pk_bf16_f32 v206, v196, v197\n" \
  "v_cvt_pk_bf16_f32 v207, v198, v199\n" \
  "v_mfma_f32_16x16x32_bf16 v[152:155], v[44:47], v[140:143], v[152:155]\n" \
  "ds_read_b64_tr_b16 v[136:137], v26 offset:28672\n" \
  "v_mfma_f32_16x16x32_bf16 v[156:159], v[68:71], v[140:143], v[156:159]\n" \
  "ds_read_b64_tr_b16 v[138:139], v26 offset:30464\n" \
  "v_mfma_f32_16x16x32_bf16 v[160:163], v[92:95], v[140:143], v[160:163]\n" \
  "s_add_u32 s73, s72, 393216\n" \
  "v_mfma_f32_16x16x32_bf16 v[164:167], v[116:119], v[140:143], v[164:167]\n" \
  "buffer_store_dwordx4 v[204:207], v22, s[36:39], s73 offen sc1\n" \
  "s_waitcnt lgkmcnt(10)\n" \
  "v_mfma_f32_16x16x32_bf16 v[152:155], v[48:51], v[144:147], v[152:155]\n" \
  "ds_read_b64_tr_b16 v[140:141], v28 offset:0\n" \
  "v_mfma_f32_16x16x32_bf16 v[156:159], v[72:75], v[144:147], v[156:159]\n" \
  "ds_read_b64_tr_b16 v[142:143], v28 offset:1792\n" \
  "v_mfma_f32_16x16x32_bf16 v[160:163], v[96:99], v[144:147], v[160:163]\n" \
  "v_readlane_b32 s69, v23, s68\n" \
  "v_readlane_b32 s71, v24, s68\n" \
  "s_add_u32 s68, s68, 1\n" \
  "v_mfma_f32_16x16x32_bf16 v[164:167], v[120:123], v[144:147], v[164:167]\n" \
  "v_readlane_b32 s70, v23, s68\n" \
  "v_readlane_b32 s72, v24, s68\n" \
  "s_add_u32 s68, s68, 1\n" \
  "s_waitcnt lgkmcnt(8)\n" \
  "v_mfma_f32_16x16x32_bf16 v[152:155], v[52:55], v[148:151], v[152:155]\n" \
  "ds_read_b64_tr_b16 v[144:145], v28 offset:14336\n" \
  "v_mfma_f32_16x16x32_bf16 v[156:159], v[76:79], v[148:151], v[156:159]\n" \
  "ds_read_b64_tr_b16 v[146:147], v28 offset:16128\n" \
  "v_mfma_f32_16x16x32_bf16 v[160:163], v[100:103], v[148:151], v[160:163]\n" \
  "s_cmp_eq_u32 s68, s77\n" \
  "s_cselect_b32 s75, 0x40000000, s75\n" \
  "v_mfma_f32_16x16x32_bf16 v[164:167], v[124:127], v[148:151], v[164:167]\n" \
  "s_barrier\n" \
  "s_waitcnt lgkmcnt(8)\n" \
  "v_mfma_f32_16x16x32_bf16 v[168:171], v[32:35], v[128:131], 0\n" \
  "ds_read_b64_tr_b16 v[148:149], v28 offset:28672\n" \
  "v_mfma_f32_16x16x32_bf16 v[172:175], v[56:59], v[128:131], 0\n" \
  "ds_read_b64_tr_b16 v[150:151], v28 offset:30464\n" \
  "v_mfma_f32_16x16x32_bf16 v[176:179], v[80:83], v[128:131], 0\n" \
  "v_cvt_pk_bf16_f32 v200, v152, v153\n" \
  "v_cvt_pk_bf16_f32 v201, v154, v155\n" \
  "v_mfma_f32_16x16x32_bf16 v[180:183], v[104:107], v[128:131], 0\n" \
  "s_add_u32 m0, s76, 0\n" \
  "s_nop 0\n" \
  "buffer_load_dwordx4 v8, s[40:43], s75 offen offset:0 lds\n" \
  "s_waitcnt lgkmcnt(8)\n" \
  "v_mfma_f32_16x16x32_bf16 v[168:171], v[36:39], v[132:135], v[168:171]\n" \
  "ds_read_b64_tr_b16 v[128:129], v7 offset:64\n" \
  "v_mfma_f32_16x16x32_bf16 v[172:175], v[60:63], v[132:135], v[172:175]\n" \
  "ds_read_b64_tr_b16 v[130:131], v7 offset:1856\n" \
  "v_mfma_f32_16x16x32_bf16 v[176:179], v[84:87], v[132:135], v[176:179]\n" \
  "v_cvt_pk_bf16_f32 v202, v156, v157\n" \
  "v_cvt_pk_bf16_f32 v203, v158, v159\n" \
  "v_mfma_f32_16x16x32_bf16 v[180:183], v[108:111], v[132:135], v[180:183]\n" \
  "s_add_u32 s73, s69, 0\n" \
  "s_waitcnt lgkmcnt(8)\n" \
  "v_mfma_f32_16x16x32_bf16 v[168:171], v[40:43], v[136:139], v[168:171]\n" \
  "ds_read_b64_tr_b16 v[132:133], v7 offset:14400\n" \
  "v_mfma_f32_16x16x32_bf16 v[172:175], v[64:67], v[136:139], v[172:175]\n" \
  "ds_read_b64_tr_b16 v[134:135], v7 offset:16192\n" \
  "v_mfma_f32_16x16x32_bf16 v[176:179], v[88:91], v[136:139], v[176:179]\n" \
  "buffer_load_dwordx4 v9, s[40:43], s75 offen offset:1024 lds\n" \
  "v_mfma_f32_16x16x32_bf16 v[180:183], v[112:115], v[136:139], v[180:183]\n" \
  "buffer_store_dwordx4 v[200:203], v22, s[36:39], s73 offen sc1\n" \
  "s_waitcnt lgkmcnt(8)\n" \
  "v_mfma_f32_16x16x32_bf16 v[168:171], v[44:47], v[140:143], v[168:171]\n" \
  "ds_read_b64_tr_b16 v[136:137], v7 offset:28736\n" \
  "v_mfma_f32_16x16x32_bf16 v[172:175], v[68:71], v[140:143], v[172:175]\n" \
  "ds_read_b64_tr_b16 v[138:139], v7 offset:30528\n" \
  "v_mfma_f32_16x16x32_bf16 v[176:179], v[92:95], v[140:143], v[176:179]\n" \
  "v_cvt_pk_bf16_f32 v204, v160, v161\n" \
  "v_cvt_pk_bf16_f32 v205, v162, v163\n" \
  "v_mfma_f32_16x16x32_bf16 v[180:183], v[116:119], v[140:143], v[180:183]\n" \
  "buffer_load_dwordx4 v10, s[40:43], s75 offen offset:2048 lds\n" \
  "s_waitcnt lgkmcnt(8)\n" \
  "v_mfma_f32_16x16x32_bf16 v[168:171], v[48:51], v[144:147], v[168:171]\n" \
  "ds_read_b64_tr_b16 v[140:141], v27 offset:64\n" \
  "v_mfma_f32_16x16x32_bf16 v[172:175], v[72:75], v[144:147], v[172:175]\n" \
  "ds_read_b64_tr_b16 v[142:143], v27 offset:1856\n" \
  "v_mfma_f32_16x16x32_bf16 v[176:179], v[96:99], v[144:147], v[176:179]\n" \
  "v_cvt_pk_bf16_f32 v206, v164, v165\n" \
  "v_cvt_pk_bf16_f32 v207, v166, v167\n" \
  "v_mfma_f32_16x16x32_bf16 v[180:183], v[120:123], v[144:147], v[180:183]\n" \
  "s_add_u32 s73, s70, 0\n" \
  "s_waitcnt lgkmcnt(8)\n" \
  "v_mfma_f32_16x16x32_bf16 v[168:171], v[52:55], v[148:151], v[168:171]\n" \
  "ds_read_b64_tr_b16 v[144:145], v27 offset:14400\n" \
  "v_mfma_f32_16x16x32_bf16 v[172:175], v[76:79], v[148:151], v[172:175]\n" \
  "ds_read_b64_tr_b16 v[146:147], v27 offset:16192\n" \
  "v_mfma_f32_16x16x32_bf16 v[176:179], v[100:103], v[148:151], v[176:179]\n" \
  "buffer_load_dwordx4 v11, s[40:43], s75 offen offset:3072 lds\n" \
  "v_mfma_f32_16x16x32_bf16 v[180:183], v[124:127], v[148:151], v[180:183]\n" \
  "buffer_store_dwordx4 v[204:207], v22, s[36:39], s73 offen sc1\n" \
  "s_waitcnt lgkmcnt(8)\n" \
  "v_mfma_f32_16x16x32_bf16 v[152:155], v[32:35], v[128:131], 0\n" \
  "ds_read_b64_tr_b16 v[148:149], v27 offset:28736\n" \
  "v_mfma_f32_16x16x32_bf16 v[156:159], v[56:59], v[128:131], 0\n" \
  "ds_read_b64_tr_b16 v[150:151], v27 offset:30528\n" \
  "v_mfma_f32_16x16x32_bf16 v[160:163], v[80:83], v[128:131], 0\n" \
  "v_cvt_pk_bf16_f32 v200, v168, v169\n" \
  "v_cvt_pk_bf16_f32 v201, v170, v171\n" \
  "v_mfma_f32_16x16x32_bf16 v[164:167], v[104:107], v[128:131], 0\n" \
  "s_add_u32 m0, s76, 4096\n" \
  "s_nop 0\n" \
  "buffer_load_dwordx4 v12, s[40:43], s75 offen offset:0 lds\n" \
  "s_waitcnt lgkmcnt(8)\n" \
  "v_mfma_f32_16x16x32_bf16 v[152:155], v[36:39], v[132:135], v[152:155]\n" \
  "ds_read_b64_tr_b16 v[128:129], v26 offset:64\n" \
  "v_mfma_f32_16x16x32_bf16 v[156:159], v[60:63], v[132:135], v[156:159]\n" \
  "ds_read_b64_tr_b16 v[130:131], v26 offset:1856\n" \
  "v_mfma_f32_16x16x32_bf16 v[160:163], v[84:87], v[132:135], v[160:163]\n" \
  "v_cvt_pk_bf16_f32 v202, v172, v173\n" \
  "v_cvt_pk_bf16_f32 v203, v174, v175\n" \
  "v_mfma_f32_16x16x32_bf16 v[164:167], v[108:111], v[132:135], v[164:167]\n" \
  "s_add_u32 s73, s69, 65536\n" \
  "s_waitcnt lgkmcnt(8)\n" \
  "v_mfma_f32_16x16x32_bf16 v[152:155], v[40:43], v[136:139], v[152:155]\n" \
  "ds_read_b64_tr_b16 v[132:133], v26 offset:14400\n" \
  "v_mfma_f32_16x16x32_bf16 v[156:159], v[64:67], v[136:139], v[156:159]\n" \
  "ds_read_b64_tr_b16 v[134:135], v26 offset:16192\n" \
  "v_mfma_f32_16x16x32_bf16 v[160:163], v[88:91], v[136:139], v[160:163]\n" \
  "buffer_load_dwordx4 v13, s[40:43], s75 offen offset:1024 lds\n" \
  "v_mfma_f32_16x16x32_bf16 v[164:167], v[112:115], v[136:139], v[164:167]\n" \
  "buffer_store_dwordx4 v[200:203], v22, s[36:39], s73 offen sc1\n" \
  "s_waitcnt lgkmcnt(8)\n" \
  "v_mfma_f32_16x16x32_bf16 v[152:155], v[44:47], v[140:143], v[152:155]\n" \
  "ds_read_b64_tr_b16 v[136:137], v26 offset:28736\n" \
  "v_mfma_f32_16x16x32_bf16 v[156:159], v[68:71], v[140:143], v[156:159]\n" \
  "ds_read_b64_tr_b16 v[138:139], v26 offset:30528\n" \
  "v_mfma_f32_16x16x32_bf16 v[160:163], v[92:95], v[140:143], v[160:163]\n" \
  "v_cvt_pk_bf16_f32 v204, v176, v177\n" \
  "v_cvt_pk_bf16_f32 v205, v178, v179\n" \
  "v_mfma_f32_16x16x32_bf16 v[164:167], v[116:119], v[140:143], v[164:167]\n" \
  "buffer_load_dwordx4 v14, s[40:43], s75 offen offset:2048 lds\n" \
  "s_waitcnt lgkmcnt(8)\n" \
  "v_mfma_f32_16x16x32_bf16 v[152:155], v[48:51], v[144:147], v[152:155]\n" \
  "ds_read_b64_tr_b16 v[140:141], v28 offset:64\n" \
  "v_mfma_f32_16x16x32_bf16 v[156:159], v[72:75], v[144:147], v[156:159]\n" \
  "ds_read_b64_tr_b16 v[142:143], v28 offset:1856\n" \
  "v_mfma_f32_16x16x32_bf16 v[160:163], v[96:99], v[144:147], v[160:163]\n" \
  "v_cvt_pk_bf16_f32 v206, v180, v181\n" \
  "v_cvt_pk_bf16_f32 v207, v182, v183\n" \
  "v_mfma_f32_16x16x32_bf16 v[164:167], v[120:123], v[144:147], v[164:167]\n" \
  "s_add_u32 s73, s70, 65536\n" \
  "s_waitcnt lgkmcnt(8)\n" \
  "v_mfma_f32_16x16x32_bf16 v[152:155], v[52:55], v[148:151], v[152:155]\n" \
  "ds_read_b64_tr_b16 v[144:145], v28 offset:14400\n" \
  "v_mfma_f32_16x16x32_bf16 v[156:159], v[76:79], v[148:151], v[156:159]\n" \
  "ds_read_b64_tr_b16 v[146:147], v28 offset:16192\n" \
  "v_mfma_f32_16x16x32_bf16 v[160:163], v[100:103], v[148:151], v[160:163]\n" \
  "buffer_load_dwordx4 v15, s[40:43], s75 offen offset:3072 lds\n" \
  "v_mfma_f32_16x16x32_bf16 v[164:167], v[124:127], v[148:151], v[164:167]\n" \
  "buffer_store_dwordx4 v[204:207], v22, s[36:39], s73 offen sc1\n" \
  "s_waitcnt lgkmcnt(8)\n" \
  "v_mfma_f32_16x16x32_bf16 v[168:171], v[32:35], v[128:131], 0\n" \
  "ds_read_b64_tr_b16 v[148:149], v28 offset:28736\n" \
  "v_mfma_f32_16x16x32_bf16 v[172:175], v[56:59], v[128:131], 0\n" \
  "ds_read_b64_tr_b16 v[150:151], v28 offset:30528\n" \
  "v_mfma_f32_16x16x32_bf16 v[176:179], v[80:83], v[128:131], 0\n" \
  "v_cvt_pk_bf16_f32 v200, v152, v153\n" \
  "v_cvt_pk_bf16_f32 v201, v154, v155\n" \
  "v_mfma_f32_16x16x32_bf16 v[180:183], v[104:107], v[128:131], 0\n" \
  "s_add_u32 m0, s76, 8192\n" \
  "s_nop 0\n" \
  "buffer_load_dwordx4 v16, s[40:43], s75 offen offset:0 lds\n" \
  "s_waitcnt lgkmcnt(8)\n" \
  "v_mfma_f32_16x16x32_bf16 v[168:171], v[36:39], v[132:135], v[168:171]\n" \
  "ds_read_b64_tr_b16 v[128:129], v7 offset:128\n" \
  "v_mfma_f32_16x16x32_bf16 v[172:175], v[60:63], v[132:135], v[172:175]\n" \
  "ds_read_b64_tr_b16 v[130:131], v7 offset:1920\n" \
  "v_mfma_f32_16x16x32_bf16 v[176:179], v[84:87], v[132:135], v[176:179]\n" \
  "v_cvt_pk_bf16_f32 v202, v156, v157\n" \
  "v_cvt_pk_bf16_f32 v203, v158, v159\n" \
  "v_mfma_f32_16x16x32_bf16 v[180:183], v[108:111], v[132:135], v[180:183]\n" \
  "s_add_u32 s73, s69, 131072\n" \
  "s_waitcnt lgkmcnt(8)\n" \
  "v_mfma_f32_16x16x32_bf16 v[168:171], v[40:43], v[136:139], v[168:171]\n" \
  "ds_read_b64_tr_b16 v[132:133], v7 offset:14464\n" \
  "v_mfma_f32_16x16x32_bf16 v[172:175], v[64:67], v[136:139], v[172:175]\n" \
  "ds_read_b64_tr_b16 v[134:135], v7 offset:16256\n" \
  "v_mfma_f32_16x16x32_bf16 v[176:179], v[88:91], v[136:139], v[176:179]\n" \
  "buffer_load_dwordx4 v17, s[40:43], s75 offen offset:1024 lds\n" \
  "v_mfma_f32_16x16x32_bf16 v[180:183], v[112:115], v[136:139], v[180:183]\n" \
  "buffer_store_dwordx4 v[200:203], v22, s[36:39], s73 offen sc1\n" \
  "s_waitcnt lgkmcnt(8)\n" \
  "v_mfma_f32_16x16x32_bf16 v[168:171], v[44:47], v[140:143], v[168:171]\n" \
  "ds_read_b64_tr_b16 v[136:137], v7 offset:28800\n" \
  "v_mfma_f32_16x16x32_bf16 v[172:175], v[68:71], v[140:143], v[172:175]\n" \
  "ds_read_b64_tr_b16 v[138:139], v7 offset:30592\n" \
  "v_mfma_f32_16x16x32_bf16 v[176:179], v[92:95], v[140:143], v[176:179]\n" \
  "v_cvt_pk_bf16_f32 v204, v160, v161\n" \
  "v_cvt_pk_bf16_f32 v205, v162, v163\n" \
  "v_mfma_f32_16x16x32_bf16 v[180:183], v[116:119], v[140:143], v[180:183]\n" \
  "buffer_load_dwordx4 v18, s[40:43], s75 offen offset:2048 lds\n" \
  "s_waitcnt lgkmcnt(8)\n" \
  "v_mfma_f32_16x16x32_bf16 v[168:171], v[48:51], v[144:147], v[168:171]\n" \
  "ds_read_b64_tr_b16 v[140:141], v27 offset:128\n" \
  "v_mfma_f32_16x16x32_bf16 v[172:175], v[72:75], v[144:147], v[172:175]\n" \
  "ds_read_b64_tr_b16 v[142:143], v27 offset:1920\n" \
  "v_mfma_f32_16x16x32_bf16 v[176:179], v[96:99], v[144:147], v[176:179]\n" \
  "v_cvt_pk_bf16_f32 v206, v164, v165\n" \
  "v_cvt_pk_bf16_f32 v207, v166, v167\n" \
  "v_mfma_f32_16x16x32_bf16 v[180:183], v[120:123], v[144:147], v[180:183]\n" \
  "s_add_u32 s73, s70, 131072\n" \
  "s_waitcnt lgkmcnt(8)\n" \
  "v_mfma_f32_16x16x32_bf16 v[168:171], v[52:55], v[148:151], v[168:171]\n" \
  "ds_read_b64_tr_b16 v[144:145], v27 offset:14464\n" \
  "v_mfma_f32_16x16x32_bf16 v[172:175], v[76:79], v[148:151], v[172:175]\n" \
  "ds_read_b64_tr_b16 v[146:147], v27 offset:16256\n" \
  "v_mfma_f32_16x16x32_bf16 v[176:179], v[100:103], v[148:151], v[176:179]\n" \
  "buffer_load_dwordx4 v19, s[40:43], s75 offen offset:3072 lds\n" \
  "s_add_u32 s75, s75, 786432\n" \
  "v_mfma_f32_16x16x32_bf16 v[180:183], v[124:127], v[148:151], v[180:183]\n" \
  "buffer_store_dwordx4 v[204:207], v22, s[36:39], s73 offen sc1\n" \
  "s_waitcnt lgkmcnt(8)\n" \
  "v_mfma_f32_16x16x32_bf16 v[152:155], v[32:35], v[128:131], 0\n" \
  "ds_read_b64_tr_b16 v[148:149], v27 offset:28800\n" \
  "v_mfma_f32_16x16x32_bf16 v[156:159], v[56:59], v[128:131], 0\n" \
  "ds_read_b64_tr_b16 v[150:151], v27 offset:30592\n" \
  "v_mfma_f32_16x16x32_bf16 v[160:163], v[80:83], v[128:131], 0\n" \
  "v_cvt_pk_bf16_f32 v200, v168, v169\n" \
  "v_cvt_pk_bf16_f32 v201, v170, v171\n" \
  "v_mfma_f32_16x16x32_bf16 v[164:167], v[104:107], v[128:131], 0\n" \
  "v_cvt_pk_bf16_f32 v202, v172, v173\n" \
  "v_cvt_pk_bf16_f32 v203, v174, v175\n" \
  "s_waitcnt lgkmcnt(8)\n" \
  "v_mfma_f32_16x16x32_bf16 v[152:155], v[36:39], v[132:135], v[152:155]\n" \
  "ds_read_b64_tr_b16 v[128:129], v26 offset:128\n" \
  "v_mfma_f32_16x16x32_bf16 v[156:159], v[60:63], v[132:135], v[156:159]\n" \
  "ds_read_b64_tr_b16 v[130:131], v26 offset:1920\n" \
  "v_mfma_f32_16x16x32_bf16 v[160:163], v[84:87], v[132:135], v[160:163]\n" \
  "s_add_u32 s73, s69, 196608\n" \
  "v_mfma_f32_16x16x32_bf16 v[164:167], v[108:111], v[132:135], v[164:167]\n" \
  "buffer_store_dwordx4 v[200:203], v22, s[36:39], s73 offen sc1\n" \
  "s_waitcnt lgkmcnt(8)\n" \
  "v_mfma_f32_16x16x32_bf16 v[152:155], v[40:43], v[136:139], v[152:155]\n" \
  "ds_read_b64_tr_b16 v[132:133], v26 offset:14464\n" \
  "v_mfma_f32_16x16x32_bf16 v[156:159], v[64:67], v[136:139], v[156:159]\n" \
  "ds_read_b64_tr_b16 v[134:135], v26 offset:16256\n" \
  "v_mfma_f32_16x16x32_bf16 v[160:163], v[88:91], v[136:139], v[160:163]\n" \
  "v_cvt_pk_bf16_f32 v204, v176, v177\n" \
  "v_cvt_pk_bf16_f32 v205, v178, v179\n" \
  "v_mfma_f32_16x16x32_bf16 v[164:167], v[112:115], v[136:139], v[164:167]\n" \
  "v_cvt_pk_bf16_f32 v206, v180, v181\n" \
  "v_cvt_pk_bf16_f32 v207, v182, v183\n" \
  "s_waitcnt lgkmcnt(8)\n" \
  "v_mfma_f32_16x16x32_bf16 v[152:155], v[44:47], v[140:143], v[152:155]\n" \
  "ds_read_b64_tr_b16 v[136:137], v26 offset:28800\n" \
  "v_mfma_f32_16x16x32_bf16 v[156:159], v[68:71], v[140:143], v[156:159]\n" \
  "ds_read_b64_tr_b16 v[138:139], v26 offset:30592\n" \
  "v_mfma_f32_16x16x32_bf16 v[160:163], v[92:95], v[140:143], v[160:163]\n" \
  "s_add_u32 s73, s70, 196608\n" \
  "v_mfma_f32_16x16x32_bf16 v[164:167], v[116:119], v[140:143], v[164:167]\n" \
  "buffer_store_dwordx4 v[204:207], v22, s[36:39], s73 offen sc1\n" \
  "s_waitcnt lgkmcnt(8)\n" \
  "v_mfma_f32_16x16x32_bf16 v[152:155], v[48:51], v[144:147], v[152:155]\n" \
  "ds_read_b64_tr_b16 v[140:141], v28 offset:128\n" \
  "v_mfma_f32_16x16x32_bf16 v[156:159], v[72:75], v[144:147], v[156:159]\n" \
  "ds_read_b64_tr_b16 v[142:143], v28 offset:1920\n" \
  "v_mfma_f32_16x16x32_bf16 v[160:163], v[96:99], v[144:147], v[160:163]\n" \
  "v_mfma_f32_16x16x32_bf16 v[164:167], v[120:123], v[144:147], v[164:167]\n" \
  "s_waitcnt lgkmcnt(8)\n" \
  "v_mfma_f32_16x16x32_bf16 v[152:155], v[52:55], v[148:151], v[152:155]\n" \
  "ds_read_b64_tr_b16 v[144:145], v28 offset:14464\n" \
  "v_mfma_f32_16x16x32_bf16 v[156:159], v[76:79], v[148:151], v[156:159]\n" \
  "ds_read_b64_tr_b16 v[146:147], v28 offset:16256\n" \
  "v_mfma_f32_16x16x32_bf16 v[160:163], v[100:103], v[148:151], v[160:163]\n" \
  "v_mfma_f32_16x16x32_bf16 v[164:167], v[124:127], v[148:151], v[164:167]\n" \
  "s_waitcnt lgkmcnt(8)\n" \
  "v_mfma_f32_16x16x32_bf16 v[168:171], v[32:35], v[128:131], 0\n" \
  "ds_read_b64_tr_b16 v[148:149], v28 offset:28800\n" \
  "v_mfma_f32_16x16x32_bf16 v[172:175], v[56:59], v[128:131], 0\n" \
  "ds_read_b64_tr_b16 v[150:151], v28 offset:30592\n" \
  "v_mfma_f32_16x16x32_bf16 v[176:179], v[80:83], v[128:131], 0\n" \
  "v_cvt_pk_bf16_f32 v200, v152, v153\n" \
  "v_cvt_pk_bf16_f32 v201, v154, v155\n" \
  "v_mfma_f32_16x16x32_bf16 v[180:183], v[104:107], v[128:131], 0\n" \
  "v_cvt_pk_bf16_f32 v202, v156, v157\n" \
  "v_cvt_pk_bf16_f32 v203, v158, v159\n" \
  "s_waitcnt lgkmcnt(8)\n" \
  "v_mfma_f32_16x16x32_bf16 v[168:171], v[36:39], v[132:135], v[168:171]\n" \
  "ds_read_b64_tr_b16 v[128:129], v7 offset:192\n" \
  "v_mfma_f32_16x16x32_bf16 v[172:175], v[60:63], v[132:135], v[172:175]\n" \
  "ds_read_b64_tr_b16 v[130:131], v7 offset:1984\n" \
  "v_mfma_f32_16x16x32_bf16 v[176:179], v[84:87], v[132:135], v[176:179]\n" \
  "s_add_u32 s73, s69, 262144\n" \
  "v_mfma_f32_16x16x32_bf16 v[180:183], v[108:111], v[132:135], v[180:183]\n" \
  "buffer_store_dwordx4 v[200:203], v22, s[36:39], s73 offen sc1\n" \
  "s_waitcnt lgkmcnt(8)\n" \
  "v_mfma_f32_16x16x32_bf16 v[168:171], v[40:43], v[136:139], v[168:171]\n" \
  "ds_read_b64_tr_b16 v[132:133], v7 offset:14528\n" \
  "v_mfma_f32_16x16x32_bf16 v[172:175], v[64:67], v[136:139], v[172:175]\n" \
  "ds_read_b64_tr_b16 v[134:135], v7 offset:16320\n" \
  "v_mfma_f32_16x16x32_bf16 v[176:179], v[88:91], v[136:139], v[176:179]\n" \
  "v_cvt_pk_bf16_f32 v204, v160, v161\n" \
  "v_cvt_pk_bf16_f32 v205, v162, v163\n" \
  "v_mfma_f32_16x16x32_bf16 v[180:183], v[112:115], v[136:139], v[180:183]\n" \
  "v_cvt_pk_bf16_f32 v206, v164, v165\n" \
  "v_cvt_pk_bf16_f32 v207, v166, v167\n" \
  "s_waitcnt lgkmcnt(8)\n" \
  "v_mfma_f32_16x16x32_bf16 v[168:171], v[44:47], v[140:143], v[168:171]\n" \
  "ds_read_b64_tr_b16 v[136:137], v7 offset:28864\n" \
  "v_mfma_f32_16x16x32_bf16 v[172:175], v[68:71], v[140:143], v[172:175]\n" \
  "ds_read_b64_tr_b16 v[138:139], v7 offset:30656\n" \
  "v_mfma_f32_16x16x32_bf16 v[176:179], v[92:95], v[140:143], v[176:179]\n" \
  "s_add_u32 s73, s70, 262144\n" \
  "v_mfma_f32_16x16x32_bf16 v[180:183], v[116:119], v[140:143], v[180:183]\n" \
  "buffer_store_dwordx4 v[204:207], v22, s[36:39], s73 offen sc1\n" \
  "s_waitcnt lgkmcnt(8)\n" \
  "v_mfma_f32_16x16x32_bf16 v[168:171], v[48:51], v[144:147], v[168:171]\n" \
  "ds_read_b64_tr_b16 v[140:141], v27 offset:192\n" \
  "v_mfma_f32_16x16x32_bf16 v[172:175], v[72:75], v[144:147], v[172:175]\n" \
  "ds_read_b64_tr_b16 v[142:143], v27 offset:1984\n" \
  "v_mfma_f32_16x16x32_bf16 v[176:179], v[96:99], v[144:147], v[176:179]\n" \
  "v_mfma_f32_16x16x32_bf16 v[180:183], v[120:123], v[144:147], v[180:183]\n" \
  "s_waitcnt lgkmcnt(8)\n" \
  "v_mfma_f32_16x16x32_bf16 v[168:171], v[52:55], v[148:151], v[168:171]\n" \
  "ds_read_b64_tr_b16 v[144:145], v27 offset:14528\n" \
  "v_mfma_f32_16x16x32_bf16 v[172:175], v[76:79], v[148:151], v[172:175]\n" \
  "ds_read_b64_tr_b16 v[146:147], v27 offset:16320\n" \
  "v_mfma_f32_16x16x32_bf16 v[176:179], v[100:103], v[148:151], v[176:179]\n" \
  "v_mfma_f32_16x16x32_bf16 v[180:183], v[124:127], v[148:151], v[180:183]\n" \
  "s_waitcnt vmcnt(5)\n" \
  "s_barrier\n" \
  "s_waitcnt lgkmcnt(8)\n" \
  "v_mfma_f32_16x16x32_bf16 v[184:187], v[32:35], v[128:131], 0\n" \
  "ds_read_b128 v[32:35], v20 offset:0\n" \
  "ds_read_b64_tr_b16 v[148:149], v27 offset:28864\n" \
  "v_mfma_f32_16x16x32_bf16 v[188:191], v[56:59], v[128:131], 0\n" \
  "ds_read_b128 v[56:59], v20 offset:768\n" \
  "ds_read_b64_tr_b16 v[150:151], v27 offset:30656\n" \
  "v_mfma_f32_16x16x32_bf16 v[192:195], v[80:83], v[128:131], 0\n" \
  "ds_read_b128 v[80:83], v20 offset:12288\n" \
  "v_cvt_pk_bf16_f32 v200, v168, v169\n" \
  "v_cvt_pk_bf16_f32 v201, v170, v171\n" \
  "v_mfma_f32_16x16x32_bf16 v[196:199], v[104:107], v[128:131], 0\n" \
  "ds_read_b128 v[104:107], v20 offset:13056\n" \
  "v_cvt_pk_bf16_f32 v202, v172, v173\n" \
  "v_cvt_pk_bf16_f32 v203, v174, v175\n" \
  "s_waitcnt lgkmcnt(12)\n" \
  "v_mfma_f32_16x16x32_bf16 v[184:187], v[36:39], v[132:135], v[184:187]\n" \
  "ds_read_b128 v[36:39], v21 offset:0\n" \
  "ds_read_b64_tr_b16 v[128:129], v7 offset:0\n" \
  "v_mfma_f32_16x16x32_bf16 v[188:191], v[60:63], v[132:135], v[188:191]\n" \
  "ds_read_b128 v[60:63], v21 offset:768\n" \
  "s_waitcnt lgkmcnt(8)\n" \
  "ds_read_b64_tr_b16 v[130:131], v7 offset:1792\n" \
  "v_mfma_f32_16x16x32_bf16 v[192:195], v[84:87], v[132:135], v[192:195]\n" \
  "ds_read_b128 v[84:87], v21 offset:12288\n" \
  "s_add_u32 s73, s69, 327680\n" \
  "v_mfma_f32_16x16x32_bf16 v[196:199], v[108:111], v[132:135], v[196:199]\n" \
  "ds_read_b128 v[108:111], v21 offset:13056\n" \
  "buffer_store_dwordx4 v[200:203], v22, s[36:39], s73 offen sc1\n" \
  "v_mfma_f32_16x16x32_bf16 v[184:187], v[40:43], v[136:139], v[184:187]\n" \
  "ds_read_b128 v[40:43], v20 offset:128\n" \
  "ds_read_b64_tr_b16 v[132:133], v7 offset:14336\n" \
  "v_mfma_f32_16x16x32_bf16 v[188:191], v[64:67], v[136:139], v[188:191]\n" \
  "ds_read_b128 v[64:67], v20 offset:896\n" \
  "ds_read_b64_tr_b16 v[134:135], v7 offset:16128\n" \
  "v_mfma_f32_16x16x32_bf16 v[192:195], v[88:91], v[136:139], v[192:195]\n" \
  "s_waitcnt lgkmcnt(8)\n" \
  "ds_read_b128 v[88:91], v20 offset:12416\n" \
  "v_cvt_pk_bf16_f32 v204, v176, v177\n" \
  "v_cvt_pk_bf16_f32 v205, v178, v179\n" \
  "v_mfma_f32_16x16x32_bf16 v[196:199], v[112:115], v[136:139], v[196:199]\n" \
  "ds_read_b128 v[112:115], v20 offset:13184\n" \
  "v_cvt_pk_bf16_f32 v206, v180, v181\n" \
  "v_cvt_pk_bf16_f32 v207, v182, v183\n" \
  "v_mfma_f32_16x16x32_bf16 v[184:187], v[44:47], v[140:143], v[184:187]\n" \
  "ds_read_b128 v[44:47], v21 offset:128\n" \
  "ds_read_b64_tr_b16 v[136:137], v7 offset:28672\n" \
  "v_mfma_f32_16x16x32_bf16 v[188:191], v[68:71], v[140:143], v[188:191]\n" \
  "ds_read_b128 v[68:71], v21 offset:896\n" \
  "ds_read_b64_tr_b16 v[138:139], v7 offset:30464\n" \
  "v_mfma_f32_16x16x32_bf16 v[192:195], v[92:95], v[140:143], v[192:195]\n" \
  "ds_read_b128 v[92:95], v21 offset:12416\n" \
  "s_add_u32 s73, s70, 327680\n" \
  "v_mfma_f32_16x16x32_bf16 v[196:199], v[116:119], v[140:143], v[196:199]\n" \
  "s_waitcnt lgkmcnt(8)\n" \
  "ds_read_b128 v[116:119], v21 offset:13184\n" \
  "buffer_store_dwordx4 v[204:207], v22, s[36:39], s73 offen sc1\n" \
  "v_mfma_f32_16x16x32_bf16 v[184:187], v[48:51], v[144:147], v[184:187]\n" \
  "ds_read_b128 v[48:51], v20 offset:256\n" \
  "ds_read_b64_tr_b16 v[140:141], v27 offset:0\n" \
  "v_mfma_f32_16x16x32_bf16 v[188:191], v[72:75], v[144:147], v[188:191]\n" \
  "ds_read_b128 v[72:75], v20 offset:1024\n" \
  "ds_read_b64_tr_b16 v[142:143], v27 offset:1792\n" \
  "v_mfma_f32_16x16x32_bf16 v[192:195], v[96:99], v[144:147], v[192:195]\n" \
  "ds_read_b128 v[96:99], v20 offset:12544\n" \
  "s_cmp_lg_u32 s68, s77\n" \
  "v_mfma_f32_16x16x32_bf16 v[196:199], v[120:123], v[144:147], v[196:199]\n" \
  "ds_read_b128 v[120:123], v20 offset:13312\n" \
  "v_mfma_f32_16x16x32_bf16 v[184:187], v[52:55], v[148:151], v[184:187]\n" \
  "s_waitcnt lgkmcnt(8)\n" \
  "ds_read_b128 v[52:55], v21 offset:256\n" \
  "ds_read_b64_tr_b16 v[144:145], v27 offset:14336\n" \
  "v_mfma_f32_16x16x32_bf16 v[188:191], v[76:79], v[148:151], v[188:191]\n" \
  "ds_read_b128 v[76:79], v21 offset:1024\n" \
  "ds_read_b64_tr_b16 v[146:147], v27 offset:16128\n" \
  "v_mfma_f32_16x16x32_bf16 v[192:195], v[100:103], v[148:151], v[192:195]\n" \
  "ds_read_b128 v[100:103], v21 offset:12544\n" \
  "v_mfma_f32_16x16x32_bf16 v[196:199], v[124:127], v[148:151], v[196:199]\n" \
  "ds_read_b128 v[124:127], v21 offset:13312\n" \
  "s_cbranch_scc1 LUR_%=\n" \
  "s_nop 7\n" \
  "v_cvt_pk_bf16_f32 v200, v184, v185\n" \
  "v_cvt_pk_bf16_f32 v201, v186, v187\n" \
  "v_cvt_pk_bf16_f32 v202, v188, v189\n" \
  "v_cvt_pk_bf16_f32 v203, v190, v191\n" \
  "s_add_u32 s73, s71, 393216\n" \
  "buffer_store_dwordx4 v[200:203], v22, s[36:39], s73 offen sc1\n" \
  "v_cvt_pk_bf16_f32 v204, v192, v193\n" \
  "v_cvt_pk_bf16_f32 v205, v194, v195\n" \
  "v_cvt_pk_bf16_f32 v206, v196, v197\n" \
  "v_cvt_pk_bf16_f32 v207, v198, v199\n" \
  "s_add_u32 s73, s72, 393216\n" \
  "buffer_store_dwordx4 v[204:207], v22, s[36:39], s73 offen sc1\n" \
  "s_waitcnt vmcnt(0) lgkmcnt(0)\n" \
  "buffer_atomic_add v0, v25, s[36:39], 0 offen sc1\n" \
  "s_barrier\n"

__device__ __forceinline__ int fresh_lane() {
  int ln;
  asm volatile("v_mbcnt_lo_u32_b32 %0, -1, 0\n\tv_mbcnt_hi_u32_b32 %0, -1, %0" : "=v"(ln));
  __builtin_assume(ln >= 0 && ln < 64);
  return ln;
}
__device__ __forceinline__ unsigned now_ticks() { return (unsigned)__builtin_amdgcn_s_memrealtime(); }
template <int SLEEP>
__device__ __forceinline__ void wait_ge(gu32* ctrl, long word, int need) {
#pragma unroll 1
  for (int spin = 0;; ++spin) {
    const int x = (int)__hip_atomic_load(ctrl + word, RLX_AGENT);
    if (__builtin_amdgcn_ballot_w64(x < need) == 0) return;
    if (spin > SPIN_CAP || ((spin & 1023) == 1023 && rfl(__hip_atomic_load(ctrl + C_ERR, RLX_AGENT)))) { __hip_atomic_store(ctrl + C_ERR, 1u, RLX_AGENT); return; }
    __builtin_amdgcn_s_sleep(SLEEP);
  }
}
template <typename VT>
__device__ __forceinline__ void set_rsrc(VT& sr, int i, const void* ptr, unsigned bytes) {
  const unsigned long long a = uni64((unsigned long long)ptr);
  sr[4 * i] = (unsigned)a; sr[4 * i + 1] = (unsigned)(a >> 32) & 0xffffu; sr[4 * i + 2] = bytes; sr[4 * i + 3] = 0x00020000u;
}
__device__ __forceinline__ int side_a(const Params& p, int blk) { return (blk << 21) | ((int)(p.ws[CTRL_OFF + blk] & 1) << 13); }
__device__ __forceinline__ int hand_off(const Params& p, int s) { return side_a(p, HB + s % HRING / 6) + (s % 6) * 2 * H_STEP; }
__device__ __forceinline__ int pt_off(const Params& p, int s) { return side_a(p, PB + s / 12) + (s % 12) * 2 * PT_SEG; }
__device__ __forceinline__ int z_off(const Params& p, int s) { return (side_a(p, slot_blk(s) + 1) ^ 8192) + s / RING * 655360; }
template <typename VT> __device__ __forceinline__ void pin(VT& v) {
#pragma unroll
  for (unsigned i = 0; i < sizeof(VT) / 4; ++i) { unsigned r = rfl(v[i]); asm volatile("" : "+s"(r)); v[i] = r; }
}
__device__ __forceinline__ void pause(unsigned ticks) {
  const unsigned t0 = now_ticks();
#pragma unroll 1
  while (now_ticks() - t0 < ticks) __builtin_amdgcn_s_sleep(8);
}
#define OPAQUE(v) asm volatile("" : "=v"(v))
__device__ __forceinline__ const __attribute__((address_space(4))) Params* fresh_params() {
  const __attribute__((address_space(4))) Params* pk = (const __attribute__((address_space(4))) Params*)__builtin_amdgcn_kernarg_segment_ptr();
  asm volatile("" : "+s"(pk) :: "memory");
  return pk;
}

__device__ __forceinline__ v4u ld16s(rsrc_t r, int vo, int so) { return __builtin_amdgcn_raw_buffer_load_b128(r, vo, so, 16); }
__device__ __forceinline__ v2u ld8s(rsrc_t r, int vo, int so) { return __builtin_amdgcn_raw_buffer_load_b64(r, vo, so, 16); }
#define TS(cond, i)
#define W240_IN(w) "{a[0:31]}"(w[0]), "{a[32:63]}"(w[1]), "{a[64:95]}"(w[2]), "{a[96:127]}"(w[3]), "{a[128:159]}"(w[4]), "{a[160:191]}"(w[5]), \
                   "{a[192:223]}"(w[6]), "{a[224:239]}"(w##7)
#define W240_SET(w, i, k, val) do { if ((i) < 224) w[(i) >> 5][((i) & 31) + (k)] = (val); else w##7[(i) - 224 + (k)] = (val); } while (0)
constexpr int L_MISC = 122880;
#define FFN_WAIT 100
__device__ __forceinline__ Params load_params() {
  const __attribute__((address_space(4))) Params* k = fresh_params();
  Params p;
  p.x = k->x; p.mem0 = k->mem0; p.wq = k->wq; p.wk = k->wk; p.wv = k->wv; p.wo = k->wo; p.wr = k->wr; p.wg = k->wg; p.wu = k->wu; p.wd = k->wd; p.c = k->c; p.wmod = k->wmod;
  p.out = k->out; p.ws = k->ws; p.dE = k->dE; p.dL = k->dL; p.dM = k->dM; p.dZ = k->dZ; p.dS = k->dS; p.lead = k->lead;
  return p;
}
__device__ __forceinline__ int look_off(int lane) { return lmap(H_PLAN) + (lane & 3) * 4; }
__device__ __forceinline__ int flag_off(const Params& p, int h) { return side_a(p, HB) + lmap(H_FLAG + h * 128); }

__device__ __forceinline__ unsigned wave_max_u32(unsigned m) {
  m = max(m, (unsigned)__builtin_amdgcn_update_dpp(0, (int)m, 0xb1, 0xf, 0xf, true)); m = max(m, (unsigned)__builtin_amdgcn_update_dpp(0, (int)m, 0x4e, 0xf, 0xf, true));
  m = max(m, (unsigned)__builtin_amdgcn_update_dpp(0, (int)m, 0x124, 0xf, 0xf, true)); m = max(m, ror8u(m));
  auto a = __builtin_amdgcn_permlane16_swap(m, m, false, false); m = max(a[0], a[1]);
  auto b = __builtin_amdgcn_permlane32_swap(m, m, false, false); return max(b[0], b[1]);
}
__device__ __forceinline__ void select_rows(gu32* ctrl, rsrc_t rT, int lane) {
  unsigned key[32];
  v4u rr[8];
#pragma unroll
  for (int i = 0; i < 8; ++i) rr[i] = ld16s(rT, lane * 16 + i * 1024, 2 * (int)HF_BYTES);
  __builtin_amdgcn_sched_barrier(0);
#pragma unroll
  for (int i = 0; i < 8; ++i) {
    const v4u r = rr[i];
#pragma unroll
    for (int k = 0; k < 4; ++k) {
      unsigned u = f2u(u2f(r[k]) + 0.f);
      u = (u & 0x80000000u) ? ~u : (u | 0x80000000u);
      key[4 * i + k] = (u & 0xffff0000u) | (unsigned)(S - 1 - (i * 256 + lane * 4 + k));
    }
  }
  unsigned mine = 0, bound = 0;
#pragma unroll 1
  for (int it = 0; it < TOPK; ++it) {
    unsigned m = 0;
#pragma unroll
    for (int i = 0; i < 32; ++i) m = max(m, key[i] - bound);
    bound += wave_max_u32(m);
    if ((lane & 7) == it) mine = S - (bound & 0xffffu);
  }
#pragma unroll
  for (int i = 0; i < 2; ++i) __hip_atomic_store(ctrl + C_IDX + ((lane >> 3) + 8 * i) * 32 + (lane & 7), mine, RLX_AGENT);
}

__device__ __forceinline__ void ffn_role(const Params& p, int role, int lane, int wave) {
  gu32* ctrl = (gu32*)(p.ws + CTRL_OFF);
  const int g = lane >> 4, n = lane & 15, q = (lane >> 2) & 3, pp = lane & 3;
  const bool up = role < 96, down = role >= 72;
  const int c = role - 72;
  v4u wg[5], wu[5], wd[12];
  {
    const rsrc_t rg = mkrsrc(p.wg, DM * FF * 2), ru = mkrsrc(p.wu, DM * FF * 2), rd = mkrsrc(p.wd, FF * DM * 2);
#pragma unroll
    for (int i = 0; i < 5; ++i) {
      const int vo = up ? ((wave * 160 + i * 32 + (lane >> 1)) * FF + role * 16) * 2 + (lane & 1) * 16 : OOB;
      dma16(rg, vo, 0, wave * 22528 + i * 1024); dma16(ru, vo, 0, wave * 22528 + 5120 + i * 1024);
    }
#pragma unroll
    for (int i = 0; i < 12; ++i) dma16(rd, down ? ((wave * 384 + i * 32 + (lane >> 1)) * DM + c * 16) * 2 + (lane & 1) * 16 : OOB, 0, wave * 22528 + 10240 + i * 1024);
    VMCNT(0);
    const unsigned a = wave * 22528 + (8 * g + q) * 32 + pp * 8;
#pragma unroll
    for (int i = 0; i < 22; ++i) {
      const v2u lo = trread(a + i * 1024), hi = trread(a + i * 1024 + 128);
      const v4u x = {lo[0], lo[1], hi[0], hi[1]};
      if (i < 5) wg[i] = x; else if (i < 10) wu[i - 5] = x; else wd[i - 10] = x;
    }
  }
  const rsrc_t rT = mkrsrc(p.ws + X1_OFF, (unsigned)(WS_END - X1_OFF));
  constexpr int SO_H2 = DM * 2, SO_R = 2 * (int)HF_BYTES, SO_ACT = (int)(ACT_OFF - X1_OFF), RED = 98304;
  if (wave == 0) {
    wait_ge<3>(ctrl, C_IDX + (role & 15) * 32 + (lane & 7), 1);
    if (lane < 8) *(LAS unsigned*)(L_MISC + lane * 4) = __hip_atomic_load(ctrl + C_IDX + (role & 15) * 32 + lane, RLX_AGENT);
  }
  __syncthreads();
  const unsigned t0 = now_ticks();
  const int row = (*(LAS int*)(L_MISC + (n & 7) * 4) - 1) & (S - 1);
  v2u x1, h2, gb; unsigned rv;
  if (down && wave == 0) { gb = ld8s(mkrsrc(p.ws + MODB_OFF, 7680), (5 * DM + c * 16 + 4 * g) * 2, 0); x1 = ld8s(rT, (row * 2 * DM + c * 16 + 4 * g) * 2, 0); h2 = ld8s(rT, (row * 2 * DM + c * 16 + 4 * g) * 2, SO_H2); rv = __builtin_amdgcn_raw_buffer_load_b32(rT, row * 4, SO_R, 16); }
  if (up) {
    v4u xb[5];
#pragma unroll
    for (int i = 0; i < 5; ++i) xb[i] = ld16s(rT, (row * 2 * DM + wave * 160 + i * 32 + 8 * g) * 2, SO_H2);
    __builtin_amdgcn_sched_barrier(0);
    f4 ag = {}, au = {};
#pragma unroll
    for (int i = 0; i < 5; ++i) { ag = mfma32(wg[i], xb[i], ag); au = mfma32(wu[i], xb[i], au); }
    *(LAS f4*)(RED + wave * 2048 + lane * 16) = ag; *(LAS f4*)(RED + wave * 2048 + 1024 + lane * 16) = au;
    __syncthreads();
    if (wave == 0) {
#pragma unroll
      for (int w = 1; w < 4; ++w) { ag += *(LAS f4*)(RED + w * 2048 + lane * 16); au += *(LAS f4*)(RED + w * 2048 + 1024 + lane * 16); }
      float a[4];
#pragma unroll
      for (int r = 0; r < 4; ++r) { const float gs = rbf(ag[r]); a[r] = rbf(gs / (1.f + __expf(-gs))) * rbf(au[r]); }
      __builtin_amdgcn_raw_buffer_store_b64(v2u{unsent(pack2(a[0], a[1])), unsent(pack2(a[2], a[3]))}, rT, n < 8 ? role * 256 + (g >> 1) * 128 + n * 16 + (g & 1) * 8 : OOB, SO_ACT, 16);
    }
  }
  if (!down) return;
  while ((int)(now_ticks() - t0) < FFN_WAIT) __builtin_amdgcn_s_sleep(1);
  v4u ab[12];
  for (int spin = 0;; ++spin) {
    asm volatile("" ::: "memory");
    unsigned m = 0;
#pragma unroll
    for (int i = 0; i < 12; ++i) ab[i] = ld16s(rT, (wave * 12 + i) * 512 + g * 128 + (n & 7) * 16, SO_ACT);
    __builtin_amdgcn_sched_barrier(0);
#pragma unroll
    for (int i = 0; i < 12; ++i) m = max(max(m, max(ab[i][0], ab[i][1])), max(ab[i][2], ab[i][3]));
    if (__builtin_amdgcn_ballot_w64(m == ~0u) == 0) break;
    if (spin > SPIN_CAP) { __hip_atomic_store(ctrl + C_ERR, 1u, RLX_AGENT); break; }
    __builtin_amdgcn_s_sleep(4);
  }
  f4 y = {};
#pragma unroll
  for (int i = 0; i < 12; ++i) y = mfma32(wd[i], ab[i], y);
  *(LAS f4*)(RED + 8192 + wave * 1024 + lane * 16) = y;
  __syncthreads();
  if (wave == 0 && n < 8) {
#pragma unroll
    for (int w = 1; w < 4; ++w) y += *(LAS f4*)(RED + 8192 + w * 1024 + lane * 16);
    const float rs = rbf(1.f / (1.f + __expf(-u2f(rv))));
    float o[4];
#pragma unroll
    for (int r = 0; r < 4; ++r) {
      const float hv = (r & 1) ? hi2f(h2[r >> 1]) : lo2f(h2[r >> 1]), xv = (r & 1) ? hi2f(x1[r >> 1]) : lo2f(x1[r >> 1]);
      o[r] = xv + rbf(((r & 1) ? hi2f(gb[r >> 1]) : lo2f(gb[r >> 1])) * rbf(hv + rbf(rbf(y[r]) * rs)));
    }
    *(v2u*)(p.out + (size_t)row * DM + c * 16 + 4 * g) = v2u{pack2(o[0], o[1]), pack2(o[2], o[3])};
  }
}

constexpr int L_LN = 123904;
__device__ __forceinline__ void ln1_fetch(const Params& p, int tile, int lane, int wave) {
  const rsrc_t rx = mkrsrc(p.x, S * DM * 2), rm = mkrsrc(p.ws + MODB_OFF, 2560);
#pragma unroll
  for (int k = 0; k < 4; ++k) dma16(rx, lane * 16, (tile * 16 + wave * 4 + k) * 1280, L_LN + (wave * 4 + k) * 1040);
  dma16(rx, (lane >> 4) * 1280 + 1024 + (lane & 15) * 16, (tile * 16 + wave * 4) * 1280, L_LN + 16640 + wave * 1024);
  dma16(rm, wave * 1024 + lane * 16, 0, L_LN + 20736 + wave * 1024);
}
__device__ __forceinline__ void ln1_rows(const Params& p, int tile, int lane, int wave) {
  const rsrc_t rh = mkrsrc(p.ws + HF_OFF, (unsigned)HF_BYTES);
    const int r = wave * 8 + (lane & 7);
    v4u a[10], sh[10], sc[10];
#pragma unroll
    for (int i = 0; i < 10; ++i) a[i] = lds128(L_LN + (i < 8 ? r * 1040 : 16640 - 1024 + r * 256) + (lane >> 3) * 16 + i * 128);
#pragma unroll
    for (int i = 0; i < 10; ++i) { sh[i] = lds128(L_LN + 20736 + (lane >> 3) * 16 + i * 128); sc[i] = lds128(L_LN + 22016 + (lane >> 3) * 16 + i * 128); }
    float s = 0.f, q = 0.f;
    const bf2 one = {(__bf16)1.f, (__bf16)1.f};
#pragma unroll
    for (int i = 0; i < 10; ++i)
#pragma unroll
      for (int k = 0; k < 4; ++k) { const unsigned w = a[i][k]; s = __builtin_amdgcn_fdot2_f32_bf16(__builtin_bit_cast(bf2, w), one, s, false); }
    s += ror8f(s); s = swapsum16(s, s); s = swapsum32(s, s);
    const float mean = s * (1.f / DM);
#pragma unroll
    for (int i = 0; i < 10; ++i)
#pragma unroll
      for (int k = 0; k < 4; ++k) { const float d0 = lo2f(a[i][k]) - mean, d1 = hi2f(a[i][k]) - mean; q = fmaf(d0, d0, fmaf(d1, d1, q)); }
    q += ror8f(q); q = swapsum16(q, q); q = swapsum32(q, q);
    const float mb = rbf(mean), isd = __builtin_amdgcn_rcpf(rbf(__builtin_amdgcn_sqrtf(rbf(rbf(q * (1.f / DM)) + 1e-5f))));
#pragma unroll
    for (int i = 0; i < 10; ++i) {
      v4u o;
#pragma unroll
      for (int k = 0; k < 4; ++k) {
        const unsigned d = pack2(lo2f(a[i][k]) - mb, hi2f(a[i][k]) - mb), n = pack2(lo2f(d) * isd, hi2f(d) * isd);
        const unsigned m = pack2(lo2f(n) * lo2f(sc[i][k]), hi2f(n) * hi2f(sc[i][k]));
        o[k] = pack2(lo2f(m) + lo2f(sh[i][k]), hi2f(m) + hi2f(sh[i][k]));
      }
      __builtin_amdgcn_raw_buffer_store_b128(o, rh, tile * 20480 + i * 2048 + (lane >> 3) * 256 + r * 16, 0, 16);
    }
}
__device__ __forceinline__ void w_issue(rsrc_t rw, int type, int h, int m, int slot, int i0, int cnt, int lane) {
#pragma unroll 1
  for (int i = i0; i < i0 + cnt; ++i) {
    const int c = i * 64 + lane;
    dma16(rw, type < 3 ? ((m * 160 + c / 24) * HD + h * DH) * 2 + (c % 24) * 16 : (h * DH + (i < 40 ? 32 * m : 96 + 16 * m)) * DM * 2 + c * 16, 0, slot * 61440 + i * 1024);
  }
}
__device__ __forceinline__ rsrc_t w_rsrc(int type) { const auto* k = fresh_params(); return mkrsrc(type == 0 ? k->wk : type == 1 ? k->wq : type == 2 ? k->wv : k->wo, DM * HD * 2); }
__device__ __forceinline__ void token_start(const Params& p, int type, int h, int lane, int wave) {
  gu32* ctrl = (gu32*)(p.ws + CTRL_OFF);
  const int tile = h * 4 + type;
  ln1_fetch(p, tile, lane, wave);
  if (wave >= 2) { w_issue(w_rsrc(type), type, h, wave - 2, wave - 2, 0, 50, lane); VMCNT(50); } else VMCNT(0);
  BAR();
  if (wave < 2) {
    ln1_rows(p, tile, lane, wave);
    VMCNT(0);
    if (lane == 0) __hip_atomic_fetch_add(ctrl + C_LN + (tile & 7) * 32, 1u, RLX_AGENT);
    TS(wave == 0 && lane == 0 && type == 3, 640 + h)
  } else w_issue(w_rsrc(type), type, h, wave - 2, wave - 2, 50, 10, lane);
}
template <typename T, typename H>
__device__ __forceinline__ void stage4(int type, int h, int lane, int wave, T take, H hook) {
  const rsrc_t rw = w_rsrc(type);
  VMCNT(0); BAR(); take(0, 0); LGKM0(); BAR(); w_issue(rw, type, h, 2, 0, wave * 15, 15, lane);
  take(1, 1); LGKM0(); BAR(); w_issue(rw, type, h, 3, 1, wave * 15, 15, lane);
  VMCNT(15); BAR(); take(2, 0);
  hook();
  VMCNT(0); BAR(); take(3, 1); LGKM0(); BAR();
}

__device__ __forceinline__ void u_role(const Params& p, int h, int th, int lane, int wave) {
  const int g = lane >> 4, n = lane & 15, q = (lane >> 2) & 3, pp = lane & 3;
  v32u vc; v12u sr; v16u sc = {};
  sc[0] = wave;
  OPAQUE(vc);
#pragma unroll
  for (int k = 0; k < 7; ++k) { const int c = k * 64 + lane; vc[k] = ((h * DH + c / 28) * DM + 208 * th) * 2 + ((c % 28) ^ (c / 28 >> 2 & 2)) * 16 - (k & 3) * 1024; }
  const int ch = wave >> 1;
  vc[7] = (8 * g + q) * 448 + pp * 8 + ((7 * ch) ^ (g & 1)) * 32;
  vc[26] = (8 * g + q) * 448 + pp * 8 + ((7 * ch + 1) ^ (g & 1)) * 32;
#pragma unroll
  for (int k = 0; k < 12; ++k) { const int c = k * 64 + lane, r = c / 24; vc[8 + k] = (r * HD + h * DH) * 2 + (((c % 24) ^ (r >> 2)) * 16) - (k & 3) * 1024; }
  const int row = 16 * (n >> 3) + 8 * ((n >> 1) & 1) + 4 * ((n >> 2) & 1) + (n & 1);
  vc[20] = 86016 + (wave & 1) * 24576 + row * 384 + (g ^ (row >> 2)) * 16;
  vc[21] = vc[20] ^ 64;
  vc[22] = (26 * th + 14 * ch + (n >> 3)) * 32768 + lmap(h * 512) + g * 128 + (n & 7) * 16;
  vc[23] = side_a(p, 2 * (USLOT + lane % 20));
  vc[24] = th == 2 ? side_a(p, 2 * (USLOT + lane % 20) + 1) - (1 << 21) : vc[23];
  if (ch) vc[23] = vc[24];
  vc[25] = lane == 0 ? CTRL_OFF + (C_U + (th * 4 + wave) * 32) * 4 : OOB;
  set_rsrc(sr, 0, p.ws, WS_BYTES); set_rsrc(sr, 1, p.wv, DM * HD * 2); set_rsrc(sr, 2, p.wo, DM * HD * 2);
  pin(sc); pin(sr);
  asm volatile(UG_ASM : "+{v[0:31]}"(vc), "+{s[52:67]}"(sc) : "{s[36:47]}"(sr) : "memory", "vcc", "scc", UG_CLOBBERS);
}

__device__ __forceinline__ void proj_role(const Params& p, int type, int head, int lane, int wave) {
  gu32* ctrl = (gu32*)(p.ws + CTRL_OFF);
  const int g = lane >> 4, q = (lane >> 2) & 3, pp = lane & 3;
  v32u wa[7]; v16u wa7;
  {
    stage4(type, head, lane, wave,
      [&](int m, int slot) {
#pragma unroll
        for (int ks = 0; ks < 5; ++ks)
#pragma unroll
          for (int t = 0; t < 3; ++t) {
            const unsigned a = slot * 61440 + (32 * ks + 8 * g + q) * 384 + (t < 2 ? 32 * wave + 8 * pp + 4 * t : 128 + 16 * wave + 4 * pp) * 2;
            const v2u lo = trread(a), hi = trread(a + 4 * 384);
            const int r = (t * 20 + m * 5 + ks) * 4;
            W240_SET(wa, r, 0, lo[0]); W240_SET(wa, r, 1, lo[1]); W240_SET(wa, r, 2, hi[0]); W240_SET(wa, r, 3, hi[1]);
          }
      },
      [&]() { if (wave == 0) { wait_ge<4>(ctrl, C_LN + (lane & 7) * 32, 32); __builtin_amdgcn_fence(__ATOMIC_ACQUIRE, "agent"); } });
  }
  TS(wave == 0 && lane == 0, 512 + type * 32 + head)
  {
    const rsrc_t rh = mkrsrc(p.ws + HF_OFF, (unsigned)HF_BYTES);
#pragma unroll
    for (int k = 0; k < 20; ++k) dma16(rh, wave * 10240 + lane * 16, (k / 10) * 40960 + (k % 10) * 1024, (k / 10) * 40960 + wave * 10240 + (k % 10) * 1024);
  }
  v12u vc; v8u sr;
  vc[0] = lane * 16; vc[1] = wave * 10240 + lane * 16;
  if (type < 2) {
    vc[2] = lmap(KQ_OFF + head * 24576 + type * 8192) + wave * 2048 + lane * 16;
    vc[3] = lmap(KQ_OFF + head * 24576 + 16384) + type * 4096 + wave * 1024 + lane * 16;
    vc[4] = side_a(p, slot_blk(lane) + 1);
    vc[5] = lane == 0 ? flag_off(p, head) + (type * 4 + wave) * 4 : OOB;
  } else {
    vc[2] = head * 32768 + wave * 2048 + lane * 16;
    vc[3] = head * 32768 + 16384 + wave * 1024 + lane * 16;
    vc[4] = side_a(p, slot_blk(lane)) ^ 8192;
    vc[5] = lane == 0 ? CTRL_OFF + (C_VF + head * 32 + wave) * 4 : OOB;
  }
  vc[6] = 0;
  {
    const int pc = wave * 64 + type * 32 + head, k = pc % 34, o = k < 10 ? k : k + 6;
    vc[7] = type < 2 && pc < 136 ? (pc / 34) * 2 * H_COPY + lmap(o * 1024) + lane * 16 : OOB;
    vc[8] = lane < 63 ? hand_off(p, lane + 1) : OOB;
  }
  vc[9] = 0; vc[10] = 0; vc[11] = 0;
  set_rsrc(sr, 0, p.ws + HF_OFF, (unsigned)HF_BYTES); set_rsrc(sr, 1, p.ws, WS_BYTES);
#pragma unroll 1
  for (int k = 0; k < 11; ++k) {
    if (wave == 0 && k > 1) {
      wait_ge<8>(ctrl, C_G + (head & 15) * 32, max(6 * k - AHEAD, 1));
      if (6 * k + 5 >= RING) wait_ge<8>(ctrl, C_AF + min(max(6 * k + lane % 6 - RING, 0), NSEG - 1 - RING) * 32, 2);
    }
    v4u sc = {6u * k, min(6u * k + 6u, 64u), (unsigned)wave * 10240u, 0u};
    v12u vt = vc;
    pin(sc);
    asm volatile(PROJ_ASM : "+{s[52:55]}"(sc), "+{v[0:11]}"(vt) : "{s[36:43]}"(sr), W240_IN(wa) : "memory", "vcc", "scc", PROJ_CLOBBERS);
    TS(wave == 0 && lane == 0 && head == 0 && (k == 4 || k == 7 || k == 10), 20 + type * 4 + (k - 4) / 3)
  }
}

__device__ __forceinline__ void vo_role(const Params& p, int h, int lane, int wave) {
  const int g = lane >> 4, n = lane & 15, q = (lane >> 2) & 3, pp = lane & 3;
  v32u wa[7]; v16u wa7;
  {
    stage4(3, h, lane, wave,
      [&](int m, int slot) {
#pragma unroll
        for (int c = 0; c < 10; ++c) {
          const unsigned col = slot * 61440 + (160 * wave + 16 * c + 4 * pp) * 2;
          const v2u lo = trread((8 * g + q) * 1280 + col), hi = trread((8 * g + 4 + q) * 1280 + col), tl = trread((32 + 4 * g + q) * 1280 + col);
          const int r = (c * 6 + m) * 4, r2 = (c * 6 + 4 + (m >> 1)) * 4 + 2 * (m & 1);
          W240_SET(wa, r, 0, lo[0]); W240_SET(wa, r, 1, lo[1]); W240_SET(wa, r, 2, hi[0]); W240_SET(wa, r, 3, hi[1]);
          W240_SET(wa, r2, 0, tl[0]); W240_SET(wa, r2, 1, tl[1]);
        }
      },
      []() {});
  }
  TS(wave == 0 && lane == 0, 608 + h)
  v16u vc; v4u sr;
  vc[0] = lane * 16;
  vc[1] = h * 32768 + wave * 2048 + lane * 16;
  vc[2] = h * 32768 + 16384 + wave * 1024 + lane * 16;
  vc[3] = (20 * wave + (n >> 3)) * 32768 + lmap(h * 512) + g * 128 + (n & 7) * 16;
  vc[4] = side_a(p, slot_blk(lane));
  vc[5] = wave == 3 ? side_a(p, slot_blk(lane) + 1) - (1 << 21) : vc[4];
  vc[6] = vc[4] ^ 8192;
  vc[7] = lane == 0 ? flag_off(p, h) + (8 + wave) * 4 : OOB;
  vc[8] = 0;
  vc[9] = CTRL_OFF + (C_VF + h * 32 + pp) * 4;
#pragma unroll
  for (int k = 10; k < 16; ++k) vc[k] = 0;
  set_rsrc(sr, 0, p.ws, WS_BYTES);
  v4u sc = {0u, 64u, (unsigned)wave, 0u};
  pin(sc); pin(sr);
  asm volatile(VO_ASM : "+{s[52:55]}"(sc), "+{v[0:15]}"(vc) : "{s[36:39]}"(sr), W240_IN(wa) : "memory", "vcc", "scc", VO_CLOBBERS);
}

typedef float f2 __attribute__((ext_vector_type(2)));
__device__ __forceinline__ f2 up2(unsigned w) { return f2{lo2f(w), hi2f(w)}; }
constexpr int L_PAR = 123904;
__device__ __forceinline__ void row_params(int wave) {
  const Params p = load_params();
  const int tid = wave * 64 + fresh_lane();
  const rsrc_t rm = mkrsrc(p.ws + MODB_OFF + 2560, 5120), rr = mkrsrc(p.wr, 1280);
  unsigned w[5];
#pragma unroll
  for (int k = 0; k < 5; ++k) w[k] = __builtin_amdgcn_raw_buffer_load_b32(rm, (k * 256 + tid) * 4, 0, 0);
  const v2u r = __builtin_amdgcn_raw_buffer_load_b64(rr, tid * 8, 0, 0);
  BAR();
#pragma unroll
  for (int k = 0; k < 5; ++k) *(LAS f2*)(L_PAR + (k * 256 + tid) * 8) = up2(w[k]);
  *(LAS v2u*)(L_PAR + 10240 + tid * 8) = r;
  LGKM0(); BAR();
}
__device__ __forceinline__ float sum16(float v) {
  v += u2f(__builtin_amdgcn_update_dpp(0, (int)f2u(v), 0xb1, 0xf, 0xf, true)); v += u2f(__builtin_amdgcn_update_dpp(0, (int)f2u(v), 0x4e, 0xf, 0xf, true));
  v += u2f(__builtin_amdgcn_update_dpp(0, (int)f2u(v), 0x124, 0xf, 0xf, true));
  return v + ror8f(v);
}
struct RowIn { v4u x[5], y[5]; };
__device__ __forceinline__ void rows_load(RowIn& r, int row0) {
  const auto* k = fresh_params();
  const int lane = fresh_lane(), row = row0 + (lane >> 4), co = (lane & 15) * 16;
  const rsrc_t rx = mkrsrc(k->x, S * DM * 2), rT = mkrsrc(k->ws + X1_OFF, 2 * HF_BYTES);
#pragma unroll
  for (int i = 0; i < 5; ++i) { r.y[i] = ld16s(rT, row * 2560 + co + i * 256, 0); r.x[i] = __builtin_amdgcn_raw_buffer_load_b128(rx, row * 1280 + co + i * 256, 0, 0); }
}
__device__ __forceinline__ void rows_do(const RowIn& r, int row0) {
  const auto* kp = fresh_params();
  const int lane = fresh_lane(), row = row0 + (lane >> 4), co = (lane & 15) * 16;
  const rsrc_t rT = mkrsrc(kp->ws + X1_OFF, (unsigned)(WS_END - X1_OFF)), rO = mkrsrc(kp->out, S * DM * 2);
  auto par = [&](int a, int i, int k) { const f4 v = *(LAS f4*)(L_PAR + a * 2560 + co * 2 + i * 512 + (k >> 1) * 16); return k & 1 ? f2{v[2], v[3]} : f2{v[0], v[1]}; };
  f2 x1[20], s = {}, q = {};
  float rs = 0.f;
#pragma unroll
  for (int i = 0; i < 5; ++i) {
    v4u w;
#pragma unroll
    for (int k = 0; k < 4; ++k) {
      const f2 t = __builtin_elementwise_fma(par(0, i, k), up2(r.y[i][k]), up2(r.x[i][k]));
      w[k] = pack2(t[0], t[1]); x1[4 * i + k] = up2(w[k]); s += x1[4 * i + k];
    }
    __builtin_amdgcn_raw_buffer_store_b128(w, rT, row * 2560 + co + i * 256, 0, 16);
  }
  const float mean = sum16(s[0] + s[1]) * (1.f / DM);
#pragma unroll
  for (int i = 0; i < 20; ++i) { const f2 d = x1[i] - mean; q = __builtin_elementwise_fma(d, d, q); }
  const float isd = __builtin_amdgcn_rcpf(rbf(__builtin_amdgcn_sqrtf(rbf(rbf(sum16(q[0] + q[1]) * (1.f / DM)) + 1e-5f)))), mi = -rbf(mean) * isd;
#pragma unroll
  for (int i = 0; i < 5; ++i) {
    const v4u wr = lds128(L_PAR + 10240 + co + i * 256);
    v4u h, o;
#pragma unroll
    for (int k = 0; k < 4; ++k) {
      const f2 hh = __builtin_elementwise_fma(__builtin_elementwise_fma(x1[4 * i + k], f2{isd, isd}, f2{mi, mi}), par(2, i, k), par(1, i, k));
      const f2 oo = __builtin_elementwise_fma(par(3, i, k), hh, x1[4 * i + k]);
      const unsigned hb = pack2(hh[0], hh[1]), wk = wr[k];
      h[k] = hb; o[k] = pack2(oo[0], oo[1]);
      rs = __builtin_amdgcn_fdot2_f32_bf16(__builtin_bit_cast(bf2, hb), __builtin_bit_cast(bf2, wk), rs, false);
    }
    __builtin_amdgcn_raw_buffer_store_b128(h, rT, row * 2560 + 1280 + co + i * 256, 0, 16);
    __builtin_amdgcn_raw_buffer_store_b128(o, rO, row * 1280 + co + i * 256, 0, 16);
  }
  __builtin_amdgcn_raw_buffer_store_b32(f2u(rbf(sum16(rs))), rT, co == 0 ? row * 4 : OOB, 2 * HF_BYTES, 16);
}
__device__ __forceinline__ void rows_fin(unsigned n, int wave) {
  if (wave) return;
  const Params p = load_params();
  gu32* ctrl = (gu32*)(p.ws + CTRL_OFF);
  const int lane = fresh_lane();
  unsigned t = 0;
  if (lane == 0) t = __hip_atomic_fetch_add(ctrl + C_FIN, n, RLX_AGENT);
  if (rfl(t) + n == 2 * NSEG) { TS(lane == 0, 17) select_rows(ctrl, mkrsrc(p.ws + X1_OFF, (unsigned)(WS_END - X1_OFF)), lane); TS(lane == 0, 18) }
}
#define OGATE (seg - 1)
template <int NT>
__device__ __forceinline__ void o_unit(const Params& p, int seg, int t0, int rep, int lane, int wave) {
  gu32* ctrl = (gu32*)(p.ws + CTRL_OFF);
  constexpr int NB = NT < 4 ? NT : 4;
  const rsrc_t rH = mkrsrc(p.ws, WS_BYTES);
  const int g = lane >> 4, n = lane & 15;
  const int sa = rfl(side_a(p, slot_blk(seg))), sb = rfl(side_a(p, slot_blk(seg) + 1)), sp = rfl(pt_off(p, seg));
  if (wave == 0) {
    const long w = ((long)flag_off(p, lane & 31) - CTRL_OFF) / 4 + 8 + (lane >> 5) * 2;
    wait_ge<16>(ctrl, C_G + rep * 32, OGATE);
    wait_ge<32>(ctrl, w, seg + 1); wait_ge<32>(ctrl, w + 1, seg + 1);
  }
  BAR();
  const int vy = (n >> 3) * 32768 + lmap(wave * 4096) + g * 128 + (n & 7) * 16, vz = (n >> 3) * 4096 + wave * 1024 + g * 128 + (n & 7) * 16;
  v4u y[NB][8], z[NB][2], pt[2][8], pm[2][2];
  auto ldy = [&](int t) {
    const int T = t0 + t, so = T < 32 ? sa + T * 65536 : sb + (T - 32) * 65536;
#pragma unroll
    for (int j = 0; j < 8; ++j) y[t % NB][j] = ld16s(rH, vy + j * 512, so);
  };
  auto ldz = [&](int t) {
#pragma unroll
    for (int j = 0; j < 2; ++j) z[t % NB][j] = ld16s(rH, vz + j * 512, (sb ^ 8192) + seg / RING * 655360 + (t0 + t) * 16384);
  };
#pragma unroll
  for (int t = 0; t < NB; ++t) ldy(t);
  __builtin_amdgcn_sched_barrier(0);
  if (wave == 0) {
    if (seg < NSEG - 3) wait_ge<8>(ctrl, C_G + rep * 32, seg + 4);
    else wait_ge<2>(ctrl, C_PT + (lane & 15) * 32, lane & 8 ? 10 : 8);
  }
  BAR();
#pragma unroll
  for (int j = 0; j < 16; ++j) pt[j & 1][j >> 1] = ld16s(rH, lane * 16 + (j & 3) * 1024, sp + wave * 32768 + (j >> 3) * 16384 + ((j >> 2) & 1) * 4096);
#pragma unroll
  for (int j = 0; j < 4; ++j) pm[j & 1][j >> 1] = ld16s(rH, g * 512 + n * 16 + (j & 1) * 256 + (j >> 1) * 2048, sp + lmap(65536 + wave * 4096));
#pragma unroll
  for (int t = 0; t < NB; ++t) ldz(t);
  __builtin_amdgcn_sched_barrier(0);
#pragma unroll
  for (int t = 0; t < NT; ++t) {
    f4 acc[2] = {};
#pragma unroll
    for (int j = 0; j < 8; ++j) { acc[0] = mfma32(y[t % NB][j], pt[0][j], acc[0]); acc[1] = mfma32(y[t % NB][j], pt[1][j], acc[1]); }
#pragma unroll
    for (int j = 0; j < 2; ++j) { acc[0] = mfma32(z[t % NB][j], pm[0][j], acc[0]); acc[1] = mfma32(z[t % NB][j], pm[1][j], acc[1]); }
    __builtin_amdgcn_sched_barrier(0);
    if (t + NB < NT) { ldy(t + NB); ldz(t + NB); }
#pragma unroll
    for (int mt = 0; mt < 2; ++mt) *(LAS f4*)(wave * (32 * (NT * 64 + 16)) + (mt * 16 + n) * (NT * 64 + 16) + t * 64 + g * 16) = acc[mt];
    __builtin_amdgcn_sched_barrier(0);
  }
  LGKM0(); BAR();
  const rsrc_t rT = mkrsrc(p.ws + X1_OFF, (unsigned)(WS_END - X1_OFF));
#pragma unroll
  for (int i = 0; i < (NT + 3) / 4; ++i) {
    const int it = i * 256 + wave * 64 + lane, tok = (it / (2 * NT)) & 31, ch = it % (2 * NT);
    f4 a = {}, b = {};
#pragma unroll
    for (int w = 0; w < 4; ++w) { const unsigned ad = w * (32 * (NT * 64 + 16)) + tok * (NT * 64 + 16) + ch * 32; a += *(LAS f4*)ad; b += *(LAS f4*)(ad + 16); }
    __builtin_amdgcn_raw_buffer_store_b128(v4u{pack2(a[0], a[1]), pack2(a[2], a[3]), pack2(b[0], b[1]), pack2(b[2], b[3])}, rT,
                                           it < 64 * NT ? (seg * 32 + tok) * 2560 + t0 * 32 + ch * 16 : OOB, 0, 16);
  }
}
#define OSPLIT 54
static_assert(OSPLIT <= 2 * RING && OSPLIT >= NSEG - RING, "Z offset, arrival counts");
__device__ __forceinline__ void o_stream(int id, int nb, int rep, int wave) {
  const Params p = load_params();
  gu32* ctrl = (gu32*)(p.ws + CTRL_OFF);
  const int lane = fresh_lane(), g = lane >> 4, n = lane & 15;
  const rsrc_t rH = mkrsrc(p.ws, WS_BYTES);
  const int ta = side_a(p, slot_blk(lane)), tb = side_a(p, slot_blk(lane) + 1), tp = pt_off(p, lane);
  const int vy = (n >> 3) * 32768 + g * 128 + (n & 7) * 16, vz = (n >> 3) * 4096 + g * 128 + (n & 7) * 16;
  v4u y[5][8];
  auto ld = [&](int u, int it, int t) {
    const int sa = __builtin_amdgcn_readlane(ta, u >> 1), sb = __builtin_amdgcn_readlane(tb, u >> 1), T = (u & 1) * 20 + wave * 5 + t;
    const int so = it < 4 ? (T < 32 ? sa : sb - (1 << 21)) + T * 65536 : (sb ^ 8192) + (u >= 2 * RING ? 655360 : 0) + T * 16384;
    const int vo = it < 4 ? vy + lmap(((it + wave) & 3) * 4096) : vz;
#pragma unroll
    for (int j = 0; j < 8; ++j) y[t][j] = ld16s(rH, vo + j * 512, so);
  };
  if (wave == 0) wait_ge<8>(ctrl, C_G + rep * 32, (id >> 1) + 1);
  BAR();
#pragma unroll
  for (int t = 0; t < 5; ++t) ld(id, 0, t);
  int up = -1, nu = 0;
  RowIn ri;
#pragma unroll 1
  for (int u = id; u < 2 * OSPLIT; u += nb) {
    const int seg = u >> 1, un = min(u + nb, 2 * OSPLIT - 1);
    TS(id == 3 && wave == 0 && lane == 0 && u == id + 2 * nb, 40)
    if (wave == 0) wait_ge<8>(ctrl, C_G + rep * 32, seg + 4);
    BAR();
    TS(id == 3 && wave == 0 && lane == 0 && u == id + 2 * nb, 41)
    const int sp = __builtin_amdgcn_readlane(tp, seg);
#pragma unroll 1
    for (int k = 0; k < 20; ++k) dma16_sc1(rH, lane * 16, sp + lmap((wave * 20 + k) * 1024), (wave * 20 + k) * 1024);
    VMCNT(0); BAR();
    if (up >= 0 && wave == 1 && lane == 0) __hip_atomic_fetch_add(ctrl + C_AF + (up >> 1) * 32, 1u, RLX_AGENT);
    TS(id == 3 && wave == 0 && lane == 0 && u == id + 2 * nb, 42)
    f4 acc[2][5] = {};
#pragma unroll
    for (int it = 0; it < 5; ++it) {
      if (it == 4) {
        if (wave == 0) wait_ge<8>(ctrl, lane & 1 ? C_G + rep * 32 : C_AF + (max(up, 0) >> 1) * 32, lane & 1 ? (un >> 1) + 1 : up >= 0 ? 2 : 0);
        BAR();
        rows_load(ri, up * 16 + wave * 4);
      }
      v4u pf[2][8];
      const unsigned lp = it < 4 ? ((it + wave) & 3) * 16384 + lane * 16 : 65536 + g * 512 + n * 16;
#pragma unroll
      for (int j = 0; j < 16; ++j) pf[j & 1][j >> 1] = lds128(lp + (j >> 1) * 2048 + (j & 1) * (it < 4 ? 1024 : 256));
#pragma unroll
      for (int t = 0; t < 5; ++t) {
#pragma unroll
        for (int j = 0; j < 8; ++j) { acc[0][t] = mfma32(y[t][j], pf[0][j], acc[0][t]); acc[1][t] = mfma32(y[t][j], pf[1][j], acc[1][t]); }
        __builtin_amdgcn_sched_barrier(0);
        if (it < 4) ld(u, it + 1, t); else ld(un, 0, t);
        __builtin_amdgcn_sched_barrier(0);
      }
    }
#pragma unroll
    for (int k = 0; k < 10; ++k) {
      const f4 a = acc[k & 1][k >> 1];
      *(LAS v2u*)(81920 + ((k & 1) * 16 + n) * 656 + (wave * 5 + (k >> 1)) * 32 + g * 8) = v2u{pack2(a[0], a[1]), pack2(a[2], a[3])};
    }
    LGKM0(); BAR();
#pragma unroll
    for (int k = 0; k < 5; ++k) {
      const int c = k * 64 + lane, r = wave * 8 + c / 40, o = (c % 40) * 16;
      __builtin_amdgcn_raw_buffer_store_b128(lds128(81920 + r * 656 + o), rH, X1_OFF + (seg * 32 + r) * 2560 + (u & 1) * 640 + o, 0, 16);
    }
    __builtin_amdgcn_sched_barrier(0);
    TS(id == 3 && wave == 0 && lane == 0 && u == id + 2 * nb, 43)
    if (up >= 0) rows_do(ri, up * 16 + wave * 4);
    TS(id == 3 && wave == 0 && lane == 0 && u == id + 2 * nb, 44)
    TS(up >= 0 && wave == 0 && lane == 0, 704 + (up >> 1))
    up = u; ++nu;
  }
  VMCNT(0); BAR();
  if (wave == 0) {
    if (lane == 0) __hip_atomic_fetch_add(ctrl + C_AF + (up >> 1) * 32, 1u, RLX_AGENT);
    wait_ge<4>(ctrl, C_AF + (up >> 1) * 32, 2);
  }
  BAR();
  rows_load(ri, up * 16 + wave * 4);
  rows_do(ri, up * 16 + wave * 4);
  VMCNT(0); BAR();
  TS(wave == 0 && lane == 0, 704 + (up >> 1))
  rows_fin(nu, wave);
}
__device__ __forceinline__ void o_role(int u, int rep, int wave) {
  static_assert((NSEG - OSPLIT) * 10 <= 128, "units");
  if (u < (NSEG - OSPLIT) * 10) {
    const Params p = load_params();
    gu32* ctrl = (gu32*)(p.ws + CTRL_OFF);
    const int lane = fresh_lane();
    const int seg = OSPLIT + u / 10;
    o_unit<4>(p, seg, (u % 10) * 4, rep, lane, wave);
    VMCNT(0); BAR();
    if (wave == 0) {
      unsigned t = 0;
      if (lane == 0) t = __hip_atomic_fetch_add(ctrl + C_AF + seg * 32, 1u, RLX_AGENT);
      t = rfl(t);
      if (t == 8) wait_ge<1>(ctrl, C_AF + seg * 32, 10);
      if (lane == 0) *(LAS unsigned*)(L_MISC + 4) = t;
    }
    __syncthreads();
    const int t = rfl(*(LAS int*)(L_MISC + 4));
    if (t < 8) return;
    RowIn ri;
    rows_load(ri, seg * 32 + (t - 8) * 16 + wave * 4);
    rows_do(ri, seg * 32 + (t - 8) * 16 + wave * 4);
    VMCNT(0); BAR();
    TS(wave == 0 && lane == 0, 704 + seg)
    rows_fin(1, wave);
  }
}

__device__ __forceinline__ void s_role(const Params& p, int h, int rep, int lane, int wave) {
  constexpr int L_K = 24576, L_D = 30720, L_Q = 57344, L_MEM = 63488, L_RES = 73728;
  const int g = lane >> 4, n = lane & 15, q = (lane >> 2) & 3, pp = lane & 3, j0 = 4 * (g & 1) + 2 * (g >> 1);
  v32u vc; v12u sr; v16u sc;
  vc[0] = n < 8 ? 0x4444 : 0xEEEE;
  vc[1] = lane * 16;
  vc[2] = L_MEM + lane * 16;
  vc[3] = L_RES + wave * 22528 + lane * 16;
  vc[4] = 0;
  vc[5] = L_Q + g * 1024 + q * 128 + (4 * g + q) * 8 + pp * 2;
  vc[6] = L_K + g * 128 + (n ^ (8 * (g >> 1))) * 8;
  vc[7] = lane < 63 ? side_a(p, slot_blk(lane + 1) + 1) : OOB;
  vc[8] = lmap(KQ_OFF + h * 24576) + lane * 16;
  vc[9] = L_K + pp * 128 + ((4 * g + q) ^ (8 * (pp >> 1))) * 8;
  vc[10] = 0;
  vc[11] = hand_off(p, lane) + rep * 2 * H_COPY;
  vc[12] = L_D + lane * 4;
  const int kt = wave == 2, tok = (g * 16 + 8 * kt + 4 * (q >> 1) + pp) * 16;
  if (wave == 1 || wave == 2) {
    vc[13] = n - 4 * g;
    vc[14] = lane == 0 ? CTRL_OFF + (C_PT + (h & 7) * 32) * 4 : OOB;
    vc[15] = pt_off(p, lane);
    vc[16] = lmap(h * 2048 + kt * 1024 + lane * 16);
    vc[17] = lmap(65536 + h * 512 + (16 * kt + n) * 16 + j0 * 2);
    vc[18] = lane * 16;
  } else {
    vc[13] = lmap(H_P) + (n < 8 ? h * 128 + n * 16 + j0 * 2 : 4096 + (h >> 3) * 1024 + ((2 * (((h >> 1) ^ h) & 1) + (g & 1)) * 16 + (h & 1) * 8 + n - 8) * 16 + ((h >> 2) & 1) * 8 + (g >> 1) * 4);
    vc[14] = j0 == (n & 7) ? 0x3f800000u : 0u;
    vc[15] = j0 + 1 == (n & 7) ? 0x3f800000u : 0u;
    vc[16] = h == 0 && lane == 0 ? CTRL_OFF + C_G0 * 4 : OOB; vc[17] = 0;
    vc[18] = flag_off(p, h) + (lane % 12) * 4;
  }
  if (wave == 3) { vc[13] = lane < 4 ? lane * 2 * H_COPY + lmap(H_NOTE + rep * 128) : OOB; vc[14] = look_off(lane); }
  vc[19] = 0;
  vc[20] = (q & 1) * 1024 + tok;
  vc[21] = 16384 + (q & 1) * 8 + tok;
  vc[22] = lmap(H_E + h * 512 + (2 * kt + (g >> 1)) * 128 + (n & 7) * 16 + (g & 1) * 8 + (n >> 3) * 4);
  vc[23] = 0;
#pragma unroll
  for (int k = 0; k < 3; ++k) { const int c = k * 64 + lane; vc[24 + k] = ((16 * wave + c / 24) * HD + h * DH) * 2 + ((c % 24) ^ (c / 24 & 4)) * 16 - k * 1024; }
  vc[27] = (16 * (g >> 1) + 8 * (q >> 1) + 4 * (g & 1) + (q & 1)) * 384 + (((32 * wave + 8 * pp) * 2) ^ (64 * (g & 1)));
  vc[28] = lane == 0 ? CTRL_OFF + (C_ANCH + 128 + rep * 32) * 4 : OOB;
  vc[29] = CTRL_OFF + (C_ANCH + rep * 32 + lane % 3) * 4;
  vc[30] = (16 * (g >> 1) + 8 * (q >> 1) + 4 * (g & 1) + (q & 1)) * 384 + (((128 + 16 * wave + 4 * pp) * 2) ^ (64 * (g & 1))); vc[31] = 0;
  set_rsrc(sr, 0, p.ws, WS_BYTES); set_rsrc(sr, 1, p.wq, DM * HD * 2); set_rsrc(sr, 2, p.wk, DM * HD * 2);
#pragma unroll
  for (int i = 4; i < 16; ++i) sc[i] = 0;
  sc[0] = wave; sc[1] = p.dL + p.dM; sc[2] = p.dS; sc[3] = rfl(side_a(p, 1)); sc[9] = p.dL * 23 / 4 + KQ_DELAY - 250; sc[10] = rfl(rep * 2 * H_COPY);
#pragma unroll
  for (int k = 1; k < 4; ++k) sc[5 + k] = rfl((((rep + k) & 3) - rep) * 2 * H_COPY);
  pin(sc); pin(sr);
  asm volatile(SROLE_ASM : "+{s[52:67]}"(sc), "+{v[0:31]}"(vc), "+{s[36:47]}"(sr) :: "memory", "vcc", "scc", SROLE_CLOBBERS);
  gu32* ctrl = (gu32*)(fresh_params()->ws + CTRL_OFF);
  if (fresh_lane() == 0) {
    if (sc[1] > 20000u || sc[4] > 20000u) __hip_atomic_store(ctrl + C_ERR, 1u, RLX_AGENT);
  }
}

__device__ __forceinline__ void c_role(const Params& p, int c8, int rep, int lane, int wave) {
  constexpr int L_Z = 10240;
  const int g = lane >> 4, n = lane & 15, j0 = 4 * (g & 1) + 2 * (g >> 1), hi = c8 >> 6;
  v32u vc; v4u sr; v16u sc;
  OPAQUE(vc);
  vc[0] = n < 8 ? 0x4444 : 0xEEEE;
  vc[1] = lane * 16;
  vc[2] = L_Z + (g >> 1) * 256 + n * 16 + (g & 1) * 8;
  vc[3] = L_Z + n * 16 + j0 * 2;
  vc[4] = hand_off(p, lane) + rep * 2 * H_COPY;
  vc[5] = (c8 & 63) * 32768 + (n >> 3) * 512 + g * 128 + (n & 7) * 16;
  vc[6] = side_a(p, 2 * (USLOT + lane % 20) + hi);
  vc[7] = lane == 0 && (wave == 0 || wave == 3) ? CTRL_OFF + (C_ANCH + 128 + rep * 32) * 4 : OOB;
  vc[8] = CTRL_OFF + (C_ANCH + rep * 32 + lane % 3) * 4;
  if (wave == 3) {
    vc[9] = side_a(p, slot_blk(lane) + hi);
    vc[10] = (n >> 3) * 512 + g * 128 + (n & 7) * 16;
    vc[11] = vc[10] ^ 512;
  } else {
    vc[9] = z_off(p, lane);
    vc[10] = lmap(c8 * 4096) + lane * 16;
    vc[11] = lane == 0 ? CTRL_OFF + (C_Z + (c8 & 7) * 32) * 4 : OOB;
  }
  vc[12] = ((n & 7) >> 1) - 2 * (g & 1);
  vc[13] = lmap(c8 * 128) + (n & 7) * 16 + j0 * 2 + ((rep + (n >> 3)) & 3) * 2 * H_COPY;
  if (wave == 3) vc[2] = lmap(c8 * 128) + (n & 7) * 16 + j0 * 2 + ((rep + 2 + (n >> 3)) & 3) * 2 * H_COPY;
  vc[14] = CTRL_OFF + (C_U + (lane % 12) * 32) * 4;
  vc[15] = 32;
#pragma unroll
  for (int i = 0; i < 5; ++i) vc[18 + i] = vc[5] + lmap((wave * 5 + i) * 1024);
  vc[16] = lane < 4 ? lane * 2 * H_COPY + lmap(H_NOTE + (4 + rep) * 128) : OOB; vc[17] = look_off(lane);
  set_rsrc(sr, 0, p.ws, WS_BYTES);
#pragma unroll
  for (int i = 4; i < 16; ++i) sc[i] = 0;
  sc[0] = wave; sc[1] = p.dL + p.dM; sc[2] = p.dS; sc[3] = p.dZ; sc[8] = rfl(rep * 2 * H_COPY);
  pin(sc); pin(sr);
  asm volatile(CROLE_ASM : "+{s[52:67]}"(sc), "+{v[0:31]}"(vc) : "{s[36:39]}"(sr) : "memory", "vcc", "scc", CROLE_CLOBBERS);
  gu32* ctrl = (gu32*)(fresh_params()->ws + CTRL_OFF);
  if (fresh_lane() == 0) {
    if (sc[1] > 20000u || sc[5] > 20000u) __hip_atomic_store(ctrl + C_ERR, 1u, RLX_AGENT);
  }
}

__device__ __forceinline__ void start_chain(const Params& p, int lane) {
  gu32* ctrl = (gu32*)(p.ws + CTRL_OFF);
  const int ho = hand_off(p, lane);
  const rsrc_t rH = mkrsrc(p.ws, WS_BYTES);
  wait_ge<1>(ctrl, C_ANCH + 128 + (lane & 3) * 32, 28);
  unsigned A = now_ticks() * 4 + p.lead;
  int x = XSTART;
  auto plan = [&](int s) {
    const v4u v = {A | (s == 0), (A + x + p.dE) | (s == 0), (A + x + p.dL) | (s == 0), 0u};
    const int hs = __builtin_amdgcn_readlane(ho, (s - 1) & 63);
    __builtin_amdgcn_raw_buffer_store_b128(v, rH, lane >= 32 ? OOB : (s ? hs + (lane >> 3) * 2 * H_COPY + lmap(H_PLAN) : CTRL_OFF + (C_ANCH + (lane >> 3) * 32) * 4) + (lane & 7) * 16, 0, 16);
    A += p.dL + p.dM + 2 * x; x >>= 1;
  };
#pragma unroll 1
  for (int s = 0; s < 4; ++s) plan(s);
#pragma unroll 1
  for (int g = 1, n1 = 0, n2 = 0, n3 = 0, calm = 0; g <= NSEG; ++g) {
    wait_ge<GPOLL>(ctrl, C_G0, g << 16);
    const int n = rfl(__hip_atomic_load(ctrl + C_G0, RLX_AGENT)) & 0xffff;
    if (n > n1 && n1 > n2 && n2 > n3 && g >= calm && g < NSEG) {
      const int hs = __builtin_amdgcn_readlane(ho, g);
      if (lane < 4) __hip_atomic_store(ctrl + ((long)hs + lane * 2 * H_COPY + lmap(H_PLAN) - CTRL_OFF) / 4 + 3, (unsigned)(n + MEET), RLX_AGENT);
      calm = g + 4;
    }
    n3 = n2; n2 = n1; n1 = n;
    if (lane < 16) __hip_atomic_store(ctrl + C_G + lane * 32, (unsigned)g, RLX_AGENT);
    if (g + 3 < NSEG) plan(g + 3);
  }
}

__global__ void __launch_bounds__(256, 1) k_main(Params p) {
  extern __shared__ __attribute__((aligned(16))) unsigned char smem[];
  const int tid = threadIdx.x, wave = rfl(tid >> 6);
  gu32* ctrl = (gu32*)(p.ws + CTRL_OFF);
  if (tid == 0) {
    const unsigned x = xcc_id();
    int role = -1;
    if (x < 4) { unsigned t = __hip_atomic_fetch_add(ctrl + C_TICK + x * 64, 1u, RLX_AGENT); if (t < 32) role = x * 32 + t; }
    if (role < 0) { unsigned t = __hip_atomic_fetch_add(ctrl + C_TICK + 4 * 64, 1u, RLX_AGENT); if (t < 128) role = 128 + t; }
    for (int gg = 0; gg < 4 && role < 0; ++gg) { unsigned t = __hip_atomic_fetch_add(ctrl + C_TICK + gg * 64, 1u, RLX_AGENT); if (t < 32) role = gg * 32 + t; }
    *(int*)smem = role;
  }
  __syncthreads();
  const int role = rfl(*(int*)smem);
  __syncthreads();
  if (role < 0) return;
  if (tid < NBLK) { const unsigned x = *(const GAS unsigned*)(p.ws + ((size_t)tid << 21) + 0x1FF000 + (role & 31) * 128); asm volatile("" ::"v"(x)); }
  const int x = role >> 5, t = role & 31;
  const int job0 = role;
  int job = job0 | wave << 8;
#define JOB() job = rfl(job); asm volatile("" : "+s"(job)); const int wv = job >> 8, jb = job & 255, jx = jb >> 5, jt = jb & 31, type = jb & 3, head = (jb - 128) >> 2; (void)jx; (void)jt; (void)type; (void)head;
#define TSJ(i) { JOB() const Params p = load_params(); TS(wv == 0 && fresh_lane() == 0, (i) + jb) }
  {
    JOB()
    if (jb < 128 && jt >= 8) {
      u_role(load_params(), ((jx + (jt - 8) / 3) & 3) * 8 + (jt - 8) / 3, (jt - 8) % 3, fresh_lane(), wv);
      TSJ(256)
    }
  }
  JOB()
  if (jb < 128 && jt < 28) {
    if (jt < 8) s_role(load_params(), jx * 8 + jt, jx, fresh_lane(), wv);
    else c_role(load_params(), jx * 20 + jt - 8, jx, fresh_lane(), wv);
    __syncthreads();
    JOB()
    ffn_role(load_params(), jx * 28 + jt, fresh_lane(), wv);
  } else {
    if (jb == 127) { if (wv == 0) start_chain(load_params(), fresh_lane()); }
    else if (jb >= 128) {
      pause(type < 2 ? KDELAY : 0);
      token_start(load_params(), type, head, fresh_lane(), wv);
      if (type < 3) proj_role(load_params(), type, head, fresh_lane(), wv);
      else vo_role(load_params(), head, fresh_lane(), wv);
      TSJ(768)
    }
    JOB()
    row_params(wv);
    if (jb < 127) o_stream(jx * 4 + jt - 28, 15, jb & 15, wv);
    else if (jb > 127) o_role(type * 32 + head, jb & 15, wv);
  }
  TSJ(1024)
}

#define SCHED_DE 396
#define SCHED_DL 468
#define SCHED_DM 184
#define SCHED_DZ 96
#define SCHED_DS 440
#define SCHED_LEAD 520

extern "C" void kernel_launch(void* const* d_in, const int* in_sizes, int n_in, void* d_out, int out_size, void* d_ws, size_t ws_size,
                              hipStream_t stream) {
  Params p;
  p.x = (const u16*)d_in[0]; p.mem0 = (const u16*)d_in[1]; p.wq = (const u16*)d_in[2]; p.wk = (const u16*)d_in[3];
  p.wv = (const u16*)d_in[4]; p.wo = (const u16*)d_in[5]; p.wr = (const u16*)d_in[8]; p.wg = (const u16*)d_in[9];
  p.wu = (const u16*)d_in[10]; p.wd = (const u16*)d_in[11]; p.c = (const u16*)d_in[12]; p.wmod = (const u16*)d_in[13];
  p.out = (u16*)d_out;
  p.ws = (unsigned char*)(((uintptr_t)d_ws + ws_size - WS_BYTES) & ~(uintptr_t)(2 * MB - 1));
  (void)hipFuncSetAttribute((const void*)k_main, hipFuncAttributeMaxDynamicSharedMemorySize, MAIN_LDS);
  p.dE = SCHED_DE; p.dL = SCHED_DL; p.dM = SCHED_DM; p.dZ = SCHED_DZ; p.dS = SCHED_DS; p.lead = SCHED_LEAD;
  k_pre<<<PRE_GRID, 256, 0, stream>>>(p);
  k_main<<<256, 256, MAIN_LDS, stream>>>(p);
}
```

```cpp
#include <hip/hip_runtime.h>
#include <hip/hip_bf16.h>
#include <stdint.h>
#include <type_traits>

#define GAS __attribute__((address_space(1)))
#define LAS __attribute__((address_space(3)))
#define RLX_AGENT __ATOMIC_RELAXED, __HIP_MEMORY_SCOPE_AGENT
typedef unsigned short u16;
typedef unsigned v4u __attribute__((ext_vector_type(4)));
typedef unsigned v2u __attribute__((ext_vector_type(2)));
typedef unsigned v8u __attribute__((ext_vector_type(8)));
typedef float f4 __attribute__((ext_vector_type(4)));
typedef __bf16 bf2 __attribute__((ext_vector_type(2)));
typedef __bf16 bf8 __attribute__((ext_vector_type(8)));
typedef __bf16 bf16v __attribute__((ext_vector_type(16)));
typedef short s4 __attribute__((ext_vector_type(4)));
typedef GAS unsigned gu32;
typedef __amdgpu_buffer_rsrc_t rsrc_t;

constexpr int S = 2048, DM = 640, NH = 32, DH = 192, HD = NH * DH, NSEG = 64, FF = 1536, TOPK = 8;

constexpr size_t MB = 1u << 20;
constexpr int RING = 32, HRING = 24, USLOT = 12, HB = 64, PB = 68, LB = 74;
__host__ __device__ constexpr int slot_blk(int s) { return 2 * (s % RING); }
constexpr int NCAL = LB + 1, NBLK = LB + 6;
constexpr unsigned WS_BYTES = (unsigned)NBLK << 21;
constexpr int CTRL_OFF = LB << 21;
constexpr int CTRL_BYTES = 131072;
constexpr int MODB_OFF = CTRL_OFF + CTRL_BYTES;
constexpr int HF_OFF = CTRL_OFF + (1 << 21);
constexpr int HF_BYTES = S * DM * 2;
constexpr int X1_OFF = HF_OFF + HF_BYTES;
constexpr int ACT_OFF = X1_OFF + 2 * HF_BYTES;
constexpr int WS_END = ACT_OFF + 2 * TOPK * FF * 2;
constexpr int TRACE_OFF = (LB + 5) << 21;
static_assert(WS_END <= TRACE_OFF, "workspace");
constexpr int H_COPY = 40960, H_STEP = 4 * H_COPY, H_FLAG = 10240, H_NOTE = 14336, H_PLAN = 15360, H_E = 16384, H_P = 32768, KQ_OFF = 262144, PT_SEG = 81920;

constexpr int C_ANCH = 0;
constexpr int C_IDX = 256;
constexpr int C_U = 768;
constexpr int C_LN = 1152;
constexpr int C_MOD = 1408;
constexpr int C_PT = 1536;
constexpr int C_Z = 1792;
constexpr int C_G = 4096;
constexpr int C_G0 = 4608;
constexpr int C_ERR = 4736;
constexpr int C_VF = 5120;
constexpr int C_AF = 8192;
constexpr int C_R = 12288;
constexpr int ZERO_BYTES = 49152;
constexpr int TAB_OFF = CTRL_OFF + 0x1C000, SUM_OFF = TAB_OFF + 0x3800, EPOCH_OFF = TAB_OFF + 0x3F00, NTAB = NCAL + 14;
static_assert(NTAB <= 112 && NCAL <= 96, "table");
constexpr int DBG_OFF = CTRL_OFF + 65536, C_STAT = 0, C_DBG = 2048;
__host__ __device__ constexpr int lmap(int off) { return ((off >> 13) << 14) | (off & 8191); }
constexpr int OOB = 0x40000000;

__device__ __forceinline__ float u2f(unsigned v) { return __builtin_bit_cast(float, v); }
__device__ __forceinline__ unsigned f2u(float v) { return __builtin_bit_cast(unsigned, v); }
__device__ __forceinline__ float bf2f(u16 v) { return __builtin_bit_cast(float, (unsigned)v << 16); }
__device__ __forceinline__ float lo2f(unsigned v) { return __builtin_bit_cast(float, v << 16); }
__device__ __forceinline__ float hi2f(unsigned v) { return __builtin_bit_cast(float, v & 0xffff0000u); }
__device__ __forceinline__ unsigned pack2(float a, float b) { bf2 v = {(__bf16)a, (__bf16)b}; return __builtin_bit_cast(unsigned, v); }
__device__ __forceinline__ float rbf(float a) { return (float)(__bf16)a; }
__device__ __forceinline__ unsigned long long uni64(unsigned long long a) {
  return (unsigned)__builtin_amdgcn_readfirstlane((unsigned)a) | (unsigned long long)(unsigned)__builtin_amdgcn_readfirstlane((unsigned)(a >> 32)) << 32;
}
__device__ __forceinline__ rsrc_t mkrsrc(const void* p, unsigned bytes) { return __builtin_amdgcn_make_buffer_rsrc((void*)uni64((unsigned long long)p), 0, bytes, 0x00020000); }
__device__ __forceinline__ unsigned xcc_id() { return (unsigned)__builtin_amdgcn_s_getreg(20 | (3 << 11)) & 0xfu; }
__device__ __forceinline__ int rfl(int v) { return __builtin_amdgcn_readfirstlane(v); }
__device__ __forceinline__ f4 mfma32(v4u a, v4u b, f4 c) { return __builtin_amdgcn_mfma_f32_16x16x32_bf16(__builtin_bit_cast(bf8, a), __builtin_bit_cast(bf8, b), c, 0, 0, 0); }
__device__ __forceinline__ f4 mfma16(v2u a, v2u b, f4 c) { return mfma32(v4u{a[0], a[1], 0u, 0u}, v4u{b[0], b[1], 0u, 0u}, c); }
__device__ __forceinline__ f4 smfma(v4u a, v8u b, f4 c, int idx) { return __builtin_amdgcn_smfmac_f32_16x16x64_bf16(__builtin_bit_cast(bf8, a), __builtin_bit_cast(bf16v, b), c, idx, 0, 0); }
__device__ __forceinline__ v2u trread(unsigned addr) { return __builtin_bit_cast(v2u, __builtin_amdgcn_ds_read_tr16_b64_v4i16((LAS s4*)addr)); }
__device__ __forceinline__ v4u lds128(unsigned addr) { return *(LAS v4u*)addr; }
__device__ __forceinline__ v2u lds64(unsigned addr) { return *(LAS v2u*)addr; }
__device__ __forceinline__ void dma16(rsrc_t r, int voff, int soff, unsigned ldsaddr) {
  asm volatile("s_mov_b32 m0, %0\n\ts_nop 3\n\tbuffer_load_dwordx4 %1, %2, %3 offen lds" ::"s"(ldsaddr), "v"(voff), "s"(r), "s"(soff) : "memory");
}
__device__ __forceinline__ void dma16_sc1(rsrc_t r, int voff, int soff, unsigned ldsaddr) {
  asm volatile("s_mov_b32 m0, %0\n\ts_nop 3\n\tbuffer_load_dwordx4 %1, %2, %3 offen sc1 lds" ::"s"(ldsaddr), "v"(voff), "s"(r), "s"(soff) : "memory");
}
#define VMCNT(n) asm volatile("s_waitcnt vmcnt(" #n ")" ::: "memory")
#define LGKM0() asm volatile("s_waitcnt lgkmcnt(0)" ::: "memory")
#define BAR() asm volatile("s_barrier" ::: "memory")
__device__ __forceinline__ float swapsum32(float a, float b) {
  auto r = __builtin_amdgcn_permlane32_swap(__builtin_bit_cast(unsigned, a), __builtin_bit_cast(unsigned, b), false, false);
  return u2f(r[0]) + u2f(r[1]);
}
__device__ __forceinline__ float swapsum16(float a, float b) {
  auto r = __builtin_amdgcn_permlane16_swap(__builtin_bit_cast(unsigned, a), __builtin_bit_cast(unsigned, b), false, false);
  return u2f(r[0]) + u2f(r[1]);
}
__device__ __forceinline__ float max_x32(float a) {
  auto r = __builtin_amdgcn_permlane32_swap(__builtin_bit_cast(unsigned, a), __builtin_bit_cast(unsigned, a), false, false);
  return fmaxf(u2f(r[0]), u2f(r[1]));
}
__device__ __forceinline__ float max_x16(float a) {
  auto r = __builtin_amdgcn_permlane16_swap(__builtin_bit_cast(unsigned, a), __builtin_bit_cast(unsigned, a), false, false);
  return fmaxf(u2f(r[0]), u2f(r[1]));
}
__device__ __forceinline__ unsigned ror8u(unsigned v) { return (unsigned)__builtin_amdgcn_update_dpp(0, (int)v, 0x128, 0xf, 0xf, true); }
__device__ __forceinline__ float ror8f(float v) { return __builtin_bit_cast(float, ror8u(__builtin_bit_cast(unsigned, v))); }
__device__ __forceinline__ float wave_sum(float v) {
  v += u2f(__builtin_amdgcn_update_dpp(0, (int)f2u(v), 0xb1, 0xf, 0xf, true)); v += u2f(__builtin_amdgcn_update_dpp(0, (int)f2u(v), 0x4e, 0xf, 0xf, true));
  v += u2f(__builtin_amdgcn_update_dpp(0, (int)f2u(v), 0x124, 0xf, 0xf, true)); v += ror8f(v);
  v = swapsum16(v, v);
  return swapsum32(v, v);
}
typedef unsigned short us2 __attribute__((ext_vector_type(2)));
__device__ __forceinline__ unsigned unsent(unsigned w) { return __builtin_bit_cast(unsigned, __builtin_elementwise_min(__builtin_bit_cast(us2, w), us2{0xfffe, 0xfffe})); }

constexpr int OSPLIT = 52;
constexpr unsigned CODE_BYTES = 111616;
struct Params {
  const u16 *x, *mem0, *wq, *wk, *wv, *wo, *wr, *wg, *wu, *wd, *c, *wmod;
  u16* out;
  unsigned char* ws;
  int dE, dL, dM, dZ, dS, lead;
  unsigned m0, m1, m2;
  int co;
};

#define XSTART 64
#define MEET 3
#define AHEAD 11
static_assert(AHEAD + 9 <= HRING, "hand-off ring");
#define SDELAY 0
#define KDELAY 0
#define FSTART 22.f
#define FDECAY 0.2f
#define LEVUP 4
#define SLOPE_E 1600.f
#define SLOPE_L 1900.f
#define SLOPE_M 500.f
#define HORIZON 2
#define GLOOK 60
#define CALREP 4
#define SUMWAIT 60
#define CALWIN 10
#define GPOLL 12
#define KQ_DELAY -100
constexpr int MAIN_LDS = 163840;
constexpr int SPIN_CAP = 1 << 18;
typedef unsigned v12u __attribute__((ext_vector_type(12)));
typedef unsigned v16u __attribute__((ext_vector_type(16)));
typedef unsigned v32u __attribute__((ext_vector_type(32)));

#define SROLE_CLOBBERS \
  "v32", "v33", "v34", "v35", "v36", "v37", "v38", "v39", "v40", "v41", "v42", "v43", "v44", "v45", "v46", "v47", "v48", "v49", "v50", "v51", "v52", \
  "v53", "v54", "v55", "v56", "v57", "v58", "v59", "v60", "v61", "v62", "v63", "v64", "v65", "v66", "v67", "v68", "v69", "v70", "v71", "v72", "v73", \
  "v74", "v75", "v76", "v77", "v78", "v79", "v80", "v81", "v82", "v83", "v84", "v85", "v86", "v87", "v88", "v89", "v90", "v91", "v92", "v93", "v94", \
  "v95", "v96", "v97", "v98", "v99", "v100", "v101", "v102", "v103", "v104", "v105", "v106", "v107", "v108", "v109", "v110", "v111", "v112", "v113", \
  "v114", "v115", "v116", "v117", "v118", "v119", "v120", "v121", "v122", "v123", "v124", "v125", "v126", "v127", "v128", "v129", "v130", "v131", \
  "v132", "v133", "v134", "v135", "v136", "v137", "v138", "v139", "v140", "v141", "v142", "v143", "v144", "v145", "v146", "v147", "v148", "v149", \
  "v150", "v151", "v152", "v153", "v154", "v155", "v156", "v157", "v158", "v159", "v160", "v161", "v162", "v163", "v164", "v165", "v166", "v167", \
  "v168", "v169", "v170", "v171", "v172", "v173", "v174", "v175", "v176", "v177", "v178", "v179", "v180", "v181", "v182", "v183", "v184", "v185", \
  "v186", "v187", "v188", "v189", "v190", "v191", "v192", "v193", "v194", "v195", "v196", "v197", "v198", "v199", "v200", "v201", "v202", "v203", \
  "v204", "v205", "v206", "v207", "v208", "v209", "v210", "v211", "v212", "v213", "v214", "v215", "v216", "v217", "v218", "v219", "v220", "v221", \
  "v222", "v223", "v224", "v225", "v226", "v227", "v228", "v229", "v230", "v231", "v232", "v233", "v234", "v235", "v236", "v237", "v238", "v239", \
  "v240", "v241", "v242", "v243", "v244", "v245", "v246", "v247", "v248", "v249", "v250", "v251", "v252", "v253", "v254", "v255", "s34", "s35", "s48", \
  "s49", "s50", "s51", "s68", "s69", "s70", "s71", "s72", "s73", "s74", "s75", "s76", "s77", "s78", "s79", "s80", "s81", "s82", "s83", "s84", "s85", \
  "s86", "s87", "s88", "s89", "s90", "s93", "s94", "s95", "s96", "a0", "a1", "a2", "a3", "a4", "a5", "a6", "a7", "a8", "a9", "a10", "a11", "a12", "a13", \
  "a14", "a15", "a16", "a17", "a18", "a19", "a20", "a21", "a22", "a23", "a24", "a25", "a26", "a27", "a28", "a29", "a30", "a31", "a32", "a33", "a34", \
  "a35", "a36", "a37", "a38", "a39", "a40", "a41", "a42", "a43", "a44", "a45", "a46", "a47", "a48", "a49", "a50", "a51", "a52", "a53", "a54", "a55", \
  "a56", "a57", "a58", "a59", "a60", "a61", "a62", "a63", "a64", "a65", "a66", "a67", "a68", "a69", "a70", "a71", "a72", "a73", "a74", "a75", "a76", \
  "a77", "a78", "a79", "a80", "a81", "a82", "a83", "a84", "a85", "a86", "a87", "a88", "a89", "a90", "a91", "a92", "a93", "a94", "a95", "a96", "a97", \
  "a98", "a99", "a100", "a101", "a102", "a103", "a104", "a105", "a106", "a107", "a108", "a109", "a110", "a111", "a112", "a113", "a114", "a115", "a116", \
  "a117", "a118", "a119", "a120", "a121", "a122", "a123", "a124", "a125", "a126", "a127", "a128", "a129", "a130", "a131", "a132", "a133", "a134", \
  "a135", "a136", "a137", "a138", "a139", "a140", "a141", "a142", "a143", "a144", "a145", "a146", "a147", "a148", "a149", "a150", "a151", "a152", \
  "a153", "a154", "a155", "a156", "a157", "a158", "a159", "a160", "a161", "a162", "a163", "a164", "a165", "a166", "a167", "a168", "a169", "a170", \
  "a171", "a172", "a173", "a174", "a175", "a176", "a177", "a178", "a179", "a180", "a181", "a182", "a183", "a184", "a185", "a186", "a187", "a188", \
  "a189", "a190", "a191", "a192", "a193", "a194", "a195", "a196", "a197", "a198", "a199", "a200", "a201", "a202", "a203", "a204", "a205", "a206", \
  "a207", "a208", "a209", "a210", "a211", "a212", "a213", "a214", "a215", "a216", "a217", "a218", "a219", "a220", "a221", "a222", "a223", "a224", \
  "a225", "a226", "a227", "a228", "a229", "a230", "a231", "a232", "a233", "a234", "a235", "a236", "a237", "a238", "a239", "a240", "a241", "a242", \
  "a243", "a244", "a245", "a246", "a247", "a248", "a249", "a250", "a251", "a252", "a253", "a254", "a255"
#define SROLE_ASM \
  "s_mov_b32 s34, 0x00ff00ff\n" \
  "s_mov_b32 s35, 0x00ff00ff\n" \
  "s_mov_b32 s68, 0\n" \
  "s_mov_b32 s73, 0\n" \
  "s_mov_b32 s56, 0\n" \
  "s_mov_b32 s57, 0\n" \
  "s_mov_b32 s51, 0\n" \
  "s_mov_b32 s71, 0\n" \
  "s_mov_b32 s50, 5900\n" \
  "v_mov_b32 v4, 0x3dd53b94\n" \
  "s_mov_b32 s93, s58\n" \
  "s_mov_b32 s94, s59\n" \
  "s_mov_b32 s95, s60\n" \
  "s_mov_b32 s96, s61\n" \
  "s_mov_b32 s58, 0\n" \
  "s_mov_b32 s59, 0\n" \
  "s_mov_b32 s60, 0\n" \
  "s_mov_b32 s61, 0\n" \
  "s_mov_b32 s62, 0\n" \
  "s_mov_b32 s63, 0\n" \
  "s_mov_b32 s64, 0\n" \
  "s_mov_b32 s65, 0\n" \
  "s_mov_b32 s66, 0\n" \
  "s_mov_b32 s67, 0\n" \
  "s_memrealtime s[76:77]\n" \
  "s_memtime s[78:79]\n" \
  "s_waitcnt lgkmcnt(0)\n" \
  "s_mov_b32 s88, s76\n" \
  "s_mov_b32 s89, s78\n" \
  "s_mul_i32 s72, s52, 6144\n" \
  "s_add_u32 m0, s72, 0\n" \
  "s_mov_b32 s74, 0\n" \
  "buffer_load_dwordx4 v24, s[40:43], s74 offen offset:0 lds\n" \
  "buffer_load_dwordx4 v25, s[40:43], s74 offen offset:1024 lds\n" \
  "buffer_load_dwordx4 v26, s[40:43], s74 offen offset:2048 lds\n" \
  "s_add_u32 m0, s72, 3072\n" \
  "s_mov_b32 s74, 98304\n" \
  "buffer_load_dwordx4 v24, s[40:43], s74 offen offset:0 lds\n" \
  "buffer_load_dwordx4 v25, s[40:43], s74 offen offset:1024 lds\n" \
  "buffer_load_dwordx4 v26, s[40:43], s74 offen offset:2048 lds\n" \
  "s_add_u32 m0, s72, 24576\n" \
  "s_mov_b32 s74, 0\n" \
  "buffer_load_dwordx4 v24, s[44:47], s74 offen offset:0 lds\n" \
  "buffer_load_dwordx4 v25, s[44:47], s74 offen offset:1024 lds\n" \
  "buffer_load_dwordx4 v26, s[44:47], s74 offen offset:2048 lds\n" \
  "s_add_u32 m0, s72, 27648\n" \
  "s_mov_b32 s74, 98304\n" \
  "buffer_load_dwordx4 v24, s[44:47], s74 offen offset:0 lds\n" \
  "buffer_load_dwordx4 v25, s[44:47], s74 offen offset:1024 lds\n" \
  "buffer_load_dwordx4 v26, s[44:47], s74 offen offset:2048 lds\n" \
  "s_add_u32 m0, s72, 49152\n" \
  "s_mov_b32 s74, 786432\n" \
  "buffer_load_dwordx4 v24, s[40:43], s74 offen offset:0 lds\n" \
  "buffer_load_dwordx4 v25, s[40:43], s74 offen offset:1024 lds\n" \
  "buffer_load_dwordx4 v26, s[40:43], s74 offen offset:2048 lds\n" \
  "s_add_u32 m0, s72, 52224\n" \
  "s_mov_b32 s74, 884736\n" \
  "buffer_load_dwordx4 v24, s[40:43], s74 offen offset:0 lds\n" \
  "buffer_load_dwordx4 v25, s[40:43], s74 offen offset:1024 lds\n" \
  "buffer_load_dwordx4 v26, s[40:43], s74 offen offset:2048 lds\n" \
  "s_add_u32 m0, s72, 73728\n" \
  "s_mov_b32 s74, 786432\n" \
  "buffer_load_dwordx4 v24, s[44:47], s74 offen offset:0 lds\n" \
  "buffer_load_dwordx4 v25, s[44:47], s74 offen offset:1024 lds\n" \
  "buffer_load_dwordx4 v26, s[44:47], s74 offen offset:2048 lds\n" \
  "s_add_u32 m0, s72, 76800\n" \
  "s_mov_b32 s74, 884736\n" \
  "buffer_load_dwordx4 v24, s[44:47], s74 offen offset:0 lds\n" \
  "buffer_load_dwordx4 v25, s[44:47], s74 offen offset:1024 lds\n" \
  "buffer_load_dwordx4 v26, s[44:47], s74 offen offset:2048 lds\n" \
  "s_add_u32 m0, s72, 98304\n" \
  "s_mov_b32 s74, 1572864\n" \
  "buffer_load_dwordx4 v24, s[40:43], s74 offen offset:0 lds\n" \
  "buffer_load_dwordx4 v25, s[40:43], s74 offen offset:1024 lds\n" \
  "buffer_load_dwordx4 v26, s[40:43], s74 offen offset:2048 lds\n" \
  "s_add_u32 m0, s72, 101376\n" \
  "s_mov_b32 s74, 1671168\n" \
  "buffer_load_dwordx4 v24, s[40:43], s74 offen offset:0 lds\n" \
  "buffer_load_dwordx4 v25, s[40:43], s74 offen offset:1024 lds\n" \
  "buffer_load_dwordx4 v26, s[40:43], s74 offen offset:2048 lds\n" \
  "v_add_u32 v31, 73728, v27\n" \
  "v_add_u32 v23, 73728, v30\n" \
  "s_waitcnt vmcnt(24) lgkmcnt(0)\n" \
  "s_barrier\n" \
  "s_add_u32 m0, s72, 122880\n" \
  "s_mov_b32 s74, 1572864\n" \
  "buffer_load_dwordx4 v24, s[44:47], s74 offen offset:0 lds\n" \
  "buffer_load_dwordx4 v25, s[44:47], s74 offen offset:1024 lds\n" \
  "buffer_load_dwordx4 v26, s[44:47], s74 offen offset:2048 lds\n" \
  "s_add_u32 m0, s72, 125952\n" \
  "s_mov_b32 s74, 1671168\n" \
  "buffer_load_dwordx4 v24, s[44:47], s74 offen offset:0 lds\n" \
  "buffer_load_dwordx4 v25, s[44:47], s74 offen offset:1024 lds\n" \
  "buffer_load_dwordx4 v26, s[44:47], s74 offen offset:2048 lds\n" \
  "ds_read_b64_tr_b16 v[32:33], v27 offset:0\n" \
  "ds_read_b64_tr_b16 v[34:35], v27 offset:768\n" \
  "ds_read_b64_tr_b16 v[36:37], v27 offset:12288\n" \
  "ds_read_b64_tr_b16 v[38:39], v27 offset:13056\n" \
  "ds_read_b64_tr_b16 v[40:41], v27 offset:8\n" \
  "ds_read_b64_tr_b16 v[42:43], v27 offset:776\n" \
  "ds_read_b64_tr_b16 v[44:45], v27 offset:12296\n" \
  "ds_read_b64_tr_b16 v[46:47], v27 offset:13064\n" \
  "ds_read_b64_tr_b16 v[48:49], v30 offset:0\n" \
  "ds_read_b64_tr_b16 v[50:51], v30 offset:768\n" \
  "ds_read_b64_tr_b16 v[52:53], v30 offset:12288\n" \
  "ds_read_b64_tr_b16 v[54:55], v30 offset:13056\n" \
  "s_waitcnt vmcnt(24) lgkmcnt(0)\n" \
  "s_barrier\n" \
  "s_add_u32 m0, s72, 0\n" \
  "s_mov_b32 s74, 2359296\n" \
  "buffer_load_dwordx4 v24, s[40:43], s74 offen offset:0 lds\n" \
  "buffer_load_dwordx4 v25, s[40:43], s74 offen offset:1024 lds\n" \
  "buffer_load_dwordx4 v26, s[40:43], s74 offen offset:2048 lds\n" \
  "s_add_u32 m0, s72, 3072\n" \
  "s_mov_b32 s74, 2457600\n" \
  "buffer_load_dwordx4 v24, s[40:43], s74 offen offset:0 lds\n" \
  "buffer_load_dwordx4 v25, s[40:43], s74 offen offset:1024 lds\n" \
  "buffer_load_dwordx4 v26, s[40:43], s74 offen offset:2048 lds\n" \
  "ds_read_b64_tr_b16 v[56:57], v27 offset:24576\n" \
  "ds_read_b64_tr_b16 v[58:59], v27 offset:25344\n" \
  "ds_read_b64_tr_b16 v[60:61], v27 offset:36864\n" \
  "ds_read_b64_tr_b16 v[62:63], v27 offset:37632\n" \
  "ds_read_b64_tr_b16 a[0:1], v27 offset:24584\n" \
  "ds_read_b64_tr_b16 a[2:3], v27 offset:25352\n" \
  "ds_read_b64_tr_b16 a[4:5], v27 offset:36872\n" \
  "ds_read_b64_tr_b16 a[6:7], v27 offset:37640\n" \
  "ds_read_b64_tr_b16 a[8:9], v30 offset:24576\n" \
  "ds_read_b64_tr_b16 a[10:11], v30 offset:25344\n" \
  "ds_read_b64_tr_b16 a[12:13], v30 offset:36864\n" \
  "ds_read_b64_tr_b16 a[14:15], v30 offset:37632\n" \
  "s_waitcnt vmcnt(24) lgkmcnt(0)\n" \
  "s_barrier\n" \
  "s_add_u32 m0, s72, 24576\n" \
  "s_mov_b32 s74, 2359296\n" \
  "buffer_load_dwordx4 v24, s[44:47], s74 offen offset:0 lds\n" \
  "buffer_load_dwordx4 v25, s[44:47], s74 offen offset:1024 lds\n" \
  "buffer_load_dwordx4 v26, s[44:47], s74 offen offset:2048 lds\n" \
  "s_add_u32 m0, s72, 27648\n" \
  "s_mov_b32 s74, 2457600\n" \
  "buffer_load_dwordx4 v24, s[44:47], s74 offen offset:0 lds\n" \
  "buffer_load_dwordx4 v25, s[44:47], s74 offen offset:1024 lds\n" \
  "buffer_load_dwordx4 v26, s[44:47], s74 offen offset:2048 lds\n" \
  "ds_read_b64_tr_b16 a[16:17], v27 offset:49152\n" \
  "ds_read_b64_tr_b16 a[18:19], v27 offset:49920\n" \
  "ds_read_b64_tr_b16 a[20:21], v27 offset:61440\n" \
  "ds_read_b64_tr_b16 a[22:23], v27 offset:62208\n" \
  "ds_read_b64_tr_b16 a[24:25], v27 offset:49160\n" \
  "ds_read_b64_tr_b16 a[26:27], v27 offset:49928\n" \
  "ds_read_b64_tr_b16 a[28:29], v27 offset:61448\n" \
  "ds_read_b64_tr_b16 a[30:31], v27 offset:62216\n" \
  "ds_read_b64_tr_b16 v[64:65], v30 offset:49152\n" \
  "ds_read_b64_tr_b16 v[66:67], v30 offset:49920\n" \
  "ds_read_b64_tr_b16 v[68:69], v30 offset:61440\n" \
  "ds_read_b64_tr_b16 v[70:71], v30 offset:62208\n" \
  "s_waitcnt vmcnt(24) lgkmcnt(0)\n" \
  "s_barrier\n" \
  "s_add_u32 m0, s72, 49152\n" \
  "s_mov_b32 s74, 3145728\n" \
  "buffer_load_dwordx4 v24, s[40:43], s74 offen offset:0 lds\n" \
  "buffer_load_dwordx4 v25, s[40:43], s74 offen offset:1024 lds\n" \
  "buffer_load_dwordx4 v26, s[40:43], s74 offen offset:2048 lds\n" \
  "s_add_u32 m0, s72, 52224\n" \
  "s_mov_b32 s74, 3244032\n" \
  "buffer_load_dwordx4 v24, s[40:43], s74 offen offset:0 lds\n" \
  "buffer_load_dwordx4 v25, s[40:43], s74 offen offset:1024 lds\n" \
  "buffer_load_dwordx4 v26, s[40:43], s74 offen offset:2048 lds\n" \
  "ds_read_b64_tr_b16 a[32:33], v31 offset:0\n" \
  "ds_read_b64_tr_b16 a[34:35], v31 offset:768\n" \
  "ds_read_b64_tr_b16 a[36:37], v31 offset:12288\n" \
  "ds_read_b64_tr_b16 a[38:39], v31 offset:13056\n" \
  "ds_read_b64_tr_b16 a[40:41], v31 offset:8\n" \
  "ds_read_b64_tr_b16 a[42:43], v31 offset:776\n" \
  "ds_read_b64_tr_b16 a[44:45], v31 offset:12296\n" \
  "ds_read_b64_tr_b16 a[46:47], v31 offset:13064\n" \
  "ds_read_b64_tr_b16 a[48:49], v23 offset:0\n" \
  "ds_read_b64_tr_b16 a[50:51], v23 offset:768\n" \
  "ds_read_b64_tr_b16 a[52:53], v23 offset:12288\n" \
  "ds_read_b64_tr_b16 a[54:55], v23 offset:13056\n" \
  "s_waitcnt vmcnt(24) lgkmcnt(0)\n" \
  "s_barrier\n" \
  "s_add_u32 m0, s72, 73728\n" \
  "s_mov_b32 s74, 3145728\n" \
  "buffer_load_dwordx4 v24, s[44:47], s74 offen offset:0 lds\n" \
  "buffer_load_dwordx4 v25, s[44:47], s74 offen offset:1024 lds\n" \
  "buffer_load_dwordx4 v26, s[44:47], s74 offen offset:2048 lds\n" \
  "s_add_u32 m0, s72, 76800\n" \
  "s_mov_b32 s74, 3244032\n" \
  "buffer_load_dwordx4 v24, s[44:47], s74 offen offset:0 lds\n" \
  "buffer_load_dwordx4 v25, s[44:47], s74 offen offset:1024 lds\n" \
  "buffer_load_dwordx4 v26, s[44:47], s74 offen offset:2048 lds\n" \
  "ds_read_b64_tr_b16 a[56:57], v31 offset:24576\n" \
  "ds_read_b64_tr_b16 a[58:59], v31 offset:25344\n" \
  "ds_read_b64_tr_b16 a[60:61], v31 offset:36864\n" \
  "ds_read_b64_tr_b16 a[62:63], v31 offset:37632\n" \
  "ds_read_b64_tr_b16 v[72:73], v31 offset:24584\n" \
  "ds_read_b64_tr_b16 v[74:75], v31 offset:25352\n" \
  "ds_read_b64_tr_b16 v[76:77], v31 offset:36872\n" \
  "ds_read_b64_tr_b16 v[78:79], v31 offset:37640\n" \
  "ds_read_b64_tr_b16 a[64:65], v23 offset:24576\n" \
  "ds_read_b64_tr_b16 a[66:67], v23 offset:25344\n" \
  "ds_read_b64_tr_b16 a[68:69], v23 offset:36864\n" \
  "ds_read_b64_tr_b16 a[70:71], v23 offset:37632\n" \
  "s_waitcnt vmcnt(24) lgkmcnt(0)\n" \
  "s_barrier\n" \
  "s_add_u32 m0, s72, 98304\n" \
  "s_mov_b32 s74, 3932160\n" \
  "buffer_load_dwordx4 v24, s[40:43], s74 offen offset:0 lds\n" \
  "buffer_load_dwordx4 v25, s[40:43], s74 offen offset:1024 lds\n" \
  "buffer_load_dwordx4 v26, s[40:43], s74 offen offset:2048 lds\n" \
  "s_add_u32 m0, s72, 101376\n" \
  "s_mov_b32 s74, 4030464\n" \
  "buffer_load_dwordx4 v24, s[40:43], s74 offen offset:0 lds\n" \
  "buffer_load_dwordx4 v25, s[40:43], s74 offen offset:1024 lds\n" \
  "buffer_load_dwordx4 v26, s[40:43], s74 offen offset:2048 lds\n" \
  "ds_read_b64_tr_b16 a[200:201], v31 offset:49152\n" \
  "ds_read_b64_tr_b16 a[202:203], v31 offset:49920\n" \
  "ds_read_b64_tr_b16 a[204:205], v31 offset:61440\n" \
  "ds_read_b64_tr_b16 a[206:207], v31 offset:62208\n" \
  "ds_read_b64_tr_b16 v[128:129], v31 offset:49160\n" \
  "ds_read_b64_tr_b16 v[130:131], v31 offset:49928\n" \
  "ds_read_b64_tr_b16 v[132:133], v31 offset:61448\n" \
  "ds_read_b64_tr_b16 v[134:135], v31 offset:62216\n" \
  "ds_read_b64_tr_b16 v[192:193], v23 offset:49152\n" \
  "ds_read_b64_tr_b16 v[194:195], v23 offset:49920\n" \
  "ds_read_b64_tr_b16 v[196:197], v23 offset:61440\n" \
  "ds_read_b64_tr_b16 v[198:199], v23 offset:62208\n" \
  "s_waitcnt vmcnt(24) lgkmcnt(0)\n" \
  "s_barrier\n" \
  "s_add_u32 m0, s72, 122880\n" \
  "s_mov_b32 s74, 3932160\n" \
  "buffer_load_dwordx4 v24, s[44:47], s74 offen offset:0 lds\n" \
  "buffer_load_dwordx4 v25, s[44:47], s74 offen offset:1024 lds\n" \
  "buffer_load_dwordx4 v26, s[44:47], s74 offen offset:2048 lds\n" \
  "s_add_u32 m0, s72, 125952\n" \
  "s_mov_b32 s74, 4030464\n" \
  "buffer_load_dwordx4 v24, s[44:47], s74 offen offset:0 lds\n" \
  "buffer_load_dwordx4 v25, s[44:47], s74 offen offset:1024 lds\n" \
  "buffer_load_dwordx4 v26, s[44:47], s74 offen offset:2048 lds\n" \
  "ds_read_b64_tr_b16 a[72:73], v27 offset:0\n" \
  "ds_read_b64_tr_b16 a[74:75], v27 offset:768\n" \
  "ds_read_b64_tr_b16 a[76:77], v27 offset:12288\n" \
  "ds_read_b64_tr_b16 a[78:79], v27 offset:13056\n" \
  "ds_read_b64_tr_b16 a[80:81], v27 offset:8\n" \
  "ds_read_b64_tr_b16 a[82:83], v27 offset:776\n" \
  "ds_read_b64_tr_b16 a[84:85], v27 offset:12296\n" \
  "ds_read_b64_tr_b16 a[86:87], v27 offset:13064\n" \
  "ds_read_b64_tr_b16 a[88:89], v30 offset:0\n" \
  "ds_read_b64_tr_b16 a[90:91], v30 offset:768\n" \
  "ds_read_b64_tr_b16 a[92:93], v30 offset:12288\n" \
  "ds_read_b64_tr_b16 a[94:95], v30 offset:13056\n" \
  "s_waitcnt vmcnt(24) lgkmcnt(0)\n" \
  "s_barrier\n" \
  "s_add_u32 m0, s72, 0\n" \
  "s_mov_b32 s74, 4718592\n" \
  "buffer_load_dwordx4 v24, s[40:43], s74 offen offset:0 lds\n" \
  "buffer_load_dwordx4 v25, s[40:43], s74 offen offset:1024 lds\n" \
  "buffer_load_dwordx4 v26, s[40:43], s74 offen offset:2048 lds\n" \
  "s_add_u32 m0, s72, 3072\n" \
  "s_mov_b32 s74, 4816896\n" \
  "buffer_load_dwordx4 v24, s[40:43], s74 offen offset:0 lds\n" \
  "buffer_load_dwordx4 v25, s[40:43], s74 offen offset:1024 lds\n" \
  "buffer_load_dwordx4 v26, s[40:43], s74 offen offset:2048 lds\n" \
  "ds_read_b64_tr_b16 a[208:209], v27 offset:24576\n" \
  "ds_read_b64_tr_b16 a[210:211], v27 offset:25344\n" \
  "ds_read_b64_tr_b16 a[212:213], v27 offset:36864\n" \
  "ds_read_b64_tr_b16 a[214:215], v27 offset:37632\n" \
  "ds_read_b64_tr_b16 v[136:137], v27 offset:24584\n" \
  "ds_read_b64_tr_b16 v[138:139], v27 offset:25352\n" \
  "ds_read_b64_tr_b16 v[140:141], v27 offset:36872\n" \
  "ds_read_b64_tr_b16 v[142:143], v27 offset:37640\n" \
  "ds_read_b64_tr_b16 v[200:201], v30 offset:24576\n" \
  "ds_read_b64_tr_b16 v[202:203], v30 offset:25344\n" \
  "ds_read_b64_tr_b16 v[204:205], v30 offset:36864\n" \
  "ds_read_b64_tr_b16 v[206:207], v30 offset:37632\n" \
  "s_waitcnt vmcnt(24) lgkmcnt(0)\n" \
  "s_barrier\n" \
  "s_add_u32 m0, s72, 24576\n" \
  "s_mov_b32 s74, 4718592\n" \
  "buffer_load_dwordx4 v24, s[44:47], s74 offen offset:0 lds\n" \
  "buffer_load_dwordx4 v25, s[44:47], s74 offen offset:1024 lds\n" \
  "buffer_load_dwordx4 v26, s[44:47], s74 offen offset:2048 lds\n" \
  "s_add_u32 m0, s72, 27648\n" \
  "s_mov_b32 s74, 4816896\n" \
  "buffer_load_dwordx4 v24, s[44:47], s74 offen offset:0 lds\n" \
  "buffer_load_dwordx4 v25, s[44:47], s74 offen offset:1024 lds\n" \
  "buffer_load_dwordx4 v26, s[44:47], s74 offen offset:2048 lds\n" \
  "ds_read_b64_tr_b16 v[80:81], v27 offset:49152\n" \
  "ds_read_b64_tr_b16 v[82:83], v27 offset:49920\n" \
  "ds_read_b64_tr_b16 v[84:85], v27 offset:61440\n" \
  "ds_read_b64_tr_b16 v[86:87], v27 offset:62208\n" \
  "ds_read_b64_tr_b16 a[96:97], v27 offset:49160\n" \
  "ds_read_b64_tr_b16 a[98:99], v27 offset:49928\n" \
  "ds_read_b64_tr_b16 a[100:101], v27 offset:61448\n" \
  "ds_read_b64_tr_b16 a[102:103], v27 offset:62216\n" \
  "ds_read_b64_tr_b16 v[88:89], v30 offset:49152\n" \
  "ds_read_b64_tr_b16 v[90:91], v30 offset:49920\n" \
  "ds_read_b64_tr_b16 v[92:93], v30 offset:61440\n" \
  "ds_read_b64_tr_b16 v[94:95], v30 offset:62208\n" \
  "s_waitcnt vmcnt(24) lgkmcnt(0)\n" \
  "s_barrier\n" \
  "s_add_u32 m0, s72, 49152\n" \
  "s_mov_b32 s74, 5505024\n" \
  "buffer_load_dwordx4 v24, s[40:43], s74 offen offset:0 lds\n" \
  "buffer_load_dwordx4 v25, s[40:43], s74 offen offset:1024 lds\n" \
  "buffer_load_dwordx4 v26, s[40:43], s74 offen offset:2048 lds\n" \
  "s_add_u32 m0, s72, 52224\n" \
  "s_mov_b32 s74, 5603328\n" \
  "buffer_load_dwordx4 v24, s[40:43], s74 offen offset:0 lds\n" \
  "buffer_load_dwordx4 v25, s[40:43], s74 offen offset:1024 lds\n" \
  "buffer_load_dwordx4 v26, s[40:43], s74 offen offset:2048 lds\n" \
  "ds_read_b64_tr_b16 a[216:217], v31 offset:0\n" \
  "ds_read_b64_tr_b16 a[218:219], v31 offset:768\n" \
  "ds_read_b64_tr_b16 a[220:221], v31 offset:12288\n" \
  "ds_read_b64_tr_b16 a[222:223], v31 offset:13056\n" \
  "ds_read_b64_tr_b16 v[144:145], v31 offset:8\n" \
  "ds_read_b64_tr_b16 v[146:147], v31 offset:776\n" \
  "ds_read_b64_tr_b16 v[148:149], v31 offset:12296\n" \
  "ds_read_b64_tr_b16 v[150:151], v31 offset:13064\n" \
  "ds_read_b64_tr_b16 v[208:209], v23 offset:0\n" \
  "ds_read_b64_tr_b16 v[210:211], v23 offset:768\n" \
  "ds_read_b64_tr_b16 v[212:213], v23 offset:12288\n" \
  "ds_read_b64_tr_b16 v[214:215], v23 offset:13056\n" \
  "s_waitcnt vmcnt(24) lgkmcnt(0)\n" \
  "s_barrier\n" \
  "s_add_u32 m0, s72, 73728\n" \
  "s_mov_b32 s74, 5505024\n" \
  "buffer_load_dwordx4 v24, s[44:47], s74 offen offset:0 lds\n" \
  "buffer_load_dwordx4 v25, s[44:47], s74 offen offset:1024 lds\n" \
  "buffer_load_dwordx4 v26, s[44:47], s74 offen offset:2048 lds\n" \
  "s_add_u32 m0, s72, 76800\n" \
  "s_mov_b32 s74, 5603328\n" \
  "buffer_load_dwordx4 v24, s[44:47], s74 offen offset:0 lds\n" \
  "buffer_load_dwordx4 v25, s[44:47], s74 offen offset:1024 lds\n" \
  "buffer_load_dwordx4 v26, s[44:47], s74 offen offset:2048 lds\n" \
  "ds_read_b64_tr_b16 a[104:105], v31 offset:24576\n" \
  "ds_read_b64_tr_b16 a[106:107], v31 offset:25344\n" \
  "ds_read_b64_tr_b16 a[108:109], v31 offset:36864\n" \
  "ds_read_b64_tr_b16 a[110:111], v31 offset:37632\n" \
  "ds_read_b64_tr_b16 v[96:97], v31 offset:24584\n" \
  "ds_read_b64_tr_b16 v[98:99], v31 offset:25352\n" \
  "ds_read_b64_tr_b16 v[100:101], v31 offset:36872\n" \
  "ds_read_b64_tr_b16 v[102:103], v31 offset:37640\n" \
  "ds_read_b64_tr_b16 a[112:113], v23 offset:24576\n" \
  "ds_read_b64_tr_b16 a[114:115], v23 offset:25344\n" \
  "ds_read_b64_tr_b16 a[116:117], v23 offset:36864\n" \
  "ds_read_b64_tr_b16 a[118:119], v23 offset:37632\n" \
  "s_waitcnt vmcnt(24) lgkmcnt(0)\n" \
  "s_barrier\n" \
  "s_add_u32 m0, s72, 98304\n" \
  "s_mov_b32 s74, 6291456\n" \
  "buffer_load_dwordx4 v24, s[40:43], s74 offen offset:0 lds\n" \
  "buffer_load_dwordx4 v25, s[40:43], s74 offen offset:1024 lds\n" \
  "buffer_load_dwordx4 v26, s[40:43], s74 offen offset:2048 lds\n" \
  "s_add_u32 m0, s72, 101376\n" \
  "s_mov_b32 s74, 6389760\n" \
  "buffer_load_dwordx4 v24, s[40:43], s74 offen offset:0 lds\n" \
  "buffer_load_dwordx4 v25, s[40:43], s74 offen offset:1024 lds\n" \
  "buffer_load_dwordx4 v26, s[40:43], s74 offen offset:2048 lds\n" \
  "ds_read_b64_tr_b16 a[224:225], v31 offset:49152\n" \
  "ds_read_b64_tr_b16 a[226:227], v31 offset:49920\n" \
  "ds_read_b64_tr_b16 a[228:229], v31 offset:61440\n" \
  "ds_read_b64_tr_b16 a[230:231], v31 offset:62208\n" \
  "ds_read_b64_tr_b16 v[152:153], v31 offset:49160\n" \
  "ds_read_b64_tr_b16 v[154:155], v31 offset:49928\n" \
  "ds_read_b64_tr_b16 v[156:157], v31 offset:61448\n" \
  "ds_read_b64_tr_b16 v[158:159], v31 offset:62216\n" \
  "ds_read_b64_tr_b16 v[216:217], v23 offset:49152\n" \
  "ds_read_b64_tr_b16 v[218:219], v23 offset:49920\n" \
  "ds_read_b64_tr_b16 v[220:221], v23 offset:61440\n" \
  "ds_read_b64_tr_b16 v[222:223], v23 offset:62208\n" \
  "s_waitcnt vmcnt(24) lgkmcnt(0)\n" \
  "s_barrier\n" \
  "s_add_u32 m0, s72, 122880\n" \
  "s_mov_b32 s74, 6291456\n" \
  "buffer_load_dwordx4 v24, s[44:47], s74 offen offset:0 lds\n" \
  "buffer_load_dwordx4 v25, s[44:47], s74 offen offset:1024 lds\n" \
  "buffer_load_dwordx4 v26, s[44:47], s74 offen offset:2048 lds\n" \
  "s_add_u32 m0, s72, 125952\n" \
  "s_mov_b32 s74, 6389760\n" \
  "buffer_load_dwordx4 v24, s[44:47], s74 offen offset:0 lds\n" \
  "buffer_load_dwordx4 v25, s[44:47], s74 offen offset:1024 lds\n" \
  "buffer_load_dwordx4 v26, s[44:47], s74 offen offset:2048 lds\n" \
  "ds_read_b64_tr_b16 a[120:121], v27 offset:0\n" \
  "ds_read_b64_tr_b16 a[122:123], v27 offset:768\n" \
  "ds_read_b64_tr_b16 a[124:125], v27 offset:12288\n" \
  "ds_read_b64_tr_b16 a[126:127], v27 offset:13056\n" \
  "ds_read_b64_tr_b16 v[104:105], v27 offset:8\n" \
  "ds_read_b64_tr_b16 v[106:107], v27 offset:776\n" \
  "ds_read_b64_tr_b16 v[108:109], v27 offset:12296\n" \
  "ds_read_b64_tr_b16 v[110:111], v27 offset:13064\n" \
  "ds_read_b64_tr_b16 a[168:169], v30 offset:0\n" \
  "ds_read_b64_tr_b16 a[170:171], v30 offset:768\n" \
  "ds_read_b64_tr_b16 a[172:173], v30 offset:12288\n" \
  "ds_read_b64_tr_b16 a[174:175], v30 offset:13056\n" \
  "s_waitcnt vmcnt(24) lgkmcnt(0)\n" \
  "s_barrier\n" \
  "s_add_u32 m0, s72, 0\n" \
  "s_mov_b32 s74, 7077888\n" \
  "buffer_load_dwordx4 v24, s[40:43], s74 offen offset:0 lds\n" \
  "buffer_load_dwordx4 v25, s[40:43], s74 offen offset:1024 lds\n" \
  "buffer_load_dwordx4 v26, s[40:43], s74 offen offset:2048 lds\n" \
  "s_add_u32 m0, s72, 3072\n" \
  "s_mov_b32 s74, 7176192\n" \
  "buffer_load_dwordx4 v24, s[40:43], s74 offen offset:0 lds\n" \
  "buffer_load_dwordx4 v25, s[40:43], s74 offen offset:1024 lds\n" \
  "buffer_load_dwordx4 v26, s[40:43], s74 offen offset:2048 lds\n" \
  "ds_read_b64_tr_b16 a[232:233], v27 offset:24576\n" \
  "ds_read_b64_tr_b16 a[234:235], v27 offset:25344\n" \
  "ds_read_b64_tr_b16 a[236:237], v27 offset:36864\n" \
  "ds_read_b64_tr_b16 a[238:239], v27 offset:37632\n" \
  "ds_read_b64_tr_b16 v[160:161], v27 offset:24584\n" \
  "ds_read_b64_tr_b16 v[162:163], v27 offset:25352\n" \
  "ds_read_b64_tr_b16 v[164:165], v27 offset:36872\n" \
  "ds_read_b64_tr_b16 v[166:167], v27 offset:37640\n" \
  "ds_read_b64_tr_b16 v[224:225], v30 offset:24576\n" \
  "ds_read_b64_tr_b16 v[226:227], v30 offset:25344\n" \
  "ds_read_b64_tr_b16 v[228:229], v30 offset:36864\n" \
  "ds_read_b64_tr_b16 v[230:231], v30 offset:37632\n" \
  "s_waitcnt vmcnt(24) lgkmcnt(0)\n" \
  "s_barrier\n" \
  "s_add_u32 m0, s72, 24576\n" \
  "s_mov_b32 s74, 7077888\n" \
  "buffer_load_dwordx4 v24, s[44:47], s74 offen offset:0 lds\n" \
  "buffer_load_dwordx4 v25, s[44:47], s74 offen offset:1024 lds\n" \
  "buffer_load_dwordx4 v26, s[44:47], s74 offen offset:2048 lds\n" \
  "s_add_u32 m0, s72, 27648\n" \
  "s_mov_b32 s74, 7176192\n" \
  "buffer_load_dwordx4 v24, s[44:47], s74 offen offset:0 lds\n" \
  "buffer_load_dwordx4 v25, s[44:47], s74 offen offset:1024 lds\n" \
  "buffer_load_dwordx4 v26, s[44:47], s74 offen offset:2048 lds\n" \
  "ds_read_b64_tr_b16 a[128:129], v27 offset:49152\n" \
  "ds_read_b64_tr_b16 a[130:131], v27 offset:49920\n" \
  "ds_read_b64_tr_b16 a[132:133], v27 offset:61440\n" \
  "ds_read_b64_tr_b16 a[134:135], v27 offset:62208\n" \
  "ds_read_b64_tr_b16 a[152:153], v27 offset:49160\n" \
  "ds_read_b64_tr_b16 a[154:155], v27 offset:49928\n" \
  "ds_read_b64_tr_b16 a[156:157], v27 offset:61448\n" \
  "ds_read_b64_tr_b16 a[158:159], v27 offset:62216\n" \
  "ds_read_b64_tr_b16 a[176:177], v30 offset:49152\n" \
  "ds_read_b64_tr_b16 a[178:179], v30 offset:49920\n" \
  "ds_read_b64_tr_b16 a[180:181], v30 offset:61440\n" \
  "ds_read_b64_tr_b16 a[182:183], v30 offset:62208\n" \
  "s_waitcnt vmcnt(24) lgkmcnt(0)\n" \
  "s_barrier\n" \
  "ds_read_b64_tr_b16 a[240:241], v31 offset:0\n" \
  "ds_read_b64_tr_b16 a[242:243], v31 offset:768\n" \
  "ds_read_b64_tr_b16 a[244:245], v31 offset:12288\n" \
  "ds_read_b64_tr_b16 a[246:247], v31 offset:13056\n" \
  "ds_read_b64_tr_b16 v[168:169], v31 offset:8\n" \
  "ds_read_b64_tr_b16 v[170:171], v31 offset:776\n" \
  "ds_read_b64_tr_b16 v[172:173], v31 offset:12296\n" \
  "ds_read_b64_tr_b16 v[174:175], v31 offset:13064\n" \
  "ds_read_b64_tr_b16 v[232:233], v23 offset:0\n" \
  "ds_read_b64_tr_b16 v[234:235], v23 offset:768\n" \
  "ds_read_b64_tr_b16 v[236:237], v23 offset:12288\n" \
  "ds_read_b64_tr_b16 v[238:239], v23 offset:13056\n" \
  "s_waitcnt vmcnt(18) lgkmcnt(0)\n" \
  "s_barrier\n" \
  "ds_read_b64_tr_b16 a[136:137], v31 offset:24576\n" \
  "ds_read_b64_tr_b16 a[138:139], v31 offset:25344\n" \
  "ds_read_b64_tr_b16 a[140:141], v31 offset:36864\n" \
  "ds_read_b64_tr_b16 a[142:143], v31 offset:37632\n" \
  "ds_read_b64_tr_b16 v[112:113], v31 offset:24584\n" \
  "ds_read_b64_tr_b16 v[114:115], v31 offset:25352\n" \
  "ds_read_b64_tr_b16 v[116:117], v31 offset:36872\n" \
  "ds_read_b64_tr_b16 v[118:119], v31 offset:37640\n" \
  "ds_read_b64_tr_b16 a[184:185], v23 offset:24576\n" \
  "ds_read_b64_tr_b16 a[186:187], v23 offset:25344\n" \
  "ds_read_b64_tr_b16 a[188:189], v23 offset:36864\n" \
  "ds_read_b64_tr_b16 a[190:191], v23 offset:37632\n" \
  "s_waitcnt vmcnt(12) lgkmcnt(0)\n" \
  "s_barrier\n" \
  "ds_read_b64_tr_b16 a[248:249], v31 offset:49152\n" \
  "ds_read_b64_tr_b16 a[250:251], v31 offset:49920\n" \
  "ds_read_b64_tr_b16 a[252:253], v31 offset:61440\n" \
  "ds_read_b64_tr_b16 a[254:255], v31 offset:62208\n" \
  "ds_read_b64_tr_b16 v[176:177], v31 offset:49160\n" \
  "ds_read_b64_tr_b16 v[178:179], v31 offset:49928\n" \
  "ds_read_b64_tr_b16 v[180:181], v31 offset:61448\n" \
  "ds_read_b64_tr_b16 v[182:183], v31 offset:62216\n" \
  "ds_read_b64_tr_b16 v[240:241], v23 offset:49152\n" \
  "ds_read_b64_tr_b16 v[242:243], v23 offset:49920\n" \
  "ds_read_b64_tr_b16 v[244:245], v23 offset:61440\n" \
  "ds_read_b64_tr_b16 v[246:247], v23 offset:62208\n" \
  "s_waitcnt vmcnt(6) lgkmcnt(0)\n" \
  "s_barrier\n" \
  "ds_read_b64_tr_b16 a[144:145], v27 offset:0\n" \
  "ds_read_b64_tr_b16 a[146:147], v27 offset:768\n" \
  "ds_read_b64_tr_b16 a[148:149], v27 offset:12288\n" \
  "ds_read_b64_tr_b16 a[150:151], v27 offset:13056\n" \
  "ds_read_b64_tr_b16 a[160:161], v27 offset:8\n" \
  "ds_read_b64_tr_b16 a[162:163], v27 offset:776\n" \
  "ds_read_b64_tr_b16 a[164:165], v27 offset:12296\n" \
  "ds_read_b64_tr_b16 a[166:167], v27 offset:13064\n" \
  "ds_read_b64_tr_b16 a[192:193], v30 offset:0\n" \
  "ds_read_b64_tr_b16 a[194:195], v30 offset:768\n" \
  "ds_read_b64_tr_b16 a[196:197], v30 offset:12288\n" \
  "ds_read_b64_tr_b16 a[198:199], v30 offset:13056\n" \
  "s_waitcnt vmcnt(0) lgkmcnt(0)\n" \
  "s_barrier\n" \
  "ds_read_b64_tr_b16 v[120:121], v27 offset:24576\n" \
  "ds_read_b64_tr_b16 v[122:123], v27 offset:25344\n" \
  "ds_read_b64_tr_b16 v[124:125], v27 offset:36864\n" \
  "ds_read_b64_tr_b16 v[126:127], v27 offset:37632\n" \
  "ds_read_b64_tr_b16 v[184:185], v27 offset:24584\n" \
  "ds_read_b64_tr_b16 v[186:187], v27 offset:25352\n" \
  "ds_read_b64_tr_b16 v[188:189], v27 offset:36872\n" \
  "ds_read_b64_tr_b16 v[190:191], v27 offset:37640\n" \
  "ds_read_b64_tr_b16 v[248:249], v30 offset:24576\n" \
  "ds_read_b64_tr_b16 v[250:251], v30 offset:25344\n" \
  "ds_read_b64_tr_b16 v[252:253], v30 offset:36864\n" \
  "ds_read_b64_tr_b16 v[254:255], v30 offset:37632\n" \
  "s_waitcnt lgkmcnt(0)\n" \
  "s_barrier\n" \
  "ds_write_b128 v3, v[32:35] offset:0\n" \
  "ds_write_b128 v3, v[36:39] offset:1024\n" \
  "ds_write_b128 v3, v[40:43] offset:2048\n" \
  "ds_write_b128 v3, v[44:47] offset:3072\n" \
  "ds_write_b128 v3, v[48:51] offset:4096\n" \
  "ds_write_b128 v3, v[52:55] offset:5120\n" \
  "ds_write_b128 v3, v[56:59] offset:6144\n" \
  "ds_write_b128 v3, v[60:63] offset:7168\n" \
  "ds_write_b128 v3, v[64:67] offset:8192\n" \
  "ds_write_b128 v3, v[68:71] offset:9216\n" \
  "ds_write_b128 v3, v[72:75] offset:10240\n" \
  "ds_write_b128 v3, v[76:79] offset:11264\n" \
  "ds_write_b128 v3, v[80:83] offset:12288\n" \
  "ds_write_b128 v3, v[84:87] offset:13312\n" \
  "ds_write_b128 v3, v[88:91] offset:14336\n" \
  "s_waitcnt lgkmcnt(8)\n" \
  "ds_write_b128 v3, v[92:95] offset:15360\n" \
  "ds_write_b128 v3, v[96:99] offset:16384\n" \
  "ds_write_b128 v3, v[100:103] offset:17408\n" \
  "ds_write_b128 v3, v[104:107] offset:18432\n" \
  "ds_write_b128 v3, v[108:111] offset:19456\n" \
  "ds_write_b128 v3, v[112:115] offset:20480\n" \
  "ds_write_b128 v3, v[116:119] offset:21504\n" \
  "s_waitcnt lgkmcnt(0)\n" \
  "s_cmp_eq_u32 s52, 1\n" \
  "s_cbranch_scc1 LSTARTs1_%=\n" \
  "s_cmp_eq_u32 s52, 2\n" \
  "s_cbranch_scc1 LSTARTs2_%=\n" \
  "s_cmp_eq_u32 s52, 3\n" \
  "s_cbranch_scc1 LSTARTs3_%=\n" \
  "LSTARTs0_%=:\n" \
  "v_mov_b32 v17, v28\n" \
  "s_mov_b32 s47, s38\n" \
  "s_mov_b32 s38, 0\n" \
  "s_mov_b32 s68, 63\n" \
  "s_mov_b32 s73, 20001\n" \
  "s_mov_b32 s56, 19998\n" \
  "LPASSs0_%=:\n" \
  "s_mov_b32 s40, 0\n" \
  "s_mov_b32 s42, 0x40000000\n" \
  "v_and_b32 v20, 0x7fff, v20\n" \
  "v_and_b32 v21, 0x7fff, v21\n" \
  "v_mov_b32 v64, 0\n" \
  "ds_write_b32 v12, v64 offset:512\n" \
  "s_waitcnt lgkmcnt(0)\n" \
  "LQGs0_%=:\n" \
  "buffer_load_dword v19, v18, s[36:39], 0 offen sc1\n" \
  "s_waitcnt vmcnt(0)\n" \
  "v_cmp_ge_u32 vcc, 1, v19\n" \
  "s_nop 3\n" \
  "s_cbranch_vccz LRGs0_%=\n" \
  "s_add_u32 s73, s73, 1\n" \
  "s_cmp_gt_u32 s73, 20000\n" \
  "s_cbranch_scc1 LRGs0_%=\n" \
  "s_sleep 4\n" \
  "s_branch LQGs0_%=\n" \
  "LRGs0_%=:\n" \
  "v_mov_b32 v64, 1\n" \
  "buffer_atomic_add v64, v17, s[36:39], 0 offen sc1\n" \
  "s_barrier\n" \
  "s_add_u32 m0, s40, 0\n" \
  "s_add_u32 s75, s55, 0\n" \
  "buffer_load_dwordx4 v8, s[36:39], s75 offen offset:0 sc1 lds\n" \
  "buffer_load_dwordx4 v8, s[36:39], s75 offen offset:1024 sc1 lds\n" \
  "buffer_load_dwordx4 v8, s[36:39], s75 offen offset:2048 sc1 lds\n" \
  "buffer_load_dwordx4 v8, s[36:39], s75 offen offset:3072 sc1 lds\n" \
  "s_add_u32 m0, s40, 4096\n" \
  "s_add_u32 s75, s55, 4096\n" \
  "buffer_load_dwordx4 v8, s[36:39], s75 offen offset:0 sc1 lds\n" \
  "buffer_load_dwordx4 v8, s[36:39], s75 offen offset:1024 sc1 lds\n" \
  "s_xor_b32 s40, s40, 0x8000\n" \
  "v_readlane_b32 s72, v11, 0\n" \
  "s_nop 0\n" \
  "s_add_u32 s69, s72, 2048\n" \
  "s_add_u32 s70, s72, 6144\n" \
  "s_add_u32 s44, s72, s93\n" \
  "s_add_u32 s45, s72, s94\n" \
  "s_add_u32 s46, s72, s95\n" \
  "v_readlane_b32 s41, v7, 0\n" \
  "s_nop 1\n" \
  "ds_read_b128 v[88:91], v3 offset:0\n" \
  "ds_read_b128 v[92:95], v3 offset:1024\n" \
  "ds_read_b128 v[96:99], v3 offset:2048\n" \
  "ds_read_b128 v[100:103], v3 offset:3072\n" \
  "ds_read_b128 v[104:107], v3 offset:4096\n" \
  "ds_read_b128 v[108:111], v3 offset:5120\n" \
  "ds_read_b128 v[112:115], v3 offset:6144\n" \
  "ds_read_b128 v[116:119], v3 offset:7168\n" \
  "v_mov_b32 v60, -1\n" \
  "v_mov_b32 v61, -1\n" \
  "ds_write_b64 v6, v[60:61] offset:32768\n" \
  "ds_write_b64 v6, v[60:61] offset:33280\n" \
  "ds_write_b64 v6, v[60:61] offset:36864\n" \
  "v_mov_b32 v64, 0\n" \
  "v_mov_b32 v65, 0\n" \
  "v_mov_b32 v66, 0\n" \
  "v_mov_b32 v67, 0\n" \
  "v_mov_b32 v68, 0\n" \
  "v_mov_b32 v69, 0\n" \
  "v_mov_b32 v70, 0\n" \
  "v_mov_b32 v71, 0\n" \
  "v_mov_b32 v72, 0\n" \
  "v_mov_b32 v73, 0\n" \
  "v_mov_b32 v74, 0\n" \
  "v_mov_b32 v75, 0\n" \
  "v_mov_b32 v76, 0\n" \
  "v_mov_b32 v77, 0\n" \
  "v_mov_b32 v78, 0\n" \
  "v_mov_b32 v79, 0\n" \
  "v_mov_b32 v80, 0\n" \
  "v_mov_b32 v81, 0\n" \
  "v_mov_b32 v82, 0\n" \
  "v_mov_b32 v83, 0\n" \
  "v_mov_b32 v84, 0\n" \
  "v_mov_b32 v85, 0\n" \
  "v_mov_b32 v86, 0\n" \
  "v_mov_b32 v87, 0\n" \
  "s_waitcnt lgkmcnt(0)\n" \
  "s_cmp_eq_u32 s38, 0\n" \
  "s_cselect_b32 s74, -1, 0\n" \
  "v_mov_b32 v64, s74\n" \
  "LOOPs0_%=:\n" \
  "s_barrier\n" \
  "buffer_load_dwordx4 v[24:27], v1, s[36:39], s72 offen offset:0 sc1\n" \
  "s_mov_b32 m0, 65536\n" \
  "s_nop 0\n" \
  "buffer_load_dwordx4 v1, s[36:39], s69 offen sc1 lds\n" \
  "buffer_load_dwordx4 v[28:31], v1, s[36:39], s72 offen offset:1024 sc1\n" \
  "s_mov_b32 m0, 69632\n" \
  "s_nop 0\n" \
  "buffer_load_dwordx4 v1, s[36:39], s70 offen sc1 lds\n" \
  "ds_write_b64 v6, v[60:61] offset:0\n" \
  "ds_write_b64 v6, v[60:61] offset:512\n" \
  "ds_write_b64 v6, v[60:61] offset:4096\n" \
  "s_add_u32 s74, s68, 2\n" \
  "s_min_u32 s43, s74, 63\n" \
  "s_add_u32 s68, s68, 1\n" \
  "LMs0_%=:\n" \
  "s_memtime s[76:77]\n" \
  "s_waitcnt vmcnt(3)\n" \
  "v_smfmac_f32_16x16x64_bf16 v[64:67], v[24:27], v[88:95], v0\n" \
  "ds_read_b128 v[88:91], v3 offset:8192\n" \
  "v_smfmac_f32_16x16x64_bf16 v[68:71], v[24:27], v[96:103], v0\n" \
  "ds_read_b128 v[92:95], v3 offset:9216\n" \
  "v_smfmac_f32_16x16x64_bf16 v[72:75], v[24:27], v[104:111], v0\n" \
  "ds_read_b128 v[96:99], v3 offset:10240\n" \
  "v_smfmac_f32_16x16x64_bf16 v[76:79], v[24:27], v[112:119], v0\n" \
  "ds_read_b128 v[100:103], v3 offset:11264\n" \
  "v_smfmac_f32_16x16x64_bf16 v[80:83], v[24:27], a[0:7], v0\n" \
  "ds_read_b128 v[104:107], v3 offset:12288\n" \
  "v_smfmac_f32_16x16x64_bf16 v[84:87], v[24:27], a[8:15], v0\n" \
  "s_waitcnt vmcnt(2)\n" \
  "s_waitcnt lgkmcnt(5)\n" \
  "s_barrier\n" \
  "ds_read_b128 v[32:35], v2 offset:2048\n" \
  "s_waitcnt vmcnt(1)\n" \
  "v_smfmac_f32_16x16x64_bf16 v[64:67], v[28:31], a[16:23], v0\n" \
  "ds_read_b128 v[36:39], v2 offset:3072\n" \
  "v_smfmac_f32_16x16x64_bf16 v[68:71], v[28:31], a[24:31], v0\n" \
  "ds_read_b128 v[40:43], v2 offset:4096\n" \
  "s_waitcnt lgkmcnt(6)\n" \
  "v_smfmac_f32_16x16x64_bf16 v[72:75], v[28:31], v[88:95], v0\n" \
  "ds_read_b128 v[44:47], v2 offset:5120\n" \
  "v_smfmac_f32_16x16x64_bf16 v[76:79], v[28:31], a[32:39], v0\n" \
  "ds_read_b128 v[108:111], v3 offset:13312\n" \
  "v_smfmac_f32_16x16x64_bf16 v[80:83], v[28:31], a[40:47], v0\n" \
  "ds_read_b128 v[112:115], v3 offset:14336\n" \
  "v_smfmac_f32_16x16x64_bf16 v[84:87], v[28:31], a[48:55], v0\n" \
  "ds_read_b128 v[116:119], v3 offset:15360\n" \
  "s_waitcnt lgkmcnt(6)\n" \
  "v_smfmac_f32_16x16x64_bf16 v[64:67], v[32:35], a[56:63], v0\n" \
  "ds_read_b128 v[88:91], v3 offset:16384\n" \
  "v_smfmac_f32_16x16x64_bf16 v[68:71], v[32:35], v[96:103], v0\n" \
  "ds_read_b128 v[92:95], v3 offset:17408\n" \
  "v_smfmac_f32_16x16x64_bf16 v[72:75], v[32:35], a[64:71], v0\n" \
  "ds_read_b128 v[96:99], v3 offset:18432\n" \
  "s_waitcnt lgkmcnt(8)\n" \
  "v_smfmac_f32_16x16x64_bf16 v[64:67], v[36:39], a[72:79], v0\n" \
  "s_waitcnt vmcnt(0)\n" \
  "s_barrier\n" \
  "ds_read_b128 v[100:103], v3 offset:19456\n" \
  "v_smfmac_f32_16x16x64_bf16 v[68:71], v[36:39], a[80:87], v0\n" \
  "ds_read_b128 v[48:51], v2 offset:6144\n" \
  "v_smfmac_f32_16x16x64_bf16 v[72:75], v[36:39], a[88:95], v0\n" \
  "ds_read_b128 v[52:55], v2 offset:7168\n" \
  "s_waitcnt lgkmcnt(8)\n" \
  "v_smfmac_f32_16x16x64_bf16 v[64:67], v[40:43], v[104:111], v0\n" \
  "ds_read_b128 v[56:59], v2 offset:8192\n" \
  "v_smfmac_f32_16x16x64_bf16 v[68:71], v[40:43], a[96:103], v0\n" \
  "ds_read_b128 v[60:63], v2 offset:9216\n" \
  "s_waitcnt lgkmcnt(8)\n" \
  "v_smfmac_f32_16x16x64_bf16 v[72:75], v[40:43], v[112:119], v0\n" \
  "ds_read_b128 v[104:107], v3 offset:20480\n" \
  "v_smfmac_f32_16x16x64_bf16 v[64:67], v[44:47], a[104:111], v0\n" \
  "ds_read_b128 v[108:111], v3 offset:21504\n" \
  "s_waitcnt lgkmcnt(8)\n" \
  "v_smfmac_f32_16x16x64_bf16 v[68:71], v[44:47], v[88:95], v0\n" \
  "v_smfmac_f32_16x16x64_bf16 v[72:75], v[44:47], a[112:119], v0\n" \
  "s_waitcnt lgkmcnt(5)\n" \
  "v_smfmac_f32_16x16x64_bf16 v[64:67], v[48:51], a[120:127], v0\n" \
  "s_waitcnt lgkmcnt(4)\n" \
  "v_smfmac_f32_16x16x64_bf16 v[64:67], v[52:55], a[128:135], v0\n" \
  "s_waitcnt lgkmcnt(3)\n" \
  "v_smfmac_f32_16x16x64_bf16 v[64:67], v[56:59], a[136:143], v0\n" \
  "s_waitcnt lgkmcnt(2)\n" \
  "v_smfmac_f32_16x16x64_bf16 v[64:67], v[60:63], a[144:151], v0\n" \
  "v_smfmac_f32_16x16x64_bf16 v[68:71], v[48:51], v[96:103], v0\n" \
  "v_smfmac_f32_16x16x64_bf16 v[68:71], v[52:55], a[152:159], v0\n" \
  "buffer_load_dword v19, v18, s[36:39], 0 offen sc1\n" \
  "s_waitcnt lgkmcnt(0)\n" \
  "v_cmp_u_f32 vcc, v64, v65\n" \
  "v_cmp_u_f32 s[82:83], v66, v67\n" \
  "v_smfmac_f32_16x16x64_bf16 v[68:71], v[56:59], v[104:111], v0\n" \
  "s_or_b64 vcc, vcc, s[82:83]\n" \
  "s_cbranch_scc1 LMQs0_%=\n" \
  "v_cvt_pk_bf16_f32 v64, v64, v65\n" \
  "v_smfmac_f32_16x16x64_bf16 v[68:71], v[60:63], a[160:167], v0\n" \
  "v_cvt_pk_bf16_f32 v65, v66, v67\n" \
  "v_smfmac_f32_16x16x64_bf16 v[72:75], v[48:51], a[168:175], v0\n" \
  "ds_write_b64 v6, v[64:65] offset:32768\n" \
  "v_smfmac_f32_16x16x64_bf16 v[72:75], v[52:55], a[176:183], v0\n" \
  "v_cvt_pk_bf16_f32 v68, v68, v69\n" \
  "v_smfmac_f32_16x16x64_bf16 v[72:75], v[56:59], a[184:191], v0\n" \
  "v_cvt_pk_bf16_f32 v69, v70, v71\n" \
  "v_smfmac_f32_16x16x64_bf16 v[72:75], v[60:63], a[192:199], v0\n" \
  "ds_write_b64 v6, v[68:69] offset:33280\n" \
  "v_smfmac_f32_16x16x64_bf16 v[76:79], v[32:35], a[200:207], v0\n" \
  "ds_read_b64_tr_b16 v[24:25], v9 offset:32768\n" \
  "v_smfmac_f32_16x16x64_bf16 v[76:79], v[36:39], a[208:215], v0\n" \
  "ds_read_b64_tr_b16 v[28:29], v9 offset:33792\n" \
  "v_cvt_pk_bf16_f32 v72, v72, v73\n" \
  "v_smfmac_f32_16x16x64_bf16 v[76:79], v[40:43], a[216:223], v0\n" \
  "ds_read_b64_tr_b16 v[112:113], v9 offset:34816\n" \
  "v_cvt_pk_bf16_f32 v73, v74, v75\n" \
  "v_smfmac_f32_16x16x64_bf16 v[76:79], v[44:47], a[224:231], v0\n" \
  "ds_write_b64 v6, v[72:73] offset:36864\n" \
  "v_smfmac_f32_16x16x64_bf16 v[76:79], v[48:51], a[232:239], v0\n" \
  "ds_read_b64_tr_b16 v[64:65], v9 offset:35840\n" \
  "v_smfmac_f32_16x16x64_bf16 v[76:79], v[52:55], a[240:247], v0\n" \
  "ds_read_b64_tr_b16 v[26:27], v9 offset:33280\n" \
  "v_smfmac_f32_16x16x64_bf16 v[76:79], v[56:59], a[248:255], v0\n" \
  "ds_read_b64_tr_b16 v[30:31], v9 offset:34304\n" \
  "v_smfmac_f32_16x16x64_bf16 v[76:79], v[60:63], v[120:127], v0\n" \
  "ds_read_b64_tr_b16 v[114:115], v9 offset:35328\n" \
  "v_smfmac_f32_16x16x64_bf16 v[80:83], v[32:35], v[128:135], v0\n" \
  "ds_read_b64_tr_b16 v[66:67], v9 offset:36352\n" \
  "v_smfmac_f32_16x16x64_bf16 v[80:83], v[36:39], v[136:143], v0\n" \
  "ds_read_b64_tr_b16 v[68:69], v9 offset:36864\n" \
  "v_cvt_pk_bf16_f32 v76, v76, v77\n" \
  "v_smfmac_f32_16x16x64_bf16 v[80:83], v[40:43], v[144:151], v0\n" \
  "ds_read_b64_tr_b16 v[70:71], v9 offset:37376\n" \
  "v_cvt_pk_bf16_f32 v77, v78, v79\n" \
  "v_smfmac_f32_16x16x64_bf16 v[80:83], v[44:47], v[152:159], v0\n" \
  "ds_write_b64 v6, v[76:77] offset:0\n" \
  "v_smfmac_f32_16x16x64_bf16 v[80:83], v[48:51], v[160:167], v0\n" \
  "ds_read_b64_tr_b16 v[72:73], v9 offset:37888\n" \
  "v_smfmac_f32_16x16x64_bf16 v[80:83], v[52:55], v[168:175], v0\n" \
  "s_waitcnt lgkmcnt(8)\n" \
  "ds_read_b64_tr_b16 v[74:75], v9 offset:38400\n" \
  "v_smfmac_f32_16x16x64_bf16 v[80:83], v[56:59], v[176:183], v0\n" \
  "v_smfmac_f32_16x16x64_bf16 v[80:83], v[60:63], v[184:191], v0\n" \
  "ds_read_b64_tr_b16 v[88:89], v9 offset:0\n" \
  "v_smfmac_f32_16x16x64_bf16 v[84:87], v[32:35], v[192:199], v0\n" \
  "ds_read_b64_tr_b16 v[92:93], v9 offset:1024\n" \
  "v_smfmac_f32_16x16x64_bf16 v[84:87], v[36:39], v[200:207], v0\n" \
  "ds_read_b64_tr_b16 v[96:97], v9 offset:2048\n" \
  "v_cvt_pk_bf16_f32 v80, v80, v81\n" \
  "v_smfmac_f32_16x16x64_bf16 v[84:87], v[40:43], v[208:215], v0\n" \
  "ds_read_b64_tr_b16 v[100:101], v9 offset:3072\n" \
  "v_cvt_pk_bf16_f32 v81, v82, v83\n" \
  "v_smfmac_f32_16x16x64_bf16 v[84:87], v[44:47], v[216:223], v0\n" \
  "ds_write_b64 v6, v[80:81] offset:512\n" \
  "v_smfmac_f32_16x16x64_bf16 v[84:87], v[48:51], v[224:231], v0\n" \
  "v_smfmac_f32_16x16x64_bf16 v[84:87], v[52:55], v[232:239], v0\n" \
  "v_smfmac_f32_16x16x64_bf16 v[84:87], v[56:59], v[240:247], v0\n" \
  "v_smfmac_f32_16x16x64_bf16 v[84:87], v[60:63], v[248:255], v0\n" \
  "s_waitcnt lgkmcnt(4)\n" \
  "s_nop 6\n" \
  "v_cvt_pk_bf16_f32 v84, v84, v85\n" \
  "v_cvt_pk_bf16_f32 v85, v86, v87\n" \
  "ds_write_b64 v6, v[84:85] offset:4096\n" \
  "ds_read_b64_tr_b16 v[90:91], v9 offset:512\n" \
  "ds_read_b64_tr_b16 v[102:103], v9 offset:3584\n" \
  "ds_read_b64_tr_b16 v[94:95], v9 offset:1536\n" \
  "ds_read_b64_tr_b16 v[98:99], v9 offset:2560\n" \
  "s_barrier ; timing only\n" \
  "ds_read_b64_tr_b16 v[104:105], v9 offset:4096\n" \
  "ds_read_b64_tr_b16 v[106:107], v9 offset:4608\n" \
  "ds_read_b64_tr_b16 v[108:109], v9 offset:5120\n" \
  "ds_read_b64_tr_b16 v[110:111], v9 offset:5632\n" \
  "ds_read2_b32 v[34:35], v12 offset0:0 offset1:64\n" \
  "s_waitcnt lgkmcnt(8)\n" \
  "v_mfma_f32_16x16x32_bf16 v[116:119], v[88:91], v[24:27], 0\n" \
  "s_waitcnt lgkmcnt(6)\n" \
  "v_mfma_f32_16x16x32_bf16 v[116:119], v[92:95], v[28:31], v[116:119]\n" \
  "s_waitcnt lgkmcnt(5)\n" \
  "v_mfma_f32_16x16x32_bf16 v[116:119], v[96:99], v[112:115], v[116:119]\n" \
  "v_mfma_f32_16x16x32_bf16 v[116:119], v[100:103], v[64:67], v[116:119]\n" \
  "s_waitcnt lgkmcnt(3)\n" \
  "v_mfma_f32_16x16x32_bf16 v[116:119], v[104:107], v[68:71], v[116:119]\n" \
  "s_waitcnt lgkmcnt(1)\n" \
  "v_mfma_f32_16x16x32_bf16 v[116:119], v[108:111], v[72:75], v[116:119]\n" \
  "s_waitcnt lgkmcnt(0)\n" \
  "v_add_f32 v34, v34, v35\n" \
  "s_nop 1\n" \
  "v_add_f32_dpp v34, v34, v34 row_ror:8 row_mask:0xf bank_mask:0xf\n" \
  "s_nop 2\n" \
  "v_permlane32_swap_b32 v116, v118\n" \
  "v_permlane32_swap_b32 v117, v119\n" \
  "v_add_f32 v118, v116, v118\n" \
  "v_add_f32 v119, v117, v119\n" \
  "v_mul_f32 v116, v4, v118\n" \
  "v_mul_f32 v117, v4, v119\n" \
  "v_fmac_f32_dpp v116, v118, v4 row_ror:8 row_mask:0xf bank_mask:0xf\n" \
  "v_fmac_f32_dpp v117, v119, v4 row_ror:8 row_mask:0xf bank_mask:0xf\n" \
  "v_exp_f32 v32, v116\n" \
  "v_exp_f32 v33, v117\n" \
  "v_add_f32 v34, v34, v32\n" \
  "v_add_f32 v36, v34, v33\n" \
  "v_add_f32 v37, v34, v33\n" \
  "v_cndmask_b32 v32, v14, v32, s[34:35]\n" \
  "v_cndmask_b32 v33, v15, v33, s[34:35]\n" \
  "v_permlane16_swap_b32 v36, v37\n" \
  "v_add_f32 v34, v36, v37\n" \
  "v_add_f32 v35, v36, v37\n" \
  "s_nop 1\n" \
  "v_permlane32_swap_b32 v34, v35\n" \
  "v_add_f32 v34, v34, v35\n" \
  "v_rcp_f32 v38, v34\n" \
  "v_subrev_u32 v34, 0x1f800000, v34\n" \
  "v_cmp_lt_u32 vcc, 0x40000000, v34\n" \
  "v_mul_f32 v32, v32, v38\n" \
  "v_mul_f32 v33, v33, v38\n" \
  "v_cvt_pk_bf16_f32 v32, v32, v33\n" \
  "s_cbranch_vccnz LMXs0_%=\n" \
  "buffer_store_dword v32, v13, s[36:39], s72 offen offset:0 sc1\n" \
  "buffer_store_dword v32, v13, s[36:39], s44 offen offset:0 sc1\n" \
  "buffer_store_dword v32, v13, s[36:39], s45 offen offset:0 sc1\n" \
  "buffer_store_dword v32, v13, s[36:39], s46 offen offset:0 sc1\n" \
  "s_barrier\n" \
  "LMYs0_%=:\n" \
  "s_waitcnt vmcnt(4)\n" \
  "v_cmp_ge_u32 vcc, s43, v19\n" \
  "s_nop 3\n" \
  "s_cbranch_vccnz LFFs0_%=\n" \
  "LFOKs0_%=:\n" \
  "v_xor_b32 v20, 0x8000, v20\n" \
  "v_xor_b32 v21, 0x8000, v21\n" \
  "ds_read_b128 v[88:91], v3 offset:0\n" \
  "ds_read_b128 v[92:95], v3 offset:1024\n" \
  "ds_read_b128 v[96:99], v3 offset:2048\n" \
  "ds_read_b128 v[100:103], v3 offset:3072\n" \
  "ds_read_b128 v[104:107], v3 offset:4096\n" \
  "ds_read_b128 v[108:111], v3 offset:5120\n" \
  "ds_read_b128 v[112:115], v3 offset:6144\n" \
  "ds_read_b128 v[116:119], v3 offset:7168\n" \
  "v_mov_b32 v60, -1\n" \
  "v_mov_b32 v61, -1\n" \
  "ds_write_b64 v6, v[60:61] offset:32768\n" \
  "ds_write_b64 v6, v[60:61] offset:33280\n" \
  "ds_write_b64 v6, v[60:61] offset:36864\n" \
  "v_mov_b32 v64, 0\n" \
  "v_mov_b32 v65, 0\n" \
  "v_mov_b32 v66, 0\n" \
  "v_mov_b32 v67, 0\n" \
  "v_mov_b32 v68, 0\n" \
  "v_mov_b32 v69, 0\n" \
  "v_mov_b32 v70, 0\n" \
  "v_mov_b32 v71, 0\n" \
  "v_mov_b32 v72, 0\n" \
  "v_mov_b32 v73, 0\n" \
  "v_mov_b32 v74, 0\n" \
  "v_mov_b32 v75, 0\n" \
  "v_mov_b32 v76, 0\n" \
  "v_mov_b32 v77, 0\n" \
  "v_mov_b32 v78, 0\n" \
  "v_mov_b32 v79, 0\n" \
  "v_mov_b32 v80, 0\n" \
  "v_mov_b32 v81, 0\n" \
  "v_mov_b32 v82, 0\n" \
  "v_mov_b32 v83, 0\n" \
  "v_mov_b32 v84, 0\n" \
  "v_mov_b32 v85, 0\n" \
  "v_mov_b32 v86, 0\n" \
  "v_mov_b32 v87, 0\n" \
  "s_waitcnt lgkmcnt(0)\n" \
  "s_add_u32 s49, s76, s96\n" \
  "LUKs0_%=:\n" \
  "s_memtime s[78:79]\n" \
  "s_waitcnt lgkmcnt(0)\n" \
  "s_sub_i32 s74, s49, s78\n" \
  "s_cmp_gt_i32 s74, 0\n" \
  "s_cbranch_scc1 LUKs0_%=\n" \
  "s_add_u32 m0, s40, 0\n" \
  "s_add_u32 s75, s41, 0\n" \
  "buffer_load_dwordx4 v8, s[36:39], s75 offen offset:0 sc1 lds\n" \
  "buffer_load_dwordx4 v8, s[36:39], s75 offen offset:1024 sc1 lds\n" \
  "buffer_load_dwordx4 v8, s[36:39], s75 offen offset:2048 sc1 lds\n" \
  "buffer_load_dwordx4 v8, s[36:39], s75 offen offset:3072 sc1 lds\n" \
  "s_add_u32 m0, s40, 4096\n" \
  "s_add_u32 s75, s41, 4096\n" \
  "buffer_load_dwordx4 v8, s[36:39], s75 offen offset:0 sc1 lds\n" \
  "buffer_load_dwordx4 v8, s[36:39], s75 offen offset:1024 sc1 lds\n" \
  "s_add_u32 m0, s40, 4096\n" \
  "s_add_u32 s75, s41, 4096\n" \
  "buffer_load_dwordx4 v8, s[36:39], s75 offen offset:2048 sc1 lds\n" \
  "buffer_load_dwordx4 v8, s[36:39], s75 offen offset:3072 sc1 lds\n" \
  "s_add_u32 m0, s40, 8192\n" \
  "s_add_u32 s75, s41, 16384\n" \
  "buffer_load_dwordx4 v8, s[36:39], s75 offen offset:0 sc1 lds\n" \
  "buffer_load_dwordx4 v8, s[36:39], s75 offen offset:1024 sc1 lds\n" \
  "buffer_load_dwordx4 v8, s[36:39], s75 offen offset:2048 sc1 lds\n" \
  "buffer_load_dwordx4 v8, s[36:39], s75 offen offset:3072 sc1 lds\n" \
  "s_add_u32 m0, s40, 12288\n" \
  "s_add_u32 s75, s41, 20480\n" \
  "buffer_load_dwordx4 v8, s[36:39], s75 offen offset:0 sc1 lds\n" \
  "buffer_load_dwordx4 v8, s[36:39], s75 offen offset:1024 sc1 lds\n" \
  "buffer_load_dwordx4 v8, s[36:39], s75 offen offset:2048 sc1 lds\n" \
  "buffer_load_dwordx4 v8, s[36:39], s75 offen offset:3072 sc1 lds\n" \
  "s_add_u32 m0, s40, 16384\n" \
  "s_add_u32 s75, s41, 32768\n" \
  "buffer_load_dwordx4 v8, s[36:39], s75 offen offset:0 sc1 lds\n" \
  "buffer_load_dwordx4 v8, s[36:39], s75 offen offset:1024 sc1 lds\n" \
  "s_add_u32 m0, s40, 16384\n" \
  "s_add_u32 s75, s41, 32768\n" \
  "buffer_load_dwordx4 v8, s[36:39], s75 offen offset:2048 sc1 lds\n" \
  "buffer_load_dwordx4 v8, s[36:39], s75 offen offset:3072 sc1 lds\n" \
  "s_add_u32 m0, s40, 20480\n" \
  "s_add_u32 s75, s41, 36864\n" \
  "buffer_load_dwordx4 v8, s[36:39], s75 offen offset:0 sc1 lds\n" \
  "buffer_load_dwordx4 v8, s[36:39], s75 offen offset:1024 sc1 lds\n" \
  "buffer_load_dwordx4 v8, s[36:39], s75 offen offset:2048 sc1 lds\n" \
  "buffer_load_dwordx4 v8, s[36:39], s75 offen offset:3072 sc1 lds\n" \
  "s_xor_b32 s40, s40, 0x8000\n" \
  "s_cmp_eq_u32 s68, 64\n" \
  "s_cbranch_scc1 LENDs0_%=\n" \
  "v_readlane_b32 s72, v11, s68\n" \
  "s_nop 0\n" \
  "s_add_u32 s69, s72, 2048\n" \
  "s_add_u32 s70, s72, 6144\n" \
  "s_add_u32 s44, s72, s93\n" \
  "s_add_u32 s45, s72, s94\n" \
  "s_add_u32 s46, s72, s95\n" \
  "v_readlane_b32 s41, v7, s68\n" \
  "s_nop 1\n" \
  "s_branch LOOPs0_%=\n" \
  "LFFs0_%=:\n" \
  "LQFs0_%=:\n" \
  "buffer_load_dword v19, v18, s[36:39], 0 offen sc1\n" \
  "s_waitcnt vmcnt(0)\n" \
  "v_cmp_ge_u32 vcc, s43, v19\n" \
  "s_nop 3\n" \
  "s_cbranch_vccz LRFs0_%=\n" \
  "s_add_u32 s73, s73, 1\n" \
  "s_cmp_gt_u32 s73, 20000\n" \
  "s_cbranch_scc1 LRFs0_%=\n" \
  "s_sleep 16\n" \
  "s_branch LQFs0_%=\n" \
  "LRFs0_%=:\n" \
  "s_branch LFOKs0_%=\n" \
  "LMFs0_%=:\n" \
  "ds_read_b128 v[88:91], v3 offset:0\n" \
  "ds_read_b128 v[92:95], v3 offset:1024\n" \
  "ds_read_b128 v[96:99], v3 offset:2048\n" \
  "ds_read_b128 v[100:103], v3 offset:3072\n" \
  "ds_read_b128 v[104:107], v3 offset:4096\n" \
  "ds_read_b128 v[108:111], v3 offset:5120\n" \
  "ds_read_b128 v[112:115], v3 offset:6144\n" \
  "ds_read_b128 v[116:119], v3 offset:7168\n" \
  "v_mov_b32 v60, -1\n" \
  "v_mov_b32 v61, -1\n" \
  "ds_write_b64 v6, v[60:61] offset:32768\n" \
  "ds_write_b64 v6, v[60:61] offset:33280\n" \
  "ds_write_b64 v6, v[60:61] offset:36864\n" \
  "v_mov_b32 v64, 0\n" \
  "v_mov_b32 v65, 0\n" \
  "v_mov_b32 v66, 0\n" \
  "v_mov_b32 v67, 0\n" \
  "v_mov_b32 v68, 0\n" \
  "v_mov_b32 v69, 0\n" \
  "v_mov_b32 v70, 0\n" \
  "v_mov_b32 v71, 0\n" \
  "v_mov_b32 v72, 0\n" \
  "v_mov_b32 v73, 0\n" \
  "v_mov_b32 v74, 0\n" \
  "v_mov_b32 v75, 0\n" \
  "v_mov_b32 v76, 0\n" \
  "v_mov_b32 v77, 0\n" \
  "v_mov_b32 v78, 0\n" \
  "v_mov_b32 v79, 0\n" \
  "v_mov_b32 v80, 0\n" \
  "v_mov_b32 v81, 0\n" \
  "v_mov_b32 v82, 0\n" \
  "v_mov_b32 v83, 0\n" \
  "v_mov_b32 v84, 0\n" \
  "v_mov_b32 v85, 0\n" \
  "v_mov_b32 v86, 0\n" \
  "v_mov_b32 v87, 0\n" \
  "s_waitcnt lgkmcnt(0)\n" \
  "s_barrier\n" \
  "buffer_load_dwordx4 v[24:27], v1, s[36:39], s72 offen offset:0 sc1\n" \
  "s_mov_b32 m0, 65536\n" \
  "s_nop 0\n" \
  "buffer_load_dwordx4 v1, s[36:39], s69 offen sc1 lds\n" \
  "buffer_load_dwordx4 v[28:31], v1, s[36:39], s72 offen offset:1024 sc1\n" \
  "s_mov_b32 m0, 69632\n" \
  "s_nop 0\n" \
  "buffer_load_dwordx4 v1, s[36:39], s70 offen sc1 lds\n" \
  "ds_write_b64 v6, v[60:61] offset:0\n" \
  "ds_write_b64 v6, v[60:61] offset:512\n" \
  "ds_write_b64 v6, v[60:61] offset:4096\n" \
  "s_branch LMs0_%=\n" \
  "LMQs0_%=:\n" \
  "s_waitcnt lgkmcnt(0)\n" \
  "s_barrier\n" \
  "v_mov_b32 v38, -1\n" \
  "LMXs0_%=:\n" \
  "v_cmp_u_f32 vcc, v38, v38\n" \
  "s_nop 3\n" \
  "s_cmp_lg_u64 vcc, 0\n" \
  "s_cselect_b32 s74, 2, 1\n" \
  "s_cmp_gt_u32 s56, 20000\n" \
  "s_cselect_b32 s74, 1, s74\n" \
  "v_mov_b32 v34, s74\n" \
  "ds_write_b32 v12, v34 offset:512\n" \
  "s_waitcnt lgkmcnt(0)\n" \
  "s_barrier\n" \
  "s_barrier\n" \
  "v_mov_b32 v34, 0\n" \
  "ds_write_b32 v12, v34 offset:512\n" \
  "s_lshr_b32 s74, s74, 1\n" \
  "s_add_u32 s56, s56, s74\n" \
  "s_cmp_lg_u32 s74, 0\n" \
  "s_cbranch_scc1 LMFs0_%=\n" \
  "s_add_u32 s57, s57, 1\n" \
  "v_mov_b32 v46, v116\n" \
  "v_mov_b32 v47, v117\n" \
  "ds_read_b128 v[88:91], v20 offset:0\n" \
  "ds_read_b128 v[92:95], v20 offset:2048\n" \
  "ds_read_b128 v[96:99], v20 offset:4096\n" \
  "ds_read_b128 v[100:103], v20 offset:6144\n" \
  "ds_read_b64 v[104:105], v21 offset:0\n" \
  "ds_read_b64 v[106:107], v21 offset:1024\n" \
  "ds_read_b64 v[108:109], v21 offset:2048\n" \
  "ds_read_b64 v[110:111], v21 offset:3072\n" \
  "s_waitcnt lgkmcnt(7)\n" \
  "v_mfma_f32_16x16x32_bf16 v[48:51], v[88:91], v[24:27], 0\n" \
  "s_waitcnt lgkmcnt(6)\n" \
  "v_mfma_f32_16x16x32_bf16 v[48:51], v[92:95], v[28:31], v[48:51]\n" \
  "s_waitcnt lgkmcnt(5)\n" \
  "v_mfma_f32_16x16x32_bf16 v[48:51], v[96:99], v[112:115], v[48:51]\n" \
  "s_waitcnt lgkmcnt(4)\n" \
  "v_mfma_f32_16x16x32_bf16 v[48:51], v[100:103], v[64:67], v[48:51]\n" \
  "s_waitcnt lgkmcnt(2)\n" \
  "v_mfma_f32_16x16x32_bf16 v[48:51], v[104:107], v[68:71], v[48:51]\n" \
  "s_waitcnt lgkmcnt(0)\n" \
  "v_mfma_f32_16x16x32_bf16 v[48:51], v[108:111], v[72:75], v[48:51]\n" \
  "s_nop 7\n" \
  "ds_read_b128 v[88:91], v20 offset:128\n" \
  "ds_read_b128 v[92:95], v20 offset:2176\n" \
  "ds_read_b128 v[96:99], v20 offset:4224\n" \
  "ds_read_b128 v[100:103], v20 offset:6272\n" \
  "ds_read_b64 v[104:105], v21 offset:128\n" \
  "ds_read_b64 v[106:107], v21 offset:1152\n" \
  "ds_read_b64 v[108:109], v21 offset:2176\n" \
  "ds_read_b64 v[110:111], v21 offset:3200\n" \
  "s_waitcnt lgkmcnt(7)\n" \
  "v_mfma_f32_16x16x32_bf16 v[52:55], v[88:91], v[24:27], 0\n" \
  "s_waitcnt lgkmcnt(6)\n" \
  "v_mfma_f32_16x16x32_bf16 v[52:55], v[92:95], v[28:31], v[52:55]\n" \
  "s_waitcnt lgkmcnt(5)\n" \
  "v_mfma_f32_16x16x32_bf16 v[52:55], v[96:99], v[112:115], v[52:55]\n" \
  "s_waitcnt lgkmcnt(4)\n" \
  "v_mfma_f32_16x16x32_bf16 v[52:55], v[100:103], v[64:67], v[52:55]\n" \
  "s_waitcnt lgkmcnt(2)\n" \
  "v_mfma_f32_16x16x32_bf16 v[52:55], v[104:107], v[68:71], v[52:55]\n" \
  "s_waitcnt lgkmcnt(0)\n" \
  "v_mfma_f32_16x16x32_bf16 v[52:55], v[108:111], v[72:75], v[52:55]\n" \
  "s_nop 7\n" \
  "v_add_f32_dpp v48, v48, v48 row_ror:8 row_mask:0xf bank_mask:0xf\n" \
  "v_mul_f32 v48, v4, v48\n" \
  "v_add_f32_dpp v49, v49, v49 row_ror:8 row_mask:0xf bank_mask:0xf\n" \
  "v_mul_f32 v49, v4, v49\n" \
  "v_add_f32_dpp v50, v50, v50 row_ror:8 row_mask:0xf bank_mask:0xf\n" \
  "v_mul_f32 v50, v4, v50\n" \
  "v_add_f32_dpp v51, v51, v51 row_ror:8 row_mask:0xf bank_mask:0xf\n" \
  "v_mul_f32 v51, v4, v51\n" \
  "v_add_f32_dpp v52, v52, v52 row_ror:8 row_mask:0xf bank_mask:0xf\n" \
  "v_mul_f32 v52, v4, v52\n" \
  "v_add_f32_dpp v53, v53, v53 row_ror:8 row_mask:0xf bank_mask:0xf\n" \
  "v_mul_f32 v53, v4, v53\n" \
  "v_add_f32_dpp v54, v54, v54 row_ror:8 row_mask:0xf bank_mask:0xf\n" \
  "v_mul_f32 v54, v4, v54\n" \
  "v_add_f32_dpp v55, v55, v55 row_ror:8 row_mask:0xf bank_mask:0xf\n" \
  "v_mul_f32 v55, v4, v55\n" \
  "v_max_f32 v32, v46, v47\n" \
  "v_max3_f32 v32, v32, v48, v49\n" \
  "v_max3_f32 v32, v32, v50, v51\n" \
  "v_max3_f32 v32, v32, v52, v53\n" \
  "v_max3_f32 v32, v32, v54, v55\n" \
  "v_mov_b32 v33, v32\n" \
  "s_nop 1\n" \
  "v_permlane16_swap_b32 v32, v33\n" \
  "v_max_f32 v32, v32, v33\n" \
  "v_mov_b32 v33, v32\n" \
  "s_nop 1\n" \
  "v_permlane32_swap_b32 v32, v33\n" \
  "v_max_f32 v32, v32, v33\n" \
  "v_sub_f32 v46, v46, v32\n" \
  "v_sub_f32 v47, v47, v32\n" \
  "v_sub_f32 v48, v48, v32\n" \
  "v_sub_f32 v49, v49, v32\n" \
  "v_sub_f32 v50, v50, v32\n" \
  "v_sub_f32 v51, v51, v32\n" \
  "v_sub_f32 v52, v52, v32\n" \
  "v_sub_f32 v53, v53, v32\n" \
  "v_sub_f32 v54, v54, v32\n" \
  "v_sub_f32 v55, v55, v32\n" \
  "v_exp_f32 v46, v46\n" \
  "v_exp_f32 v47, v47\n" \
  "v_exp_f32 v48, v48\n" \
  "v_exp_f32 v49, v49\n" \
  "v_exp_f32 v50, v50\n" \
  "v_exp_f32 v51, v51\n" \
  "v_exp_f32 v52, v52\n" \
  "v_exp_f32 v53, v53\n" \
  "v_exp_f32 v54, v54\n" \
  "v_exp_f32 v55, v55\n" \
  "v_add_f32 v32, v46, v47\n" \
  "v_add_f32 v32, v32, v48\n" \
  "v_add_f32 v32, v32, v49\n" \
  "v_add_f32 v32, v32, v50\n" \
  "v_add_f32 v32, v32, v51\n" \
  "v_add_f32 v32, v32, v52\n" \
  "v_add_f32 v32, v32, v53\n" \
  "v_add_f32 v32, v32, v54\n" \
  "v_add_f32 v32, v32, v55\n" \
  "v_mov_b32 v33, v32\n" \
  "s_nop 1\n" \
  "v_permlane16_swap_b32 v32, v33\n" \
  "v_add_f32 v32, v32, v33\n" \
  "v_mov_b32 v33, v32\n" \
  "s_nop 1\n" \
  "v_permlane32_swap_b32 v32, v33\n" \
  "v_add_f32 v32, v32, v33\n" \
  "v_rcp_f32 v32, v32\n" \
  "v_cvt_pk_bf16_f32 v34, v48, v49\n" \
  "v_pk_min_u16 v34, v34, -2 op_sel_hi:[1,0]\n" \
  "v_cvt_pk_bf16_f32 v35, v50, v51\n" \
  "v_pk_min_u16 v35, v35, -2 op_sel_hi:[1,0]\n" \
  "v_cvt_pk_bf16_f32 v36, v52, v53\n" \
  "v_pk_min_u16 v36, v36, -2 op_sel_hi:[1,0]\n" \
  "v_cvt_pk_bf16_f32 v37, v54, v55\n" \
  "v_pk_min_u16 v37, v37, -2 op_sel_hi:[1,0]\n" \
  "v_cndmask_b32 v34, v35, v34, s[34:35]\n" \
  "buffer_store_dword v34, v22, s[36:39], s72 offen offset:0 sc1\n" \
  "buffer_store_dword v34, v22, s[36:39], s44 offen offset:0 sc1\n" \
  "buffer_store_dword v34, v22, s[36:39], s45 offen offset:0 sc1\n" \
  "buffer_store_dword v34, v22, s[36:39], s46 offen offset:0 sc1\n" \
  "v_cndmask_b32 v36, v37, v36, s[34:35]\n" \
  "buffer_store_dword v36, v22, s[36:39], s72 offen offset:256 sc1\n" \
  "buffer_store_dword v36, v22, s[36:39], s44 offen offset:256 sc1\n" \
  "buffer_store_dword v36, v22, s[36:39], s45 offen offset:256 sc1\n" \
  "buffer_store_dword v36, v22, s[36:39], s46 offen offset:256 sc1\n" \
  "v_cndmask_b32 v46, v14, v46, s[34:35]\n" \
  "v_cndmask_b32 v47, v15, v47, s[34:35]\n" \
  "v_mul_f32 v46, v46, v32\n" \
  "v_mul_f32 v47, v47, v32\n" \
  "v_cvt_pk_bf16_f32 v46, v46, v47\n" \
  "v_pk_min_u16 v46, v46, -2 op_sel_hi:[1,0]\n" \
  "v_or_b32 v33, v14, v15\n" \
  "v_cmp_eq_u32 vcc, 0, v33\n" \
  "v_mov_b32 v33, 0xfffe\n" \
  "s_nop 3\n" \
  "s_andn2_b64 vcc, vcc, s[34:35]\n" \
  "s_nop 3\n" \
  "v_cndmask_b32 v46, v46, v33, vcc\n" \
  "s_waitcnt vmcnt(0) lgkmcnt(0)\n" \
  "buffer_store_dword v46, v13, s[36:39], s72 offen offset:0 sc1\n" \
  "buffer_store_dword v46, v13, s[36:39], s44 offen offset:0 sc1\n" \
  "buffer_store_dword v46, v13, s[36:39], s45 offen offset:0 sc1\n" \
  "buffer_store_dword v46, v13, s[36:39], s46 offen offset:0 sc1\n" \
  "s_branch LMYs0_%=\n" \
  "LENDs0_%=:\n" \
  "s_cmp_lg_u32 s38, 0\n" \
  "s_cbranch_scc1 LFINs0_%=\n" \
  "s_waitcnt vmcnt(0) lgkmcnt(0)\n" \
  "s_mov_b32 s38, s47\n" \
  "s_mov_b32 s68, 0\n" \
  "s_mov_b32 s73, 0\n" \
  "s_mov_b32 s56, 0\n" \
  "s_mov_b32 s57, 0\n" \
  "s_branch LPASSs0_%=\n" \
  "LFINs0_%=:\n" \
  "s_branch LDONE_%=\n" \
  "LSTARTs1_%=:\n" \
  "s_mov_b32 s47, s38\n" \
  "s_mov_b32 s38, 0\n" \
  "s_mov_b32 s68, 63\n" \
  "s_mov_b32 s73, 20001\n" \
  "s_mov_b32 s56, 19998\n" \
  "LPASSs1_%=:\n" \
  "s_mov_b32 s40, 0\n" \
  "s_mov_b32 s42, 0x40000000\n" \
  "v_and_b32 v20, 0x7fff, v20\n" \
  "v_and_b32 v21, 0x7fff, v21\n" \
  "v_and_b32 v18, 0x7fff, v18\n" \
  "s_barrier\n" \
  "s_add_u32 m0, s40, 4096\n" \
  "s_add_u32 s75, s55, 4096\n" \
  "buffer_load_dwordx4 v8, s[36:39], s75 offen offset:2048 sc1 lds\n" \
  "buffer_load_dwordx4 v8, s[36:39], s75 offen offset:3072 sc1 lds\n" \
  "s_add_u32 m0, s40, 8192\n" \
  "s_add_u32 s75, s55, 16384\n" \
  "buffer_load_dwordx4 v8, s[36:39], s75 offen offset:0 sc1 lds\n" \
  "buffer_load_dwordx4 v8, s[36:39], s75 offen offset:1024 sc1 lds\n" \
  "buffer_load_dwordx4 v8, s[36:39], s75 offen offset:2048 sc1 lds\n" \
  "buffer_load_dwordx4 v8, s[36:39], s75 offen offset:3072 sc1 lds\n" \
  "s_xor_b32 s40, s40, 0x8000\n" \
  "v_readlane_b32 s72, v11, 0\n" \
  "s_nop 0\n" \
  "s_add_u32 s69, s72, 3072\n" \
  "s_add_u32 s70, s72, 7168\n" \
  "s_add_u32 s44, s72, s93\n" \
  "s_add_u32 s45, s72, s94\n" \
  "s_add_u32 s46, s72, s95\n" \
  "v_readlane_b32 s41, v7, 0\n" \
  "s_nop 1\n" \
  "LOOPs1_%=:\n" \
  "v_mov_b32 v60, -1\n" \
  "v_mov_b32 v61, -1\n" \
  "v_mov_b32 v64, 0\n" \
  "v_mov_b32 v65, 0\n" \
  "v_mov_b32 v66, 0\n" \
  "v_mov_b32 v67, 0\n" \
  "v_mov_b32 v68, 0\n" \
  "v_mov_b32 v69, 0\n" \
  "v_mov_b32 v70, 0\n" \
  "v_mov_b32 v71, 0\n" \
  "v_mov_b32 v72, 0\n" \
  "v_mov_b32 v73, 0\n" \
  "v_mov_b32 v74, 0\n" \
  "v_mov_b32 v75, 0\n" \
  "v_mov_b32 v76, 0\n" \
  "v_mov_b32 v77, 0\n" \
  "v_mov_b32 v78, 0\n" \
  "v_mov_b32 v79, 0\n" \
  "v_mov_b32 v80, 0\n" \
  "v_mov_b32 v81, 0\n" \
  "v_mov_b32 v82, 0\n" \
  "v_mov_b32 v83, 0\n" \
  "v_mov_b32 v84, 0\n" \
  "v_mov_b32 v85, 0\n" \
  "v_mov_b32 v86, 0\n" \
  "v_mov_b32 v87, 0\n" \
  "s_barrier\n" \
  "buffer_load_dwordx4 v[24:27], v1, s[36:39], s72 offen offset:0 sc1\n" \
  "s_mov_b32 m0, 66560\n" \
  "s_nop 0\n" \
  "buffer_load_dwordx4 v1, s[36:39], s69 offen sc1 lds\n" \
  "buffer_load_dwordx4 v[28:31], v1, s[36:39], s72 offen offset:1024 sc1\n" \
  "s_mov_b32 m0, 70656\n" \
  "s_nop 0\n" \
  "buffer_load_dwordx4 v1, s[36:39], s70 offen sc1 lds\n" \
  "ds_read_b128 v[88:91], v3 offset:0\n" \
  "ds_read_b128 v[92:95], v3 offset:1024\n" \
  "ds_read_b128 v[96:99], v3 offset:2048\n" \
  "ds_read_b128 v[100:103], v3 offset:3072\n" \
  "ds_read_b128 v[104:107], v3 offset:4096\n" \
  "ds_read_b128 v[108:111], v3 offset:5120\n" \
  "ds_read_b128 v[112:115], v3 offset:6144\n" \
  "ds_read_b128 v[116:119], v3 offset:7168\n" \
  "ds_write_b64 v6, v[60:61] offset:33792\n" \
  "ds_write_b64 v6, v[60:61] offset:34304\n" \
  "ds_write_b64 v6, v[60:61] offset:37376\n" \
  "ds_write_b64 v6, v[60:61] offset:1024\n" \
  "ds_write_b64 v6, v[60:61] offset:1536\n" \
  "ds_write_b64 v6, v[60:61] offset:4608\n" \
  "ds_write_b32 v12, v60 offset:0\n" \
  "s_add_u32 s68, s68, 1\n" \
  "LMs1_%=:\n" \
  "buffer_store_dwordx4 v[52:55], v16, s[36:39], s42 offen sc1\n" \
  "buffer_store_dword v56, v17, s[36:39], s42 offen sc1\n" \
  "s_mov_b32 s42, 0x40000000\n" \
  "s_waitcnt vmcnt(5)\n" \
  "s_waitcnt lgkmcnt(13)\n" \
  "v_smfmac_f32_16x16x64_bf16 v[64:67], v[24:27], v[88:95], v0\n" \
  "ds_read_b128 v[88:91], v3 offset:8192\n" \
  "s_waitcnt lgkmcnt(12)\n" \
  "v_smfmac_f32_16x16x64_bf16 v[68:71], v[24:27], v[96:103], v0\n" \
  "ds_read_b128 v[92:95], v3 offset:9216\n" \
  "s_waitcnt lgkmcnt(11)\n" \
  "v_smfmac_f32_16x16x64_bf16 v[72:75], v[24:27], v[104:111], v0\n" \
  "ds_read_b128 v[96:99], v3 offset:10240\n" \
  "s_waitcnt lgkmcnt(10)\n" \
  "v_smfmac_f32_16x16x64_bf16 v[76:79], v[24:27], v[112:119], v0\n" \
  "ds_read_b128 v[100:103], v3 offset:11264\n" \
  "v_smfmac_f32_16x16x64_bf16 v[80:83], v[24:27], a[0:7], v0\n" \
  "ds_read_b128 v[104:107], v3 offset:12288\n" \
  "v_smfmac_f32_16x16x64_bf16 v[84:87], v[24:27], a[8:15], v0\n" \
  "s_waitcnt vmcnt(4)\n" \
  "s_waitcnt lgkmcnt(5)\n" \
  "s_barrier\n" \
  "ds_read_b128 v[32:35], v2 offset:2048\n" \
  "s_waitcnt vmcnt(3)\n" \
  "v_smfmac_f32_16x16x64_bf16 v[64:67], v[28:31], a[16:23], v0\n" \
  "ds_read_b128 v[36:39], v2 offset:3072\n" \
  "v_smfmac_f32_16x16x64_bf16 v[68:71], v[28:31], a[24:31], v0\n" \
  "ds_read_b128 v[40:43], v2 offset:4096\n" \
  "s_waitcnt lgkmcnt(6)\n" \
  "v_smfmac_f32_16x16x64_bf16 v[72:75], v[28:31], v[88:95], v0\n" \
  "ds_read_b128 v[44:47], v2 offset:5120\n" \
  "v_smfmac_f32_16x16x64_bf16 v[76:79], v[28:31], a[32:39], v0\n" \
  "ds_read_b128 v[108:111], v3 offset:13312\n" \
  "v_smfmac_f32_16x16x64_bf16 v[80:83], v[28:31], a[40:47], v0\n" \
  "ds_read_b128 v[112:115], v3 offset:14336\n" \
  "v_smfmac_f32_16x16x64_bf16 v[84:87], v[28:31], a[48:55], v0\n" \
  "ds_read_b128 v[116:119], v3 offset:15360\n" \
  "s_waitcnt lgkmcnt(6)\n" \
  "v_smfmac_f32_16x16x64_bf16 v[64:67], v[32:35], a[56:63], v0\n" \
  "ds_read_b128 v[88:91], v3 offset:16384\n" \
  "v_smfmac_f32_16x16x64_bf16 v[68:71], v[32:35], v[96:103], v0\n" \
  "ds_read_b128 v[92:95], v3 offset:17408\n" \
  "v_smfmac_f32_16x16x64_bf16 v[72:75], v[32:35], a[64:71], v0\n" \
  "ds_read_b128 v[96:99], v3 offset:18432\n" \
  "s_waitcnt lgkmcnt(8)\n" \
  "v_smfmac_f32_16x16x64_bf16 v[64:67], v[36:39], a[72:79], v0\n" \
  "s_waitcnt vmcnt(2)\n" \
  "s_barrier\n" \
  "ds_read_b128 v[100:103], v3 offset:19456\n" \
  "v_smfmac_f32_16x16x64_bf16 v[68:71], v[36:39], a[80:87], v0\n" \
  "ds_read_b128 v[48:51], v2 offset:6144\n" \
  "v_smfmac_f32_16x16x64_bf16 v[72:75], v[36:39], a[88:95], v0\n" \
  "ds_read_b128 v[52:55], v2 offset:7168\n" \
  "s_waitcnt lgkmcnt(8)\n" \
  "v_smfmac_f32_16x16x64_bf16 v[64:67], v[40:43], v[104:111], v0\n" \
  "ds_read_b128 v[56:59], v2 offset:8192\n" \
  "v_smfmac_f32_16x16x64_bf16 v[68:71], v[40:43], a[96:103], v0\n" \
  "ds_read_b128 v[60:63], v2 offset:9216\n" \
  "s_waitcnt lgkmcnt(8)\n" \
  "v_smfmac_f32_16x16x64_bf16 v[72:75], v[40:43], v[112:119], v0\n" \
  "ds_read_b128 v[104:107], v3 offset:20480\n" \
  "v_smfmac_f32_16x16x64_bf16 v[64:67], v[44:47], a[104:111], v0\n" \
  "ds_read_b128 v[108:111], v3 offset:21504\n" \
  "s_waitcnt lgkmcnt(8)\n" \
  "v_smfmac_f32_16x16x64_bf16 v[68:71], v[44:47], v[88:95], v0\n" \
  "ds_read_b128 v[88:91], v20 offset:0\n" \
  "v_smfmac_f32_16x16x64_bf16 v[72:75], v[44:47], a[112:119], v0\n" \
  "ds_read_b128 v[92:95], v20 offset:2048\n" \
  "s_waitcnt lgkmcnt(7)\n" \
  "v_smfmac_f32_16x16x64_bf16 v[64:67], v[48:51], a[120:127], v0\n" \
  "s_waitcnt lgkmcnt(6)\n" \
  "v_smfmac_f32_16x16x64_bf16 v[64:67], v[52:55], a[128:135], v0\n" \
  "s_waitcnt lgkmcnt(5)\n" \
  "v_smfmac_f32_16x16x64_bf16 v[64:67], v[56:59], a[136:143], v0\n" \
  "s_waitcnt lgkmcnt(4)\n" \
  "v_smfmac_f32_16x16x64_bf16 v[64:67], v[60:63], a[144:151], v0\n" \
  "v_smfmac_f32_16x16x64_bf16 v[68:71], v[48:51], v[96:103], v0\n" \
  "ds_read_b128 v[96:99], v20 offset:4096\n" \
  "v_smfmac_f32_16x16x64_bf16 v[68:71], v[52:55], a[152:159], v0\n" \
  "ds_read_b128 v[100:103], v20 offset:6144\n" \
  "s_waitcnt lgkmcnt(4)\n" \
  "v_cmp_u_f32 vcc, v64, v65\n" \
  "v_cmp_u_f32 s[82:83], v66, v67\n" \
  "v_smfmac_f32_16x16x64_bf16 v[68:71], v[56:59], v[104:111], v0\n" \
  "s_or_b64 vcc, vcc, s[82:83]\n" \
  "s_cbranch_scc1 LMQs1_%=\n" \
  "ds_read_b64 v[104:105], v21 offset:0\n" \
  "v_cvt_pk_bf16_f32 v64, v64, v65\n" \
  "v_smfmac_f32_16x16x64_bf16 v[68:71], v[60:63], a[160:167], v0\n" \
  "ds_read_b64 v[106:107], v21 offset:1024\n" \
  "v_cvt_pk_bf16_f32 v65, v66, v67\n" \
  "v_smfmac_f32_16x16x64_bf16 v[72:75], v[48:51], a[168:175], v0\n" \
  "ds_write_b64 v6, v[64:65] offset:33792\n" \
  "v_smfmac_f32_16x16x64_bf16 v[72:75], v[52:55], a[176:183], v0\n" \
  "ds_read_b64 v[108:109], v21 offset:2048\n" \
  "v_cvt_pk_bf16_f32 v68, v68, v69\n" \
  "v_smfmac_f32_16x16x64_bf16 v[72:75], v[56:59], a[184:191], v0\n" \
  "ds_read_b64 v[110:111], v21 offset:3072\n" \
  "v_cvt_pk_bf16_f32 v69, v70, v71\n" \
  "v_smfmac_f32_16x16x64_bf16 v[72:75], v[60:63], a[192:199], v0\n" \
  "ds_write_b64 v6, v[68:69] offset:34304\n" \
  "v_smfmac_f32_16x16x64_bf16 v[76:79], v[32:35], a[200:207], v0\n" \
  "ds_read_b64_tr_b16 v[24:25], v9 offset:32768\n" \
  "v_smfmac_f32_16x16x64_bf16 v[76:79], v[36:39], a[208:215], v0\n" \
  "ds_read_b64_tr_b16 v[28:29], v9 offset:33792\n" \
  "v_cvt_pk_bf16_f32 v72, v72, v73\n" \
  "v_smfmac_f32_16x16x64_bf16 v[76:79], v[40:43], a[216:223], v0\n" \
  "ds_read_b64_tr_b16 v[112:113], v9 offset:34816\n" \
  "v_cvt_pk_bf16_f32 v73, v74, v75\n" \
  "v_smfmac_f32_16x16x64_bf16 v[76:79], v[44:47], a[224:231], v0\n" \
  "ds_write_b64 v6, v[72:73] offset:37376\n" \
  "v_smfmac_f32_16x16x64_bf16 v[76:79], v[48:51], a[232:239], v0\n" \
  "ds_read_b64_tr_b16 v[64:65], v9 offset:35840\n" \
  "v_smfmac_f32_16x16x64_bf16 v[76:79], v[52:55], a[240:247], v0\n" \
  "s_waitcnt lgkmcnt(8)\n" \
  "ds_read_b64_tr_b16 v[26:27], v9 offset:33280\n" \
  "v_smfmac_f32_16x16x64_bf16 v[76:79], v[56:59], a[248:255], v0\n" \
  "ds_read_b64_tr_b16 v[30:31], v9 offset:34304\n" \
  "v_smfmac_f32_16x16x64_bf16 v[76:79], v[60:63], v[120:127], v0\n" \
  "ds_read_b64_tr_b16 v[114:115], v9 offset:35328\n" \
  "v_smfmac_f32_16x16x64_bf16 v[80:83], v[32:35], v[128:135], v0\n" \
  "ds_read_b64_tr_b16 v[66:67], v9 offset:36352\n" \
  "v_smfmac_f32_16x16x64_bf16 v[80:83], v[36:39], v[136:143], v0\n" \
  "ds_read_b64_tr_b16 v[68:69], v9 offset:36864\n" \
  "v_cvt_pk_bf16_f32 v76, v76, v77\n" \
  "v_smfmac_f32_16x16x64_bf16 v[80:83], v[40:43], v[144:151], v0\n" \
  "ds_read_b64_tr_b16 v[70:71], v9 offset:37376\n" \
  "v_cvt_pk_bf16_f32 v77, v78, v79\n" \
  "v_smfmac_f32_16x16x64_bf16 v[80:83], v[44:47], v[152:159], v0\n" \
  "ds_write_b64 v6, v[76:77] offset:1024\n" \
  "v_smfmac_f32_16x16x64_bf16 v[80:83], v[48:51], v[160:167], v0\n" \
  "s_waitcnt lgkmcnt(8)\n" \
  "ds_read_b64_tr_b16 v[72:73], v9 offset:37888\n" \
  "v_smfmac_f32_16x16x64_bf16 v[80:83], v[52:55], v[168:175], v0\n" \
  "ds_read_b64_tr_b16 v[74:75], v9 offset:38400\n" \
  "v_smfmac_f32_16x16x64_bf16 v[80:83], v[56:59], v[176:183], v0\n" \
  "v_smfmac_f32_16x16x64_bf16 v[80:83], v[60:63], v[184:191], v0\n" \
  "s_waitcnt lgkmcnt(8)\n" \
  "v_mfma_f32_16x16x32_bf16 v[116:119], v[88:91], v[24:27], 0\n" \
  "s_waitcnt lgkmcnt(7)\n" \
  "v_mfma_f32_16x16x32_bf16 v[116:119], v[92:95], v[28:31], v[116:119]\n" \
  "s_waitcnt lgkmcnt(6)\n" \
  "v_cvt_pk_bf16_f32 v80, v80, v81\n" \
  "v_mfma_f32_16x16x32_bf16 v[116:119], v[96:99], v[112:115], v[116:119]\n" \
  "s_waitcnt lgkmcnt(5)\n" \
  "v_cvt_pk_bf16_f32 v81, v82, v83\n" \
  "v_mfma_f32_16x16x32_bf16 v[116:119], v[100:103], v[64:67], v[116:119]\n" \
  "ds_write_b64 v6, v[80:81] offset:1536\n" \
  "s_waitcnt lgkmcnt(4)\n" \
  "v_mfma_f32_16x16x32_bf16 v[116:119], v[104:107], v[68:71], v[116:119]\n" \
  "s_waitcnt lgkmcnt(1)\n" \
  "v_mfma_f32_16x16x32_bf16 v[116:119], v[108:111], v[72:75], v[116:119]\n" \
  "v_smfmac_f32_16x16x64_bf16 v[84:87], v[32:35], v[192:199], v0\n" \
  "v_smfmac_f32_16x16x64_bf16 v[84:87], v[36:39], v[200:207], v0\n" \
  "v_add_f32_dpp v88, v116, v116 row_ror:8 row_mask:0xf bank_mask:0x3\n" \
  "v_add_f32_dpp v89, v117, v117 row_ror:8 row_mask:0xf bank_mask:0x3\n" \
  "v_smfmac_f32_16x16x64_bf16 v[84:87], v[40:43], v[208:215], v0\n" \
  "v_add_f32_dpp v88, v118, v118 row_ror:8 row_mask:0xf bank_mask:0xc\n" \
  "v_add_f32_dpp v89, v119, v119 row_ror:8 row_mask:0xf bank_mask:0xc\n" \
  "v_smfmac_f32_16x16x64_bf16 v[84:87], v[44:47], v[216:223], v0\n" \
  "v_mul_f32 v88, v4, v88\n" \
  "v_mul_f32 v89, v4, v89\n" \
  "v_smfmac_f32_16x16x64_bf16 v[84:87], v[48:51], v[224:231], v0\n" \
  "v_exp_f32 v88, v88\n" \
  "v_smfmac_f32_16x16x64_bf16 v[84:87], v[52:55], v[232:239], v0\n" \
  "v_exp_f32 v89, v89\n" \
  "v_smfmac_f32_16x16x64_bf16 v[84:87], v[56:59], v[240:247], v0\n" \
  "v_cmp_o_f32 vcc, v88, v89\n" \
  "v_cvt_pk_bf16_f32 v91, v88, v89\n" \
  "v_smfmac_f32_16x16x64_bf16 v[84:87], v[60:63], v[248:255], v0\n" \
  "s_mov_b64 exec, vcc\n" \
  "v_add_f32 v90, v88, v89\n" \
  "buffer_store_dword v91, v22, s[36:39], s72 offen offset:0 sc1\n" \
  "buffer_store_dword v91, v22, s[36:39], s44 offen offset:0 sc1\n" \
  "buffer_store_dword v91, v22, s[36:39], s45 offen offset:0 sc1\n" \
  "buffer_store_dword v91, v22, s[36:39], s46 offen offset:0 sc1\n" \
  "ds_write_b32 v12, v90 offset:0\n" \
  "s_mov_b64 exec, -1\n" \
  "v_cvt_pk_bf16_f32 v84, v84, v85\n" \
  "v_cvt_pk_bf16_f32 v85, v86, v87\n" \
  "ds_write_b64 v6, v[84:85] offset:4608\n" \
  "s_barrier ; timing only\n" \
  "v_mov_b32 v61, 0xf149f2ca\n" \
  "v_cmp_gt_i32 vcc, 0, v13\n" \
  "s_nop 1\n" \
  "v_cndmask_b32 v112, 0, v61, vcc\n" \
  "v_cmp_gt_i32 vcc, 1, v13\n" \
  "s_nop 1\n" \
  "v_cndmask_b32 v113, 0, v61, vcc\n" \
  "v_cmp_gt_i32 vcc, 2, v13\n" \
  "s_nop 1\n" \
  "v_cndmask_b32 v114, 0, v61, vcc\n" \
  "v_cmp_gt_i32 vcc, 3, v13\n" \
  "s_nop 1\n" \
  "v_cndmask_b32 v115, 0, v61, vcc\n" \
  "ds_read_b128 v[24:27], v18 offset:8192\n" \
  "ds_read_b128 v[64:67], v18 offset:0\n" \
  "ds_read_b64_tr_b16 v[48:49], v9 offset:0\n" \
  "ds_read_b64_tr_b16 v[50:51], v9 offset:512\n" \
  "ds_read_b128 v[28:31], v18 offset:10240\n" \
  "ds_read_b128 v[68:71], v18 offset:2048\n" \
  "ds_read_b64_tr_b16 v[52:53], v9 offset:1024\n" \
  "ds_read_b64_tr_b16 v[54:55], v9 offset:1536\n" \
  "ds_read_b128 v[32:35], v18 offset:12288\n" \
  "ds_read_b128 v[72:75], v18 offset:4096\n" \
  "ds_read_b64_tr_b16 v[56:57], v9 offset:2048\n" \
  "ds_read_b64_tr_b16 v[58:59], v9 offset:2560\n" \
  "s_waitcnt lgkmcnt(10)\n" \
  "v_mfma_f32_16x16x32_bf16 v[112:115], v[64:67], v[24:27], v[112:115]\n" \
  "s_waitcnt lgkmcnt(8)\n" \
  "v_mfma_f32_16x16x32_bf16 v[108:111], v[48:51], v[24:27], 0\n" \
  "ds_read_b128 v[36:39], v18 offset:14336\n" \
  "ds_read_b128 v[76:79], v18 offset:6144\n" \
  "ds_read_b64_tr_b16 v[48:49], v9 offset:3072\n" \
  "ds_read_b64_tr_b16 v[50:51], v9 offset:3584\n" \
  "s_waitcnt lgkmcnt(10)\n" \
  "v_mfma_f32_16x16x32_bf16 v[112:115], v[68:71], v[28:31], v[112:115]\n" \
  "s_waitcnt lgkmcnt(8)\n" \
  "v_mfma_f32_16x16x32_bf16 v[108:111], v[52:55], v[28:31], v[108:111]\n" \
  "ds_read_b64 v[40:41], v18 offset:20480\n" \
  "ds_read_b64 v[42:43], v18 offset:21504\n" \
  "ds_read_b64 v[80:81], v18 offset:16384\n" \
  "ds_read_b64 v[82:83], v18 offset:17408\n" \
  "ds_read_b64_tr_b16 v[52:53], v9 offset:4096\n" \
  "ds_read_b64_tr_b16 v[54:55], v9 offset:4608\n" \
  "s_waitcnt lgkmcnt(12)\n" \
  "v_mfma_f32_16x16x32_bf16 v[112:115], v[72:75], v[32:35], v[112:115]\n" \
  "s_waitcnt lgkmcnt(10)\n" \
  "v_mfma_f32_16x16x32_bf16 v[108:111], v[56:59], v[32:35], v[108:111]\n" \
  "ds_read_b64 v[44:45], v18 offset:22528\n" \
  "ds_read_b64 v[46:47], v18 offset:23552\n" \
  "ds_read_b64 v[84:85], v18 offset:18432\n" \
  "ds_read_b64 v[86:87], v18 offset:19456\n" \
  "ds_read_b64_tr_b16 v[56:57], v9 offset:5120\n" \
  "s_waitcnt lgkmcnt(8)\n" \
  "ds_read_b64_tr_b16 v[58:59], v9 offset:5632\n" \
  "v_mfma_f32_16x16x32_bf16 v[112:115], v[76:79], v[36:39], v[112:115]\n" \
  "v_mfma_f32_16x16x32_bf16 v[108:111], v[48:51], v[36:39], v[108:111]\n" \
  "s_waitcnt lgkmcnt(8)\n" \
  "v_mfma_f32_16x16x32_bf16 v[112:115], v[80:83], v[40:43], v[112:115]\n" \
  "s_waitcnt lgkmcnt(6)\n" \
  "v_mfma_f32_16x16x32_bf16 v[108:111], v[52:55], v[40:43], v[108:111]\n" \
  "s_barrier\n" \
  "ds_read_b32 v60, v12 offset:512\n" \
  "s_waitcnt lgkmcnt(3)\n" \
  "v_mfma_f32_16x16x32_bf16 v[112:115], v[84:87], v[44:47], v[112:115]\n" \
  "s_waitcnt lgkmcnt(1)\n" \
  "v_mfma_f32_16x16x32_bf16 v[108:111], v[56:59], v[44:47], v[108:111]\n" \
  "s_waitcnt lgkmcnt(0)\n" \
  "v_cmp_ne_u32 vcc, 0, v60\n" \
  "s_nop 3\n" \
  "s_cbranch_vccnz LMXs1_%=\n" \
  "LMYs1_%=:\n" \
  "s_nop 0\n" \
  "v_cmp_u_f32 vcc, v108, v109\n" \
  "v_cmp_u_f32 s[82:83], v110, v111\n" \
  "s_nop 1\n" \
  "s_or_b64 vcc, vcc, s[82:83]\n" \
  "s_cbranch_scc1 LAR1_%=\n" \
  "LAK1_%=:\n" \
  "v_permlane32_swap_b32 v108, v110\n" \
  "v_permlane32_swap_b32 v109, v111\n" \
  "v_pk_add_f32 v[110:111], v[108:109], v[110:111]\n" \
  "v_mov_b32 v32, 0x3d93cd3a\n" \
  "v_mov_b32 v34, 0x3fb8aa3b\n" \
  "v_cvt_pk_bf16_f32 v24, v110, v111\n" \
  "v_cvt_pk_bf16_f32 v25, v112, v113\n" \
  "v_cvt_pk_bf16_f32 v26, v114, v115\n" \
  "v_lshlrev_b32 v110, 16, v24\n" \
  "v_and_b32 v111, 0xffff0000, v24\n" \
  "v_pk_mul_f32 v[110:111], v[110:111], v[32:33] op_sel_hi:[1,0]\n" \
  "v_cvt_pk_bf16_f32 v24, v110, v111\n" \
  "v_lshlrev_b32 v110, 16, v24\n" \
  "v_and_b32 v111, 0xffff0000, v24\n" \
  "v_lshlrev_b32 v112, 16, v25\n" \
  "v_and_b32 v113, 0xffff0000, v25\n" \
  "v_pk_mul_f32 v[112:113], v[112:113], v[32:33] op_sel_hi:[1,0]\n" \
  "v_cvt_pk_bf16_f32 v25, v112, v113\n" \
  "v_lshlrev_b32 v112, 16, v25\n" \
  "v_and_b32 v113, 0xffff0000, v25\n" \
  "v_lshlrev_b32 v114, 16, v26\n" \
  "v_and_b32 v115, 0xffff0000, v26\n" \
  "v_pk_mul_f32 v[114:115], v[114:115], v[32:33] op_sel_hi:[1,0]\n" \
  "v_cvt_pk_bf16_f32 v26, v114, v115\n" \
  "v_lshlrev_b32 v114, 16, v26\n" \
  "v_and_b32 v115, 0xffff0000, v26\n" \
  "v_max_f32 v24, v110, v111\n" \
  "v_max3_f32 v24, v24, v112, v113\n" \
  "v_max3_f32 v24, v24, v114, v115\n" \
  "v_mov_b32 v25, v24\n" \
  "s_nop 1\n" \
  "v_permlane16_swap_b32 v24, v25\n" \
  "v_max_f32 v24, v24, v25\n" \
  "v_mov_b32 v25, v24\n" \
  "s_nop 1\n" \
  "v_permlane32_swap_b32 v24, v25\n" \
  "v_max_f32 v24, v24, v25\n" \
  "v_mul_f32 v24, v24, v34\n" \
  "v_pk_fma_f32 v[110:111], v[110:111], v[34:35], v[24:25] op_sel_hi:[1,0,0] neg_lo:[0,0,1] neg_hi:[0,0,1]\n" \
  "v_pk_fma_f32 v[112:113], v[112:113], v[34:35], v[24:25] op_sel_hi:[1,0,0] neg_lo:[0,0,1] neg_hi:[0,0,1]\n" \
  "v_pk_fma_f32 v[114:115], v[114:115], v[34:35], v[24:25] op_sel_hi:[1,0,0] neg_lo:[0,0,1] neg_hi:[0,0,1]\n" \
  "v_exp_f32 v110, v110\n" \
  "v_exp_f32 v111, v111\n" \
  "v_exp_f32 v112, v112\n" \
  "v_exp_f32 v113, v113\n" \
  "v_exp_f32 v114, v114\n" \
  "v_exp_f32 v115, v115\n" \
  "v_pk_add_f32 v[24:25], v[110:111], v[112:113]\n" \
  "v_pk_add_f32 v[24:25], v[24:25], v[114:115]\n" \
  "v_add_f32 v24, v24, v25\n" \
  "v_mov_b32 v25, v24\n" \
  "s_nop 1\n" \
  "v_permlane16_swap_b32 v24, v25\n" \
  "v_add_f32 v24, v24, v25\n" \
  "v_mov_b32 v25, v24\n" \
  "s_nop 1\n" \
  "v_permlane32_swap_b32 v24, v25\n" \
  "v_add_f32 v24, v24, v25\n" \
  "v_rcp_f32 v24, v24\n" \
  "v_mov_b32 v54, 0\n" \
  "v_mov_b32 v55, 0\n" \
  "v_pk_mul_f32 v[110:111], v[110:111], v[24:25] op_sel_hi:[1,0]\n" \
  "v_pk_mul_f32 v[112:113], v[112:113], v[24:25] op_sel_hi:[1,0]\n" \
  "v_pk_mul_f32 v[114:115], v[114:115], v[24:25] op_sel_hi:[1,0]\n" \
  "v_cvt_pk_bf16_f32 v52, v112, v113\n" \
  "v_cvt_pk_bf16_f32 v53, v114, v115\n" \
  "v_cvt_pk_bf16_f32 v56, v110, v111\n" \
  "s_sub_u32 s74, s68, 1\n" \
  "v_readlane_b32 s42, v15, s74\n" \
  "v_xor_b32 v20, 0x8000, v20\n" \
  "v_xor_b32 v21, 0x8000, v21\n" \
  "v_xor_b32 v18, 0x8000, v18\n" \
  "s_cmp_eq_u32 s68, 64\n" \
  "s_cbranch_scc1 LENDs1_%=\n" \
  "v_readlane_b32 s72, v11, s68\n" \
  "s_nop 0\n" \
  "s_add_u32 s69, s72, 3072\n" \
  "s_add_u32 s70, s72, 7168\n" \
  "s_add_u32 s44, s72, s93\n" \
  "s_add_u32 s45, s72, s94\n" \
  "s_add_u32 s46, s72, s95\n" \
  "v_readlane_b32 s41, v7, s68\n" \
  "s_nop 1\n" \
  "s_branch LOOPs1_%=\n" \
  "LAR1_%=:\n" \
  "ds_read_b64_tr_b16 v[64:65], v9 offset:0\n" \
  "ds_read_b64_tr_b16 v[66:67], v9 offset:512\n" \
  "ds_read_b64_tr_b16 v[68:69], v9 offset:1024\n" \
  "ds_read_b64_tr_b16 v[70:71], v9 offset:1536\n" \
  "ds_read_b64_tr_b16 v[72:73], v9 offset:2048\n" \
  "ds_read_b64_tr_b16 v[74:75], v9 offset:2560\n" \
  "ds_read_b64_tr_b16 v[76:77], v9 offset:3072\n" \
  "ds_read_b64_tr_b16 v[78:79], v9 offset:3584\n" \
  "ds_read_b64_tr_b16 v[80:81], v9 offset:4096\n" \
  "ds_read_b64_tr_b16 v[82:83], v9 offset:4608\n" \
  "ds_read_b64_tr_b16 v[84:85], v9 offset:5120\n" \
  "ds_read_b64_tr_b16 v[86:87], v9 offset:5632\n" \
  "s_waitcnt lgkmcnt(10)\n" \
  "v_mfma_f32_16x16x32_bf16 v[108:111], v[64:67], v[24:27], 0\n" \
  "s_waitcnt lgkmcnt(8)\n" \
  "v_mfma_f32_16x16x32_bf16 v[108:111], v[68:71], v[28:31], v[108:111]\n" \
  "s_waitcnt lgkmcnt(6)\n" \
  "v_mfma_f32_16x16x32_bf16 v[108:111], v[72:75], v[32:35], v[108:111]\n" \
  "s_waitcnt lgkmcnt(4)\n" \
  "v_mfma_f32_16x16x32_bf16 v[108:111], v[76:79], v[36:39], v[108:111]\n" \
  "s_waitcnt lgkmcnt(2)\n" \
  "v_mfma_f32_16x16x32_bf16 v[108:111], v[80:83], v[40:43], v[108:111]\n" \
  "s_waitcnt lgkmcnt(0)\n" \
  "v_mfma_f32_16x16x32_bf16 v[108:111], v[84:87], v[44:47], v[108:111]\n" \
  "s_nop 7\n" \
  "s_branch LAK1_%=\n" \
  "LMFs1_%=:\n" \
  "ds_read_b128 v[88:91], v3 offset:0\n" \
  "ds_read_b128 v[92:95], v3 offset:1024\n" \
  "ds_read_b128 v[96:99], v3 offset:2048\n" \
  "ds_read_b128 v[100:103], v3 offset:3072\n" \
  "ds_read_b128 v[104:107], v3 offset:4096\n" \
  "ds_read_b128 v[108:111], v3 offset:5120\n" \
  "ds_read_b128 v[112:115], v3 offset:6144\n" \
  "ds_read_b128 v[116:119], v3 offset:7168\n" \
  "v_mov_b32 v60, -1\n" \
  "v_mov_b32 v61, -1\n" \
  "ds_write_b64 v6, v[60:61] offset:33792\n" \
  "ds_write_b64 v6, v[60:61] offset:34304\n" \
  "ds_write_b64 v6, v[60:61] offset:37376\n" \
  "v_mov_b32 v64, 0\n" \
  "v_mov_b32 v65, 0\n" \
  "v_mov_b32 v66, 0\n" \
  "v_mov_b32 v67, 0\n" \
  "v_mov_b32 v68, 0\n" \
  "v_mov_b32 v69, 0\n" \
  "v_mov_b32 v70, 0\n" \
  "v_mov_b32 v71, 0\n" \
  "v_mov_b32 v72, 0\n" \
  "v_mov_b32 v73, 0\n" \
  "v_mov_b32 v74, 0\n" \
  "v_mov_b32 v75, 0\n" \
  "v_mov_b32 v76, 0\n" \
  "v_mov_b32 v77, 0\n" \
  "v_mov_b32 v78, 0\n" \
  "v_mov_b32 v79, 0\n" \
  "v_mov_b32 v80, 0\n" \
  "v_mov_b32 v81, 0\n" \
  "v_mov_b32 v82, 0\n" \
  "v_mov_b32 v83, 0\n" \
  "v_mov_b32 v84, 0\n" \
  "v_mov_b32 v85, 0\n" \
  "v_mov_b32 v86, 0\n" \
  "v_mov_b32 v87, 0\n" \
  "s_waitcnt lgkmcnt(0)\n" \
  "s_barrier\n" \
  "buffer_load_dwordx4 v[24:27], v1, s[36:39], s72 offen offset:0 sc1\n" \
  "s_mov_b32 m0, 66560\n" \
  "s_nop 0\n" \
  "buffer_load_dwordx4 v1, s[36:39], s69 offen sc1 lds\n" \
  "buffer_load_dwordx4 v[28:31], v1, s[36:39], s72 offen offset:1024 sc1\n" \
  "s_mov_b32 m0, 70656\n" \
  "s_nop 0\n" \
  "buffer_load_dwordx4 v1, s[36:39], s70 offen sc1 lds\n" \
  "ds_write_b64 v6, v[60:61] offset:1024\n" \
  "ds_write_b64 v6, v[60:61] offset:1536\n" \
  "ds_write_b64 v6, v[60:61] offset:4608\n" \
  "ds_write_b32 v12, v60 offset:0\n" \
  "s_branch LMs1_%=\n" \
  "LMQs1_%=:\n" \
  "s_waitcnt lgkmcnt(0)\n" \
  "s_barrier\n" \
  "s_barrier\n" \
  "ds_read_b32 v60, v12 offset:512\n" \
  "s_waitcnt lgkmcnt(0)\n" \
  "LMXs1_%=:\n" \
  "v_cmp_eq_u32 vcc, 2, v60\n" \
  "s_waitcnt lgkmcnt(0)\n" \
  "s_nop 2\n" \
  "s_cbranch_vccnz LMVs1_%=\n" \
  "s_waitcnt vmcnt(0)\n" \
  "LMVs1_%=:\n" \
  "s_barrier\n" \
  "s_cbranch_vccnz LMFs1_%=\n" \
  "s_branch LMYs1_%=\n" \
  "LENDs1_%=:\n" \
  "s_cmp_lg_u32 s38, 0\n" \
  "s_cbranch_scc1 LFINs1_%=\n" \
  "s_waitcnt vmcnt(0) lgkmcnt(0)\n" \
  "s_mov_b32 s38, s47\n" \
  "s_mov_b32 s68, 0\n" \
  "s_mov_b32 s73, 0\n" \
  "s_mov_b32 s56, 0\n" \
  "s_mov_b32 s57, 0\n" \
  "s_branch LPASSs1_%=\n" \
  "LFINs1_%=:\n" \
  "s_nop 1\n" \
  "buffer_store_dwordx4 v[52:55], v16, s[36:39], s42 offen sc1\n" \
  "buffer_store_dword v56, v17, s[36:39], s42 offen sc1\n" \
  "s_waitcnt vmcnt(0)\n" \
  "v_mov_b32 v19, 1\n" \
  "buffer_atomic_add v19, v14, s[36:39], 0 offen sc1\n" \
  "s_branch LDONE_%=\n" \
  "LSTARTs2_%=:\n" \
  "s_mov_b32 s47, s38\n" \
  "s_mov_b32 s38, 0\n" \
  "s_mov_b32 s68, 63\n" \
  "s_mov_b32 s73, 20001\n" \
  "s_mov_b32 s56, 19998\n" \
  "LPASSs2_%=:\n" \
  "s_mov_b32 s40, 0\n" \
  "s_mov_b32 s42, 0x40000000\n" \
  "v_and_b32 v20, 0x7fff, v20\n" \
  "v_and_b32 v21, 0x7fff, v21\n" \
  "v_and_b32 v18, 0x7fff, v18\n" \
  "s_barrier\n" \
  "s_add_u32 m0, s40, 12288\n" \
  "s_add_u32 s75, s55, 20480\n" \
  "buffer_load_dwordx4 v8, s[36:39], s75 offen offset:0 sc1 lds\n" \
  "buffer_load_dwordx4 v8, s[36:39], s75 offen offset:1024 sc1 lds\n" \
  "buffer_load_dwordx4 v8, s[36:39], s75 offen offset:2048 sc1 lds\n" \
  "buffer_load_dwordx4 v8, s[36:39], s75 offen offset:3072 sc1 lds\n" \
  "s_add_u32 m0, s40, 16384\n" \
  "s_add_u32 s75, s55, 32768\n" \
  "buffer_load_dwordx4 v8, s[36:39], s75 offen offset:0 sc1 lds\n" \
  "buffer_load_dwordx4 v8, s[36:39], s75 offen offset:1024 sc1 lds\n" \
  "s_xor_b32 s40, s40, 0x8000\n" \
  "v_readlane_b32 s72, v11, 0\n" \
  "s_nop 0\n" \
  "s_add_u32 s69, s72, 4096\n" \
  "s_add_u32 s70, s72, 16384\n" \
  "s_add_u32 s44, s72, s93\n" \
  "s_add_u32 s45, s72, s94\n" \
  "s_add_u32 s46, s72, s95\n" \
  "v_readlane_b32 s41, v7, 0\n" \
  "s_nop 1\n" \
  "LOOPs2_%=:\n" \
  "v_mov_b32 v60, -1\n" \
  "v_mov_b32 v61, -1\n" \
  "v_mov_b32 v64, 0\n" \
  "v_mov_b32 v65, 0\n" \
  "v_mov_b32 v66, 0\n" \
  "v_mov_b32 v67, 0\n" \
  "v_mov_b32 v68, 0\n" \
  "v_mov_b32 v69, 0\n" \
  "v_mov_b32 v70, 0\n" \
  "v_mov_b32 v71, 0\n" \
  "v_mov_b32 v72, 0\n" \
  "v_mov_b32 v73, 0\n" \
  "v_mov_b32 v74, 0\n" \
  "v_mov_b32 v75, 0\n" \
  "v_mov_b32 v76, 0\n" \
  "v_mov_b32 v77, 0\n" \
  "v_mov_b32 v78, 0\n" \
  "v_mov_b32 v79, 0\n" \
  "v_mov_b32 v80, 0\n" \
  "v_mov_b32 v81, 0\n" \
  "v_mov_b32 v82, 0\n" \
  "v_mov_b32 v83, 0\n" \
  "v_mov_b32 v84, 0\n" \
  "v_mov_b32 v85, 0\n" \
  "v_mov_b32 v86, 0\n" \
  "v_mov_b32 v87, 0\n" \
  "s_barrier\n" \
  "buffer_load_dwordx4 v[24:27], v1, s[36:39], s72 offen offset:0 sc1\n" \
  "s_mov_b32 m0, 67584\n" \
  "s_nop 0\n" \
  "buffer_load_dwordx4 v1, s[36:39], s69 offen sc1 lds\n" \
  "buffer_load_dwordx4 v[28:31], v1, s[36:39], s72 offen offset:1024 sc1\n" \
  "s_mov_b32 m0, 71680\n" \
  "s_nop 0\n" \
  "buffer_load_dwordx4 v1, s[36:39], s70 offen sc1 lds\n" \
  "ds_read_b128 v[88:91], v3 offset:0\n" \
  "ds_read_b128 v[92:95], v3 offset:1024\n" \
  "ds_read_b128 v[96:99], v3 offset:2048\n" \
  "ds_read_b128 v[100:103], v3 offset:3072\n" \
  "ds_read_b128 v[104:107], v3 offset:4096\n" \
  "ds_read_b128 v[108:111], v3 offset:5120\n" \
  "ds_read_b128 v[112:115], v3 offset:6144\n" \
  "ds_read_b128 v[116:119], v3 offset:7168\n" \
  "ds_write_b64 v6, v[60:61] offset:34816\n" \
  "ds_write_b64 v6, v[60:61] offset:35328\n" \
  "ds_write_b64 v6, v[60:61] offset:37888\n" \
  "ds_write_b64 v6, v[60:61] offset:2048\n" \
  "ds_write_b64 v6, v[60:61] offset:2560\n" \
  "ds_write_b64 v6, v[60:61] offset:5120\n" \
  "ds_write_b32 v12, v60 offset:256\n" \
  "s_add_u32 s68, s68, 1\n" \
  "LMs2_%=:\n" \
  "buffer_store_dwordx4 v[52:55], v16, s[36:39], s42 offen sc1\n" \
  "buffer_store_dword v56, v17, s[36:39], s42 offen sc1\n" \
  "s_mov_b32 s42, 0x40000000\n" \
  "s_waitcnt vmcnt(5)\n" \
  "s_waitcnt lgkmcnt(13)\n" \
  "v_smfmac_f32_16x16x64_bf16 v[64:67], v[24:27], v[88:95], v0\n" \
  "ds_read_b128 v[88:91], v3 offset:8192\n" \
  "s_waitcnt lgkmcnt(12)\n" \
  "v_smfmac_f32_16x16x64_bf16 v[68:71], v[24:27], v[96:103], v0\n" \
  "ds_read_b128 v[92:95], v3 offset:9216\n" \
  "s_waitcnt lgkmcnt(11)\n" \
  "v_smfmac_f32_16x16x64_bf16 v[72:75], v[24:27], v[104:111], v0\n" \
  "ds_read_b128 v[96:99], v3 offset:10240\n" \
  "s_waitcnt lgkmcnt(10)\n" \
  "v_smfmac_f32_16x16x64_bf16 v[76:79], v[24:27], v[112:119], v0\n" \
  "ds_read_b128 v[100:103], v3 offset:11264\n" \
  "v_smfmac_f32_16x16x64_bf16 v[80:83], v[24:27], a[0:7], v0\n" \
  "ds_read_b128 v[104:107], v3 offset:12288\n" \
  "v_smfmac_f32_16x16x64_bf16 v[84:87], v[24:27], a[8:15], v0\n" \
  "s_waitcnt vmcnt(4)\n" \
  "s_waitcnt lgkmcnt(5)\n" \
  "s_barrier\n" \
  "ds_read_b128 v[32:35], v2 offset:2048\n" \
  "s_waitcnt vmcnt(3)\n" \
  "v_smfmac_f32_16x16x64_bf16 v[64:67], v[28:31], a[16:23], v0\n" \
  "ds_read_b128 v[36:39], v2 offset:3072\n" \
  "v_smfmac_f32_16x16x64_bf16 v[68:71], v[28:31], a[24:31], v0\n" \
  "ds_read_b128 v[40:43], v2 offset:4096\n" \
  "s_waitcnt lgkmcnt(6)\n" \
  "v_smfmac_f32_16x16x64_bf16 v[72:75], v[28:31], v[88:95], v0\n" \
  "ds_read_b128 v[44:47], v2 offset:5120\n" \
  "v_smfmac_f32_16x16x64_bf16 v[76:79], v[28:31], a[32:39], v0\n" \
  "ds_read_b128 v[108:111], v3 offset:13312\n" \
  "v_smfmac_f32_16x16x64_bf16 v[80:83], v[28:31], a[40:47], v0\n" \
  "ds_read_b128 v[112:115], v3 offset:14336\n" \
  "v_smfmac_f32_16x16x64_bf16 v[84:87], v[28:31], a[48:55], v0\n" \
  "ds_read_b128 v[116:119], v3 offset:15360\n" \
  "s_waitcnt lgkmcnt(6)\n" \
  "v_smfmac_f32_16x16x64_bf16 v[64:67], v[32:35], a[56:63], v0\n" \
  "ds_read_b128 v[88:91], v3 offset:16384\n" \
  "v_smfmac_f32_16x16x64_bf16 v[68:71], v[32:35], v[96:103], v0\n" \
  "ds_read_b128 v[92:95], v3 offset:17408\n" \
  "v_smfmac_f32_16x16x64_bf16 v[72:75], v[32:35], a[64:71], v0\n" \
  "ds_read_b128 v[96:99], v3 offset:18432\n" \
  "s_waitcnt lgkmcnt(8)\n" \
  "v_smfmac_f32_16x16x64_bf16 v[64:67], v[36:39], a[72:79], v0\n" \
  "s_waitcnt vmcnt(2)\n" \
  "s_barrier\n" \
  "ds_read_b128 v[100:103], v3 offset:19456\n" \
  "v_smfmac_f32_16x16x64_bf16 v[68:71], v[36:39], a[80:87], v0\n" \
  "ds_read_b128 v[48:51], v2 offset:6144\n" \
  "v_smfmac_f32_16x16x64_bf16 v[72:75], v[36:39], a[88:95], v0\n" \
  "ds_read_b128 v[52:55], v2 offset:7168\n" \
  "s_waitcnt lgkmcnt(8)\n" \
  "v_smfmac_f32_16x16x64_bf16 v[64:67], v[40:43], v[104:111], v0\n" \
  "ds_read_b128 v[56:59], v2 offset:8192\n" \
  "v_smfmac_f32_16x16x64_bf16 v[68:71], v[40:43], a[96:103], v0\n" \
  "ds_read_b128 v[60:63], v2 offset:9216\n" \
  "s_waitcnt lgkmcnt(8)\n" \
  "v_smfmac_f32_16x16x64_bf16 v[72:75], v[40:43], v[112:119], v0\n" \
  "ds_read_b128 v[104:107], v3 offset:20480\n" \
  "v_smfmac_f32_16x16x64_bf16 v[64:67], v[44:47], a[104:111], v0\n" \
  "ds_read_b128 v[108:111], v3 offset:21504\n" \
  "s_waitcnt lgkmcnt(8)\n" \
  "v_smfmac_f32_16x16x64_bf16 v[68:71], v[44:47], v[88:95], v0\n" \
  "ds_read_b128 v[88:91], v20 offset:0\n" \
  "v_smfmac_f32_16x16x64_bf16 v[72:75], v[44:47], a[112:119], v0\n" \
  "ds_read_b128 v[92:95], v20 offset:2048\n" \
  "s_waitcnt lgkmcnt(7)\n" \
  "v_smfmac_f32_16x16x64_bf16 v[64:67], v[48:51], a[120:127], v0\n" \
  "s_waitcnt lgkmcnt(6)\n" \
  "v_smfmac_f32_16x16x64_bf16 v[64:67], v[52:55], a[128:135], v0\n" \
  "s_waitcnt lgkmcnt(5)\n" \
  "v_smfmac_f32_16x16x64_bf16 v[64:67], v[56:59], a[136:143], v0\n" \
  "s_waitcnt lgkmcnt(4)\n" \
  "v_smfmac_f32_16x16x64_bf16 v[64:67], v[60:63], a[144:151], v0\n" \
  "v_smfmac_f32_16x16x64_bf16 v[68:71], v[48:51], v[96:103], v0\n" \
  "ds_read_b128 v[96:99], v20 offset:4096\n" \
  "v_smfmac_f32_16x16x64_bf16 v[68:71], v[52:55], a[152:159], v0\n" \
  "ds_read_b128 v[100:103], v20 offset:6144\n" \
  "s_waitcnt lgkmcnt(4)\n" \
  "v_cmp_u_f32 vcc, v64, v65\n" \
  "v_cmp_u_f32 s[82:83], v66, v67\n" \
  "v_smfmac_f32_16x16x64_bf16 v[68:71], v[56:59], v[104:111], v0\n" \
  "s_or_b64 vcc, vcc, s[82:83]\n" \
  "s_cbranch_scc1 LMQs2_%=\n" \
  "ds_read_b64 v[104:105], v21 offset:0\n" \
  "v_cvt_pk_bf16_f32 v64, v64, v65\n" \
  "v_smfmac_f32_16x16x64_bf16 v[68:71], v[60:63], a[160:167], v0\n" \
  "ds_read_b64 v[106:107], v21 offset:1024\n" \
  "v_cvt_pk_bf16_f32 v65, v66, v67\n" \
  "v_smfmac_f32_16x16x64_bf16 v[72:75], v[48:51], a[168:175], v0\n" \
  "ds_write_b64 v6, v[64:65] offset:34816\n" \
  "v_smfmac_f32_16x16x64_bf16 v[72:75], v[52:55], a[176:183], v0\n" \
  "ds_read_b64 v[108:109], v21 offset:2048\n" \
  "v_cvt_pk_bf16_f32 v68, v68, v69\n" \
  "v_smfmac_f32_16x16x64_bf16 v[72:75], v[56:59], a[184:191], v0\n" \
  "ds_read_b64 v[110:111], v21 offset:3072\n" \
  "v_cvt_pk_bf16_f32 v69, v70, v71\n" \
  "v_smfmac_f32_16x16x64_bf16 v[72:75], v[60:63], a[192:199], v0\n" \
  "ds_write_b64 v6, v[68:69] offset:35328\n" \
  "v_smfmac_f32_16x16x64_bf16 v[76:79], v[32:35], a[200:207], v0\n" \
  "ds_read_b64_tr_b16 v[24:25], v9 offset:32768\n" \
  "v_smfmac_f32_16x16x64_bf16 v[76:79], v[36:39], a[208:215], v0\n" \
  "ds_read_b64_tr_b16 v[28:29], v9 offset:33792\n" \
  "v_cvt_pk_bf16_f32 v72, v72, v73\n" \
  "v_smfmac_f32_16x16x64_bf16 v[76:79], v[40:43], a[216:223], v0\n" \
  "ds_read_b64_tr_b16 v[112:113], v9 offset:34816\n" \
  "v_cvt_pk_bf16_f32 v73, v74, v75\n" \
  "v_smfmac_f32_16x16x64_bf16 v[76:79], v[44:47], a[224:231], v0\n" \
  "ds_write_b64 v6, v[72:73] offset:37888\n" \
  "v_smfmac_f32_16x16x64_bf16 v[76:79], v[48:51], a[232:239], v0\n" \
  "ds_read_b64_tr_b16 v[64:65], v9 offset:35840\n" \
  "v_smfmac_f32_16x16x64_bf16 v[76:79], v[52:55], a[240:247], v0\n" \
  "s_waitcnt lgkmcnt(8)\n" \
  "ds_read_b64_tr_b16 v[26:27], v9 offset:33280\n" \
  "v_smfmac_f32_16x16x64_bf16 v[76:79], v[56:59], a[248:255], v0\n" \
  "ds_read_b64_tr_b16 v[30:31], v9 offset:34304\n" \
  "v_smfmac_f32_16x16x64_bf16 v[76:79], v[60:63], v[120:127], v0\n" \
  "ds_read_b64_tr_b16 v[114:115], v9 offset:35328\n" \
  "v_smfmac_f32_16x16x64_bf16 v[80:83], v[32:35], v[128:135], v0\n" \
  "ds_read_b64_tr_b16 v[66:67], v9 offset:36352\n" \
  "v_smfmac_f32_16x16x64_bf16 v[80:83], v[36:39], v[136:143], v0\n" \
  "ds_read_b64_tr_b16 v[68:69], v9 offset:36864\n" \
  "v_cvt_pk_bf16_f32 v76, v76, v77\n" \
  "v_smfmac_f32_16x16x64_bf16 v[80:83], v[40:43], v[144:151], v0\n" \
  "ds_read_b64_tr_b16 v[70:71], v9 offset:37376\n" \
  "v_cvt_pk_bf16_f32 v77, v78, v79\n" \
  "v_smfmac_f32_16x16x64_bf16 v[80:83], v[44:47], v[152:159], v0\n" \
  "ds_write_b64 v6, v[76:77] offset:2048\n" \
  "v_smfmac_f32_16x16x64_bf16 v[80:83], v[48:51], v[160:167], v0\n" \
  "s_waitcnt lgkmcnt(8)\n" \
  "ds_read_b64_tr_b16 v[72:73], v9 offset:37888\n" \
  "v_smfmac_f32_16x16x64_bf16 v[80:83], v[52:55], v[168:175], v0\n" \
  "ds_read_b64_tr_b16 v[74:75], v9 offset:38400\n" \
  "v_smfmac_f32_16x16x64_bf16 v[80:83], v[56:59], v[176:183], v0\n" \
  "v_smfmac_f32_16x16x64_bf16 v[80:83], v[60:63], v[184:191], v0\n" \
  "s_waitcnt lgkmcnt(8)\n" \
  "v_mfma_f32_16x16x32_bf16 v[116:119], v[88:91], v[24:27], 0\n" \
  "s_waitcnt lgkmcnt(7)\n" \
  "v_mfma_f32_16x16x32_bf16 v[116:119], v[92:95], v[28:31], v[116:119]\n" \
  "s_waitcnt lgkmcnt(6)\n" \
  "v_cvt_pk_bf16_f32 v80, v80, v81\n" \
  "v_mfma_f32_16x16x32_bf16 v[116:119], v[96:99], v[112:115], v[116:119]\n" \
  "s_waitcnt lgkmcnt(5)\n" \
  "v_cvt_pk_bf16_f32 v81, v82, v83\n" \
  "v_mfma_f32_16x16x32_bf16 v[116:119], v[100:103], v[64:67], v[116:119]\n" \
  "ds_write_b64 v6, v[80:81] offset:2560\n" \
  "s_waitcnt lgkmcnt(4)\n" \
  "v_mfma_f32_16x16x32_bf16 v[116:119], v[104:107], v[68:71], v[116:119]\n" \
  "s_waitcnt lgkmcnt(1)\n" \
  "v_mfma_f32_16x16x32_bf16 v[116:119], v[108:111], v[72:75], v[116:119]\n" \
  "v_smfmac_f32_16x16x64_bf16 v[84:87], v[32:35], v[192:199], v0\n" \
  "v_smfmac_f32_16x16x64_bf16 v[84:87], v[36:39], v[200:207], v0\n" \
  "v_add_f32_dpp v88, v116, v116 row_ror:8 row_mask:0xf bank_mask:0x3\n" \
  "v_add_f32_dpp v89, v117, v117 row_ror:8 row_mask:0xf bank_mask:0x3\n" \
  "v_smfmac_f32_16x16x64_bf16 v[84:87], v[40:43], v[208:215], v0\n" \
  "v_add_f32_dpp v88, v118, v118 row_ror:8 row_mask:0xf bank_mask:0xc\n" \
  "v_add_f32_dpp v89, v119, v119 row_ror:8 row_mask:0xf bank_mask:0xc\n" \
  "v_smfmac_f32_16x16x64_bf16 v[84:87], v[44:47], v[216:223], v0\n" \
  "v_mul_f32 v88, v4, v88\n" \
  "v_mul_f32 v89, v4, v89\n" \
  "v_smfmac_f32_16x16x64_bf16 v[84:87], v[48:51], v[224:231], v0\n" \
  "v_exp_f32 v88, v88\n" \
  "v_smfmac_f32_16x16x64_bf16 v[84:87], v[52:55], v[232:239], v0\n" \
  "v_exp_f32 v89, v89\n" \
  "v_smfmac_f32_16x16x64_bf16 v[84:87], v[56:59], v[240:247], v0\n" \
  "v_cmp_o_f32 vcc, v88, v89\n" \
  "v_cvt_pk_bf16_f32 v91, v88, v89\n" \
  "v_smfmac_f32_16x16x64_bf16 v[84:87], v[60:63], v[248:255], v0\n" \
  "s_mov_b64 exec, vcc\n" \
  "v_add_f32 v90, v88, v89\n" \
  "buffer_store_dword v91, v22, s[36:39], s72 offen offset:0 sc1\n" \
  "buffer_store_dword v91, v22, s[36:39], s44 offen offset:0 sc1\n" \
  "buffer_store_dword v91, v22, s[36:39], s45 offen offset:0 sc1\n" \
  "buffer_store_dword v91, v22, s[36:39], s46 offen offset:0 sc1\n" \
  "ds_write_b32 v12, v90 offset:256\n" \
  "s_mov_b64 exec, -1\n" \
  "v_cvt_pk_bf16_f32 v84, v84, v85\n" \
  "v_cvt_pk_bf16_f32 v85, v86, v87\n" \
  "ds_write_b64 v6, v[84:85] offset:5120\n" \
  "s_barrier ; timing only\n" \
  "v_mov_b32 v61, 0xf149f2ca\n" \
  "v_cmp_gt_i32 vcc, 0, v13\n" \
  "s_nop 1\n" \
  "v_cndmask_b32 v116, 0, v61, vcc\n" \
  "v_cmp_gt_i32 vcc, 1, v13\n" \
  "s_nop 1\n" \
  "v_cndmask_b32 v117, 0, v61, vcc\n" \
  "v_cmp_gt_i32 vcc, 2, v13\n" \
  "s_nop 1\n" \
  "v_cndmask_b32 v118, 0, v61, vcc\n" \
  "v_cmp_gt_i32 vcc, 3, v13\n" \
  "s_nop 1\n" \
  "v_cndmask_b32 v119, 0, v61, vcc\n" \
  "ds_read_b128 v[24:27], v18 offset:9216\n" \
  "ds_read_b128 v[64:67], v18 offset:0\n" \
  "ds_read_b128 v[88:91], v18 offset:1024\n" \
  "ds_read_b64_tr_b16 v[48:49], v9 offset:0\n" \
  "ds_read_b64_tr_b16 v[50:51], v9 offset:512\n" \
  "ds_read_b128 v[28:31], v18 offset:11264\n" \
  "ds_read_b128 v[68:71], v18 offset:2048\n" \
  "ds_read_b128 v[92:95], v18 offset:3072\n" \
  "ds_read_b64_tr_b16 v[52:53], v9 offset:1024\n" \
  "ds_read_b64_tr_b16 v[54:55], v9 offset:1536\n" \
  "ds_read_b128 v[32:35], v18 offset:13312\n" \
  "ds_read_b128 v[72:75], v18 offset:4096\n" \
  "s_waitcnt lgkmcnt(8)\n" \
  "ds_read_b128 v[96:99], v18 offset:5120\n" \
  "ds_read_b64_tr_b16 v[56:57], v9 offset:2048\n" \
  "ds_read_b64_tr_b16 v[58:59], v9 offset:2560\n" \
  "v_mfma_f32_16x16x32_bf16 v[112:115], v[64:67], v[24:27], 0\n" \
  "v_mfma_f32_16x16x32_bf16 v[116:119], v[88:91], v[24:27], v[116:119]\n" \
  "s_waitcnt lgkmcnt(10)\n" \
  "v_mfma_f32_16x16x32_bf16 v[108:111], v[48:51], v[24:27], 0\n" \
  "ds_read_b128 v[36:39], v18 offset:15360\n" \
  "ds_read_b128 v[76:79], v18 offset:6144\n" \
  "ds_read_b128 v[100:103], v18 offset:7168\n" \
  "ds_read_b64_tr_b16 v[48:49], v9 offset:3072\n" \
  "ds_read_b64_tr_b16 v[50:51], v9 offset:3584\n" \
  "s_waitcnt lgkmcnt(13)\n" \
  "v_mfma_f32_16x16x32_bf16 v[112:115], v[68:71], v[28:31], v[112:115]\n" \
  "s_waitcnt lgkmcnt(12)\n" \
  "v_mfma_f32_16x16x32_bf16 v[116:119], v[92:95], v[28:31], v[116:119]\n" \
  "s_waitcnt lgkmcnt(10)\n" \
  "v_mfma_f32_16x16x32_bf16 v[108:111], v[52:55], v[28:31], v[108:111]\n" \
  "ds_read_b64 v[40:41], v18 offset:20488\n" \
  "ds_read_b64 v[42:43], v18 offset:21512\n" \
  "ds_read_b64 v[80:81], v18 offset:16384\n" \
  "ds_read_b64 v[82:83], v18 offset:17408\n" \
  "ds_read_b64 v[104:105], v18 offset:16392\n" \
  "s_waitcnt lgkmcnt(8)\n" \
  "ds_read_b64 v[106:107], v18 offset:17416\n" \
  "ds_read_b64_tr_b16 v[52:53], v9 offset:4096\n" \
  "ds_read_b64_tr_b16 v[54:55], v9 offset:4608\n" \
  "v_mfma_f32_16x16x32_bf16 v[112:115], v[72:75], v[32:35], v[112:115]\n" \
  "v_mfma_f32_16x16x32_bf16 v[116:119], v[96:99], v[32:35], v[116:119]\n" \
  "v_mfma_f32_16x16x32_bf16 v[108:111], v[56:59], v[32:35], v[108:111]\n" \
  "ds_read_b64 v[44:45], v18 offset:22536\n" \
  "ds_read_b64 v[46:47], v18 offset:23560\n" \
  "ds_read_b64 v[84:85], v18 offset:18432\n" \
  "ds_read_b64 v[86:87], v18 offset:19456\n" \
  "s_waitcnt lgkmcnt(8)\n" \
  "ds_read_b64 v[88:89], v18 offset:18440\n" \
  "ds_read_b64 v[90:91], v18 offset:19464\n" \
  "ds_read_b64_tr_b16 v[56:57], v9 offset:5120\n" \
  "ds_read_b64_tr_b16 v[58:59], v9 offset:5632\n" \
  "s_barrier\n" \
  "ds_read_b32 v60, v12 offset:512\n" \
  "v_mfma_f32_16x16x32_bf16 v[112:115], v[76:79], v[36:39], v[112:115]\n" \
  "v_mfma_f32_16x16x32_bf16 v[116:119], v[100:103], v[36:39], v[116:119]\n" \
  "v_mfma_f32_16x16x32_bf16 v[108:111], v[48:51], v[36:39], v[108:111]\n" \
  "v_mfma_f32_16x16x32_bf16 v[112:115], v[80:83], v[40:43], v[112:115]\n" \
  "s_waitcnt lgkmcnt(11)\n" \
  "v_mfma_f32_16x16x32_bf16 v[116:119], v[104:107], v[40:43], v[116:119]\n" \
  "s_waitcnt lgkmcnt(9)\n" \
  "v_mfma_f32_16x16x32_bf16 v[108:111], v[52:55], v[40:43], v[108:111]\n" \
  "s_waitcnt lgkmcnt(5)\n" \
  "v_mfma_f32_16x16x32_bf16 v[112:115], v[84:87], v[44:47], v[112:115]\n" \
  "s_waitcnt lgkmcnt(3)\n" \
  "v_mfma_f32_16x16x32_bf16 v[116:119], v[88:91], v[44:47], v[116:119]\n" \
  "s_waitcnt lgkmcnt(1)\n" \
  "v_mfma_f32_16x16x32_bf16 v[108:111], v[56:59], v[44:47], v[108:111]\n" \
  "s_waitcnt lgkmcnt(0)\n" \
  "v_cmp_ne_u32 vcc, 0, v60\n" \
  "s_nop 3\n" \
  "s_cbranch_vccnz LMXs2_%=\n" \
  "LMYs2_%=:\n" \
  "s_nop 0\n" \
  "v_cmp_u_f32 vcc, v108, v109\n" \
  "v_cmp_u_f32 s[82:83], v110, v111\n" \
  "s_nop 1\n" \
  "s_or_b64 vcc, vcc, s[82:83]\n" \
  "s_cbranch_scc1 LAR2_%=\n" \
  "LAK2_%=:\n" \
  "v_permlane32_swap_b32 v108, v110\n" \
  "v_permlane32_swap_b32 v109, v111\n" \
  "v_pk_add_f32 v[110:111], v[108:109], v[110:111]\n" \
  "v_mov_b32 v32, 0x3d93cd3a\n" \
  "v_mov_b32 v34, 0x3fb8aa3b\n" \
  "v_cvt_pk_bf16_f32 v24, v110, v111\n" \
  "v_cvt_pk_bf16_f32 v25, v112, v113\n" \
  "v_cvt_pk_bf16_f32 v26, v114, v115\n" \
  "v_cvt_pk_bf16_f32 v27, v116, v117\n" \
  "v_cvt_pk_bf16_f32 v28, v118, v119\n" \
  "v_lshlrev_b32 v110, 16, v24\n" \
  "v_and_b32 v111, 0xffff0000, v24\n" \
  "v_pk_mul_f32 v[110:111], v[110:111], v[32:33] op_sel_hi:[1,0]\n" \
  "v_cvt_pk_bf16_f32 v24, v110, v111\n" \
  "v_lshlrev_b32 v110, 16, v24\n" \
  "v_and_b32 v111, 0xffff0000, v24\n" \
  "v_lshlrev_b32 v112, 16, v25\n" \
  "v_and_b32 v113, 0xffff0000, v25\n" \
  "v_pk_mul_f32 v[112:113], v[112:113], v[32:33] op_sel_hi:[1,0]\n" \
  "v_cvt_pk_bf16_f32 v25, v112, v113\n" \
  "v_lshlrev_b32 v112, 16, v25\n" \
  "v_and_b32 v113, 0xffff0000, v25\n" \
  "v_lshlrev_b32 v114, 16, v26\n" \
  "v_and_b32 v115, 0xffff0000, v26\n" \
  "v_pk_mul_f32 v[114:115], v[114:115], v[32:33] op_sel_hi:[1,0]\n" \
  "v_cvt_pk_bf16_f32 v26, v114, v115\n" \
  "v_lshlrev_b32 v114, 16, v26\n" \
  "v_and_b32 v115, 0xffff0000, v26\n" \
  "v_lshlrev_b32 v116, 16, v27\n" \
  "v_and_b32 v117, 0xffff0000, v27\n" \
  "v_pk_mul_f32 v[116:117], v[116:117], v[32:33] op_sel_hi:[1,0]\n" \
  "v_cvt_pk_bf16_f32 v27, v116, v117\n" \
  "v_lshlrev_b32 v116, 16, v27\n" \
  "v_and_b32 v117, 0xffff0000, v27\n" \
  "v_lshlrev_b32 v118, 16, v28\n" \
  "v_and_b32 v119, 0xffff0000, v28\n" \
  "v_pk_mul_f32 v[118:119], v[118:119], v[32:33] op_sel_hi:[1,0]\n" \
  "v_cvt_pk_bf16_f32 v28, v118, v119\n" \
  "v_lshlrev_b32 v118, 16, v28\n" \
  "v_and_b32 v119, 0xffff0000, v28\n" \
  "v_max_f32 v24, v110, v111\n" \
  "v_max3_f32 v24, v24, v112, v113\n" \
  "v_max3_f32 v24, v24, v114, v115\n" \
  "v_max3_f32 v24, v24, v116, v117\n" \
  "v_max3_f32 v24, v24, v118, v119\n" \
  "v_mov_b32 v25, v24\n" \
  "s_nop 1\n" \
  "v_permlane16_swap_b32 v24, v25\n" \
  "v_max_f32 v24, v24, v25\n" \
  "v_mov_b32 v25, v24\n" \
  "s_nop 1\n" \
  "v_permlane32_swap_b32 v24, v25\n" \
  "v_max_f32 v24, v24, v25\n" \
  "v_mul_f32 v24, v24, v34\n" \
  "v_pk_fma_f32 v[110:111], v[110:111], v[34:35], v[24:25] op_sel_hi:[1,0,0] neg_lo:[0,0,1] neg_hi:[0,0,1]\n" \
  "v_pk_fma_f32 v[112:113], v[112:113], v[34:35], v[24:25] op_sel_hi:[1,0,0] neg_lo:[0,0,1] neg_hi:[0,0,1]\n" \
  "v_pk_fma_f32 v[114:115], v[114:115], v[34:35], v[24:25] op_sel_hi:[1,0,0] neg_lo:[0,0,1] neg_hi:[0,0,1]\n" \
  "v_pk_fma_f32 v[116:117], v[116:117], v[34:35], v[24:25] op_sel_hi:[1,0,0] neg_lo:[0,0,1] neg_hi:[0,0,1]\n" \
  "v_pk_fma_f32 v[118:119], v[118:119], v[34:35], v[24:25] op_sel_hi:[1,0,0] neg_lo:[0,0,1] neg_hi:[0,0,1]\n" \
  "v_exp_f32 v110, v110\n" \
  "v_exp_f32 v111, v111\n" \
  "v_exp_f32 v112, v112\n" \
  "v_exp_f32 v113, v113\n" \
  "v_exp_f32 v114, v114\n" \
  "v_exp_f32 v115, v115\n" \
  "v_exp_f32 v116, v116\n" \
  "v_exp_f32 v117, v117\n" \
  "v_exp_f32 v118, v118\n" \
  "v_exp_f32 v119, v119\n" \
  "v_pk_add_f32 v[24:25], v[110:111], v[112:113]\n" \
  "v_pk_add_f32 v[24:25], v[24:25], v[114:115]\n" \
  "v_pk_add_f32 v[24:25], v[24:25], v[116:117]\n" \
  "v_pk_add_f32 v[24:25], v[24:25], v[118:119]\n" \
  "v_add_f32 v24, v24, v25\n" \
  "v_mov_b32 v25, v24\n" \
  "s_nop 1\n" \
  "v_permlane16_swap_b32 v24, v25\n" \
  "v_add_f32 v24, v24, v25\n" \
  "v_mov_b32 v25, v24\n" \
  "s_nop 1\n" \
  "v_permlane32_swap_b32 v24, v25\n" \
  "v_add_f32 v24, v24, v25\n" \
  "v_rcp_f32 v24, v24\n" \
  "s_nop 0\n" \
  "v_pk_mul_f32 v[110:111], v[110:111], v[24:25] op_sel_hi:[1,0]\n" \
  "v_pk_mul_f32 v[112:113], v[112:113], v[24:25] op_sel_hi:[1,0]\n" \
  "v_pk_mul_f32 v[114:115], v[114:115], v[24:25] op_sel_hi:[1,0]\n" \
  "v_pk_mul_f32 v[116:117], v[116:117], v[24:25] op_sel_hi:[1,0]\n" \
  "v_pk_mul_f32 v[118:119], v[118:119], v[24:25] op_sel_hi:[1,0]\n" \
  "v_cvt_pk_bf16_f32 v52, v112, v113\n" \
  "v_cvt_pk_bf16_f32 v53, v114, v115\n" \
  "v_cvt_pk_bf16_f32 v54, v116, v117\n" \
  "v_cvt_pk_bf16_f32 v55, v118, v119\n" \
  "v_cvt_pk_bf16_f32 v56, v110, v111\n" \
  "s_sub_u32 s74, s68, 1\n" \
  "v_readlane_b32 s42, v15, s74\n" \
  "v_xor_b32 v20, 0x8000, v20\n" \
  "v_xor_b32 v21, 0x8000, v21\n" \
  "v_xor_b32 v18, 0x8000, v18\n" \
  "s_cmp_eq_u32 s68, 64\n" \
  "s_cbranch_scc1 LENDs2_%=\n" \
  "v_readlane_b32 s72, v11, s68\n" \
  "s_nop 0\n" \
  "s_add_u32 s69, s72, 4096\n" \
  "s_add_u32 s70, s72, 16384\n" \
  "s_add_u32 s44, s72, s93\n" \
  "s_add_u32 s45, s72, s94\n" \
  "s_add_u32 s46, s72, s95\n" \
  "v_readlane_b32 s41, v7, s68\n" \
  "s_nop 1\n" \
  "s_branch LOOPs2_%=\n" \
  "LAR2_%=:\n" \
  "ds_read_b64_tr_b16 v[64:65], v9 offset:0\n" \
  "ds_read_b64_tr_b16 v[66:67], v9 offset:512\n" \
  "ds_read_b64_tr_b16 v[68:69], v9 offset:1024\n" \
  "ds_read_b64_tr_b16 v[70:71], v9 offset:1536\n" \
  "ds_read_b64_tr_b16 v[72:73], v9 offset:2048\n" \
  "ds_read_b64_tr_b16 v[74:75], v9 offset:2560\n" \
  "ds_read_b64_tr_b16 v[76:77], v9 offset:3072\n" \
  "ds_read_b64_tr_b16 v[78:79], v9 offset:3584\n" \
  "ds_read_b64_tr_b16 v[80:81], v9 offset:4096\n" \
  "ds_read_b64_tr_b16 v[82:83], v9 offset:4608\n" \
  "ds_read_b64_tr_b16 v[84:85], v9 offset:5120\n" \
  "ds_read_b64_tr_b16 v[86:87], v9 offset:5632\n" \
  "s_waitcnt lgkmcnt(10)\n" \
  "v_mfma_f32_16x16x32_bf16 v[108:111], v[64:67], v[24:27], 0\n" \
  "s_waitcnt lgkmcnt(8)\n" \
  "v_mfma_f32_16x16x32_bf16 v[108:111], v[68:71], v[28:31], v[108:111]\n" \
  "s_waitcnt lgkmcnt(6)\n" \
  "v_mfma_f32_16x16x32_bf16 v[108:111], v[72:75], v[32:35], v[108:111]\n" \
  "s_waitcnt lgkmcnt(4)\n" \
  "v_mfma_f32_16x16x32_bf16 v[108:111], v[76:79], v[36:39], v[108:111]\n" \
  "s_waitcnt lgkmcnt(2)\n" \
  "v_mfma_f32_16x16x32_bf16 v[108:111], v[80:83], v[40:43], v[108:111]\n" \
  "s_waitcnt lgkmcnt(0)\n" \
  "v_mfma_f32_16x16x32_bf16 v[108:111], v[84:87], v[44:47], v[108:111]\n" \
  "s_nop 7\n" \
  "s_branch LAK2_%=\n" \
  "LMFs2_%=:\n" \
  "ds_read_b128 v[88:91], v3 offset:0\n" \
  "ds_read_b128 v[92:95], v3 offset:1024\n" \
  "ds_read_b128 v[96:99], v3 offset:2048\n" \
  "ds_read_b128 v[100:103], v3 offset:3072\n" \
  "ds_read_b128 v[104:107], v3 offset:4096\n" \
  "ds_read_b128 v[108:111], v3 offset:5120\n" \
  "ds_read_b128 v[112:115], v3 offset:6144\n" \
  "ds_read_b128 v[116:119], v3 offset:7168\n" \
  "v_mov_b32 v60, -1\n" \
  "v_mov_b32 v61, -1\n" \
  "ds_write_b64 v6, v[60:61] offset:34816\n" \
  "ds_write_b64 v6, v[60:61] offset:35328\n" \
  "ds_write_b64 v6, v[60:61] offset:37888\n" \
  "v_mov_b32 v64, 0\n" \
  "v_mov_b32 v65, 0\n" \
  "v_mov_b32 v66, 0\n" \
  "v_mov_b32 v67, 0\n" \
  "v_mov_b32 v68, 0\n" \
  "v_mov_b32 v69, 0\n" \
  "v_mov_b32 v70, 0\n" \
  "v_mov_b32 v71, 0\n" \
  "v_mov_b32 v72, 0\n" \
  "v_mov_b32 v73, 0\n" \
  "v_mov_b32 v74, 0\n" \
  "v_mov_b32 v75, 0\n" \
  "v_mov_b32 v76, 0\n" \
  "v_mov_b32 v77, 0\n" \
  "v_mov_b32 v78, 0\n" \
  "v_mov_b32 v79, 0\n" \
  "v_mov_b32 v80, 0\n" \
  "v_mov_b32 v81, 0\n" \
  "v_mov_b32 v82, 0\n" \
  "v_mov_b32 v83, 0\n" \
  "v_mov_b32 v84, 0\n" \
  "v_mov_b32 v85, 0\n" \
  "v_mov_b32 v86, 0\n" \
  "v_mov_b32 v87, 0\n" \
  "s_waitcnt lgkmcnt(0)\n" \
  "s_barrier\n" \
  "buffer_load_dwordx4 v[24:27], v1, s[36:39], s72 offen offset:0 sc1\n" \
  "s_mov_b32 m0, 67584\n" \
  "s_nop 0\n" \
  "buffer_load_dwordx4 v1, s[36:39], s69 offen sc1 lds\n" \
  "buffer_load_dwordx4 v[28:31], v1, s[36:39], s72 offen offset:1024 sc1\n" \
  "s_mov_b32 m0, 71680\n" \
  "s_nop 0\n" \
  "buffer_load_dwordx4 v1, s[36:39], s70 offen sc1 lds\n" \
  "ds_write_b64 v6, v[60:61] offset:2048\n" \
  "ds_write_b64 v6, v[60:61] offset:2560\n" \
  "ds_write_b64 v6, v[60:61] offset:5120\n" \
  "ds_write_b32 v12, v60 offset:256\n" \
  "s_branch LMs2_%=\n" \
  "LMQs2_%=:\n" \
  "s_waitcnt lgkmcnt(0)\n" \
  "s_barrier\n" \
  "s_barrier\n" \
  "ds_read_b32 v60, v12 offset:512\n" \
  "s_waitcnt lgkmcnt(0)\n" \
  "LMXs2_%=:\n" \
  "v_cmp_eq_u32 vcc, 2, v60\n" \
  "s_waitcnt lgkmcnt(0)\n" \
  "s_nop 2\n" \
  "s_cbranch_vccnz LMVs2_%=\n" \
  "s_waitcnt vmcnt(0)\n" \
  "LMVs2_%=:\n" \
  "s_barrier\n" \
  "s_cbranch_vccnz LMFs2_%=\n" \
  "s_branch LMYs2_%=\n" \
  "LENDs2_%=:\n" \
  "s_cmp_lg_u32 s38, 0\n" \
  "s_cbranch_scc1 LFINs2_%=\n" \
  "s_waitcnt vmcnt(0) lgkmcnt(0)\n" \
  "s_mov_b32 s38, s47\n" \
  "s_mov_b32 s68, 0\n" \
  "s_mov_b32 s73, 0\n" \
  "s_mov_b32 s56, 0\n" \
  "s_mov_b32 s57, 0\n" \
  "s_branch LPASSs2_%=\n" \
  "LFINs2_%=:\n" \
  "s_nop 1\n" \
  "buffer_store_dwordx4 v[52:55], v16, s[36:39], s42 offen sc1\n" \
  "buffer_store_dword v56, v17, s[36:39], s42 offen sc1\n" \
  "s_waitcnt vmcnt(0)\n" \
  "v_mov_b32 v19, 1\n" \
  "buffer_atomic_add v19, v14, s[36:39], 0 offen sc1\n" \
  "s_branch LDONE_%=\n" \
  "LSTARTs3_%=:\n" \
  "v_mov_b32 v17, v29\n" \
  "s_mov_b32 s47, s38\n" \
  "s_mov_b32 s38, 0\n" \
  "s_mov_b32 s68, 63\n" \
  "s_mov_b32 s73, 20001\n" \
  "s_mov_b32 s56, 19998\n" \
  "s_getpc_b64 s[82:83]\n" \
  "LPCAs3_%=:\n" \
  "s_add_u32 s84, s82, LEAs3_%=-LPCAs3_%=+24\n" \
  "s_addc_u32 s85, s83, 0\n" \
  "LPASSs3_%=:\n" \
  "s_mov_b32 s40, 0\n" \
  "s_mov_b32 s42, 0x40000000\n" \
  "s_barrier\n" \
  "s_add_u32 m0, s40, 16384\n" \
  "s_add_u32 s75, s55, 32768\n" \
  "buffer_load_dwordx4 v8, s[36:39], s75 offen offset:2048 sc1 lds\n" \
  "buffer_load_dwordx4 v8, s[36:39], s75 offen offset:3072 sc1 lds\n" \
  "s_add_u32 m0, s40, 20480\n" \
  "s_add_u32 s75, s55, 36864\n" \
  "buffer_load_dwordx4 v8, s[36:39], s75 offen offset:0 sc1 lds\n" \
  "buffer_load_dwordx4 v8, s[36:39], s75 offen offset:1024 sc1 lds\n" \
  "buffer_load_dwordx4 v8, s[36:39], s75 offen offset:2048 sc1 lds\n" \
  "buffer_load_dwordx4 v8, s[36:39], s75 offen offset:3072 sc1 lds\n" \
  "s_xor_b32 s40, s40, 0x8000\n" \
  "s_memrealtime s[76:77]\n" \
  "s_memtime s[78:79]\n" \
  "s_waitcnt lgkmcnt(0)\n" \
  "s_lshl_b32 s75, s76, 2\n" \
  "s_sub_i32 s75, s48, s75\n" \
  "s_mul_i32 s75, s75, s50\n" \
  "s_ashr_i32 s75, s75, 10\n" \
  "s_sub_i32 s74, s49, s78\n" \
  "s_min_i32 s74, s74, s75\n" \
  "s_sub_i32 s75, s75, 92\n" \
  "s_max_i32 s74, s74, s75\n" \
  "s_add_u32 s49, s74, s78\n" \
  "s_sub_u32 s74, s78, s89\n" \
  "s_sub_u32 s75, s76, s88\n" \
  "v_cvt_f32_u32 v24, s74\n" \
  "v_cvt_f32_u32 v25, s75\n" \
  "v_rcp_f32 v25, v25\n" \
  "v_mul_f32 v24, 0x43820000, v24\n" \
  "v_mul_f32 v24, v24, v25\n" \
  "v_cvt_u32_f32 v24, v24\n" \
  "s_nop 0\n" \
  "v_readfirstlane_b32 s74, v24\n" \
  "s_min_u32 s74, s74, 6400\n" \
  "s_cmp_gt_u32 s75, 150\n" \
  "s_cselect_b32 s50, s74, s50\n" \
  "s_cselect_b32 s88, s76, s88\n" \
  "s_cselect_b32 s89, s78, s89\n" \
  "v_mov_b32 v24, 0\n" \
  "v_mov_b32 v25, 0\n" \
  "v_mov_b32 v26, 0\n" \
  "v_mov_b32 v27, s50\n" \
  "buffer_store_dwordx4 v[24:27], v16, s[36:39], 0 offen sc1\n" \
  "LQAs3_%=:\n" \
  "buffer_load_dword v64, v17, s[36:39], 0 offen sc1\n" \
  "s_waitcnt vmcnt(0)\n" \
  "v_cmp_eq_u32 vcc, 0, v64\n" \
  "s_nop 3\n" \
  "s_cbranch_vccz LRAs3_%=\n" \
  "s_add_u32 s73, s73, 1\n" \
  "s_cmp_gt_u32 s73, 20000\n" \
  "s_cbranch_scc1 LRAs3_%=\n" \
  "s_sleep 2\n" \
  "s_branch LQAs3_%=\n" \
  "LRAs3_%=:\n" \
  "s_memrealtime s[76:77]\n" \
  "s_memtime s[78:79]\n" \
  "s_waitcnt lgkmcnt(0)\n" \
  "s_lshl_b32 s48, s76, 2\n" \
  "v_readlane_b32 s90, v64, 0\n" \
  "s_nop 1\n" \
  "s_sub_u32 s74, s90, s48\n" \
  "s_add_u32 s74, s74, 40000\n" \
  "s_cmp_gt_u32 s74, 80000\n" \
  "s_cselect_b32 s90, s48, s90\n" \
  "s_sub_u32 s74, s78, s89\n" \
  "s_sub_u32 s75, s76, s88\n" \
  "v_cvt_f32_u32 v24, s74\n" \
  "v_cvt_f32_u32 v25, s75\n" \
  "v_rcp_f32 v25, v25\n" \
  "v_mul_f32 v24, 0x43820000, v24\n" \
  "v_mul_f32 v24, v24, v25\n" \
  "v_cvt_u32_f32 v24, v24\n" \
  "s_nop 0\n" \
  "v_readfirstlane_b32 s74, v24\n" \
  "s_min_u32 s74, s74, 6400\n" \
  "s_cmp_gt_u32 s75, 150\n" \
  "s_cselect_b32 s50, s74, s50\n" \
  "s_cselect_b32 s88, s76, s88\n" \
  "s_cselect_b32 s89, s78, s89\n" \
  "s_add_u32 s49, s78, 0x1000000\n" \
  "s_mul_i32 s86, s54, 6\n" \
  "s_mov_b32 s87, -200\n" \
  "s_mov_b32 s51, 0\n" \
  "s_mov_b32 s71, 0\n" \
  "s_mul_i32 s74, s51, s54\n" \
  "s_add_u32 s74, s74, s90\n" \
  "s_sub_i32 s75, s74, s48\n" \
  "s_mov_b32 s48, s74\n" \
  "s_mul_i32 s75, s75, s50\n" \
  "s_ashr_i32 s75, s75, 10\n" \
  "s_add_u32 s49, s49, s75\n" \
  "v_readlane_b32 s72, v11, 0\n" \
  "s_nop 0\n" \
  "s_add_u32 s69, s72, 5120\n" \
  "s_add_u32 s70, s72, 17408\n" \
  "s_add_u32 s44, s72, s93\n" \
  "s_add_u32 s45, s72, s94\n" \
  "s_add_u32 s46, s72, s95\n" \
  "v_readlane_b32 s41, v7, 0\n" \
  "s_nop 1\n" \
  "ds_read_b128 v[88:91], v3 offset:0\n" \
  "ds_read_b128 v[92:95], v3 offset:1024\n" \
  "ds_read_b128 v[96:99], v3 offset:2048\n" \
  "ds_read_b128 v[100:103], v3 offset:3072\n" \
  "ds_read_b128 v[104:107], v3 offset:4096\n" \
  "ds_read_b128 v[108:111], v3 offset:5120\n" \
  "ds_read_b128 v[112:115], v3 offset:6144\n" \
  "ds_read_b128 v[116:119], v3 offset:7168\n" \
  "v_mov_b32 v60, -1\n" \
  "v_mov_b32 v61, -1\n" \
  "ds_write_b64 v6, v[60:61] offset:35840\n" \
  "ds_write_b64 v6, v[60:61] offset:36352\n" \
  "ds_write_b64 v6, v[60:61] offset:38400\n" \
  "v_mov_b32 v64, 0\n" \
  "v_mov_b32 v65, 0\n" \
  "v_mov_b32 v66, 0\n" \
  "v_mov_b32 v67, 0\n" \
  "v_mov_b32 v68, 0\n" \
  "v_mov_b32 v69, 0\n" \
  "v_mov_b32 v70, 0\n" \
  "v_mov_b32 v71, 0\n" \
  "v_mov_b32 v72, 0\n" \
  "v_mov_b32 v73, 0\n" \
  "v_mov_b32 v74, 0\n" \
  "v_mov_b32 v75, 0\n" \
  "v_mov_b32 v76, 0\n" \
  "v_mov_b32 v77, 0\n" \
  "v_mov_b32 v78, 0\n" \
  "v_mov_b32 v79, 0\n" \
  "v_mov_b32 v80, 0\n" \
  "v_mov_b32 v81, 0\n" \
  "v_mov_b32 v82, 0\n" \
  "v_mov_b32 v83, 0\n" \
  "v_mov_b32 v84, 0\n" \
  "v_mov_b32 v85, 0\n" \
  "v_mov_b32 v86, 0\n" \
  "v_mov_b32 v87, 0\n" \
  "s_waitcnt lgkmcnt(0)\n" \
  "s_cmp_eq_u32 s38, 0\n" \
  "s_cselect_b32 s74, -1, 0\n" \
  "v_mov_b32 v64, s74\n" \
  "LOOPs3_%=:\n" \
  "LWAs3_%=:\n" \
  "s_memrealtime s[76:77]\n" \
  "s_memtime s[78:79]\n" \
  "s_waitcnt lgkmcnt(0)\n" \
  "s_lshl_b32 s75, s76, 2\n" \
  "s_sub_i32 s75, s48, s75\n" \
  "s_mul_i32 s75, s75, s50\n" \
  "s_ashr_i32 s75, s75, 10\n" \
  "s_sub_i32 s74, s49, s78\n" \
  "s_min_i32 s74, s74, s75\n" \
  "s_sub_i32 s75, s75, 92\n" \
  "s_max_i32 s74, s74, s75\n" \
  "s_add_u32 s49, s74, s78\n" \
  "s_cmp_gt_i32 s74, 168\n" \
  "s_cbranch_scc1 LWAs3_%=\n" \
  "s_cmp_lt_i32 s74, s87\n" \
  "s_cbranch_scc1 LSAs3_%=\n" \
  "s_max_i32 s74, s74, 48\n" \
  "s_lshr_b32 s74, s74, 3\n" \
  "s_lshl_b32 s74, s74, 2\n" \
  "s_sub_u32 s80, s84, s74\n" \
  "s_subb_u32 s81, s85, 0\n" \
  "s_setpc_b64 s[80:81]\n" \
  "s_nop 1\n" \
  "s_nop 1\n" \
  "s_nop 1\n" \
  "s_nop 1\n" \
  "s_nop 1\n" \
  "s_nop 1\n" \
  "s_nop 1\n" \
  "s_nop 1\n" \
  "s_nop 1\n" \
  "s_nop 1\n" \
  "s_nop 1\n" \
  "s_nop 1\n" \
  "s_nop 1\n" \
  "s_nop 1\n" \
  "s_nop 1\n" \
  "s_nop 1\n" \
  "LEAs3_%=:\n" \
  "s_barrier\n" \
  "buffer_load_dwordx4 v[24:27], v1, s[36:39], s72 offen offset:0 sc1\n" \
  "s_mov_b32 m0, 68608\n" \
  "s_nop 0\n" \
  "buffer_load_dwordx4 v1, s[36:39], s69 offen sc1 lds\n" \
  "buffer_load_dwordx4 v[28:31], v1, s[36:39], s72 offen offset:1024 sc1\n" \
  "s_mov_b32 m0, 72704\n" \
  "s_nop 0\n" \
  "buffer_load_dwordx4 v1, s[36:39], s70 offen sc1 lds\n" \
  "ds_write_b64 v6, v[60:61] offset:3072\n" \
  "ds_write_b64 v6, v[60:61] offset:3584\n" \
  "ds_write_b64 v6, v[60:61] offset:5632\n" \
  "s_add_u32 s68, s68, 1\n" \
  "LMs3_%=:\n" \
  "s_waitcnt vmcnt(3)\n" \
  "v_smfmac_f32_16x16x64_bf16 v[64:67], v[24:27], v[88:95], v0\n" \
  "ds_read_b128 v[88:91], v3 offset:8192\n" \
  "v_smfmac_f32_16x16x64_bf16 v[68:71], v[24:27], v[96:103], v0\n" \
  "ds_read_b128 v[92:95], v3 offset:9216\n" \
  "v_smfmac_f32_16x16x64_bf16 v[72:75], v[24:27], v[104:111], v0\n" \
  "ds_read_b128 v[96:99], v3 offset:10240\n" \
  "v_smfmac_f32_16x16x64_bf16 v[76:79], v[24:27], v[112:119], v0\n" \
  "ds_read_b128 v[100:103], v3 offset:11264\n" \
  "v_smfmac_f32_16x16x64_bf16 v[80:83], v[24:27], a[0:7], v0\n" \
  "ds_read_b128 v[104:107], v3 offset:12288\n" \
  "v_smfmac_f32_16x16x64_bf16 v[84:87], v[24:27], a[8:15], v0\n" \
  "s_waitcnt vmcnt(2)\n" \
  "s_waitcnt lgkmcnt(5)\n" \
  "s_barrier\n" \
  "ds_read_b128 v[32:35], v2 offset:2048\n" \
  "s_waitcnt vmcnt(1)\n" \
  "v_smfmac_f32_16x16x64_bf16 v[64:67], v[28:31], a[16:23], v0\n" \
  "ds_read_b128 v[36:39], v2 offset:3072\n" \
  "v_smfmac_f32_16x16x64_bf16 v[68:71], v[28:31], a[24:31], v0\n" \
  "ds_read_b128 v[40:43], v2 offset:4096\n" \
  "s_waitcnt lgkmcnt(6)\n" \
  "v_smfmac_f32_16x16x64_bf16 v[72:75], v[28:31], v[88:95], v0\n" \
  "ds_read_b128 v[44:47], v2 offset:5120\n" \
  "v_smfmac_f32_16x16x64_bf16 v[76:79], v[28:31], a[32:39], v0\n" \
  "ds_read_b128 v[108:111], v3 offset:13312\n" \
  "v_smfmac_f32_16x16x64_bf16 v[80:83], v[28:31], a[40:47], v0\n" \
  "ds_read_b128 v[112:115], v3 offset:14336\n" \
  "v_smfmac_f32_16x16x64_bf16 v[84:87], v[28:31], a[48:55], v0\n" \
  "ds_read_b128 v[116:119], v3 offset:15360\n" \
  "s_waitcnt lgkmcnt(6)\n" \
  "v_smfmac_f32_16x16x64_bf16 v[64:67], v[32:35], a[56:63], v0\n" \
  "ds_read_b128 v[88:91], v3 offset:16384\n" \
  "v_smfmac_f32_16x16x64_bf16 v[68:71], v[32:35], v[96:103], v0\n" \
  "ds_read_b128 v[92:95], v3 offset:17408\n" \
  "v_smfmac_f32_16x16x64_bf16 v[72:75], v[32:35], a[64:71], v0\n" \
  "ds_read_b128 v[96:99], v3 offset:18432\n" \
  "s_waitcnt lgkmcnt(8)\n" \
  "v_smfmac_f32_16x16x64_bf16 v[64:67], v[36:39], a[72:79], v0\n" \
  "s_waitcnt vmcnt(0)\n" \
  "s_barrier\n" \
  "ds_read_b128 v[100:103], v3 offset:19456\n" \
  "v_smfmac_f32_16x16x64_bf16 v[68:71], v[36:39], a[80:87], v0\n" \
  "ds_read_b128 v[48:51], v2 offset:6144\n" \
  "v_smfmac_f32_16x16x64_bf16 v[72:75], v[36:39], a[88:95], v0\n" \
  "ds_read_b128 v[52:55], v2 offset:7168\n" \
  "s_waitcnt lgkmcnt(8)\n" \
  "v_smfmac_f32_16x16x64_bf16 v[64:67], v[40:43], v[104:111], v0\n" \
  "ds_read_b128 v[56:59], v2 offset:8192\n" \
  "v_smfmac_f32_16x16x64_bf16 v[68:71], v[40:43], a[96:103], v0\n" \
  "ds_read_b128 v[60:63], v2 offset:9216\n" \
  "s_waitcnt lgkmcnt(8)\n" \
  "v_smfmac_f32_16x16x64_bf16 v[72:75], v[40:43], v[112:119], v0\n" \
  "ds_read_b128 v[104:107], v3 offset:20480\n" \
  "v_smfmac_f32_16x16x64_bf16 v[64:67], v[44:47], a[104:111], v0\n" \
  "ds_read_b128 v[108:111], v3 offset:21504\n" \
  "s_waitcnt lgkmcnt(8)\n" \
  "v_smfmac_f32_16x16x64_bf16 v[68:71], v[44:47], v[88:95], v0\n" \
  "v_smfmac_f32_16x16x64_bf16 v[72:75], v[44:47], a[112:119], v0\n" \
  "s_waitcnt lgkmcnt(5)\n" \
  "v_smfmac_f32_16x16x64_bf16 v[64:67], v[48:51], a[120:127], v0\n" \
  "s_waitcnt lgkmcnt(4)\n" \
  "v_smfmac_f32_16x16x64_bf16 v[64:67], v[52:55], a[128:135], v0\n" \
  "s_waitcnt lgkmcnt(3)\n" \
  "v_smfmac_f32_16x16x64_bf16 v[64:67], v[56:59], a[136:143], v0\n" \
  "s_waitcnt lgkmcnt(2)\n" \
  "v_smfmac_f32_16x16x64_bf16 v[64:67], v[60:63], a[144:151], v0\n" \
  "v_smfmac_f32_16x16x64_bf16 v[68:71], v[48:51], v[96:103], v0\n" \
  "v_smfmac_f32_16x16x64_bf16 v[68:71], v[52:55], a[152:159], v0\n" \
  "s_waitcnt lgkmcnt(0)\n" \
  "v_cmp_u_f32 vcc, v64, v65\n" \
  "v_cmp_u_f32 s[82:83], v66, v67\n" \
  "v_smfmac_f32_16x16x64_bf16 v[68:71], v[56:59], v[104:111], v0\n" \
  "s_or_b64 vcc, vcc, s[82:83]\n" \
  "s_cbranch_scc1 LMQs3_%=\n" \
  "v_cvt_pk_bf16_f32 v64, v64, v65\n" \
  "v_smfmac_f32_16x16x64_bf16 v[68:71], v[60:63], a[160:167], v0\n" \
  "v_cvt_pk_bf16_f32 v65, v66, v67\n" \
  "v_smfmac_f32_16x16x64_bf16 v[72:75], v[48:51], a[168:175], v0\n" \
  "ds_write_b64 v6, v[64:65] offset:35840\n" \
  "v_smfmac_f32_16x16x64_bf16 v[72:75], v[52:55], a[176:183], v0\n" \
  "v_cvt_pk_bf16_f32 v68, v68, v69\n" \
  "v_smfmac_f32_16x16x64_bf16 v[72:75], v[56:59], a[184:191], v0\n" \
  "v_cvt_pk_bf16_f32 v69, v70, v71\n" \
  "v_smfmac_f32_16x16x64_bf16 v[72:75], v[60:63], a[192:199], v0\n" \
  "ds_write_b64 v6, v[68:69] offset:36352\n" \
  "v_smfmac_f32_16x16x64_bf16 v[76:79], v[32:35], a[200:207], v0\n" \
  "v_smfmac_f32_16x16x64_bf16 v[76:79], v[36:39], a[208:215], v0\n" \
  "v_cvt_pk_bf16_f32 v72, v72, v73\n" \
  "v_smfmac_f32_16x16x64_bf16 v[76:79], v[40:43], a[216:223], v0\n" \
  "v_cvt_pk_bf16_f32 v73, v74, v75\n" \
  "v_smfmac_f32_16x16x64_bf16 v[76:79], v[44:47], a[224:231], v0\n" \
  "ds_write_b64 v6, v[72:73] offset:38400\n" \
  "v_smfmac_f32_16x16x64_bf16 v[76:79], v[48:51], a[232:239], v0\n" \
  "v_smfmac_f32_16x16x64_bf16 v[76:79], v[52:55], a[240:247], v0\n" \
  "v_smfmac_f32_16x16x64_bf16 v[76:79], v[56:59], a[248:255], v0\n" \
  "v_smfmac_f32_16x16x64_bf16 v[76:79], v[60:63], v[120:127], v0\n" \
  "v_smfmac_f32_16x16x64_bf16 v[80:83], v[32:35], v[128:135], v0\n" \
  "v_smfmac_f32_16x16x64_bf16 v[80:83], v[36:39], v[136:143], v0\n" \
  "v_cvt_pk_bf16_f32 v76, v76, v77\n" \
  "v_smfmac_f32_16x16x64_bf16 v[80:83], v[40:43], v[144:151], v0\n" \
  "v_cvt_pk_bf16_f32 v77, v78, v79\n" \
  "v_smfmac_f32_16x16x64_bf16 v[80:83], v[44:47], v[152:159], v0\n" \
  "ds_write_b64 v6, v[76:77] offset:3072\n" \
  "v_smfmac_f32_16x16x64_bf16 v[80:83], v[48:51], v[160:167], v0\n" \
  "v_smfmac_f32_16x16x64_bf16 v[80:83], v[52:55], v[168:175], v0\n" \
  "v_smfmac_f32_16x16x64_bf16 v[80:83], v[56:59], v[176:183], v0\n" \
  "v_smfmac_f32_16x16x64_bf16 v[80:83], v[60:63], v[184:191], v0\n" \
  "v_smfmac_f32_16x16x64_bf16 v[84:87], v[32:35], v[192:199], v0\n" \
  "v_smfmac_f32_16x16x64_bf16 v[84:87], v[36:39], v[200:207], v0\n" \
  "v_cvt_pk_bf16_f32 v80, v80, v81\n" \
  "v_smfmac_f32_16x16x64_bf16 v[84:87], v[40:43], v[208:215], v0\n" \
  "v_cvt_pk_bf16_f32 v81, v82, v83\n" \
  "v_smfmac_f32_16x16x64_bf16 v[84:87], v[44:47], v[216:223], v0\n" \
  "ds_write_b64 v6, v[80:81] offset:3584\n" \
  "v_smfmac_f32_16x16x64_bf16 v[84:87], v[48:51], v[224:231], v0\n" \
  "v_smfmac_f32_16x16x64_bf16 v[84:87], v[52:55], v[232:239], v0\n" \
  "v_smfmac_f32_16x16x64_bf16 v[84:87], v[56:59], v[240:247], v0\n" \
  "v_smfmac_f32_16x16x64_bf16 v[84:87], v[60:63], v[248:255], v0\n" \
  "s_nop 7\n" \
  "v_cvt_pk_bf16_f32 v84, v84, v85\n" \
  "v_cvt_pk_bf16_f32 v85, v86, v87\n" \
  "ds_write_b64 v6, v[84:85] offset:5632\n" \
  "s_barrier ; timing only\n" \
  "s_waitcnt lgkmcnt(0)\n" \
  "s_barrier\n" \
  "ds_read_b32 v60, v12 offset:512\n" \
  "s_waitcnt lgkmcnt(0)\n" \
  "v_cmp_ne_u32 vcc, 0, v60\n" \
  "s_nop 3\n" \
  "s_cbranch_vccnz LMXs3_%=\n" \
  "LMYs3_%=:\n" \
  "buffer_load_dword v15, v14, s[36:39], s72 offen sc1\n" \
  "s_lshl_b32 s74, s68, 16\n" \
  "s_or_b32 s74, s74, s51\n" \
  "v_mov_b32 v24, s74\n" \
  "v_mov_b32 v25, s49\n" \
  "v_mov_b32 v26, s48\n" \
  "v_mov_b32 v27, s50\n" \
  "buffer_store_dwordx4 v[24:27], v16, s[36:39], 0 offen sc1\n" \
  "ds_read_b128 v[88:91], v3 offset:0\n" \
  "ds_read_b128 v[92:95], v3 offset:1024\n" \
  "ds_read_b128 v[96:99], v3 offset:2048\n" \
  "ds_read_b128 v[100:103], v3 offset:3072\n" \
  "ds_read_b128 v[104:107], v3 offset:4096\n" \
  "ds_read_b128 v[108:111], v3 offset:5120\n" \
  "ds_read_b128 v[112:115], v3 offset:6144\n" \
  "ds_read_b128 v[116:119], v3 offset:7168\n" \
  "v_mov_b32 v60, -1\n" \
  "v_mov_b32 v61, -1\n" \
  "ds_write_b64 v6, v[60:61] offset:35840\n" \
  "ds_write_b64 v6, v[60:61] offset:36352\n" \
  "ds_write_b64 v6, v[60:61] offset:38400\n" \
  "v_mov_b32 v64, 0\n" \
  "v_mov_b32 v65, 0\n" \
  "v_mov_b32 v66, 0\n" \
  "v_mov_b32 v67, 0\n" \
  "v_mov_b32 v68, 0\n" \
  "v_mov_b32 v69, 0\n" \
  "v_mov_b32 v70, 0\n" \
  "v_mov_b32 v71, 0\n" \
  "v_mov_b32 v72, 0\n" \
  "v_mov_b32 v73, 0\n" \
  "v_mov_b32 v74, 0\n" \
  "v_mov_b32 v75, 0\n" \
  "v_mov_b32 v76, 0\n" \
  "v_mov_b32 v77, 0\n" \
  "v_mov_b32 v78, 0\n" \
  "v_mov_b32 v79, 0\n" \
  "v_mov_b32 v80, 0\n" \
  "v_mov_b32 v81, 0\n" \
  "v_mov_b32 v82, 0\n" \
  "v_mov_b32 v83, 0\n" \
  "v_mov_b32 v84, 0\n" \
  "v_mov_b32 v85, 0\n" \
  "v_mov_b32 v86, 0\n" \
  "v_mov_b32 v87, 0\n" \
  "s_waitcnt lgkmcnt(0)\n" \
  "s_memrealtime s[76:77]\n" \
  "s_memtime s[78:79]\n" \
  "s_waitcnt lgkmcnt(0)\n" \
  "s_lshl_b32 s75, s76, 2\n" \
  "s_sub_i32 s75, s48, s75\n" \
  "s_mul_i32 s75, s75, s50\n" \
  "s_ashr_i32 s75, s75, 10\n" \
  "s_sub_i32 s74, s49, s78\n" \
  "s_min_i32 s74, s74, s75\n" \
  "s_sub_i32 s75, s75, 92\n" \
  "s_max_i32 s74, s74, s75\n" \
  "s_add_u32 s49, s74, s78\n" \
  "s_sub_u32 s74, s78, s89\n" \
  "s_sub_u32 s75, s76, s88\n" \
  "v_cvt_f32_u32 v24, s74\n" \
  "v_cvt_f32_u32 v25, s75\n" \
  "v_rcp_f32 v25, v25\n" \
  "v_mul_f32 v24, 0x43820000, v24\n" \
  "v_mul_f32 v24, v24, v25\n" \
  "v_cvt_u32_f32 v24, v24\n" \
  "s_nop 0\n" \
  "v_readfirstlane_b32 s74, v24\n" \
  "s_min_u32 s74, s74, 6400\n" \
  "s_cmp_gt_u32 s75, 900\n" \
  "s_cselect_b32 s50, s74, s50\n" \
  "s_cselect_b32 s88, s76, s88\n" \
  "s_cselect_b32 s89, s78, s89\n" \
  "s_waitcnt vmcnt(1)\n" \
  "v_readlane_b32 s74, v15, 3\n" \
  "s_nop 0\n" \
  "s_sub_u32 s75, s74, s51\n" \
  "s_cmp_lt_u32 s75, 16\n" \
  "s_cselect_b32 s51, s74, s51\n" \
  "v_readlane_b32 s74, v15, 0\n" \
  "s_add_u32 s90, s90, s53\n" \
  "s_sub_u32 s75, s74, s90\n" \
  "s_add_u32 s75, s75, 2000\n" \
  "s_cmp_lt_u32 s75, 4000\n" \
  "s_cselect_b32 s90, s74, s90\n" \
  "s_mul_i32 s74, s51, s54\n" \
  "s_add_u32 s74, s74, s90\n" \
  "s_sub_i32 s75, s74, s48\n" \
  "s_mov_b32 s48, s74\n" \
  "s_mul_i32 s75, s75, s50\n" \
  "s_ashr_i32 s75, s75, 10\n" \
  "s_add_u32 s49, s49, s75\n" \
  "s_mov_b32 s71, 0\n" \
  "s_cmp_eq_u32 s68, 64\n" \
  "s_cbranch_scc1 LENDs3_%=\n" \
  "v_readlane_b32 s72, v11, s68\n" \
  "s_nop 0\n" \
  "s_add_u32 s69, s72, 5120\n" \
  "s_add_u32 s70, s72, 17408\n" \
  "s_add_u32 s44, s72, s93\n" \
  "s_add_u32 s45, s72, s94\n" \
  "s_add_u32 s46, s72, s95\n" \
  "v_readlane_b32 s41, v7, s68\n" \
  "s_nop 1\n" \
  "s_branch LOOPs3_%=\n" \
  "LSAs3_%=:\n" \
  "s_add_i32 s71, s71, 48\n" \
  "s_cmp_gt_i32 s71, 0\n" \
  "s_cselect_b32 s74, s54, 0\n" \
  "s_addc_u32 s51, s51, 0\n" \
  "s_sub_i32 s71, s71, s74\n" \
  "s_mul_i32 s74, s51, s54\n" \
  "s_add_u32 s74, s74, s90\n" \
  "s_add_u32 s74, s74, s71\n" \
  "s_sub_i32 s75, s74, s48\n" \
  "s_mov_b32 s48, s74\n" \
  "s_mul_i32 s75, s75, s50\n" \
  "s_ashr_i32 s75, s75, 10\n" \
  "s_add_u32 s49, s49, s75\n" \
  "s_branch LWAs3_%=\n" \
  "LMFs3_%=:\n" \
  "ds_read_b128 v[88:91], v3 offset:0\n" \
  "ds_read_b128 v[92:95], v3 offset:1024\n" \
  "ds_read_b128 v[96:99], v3 offset:2048\n" \
  "ds_read_b128 v[100:103], v3 offset:3072\n" \
  "ds_read_b128 v[104:107], v3 offset:4096\n" \
  "ds_read_b128 v[108:111], v3 offset:5120\n" \
  "ds_read_b128 v[112:115], v3 offset:6144\n" \
  "ds_read_b128 v[116:119], v3 offset:7168\n" \
  "v_mov_b32 v60, -1\n" \
  "v_mov_b32 v61, -1\n" \
  "ds_write_b64 v6, v[60:61] offset:35840\n" \
  "ds_write_b64 v6, v[60:61] offset:36352\n" \
  "ds_write_b64 v6, v[60:61] offset:38400\n" \
  "v_mov_b32 v64, 0\n" \
  "v_mov_b32 v65, 0\n" \
  "v_mov_b32 v66, 0\n" \
  "v_mov_b32 v67, 0\n" \
  "v_mov_b32 v68, 0\n" \
  "v_mov_b32 v69, 0\n" \
  "v_mov_b32 v70, 0\n" \
  "v_mov_b32 v71, 0\n" \
  "v_mov_b32 v72, 0\n" \
  "v_mov_b32 v73, 0\n" \
  "v_mov_b32 v74, 0\n" \
  "v_mov_b32 v75, 0\n" \
  "v_mov_b32 v76, 0\n" \
  "v_mov_b32 v77, 0\n" \
  "v_mov_b32 v78, 0\n" \
  "v_mov_b32 v79, 0\n" \
  "v_mov_b32 v80, 0\n" \
  "v_mov_b32 v81, 0\n" \
  "v_mov_b32 v82, 0\n" \
  "v_mov_b32 v83, 0\n" \
  "v_mov_b32 v84, 0\n" \
  "v_mov_b32 v85, 0\n" \
  "v_mov_b32 v86, 0\n" \
  "v_mov_b32 v87, 0\n" \
  "s_waitcnt lgkmcnt(0)\n" \
  "s_mov_b32 s71, 0\n" \
  "s_mul_i32 s74, s51, s54\n" \
  "s_add_u32 s74, s74, s90\n" \
  "s_sub_i32 s75, s74, s48\n" \
  "s_mov_b32 s48, s74\n" \
  "s_mul_i32 s75, s75, s50\n" \
  "s_ashr_i32 s75, s75, 10\n" \
  "s_add_u32 s49, s49, s75\n" \
  "LHMs3_%=:\n" \
  "s_add_u32 s48, s48, s54\n" \
  "s_add_u32 s49, s49, s86\n" \
  "s_add_u32 s51, s51, 1\n" \
  "LGMs3_%=:\n" \
  "s_memrealtime s[76:77]\n" \
  "s_memtime s[78:79]\n" \
  "s_waitcnt lgkmcnt(0)\n" \
  "s_lshl_b32 s75, s76, 2\n" \
  "s_sub_i32 s75, s48, s75\n" \
  "s_mul_i32 s75, s75, s50\n" \
  "s_ashr_i32 s75, s75, 10\n" \
  "s_sub_i32 s74, s49, s78\n" \
  "s_min_i32 s74, s74, s75\n" \
  "s_sub_i32 s75, s75, 92\n" \
  "s_max_i32 s74, s74, s75\n" \
  "s_add_u32 s49, s74, s78\n" \
  "s_cmp_gt_i32 s74, 20\n" \
  "s_cbranch_scc1 LGMs3_%=\n" \
  "s_cmp_lt_i32 s74, s87\n" \
  "s_cbranch_scc1 LHMs3_%=\n" \
  "s_barrier\n" \
  "buffer_load_dwordx4 v[24:27], v1, s[36:39], s72 offen offset:0 sc1\n" \
  "s_mov_b32 m0, 68608\n" \
  "s_nop 0\n" \
  "buffer_load_dwordx4 v1, s[36:39], s69 offen sc1 lds\n" \
  "buffer_load_dwordx4 v[28:31], v1, s[36:39], s72 offen offset:1024 sc1\n" \
  "s_mov_b32 m0, 72704\n" \
  "s_nop 0\n" \
  "buffer_load_dwordx4 v1, s[36:39], s70 offen sc1 lds\n" \
  "ds_write_b64 v6, v[60:61] offset:3072\n" \
  "ds_write_b64 v6, v[60:61] offset:3584\n" \
  "ds_write_b64 v6, v[60:61] offset:5632\n" \
  "s_branch LMs3_%=\n" \
  "LMQs3_%=:\n" \
  "s_waitcnt lgkmcnt(0)\n" \
  "s_barrier\n" \
  "s_barrier\n" \
  "ds_read_b32 v60, v12 offset:512\n" \
  "s_waitcnt lgkmcnt(0)\n" \
  "LMXs3_%=:\n" \
  "v_cmp_eq_u32 vcc, 2, v60\n" \
  "s_waitcnt lgkmcnt(0)\n" \
  "s_nop 2\n" \
  "s_barrier\n" \
  "s_cbranch_vccnz LMFs3_%=\n" \
  "s_branch LMYs3_%=\n" \
  "LENDs3_%=:\n" \
  "s_cmp_lg_u32 s38, 0\n" \
  "s_cbranch_scc1 LFINs3_%=\n" \
  "s_waitcnt vmcnt(0) lgkmcnt(0)\n" \
  "s_mov_b32 s38, s47\n" \
  "s_mov_b32 s68, 0\n" \
  "s_mov_b32 s73, 0\n" \
  "s_mov_b32 s56, 0\n" \
  "s_mov_b32 s57, 0\n" \
  "s_branch LPASSs3_%=\n" \
  "LFINs3_%=:\n" \
  "s_branch LDONE_%=\n" \
  "LDONE_%=:\n" \
  "s_waitcnt vmcnt(0) lgkmcnt(0)\n" \
  "s_mov_b32 s53, s73\n" \
  "s_mov_b32 s54, s51\n" \
  "s_mov_b32 s55, s50\n"
#define CROLE_CLOBBERS \
  "v32", "v33", "v34", "v35", "v36", "v37", "v38", "v39", "v40", "v41", "v42", "v43", "v44", "v45", "v46", "v47", "v48", "v49", "v50", "v51", "v52", \
  "v53", "v54", "v55", "v56", "v57", "v58", "v59", "v60", "v61", "v62", "v63", "v64", "v65", "v66", "v67", "v68", "v69", "v70", "v71", "v72", "v73", \
  "v74", "v75", "v76", "v77", "v78", "v79", "v80", "v81", "v82", "v83", "v84", "v85", "v86", "v87", "v88", "v89", "v90", "v91", "v92", "v93", "v94", \
  "v95", "v96", "v97", "v98", "v99", "v100", "v101", "v102", "v103", "v104", "v105", "v106", "v107", "v108", "v109", "v110", "v111", "v112", "v113", \
  "v114", "v115", "v116", "v117", "v118", "v119", "v120", "v121", "v122", "v123", "v124", "v125", "v126", "v127", "v128", "v129", "v130", "v131", \
  "v132", "v133", "v134", "v135", "v136", "v137", "v138", "v139", "v140", "v141", "v142", "v143", "v144", "v145", "v146", "v147", "v148", "v149", \
  "v150", "v151", "v152", "v153", "v154", "v155", "v156", "v157", "v158", "v159", "v160", "v161", "v162", "v163", "v164", "v165", "v166", "v167", \
  "v168", "v169", "v170", "v171", "v172", "v173", "v174", "v175", "v176", "v177", "v178", "v179", "v180", "v181", "v182", "v183", "v184", "v185", \
  "v186", "v187", "v188", "v189", "v190", "v191", "v192", "v193", "v194", "v195", "v196", "v197", "v198", "v199", "v200", "v201", "v202", "v203", \
  "v204", "v205", "v206", "v207", "v208", "v209", "v210", "v211", "v212", "v213", "v214", "v215", "v216", "v217", "v218", "v219", "v220", "v221", \
  "v222", "v223", "v224", "v225", "v226", "v227", "v228", "v229", "v230", "v231", "v232", "v233", "v234", "v235", "v236", "v237", "v238", "v239", \
  "v240", "v241", "v242", "v243", "v244", "v245", "v246", "v247", "v248", "v249", "v250", "v251", "v252", "v253", "v254", "v255", "s34", "s35", "s40", \
  "s41", "s42", "s43", "s44", "s45", "s46", "s47", "s48", "s49", "s50", "s51", "s68", "s69", "s70", "s71", "s72", "s73", "s74", "s75", "s76", "s77", \
  "s78", "s79", "s80", "s81", "s82", "s83", "s84", "s85", "s86", "s87", "s88", "s89", "s90", "s91", "s93", "s94", "s95", "s96", "a0", "a1", "a2", "a3", \
  "a4", "a5", "a6", "a7", "a8", "a9", "a10", "a11", "a12", "a13", "a14", "a15", "a16", "a17", "a18", "a19", "a20", "a21", "a22", "a23", "a24", "a25", \
  "a26", "a27", "a28", "a29", "a30", "a31", "a32", "a33", "a34", "a35", "a36", "a37", "a38", "a39", "a40", "a41", "a42", "a43", "a44", "a45", "a46", \
  "a47", "a48", "a49", "a50", "a51", "a52", "a53", "a54", "a55", "a56", "a57", "a58", "a59", "a60", "a61", "a62", "a63", "a64", "a65", "a66", "a67", \
  "a68", "a69", "a70", "a71", "a72", "a73", "a74", "a75", "a76", "a77", "a78", "a79", "a80", "a81", "a82", "a83", "a84", "a85", "a86", "a87", "a88", \
  "a89", "a90", "a91", "a92", "a93", "a94", "a95", "a96", "a97", "a98", "a99", "a100", "a101", "a102", "a103", "a104", "a105", "a106", "a107", "a108", \
  "a109", "a110", "a111", "a112", "a113", "a114", "a115", "a116", "a117", "a118", "a119", "a120", "a121", "a122", "a123", "a124", "a125", "a126", \
  "a127", "a128", "a129", "a130", "a131", "a132", "a133", "a134", "a135", "a136", "a137", "a138", "a139", "a140", "a141", "a142", "a143", "a144", \
  "a145", "a146", "a147", "a148", "a149", "a150", "a151", "a152", "a153", "a154", "a155", "a156", "a157", "a158", "a159", "a160", "a161", "a162", \
  "a163", "a164", "a165", "a166", "a167", "a168", "a169", "a170", "a171", "a172", "a173", "a174", "a175", "a176", "a177", "a178", "a179", "a180", \
  "a181", "a182", "a183", "a184", "a185", "a186", "a187", "a188", "a189", "a190", "a191", "a192", "a193", "a194", "a195", "a196", "a197", "a198", \
  "a199", "a200", "a201", "a202", "a203", "a204", "a205", "a206", "a207", "a208", "a209", "a210", "a211", "a212", "a213", "a214", "a215", "a216", \
  "a217", "a218", "a219", "a220", "a221", "a222", "a223", "a224", "a225", "a226", "a227", "a228", "a229", "a230", "a231", "a232", "a233", "a234", \
  "a235", "a236", "a237", "a238", "a239", "a240", "a241", "a242", "a243", "a244", "a245", "a246", "a247", "a248", "a249", "a250", "a251", "a252", \
  "a253", "a254", "a255"
#define CROLE_ASM \
  "s_mov_b32 s34, 0x00ff00ff\n" \
  "s_mov_b32 s35, 0x00ff00ff\n" \
  "s_mov_b32 s40, 0x00ff00ff\n" \
  "s_mov_b32 s41, 0xff00ff00\n" \
  "s_mov_b32 s68, 0\n" \
  "s_mov_b32 s73, 0\n" \
  "s_mov_b32 s58, 0\n" \
  "s_mov_b32 s59, 0\n" \
  "s_mov_b32 s51, 0\n" \
  "s_mov_b32 s71, 0\n" \
  "s_mov_b32 s44, 0\n" \
  "s_mov_b32 s50, 5900\n" \
  "s_mov_b32 s42, 0x40000000\n" \
  "s_mov_b32 s93, s60\n" \
  "s_mov_b32 s60, 0\n" \
  "s_mov_b32 s61, 0\n" \
  "s_mov_b32 s62, 0\n" \
  "s_mov_b32 s63, 0\n" \
  "s_mov_b32 s64, 0\n" \
  "s_mov_b32 s65, 0\n" \
  "s_mov_b32 s66, 0\n" \
  "s_mov_b32 s67, 0\n" \
  "s_cmp_eq_u32 s52, 3\n" \
  "s_cbranch_scc1 LSTARTc3_%=\n" \
  "LSTARTc0_%=:\n" \
  "s_barrier\n" \
  "v_mov_b32 v15, 1\n" \
  "v_readlane_b32 s46, v6, 0\n" \
  "s_nop 3\n" \
  "v_readlane_b32 s47, v6, 1\n" \
  "buffer_load_dwordx4 a[0:3], v18, s[36:39], s46 offen sc1\n" \
  "buffer_load_dwordx4 a[80:83], v19, s[36:39], s46 offen sc1\n" \
  "buffer_load_dwordx4 a[160:163], v20, s[36:39], s46 offen sc1\n" \
  "buffer_load_dwordx4 a[240:243], v21, s[36:39], s46 offen sc1\n" \
  "buffer_load_dwordx4 v[176:179], v22, s[36:39], s46 offen sc1\n" \
  "s_barrier\n" \
  "v_readlane_b32 s46, v6, 2\n" \
  "buffer_load_dwordx4 a[4:7], v18, s[36:39], s47 offen sc1\n" \
  "buffer_load_dwordx4 a[84:87], v19, s[36:39], s47 offen sc1\n" \
  "buffer_load_dwordx4 a[164:167], v20, s[36:39], s47 offen sc1\n" \
  "buffer_load_dwordx4 a[244:247], v21, s[36:39], s47 offen sc1\n" \
  "buffer_load_dwordx4 v[180:183], v22, s[36:39], s47 offen sc1\n" \
  "v_readlane_b32 s47, v6, 3\n" \
  "buffer_load_dwordx4 a[8:11], v18, s[36:39], s46 offen sc1\n" \
  "buffer_load_dwordx4 a[88:91], v19, s[36:39], s46 offen sc1\n" \
  "buffer_load_dwordx4 a[168:171], v20, s[36:39], s46 offen sc1\n" \
  "buffer_load_dwordx4 a[248:251], v21, s[36:39], s46 offen sc1\n" \
  "buffer_load_dwordx4 v[184:187], v22, s[36:39], s46 offen sc1\n" \
  "s_barrier\n" \
  "v_readlane_b32 s46, v6, 4\n" \
  "buffer_load_dwordx4 a[12:15], v18, s[36:39], s47 offen sc1\n" \
  "buffer_load_dwordx4 a[92:95], v19, s[36:39], s47 offen sc1\n" \
  "buffer_load_dwordx4 a[172:175], v20, s[36:39], s47 offen sc1\n" \
  "buffer_load_dwordx4 a[252:255], v21, s[36:39], s47 offen sc1\n" \
  "buffer_load_dwordx4 v[188:191], v22, s[36:39], s47 offen sc1\n" \
  "v_readlane_b32 s47, v6, 5\n" \
  "buffer_load_dwordx4 a[16:19], v18, s[36:39], s46 offen sc1\n" \
  "buffer_load_dwordx4 a[96:99], v19, s[36:39], s46 offen sc1\n" \
  "buffer_load_dwordx4 a[176:179], v20, s[36:39], s46 offen sc1\n" \
  "buffer_load_dwordx4 v[112:115], v21, s[36:39], s46 offen sc1\n" \
  "buffer_load_dwordx4 v[192:195], v22, s[36:39], s46 offen sc1\n" \
  "v_readlane_b32 s46, v6, 6\n" \
  "buffer_load_dwordx4 a[20:23], v18, s[36:39], s47 offen sc1\n" \
  "buffer_load_dwordx4 a[100:103], v19, s[36:39], s47 offen sc1\n" \
  "buffer_load_dwordx4 a[180:183], v20, s[36:39], s47 offen sc1\n" \
  "buffer_load_dwordx4 v[116:119], v21, s[36:39], s47 offen sc1\n" \
  "buffer_load_dwordx4 v[196:199], v22, s[36:39], s47 offen sc1\n" \
  "v_readlane_b32 s47, v6, 7\n" \
  "buffer_load_dwordx4 a[24:27], v18, s[36:39], s46 offen sc1\n" \
  "buffer_load_dwordx4 a[104:107], v19, s[36:39], s46 offen sc1\n" \
  "buffer_load_dwordx4 a[184:187], v20, s[36:39], s46 offen sc1\n" \
  "buffer_load_dwordx4 v[120:123], v21, s[36:39], s46 offen sc1\n" \
  "buffer_load_dwordx4 v[200:203], v22, s[36:39], s46 offen sc1\n" \
  "v_readlane_b32 s46, v6, 8\n" \
  "buffer_load_dwordx4 a[28:31], v18, s[36:39], s47 offen sc1\n" \
  "buffer_load_dwordx4 a[108:111], v19, s[36:39], s47 offen sc1\n" \
  "buffer_load_dwordx4 a[188:191], v20, s[36:39], s47 offen sc1\n" \
  "buffer_load_dwordx4 v[124:127], v21, s[36:39], s47 offen sc1\n" \
  "buffer_load_dwordx4 v[204:207], v22, s[36:39], s47 offen sc1\n" \
  "s_barrier\n" \
  "v_readlane_b32 s47, v6, 9\n" \
  "buffer_load_dwordx4 a[32:35], v18, s[36:39], s46 offen sc1\n" \
  "buffer_load_dwordx4 a[112:115], v19, s[36:39], s46 offen sc1\n" \
  "buffer_load_dwordx4 a[192:195], v20, s[36:39], s46 offen sc1\n" \
  "buffer_load_dwordx4 v[128:131], v21, s[36:39], s46 offen sc1\n" \
  "buffer_load_dwordx4 v[208:211], v22, s[36:39], s46 offen sc1\n" \
  "v_readlane_b32 s46, v6, 10\n" \
  "buffer_load_dwordx4 a[36:39], v18, s[36:39], s47 offen sc1\n" \
  "buffer_load_dwordx4 a[116:119], v19, s[36:39], s47 offen sc1\n" \
  "buffer_load_dwordx4 a[196:199], v20, s[36:39], s47 offen sc1\n" \
  "buffer_load_dwordx4 v[132:135], v21, s[36:39], s47 offen sc1\n" \
  "buffer_load_dwordx4 v[212:215], v22, s[36:39], s47 offen sc1\n" \
  "v_readlane_b32 s47, v6, 11\n" \
  "buffer_load_dwordx4 a[40:43], v18, s[36:39], s46 offen sc1\n" \
  "buffer_load_dwordx4 a[120:123], v19, s[36:39], s46 offen sc1\n" \
  "buffer_load_dwordx4 a[200:203], v20, s[36:39], s46 offen sc1\n" \
  "buffer_load_dwordx4 v[136:139], v21, s[36:39], s46 offen sc1\n" \
  "buffer_load_dwordx4 v[216:219], v22, s[36:39], s46 offen sc1\n" \
  "v_readlane_b32 s46, v6, 12\n" \
  "buffer_load_dwordx4 a[44:47], v18, s[36:39], s47 offen sc1\n" \
  "buffer_load_dwordx4 a[124:127], v19, s[36:39], s47 offen sc1\n" \
  "buffer_load_dwordx4 a[204:207], v20, s[36:39], s47 offen sc1\n" \
  "buffer_load_dwordx4 v[140:143], v21, s[36:39], s47 offen sc1\n" \
  "buffer_load_dwordx4 v[220:223], v22, s[36:39], s47 offen sc1\n" \
  "v_readlane_b32 s47, v6, 13\n" \
  "buffer_load_dwordx4 a[48:51], v18, s[36:39], s46 offen sc1\n" \
  "buffer_load_dwordx4 a[128:131], v19, s[36:39], s46 offen sc1\n" \
  "buffer_load_dwordx4 a[208:211], v20, s[36:39], s46 offen sc1\n" \
  "buffer_load_dwordx4 v[144:147], v21, s[36:39], s46 offen sc1\n" \
  "buffer_load_dwordx4 v[224:227], v22, s[36:39], s46 offen sc1\n" \
  "s_barrier\n" \
  "v_readlane_b32 s46, v6, 14\n" \
  "buffer_load_dwordx4 a[52:55], v18, s[36:39], s47 offen sc1\n" \
  "buffer_load_dwordx4 a[132:135], v19, s[36:39], s47 offen sc1\n" \
  "buffer_load_dwordx4 a[212:215], v20, s[36:39], s47 offen sc1\n" \
  "buffer_load_dwordx4 v[148:151], v21, s[36:39], s47 offen sc1\n" \
  "buffer_load_dwordx4 v[228:231], v22, s[36:39], s47 offen sc1\n" \
  "v_readlane_b32 s47, v6, 15\n" \
  "buffer_load_dwordx4 a[56:59], v18, s[36:39], s46 offen sc1\n" \
  "buffer_load_dwordx4 a[136:139], v19, s[36:39], s46 offen sc1\n" \
  "buffer_load_dwordx4 a[216:219], v20, s[36:39], s46 offen sc1\n" \
  "buffer_load_dwordx4 v[152:155], v21, s[36:39], s46 offen sc1\n" \
  "buffer_load_dwordx4 v[232:235], v22, s[36:39], s46 offen sc1\n" \
  "v_readlane_b32 s46, v6, 16\n" \
  "buffer_load_dwordx4 a[60:63], v18, s[36:39], s47 offen sc1\n" \
  "buffer_load_dwordx4 a[140:143], v19, s[36:39], s47 offen sc1\n" \
  "buffer_load_dwordx4 a[220:223], v20, s[36:39], s47 offen sc1\n" \
  "buffer_load_dwordx4 v[156:159], v21, s[36:39], s47 offen sc1\n" \
  "buffer_load_dwordx4 v[236:239], v22, s[36:39], s47 offen sc1\n" \
  "v_readlane_b32 s47, v6, 17\n" \
  "buffer_load_dwordx4 a[64:67], v18, s[36:39], s46 offen sc1\n" \
  "buffer_load_dwordx4 a[144:147], v19, s[36:39], s46 offen sc1\n" \
  "buffer_load_dwordx4 a[224:227], v20, s[36:39], s46 offen sc1\n" \
  "buffer_load_dwordx4 v[160:163], v21, s[36:39], s46 offen sc1\n" \
  "buffer_load_dwordx4 v[240:243], v22, s[36:39], s46 offen sc1\n" \
  "v_readlane_b32 s46, v6, 18\n" \
  "buffer_load_dwordx4 a[68:71], v18, s[36:39], s47 offen sc1\n" \
  "buffer_load_dwordx4 a[148:151], v19, s[36:39], s47 offen sc1\n" \
  "buffer_load_dwordx4 a[228:231], v20, s[36:39], s47 offen sc1\n" \
  "buffer_load_dwordx4 v[164:167], v21, s[36:39], s47 offen sc1\n" \
  "buffer_load_dwordx4 v[244:247], v22, s[36:39], s47 offen sc1\n" \
  "buffer_atomic_add v15, v7, s[36:39], 0 offen sc1\n" \
  "v_readlane_b32 s47, v6, 19\n" \
  "buffer_load_dwordx4 a[72:75], v18, s[36:39], s46 offen sc1\n" \
  "buffer_load_dwordx4 a[152:155], v19, s[36:39], s46 offen sc1\n" \
  "buffer_load_dwordx4 a[232:235], v20, s[36:39], s46 offen sc1\n" \
  "buffer_load_dwordx4 v[168:171], v21, s[36:39], s46 offen sc1\n" \
  "buffer_load_dwordx4 v[248:251], v22, s[36:39], s46 offen sc1\n" \
  "buffer_load_dwordx4 a[76:79], v18, s[36:39], s47 offen sc1\n" \
  "buffer_load_dwordx4 a[156:159], v19, s[36:39], s47 offen sc1\n" \
  "buffer_load_dwordx4 a[236:239], v20, s[36:39], s47 offen sc1\n" \
  "buffer_load_dwordx4 v[172:175], v21, s[36:39], s47 offen sc1\n" \
  "buffer_load_dwordx4 v[252:255], v22, s[36:39], s47 offen sc1\n" \
  "s_cmp_eq_u32 s52, 1\n" \
  "s_cbranch_scc1 LSTARTc1_%=\n" \
  "s_cmp_eq_u32 s52, 2\n" \
  "s_cbranch_scc1 LSTARTc2_%=\n" \
  "s_mov_b32 s57, 0\n" \
  "v_readlane_b32 s44, v4, 0\n" \
  "s_nop 3\n" \
  "LOOPc0_%=:\n" \
  "s_mov_b32 m0, 0\n" \
  "s_add_u32 s75, s44, 0\n" \
  "s_barrier\n" \
  "buffer_load_dwordx4 v1, s[36:39], s75 offen offset:0 sc1 lds\n" \
  "buffer_load_dwordx4 v1, s[36:39], s75 offen offset:1024 sc1 lds\n" \
  "buffer_load_dwordx4 v1, s[36:39], s75 offen offset:2048 sc1 lds\n" \
  "buffer_load_dwordx4 v1, s[36:39], s75 offen offset:3072 sc1 lds\n" \
  "v_mov_b32 v56, 0\n" \
  "v_mov_b32 v57, 0\n" \
  "v_mov_b32 v58, 0\n" \
  "v_mov_b32 v59, 0\n" \
  "v_mov_b32 v60, 0\n" \
  "v_mov_b32 v61, 0\n" \
  "v_mov_b32 v62, 0\n" \
  "v_mov_b32 v63, 0\n" \
  "v_mov_b32 v64, 0\n" \
  "v_mov_b32 v65, 0\n" \
  "v_mov_b32 v66, 0\n" \
  "v_mov_b32 v67, 0\n" \
  "v_mov_b32 v68, 0\n" \
  "v_mov_b32 v69, 0\n" \
  "v_mov_b32 v70, 0\n" \
  "v_mov_b32 v71, 0\n" \
  "v_mov_b32 v72, 0\n" \
  "v_mov_b32 v73, 0\n" \
  "v_mov_b32 v74, 0\n" \
  "v_mov_b32 v75, 0\n" \
  "s_add_u32 s68, s68, 1\n" \
  "s_min_u32 s74, s68, 63\n" \
  "v_readlane_b32 s45, v4, s74\n" \
  "s_waitcnt vmcnt(0)\n" \
  "s_barrier\n" \
  "buffer_store_dwordx4 v[76:79], v10, s[36:39], s42 offen offset:0 sc1\n" \
  "buffer_store_dwordx4 v[80:83], v10, s[36:39], s42 offen offset:1024 sc1\n" \
  "buffer_store_dwordx4 v[84:87], v10, s[36:39], s42 offen offset:2048 sc1\n" \
  "buffer_store_dwordx4 v[88:91], v10, s[36:39], s42 offen offset:3072 sc1\n" \
  "LMc0_%=:\n" \
  "ds_read_b128 v[16:19], v1 offset:0\n" \
  "ds_read_b128 v[20:23], v1 offset:1024\n" \
  "ds_read_b128 v[24:27], v1 offset:2048\n" \
  "ds_read_b128 v[28:31], v1 offset:3072\n" \
  "ds_read_b128 v[32:35], v1 offset:4096\n" \
  "ds_read_b128 v[36:39], v1 offset:5120\n" \
  "ds_read_b128 v[40:43], v1 offset:6144\n" \
  "ds_read_b128 v[44:47], v1 offset:7168\n" \
  "ds_read_b128 v[48:51], v1 offset:8192\n" \
  "ds_read_b128 v[52:55], v1 offset:9216\n" \
  "s_waitcnt lgkmcnt(9)\n" \
  "v_smfmac_f32_16x16x64_bf16 v[56:59], v[16:19], a[0:7], v0\n" \
  "s_waitcnt lgkmcnt(8)\n" \
  "v_smfmac_f32_16x16x64_bf16 v[56:59], v[20:23], a[8:15], v0\n" \
  "s_waitcnt lgkmcnt(7)\n" \
  "v_smfmac_f32_16x16x64_bf16 v[56:59], v[24:27], a[16:23], v0\n" \
  "s_waitcnt lgkmcnt(6)\n" \
  "v_smfmac_f32_16x16x64_bf16 v[56:59], v[28:31], a[24:31], v0\n" \
  "s_waitcnt lgkmcnt(5)\n" \
  "v_smfmac_f32_16x16x64_bf16 v[56:59], v[32:35], a[32:39], v0\n" \
  "s_waitcnt lgkmcnt(4)\n" \
  "v_smfmac_f32_16x16x64_bf16 v[56:59], v[36:39], a[40:47], v0\n" \
  "s_waitcnt lgkmcnt(3)\n" \
  "v_smfmac_f32_16x16x64_bf16 v[56:59], v[40:43], a[48:55], v0\n" \
  "s_waitcnt lgkmcnt(2)\n" \
  "v_smfmac_f32_16x16x64_bf16 v[56:59], v[44:47], a[56:63], v0\n" \
  "s_waitcnt lgkmcnt(1)\n" \
  "v_smfmac_f32_16x16x64_bf16 v[56:59], v[48:51], a[64:71], v0\n" \
  "s_waitcnt lgkmcnt(0)\n" \
  "v_smfmac_f32_16x16x64_bf16 v[56:59], v[52:55], a[72:79], v0\n" \
  "v_smfmac_f32_16x16x64_bf16 v[60:63], v[16:19], a[80:87], v0\n" \
  "v_smfmac_f32_16x16x64_bf16 v[64:67], v[16:19], a[160:167], v0\n" \
  "v_cmp_u_f32 vcc, v56, v57\n" \
  "v_cmp_u_f32 s[82:83], v58, v59\n" \
  "s_nop 1\n" \
  "s_or_b64 vcc, vcc, s[82:83]\n" \
  "s_cbranch_scc1 LMFc0_%=\n" \
  "LMOKc0_%=:\n" \
  "v_smfmac_f32_16x16x64_bf16 v[68:71], v[16:19], a[240:247], v0\n" \
  "v_smfmac_f32_16x16x64_bf16 v[72:75], v[16:19], v[176:183], v0\n" \
  "v_smfmac_f32_16x16x64_bf16 v[60:63], v[20:23], a[88:95], v0\n" \
  "v_smfmac_f32_16x16x64_bf16 v[64:67], v[20:23], a[168:175], v0\n" \
  "v_smfmac_f32_16x16x64_bf16 v[68:71], v[20:23], a[248:255], v0\n" \
  "v_smfmac_f32_16x16x64_bf16 v[72:75], v[20:23], v[184:191], v0\n" \
  "v_smfmac_f32_16x16x64_bf16 v[60:63], v[24:27], a[96:103], v0\n" \
  "v_smfmac_f32_16x16x64_bf16 v[64:67], v[24:27], a[176:183], v0\n" \
  "v_smfmac_f32_16x16x64_bf16 v[68:71], v[24:27], v[112:119], v0\n" \
  "v_smfmac_f32_16x16x64_bf16 v[72:75], v[24:27], v[192:199], v0\n" \
  "v_smfmac_f32_16x16x64_bf16 v[60:63], v[28:31], a[104:111], v0\n" \
  "v_smfmac_f32_16x16x64_bf16 v[64:67], v[28:31], a[184:191], v0\n" \
  "v_smfmac_f32_16x16x64_bf16 v[68:71], v[28:31], v[120:127], v0\n" \
  "v_smfmac_f32_16x16x64_bf16 v[72:75], v[28:31], v[200:207], v0\n" \
  "v_smfmac_f32_16x16x64_bf16 v[60:63], v[32:35], a[112:119], v0\n" \
  "v_smfmac_f32_16x16x64_bf16 v[64:67], v[32:35], a[192:199], v0\n" \
  "v_smfmac_f32_16x16x64_bf16 v[68:71], v[32:35], v[128:135], v0\n" \
  "v_smfmac_f32_16x16x64_bf16 v[72:75], v[32:35], v[208:215], v0\n" \
  "v_smfmac_f32_16x16x64_bf16 v[60:63], v[36:39], a[120:127], v0\n" \
  "v_smfmac_f32_16x16x64_bf16 v[64:67], v[36:39], a[200:207], v0\n" \
  "v_smfmac_f32_16x16x64_bf16 v[68:71], v[36:39], v[136:143], v0\n" \
  "v_smfmac_f32_16x16x64_bf16 v[72:75], v[36:39], v[216:223], v0\n" \
  "v_smfmac_f32_16x16x64_bf16 v[60:63], v[40:43], a[128:135], v0\n" \
  "v_smfmac_f32_16x16x64_bf16 v[64:67], v[40:43], a[208:215], v0\n" \
  "v_smfmac_f32_16x16x64_bf16 v[68:71], v[40:43], v[144:151], v0\n" \
  "v_smfmac_f32_16x16x64_bf16 v[72:75], v[40:43], v[224:231], v0\n" \
  "v_smfmac_f32_16x16x64_bf16 v[60:63], v[44:47], a[136:143], v0\n" \
  "v_smfmac_f32_16x16x64_bf16 v[64:67], v[44:47], a[216:223], v0\n" \
  "v_smfmac_f32_16x16x64_bf16 v[68:71], v[44:47], v[152:159], v0\n" \
  "v_smfmac_f32_16x16x64_bf16 v[72:75], v[44:47], v[232:239], v0\n" \
  "v_smfmac_f32_16x16x64_bf16 v[60:63], v[48:51], a[144:151], v0\n" \
  "v_smfmac_f32_16x16x64_bf16 v[64:67], v[48:51], a[224:231], v0\n" \
  "v_smfmac_f32_16x16x64_bf16 v[68:71], v[48:51], v[160:167], v0\n" \
  "v_smfmac_f32_16x16x64_bf16 v[72:75], v[48:51], v[240:247], v0\n" \
  "v_smfmac_f32_16x16x64_bf16 v[60:63], v[52:55], a[152:159], v0\n" \
  "v_smfmac_f32_16x16x64_bf16 v[64:67], v[52:55], a[232:239], v0\n" \
  "v_smfmac_f32_16x16x64_bf16 v[68:71], v[52:55], v[168:175], v0\n" \
  "v_smfmac_f32_16x16x64_bf16 v[72:75], v[52:55], v[248:255], v0\n" \
  "v_permlane32_swap_b32 v56, v60\n" \
  "v_permlane32_swap_b32 v57, v61\n" \
  "v_permlane32_swap_b32 v58, v62\n" \
  "v_permlane32_swap_b32 v59, v63\n" \
  "v_pk_add_f32 v[56:57], v[56:57], v[60:61]\n" \
  "v_pk_add_f32 v[58:59], v[58:59], v[62:63]\n" \
  "v_cvt_pk_bf16_f32 v56, v56, v57\n" \
  "v_cvt_pk_bf16_f32 v57, v58, v59\n" \
  "ds_write_b64 v2, v[56:57] offset:0\n" \
  "v_permlane32_swap_b32 v64, v68\n" \
  "v_permlane32_swap_b32 v65, v69\n" \
  "v_permlane32_swap_b32 v66, v70\n" \
  "v_permlane32_swap_b32 v67, v71\n" \
  "v_pk_add_f32 v[64:65], v[64:65], v[68:69]\n" \
  "v_pk_add_f32 v[66:67], v[66:67], v[70:71]\n" \
  "v_cvt_pk_bf16_f32 v64, v64, v65\n" \
  "v_cvt_pk_bf16_f32 v65, v66, v67\n" \
  "ds_write_b64 v2, v[64:65] offset:512\n" \
  "v_permlane32_swap_b32 v72, v74\n" \
  "v_permlane32_swap_b32 v73, v75\n" \
  "v_pk_add_f32 v[72:73], v[72:73], v[74:75]\n" \
  "v_cvt_pk_bf16_f32 v72, v72, v73\n" \
  "ds_write_b32 v3, v72 offset:1024\n" \
  "s_waitcnt lgkmcnt(0)\n" \
  "s_waitcnt vmcnt(0)\n" \
  "s_barrier\n" \
  "ds_read_b128 v[76:79], v1 offset:10240\n" \
  "ds_read_b128 v[80:83], v1 offset:11264\n" \
  "ds_read_b128 v[84:87], v1 offset:12288\n" \
  "ds_read_b128 v[88:91], v1 offset:13312\n" \
  "s_sub_u32 s74, s68, 1\n" \
  "v_readlane_b32 s42, v9, s74\n" \
  "s_waitcnt lgkmcnt(0)\n" \
  "s_mov_b32 s44, s45\n" \
  "s_barrier\n" \
  "s_cmp_eq_u32 s68, 64\n" \
  "s_cbranch_scc0 LOOPc0_%=\n" \
  "buffer_store_dwordx4 v[76:79], v10, s[36:39], s42 offen offset:0 sc1\n" \
  "buffer_store_dwordx4 v[80:83], v10, s[36:39], s42 offen offset:1024 sc1\n" \
  "buffer_store_dwordx4 v[84:87], v10, s[36:39], s42 offen offset:2048 sc1\n" \
  "buffer_store_dwordx4 v[88:91], v10, s[36:39], s42 offen offset:3072 sc1\n" \
  "s_waitcnt vmcnt(0)\n" \
  "buffer_atomic_add v15, v11, s[36:39], 0 offen sc1\n" \
  "s_branch LDONE_%=\n" \
  "LMFc0_%=:\n" \
  "v_max_u32 v92, v16, v17\n" \
  "v_max3_u32 v92, v92, v18, v19\n" \
  "v_max3_u32 v92, v92, v20, v21\n" \
  "v_max3_u32 v92, v92, v22, v23\n" \
  "v_max3_u32 v92, v92, v24, v25\n" \
  "v_max3_u32 v92, v92, v26, v27\n" \
  "v_max3_u32 v92, v92, v28, v29\n" \
  "v_max3_u32 v92, v92, v30, v31\n" \
  "v_max3_u32 v92, v92, v32, v33\n" \
  "v_max3_u32 v92, v92, v34, v35\n" \
  "v_max3_u32 v92, v92, v36, v37\n" \
  "v_max3_u32 v92, v92, v38, v39\n" \
  "v_max3_u32 v92, v92, v40, v41\n" \
  "v_max3_u32 v92, v92, v42, v43\n" \
  "v_max3_u32 v92, v92, v44, v45\n" \
  "v_max3_u32 v92, v92, v46, v47\n" \
  "v_max3_u32 v92, v92, v48, v49\n" \
  "v_max3_u32 v92, v92, v50, v51\n" \
  "v_max3_u32 v92, v92, v52, v53\n" \
  "v_max3_u32 v92, v92, v54, v55\n" \
  "v_cmp_eq_u32 vcc, -1, v92\n" \
  "s_nop 3\n" \
  "s_cbranch_vccz LMOKc0_%=\n" \
  "s_cmp_gt_u32 s57, 20000\n" \
  "s_cbranch_scc1 LMOKc0_%=\n" \
  "s_add_u32 s57, s57, 1\n" \
  "s_barrier\n" \
  "buffer_load_dwordx4 v1, s[36:39], s75 offen offset:0 sc1 lds\n" \
  "buffer_load_dwordx4 v1, s[36:39], s75 offen offset:1024 sc1 lds\n" \
  "buffer_load_dwordx4 v1, s[36:39], s75 offen offset:2048 sc1 lds\n" \
  "buffer_load_dwordx4 v1, s[36:39], s75 offen offset:3072 sc1 lds\n" \
  "v_mov_b32 v56, 0\n" \
  "v_mov_b32 v57, 0\n" \
  "v_mov_b32 v58, 0\n" \
  "v_mov_b32 v59, 0\n" \
  "v_mov_b32 v60, 0\n" \
  "v_mov_b32 v61, 0\n" \
  "v_mov_b32 v62, 0\n" \
  "v_mov_b32 v63, 0\n" \
  "v_mov_b32 v64, 0\n" \
  "v_mov_b32 v65, 0\n" \
  "v_mov_b32 v66, 0\n" \
  "v_mov_b32 v67, 0\n" \
  "v_mov_b32 v68, 0\n" \
  "v_mov_b32 v69, 0\n" \
  "v_mov_b32 v70, 0\n" \
  "v_mov_b32 v71, 0\n" \
  "v_mov_b32 v72, 0\n" \
  "v_mov_b32 v73, 0\n" \
  "v_mov_b32 v74, 0\n" \
  "v_mov_b32 v75, 0\n" \
  "s_waitcnt vmcnt(0)\n" \
  "s_barrier\n" \
  "s_branch LMc0_%=\n" \
  "LSTARTc1_%=:\n" \
  "s_mov_b32 s57, 0\n" \
  "v_readlane_b32 s44, v4, 0\n" \
  "s_nop 3\n" \
  "LOOPc1_%=:\n" \
  "s_mov_b32 m0, 4096\n" \
  "s_add_u32 s75, s44, 4096\n" \
  "s_barrier\n" \
  "buffer_load_dwordx4 v1, s[36:39], s75 offen offset:0 sc1 lds\n" \
  "buffer_load_dwordx4 v1, s[36:39], s75 offen offset:1024 sc1 lds\n" \
  "buffer_load_dwordx4 v1, s[36:39], s75 offen offset:2048 sc1 lds\n" \
  "buffer_load_dwordx4 v1, s[36:39], s75 offen offset:3072 sc1 lds\n" \
  "v_mov_b32 v56, 0\n" \
  "v_mov_b32 v57, 0\n" \
  "v_mov_b32 v58, 0\n" \
  "v_mov_b32 v59, 0\n" \
  "v_mov_b32 v60, 0\n" \
  "v_mov_b32 v61, 0\n" \
  "v_mov_b32 v62, 0\n" \
  "v_mov_b32 v63, 0\n" \
  "v_mov_b32 v64, 0\n" \
  "v_mov_b32 v65, 0\n" \
  "v_mov_b32 v66, 0\n" \
  "v_mov_b32 v67, 0\n" \
  "v_mov_b32 v68, 0\n" \
  "v_mov_b32 v69, 0\n" \
  "v_mov_b32 v70, 0\n" \
  "v_mov_b32 v71, 0\n" \
  "v_mov_b32 v72, 0\n" \
  "v_mov_b32 v73, 0\n" \
  "v_mov_b32 v74, 0\n" \
  "v_mov_b32 v75, 0\n" \
  "s_add_u32 s68, s68, 1\n" \
  "s_min_u32 s74, s68, 63\n" \
  "v_readlane_b32 s45, v4, s74\n" \
  "s_waitcnt vmcnt(0)\n" \
  "s_barrier\n" \
  "LMc1_%=:\n" \
  "ds_read_b128 v[16:19], v1 offset:0\n" \
  "ds_read_b128 v[20:23], v1 offset:1024\n" \
  "ds_read_b128 v[24:27], v1 offset:2048\n" \
  "ds_read_b128 v[28:31], v1 offset:3072\n" \
  "ds_read_b128 v[32:35], v1 offset:4096\n" \
  "ds_read_b128 v[36:39], v1 offset:5120\n" \
  "ds_read_b128 v[40:43], v1 offset:6144\n" \
  "ds_read_b128 v[44:47], v1 offset:7168\n" \
  "ds_read_b128 v[48:51], v1 offset:8192\n" \
  "ds_read_b128 v[52:55], v1 offset:9216\n" \
  "s_waitcnt lgkmcnt(9)\n" \
  "v_smfmac_f32_16x16x64_bf16 v[56:59], v[16:19], a[0:7], v0\n" \
  "s_waitcnt lgkmcnt(8)\n" \
  "v_smfmac_f32_16x16x64_bf16 v[56:59], v[20:23], a[8:15], v0\n" \
  "s_waitcnt lgkmcnt(7)\n" \
  "v_smfmac_f32_16x16x64_bf16 v[56:59], v[24:27], a[16:23], v0\n" \
  "s_waitcnt lgkmcnt(6)\n" \
  "v_smfmac_f32_16x16x64_bf16 v[56:59], v[28:31], a[24:31], v0\n" \
  "s_waitcnt lgkmcnt(5)\n" \
  "v_smfmac_f32_16x16x64_bf16 v[56:59], v[32:35], a[32:39], v0\n" \
  "s_waitcnt lgkmcnt(4)\n" \
  "v_smfmac_f32_16x16x64_bf16 v[56:59], v[36:39], a[40:47], v0\n" \
  "s_waitcnt lgkmcnt(3)\n" \
  "v_smfmac_f32_16x16x64_bf16 v[56:59], v[40:43], a[48:55], v0\n" \
  "s_waitcnt lgkmcnt(2)\n" \
  "v_smfmac_f32_16x16x64_bf16 v[56:59], v[44:47], a[56:63], v0\n" \
  "s_waitcnt lgkmcnt(1)\n" \
  "v_smfmac_f32_16x16x64_bf16 v[56:59], v[48:51], a[64:71], v0\n" \
  "s_waitcnt lgkmcnt(0)\n" \
  "v_smfmac_f32_16x16x64_bf16 v[56:59], v[52:55], a[72:79], v0\n" \
  "v_smfmac_f32_16x16x64_bf16 v[60:63], v[16:19], a[80:87], v0\n" \
  "v_smfmac_f32_16x16x64_bf16 v[64:67], v[16:19], a[160:167], v0\n" \
  "v_cmp_u_f32 vcc, v56, v57\n" \
  "v_cmp_u_f32 s[82:83], v58, v59\n" \
  "s_nop 1\n" \
  "s_or_b64 vcc, vcc, s[82:83]\n" \
  "s_cbranch_scc1 LMFc1_%=\n" \
  "LMOKc1_%=:\n" \
  "v_smfmac_f32_16x16x64_bf16 v[68:71], v[16:19], a[240:247], v0\n" \
  "v_smfmac_f32_16x16x64_bf16 v[72:75], v[16:19], v[176:183], v0\n" \
  "v_smfmac_f32_16x16x64_bf16 v[60:63], v[20:23], a[88:95], v0\n" \
  "v_smfmac_f32_16x16x64_bf16 v[64:67], v[20:23], a[168:175], v0\n" \
  "v_smfmac_f32_16x16x64_bf16 v[68:71], v[20:23], a[248:255], v0\n" \
  "v_smfmac_f32_16x16x64_bf16 v[72:75], v[20:23], v[184:191], v0\n" \
  "v_smfmac_f32_16x16x64_bf16 v[60:63], v[24:27], a[96:103], v0\n" \
  "v_smfmac_f32_16x16x64_bf16 v[64:67], v[24:27], a[176:183], v0\n" \
  "v_smfmac_f32_16x16x64_bf16 v[68:71], v[24:27], v[112:119], v0\n" \
  "v_smfmac_f32_16x16x64_bf16 v[72:75], v[24:27], v[192:199], v0\n" \
  "v_smfmac_f32_16x16x64_bf16 v[60:63], v[28:31], a[104:111], v0\n" \
  "v_smfmac_f32_16x16x64_bf16 v[64:67], v[28:31], a[184:191], v0\n" \
  "v_smfmac_f32_16x16x64_bf16 v[68:71], v[28:31], v[120:127], v0\n" \
  "v_smfmac_f32_16x16x64_bf16 v[72:75], v[28:31], v[200:207], v0\n" \
  "v_smfmac_f32_16x16x64_bf16 v[60:63], v[32:35], a[112:119], v0\n" \
  "v_smfmac_f32_16x16x64_bf16 v[64:67], v[32:35], a[192:199], v0\n" \
  "v_smfmac_f32_16x16x64_bf16 v[68:71], v[32:35], v[128:135], v0\n" \
  "v_smfmac_f32_16x16x64_bf16 v[72:75], v[32:35], v[208:215], v0\n" \
  "v_smfmac_f32_16x16x64_bf16 v[60:63], v[36:39], a[120:127], v0\n" \
  "v_smfmac_f32_16x16x64_bf16 v[64:67], v[36:39], a[200:207], v0\n" \
  "v_smfmac_f32_16x16x64_bf16 v[68:71], v[36:39], v[136:143], v0\n" \
  "v_smfmac_f32_16x16x64_bf16 v[72:75], v[36:39], v[216:223], v0\n" \
  "v_smfmac_f32_16x16x64_bf16 v[60:63], v[40:43], a[128:135], v0\n" \
  "v_smfmac_f32_16x16x64_bf16 v[64:67], v[40:43], a[208:215], v0\n" \
  "v_smfmac_f32_16x16x64_bf16 v[68:71], v[40:43], v[144:151], v0\n" \
  "v_smfmac_f32_16x16x64_bf16 v[72:75], v[40:43], v[224:231], v0\n" \
  "v_smfmac_f32_16x16x64_bf16 v[60:63], v[44:47], a[136:143], v0\n" \
  "v_smfmac_f32_16x16x64_bf16 v[64:67], v[44:47], a[216:223], v0\n" \
  "v_smfmac_f32_16x16x64_bf16 v[68:71], v[44:47], v[152:159], v0\n" \
  "v_smfmac_f32_16x16x64_bf16 v[72:75], v[44:47], v[232:239], v0\n" \
  "v_smfmac_f32_16x16x64_bf16 v[60:63], v[48:51], a[144:151], v0\n" \
  "v_smfmac_f32_16x16x64_bf16 v[64:67], v[48:51], a[224:231], v0\n" \
  "v_smfmac_f32_16x16x64_bf16 v[68:71], v[48:51], v[160:167], v0\n" \
  "v_smfmac_f32_16x16x64_bf16 v[72:75], v[48:51], v[240:247], v0\n" \
  "v_smfmac_f32_16x16x64_bf16 v[60:63], v[52:55], a[152:159], v0\n" \
  "v_smfmac_f32_16x16x64_bf16 v[64:67], v[52:55], a[232:239], v0\n" \
  "v_smfmac_f32_16x16x64_bf16 v[68:71], v[52:55], v[168:175], v0\n" \
  "v_smfmac_f32_16x16x64_bf16 v[72:75], v[52:55], v[248:255], v0\n" \
  "v_permlane32_swap_b32 v56, v60\n" \
  "v_permlane32_swap_b32 v57, v61\n" \
  "v_permlane32_swap_b32 v58, v62\n" \
  "v_permlane32_swap_b32 v59, v63\n" \
  "v_pk_add_f32 v[56:57], v[56:57], v[60:61]\n" \
  "v_pk_add_f32 v[58:59], v[58:59], v[62:63]\n" \
  "v_cvt_pk_bf16_f32 v56, v56, v57\n" \
  "v_cvt_pk_bf16_f32 v57, v58, v59\n" \
  "ds_write_b64 v2, v[56:57] offset:1280\n" \
  "v_permlane32_swap_b32 v64, v68\n" \
  "v_permlane32_swap_b32 v65, v69\n" \
  "v_permlane32_swap_b32 v66, v70\n" \
  "v_permlane32_swap_b32 v67, v71\n" \
  "v_pk_add_f32 v[64:65], v[64:65], v[68:69]\n" \
  "v_pk_add_f32 v[66:67], v[66:67], v[70:71]\n" \
  "v_cvt_pk_bf16_f32 v64, v64, v65\n" \
  "v_cvt_pk_bf16_f32 v65, v66, v67\n" \
  "ds_write_b64 v2, v[64:65] offset:1792\n" \
  "v_permlane32_swap_b32 v72, v74\n" \
  "v_permlane32_swap_b32 v73, v75\n" \
  "v_pk_add_f32 v[72:73], v[72:73], v[74:75]\n" \
  "v_cvt_pk_bf16_f32 v72, v72, v73\n" \
  "ds_write_b32 v3, v72 offset:2304\n" \
  "s_waitcnt lgkmcnt(0)\n" \
  "s_barrier\n" \
  "s_mov_b32 s44, s45\n" \
  "s_barrier\n" \
  "s_cmp_eq_u32 s68, 64\n" \
  "s_cbranch_scc0 LOOPc1_%=\n" \
  "s_branch LDONE_%=\n" \
  "LMFc1_%=:\n" \
  "v_max_u32 v92, v16, v17\n" \
  "v_max3_u32 v92, v92, v18, v19\n" \
  "v_max3_u32 v92, v92, v20, v21\n" \
  "v_max3_u32 v92, v92, v22, v23\n" \
  "v_max3_u32 v92, v92, v24, v25\n" \
  "v_max3_u32 v92, v92, v26, v27\n" \
  "v_max3_u32 v92, v92, v28, v29\n" \
  "v_max3_u32 v92, v92, v30, v31\n" \
  "v_max3_u32 v92, v92, v32, v33\n" \
  "v_max3_u32 v92, v92, v34, v35\n" \
  "v_max3_u32 v92, v92, v36, v37\n" \
  "v_max3_u32 v92, v92, v38, v39\n" \
  "v_max3_u32 v92, v92, v40, v41\n" \
  "v_max3_u32 v92, v92, v42, v43\n" \
  "v_max3_u32 v92, v92, v44, v45\n" \
  "v_max3_u32 v92, v92, v46, v47\n" \
  "v_max3_u32 v92, v92, v48, v49\n" \
  "v_max3_u32 v92, v92, v50, v51\n" \
  "v_max3_u32 v92, v92, v52, v53\n" \
  "v_max3_u32 v92, v92, v54, v55\n" \
  "v_cmp_eq_u32 vcc, -1, v92\n" \
  "s_nop 3\n" \
  "s_cbranch_vccz LMOKc1_%=\n" \
  "s_cmp_gt_u32 s57, 20000\n" \
  "s_cbranch_scc1 LMOKc1_%=\n" \
  "s_add_u32 s57, s57, 1\n" \
  "s_barrier\n" \
  "buffer_load_dwordx4 v1, s[36:39], s75 offen offset:0 sc1 lds\n" \
  "buffer_load_dwordx4 v1, s[36:39], s75 offen offset:1024 sc1 lds\n" \
  "buffer_load_dwordx4 v1, s[36:39], s75 offen offset:2048 sc1 lds\n" \
  "buffer_load_dwordx4 v1, s[36:39], s75 offen offset:3072 sc1 lds\n" \
  "v_mov_b32 v56, 0\n" \
  "v_mov_b32 v57, 0\n" \
  "v_mov_b32 v58, 0\n" \
  "v_mov_b32 v59, 0\n" \
  "v_mov_b32 v60, 0\n" \
  "v_mov_b32 v61, 0\n" \
  "v_mov_b32 v62, 0\n" \
  "v_mov_b32 v63, 0\n" \
  "v_mov_b32 v64, 0\n" \
  "v_mov_b32 v65, 0\n" \
  "v_mov_b32 v66, 0\n" \
  "v_mov_b32 v67, 0\n" \
  "v_mov_b32 v68, 0\n" \
  "v_mov_b32 v69, 0\n" \
  "v_mov_b32 v70, 0\n" \
  "v_mov_b32 v71, 0\n" \
  "v_mov_b32 v72, 0\n" \
  "v_mov_b32 v73, 0\n" \
  "v_mov_b32 v74, 0\n" \
  "v_mov_b32 v75, 0\n" \
  "s_waitcnt vmcnt(0)\n" \
  "s_barrier\n" \
  "s_branch LMc1_%=\n" \
  "LSTARTc2_%=:\n" \
  "s_mov_b32 s57, 0\n" \
  "v_readlane_b32 s44, v4, 0\n" \
  "s_nop 3\n" \
  "LOOPc2_%=:\n" \
  "s_mov_b32 m0, 8192\n" \
  "s_add_u32 s75, s44, 16384\n" \
  "s_barrier\n" \
  "buffer_load_dwordx4 v1, s[36:39], s75 offen offset:0 sc1 lds\n" \
  "buffer_load_dwordx4 v1, s[36:39], s75 offen offset:1024 sc1 lds\n" \
  "v_mov_b32 v56, 0\n" \
  "v_mov_b32 v57, 0\n" \
  "v_mov_b32 v58, 0\n" \
  "v_mov_b32 v59, 0\n" \
  "v_mov_b32 v60, 0\n" \
  "v_mov_b32 v61, 0\n" \
  "v_mov_b32 v62, 0\n" \
  "v_mov_b32 v63, 0\n" \
  "v_mov_b32 v64, 0\n" \
  "v_mov_b32 v65, 0\n" \
  "v_mov_b32 v66, 0\n" \
  "v_mov_b32 v67, 0\n" \
  "v_mov_b32 v68, 0\n" \
  "v_mov_b32 v69, 0\n" \
  "v_mov_b32 v70, 0\n" \
  "v_mov_b32 v71, 0\n" \
  "v_mov_b32 v72, 0\n" \
  "v_mov_b32 v73, 0\n" \
  "v_mov_b32 v74, 0\n" \
  "v_mov_b32 v75, 0\n" \
  "s_add_u32 s68, s68, 1\n" \
  "s_min_u32 s74, s68, 63\n" \
  "v_readlane_b32 s45, v4, s74\n" \
  "s_waitcnt vmcnt(0)\n" \
  "s_barrier\n" \
  "LMc2_%=:\n" \
  "ds_read_b128 v[16:19], v1 offset:0\n" \
  "ds_read_b128 v[20:23], v1 offset:1024\n" \
  "ds_read_b128 v[24:27], v1 offset:2048\n" \
  "ds_read_b128 v[28:31], v1 offset:3072\n" \
  "ds_read_b128 v[32:35], v1 offset:4096\n" \
  "ds_read_b128 v[36:39], v1 offset:5120\n" \
  "ds_read_b128 v[40:43], v1 offset:6144\n" \
  "ds_read_b128 v[44:47], v1 offset:7168\n" \
  "ds_read_b128 v[48:51], v1 offset:8192\n" \
  "ds_read_b128 v[52:55], v1 offset:9216\n" \
  "s_waitcnt lgkmcnt(9)\n" \
  "v_smfmac_f32_16x16x64_bf16 v[56:59], v[16:19], a[0:7], v0\n" \
  "s_waitcnt lgkmcnt(8)\n" \
  "v_smfmac_f32_16x16x64_bf16 v[56:59], v[20:23], a[8:15], v0\n" \
  "s_waitcnt lgkmcnt(7)\n" \
  "v_smfmac_f32_16x16x64_bf16 v[56:59], v[24:27], a[16:23], v0\n" \
  "s_waitcnt lgkmcnt(6)\n" \
  "v_smfmac_f32_16x16x64_bf16 v[56:59], v[28:31], a[24:31], v0\n" \
  "s_waitcnt lgkmcnt(5)\n" \
  "v_smfmac_f32_16x16x64_bf16 v[56:59], v[32:35], a[32:39], v0\n" \
  "s_waitcnt lgkmcnt(4)\n" \
  "v_smfmac_f32_16x16x64_bf16 v[56:59], v[36:39], a[40:47], v0\n" \
  "s_waitcnt lgkmcnt(3)\n" \
  "v_smfmac_f32_16x16x64_bf16 v[56:59], v[40:43], a[48:55], v0\n" \
  "s_waitcnt lgkmcnt(2)\n" \
  "v_smfmac_f32_16x16x64_bf16 v[56:59], v[44:47], a[56:63], v0\n" \
  "s_waitcnt lgkmcnt(1)\n" \
  "v_smfmac_f32_16x16x64_bf16 v[56:59], v[48:51], a[64:71], v0\n" \
  "s_waitcnt lgkmcnt(0)\n" \
  "v_smfmac_f32_16x16x64_bf16 v[56:59], v[52:55], a[72:79], v0\n" \
  "v_smfmac_f32_16x16x64_bf16 v[60:63], v[16:19], a[80:87], v0\n" \
  "v_smfmac_f32_16x16x64_bf16 v[64:67], v[16:19], a[160:167], v0\n" \
  "v_cmp_u_f32 vcc, v56, v57\n" \
  "v_cmp_u_f32 s[82:83], v58, v59\n" \
  "s_nop 1\n" \
  "s_or_b64 vcc, vcc, s[82:83]\n" \
  "s_cbranch_scc1 LMFc2_%=\n" \
  "LMOKc2_%=:\n" \
  "v_smfmac_f32_16x16x64_bf16 v[68:71], v[16:19], a[240:247], v0\n" \
  "v_smfmac_f32_16x16x64_bf16 v[72:75], v[16:19], v[176:183], v0\n" \
  "v_smfmac_f32_16x16x64_bf16 v[60:63], v[20:23], a[88:95], v0\n" \
  "v_smfmac_f32_16x16x64_bf16 v[64:67], v[20:23], a[168:175], v0\n" \
  "v_smfmac_f32_16x16x64_bf16 v[68:71], v[20:23], a[248:255], v0\n" \
  "v_smfmac_f32_16x16x64_bf16 v[72:75], v[20:23], v[184:191], v0\n" \
  "v_smfmac_f32_16x16x64_bf16 v[60:63], v[24:27], a[96:103], v0\n" \
  "v_smfmac_f32_16x16x64_bf16 v[64:67], v[24:27], a[176:183], v0\n" \
  "v_smfmac_f32_16x16x64_bf16 v[68:71], v[24:27], v[112:119], v0\n" \
  "v_smfmac_f32_16x16x64_bf16 v[72:75], v[24:27], v[192:199], v0\n" \
  "v_smfmac_f32_16x16x64_bf16 v[60:63], v[28:31], a[104:111], v0\n" \
  "v_smfmac_f32_16x16x64_bf16 v[64:67], v[28:31], a[184:191], v0\n" \
  "v_smfmac_f32_16x16x64_bf16 v[68:71], v[28:31], v[120:127], v0\n" \
  "v_smfmac_f32_16x16x64_bf16 v[72:75], v[28:31], v[200:207], v0\n" \
  "v_smfmac_f32_16x16x64_bf16 v[60:63], v[32:35], a[112:119], v0\n" \
  "v_smfmac_f32_16x16x64_bf16 v[64:67], v[32:35], a[192:199], v0\n" \
  "v_smfmac_f32_16x16x64_bf16 v[68:71], v[32:35], v[128:135], v0\n" \
  "v_smfmac_f32_16x16x64_bf16 v[72:75], v[32:35], v[208:215], v0\n" \
  "v_smfmac_f32_16x16x64_bf16 v[60:63], v[36:39], a[120:127], v0\n" \
  "v_smfmac_f32_16x16x64_bf16 v[64:67], v[36:39], a[200:207], v0\n" \
  "v_smfmac_f32_16x16x64_bf16 v[68:71], v[36:39], v[136:143], v0\n" \
  "v_smfmac_f32_16x16x64_bf16 v[72:75], v[36:39], v[216:223], v0\n" \
  "v_smfmac_f32_16x16x64_bf16 v[60:63], v[40:43], a[128:135], v0\n" \
  "v_smfmac_f32_16x16x64_bf16 v[64:67], v[40:43], a[208:215], v0\n" \
  "v_smfmac_f32_16x16x64_bf16 v[68:71], v[40:43], v[144:151], v0\n" \
  "v_smfmac_f32_16x16x64_bf16 v[72:75], v[40:43], v[224:231], v0\n" \
  "v_smfmac_f32_16x16x64_bf16 v[60:63], v[44:47], a[136:143], v0\n" \
  "v_smfmac_f32_16x16x64_bf16 v[64:67], v[44:47], a[216:223], v0\n" \
  "v_smfmac_f32_16x16x64_bf16 v[68:71], v[44:47], v[152:159], v0\n" \
  "v_smfmac_f32_16x16x64_bf16 v[72:75], v[44:47], v[232:239], v0\n" \
  "v_smfmac_f32_16x16x64_bf16 v[60:63], v[48:51], a[144:151], v0\n" \
  "v_smfmac_f32_16x16x64_bf16 v[64:67], v[48:51], a[224:231], v0\n" \
  "v_smfmac_f32_16x16x64_bf16 v[68:71], v[48:51], v[160:167], v0\n" \
  "v_smfmac_f32_16x16x64_bf16 v[72:75], v[48:51], v[240:247], v0\n" \
  "v_smfmac_f32_16x16x64_bf16 v[60:63], v[52:55], a[152:159], v0\n" \
  "v_smfmac_f32_16x16x64_bf16 v[64:67], v[52:55], a[232:239], v0\n" \
  "v_smfmac_f32_16x16x64_bf16 v[68:71], v[52:55], v[168:175], v0\n" \
  "v_smfmac_f32_16x16x64_bf16 v[72:75], v[52:55], v[248:255], v0\n" \
  "v_permlane32_swap_b32 v56, v60\n" \
  "v_permlane32_swap_b32 v57, v61\n" \
  "v_permlane32_swap_b32 v58, v62\n" \
  "v_permlane32_swap_b32 v59, v63\n" \
  "v_pk_add_f32 v[56:57], v[56:57], v[60:61]\n" \
  "v_pk_add_f32 v[58:59], v[58:59], v[62:63]\n" \
  "v_cvt_pk_bf16_f32 v56, v56, v57\n" \
  "v_cvt_pk_bf16_f32 v57, v58, v59\n" \
  "ds_write_b64 v2, v[56:57] offset:2560\n" \
  "v_permlane32_swap_b32 v64, v68\n" \
  "v_permlane32_swap_b32 v65, v69\n" \
  "v_permlane32_swap_b32 v66, v70\n" \
  "v_permlane32_swap_b32 v67, v71\n" \
  "v_pk_add_f32 v[64:65], v[64:65], v[68:69]\n" \
  "v_pk_add_f32 v[66:67], v[66:67], v[70:71]\n" \
  "v_cvt_pk_bf16_f32 v64, v64, v65\n" \
  "v_cvt_pk_bf16_f32 v65, v66, v67\n" \
  "ds_write_b64 v2, v[64:65] offset:3072\n" \
  "v_permlane32_swap_b32 v72, v74\n" \
  "v_permlane32_swap_b32 v73, v75\n" \
  "v_pk_add_f32 v[72:73], v[72:73], v[74:75]\n" \
  "v_cvt_pk_bf16_f32 v72, v72, v73\n" \
  "ds_write_b32 v3, v72 offset:3584\n" \
  "s_waitcnt lgkmcnt(0)\n" \
  "s_barrier\n" \
  "s_mov_b32 s44, s45\n" \
  "s_barrier\n" \
  "s_cmp_eq_u32 s68, 64\n" \
  "s_cbranch_scc0 LOOPc2_%=\n" \
  "s_branch LDONE_%=\n" \
  "LMFc2_%=:\n" \
  "v_max_u32 v92, v16, v17\n" \
  "v_max3_u32 v92, v92, v18, v19\n" \
  "v_max3_u32 v92, v92, v20, v21\n" \
  "v_max3_u32 v92, v92, v22, v23\n" \
  "v_max3_u32 v92, v92, v24, v25\n" \
  "v_max3_u32 v92, v92, v26, v27\n" \
  "v_max3_u32 v92, v92, v28, v29\n" \
  "v_max3_u32 v92, v92, v30, v31\n" \
  "v_max3_u32 v92, v92, v32, v33\n" \
  "v_max3_u32 v92, v92, v34, v35\n" \
  "v_max3_u32 v92, v92, v36, v37\n" \
  "v_max3_u32 v92, v92, v38, v39\n" \
  "v_max3_u32 v92, v92, v40, v41\n" \
  "v_max3_u32 v92, v92, v42, v43\n" \
  "v_max3_u32 v92, v92, v44, v45\n" \
  "v_max3_u32 v92, v92, v46, v47\n" \
  "v_max3_u32 v92, v92, v48, v49\n" \
  "v_max3_u32 v92, v92, v50, v51\n" \
  "v_max3_u32 v92, v92, v52, v53\n" \
  "v_max3_u32 v92, v92, v54, v55\n" \
  "v_cmp_eq_u32 vcc, -1, v92\n" \
  "s_nop 3\n" \
  "s_cbranch_vccz LMOKc2_%=\n" \
  "s_cmp_gt_u32 s57, 20000\n" \
  "s_cbranch_scc1 LMOKc2_%=\n" \
  "s_add_u32 s57, s57, 1\n" \
  "s_barrier\n" \
  "buffer_load_dwordx4 v1, s[36:39], s75 offen offset:0 sc1 lds\n" \
  "buffer_load_dwordx4 v1, s[36:39], s75 offen offset:1024 sc1 lds\n" \
  "v_mov_b32 v56, 0\n" \
  "v_mov_b32 v57, 0\n" \
  "v_mov_b32 v58, 0\n" \
  "v_mov_b32 v59, 0\n" \
  "v_mov_b32 v60, 0\n" \
  "v_mov_b32 v61, 0\n" \
  "v_mov_b32 v62, 0\n" \
  "v_mov_b32 v63, 0\n" \
  "v_mov_b32 v64, 0\n" \
  "v_mov_b32 v65, 0\n" \
  "v_mov_b32 v66, 0\n" \
  "v_mov_b32 v67, 0\n" \
  "v_mov_b32 v68, 0\n" \
  "v_mov_b32 v69, 0\n" \
  "v_mov_b32 v70, 0\n" \
  "v_mov_b32 v71, 0\n" \
  "v_mov_b32 v72, 0\n" \
  "v_mov_b32 v73, 0\n" \
  "v_mov_b32 v74, 0\n" \
  "v_mov_b32 v75, 0\n" \
  "s_waitcnt vmcnt(0)\n" \
  "s_barrier\n" \
  "s_branch LMc2_%=\n" \
  "LSTARTc3_%=:\n" \
  "v_mov_b32 v96, v16\n" \
  "v_mov_b32 v97, v17\n" \
  "v_mov_b32 v100, v18\n" \
  "s_memrealtime s[76:77]\n" \
  "s_memtime s[78:79]\n" \
  "s_waitcnt lgkmcnt(0)\n" \
  "s_mov_b32 s88, s76\n" \
  "s_mov_b32 s89, s78\n" \
  "LQUc3_%=:\n" \
  "buffer_load_dword v92, v14, s[36:39], 0 offen sc1\n" \
  "s_waitcnt vmcnt(0)\n" \
  "v_cmp_gt_u32 vcc, v15, v92\n" \
  "s_nop 3\n" \
  "s_cbranch_vccz LRUc3_%=\n" \
  "s_add_u32 s73, s73, 1\n" \
  "s_cmp_gt_u32 s73, 20000\n" \
  "s_cbranch_scc1 LRUc3_%=\n" \
  "s_sleep 8\n" \
  "s_branch LQUc3_%=\n" \
  "LRUc3_%=:\n" \
  "s_barrier\n" \
  "v_mov_b32 v15, 1\n" \
  "s_mov_b32 s56, s38\n" \
  "s_mov_b32 s38, 0\n" \
  "s_mov_b32 s73, 20001\n" \
  "s_mov_b32 s57, 20001\n" \
  "s_getpc_b64 s[82:83]\n" \
  "LPCEc3_%=:\n" \
  "s_add_u32 s84, s82, LEEc3_%=-LPCEc3_%=+24\n" \
  "s_addc_u32 s85, s83, 0\n" \
  "s_getpc_b64 s[82:83]\n" \
  "LPCLc3_%=:\n" \
  "s_add_u32 s90, s82, LELc3_%=-LPCLc3_%=+24\n" \
  "s_addc_u32 s91, s83, 0\n" \
  "v_mov_b32 v92, 0\n" \
  "s_memrealtime s[76:77]\n" \
  "s_memtime s[78:79]\n" \
  "s_waitcnt lgkmcnt(0)\n" \
  "s_lshl_b32 s48, s76, 2\n" \
  "v_readlane_b32 s72, v92, 0\n" \
  "s_nop 1\n" \
  "s_sub_u32 s74, s72, s48\n" \
  "s_add_u32 s74, s74, 40000\n" \
  "s_cmp_gt_u32 s74, 80000\n" \
  "s_cselect_b32 s72, s48, s72\n" \
  "v_readlane_b32 s94, v92, 1\n" \
  "s_nop 1\n" \
  "s_sub_u32 s74, s94, s48\n" \
  "s_add_u32 s74, s74, 40000\n" \
  "s_cmp_gt_u32 s74, 80000\n" \
  "s_cselect_b32 s94, s48, s94\n" \
  "v_readlane_b32 s95, v92, 2\n" \
  "s_nop 1\n" \
  "s_sub_u32 s74, s95, s48\n" \
  "s_add_u32 s74, s74, 40000\n" \
  "s_cmp_gt_u32 s74, 80000\n" \
  "s_cselect_b32 s95, s48, s95\n" \
  "s_sub_u32 s74, s78, s89\n" \
  "s_sub_u32 s75, s76, s88\n" \
  "v_cvt_f32_u32 v92, s74\n" \
  "v_cvt_f32_u32 v93, s75\n" \
  "v_rcp_f32 v93, v93\n" \
  "v_mul_f32 v92, 0x43820000, v92\n" \
  "v_mul_f32 v92, v92, v93\n" \
  "v_cvt_u32_f32 v92, v92\n" \
  "s_nop 0\n" \
  "v_readfirstlane_b32 s74, v92\n" \
  "s_min_u32 s74, s74, 6400\n" \
  "s_cmp_gt_u32 s75, 150\n" \
  "s_cselect_b32 s50, s74, s50\n" \
  "s_cselect_b32 s88, s76, s88\n" \
  "s_cselect_b32 s89, s78, s89\n" \
  "s_add_u32 s49, s78, 0x1000000\n" \
  "s_mul_i32 s86, s54, 6\n" \
  "s_mov_b32 s87, -200\n" \
  "s_mov_b32 s51, 0\n" \
  "s_mov_b32 s96, 0\n" \
  "s_mul_i32 s74, s51, s54\n" \
  "s_add_u32 s74, s74, s72\n" \
  "s_sub_i32 s75, s74, s48\n" \
  "s_mov_b32 s48, s74\n" \
  "s_mul_i32 s75, s75, s50\n" \
  "s_ashr_i32 s75, s75, 10\n" \
  "s_add_u32 s49, s49, s75\n" \
  "s_mov_b32 s87, 0x80000000\n" \
  "v_accvgpr_write_b32 a0, -1\n" \
  "s_branch LOOPc3_%=\n" \
  "LPASSc3_%=:\n" \
  "s_waitcnt vmcnt(0)\n" \
  "s_mov_b32 s38, s56\n" \
  "s_mov_b32 s68, 0\n" \
  "s_mov_b32 s73, 0\n" \
  "s_mov_b32 s58, 0\n" \
  "s_mov_b32 s59, 0\n" \
  "v_readlane_b32 s46, v6, 0\n" \
  "s_nop 3\n" \
  "v_readlane_b32 s47, v6, 1\n" \
  "buffer_load_dwordx4 a[0:3], v100, s[36:39], s46 offen sc1\n" \
  "s_nop 3\n" \
  "v_readlane_b32 s46, v6, 2\n" \
  "buffer_load_dwordx4 a[4:7], v100, s[36:39], s47 offen sc1\n" \
  "s_nop 3\n" \
  "v_readlane_b32 s47, v6, 3\n" \
  "buffer_load_dwordx4 a[8:11], v100, s[36:39], s46 offen sc1\n" \
  "s_nop 3\n" \
  "v_readlane_b32 s46, v6, 4\n" \
  "buffer_load_dwordx4 a[12:15], v100, s[36:39], s47 offen sc1\n" \
  "s_nop 3\n" \
  "v_readlane_b32 s47, v6, 5\n" \
  "buffer_load_dwordx4 a[16:19], v100, s[36:39], s46 offen sc1\n" \
  "s_nop 3\n" \
  "v_readlane_b32 s46, v6, 6\n" \
  "buffer_load_dwordx4 a[20:23], v100, s[36:39], s47 offen sc1\n" \
  "s_nop 3\n" \
  "v_readlane_b32 s47, v6, 7\n" \
  "buffer_load_dwordx4 a[24:27], v100, s[36:39], s46 offen sc1\n" \
  "s_nop 3\n" \
  "v_readlane_b32 s46, v6, 8\n" \
  "buffer_load_dwordx4 a[28:31], v100, s[36:39], s47 offen sc1\n" \
  "s_nop 3\n" \
  "v_readlane_b32 s47, v6, 9\n" \
  "buffer_load_dwordx4 a[32:35], v100, s[36:39], s46 offen sc1\n" \
  "s_nop 3\n" \
  "v_readlane_b32 s46, v6, 10\n" \
  "buffer_load_dwordx4 a[36:39], v100, s[36:39], s47 offen sc1\n" \
  "s_nop 3\n" \
  "v_readlane_b32 s47, v6, 11\n" \
  "buffer_load_dwordx4 a[40:43], v100, s[36:39], s46 offen sc1\n" \
  "s_nop 3\n" \
  "v_readlane_b32 s46, v6, 12\n" \
  "buffer_load_dwordx4 a[44:47], v100, s[36:39], s47 offen sc1\n" \
  "s_nop 3\n" \
  "v_readlane_b32 s47, v6, 13\n" \
  "buffer_load_dwordx4 a[48:51], v100, s[36:39], s46 offen sc1\n" \
  "s_nop 3\n" \
  "v_readlane_b32 s46, v6, 14\n" \
  "buffer_load_dwordx4 a[52:55], v100, s[36:39], s47 offen sc1\n" \
  "s_nop 3\n" \
  "v_readlane_b32 s47, v6, 15\n" \
  "buffer_load_dwordx4 a[56:59], v100, s[36:39], s46 offen sc1\n" \
  "s_nop 3\n" \
  "v_readlane_b32 s46, v6, 16\n" \
  "buffer_load_dwordx4 a[60:63], v100, s[36:39], s47 offen sc1\n" \
  "s_nop 3\n" \
  "v_readlane_b32 s47, v6, 17\n" \
  "buffer_load_dwordx4 a[64:67], v100, s[36:39], s46 offen sc1\n" \
  "s_nop 3\n" \
  "v_readlane_b32 s46, v6, 18\n" \
  "buffer_load_dwordx4 a[68:71], v100, s[36:39], s47 offen sc1\n" \
  "s_nop 3\n" \
  "v_readlane_b32 s47, v6, 19\n" \
  "buffer_load_dwordx4 a[72:75], v100, s[36:39], s46 offen sc1\n" \
  "s_nop 3\n" \
  "buffer_load_dwordx4 a[76:79], v100, s[36:39], s47 offen sc1\n" \
  "s_nop 3\n" \
  "LQAc3_%=:\n" \
  "buffer_load_dword v92, v8, s[36:39], 0 offen sc1\n" \
  "s_waitcnt vmcnt(0)\n" \
  "v_cmp_eq_u32 vcc, 0, v92\n" \
  "s_nop 3\n" \
  "s_cbranch_vccz LRAc3_%=\n" \
  "s_add_u32 s73, s73, 1\n" \
  "s_cmp_gt_u32 s73, 20000\n" \
  "s_cbranch_scc1 LRAc3_%=\n" \
  "s_sleep 2\n" \
  "s_branch LQAc3_%=\n" \
  "LRAc3_%=:\n" \
  "s_memrealtime s[76:77]\n" \
  "s_memtime s[78:79]\n" \
  "s_waitcnt lgkmcnt(0)\n" \
  "s_lshl_b32 s48, s76, 2\n" \
  "v_readlane_b32 s72, v92, 0\n" \
  "s_nop 1\n" \
  "s_sub_u32 s74, s72, s48\n" \
  "s_add_u32 s74, s74, 40000\n" \
  "s_cmp_gt_u32 s74, 80000\n" \
  "s_cselect_b32 s72, s48, s72\n" \
  "v_readlane_b32 s94, v92, 1\n" \
  "s_nop 1\n" \
  "s_sub_u32 s74, s94, s48\n" \
  "s_add_u32 s74, s74, 40000\n" \
  "s_cmp_gt_u32 s74, 80000\n" \
  "s_cselect_b32 s94, s48, s94\n" \
  "v_readlane_b32 s95, v92, 2\n" \
  "s_nop 1\n" \
  "s_sub_u32 s74, s95, s48\n" \
  "s_add_u32 s74, s74, 40000\n" \
  "s_cmp_gt_u32 s74, 80000\n" \
  "s_cselect_b32 s95, s48, s95\n" \
  "s_sub_u32 s74, s78, s89\n" \
  "s_sub_u32 s75, s76, s88\n" \
  "v_cvt_f32_u32 v92, s74\n" \
  "v_cvt_f32_u32 v93, s75\n" \
  "v_rcp_f32 v93, v93\n" \
  "v_mul_f32 v92, 0x43820000, v92\n" \
  "v_mul_f32 v92, v92, v93\n" \
  "v_cvt_u32_f32 v92, v92\n" \
  "s_nop 0\n" \
  "v_readfirstlane_b32 s74, v92\n" \
  "s_min_u32 s74, s74, 6400\n" \
  "s_cmp_gt_u32 s75, 150\n" \
  "s_cselect_b32 s50, s74, s50\n" \
  "s_cselect_b32 s88, s76, s88\n" \
  "s_cselect_b32 s89, s78, s89\n" \
  "s_add_u32 s49, s78, 0x1000000\n" \
  "s_mul_i32 s86, s54, 6\n" \
  "s_mov_b32 s87, -200\n" \
  "s_mov_b32 s51, 0\n" \
  "s_mov_b32 s96, 0\n" \
  "s_mov_b32 s57, 0\n" \
  "v_readlane_b32 s44, v4, 0\n" \
  "s_nop 3\n" \
  "LOOPc3_%=:\n" \
  "LZWc3_%=:\n" \
  "s_memrealtime s[76:77]\n" \
  "s_memtime s[78:79]\n" \
  "s_waitcnt lgkmcnt(0)\n" \
  "s_lshl_b32 s75, s76, 2\n" \
  "s_sub_i32 s75, s48, s75\n" \
  "s_mul_i32 s75, s75, s50\n" \
  "s_ashr_i32 s75, s75, 10\n" \
  "s_sub_i32 s74, s49, s78\n" \
  "s_min_i32 s74, s74, s75\n" \
  "s_sub_i32 s75, s75, 92\n" \
  "s_max_i32 s74, s74, s75\n" \
  "s_add_u32 s49, s74, s78\n" \
  "s_cmp_gt_i32 s74, 60\n" \
  "s_cbranch_scc1 LZWc3_%=\n" \
  "s_barrier\n" \
  "v_mov_b32 v56, 0\n" \
  "v_mov_b32 v57, 0\n" \
  "v_mov_b32 v58, 0\n" \
  "v_mov_b32 v59, 0\n" \
  "v_readlane_b32 s74, v9, s68\n" \
  "s_nop 0\n" \
  "s_add_u32 s46, s74, 0\n" \
  "s_add_u32 s47, s74, 4096\n" \
  "s_add_u32 s69, s74, 16384\n" \
  "s_add_u32 s70, s74, 20480\n" \
  "s_add_u32 s68, s68, 1\n" \
  "s_min_u32 s74, s68, 63\n" \
  "v_readlane_b32 s45, v4, s74\n" \
  "s_nop 15\n" \
  "buffer_load_dwordx4 a[80:83], v5, s[36:39], s46 offen offset:0 sc1\n" \
  "buffer_load_dwordx4 a[84:87], v5, s[36:39], s46 offen offset:1024 sc1\n" \
  "buffer_load_dwordx4 a[88:91], v5, s[36:39], s46 offen offset:2048 sc1\n" \
  "buffer_load_dwordx4 a[92:95], v5, s[36:39], s46 offen offset:3072 sc1\n" \
  "buffer_load_dwordx4 a[96:99], v5, s[36:39], s47 offen offset:0 sc1\n" \
  "buffer_load_dwordx4 a[100:103], v5, s[36:39], s47 offen offset:1024 sc1\n" \
  "buffer_load_dwordx4 a[104:107], v5, s[36:39], s47 offen offset:2048 sc1\n" \
  "buffer_load_dwordx4 a[108:111], v5, s[36:39], s47 offen offset:3072 sc1\n" \
  "buffer_load_dwordx4 a[112:115], v5, s[36:39], s69 offen offset:0 sc1\n" \
  "buffer_load_dwordx4 a[116:119], v5, s[36:39], s69 offen offset:1024 sc1\n" \
  "buffer_load_dwordx4 a[120:123], v5, s[36:39], s69 offen offset:2048 sc1\n" \
  "buffer_load_dwordx4 a[124:127], v5, s[36:39], s69 offen offset:3072 sc1\n" \
  "buffer_load_dwordx4 a[128:131], v5, s[36:39], s70 offen offset:0 sc1\n" \
  "buffer_load_dwordx4 a[132:135], v5, s[36:39], s70 offen offset:1024 sc1\n" \
  "buffer_load_dwordx4 a[136:139], v5, s[36:39], s70 offen offset:2048 sc1\n" \
  "buffer_load_dwordx4 a[140:143], v5, s[36:39], s70 offen offset:3072 sc1\n" \
  "buffer_load_dword v98, v97, s[36:39], s44 offen sc1\n" \
  "s_barrier\n" \
  "LMc3_%=:\n" \
  "ds_read_b128 v[16:19], v1 offset:0\n" \
  "ds_read_b128 v[20:23], v1 offset:1024\n" \
  "ds_read_b128 v[24:27], v1 offset:2048\n" \
  "ds_read_b128 v[28:31], v1 offset:3072\n" \
  "ds_read_b128 v[32:35], v1 offset:4096\n" \
  "ds_read_b128 v[36:39], v1 offset:5120\n" \
  "ds_read_b128 v[40:43], v1 offset:6144\n" \
  "ds_read_b128 v[44:47], v1 offset:7168\n" \
  "ds_read_b128 v[48:51], v1 offset:8192\n" \
  "ds_read_b128 v[52:55], v1 offset:9216\n" \
  "s_waitcnt lgkmcnt(9)\n" \
  "v_smfmac_f32_16x16x64_bf16 v[56:59], v[16:19], a[0:7], v0\n" \
  "s_waitcnt lgkmcnt(8)\n" \
  "v_smfmac_f32_16x16x64_bf16 v[56:59], v[20:23], a[8:15], v0\n" \
  "s_waitcnt lgkmcnt(7)\n" \
  "v_smfmac_f32_16x16x64_bf16 v[56:59], v[24:27], a[16:23], v0\n" \
  "s_waitcnt lgkmcnt(6)\n" \
  "v_smfmac_f32_16x16x64_bf16 v[56:59], v[28:31], a[24:31], v0\n" \
  "s_waitcnt lgkmcnt(5)\n" \
  "v_smfmac_f32_16x16x64_bf16 v[56:59], v[32:35], a[32:39], v0\n" \
  "s_waitcnt lgkmcnt(4)\n" \
  "v_smfmac_f32_16x16x64_bf16 v[56:59], v[36:39], a[40:47], v0\n" \
  "s_waitcnt lgkmcnt(3)\n" \
  "v_smfmac_f32_16x16x64_bf16 v[56:59], v[40:43], a[48:55], v0\n" \
  "s_waitcnt lgkmcnt(2)\n" \
  "v_smfmac_f32_16x16x64_bf16 v[56:59], v[44:47], a[56:63], v0\n" \
  "s_waitcnt lgkmcnt(1)\n" \
  "v_smfmac_f32_16x16x64_bf16 v[56:59], v[48:51], a[64:71], v0\n" \
  "s_waitcnt lgkmcnt(0)\n" \
  "v_smfmac_f32_16x16x64_bf16 v[56:59], v[52:55], a[72:79], v0\n" \
  "s_nop 7\n" \
  "v_cmp_u_f32 vcc, v56, v57\n" \
  "v_cmp_u_f32 s[82:83], v58, v59\n" \
  "s_nop 1\n" \
  "s_or_b64 vcc, vcc, s[82:83]\n" \
  "s_cbranch_scc1 LMFc3_%=\n" \
  "LMOKc3_%=:\n" \
  "v_permlane32_swap_b32 v56, v58\n" \
  "v_permlane32_swap_b32 v57, v59\n" \
  "v_pk_add_f32 v[56:57], v[56:57], v[58:59]\n" \
  "v_cvt_pk_bf16_f32 v56, v56, v57\n" \
  "ds_write_b32 v3, v56 offset:3840\n" \
  "s_waitcnt lgkmcnt(0)\n" \
  "s_cmp_eq_u32 s68, 64\n" \
  "s_cbranch_scc1 LENDc3_%=\n" \
  "s_add_u32 s46, s44, 32768\n" \
  "s_add_u32 s47, s44, 36864\n" \
  "s_add_u32 s69, s44, 49152\n" \
  "s_add_u32 s70, s44, 53248\n" \
  "s_sub_u32 s43, s45, s93\n" \
  "s_mul_i32 s74, s51, s54\n" \
  "s_add_u32 s74, s74, s94\n" \
  "s_sub_i32 s75, s74, s48\n" \
  "s_mov_b32 s48, s74\n" \
  "s_mul_i32 s75, s75, s50\n" \
  "s_ashr_i32 s75, s75, 10\n" \
  "s_add_u32 s49, s49, s75\n" \
  "s_memrealtime s[76:77]\n" \
  "s_memtime s[78:79]\n" \
  "s_waitcnt lgkmcnt(0)\n" \
  "s_lshl_b32 s75, s76, 2\n" \
  "s_sub_i32 s75, s48, s75\n" \
  "s_mul_i32 s75, s75, s50\n" \
  "s_ashr_i32 s75, s75, 10\n" \
  "s_sub_i32 s74, s49, s78\n" \
  "s_min_i32 s74, s74, s75\n" \
  "s_sub_i32 s75, s75, 92\n" \
  "s_max_i32 s74, s74, s75\n" \
  "s_add_u32 s49, s74, s78\n" \
  "s_sub_u32 s74, s78, s89\n" \
  "s_sub_u32 s75, s76, s88\n" \
  "v_cvt_f32_u32 v92, s74\n" \
  "v_cvt_f32_u32 v93, s75\n" \
  "v_rcp_f32 v93, v93\n" \
  "v_mul_f32 v92, 0x43820000, v92\n" \
  "v_mul_f32 v92, v92, v93\n" \
  "v_cvt_u32_f32 v92, v92\n" \
  "s_nop 0\n" \
  "v_readfirstlane_b32 s74, v92\n" \
  "s_min_u32 s74, s74, 6400\n" \
  "s_cmp_gt_u32 s75, 900\n" \
  "s_cselect_b32 s50, s74, s50\n" \
  "s_cselect_b32 s88, s76, s88\n" \
  "s_cselect_b32 s89, s78, s89\n" \
  "LWEc3_%=:\n" \
  "s_memrealtime s[76:77]\n" \
  "s_memtime s[78:79]\n" \
  "s_waitcnt lgkmcnt(0)\n" \
  "s_lshl_b32 s75, s76, 2\n" \
  "s_sub_i32 s75, s48, s75\n" \
  "s_mul_i32 s75, s75, s50\n" \
  "s_ashr_i32 s75, s75, 10\n" \
  "s_sub_i32 s74, s49, s78\n" \
  "s_min_i32 s74, s74, s75\n" \
  "s_sub_i32 s75, s75, 92\n" \
  "s_max_i32 s74, s74, s75\n" \
  "s_add_u32 s49, s74, s78\n" \
  "s_cmp_gt_i32 s74, 168\n" \
  "s_cbranch_scc1 LWEc3_%=\n" \
  "s_cmp_lt_i32 s74, s87\n" \
  "s_cbranch_scc1 LSEc3_%=\n" \
  "s_max_i32 s74, s74, 48\n" \
  "s_lshr_b32 s74, s74, 3\n" \
  "s_lshl_b32 s74, s74, 2\n" \
  "s_sub_u32 s80, s84, s74\n" \
  "s_subb_u32 s81, s85, 0\n" \
  "s_setpc_b64 s[80:81]\n" \
  "s_nop 1\n" \
  "s_nop 1\n" \
  "s_nop 1\n" \
  "s_nop 1\n" \
  "s_nop 1\n" \
  "s_nop 1\n" \
  "s_nop 1\n" \
  "s_nop 1\n" \
  "s_nop 1\n" \
  "s_nop 1\n" \
  "s_nop 1\n" \
  "s_nop 1\n" \
  "s_nop 1\n" \
  "s_nop 1\n" \
  "s_nop 1\n" \
  "s_nop 1\n" \
  "LEEc3_%=:\n" \
  "buffer_load_dwordx4 v[112:115], v10, s[36:39], s46 offen offset:0 sc1\n" \
  "buffer_load_dwordx4 v[120:123], v10, s[36:39], s46 offen offset:2048 sc1\n" \
  "buffer_load_dwordx4 v[116:119], v11, s[36:39], s46 offen offset:1024 sc1\n" \
  "buffer_load_dwordx4 v[124:127], v11, s[36:39], s46 offen offset:3072 sc1\n" \
  "buffer_load_dwordx4 v[132:135], v11, s[36:39], s47 offen offset:1024 sc1\n" \
  "buffer_load_dwordx4 v[140:143], v11, s[36:39], s47 offen offset:3072 sc1\n" \
  "buffer_load_dwordx4 v[128:131], v10, s[36:39], s47 offen offset:0 sc1\n" \
  "buffer_load_dwordx4 v[136:139], v10, s[36:39], s47 offen offset:2048 sc1\n" \
  "buffer_load_dwordx4 v[144:147], v10, s[36:39], s69 offen offset:0 sc1\n" \
  "buffer_load_dwordx4 v[152:155], v10, s[36:39], s69 offen offset:2048 sc1\n" \
  "buffer_load_dwordx4 v[148:151], v11, s[36:39], s69 offen offset:1024 sc1\n" \
  "buffer_load_dwordx4 v[156:159], v11, s[36:39], s69 offen offset:3072 sc1\n" \
  "buffer_load_dwordx4 v[164:167], v11, s[36:39], s70 offen offset:1024 sc1\n" \
  "buffer_load_dwordx4 v[172:175], v11, s[36:39], s70 offen offset:3072 sc1\n" \
  "buffer_load_dwordx4 v[160:163], v10, s[36:39], s70 offen offset:0 sc1\n" \
  "buffer_load_dwordx4 v[168:171], v10, s[36:39], s70 offen offset:2048 sc1\n" \
  "s_add_u32 s46, s44, 65536\n" \
  "s_add_u32 s47, s44, 69632\n" \
  "s_mul_i32 s74, s51, s54\n" \
  "s_add_u32 s74, s74, s95\n" \
  "s_add_u32 s74, s74, s96\n" \
  "s_sub_i32 s75, s74, s48\n" \
  "s_mov_b32 s48, s74\n" \
  "s_mul_i32 s75, s75, s50\n" \
  "s_ashr_i32 s75, s75, 10\n" \
  "s_add_u32 s49, s49, s75\n" \
  "LULc3_%=:\n" \
  "s_memtime s[78:79]\n" \
  "s_waitcnt lgkmcnt(0)\n" \
  "s_sub_i32 s74, s49, s78\n" \
  "s_cmp_gt_i32 s74, 168\n" \
  "s_cbranch_scc1 LULc3_%=\n" \
  "s_max_i32 s74, s74, 48\n" \
  "s_lshr_b32 s74, s74, 3\n" \
  "s_lshl_b32 s74, s74, 2\n" \
  "s_sub_u32 s80, s90, s74\n" \
  "s_subb_u32 s81, s91, 0\n" \
  "s_setpc_b64 s[80:81]\n" \
  "s_nop 1\n" \
  "s_nop 1\n" \
  "s_nop 1\n" \
  "s_nop 1\n" \
  "s_nop 1\n" \
  "s_nop 1\n" \
  "s_nop 1\n" \
  "s_nop 1\n" \
  "s_nop 1\n" \
  "s_nop 1\n" \
  "s_nop 1\n" \
  "s_nop 1\n" \
  "s_nop 1\n" \
  "s_nop 1\n" \
  "s_nop 1\n" \
  "s_nop 1\n" \
  "LELc3_%=:\n" \
  "buffer_load_dwordx4 v[16:19], v1, s[36:39], s46 offen offset:0 sc1\n" \
  "buffer_load_dwordx4 v[20:23], v1, s[36:39], s46 offen offset:1024 sc1\n" \
  "buffer_load_dwordx4 v[24:27], v1, s[36:39], s46 offen offset:2048 sc1\n" \
  "buffer_load_dwordx4 v[28:31], v1, s[36:39], s46 offen offset:3072 sc1\n" \
  "buffer_load_dwordx4 v[32:35], v1, s[36:39], s47 offen offset:0 sc1\n" \
  "buffer_load_dwordx4 v[36:39], v1, s[36:39], s47 offen offset:1024 sc1\n" \
  "buffer_load_dwordx4 v[40:43], v1, s[36:39], s47 offen offset:2048 sc1\n" \
  "buffer_load_dwordx4 v[44:47], v1, s[36:39], s47 offen offset:3072 sc1\n" \
  "s_cmp_lg_u32 s71, 0\n" \
  "s_cbranch_scc1 LNBc3_%=\n" \
  "s_barrier\n" \
  "LNBc3_%=:\n" \
  "s_mov_b32 s71, 0\n" \
  "ds_read_b128 v[224:227], v1 offset:10240\n" \
  "ds_read_b128 v[228:231], v1 offset:11264\n" \
  "ds_read_b128 v[232:235], v1 offset:12288\n" \
  "ds_read_b128 v[236:239], v1 offset:13312\n" \
  "s_mov_b64 exec, s[40:41]\n" \
  "s_waitcnt vmcnt(23)\n" \
  "v_mfma_f32_16x16x32_bf16 v[176:179], v[112:115], a[80:83], 0\n" \
  "s_waitcnt vmcnt(22)\n" \
  "v_mfma_f32_16x16x32_bf16 v[184:187], v[120:123], a[88:91], 0\n" \
  "s_waitcnt vmcnt(20)\n" \
  "v_mfma_f32_16x16x32_bf16 v[180:183], v[116:119], a[84:87], 0\n" \
  "v_cvt_pk_bf16_f32 v208, v176, v177\n" \
  "v_cvt_pk_bf16_f32 v209, v178, v179\n" \
  "v_mfma_f32_16x16x32_bf16 v[188:191], v[124:127], a[92:95], 0\n" \
  "s_waitcnt vmcnt(18)\n" \
  "v_cvt_pk_bf16_f32 v210, v184, v185\n" \
  "v_cvt_pk_bf16_f32 v211, v186, v187\n" \
  "v_mfma_f32_16x16x32_bf16 v[196:199], v[132:135], a[100:103], 0\n" \
  "s_not_b64 exec, exec\n" \
  "v_cvt_pk_bf16_f32 v208, v180, v181\n" \
  "v_cvt_pk_bf16_f32 v209, v182, v183\n" \
  "v_mfma_f32_16x16x32_bf16 v[204:207], v[140:143], a[108:111], 0\n" \
  "s_waitcnt vmcnt(16)\n" \
  "v_cvt_pk_bf16_f32 v210, v188, v189\n" \
  "v_cvt_pk_bf16_f32 v211, v190, v191\n" \
  "v_mfma_f32_16x16x32_bf16 v[192:195], v[128:131], a[96:99], 0\n" \
  "v_cvt_pk_bf16_f32 v212, v196, v197\n" \
  "v_cvt_pk_bf16_f32 v213, v198, v199\n" \
  "v_mfma_f32_16x16x32_bf16 v[200:203], v[136:139], a[104:107], 0\n" \
  "s_waitcnt vmcnt(14)\n" \
  "v_cvt_pk_bf16_f32 v214, v204, v205\n" \
  "v_cvt_pk_bf16_f32 v215, v206, v207\n" \
  "v_mfma_f32_16x16x32_bf16 v[176:179], v[144:147], a[112:115], 0\n" \
  "s_not_b64 exec, exec\n" \
  "v_cvt_pk_bf16_f32 v212, v192, v193\n" \
  "v_cvt_pk_bf16_f32 v213, v194, v195\n" \
  "v_mfma_f32_16x16x32_bf16 v[184:187], v[152:155], a[120:123], 0\n" \
  "s_waitcnt vmcnt(12)\n" \
  "v_cvt_pk_bf16_f32 v214, v200, v201\n" \
  "v_cvt_pk_bf16_f32 v215, v202, v203\n" \
  "v_mfma_f32_16x16x32_bf16 v[180:183], v[148:151], a[116:119], 0\n" \
  "v_cvt_pk_bf16_f32 v216, v176, v177\n" \
  "v_cvt_pk_bf16_f32 v217, v178, v179\n" \
  "v_mfma_f32_16x16x32_bf16 v[188:191], v[156:159], a[124:127], 0\n" \
  "s_waitcnt vmcnt(10)\n" \
  "v_cvt_pk_bf16_f32 v218, v184, v185\n" \
  "v_cvt_pk_bf16_f32 v219, v186, v187\n" \
  "v_mfma_f32_16x16x32_bf16 v[196:199], v[164:167], a[132:135], 0\n" \
  "s_not_b64 exec, exec\n" \
  "v_cvt_pk_bf16_f32 v216, v180, v181\n" \
  "v_cvt_pk_bf16_f32 v217, v182, v183\n" \
  "v_mfma_f32_16x16x32_bf16 v[204:207], v[172:175], a[140:143], 0\n" \
  "s_waitcnt vmcnt(9)\n" \
  "v_cvt_pk_bf16_f32 v218, v188, v189\n" \
  "v_cvt_pk_bf16_f32 v219, v190, v191\n" \
  "v_mfma_f32_16x16x32_bf16 v[192:195], v[160:163], a[128:131], 0\n" \
  "s_waitcnt vmcnt(8)\n" \
  "v_cvt_pk_bf16_f32 v220, v196, v197\n" \
  "v_cvt_pk_bf16_f32 v221, v198, v199\n" \
  "v_mfma_f32_16x16x32_bf16 v[200:203], v[168:171], a[136:139], 0\n" \
  "v_cvt_pk_bf16_f32 v222, v204, v205\n" \
  "v_cvt_pk_bf16_f32 v223, v206, v207\n" \
  "s_waitcnt vmcnt(7)\n" \
  "s_waitcnt lgkmcnt(3)\n" \
  "v_mfma_f32_16x16x32_bf16 v[240:243], v[224:227], v[16:19], 0\n" \
  "s_not_b64 exec, exec\n" \
  "v_cvt_pk_bf16_f32 v220, v192, v193\n" \
  "v_cvt_pk_bf16_f32 v221, v194, v195\n" \
  "s_waitcnt vmcnt(6)\n" \
  "s_waitcnt lgkmcnt(2)\n" \
  "v_mfma_f32_16x16x32_bf16 v[240:243], v[228:231], v[20:23], v[240:243]\n" \
  "v_cvt_pk_bf16_f32 v222, v200, v201\n" \
  "v_cvt_pk_bf16_f32 v223, v202, v203\n" \
  "s_waitcnt vmcnt(5)\n" \
  "s_waitcnt lgkmcnt(1)\n" \
  "v_mfma_f32_16x16x32_bf16 v[240:243], v[232:235], v[24:27], v[240:243]\n" \
  "s_mov_b64 exec, -1\n" \
  "s_waitcnt vmcnt(4)\n" \
  "s_waitcnt lgkmcnt(0)\n" \
  "v_mfma_f32_16x16x32_bf16 v[240:243], v[236:239], v[28:31], v[240:243]\n" \
  "s_waitcnt vmcnt(3)\n" \
  "v_mfma_f32_16x16x32_bf16 v[240:243], v[208:211], v[32:35], v[240:243]\n" \
  "s_waitcnt vmcnt(2)\n" \
  "v_mfma_f32_16x16x32_bf16 v[240:243], v[212:215], v[36:39], v[240:243]\n" \
  "s_waitcnt vmcnt(1)\n" \
  "v_mfma_f32_16x16x32_bf16 v[240:243], v[216:219], v[40:43], v[240:243]\n" \
  "s_waitcnt vmcnt(0)\n" \
  "v_mfma_f32_16x16x32_bf16 v[240:243], v[220:223], v[44:47], v[240:243]\n" \
  "s_nop 7\n" \
  "v_permlane32_swap_b32 v240, v242\n" \
  "v_permlane32_swap_b32 v241, v243\n" \
  "s_nop 0\n" \
  "v_add_f32_dpp v240, v242, v240 row_ror:8 row_mask:0xf bank_mask:0xf\n" \
  "v_add_f32_dpp v241, v243, v241 row_ror:8 row_mask:0xf bank_mask:0xf\n" \
  "v_cmp_u_f32 vcc, v240, v241\n" \
  "v_cvt_pk_bf16_f32 v240, v240, v241\n" \
  "s_and_b64 vcc, vcc, s[34:35]\n" \
  "s_cbranch_scc1 LPFc3_%=\n" \
  "LPOKc3_%=:\n" \
  "v_mov_b32_dpp v240, v240 row_ror:8 row_mask:0xf bank_mask:0xc\n" \
  "buffer_store_dword v240, v13, s[36:39], s43 offen sc1\n" \
  "buffer_store_dword v240, v2, s[36:39], s43 offen sc1\n" \
  "v_readlane_b32 s74, v98, 3\n" \
  "s_nop 0\n" \
  "s_sub_u32 s75, s74, s51\n" \
  "s_cmp_lt_u32 s75, 16\n" \
  "s_cselect_b32 s51, s74, s51\n" \
  "v_readlane_b32 s74, v98, 0\n" \
  "s_add_u32 s72, s72, s53\n" \
  "s_sub_u32 s75, s74, s72\n" \
  "s_add_u32 s75, s75, 2000\n" \
  "s_cmp_lt_u32 s75, 4000\n" \
  "s_cselect_b32 s72, s74, s72\n" \
  "v_readlane_b32 s74, v98, 1\n" \
  "s_add_u32 s94, s94, s53\n" \
  "s_sub_u32 s75, s74, s94\n" \
  "s_add_u32 s75, s75, 2000\n" \
  "s_cmp_lt_u32 s75, 4000\n" \
  "s_cselect_b32 s94, s74, s94\n" \
  "v_readlane_b32 s74, v98, 2\n" \
  "s_add_u32 s95, s95, s53\n" \
  "s_sub_u32 s75, s74, s95\n" \
  "s_add_u32 s75, s75, 2000\n" \
  "s_cmp_lt_u32 s75, 4000\n" \
  "s_cselect_b32 s95, s74, s95\n" \
  "s_mul_i32 s74, s51, s54\n" \
  "s_add_u32 s74, s74, s72\n" \
  "s_add_u32 s74, s74, s55\n" \
  "s_sub_i32 s75, s74, s48\n" \
  "s_mov_b32 s48, s74\n" \
  "s_mul_i32 s75, s75, s50\n" \
  "s_ashr_i32 s75, s75, 10\n" \
  "s_add_u32 s49, s49, s75\n" \
  "s_mov_b32 s96, 0\n" \
  "s_barrier\n" \
  "s_mov_b32 s44, s45\n" \
  "s_cmp_lg_u32 s38, 0\n" \
  "s_cbranch_scc1 LOOPc3_%=\n" \
  "s_branch LPASSc3_%=\n" \
  "LSEc3_%=:\n" \
  "s_add_i32 s96, s96, 48\n" \
  "s_cmp_gt_i32 s96, 0\n" \
  "s_cselect_b32 s74, s54, 0\n" \
  "s_addc_u32 s51, s51, 0\n" \
  "s_sub_i32 s96, s96, s74\n" \
  "s_mul_i32 s74, s51, s54\n" \
  "s_add_u32 s74, s74, s94\n" \
  "s_add_u32 s74, s74, s96\n" \
  "s_sub_i32 s75, s74, s48\n" \
  "s_mov_b32 s48, s74\n" \
  "s_mul_i32 s75, s75, s50\n" \
  "s_ashr_i32 s75, s75, 10\n" \
  "s_add_u32 s49, s49, s75\n" \
  "s_branch LWEc3_%=\n" \
  "LENDc3_%=:\n" \
  "s_barrier\n" \
  "s_barrier\n" \
  "s_branch LDONE_%=\n" \
  "LPFc3_%=:\n" \
  "s_add_u32 s46, s44, 32768\n" \
  "s_add_u32 s47, s44, 36864\n" \
  "s_add_u32 s69, s44, 49152\n" \
  "s_add_u32 s70, s44, 53248\n" \
  "s_sub_i32 s71, s54, s96\n" \
  "s_sub_i32 s71, s71, 40\n" \
  "v_max_u32 v92, v112, v113\n" \
  "v_max3_u32 v92, v92, v114, v115\n" \
  "v_max3_u32 v92, v92, v116, v117\n" \
  "v_max3_u32 v92, v92, v118, v119\n" \
  "v_max3_u32 v92, v92, v120, v121\n" \
  "v_max3_u32 v92, v92, v122, v123\n" \
  "v_max3_u32 v92, v92, v124, v125\n" \
  "v_max3_u32 v92, v92, v126, v127\n" \
  "v_max3_u32 v92, v92, v128, v129\n" \
  "v_max3_u32 v92, v92, v130, v131\n" \
  "v_max3_u32 v92, v92, v132, v133\n" \
  "v_max3_u32 v92, v92, v134, v135\n" \
  "v_max3_u32 v92, v92, v136, v137\n" \
  "v_max3_u32 v92, v92, v138, v139\n" \
  "v_max3_u32 v92, v92, v140, v141\n" \
  "v_max3_u32 v92, v92, v142, v143\n" \
  "v_max3_u32 v92, v92, v144, v145\n" \
  "v_max3_u32 v92, v92, v146, v147\n" \
  "v_max3_u32 v92, v92, v148, v149\n" \
  "v_max3_u32 v92, v92, v150, v151\n" \
  "v_max3_u32 v92, v92, v152, v153\n" \
  "v_max3_u32 v92, v92, v154, v155\n" \
  "v_max3_u32 v92, v92, v156, v157\n" \
  "v_max3_u32 v92, v92, v158, v159\n" \
  "v_max3_u32 v92, v92, v160, v161\n" \
  "v_max3_u32 v92, v92, v162, v163\n" \
  "v_max3_u32 v92, v92, v164, v165\n" \
  "v_max3_u32 v92, v92, v166, v167\n" \
  "v_max3_u32 v92, v92, v168, v169\n" \
  "v_max3_u32 v92, v92, v170, v171\n" \
  "v_max3_u32 v92, v92, v172, v173\n" \
  "v_max3_u32 v92, v92, v174, v175\n" \
  "v_cmp_eq_u32 vcc, -1, v92\n" \
  "s_nop 3\n" \
  "s_cbranch_vccnz LPRc3_%=\n" \
  "s_mov_b32 s71, 256\n" \
  "s_add_u32 s59, s59, 1\n" \
  "v_max_u32 v92, v16, v17\n" \
  "v_max3_u32 v92, v92, v18, v19\n" \
  "v_max3_u32 v92, v92, v20, v21\n" \
  "v_max3_u32 v92, v92, v22, v23\n" \
  "v_max3_u32 v92, v92, v24, v25\n" \
  "v_max3_u32 v92, v92, v26, v27\n" \
  "v_max3_u32 v92, v92, v28, v29\n" \
  "v_max3_u32 v92, v92, v30, v31\n" \
  "v_max3_u32 v92, v92, v32, v33\n" \
  "v_max3_u32 v92, v92, v34, v35\n" \
  "v_max3_u32 v92, v92, v36, v37\n" \
  "v_max3_u32 v92, v92, v38, v39\n" \
  "v_max3_u32 v92, v92, v40, v41\n" \
  "v_max3_u32 v92, v92, v42, v43\n" \
  "v_max3_u32 v92, v92, v44, v45\n" \
  "v_max3_u32 v92, v92, v46, v47\n" \
  "v_cmp_eq_u32 vcc, -1, v92\n" \
  "s_nop 3\n" \
  "s_cbranch_vccz LPEc3_%=\n" \
  "LPRc3_%=:\n" \
  "s_add_u32 s73, s73, 1\n" \
  "s_cmp_gt_u32 s73, 20000\n" \
  "s_cbranch_scc1 LPEc3_%=\n" \
  "s_add_u32 s58, s58, 1\n" \
  "s_add_i32 s96, s96, s71\n" \
  "s_cmp_gt_i32 s96, 0\n" \
  "s_cselect_b32 s74, s54, 0\n" \
  "s_addc_u32 s51, s51, 0\n" \
  "s_sub_i32 s96, s96, s74\n" \
  "s_mul_i32 s74, s51, s54\n" \
  "s_add_u32 s74, s74, s94\n" \
  "s_add_u32 s74, s74, s96\n" \
  "s_sub_i32 s75, s74, s48\n" \
  "s_mov_b32 s48, s74\n" \
  "s_mul_i32 s75, s75, s50\n" \
  "s_ashr_i32 s75, s75, 10\n" \
  "s_add_u32 s49, s49, s75\n" \
  "s_branch LWEc3_%=\n" \
  "LPEc3_%=:\n" \
  "s_mov_b32 s71, 0\n" \
  "buffer_load_dwordx4 v[112:115], v10, s[36:39], s46 offen offset:0 sc1\n" \
  "buffer_load_dwordx4 v[120:123], v10, s[36:39], s46 offen offset:2048 sc1\n" \
  "buffer_load_dwordx4 v[116:119], v11, s[36:39], s46 offen offset:1024 sc1\n" \
  "buffer_load_dwordx4 v[124:127], v11, s[36:39], s46 offen offset:3072 sc1\n" \
  "buffer_load_dwordx4 v[132:135], v11, s[36:39], s47 offen offset:1024 sc1\n" \
  "buffer_load_dwordx4 v[140:143], v11, s[36:39], s47 offen offset:3072 sc1\n" \
  "buffer_load_dwordx4 v[128:131], v10, s[36:39], s47 offen offset:0 sc1\n" \
  "buffer_load_dwordx4 v[136:139], v10, s[36:39], s47 offen offset:2048 sc1\n" \
  "buffer_load_dwordx4 v[144:147], v10, s[36:39], s69 offen offset:0 sc1\n" \
  "buffer_load_dwordx4 v[152:155], v10, s[36:39], s69 offen offset:2048 sc1\n" \
  "buffer_load_dwordx4 v[148:151], v11, s[36:39], s69 offen offset:1024 sc1\n" \
  "buffer_load_dwordx4 v[156:159], v11, s[36:39], s69 offen offset:3072 sc1\n" \
  "buffer_load_dwordx4 v[164:167], v11, s[36:39], s70 offen offset:1024 sc1\n" \
  "buffer_load_dwordx4 v[172:175], v11, s[36:39], s70 offen offset:3072 sc1\n" \
  "buffer_load_dwordx4 v[160:163], v10, s[36:39], s70 offen offset:0 sc1\n" \
  "buffer_load_dwordx4 v[168:171], v10, s[36:39], s70 offen offset:2048 sc1\n" \
  "s_waitcnt vmcnt(0)\n" \
  "LPGc3_%=:\n" \
  "v_cmp_eq_u32 vcc, 0, v12\n" \
  "s_nop 1\n" \
  "v_cndmask_b32 v32, 0, v32, vcc\n" \
  "s_nop 1\n" \
  "v_cndmask_b32 v36, 0, v36, vcc\n" \
  "s_nop 1\n" \
  "v_cndmask_b32 v40, 0, v40, vcc\n" \
  "s_nop 1\n" \
  "v_cndmask_b32 v44, 0, v44, vcc\n" \
  "v_cmp_eq_u32 vcc, 1, v12\n" \
  "s_nop 1\n" \
  "v_cndmask_b32 v33, 0, v33, vcc\n" \
  "s_nop 1\n" \
  "v_cndmask_b32 v37, 0, v37, vcc\n" \
  "s_nop 1\n" \
  "v_cndmask_b32 v41, 0, v41, vcc\n" \
  "s_nop 1\n" \
  "v_cndmask_b32 v45, 0, v45, vcc\n" \
  "v_cmp_eq_u32 vcc, 0, v12\n" \
  "s_nop 1\n" \
  "v_cndmask_b32 v34, 0, v34, vcc\n" \
  "s_nop 1\n" \
  "v_cndmask_b32 v38, 0, v38, vcc\n" \
  "s_nop 1\n" \
  "v_cndmask_b32 v42, 0, v42, vcc\n" \
  "s_nop 1\n" \
  "v_cndmask_b32 v46, 0, v46, vcc\n" \
  "v_cmp_eq_u32 vcc, 1, v12\n" \
  "s_nop 1\n" \
  "v_cndmask_b32 v35, 0, v35, vcc\n" \
  "s_nop 1\n" \
  "v_cndmask_b32 v39, 0, v39, vcc\n" \
  "s_nop 1\n" \
  "v_cndmask_b32 v43, 0, v43, vcc\n" \
  "s_nop 1\n" \
  "v_cndmask_b32 v47, 0, v47, vcc\n" \
  "v_mfma_f32_16x16x32_bf16 v[176:179], v[112:115], a[80:83], 0\n" \
  "v_mfma_f32_16x16x32_bf16 v[180:183], v[116:119], a[84:87], 0\n" \
  "v_mfma_f32_16x16x32_bf16 v[184:187], v[120:123], a[88:91], 0\n" \
  "v_mfma_f32_16x16x32_bf16 v[188:191], v[124:127], a[92:95], 0\n" \
  "v_mfma_f32_16x16x32_bf16 v[192:195], v[128:131], a[96:99], 0\n" \
  "v_mfma_f32_16x16x32_bf16 v[196:199], v[132:135], a[100:103], 0\n" \
  "v_mfma_f32_16x16x32_bf16 v[200:203], v[136:139], a[104:107], 0\n" \
  "v_mfma_f32_16x16x32_bf16 v[204:207], v[140:143], a[108:111], 0\n" \
  "s_mov_b64 exec, s[40:41]\n" \
  "v_cvt_pk_bf16_f32 v208, v176, v177\n" \
  "v_cvt_pk_bf16_f32 v209, v178, v179\n" \
  "v_cvt_pk_bf16_f32 v210, v184, v185\n" \
  "v_cvt_pk_bf16_f32 v211, v186, v187\n" \
  "v_cvt_pk_bf16_f32 v212, v192, v193\n" \
  "v_cvt_pk_bf16_f32 v213, v194, v195\n" \
  "v_cvt_pk_bf16_f32 v214, v200, v201\n" \
  "v_cvt_pk_bf16_f32 v215, v202, v203\n" \
  "s_not_b64 exec, exec\n" \
  "v_cvt_pk_bf16_f32 v208, v180, v181\n" \
  "v_cvt_pk_bf16_f32 v209, v182, v183\n" \
  "v_cvt_pk_bf16_f32 v210, v188, v189\n" \
  "v_cvt_pk_bf16_f32 v211, v190, v191\n" \
  "v_cvt_pk_bf16_f32 v212, v196, v197\n" \
  "v_cvt_pk_bf16_f32 v213, v198, v199\n" \
  "v_cvt_pk_bf16_f32 v214, v204, v205\n" \
  "v_cvt_pk_bf16_f32 v215, v206, v207\n" \
  "s_mov_b64 exec, -1\n" \
  "v_mfma_f32_16x16x32_bf16 v[176:179], v[144:147], a[112:115], 0\n" \
  "v_mfma_f32_16x16x32_bf16 v[180:183], v[148:151], a[116:119], 0\n" \
  "v_mfma_f32_16x16x32_bf16 v[184:187], v[152:155], a[120:123], 0\n" \
  "v_mfma_f32_16x16x32_bf16 v[188:191], v[156:159], a[124:127], 0\n" \
  "v_mfma_f32_16x16x32_bf16 v[192:195], v[160:163], a[128:131], 0\n" \
  "v_mfma_f32_16x16x32_bf16 v[196:199], v[164:167], a[132:135], 0\n" \
  "v_mfma_f32_16x16x32_bf16 v[200:203], v[168:171], a[136:139], 0\n" \
  "v_mfma_f32_16x16x32_bf16 v[204:207], v[172:175], a[140:143], 0\n" \
  "s_mov_b64 exec, s[40:41]\n" \
  "v_cvt_pk_bf16_f32 v216, v176, v177\n" \
  "v_cvt_pk_bf16_f32 v217, v178, v179\n" \
  "v_cvt_pk_bf16_f32 v218, v184, v185\n" \
  "v_cvt_pk_bf16_f32 v219, v186, v187\n" \
  "v_cvt_pk_bf16_f32 v220, v192, v193\n" \
  "v_cvt_pk_bf16_f32 v221, v194, v195\n" \
  "v_cvt_pk_bf16_f32 v222, v200, v201\n" \
  "v_cvt_pk_bf16_f32 v223, v202, v203\n" \
  "s_not_b64 exec, exec\n" \
  "v_cvt_pk_bf16_f32 v216, v180, v181\n" \
  "v_cvt_pk_bf16_f32 v217, v182, v183\n" \
  "v_cvt_pk_bf16_f32 v218, v188, v189\n" \
  "v_cvt_pk_bf16_f32 v219, v190, v191\n" \
  "v_cvt_pk_bf16_f32 v220, v196, v197\n" \
  "v_cvt_pk_bf16_f32 v221, v198, v199\n" \
  "v_cvt_pk_bf16_f32 v222, v204, v205\n" \
  "v_cvt_pk_bf16_f32 v223, v206, v207\n" \
  "s_mov_b64 exec, -1\n" \
  "v_mfma_f32_16x16x32_bf16 v[240:243], v[224:227], v[16:19], 0\n" \
  "v_mfma_f32_16x16x32_bf16 v[240:243], v[228:231], v[20:23], v[240:243]\n" \
  "v_mfma_f32_16x16x32_bf16 v[240:243], v[232:235], v[24:27], v[240:243]\n" \
  "v_mfma_f32_16x16x32_bf16 v[240:243], v[236:239], v[28:31], v[240:243]\n" \
  "v_mfma_f32_16x16x32_bf16 v[240:243], v[208:211], v[32:35], v[240:243]\n" \
  "v_mfma_f32_16x16x32_bf16 v[240:243], v[212:215], v[36:39], v[240:243]\n" \
  "v_mfma_f32_16x16x32_bf16 v[240:243], v[216:219], v[40:43], v[240:243]\n" \
  "v_mfma_f32_16x16x32_bf16 v[240:243], v[220:223], v[44:47], v[240:243]\n" \
  "s_nop 7\n" \
  "v_permlane32_swap_b32 v240, v242\n" \
  "v_permlane32_swap_b32 v241, v243\n" \
  "s_nop 0\n" \
  "v_add_f32_dpp v240, v242, v240 row_ror:8 row_mask:0xf bank_mask:0xf\n" \
  "v_add_f32_dpp v241, v243, v241 row_ror:8 row_mask:0xf bank_mask:0xf\n" \
  "v_cvt_pk_bf16_f32 v240, v240, v241\n" \
  "v_pk_min_u16 v240, v240, -2 op_sel_hi:[1,0]\n" \
  "s_nop 1\n" \
  "s_branch LPOKc3_%=\n" \
  "LMFc3_%=:\n" \
  "v_max_u32 v92, v16, v17\n" \
  "v_max3_u32 v92, v92, v18, v19\n" \
  "v_max3_u32 v92, v92, v20, v21\n" \
  "v_max3_u32 v92, v92, v22, v23\n" \
  "v_max3_u32 v92, v92, v24, v25\n" \
  "v_max3_u32 v92, v92, v26, v27\n" \
  "v_max3_u32 v92, v92, v28, v29\n" \
  "v_max3_u32 v92, v92, v30, v31\n" \
  "v_max3_u32 v92, v92, v32, v33\n" \
  "v_max3_u32 v92, v92, v34, v35\n" \
  "v_max3_u32 v92, v92, v36, v37\n" \
  "v_max3_u32 v92, v92, v38, v39\n" \
  "v_max3_u32 v92, v92, v40, v41\n" \
  "v_max3_u32 v92, v92, v42, v43\n" \
  "v_max3_u32 v92, v92, v44, v45\n" \
  "v_max3_u32 v92, v92, v46, v47\n" \
  "v_max3_u32 v92, v92, v48, v49\n" \
  "v_max3_u32 v92, v92, v50, v51\n" \
  "v_max3_u32 v92, v92, v52, v53\n" \
  "v_max3_u32 v92, v92, v54, v55\n" \
  "v_cmp_eq_u32 vcc, -1, v92\n" \
  "s_nop 3\n" \
  "s_cbranch_vccz LMOKc3_%=\n" \
  "s_cmp_gt_u32 s57, 20000\n" \
  "s_cbranch_scc1 LMOKc3_%=\n" \
  "s_add_u32 s57, s57, 1\n" \
  "LHMc3_%=:\n" \
  "s_add_u32 s48, s48, s54\n" \
  "s_add_u32 s49, s49, s86\n" \
  "s_add_u32 s51, s51, 1\n" \
  "LGMc3_%=:\n" \
  "s_memrealtime s[76:77]\n" \
  "s_memtime s[78:79]\n" \
  "s_waitcnt lgkmcnt(0)\n" \
  "s_lshl_b32 s75, s76, 2\n" \
  "s_sub_i32 s75, s48, s75\n" \
  "s_mul_i32 s75, s75, s50\n" \
  "s_ashr_i32 s75, s75, 10\n" \
  "s_sub_i32 s74, s49, s78\n" \
  "s_min_i32 s74, s74, s75\n" \
  "s_sub_i32 s75, s75, 92\n" \
  "s_max_i32 s74, s74, s75\n" \
  "s_add_u32 s49, s74, s78\n" \
  "s_cmp_gt_i32 s74, 60\n" \
  "s_cbranch_scc1 LGMc3_%=\n" \
  "s_cmp_lt_i32 s74, s87\n" \
  "s_cbranch_scc1 LHMc3_%=\n" \
  "s_barrier\n" \
  "v_mov_b32 v56, 0\n" \
  "v_mov_b32 v57, 0\n" \
  "v_mov_b32 v58, 0\n" \
  "v_mov_b32 v59, 0\n" \
  "s_waitcnt vmcnt(0)\n" \
  "s_barrier\n" \
  "s_branch LMc3_%=\n" \
  "LDONE_%=:\n" \
  "s_waitcnt vmcnt(0) lgkmcnt(0)\n" \
  "s_mov_b32 s53, s73\n" \
  "s_mov_b32 s54, s51\n" \
  "s_mov_b32 s55, s50\n"
#define PROJ_CLOBBERS \
  "v12", "v13", "v14", "v15", "v16", "v17", "v18", "v19", "v20", "v21", "v22", "v23", "v24", "v25", "v26", "v27", "v28", "v29", "v30", "v31", "v32", \
  "v33", "v34", "v35", "v36", "v37", "v38", "v39", "v40", "v41", "v42", "v43", "v44", "v45", "v46", "v47", "v48", "v49", "v50", "v51", "v52", "v53", \
  "v54", "v55", "v56", "v57", "v58", "v59", "v60", "v61", "v62", "v63", "v64", "v65", "v66", "v67", "v68", "v69", "v70", "v71", "v72", "v73", "v74", \
  "v75", "v76", "v77", "v78", "v79", "v100", "v101", "v102", "v103", "v104", "v105", "v106", "v107", "v108", "v109", "v110", "v111", "v112", "v113", \
  "v114", "v115", "v116", "v117", "v118", "v119", "v120", "v121", "v122", "v123", "s68", "s69", "s70", "s71", "s72", "s73", "s74", "s75", "s76", "s77"
#define PROJ_ASM \
  "s_waitcnt vmcnt(0)\n" \
  "s_barrier\n" \
  "ds_read_b128 v[32:35], v0 offset:0\n" \
  "ds_read_b128 v[36:39], v0 offset:20480\n" \
  "ds_read_b128 v[40:43], v0 offset:1024\n" \
  "ds_read_b128 v[44:47], v0 offset:21504\n" \
  "v_add_u32 v9, 40960, v0\n" \
  "v_add_u32 v10, 81920, v0\n" \
  "s_mul_i32 s68, s52, 40960\n" \
  "s_add_u32 s68, s68, 81920\n" \
  "s_mov_b32 s69, 0x40000000\n" \
  "s_mov_b32 s71, 0x40000000\n" \
  "v_mov_b32 v12, -1\n" \
  "v_mov_b32 v13, -1\n" \
  "v_mov_b32 v14, -1\n" \
  "v_mov_b32 v15, -1\n" \
  "LPL_%=:\n" \
  "s_waitcnt lgkmcnt(2)\n" \
  "v_mfma_f32_16x16x32_bf16 v[56:59], a[0:3], v[32:35], 0\n" \
  "ds_read_b128 v[48:51], v0 offset:2048\n" \
  "v_mfma_f32_16x16x32_bf16 v[68:71], a[0:3], v[36:39], 0\n" \
  "ds_read_b128 v[52:55], v0 offset:22528\n" \
  "v_mfma_f32_16x16x32_bf16 v[60:63], a[80:83], v[32:35], 0\n" \
  "buffer_store_dwordx4 v[12:15], v7, s[40:43], s71 offen sc1\n" \
  "v_mfma_f32_16x16x32_bf16 v[72:75], a[80:83], v[36:39], 0\n" \
  "v_cvt_pk_bf16_f32 v16, v100, v101\n" \
  "v_mfma_f32_16x16x32_bf16 v[64:67], a[160:163], v[32:35], 0\n" \
  "v_cvt_pk_bf16_f32 v17, v102, v103\n" \
  "v_mfma_f32_16x16x32_bf16 v[76:79], a[160:163], v[36:39], 0\n" \
  "v_cvt_pk_bf16_f32 v18, v104, v105\n" \
  "s_waitcnt lgkmcnt(2)\n" \
  "v_mfma_f32_16x16x32_bf16 v[56:59], a[4:7], v[40:43], v[56:59]\n" \
  "ds_read_b128 v[32:35], v0 offset:3072\n" \
  "v_mfma_f32_16x16x32_bf16 v[68:71], a[4:7], v[44:47], v[68:71]\n" \
  "ds_read_b128 v[36:39], v0 offset:23552\n" \
  "v_mfma_f32_16x16x32_bf16 v[60:63], a[84:87], v[40:43], v[60:63]\n" \
  "v_cvt_pk_bf16_f32 v19, v106, v107\n" \
  "v_mfma_f32_16x16x32_bf16 v[72:75], a[84:87], v[44:47], v[72:75]\n" \
  "buffer_store_dwordx4 v[16:19], v2, s[40:43], s69 offen sc1\n" \
  "v_mfma_f32_16x16x32_bf16 v[64:67], a[164:167], v[40:43], v[64:67]\n" \
  "v_cvt_pk_bf16_f32 v20, v112, v113\n" \
  "v_mfma_f32_16x16x32_bf16 v[76:79], a[164:167], v[44:47], v[76:79]\n" \
  "v_cvt_pk_bf16_f32 v21, v114, v115\n" \
  "s_waitcnt lgkmcnt(2)\n" \
  "v_mfma_f32_16x16x32_bf16 v[56:59], a[8:11], v[48:51], v[56:59]\n" \
  "ds_read_b128 v[40:43], v0 offset:4096\n" \
  "v_mfma_f32_16x16x32_bf16 v[68:71], a[8:11], v[52:55], v[68:71]\n" \
  "ds_read_b128 v[44:47], v0 offset:24576\n" \
  "v_mfma_f32_16x16x32_bf16 v[60:63], a[88:91], v[48:51], v[60:63]\n" \
  "v_cvt_pk_bf16_f32 v22, v116, v117\n" \
  "v_mfma_f32_16x16x32_bf16 v[72:75], a[88:91], v[52:55], v[72:75]\n" \
  "v_cvt_pk_bf16_f32 v23, v118, v119\n" \
  "v_mfma_f32_16x16x32_bf16 v[64:67], a[168:171], v[48:51], v[64:67]\n" \
  "buffer_store_dwordx4 v[20:23], v2, s[40:43], s69 offen offset:1024 sc1\n" \
  "v_mfma_f32_16x16x32_bf16 v[76:79], a[168:171], v[52:55], v[76:79]\n" \
  "v_cvt_pk_bf16_f32 v24, v108, v109\n" \
  "s_waitcnt lgkmcnt(2)\n" \
  "v_mfma_f32_16x16x32_bf16 v[56:59], a[12:15], v[32:35], v[56:59]\n" \
  "ds_read_b128 v[48:51], v0 offset:5120\n" \
  "v_mfma_f32_16x16x32_bf16 v[68:71], a[12:15], v[36:39], v[68:71]\n" \
  "ds_read_b128 v[52:55], v0 offset:25600\n" \
  "v_mfma_f32_16x16x32_bf16 v[60:63], a[92:95], v[32:35], v[60:63]\n" \
  "v_cvt_pk_bf16_f32 v25, v110, v111\n" \
  "v_mfma_f32_16x16x32_bf16 v[72:75], a[92:95], v[36:39], v[72:75]\n" \
  "v_cvt_pk_bf16_f32 v26, v120, v121\n" \
  "v_mfma_f32_16x16x32_bf16 v[64:67], a[172:175], v[32:35], v[64:67]\n" \
  "v_cvt_pk_bf16_f32 v27, v122, v123\n" \
  "v_mfma_f32_16x16x32_bf16 v[76:79], a[172:175], v[36:39], v[76:79]\n" \
  "buffer_store_dwordx4 v[24:27], v3, s[40:43], s69 offen sc1\n" \
  "s_waitcnt lgkmcnt(2)\n" \
  "v_mfma_f32_16x16x32_bf16 v[56:59], a[16:19], v[40:43], v[56:59]\n" \
  "ds_read_b128 v[32:35], v0 offset:6144\n" \
  "v_mfma_f32_16x16x32_bf16 v[68:71], a[16:19], v[44:47], v[68:71]\n" \
  "ds_read_b128 v[36:39], v0 offset:26624\n" \
  "v_mfma_f32_16x16x32_bf16 v[60:63], a[96:99], v[40:43], v[60:63]\n" \
  "s_add_u32 s72, s68, 4096\n" \
  "v_mfma_f32_16x16x32_bf16 v[72:75], a[96:99], v[44:47], v[72:75]\n" \
  "s_add_u32 s73, s68, 8192\n" \
  "v_mfma_f32_16x16x32_bf16 v[64:67], a[176:179], v[40:43], v[64:67]\n" \
  "s_add_u32 m0, s54, 81920\n" \
  "v_mfma_f32_16x16x32_bf16 v[76:79], a[176:179], v[44:47], v[76:79]\n" \
  "buffer_load_dwordx4 v1, s[36:39], s68 offen offset:0 sc1 lds\n" \
  "s_waitcnt lgkmcnt(2)\n" \
  "v_mfma_f32_16x16x32_bf16 v[56:59], a[20:23], v[48:51], v[56:59]\n" \
  "ds_read_b128 v[40:43], v0 offset:7168\n" \
  "v_mfma_f32_16x16x32_bf16 v[68:71], a[20:23], v[52:55], v[68:71]\n" \
  "ds_read_b128 v[44:47], v0 offset:27648\n" \
  "v_mfma_f32_16x16x32_bf16 v[60:63], a[100:103], v[48:51], v[60:63]\n" \
  "buffer_load_dwordx4 v1, s[36:39], s68 offen offset:1024 sc1 lds\n" \
  "v_mfma_f32_16x16x32_bf16 v[72:75], a[100:103], v[52:55], v[72:75]\n" \
  "v_mfma_f32_16x16x32_bf16 v[64:67], a[180:183], v[48:51], v[64:67]\n" \
  "v_mfma_f32_16x16x32_bf16 v[76:79], a[180:183], v[52:55], v[76:79]\n" \
  "s_waitcnt lgkmcnt(2)\n" \
  "v_mfma_f32_16x16x32_bf16 v[56:59], a[24:27], v[32:35], v[56:59]\n" \
  "ds_read_b128 v[48:51], v0 offset:8192\n" \
  "v_mfma_f32_16x16x32_bf16 v[68:71], a[24:27], v[36:39], v[68:71]\n" \
  "ds_read_b128 v[52:55], v0 offset:28672\n" \
  "v_mfma_f32_16x16x32_bf16 v[60:63], a[104:107], v[32:35], v[60:63]\n" \
  "buffer_load_dwordx4 v1, s[36:39], s68 offen offset:2048 sc1 lds\n" \
  "v_mfma_f32_16x16x32_bf16 v[72:75], a[104:107], v[36:39], v[72:75]\n" \
  "v_mfma_f32_16x16x32_bf16 v[64:67], a[184:187], v[32:35], v[64:67]\n" \
  "v_mfma_f32_16x16x32_bf16 v[76:79], a[184:187], v[36:39], v[76:79]\n" \
  "s_waitcnt lgkmcnt(2)\n" \
  "v_mfma_f32_16x16x32_bf16 v[56:59], a[28:31], v[40:43], v[56:59]\n" \
  "ds_read_b128 v[32:35], v0 offset:9216\n" \
  "v_mfma_f32_16x16x32_bf16 v[68:71], a[28:31], v[44:47], v[68:71]\n" \
  "ds_read_b128 v[36:39], v0 offset:29696\n" \
  "v_mfma_f32_16x16x32_bf16 v[60:63], a[108:111], v[40:43], v[60:63]\n" \
  "buffer_load_dwordx4 v1, s[36:39], s68 offen offset:3072 sc1 lds\n" \
  "v_mfma_f32_16x16x32_bf16 v[72:75], a[108:111], v[44:47], v[72:75]\n" \
  "v_mfma_f32_16x16x32_bf16 v[64:67], a[188:191], v[40:43], v[64:67]\n" \
  "v_mfma_f32_16x16x32_bf16 v[76:79], a[188:191], v[44:47], v[76:79]\n" \
  "s_waitcnt lgkmcnt(2)\n" \
  "v_mfma_f32_16x16x32_bf16 v[56:59], a[32:35], v[48:51], v[56:59]\n" \
  "ds_read_b128 v[40:43], v0 offset:10240\n" \
  "v_mfma_f32_16x16x32_bf16 v[68:71], a[32:35], v[52:55], v[68:71]\n" \
  "ds_read_b128 v[44:47], v0 offset:30720\n" \
  "v_mfma_f32_16x16x32_bf16 v[60:63], a[112:115], v[48:51], v[60:63]\n" \
  "s_add_u32 m0, s54, 86016\n" \
  "v_mfma_f32_16x16x32_bf16 v[72:75], a[112:115], v[52:55], v[72:75]\n" \
  "buffer_load_dwordx4 v1, s[36:39], s72 offen offset:0 sc1 lds\n" \
  "v_mfma_f32_16x16x32_bf16 v[64:67], a[192:195], v[48:51], v[64:67]\n" \
  "v_mfma_f32_16x16x32_bf16 v[76:79], a[192:195], v[52:55], v[76:79]\n" \
  "s_waitcnt lgkmcnt(2)\n" \
  "v_mfma_f32_16x16x32_bf16 v[56:59], a[36:39], v[32:35], v[56:59]\n" \
  "ds_read_b128 v[48:51], v0 offset:11264\n" \
  "v_mfma_f32_16x16x32_bf16 v[68:71], a[36:39], v[36:39], v[68:71]\n" \
  "ds_read_b128 v[52:55], v0 offset:31744\n" \
  "v_mfma_f32_16x16x32_bf16 v[60:63], a[116:119], v[32:35], v[60:63]\n" \
  "buffer_load_dwordx4 v1, s[36:39], s72 offen offset:1024 sc1 lds\n" \
  "v_mfma_f32_16x16x32_bf16 v[72:75], a[116:119], v[36:39], v[72:75]\n" \
  "v_mfma_f32_16x16x32_bf16 v[64:67], a[196:199], v[32:35], v[64:67]\n" \
  "v_mfma_f32_16x16x32_bf16 v[76:79], a[196:199], v[36:39], v[76:79]\n" \
  "s_waitcnt lgkmcnt(2)\n" \
  "v_mfma_f32_16x16x32_bf16 v[56:59], a[40:43], v[40:43], v[56:59]\n" \
  "ds_read_b128 v[32:35], v0 offset:12288\n" \
  "v_mfma_f32_16x16x32_bf16 v[68:71], a[40:43], v[44:47], v[68:71]\n" \
  "ds_read_b128 v[36:39], v0 offset:32768\n" \
  "v_mfma_f32_16x16x32_bf16 v[60:63], a[120:123], v[40:43], v[60:63]\n" \
  "buffer_load_dwordx4 v1, s[36:39], s72 offen offset:2048 sc1 lds\n" \
  "v_mfma_f32_16x16x32_bf16 v[72:75], a[120:123], v[44:47], v[72:75]\n" \
  "v_mfma_f32_16x16x32_bf16 v[64:67], a[200:203], v[40:43], v[64:67]\n" \
  "v_mfma_f32_16x16x32_bf16 v[76:79], a[200:203], v[44:47], v[76:79]\n" \
  "s_waitcnt lgkmcnt(2)\n" \
  "v_mfma_f32_16x16x32_bf16 v[56:59], a[44:47], v[48:51], v[56:59]\n" \
  "ds_read_b128 v[40:43], v0 offset:13312\n" \
  "v_mfma_f32_16x16x32_bf16 v[68:71], a[44:47], v[52:55], v[68:71]\n" \
  "ds_read_b128 v[44:47], v0 offset:33792\n" \
  "v_mfma_f32_16x16x32_bf16 v[60:63], a[124:127], v[48:51], v[60:63]\n" \
  "buffer_load_dwordx4 v1, s[36:39], s72 offen offset:3072 sc1 lds\n" \
  "v_mfma_f32_16x16x32_bf16 v[72:75], a[124:127], v[52:55], v[72:75]\n" \
  "v_mfma_f32_16x16x32_bf16 v[64:67], a[204:207], v[48:51], v[64:67]\n" \
  "v_mfma_f32_16x16x32_bf16 v[76:79], a[204:207], v[52:55], v[76:79]\n" \
  "s_waitcnt lgkmcnt(2)\n" \
  "v_mfma_f32_16x16x32_bf16 v[56:59], a[48:51], v[32:35], v[56:59]\n" \
  "ds_read_b128 v[48:51], v0 offset:14336\n" \
  "v_mfma_f32_16x16x32_bf16 v[68:71], a[48:51], v[36:39], v[68:71]\n" \
  "ds_read_b128 v[52:55], v0 offset:34816\n" \
  "v_mfma_f32_16x16x32_bf16 v[60:63], a[128:131], v[32:35], v[60:63]\n" \
  "s_add_u32 m0, s54, 90112\n" \
  "v_mfma_f32_16x16x32_bf16 v[72:75], a[128:131], v[36:39], v[72:75]\n" \
  "buffer_load_dwordx4 v1, s[36:39], s73 offen offset:0 sc1 lds\n" \
  "v_mfma_f32_16x16x32_bf16 v[64:67], a[208:211], v[32:35], v[64:67]\n" \
  "v_mfma_f32_16x16x32_bf16 v[76:79], a[208:211], v[36:39], v[76:79]\n" \
  "s_waitcnt lgkmcnt(2)\n" \
  "v_mfma_f32_16x16x32_bf16 v[56:59], a[52:55], v[40:43], v[56:59]\n" \
  "ds_read_b128 v[32:35], v0 offset:15360\n" \
  "v_mfma_f32_16x16x32_bf16 v[68:71], a[52:55], v[44:47], v[68:71]\n" \
  "ds_read_b128 v[36:39], v0 offset:35840\n" \
  "v_mfma_f32_16x16x32_bf16 v[60:63], a[132:135], v[40:43], v[60:63]\n" \
  "buffer_load_dwordx4 v1, s[36:39], s73 offen offset:1024 sc1 lds\n" \
  "v_mfma_f32_16x16x32_bf16 v[72:75], a[132:135], v[44:47], v[72:75]\n" \
  "v_mfma_f32_16x16x32_bf16 v[64:67], a[212:215], v[40:43], v[64:67]\n" \
  "v_mfma_f32_16x16x32_bf16 v[76:79], a[212:215], v[44:47], v[76:79]\n" \
  "s_waitcnt lgkmcnt(2)\n" \
  "v_mfma_f32_16x16x32_bf16 v[56:59], a[56:59], v[48:51], v[56:59]\n" \
  "ds_read_b128 v[40:43], v0 offset:16384\n" \
  "v_mfma_f32_16x16x32_bf16 v[68:71], a[56:59], v[52:55], v[68:71]\n" \
  "ds_read_b128 v[44:47], v0 offset:36864\n" \
  "v_mfma_f32_16x16x32_bf16 v[60:63], a[136:139], v[48:51], v[60:63]\n" \
  "s_add_u32 s68, s68, 40960\n" \
  "v_mfma_f32_16x16x32_bf16 v[72:75], a[136:139], v[52:55], v[72:75]\n" \
  "v_mfma_f32_16x16x32_bf16 v[64:67], a[216:219], v[48:51], v[64:67]\n" \
  "v_mfma_f32_16x16x32_bf16 v[76:79], a[216:219], v[52:55], v[76:79]\n" \
  "s_waitcnt lgkmcnt(2)\n" \
  "v_mfma_f32_16x16x32_bf16 v[56:59], a[60:63], v[32:35], v[56:59]\n" \
  "ds_read_b128 v[48:51], v0 offset:17408\n" \
  "v_mfma_f32_16x16x32_bf16 v[68:71], a[60:63], v[36:39], v[68:71]\n" \
  "ds_read_b128 v[52:55], v0 offset:37888\n" \
  "v_mfma_f32_16x16x32_bf16 v[60:63], a[140:143], v[32:35], v[60:63]\n" \
  "v_mov_b32 v11, s52\n" \
  "v_mfma_f32_16x16x32_bf16 v[72:75], a[140:143], v[36:39], v[72:75]\n" \
  "s_waitcnt vmcnt(10)\n" \
  "v_mfma_f32_16x16x32_bf16 v[64:67], a[220:223], v[32:35], v[64:67]\n" \
  "buffer_store_dword v11, v5, s[40:43], 0 offen sc1\n" \
  "v_mfma_f32_16x16x32_bf16 v[76:79], a[220:223], v[36:39], v[76:79]\n" \
  "s_waitcnt lgkmcnt(2)\n" \
  "v_mfma_f32_16x16x32_bf16 v[56:59], a[64:67], v[40:43], v[56:59]\n" \
  "ds_read_b128 v[32:35], v0 offset:18432\n" \
  "v_mfma_f32_16x16x32_bf16 v[68:71], a[64:67], v[44:47], v[68:71]\n" \
  "ds_read_b128 v[36:39], v0 offset:38912\n" \
  "v_mfma_f32_16x16x32_bf16 v[60:63], a[144:147], v[40:43], v[60:63]\n" \
  "v_mfma_f32_16x16x32_bf16 v[72:75], a[144:147], v[44:47], v[72:75]\n" \
  "v_mfma_f32_16x16x32_bf16 v[64:67], a[224:227], v[40:43], v[64:67]\n" \
  "v_mfma_f32_16x16x32_bf16 v[76:79], a[224:227], v[44:47], v[76:79]\n" \
  "s_waitcnt lgkmcnt(2)\n" \
  "v_mfma_f32_16x16x32_bf16 v[56:59], a[68:71], v[48:51], v[56:59]\n" \
  "ds_read_b128 v[40:43], v0 offset:19456\n" \
  "v_mfma_f32_16x16x32_bf16 v[68:71], a[68:71], v[52:55], v[68:71]\n" \
  "ds_read_b128 v[44:47], v0 offset:39936\n" \
  "v_mfma_f32_16x16x32_bf16 v[60:63], a[148:151], v[48:51], v[60:63]\n" \
  "v_readlane_b32 s69, v4, s52\n" \
  "v_mfma_f32_16x16x32_bf16 v[72:75], a[148:151], v[52:55], v[72:75]\n" \
  "v_readlane_b32 s71, v8, s52\n" \
  "v_mfma_f32_16x16x32_bf16 v[64:67], a[228:231], v[48:51], v[64:67]\n" \
  "s_add_u32 s52, s52, 1\n" \
  "v_mfma_f32_16x16x32_bf16 v[76:79], a[228:231], v[52:55], v[76:79]\n" \
  "s_waitcnt lgkmcnt(0)\n" \
  "s_barrier\n" \
  "v_mfma_f32_16x16x32_bf16 v[56:59], a[72:75], v[32:35], v[56:59]\n" \
  "ds_read_b128 v[48:51], v9 offset:0\n" \
  "v_mfma_f32_16x16x32_bf16 v[68:71], a[72:75], v[36:39], v[68:71]\n" \
  "ds_read_b128 v[52:55], v9 offset:20480\n" \
  "v_mfma_f32_16x16x32_bf16 v[60:63], a[152:155], v[32:35], v[60:63]\n" \
  "v_mfma_f32_16x16x32_bf16 v[72:75], a[152:155], v[36:39], v[72:75]\n" \
  "v_mfma_f32_16x16x32_bf16 v[64:67], a[232:235], v[32:35], v[64:67]\n" \
  "v_mfma_f32_16x16x32_bf16 v[76:79], a[232:235], v[36:39], v[76:79]\n" \
  "v_mfma_f32_16x16x32_bf16 v[56:59], a[76:79], v[40:43], v[56:59]\n" \
  "ds_read_b128 v[32:35], v9 offset:1024\n" \
  "v_mfma_f32_16x16x32_bf16 v[68:71], a[76:79], v[44:47], v[68:71]\n" \
  "ds_read_b128 v[36:39], v9 offset:21504\n" \
  "v_mfma_f32_16x16x32_bf16 v[60:63], a[156:159], v[40:43], v[60:63]\n" \
  "s_cmp_eq_u32 s52, s53\n" \
  "v_mfma_f32_16x16x32_bf16 v[72:75], a[156:159], v[44:47], v[72:75]\n" \
  "v_mfma_f32_16x16x32_bf16 v[64:67], a[236:239], v[40:43], v[64:67]\n" \
  "v_mfma_f32_16x16x32_bf16 v[76:79], a[236:239], v[44:47], v[76:79]\n" \
  "s_waitcnt lgkmcnt(2)\n" \
  "v_mfma_f32_16x16x32_bf16 v[100:103], a[0:3], v[48:51], 0\n" \
  "ds_read_b128 v[40:43], v9 offset:2048\n" \
  "v_mfma_f32_16x16x32_bf16 v[112:115], a[0:3], v[52:55], 0\n" \
  "ds_read_b128 v[44:47], v9 offset:22528\n" \
  "v_mfma_f32_16x16x32_bf16 v[104:107], a[80:83], v[48:51], 0\n" \
  "buffer_store_dwordx4 v[12:15], v7, s[40:43], s71 offen sc1\n" \
  "v_mfma_f32_16x16x32_bf16 v[116:119], a[80:83], v[52:55], 0\n" \
  "v_cvt_pk_bf16_f32 v16, v56, v57\n" \
  "v_mfma_f32_16x16x32_bf16 v[108:111], a[160:163], v[48:51], 0\n" \
  "v_cvt_pk_bf16_f32 v17, v58, v59\n" \
  "v_mfma_f32_16x16x32_bf16 v[120:123], a[160:163], v[52:55], 0\n" \
  "v_cvt_pk_bf16_f32 v18, v60, v61\n" \
  "s_waitcnt lgkmcnt(2)\n" \
  "v_mfma_f32_16x16x32_bf16 v[100:103], a[4:7], v[32:35], v[100:103]\n" \
  "ds_read_b128 v[48:51], v9 offset:3072\n" \
  "v_mfma_f32_16x16x32_bf16 v[112:115], a[4:7], v[36:39], v[112:115]\n" \
  "ds_read_b128 v[52:55], v9 offset:23552\n" \
  "v_mfma_f32_16x16x32_bf16 v[104:107], a[84:87], v[32:35], v[104:107]\n" \
  "v_cvt_pk_bf16_f32 v19, v62, v63\n" \
  "v_mfma_f32_16x16x32_bf16 v[116:119], a[84:87], v[36:39], v[116:119]\n" \
  "buffer_store_dwordx4 v[16:19], v2, s[40:43], s69 offen sc1\n" \
  "v_mfma_f32_16x16x32_bf16 v[108:111], a[164:167], v[32:35], v[108:111]\n" \
  "v_cvt_pk_bf16_f32 v20, v68, v69\n" \
  "v_mfma_f32_16x16x32_bf16 v[120:123], a[164:167], v[36:39], v[120:123]\n" \
  "v_cvt_pk_bf16_f32 v21, v70, v71\n" \
  "s_waitcnt lgkmcnt(2)\n" \
  "v_mfma_f32_16x16x32_bf16 v[100:103], a[8:11], v[40:43], v[100:103]\n" \
  "ds_read_b128 v[32:35], v9 offset:4096\n" \
  "v_mfma_f32_16x16x32_bf16 v[112:115], a[8:11], v[44:47], v[112:115]\n" \
  "ds_read_b128 v[36:39], v9 offset:24576\n" \
  "v_mfma_f32_16x16x32_bf16 v[104:107], a[88:91], v[40:43], v[104:107]\n" \
  "v_cvt_pk_bf16_f32 v22, v72, v73\n" \
  "v_mfma_f32_16x16x32_bf16 v[116:119], a[88:91], v[44:47], v[116:119]\n" \
  "v_cvt_pk_bf16_f32 v23, v74, v75\n" \
  "v_mfma_f32_16x16x32_bf16 v[108:111], a[168:171], v[40:43], v[108:111]\n" \
  "buffer_store_dwordx4 v[20:23], v2, s[40:43], s69 offen offset:1024 sc1\n" \
  "v_mfma_f32_16x16x32_bf16 v[120:123], a[168:171], v[44:47], v[120:123]\n" \
  "v_cvt_pk_bf16_f32 v24, v64, v65\n" \
  "s_waitcnt lgkmcnt(2)\n" \
  "v_mfma_f32_16x16x32_bf16 v[100:103], a[12:15], v[48:51], v[100:103]\n" \
  "ds_read_b128 v[40:43], v9 offset:5120\n" \
  "v_mfma_f32_16x16x32_bf16 v[112:115], a[12:15], v[52:55], v[112:115]\n" \
  "ds_read_b128 v[44:47], v9 offset:25600\n" \
  "v_mfma_f32_16x16x32_bf16 v[104:107], a[92:95], v[48:51], v[104:107]\n" \
  "v_cvt_pk_bf16_f32 v25, v66, v67\n" \
  "v_mfma_f32_16x16x32_bf16 v[116:119], a[92:95], v[52:55], v[116:119]\n" \
  "v_cvt_pk_bf16_f32 v26, v76, v77\n" \
  "v_mfma_f32_16x16x32_bf16 v[108:111], a[172:175], v[48:51], v[108:111]\n" \
  "v_cvt_pk_bf16_f32 v27, v78, v79\n" \
  "v_mfma_f32_16x16x32_bf16 v[120:123], a[172:175], v[52:55], v[120:123]\n" \
  "buffer_store_dwordx4 v[24:27], v3, s[40:43], s69 offen sc1\n" \
  "s_waitcnt lgkmcnt(2)\n" \
  "v_mfma_f32_16x16x32_bf16 v[100:103], a[16:19], v[32:35], v[100:103]\n" \
  "ds_read_b128 v[48:51], v9 offset:6144\n" \
  "v_mfma_f32_16x16x32_bf16 v[112:115], a[16:19], v[36:39], v[112:115]\n" \
  "ds_read_b128 v[52:55], v9 offset:26624\n" \
  "v_mfma_f32_16x16x32_bf16 v[104:107], a[96:99], v[32:35], v[104:107]\n" \
  "s_add_u32 s72, s68, 4096\n" \
  "v_mfma_f32_16x16x32_bf16 v[116:119], a[96:99], v[36:39], v[116:119]\n" \
  "s_add_u32 s73, s68, 8192\n" \
  "v_mfma_f32_16x16x32_bf16 v[108:111], a[176:179], v[32:35], v[108:111]\n" \
  "s_add_u32 m0, s54, 0\n" \
  "v_mfma_f32_16x16x32_bf16 v[120:123], a[176:179], v[36:39], v[120:123]\n" \
  "buffer_load_dwordx4 v1, s[36:39], s68 offen offset:0 sc1 lds\n" \
  "s_waitcnt lgkmcnt(2)\n" \
  "v_mfma_f32_16x16x32_bf16 v[100:103], a[20:23], v[40:43], v[100:103]\n" \
  "ds_read_b128 v[32:35], v9 offset:7168\n" \
  "v_mfma_f32_16x16x32_bf16 v[112:115], a[20:23], v[44:47], v[112:115]\n" \
  "ds_read_b128 v[36:39], v9 offset:27648\n" \
  "v_mfma_f32_16x16x32_bf16 v[104:107], a[100:103], v[40:43], v[104:107]\n" \
  "buffer_load_dwordx4 v1, s[36:39], s68 offen offset:1024 sc1 lds\n" \
  "v_mfma_f32_16x16x32_bf16 v[116:119], a[100:103], v[44:47], v[116:119]\n" \
  "v_mfma_f32_16x16x32_bf16 v[108:111], a[180:183], v[40:43], v[108:111]\n" \
  "v_mfma_f32_16x16x32_bf16 v[120:123], a[180:183], v[44:47], v[120:123]\n" \
  "s_waitcnt lgkmcnt(2)\n" \
  "v_mfma_f32_16x16x32_bf16 v[100:103], a[24:27], v[48:51], v[100:103]\n" \
  "ds_read_b128 v[40:43], v9 offset:8192\n" \
  "v_mfma_f32_16x16x32_bf16 v[112:115], a[24:27], v[52:55], v[112:115]\n" \
  "ds_read_b128 v[44:47], v9 offset:28672\n" \
  "v_mfma_f32_16x16x32_bf16 v[104:107], a[104:107], v[48:51], v[104:107]\n" \
  "buffer_load_dwordx4 v1, s[36:39], s68 offen offset:2048 sc1 lds\n" \
  "v_mfma_f32_16x16x32_bf16 v[116:119], a[104:107], v[52:55], v[116:119]\n" \
  "v_mfma_f32_16x16x32_bf16 v[108:111], a[184:187], v[48:51], v[108:111]\n" \
  "v_mfma_f32_16x16x32_bf16 v[120:123], a[184:187], v[52:55], v[120:123]\n" \
  "s_waitcnt lgkmcnt(2)\n" \
  "v_mfma_f32_16x16x32_bf16 v[100:103], a[28:31], v[32:35], v[100:103]\n" \
  "ds_read_b128 v[48:51], v9 offset:9216\n" \
  "v_mfma_f32_16x16x32_bf16 v[112:115], a[28:31], v[36:39], v[112:115]\n" \
  "ds_read_b128 v[52:55], v9 offset:29696\n" \
  "v_mfma_f32_16x16x32_bf16 v[104:107], a[108:111], v[32:35], v[104:107]\n" \
  "buffer_load_dwordx4 v1, s[36:39], s68 offen offset:3072 sc1 lds\n" \
  "v_mfma_f32_16x16x32_bf16 v[116:119], a[108:111], v[36:39], v[116:119]\n" \
  "v_mfma_f32_16x16x32_bf16 v[108:111], a[188:191], v[32:35], v[108:111]\n" \
  "v_mfma_f32_16x16x32_bf16 v[120:123], a[188:191], v[36:39], v[120:123]\n" \
  "s_waitcnt lgkmcnt(2)\n" \
  "v_mfma_f32_16x16x32_bf16 v[100:103], a[32:35], v[40:43], v[100:103]\n" \
  "ds_read_b128 v[32:35], v9 offset:10240\n" \
  "v_mfma_f32_16x16x32_bf16 v[112:115], a[32:35], v[44:47], v[112:115]\n" \
  "ds_read_b128 v[36:39], v9 offset:30720\n" \
  "v_mfma_f32_16x16x32_bf16 v[104:107], a[112:115], v[40:43], v[104:107]\n" \
  "s_add_u32 m0, s54, 4096\n" \
  "v_mfma_f32_16x16x32_bf16 v[116:119], a[112:115], v[44:47], v[116:119]\n" \
  "buffer_load_dwordx4 v1, s[36:39], s72 offen offset:0 sc1 lds\n" \
  "v_mfma_f32_16x16x32_bf16 v[108:111], a[192:195], v[40:43], v[108:111]\n" \
  "v_mfma_f32_16x16x32_bf16 v[120:123], a[192:195], v[44:47], v[120:123]\n" \
  "s_waitcnt lgkmcnt(2)\n" \
  "v_mfma_f32_16x16x32_bf16 v[100:103], a[36:39], v[48:51], v[100:103]\n" \
  "ds_read_b128 v[40:43], v9 offset:11264\n" \
  "v_mfma_f32_16x16x32_bf16 v[112:115], a[36:39], v[52:55], v[112:115]\n" \
  "ds_read_b128 v[44:47], v9 offset:31744\n" \
  "v_mfma_f32_16x16x32_bf16 v[104:107], a[116:119], v[48:51], v[104:107]\n" \
  "buffer_load_dwordx4 v1, s[36:39], s72 offen offset:1024 sc1 lds\n" \
  "v_mfma_f32_16x16x32_bf16 v[116:119], a[116:119], v[52:55], v[116:119]\n" \
  "v_mfma_f32_16x16x32_bf16 v[108:111], a[196:199], v[48:51], v[108:111]\n" \
  "v_mfma_f32_16x16x32_bf16 v[120:123], a[196:199], v[52:55], v[120:123]\n" \
  "s_waitcnt lgkmcnt(2)\n" \
  "v_mfma_f32_16x16x32_bf16 v[100:103], a[40:43], v[32:35], v[100:103]\n" \
  "ds_read_b128 v[48:51], v9 offset:12288\n" \
  "v_mfma_f32_16x16x32_bf16 v[112:115], a[40:43], v[36:39], v[112:115]\n" \
  "ds_read_b128 v[52:55], v9 offset:32768\n" \
  "v_mfma_f32_16x16x32_bf16 v[104:107], a[120:123], v[32:35], v[104:107]\n" \
  "buffer_load_dwordx4 v1, s[36:39], s72 offen offset:2048 sc1 lds\n" \
  "v_mfma_f32_16x16x32_bf16 v[116:119], a[120:123], v[36:39], v[116:119]\n" \
  "v_mfma_f32_16x16x32_bf16 v[108:111], a[200:203], v[32:35], v[108:111]\n" \
  "v_mfma_f32_16x16x32_bf16 v[120:123], a[200:203], v[36:39], v[120:123]\n" \
  "s_waitcnt lgkmcnt(2)\n" \
  "v_mfma_f32_16x16x32_bf16 v[100:103], a[44:47], v[40:43], v[100:103]\n" \
  "ds_read_b128 v[32:35], v9 offset:13312\n" \
  "v_mfma_f32_16x16x32_bf16 v[112:115], a[44:47], v[44:47], v[112:115]\n" \
  "ds_read_b128 v[36:39], v9 offset:33792\n" \
  "v_mfma_f32_16x16x32_bf16 v[104:107], a[124:127], v[40:43], v[104:107]\n" \
  "buffer_load_dwordx4 v1, s[36:39], s72 offen offset:3072 sc1 lds\n" \
  "v_mfma_f32_16x16x32_bf16 v[116:119], a[124:127], v[44:47], v[116:119]\n" \
  "v_mfma_f32_16x16x32_bf16 v[108:111], a[204:207], v[40:43], v[108:111]\n" \
  "v_mfma_f32_16x16x32_bf16 v[120:123], a[204:207], v[44:47], v[120:123]\n" \
  "s_waitcnt lgkmcnt(2)\n" \
  "v_mfma_f32_16x16x32_bf16 v[100:103], a[48:51], v[48:51], v[100:103]\n" \
  "ds_read_b128 v[40:43], v9 offset:14336\n" \
  "v_mfma_f32_16x16x32_bf16 v[112:115], a[48:51], v[52:55], v[112:115]\n" \
  "ds_read_b128 v[44:47], v9 offset:34816\n" \
  "v_mfma_f32_16x16x32_bf16 v[104:107], a[128:131], v[48:51], v[104:107]\n" \
  "s_add_u32 m0, s54, 8192\n" \
  "v_mfma_f32_16x16x32_bf16 v[116:119], a[128:131], v[52:55], v[116:119]\n" \
  "buffer_load_dwordx4 v1, s[36:39], s73 offen offset:0 sc1 lds\n" \
  "v_mfma_f32_16x16x32_bf16 v[108:111], a[208:211], v[48:51], v[108:111]\n" \
  "v_mfma_f32_16x16x32_bf16 v[120:123], a[208:211], v[52:55], v[120:123]\n" \
  "s_waitcnt lgkmcnt(2)\n" \
  "v_mfma_f32_16x16x32_bf16 v[100:103], a[52:55], v[32:35], v[100:103]\n" \
  "ds_read_b128 v[48:51], v9 offset:15360\n" \
  "v_mfma_f32_16x16x32_bf16 v[112:115], a[52:55], v[36:39], v[112:115]\n" \
  "ds_read_b128 v[52:55], v9 offset:35840\n" \
  "v_mfma_f32_16x16x32_bf16 v[104:107], a[132:135], v[32:35], v[104:107]\n" \
  "buffer_load_dwordx4 v1, s[36:39], s73 offen offset:1024 sc1 lds\n" \
  "v_mfma_f32_16x16x32_bf16 v[116:119], a[132:135], v[36:39], v[116:119]\n" \
  "v_mfma_f32_16x16x32_bf16 v[108:111], a[212:215], v[32:35], v[108:111]\n" \
  "v_mfma_f32_16x16x32_bf16 v[120:123], a[212:215], v[36:39], v[120:123]\n" \
  "s_waitcnt lgkmcnt(2)\n" \
  "v_mfma_f32_16x16x32_bf16 v[100:103], a[56:59], v[40:43], v[100:103]\n" \
  "ds_read_b128 v[32:35], v9 offset:16384\n" \
  "v_mfma_f32_16x16x32_bf16 v[112:115], a[56:59], v[44:47], v[112:115]\n" \
  "ds_read_b128 v[36:39], v9 offset:36864\n" \
  "v_mfma_f32_16x16x32_bf16 v[104:107], a[136:139], v[40:43], v[104:107]\n" \
  "s_add_u32 s68, s68, 40960\n" \
  "v_mfma_f32_16x16x32_bf16 v[116:119], a[136:139], v[44:47], v[116:119]\n" \
  "v_mfma_f32_16x16x32_bf16 v[108:111], a[216:219], v[40:43], v[108:111]\n" \
  "v_mfma_f32_16x16x32_bf16 v[120:123], a[216:219], v[44:47], v[120:123]\n" \
  "s_waitcnt lgkmcnt(2)\n" \
  "v_mfma_f32_16x16x32_bf16 v[100:103], a[60:63], v[48:51], v[100:103]\n" \
  "ds_read_b128 v[40:43], v9 offset:17408\n" \
  "v_mfma_f32_16x16x32_bf16 v[112:115], a[60:63], v[52:55], v[112:115]\n" \
  "ds_read_b128 v[44:47], v9 offset:37888\n" \
  "v_mfma_f32_16x16x32_bf16 v[104:107], a[140:143], v[48:51], v[104:107]\n" \
  "v_mov_b32 v11, s52\n" \
  "v_mfma_f32_16x16x32_bf16 v[116:119], a[140:143], v[52:55], v[116:119]\n" \
  "s_waitcnt vmcnt(10)\n" \
  "v_mfma_f32_16x16x32_bf16 v[108:111], a[220:223], v[48:51], v[108:111]\n" \
  "buffer_store_dword v11, v5, s[40:43], 0 offen sc1\n" \
  "v_mfma_f32_16x16x32_bf16 v[120:123], a[220:223], v[52:55], v[120:123]\n" \
  "s_waitcnt lgkmcnt(2)\n" \
  "v_mfma_f32_16x16x32_bf16 v[100:103], a[64:67], v[32:35], v[100:103]\n" \
  "ds_read_b128 v[48:51], v9 offset:18432\n" \
  "v_mfma_f32_16x16x32_bf16 v[112:115], a[64:67], v[36:39], v[112:115]\n" \
  "ds_read_b128 v[52:55], v9 offset:38912\n" \
  "v_mfma_f32_16x16x32_bf16 v[104:107], a[144:147], v[32:35], v[104:107]\n" \
  "v_mfma_f32_16x16x32_bf16 v[116:119], a[144:147], v[36:39], v[116:119]\n" \
  "v_mfma_f32_16x16x32_bf16 v[108:111], a[224:227], v[32:35], v[108:111]\n" \
  "v_mfma_f32_16x16x32_bf16 v[120:123], a[224:227], v[36:39], v[120:123]\n" \
  "s_waitcnt lgkmcnt(2)\n" \
  "v_mfma_f32_16x16x32_bf16 v[100:103], a[68:71], v[40:43], v[100:103]\n" \
  "ds_read_b128 v[32:35], v9 offset:19456\n" \
  "v_mfma_f32_16x16x32_bf16 v[112:115], a[68:71], v[44:47], v[112:115]\n" \
  "ds_read_b128 v[36:39], v9 offset:39936\n" \
  "v_mfma_f32_16x16x32_bf16 v[104:107], a[148:151], v[40:43], v[104:107]\n" \
  "v_readlane_b32 s69, v4, s52\n" \
  "v_mfma_f32_16x16x32_bf16 v[116:119], a[148:151], v[44:47], v[116:119]\n" \
  "v_readlane_b32 s71, v8, s52\n" \
  "v_mfma_f32_16x16x32_bf16 v[108:111], a[228:231], v[40:43], v[108:111]\n" \
  "s_add_u32 s52, s52, 1\n" \
  "v_mfma_f32_16x16x32_bf16 v[120:123], a[228:231], v[44:47], v[120:123]\n" \
  "s_waitcnt lgkmcnt(0)\n" \
  "s_barrier\n" \
  "v_mfma_f32_16x16x32_bf16 v[100:103], a[72:75], v[48:51], v[100:103]\n" \
  "ds_read_b128 v[40:43], v10 offset:0\n" \
  "v_mfma_f32_16x16x32_bf16 v[112:115], a[72:75], v[52:55], v[112:115]\n" \
  "ds_read_b128 v[44:47], v10 offset:20480\n" \
  "v_mfma_f32_16x16x32_bf16 v[104:107], a[152:155], v[48:51], v[104:107]\n" \
  "v_mfma_f32_16x16x32_bf16 v[116:119], a[152:155], v[52:55], v[116:119]\n" \
  "v_mfma_f32_16x16x32_bf16 v[108:111], a[232:235], v[48:51], v[108:111]\n" \
  "v_mfma_f32_16x16x32_bf16 v[120:123], a[232:235], v[52:55], v[120:123]\n" \
  "v_mfma_f32_16x16x32_bf16 v[100:103], a[76:79], v[32:35], v[100:103]\n" \
  "ds_read_b128 v[48:51], v10 offset:1024\n" \
  "v_mfma_f32_16x16x32_bf16 v[112:115], a[76:79], v[36:39], v[112:115]\n" \
  "ds_read_b128 v[52:55], v10 offset:21504\n" \
  "v_mfma_f32_16x16x32_bf16 v[104:107], a[156:159], v[32:35], v[104:107]\n" \
  "s_cmp_eq_u32 s52, s53\n" \
  "v_mfma_f32_16x16x32_bf16 v[116:119], a[156:159], v[36:39], v[116:119]\n" \
  "v_mfma_f32_16x16x32_bf16 v[108:111], a[236:239], v[32:35], v[108:111]\n" \
  "v_mfma_f32_16x16x32_bf16 v[120:123], a[236:239], v[36:39], v[120:123]\n" \
  "s_waitcnt lgkmcnt(2)\n" \
  "v_mfma_f32_16x16x32_bf16 v[56:59], a[0:3], v[40:43], 0\n" \
  "ds_read_b128 v[32:35], v10 offset:2048\n" \
  "v_mfma_f32_16x16x32_bf16 v[68:71], a[0:3], v[44:47], 0\n" \
  "ds_read_b128 v[36:39], v10 offset:22528\n" \
  "v_mfma_f32_16x16x32_bf16 v[60:63], a[80:83], v[40:43], 0\n" \
  "buffer_store_dwordx4 v[12:15], v7, s[40:43], s71 offen sc1\n" \
  "v_mfma_f32_16x16x32_bf16 v[72:75], a[80:83], v[44:47], 0\n" \
  "v_cvt_pk_bf16_f32 v16, v100, v101\n" \
  "v_mfma_f32_16x16x32_bf16 v[64:67], a[160:163], v[40:43], 0\n" \
  "v_cvt_pk_bf16_f32 v17, v102, v103\n" \
  "v_mfma_f32_16x16x32_bf16 v[76:79], a[160:163], v[44:47], 0\n" \
  "v_cvt_pk_bf16_f32 v18, v104, v105\n" \
  "s_waitcnt lgkmcnt(2)\n" \
  "v_mfma_f32_16x16x32_bf16 v[56:59], a[4:7], v[48:51], v[56:59]\n" \
  "ds_read_b128 v[40:43], v10 offset:3072\n" \
  "v_mfma_f32_16x16x32_bf16 v[68:71], a[4:7], v[52:55], v[68:71]\n" \
  "ds_read_b128 v[44:47], v10 offset:23552\n" \
  "v_mfma_f32_16x16x32_bf16 v[60:63], a[84:87], v[48:51], v[60:63]\n" \
  "v_cvt_pk_bf16_f32 v19, v106, v107\n" \
  "v_mfma_f32_16x16x32_bf16 v[72:75], a[84:87], v[52:55], v[72:75]\n" \
  "buffer_store_dwordx4 v[16:19], v2, s[40:43], s69 offen sc1\n" \
  "v_mfma_f32_16x16x32_bf16 v[64:67], a[164:167], v[48:51], v[64:67]\n" \
  "v_cvt_pk_bf16_f32 v20, v112, v113\n" \
  "v_mfma_f32_16x16x32_bf16 v[76:79], a[164:167], v[52:55], v[76:79]\n" \
  "v_cvt_pk_bf16_f32 v21, v114, v115\n" \
  "s_waitcnt lgkmcnt(2)\n" \
  "v_mfma_f32_16x16x32_bf16 v[56:59], a[8:11], v[32:35], v[56:59]\n" \
  "ds_read_b128 v[48:51], v10 offset:4096\n" \
  "v_mfma_f32_16x16x32_bf16 v[68:71], a[8:11], v[36:39], v[68:71]\n" \
  "ds_read_b128 v[52:55], v10 offset:24576\n" \
  "v_mfma_f32_16x16x32_bf16 v[60:63], a[88:91], v[32:35], v[60:63]\n" \
  "v_cvt_pk_bf16_f32 v22, v116, v117\n" \
  "v_mfma_f32_16x16x32_bf16 v[72:75], a[88:91], v[36:39], v[72:75]\n" \
  "v_cvt_pk_bf16_f32 v23, v118, v119\n" \
  "v_mfma_f32_16x16x32_bf16 v[64:67], a[168:171], v[32:35], v[64:67]\n" \
  "buffer_store_dwordx4 v[20:23], v2, s[40:43], s69 offen offset:1024 sc1\n" \
  "v_mfma_f32_16x16x32_bf16 v[76:79], a[168:171], v[36:39], v[76:79]\n" \
  "v_cvt_pk_bf16_f32 v24, v108, v109\n" \
  "s_waitcnt lgkmcnt(2)\n" \
  "v_mfma_f32_16x16x32_bf16 v[56:59], a[12:15], v[40:43], v[56:59]\n" \
  "ds_read_b128 v[32:35], v10 offset:5120\n" \
  "v_mfma_f32_16x16x32_bf16 v[68:71], a[12:15], v[44:47], v[68:71]\n" \
  "ds_read_b128 v[36:39], v10 offset:25600\n" \
  "v_mfma_f32_16x16x32_bf16 v[60:63], a[92:95], v[40:43], v[60:63]\n" \
  "v_cvt_pk_bf16_f32 v25, v110, v111\n" \
  "v_mfma_f32_16x16x32_bf16 v[72:75], a[92:95], v[44:47], v[72:75]\n" \
  "v_cvt_pk_bf16_f32 v26, v120, v121\n" \
  "v_mfma_f32_16x16x32_bf16 v[64:67], a[172:175], v[40:43], v[64:67]\n" \
  "v_cvt_pk_bf16_f32 v27, v122, v123\n" \
  "v_mfma_f32_16x16x32_bf16 v[76:79], a[172:175], v[44:47], v[76:79]\n" \
  "buffer_store_dwordx4 v[24:27], v3, s[40:43], s69 offen sc1\n" \
  "s_waitcnt lgkmcnt(2)\n" \
  "v_mfma_f32_16x16x32_bf16 v[56:59], a[16:19], v[48:51], v[56:59]\n" \
  "ds_read_b128 v[40:43], v10 offset:6144\n" \
  "v_mfma_f32_16x16x32_bf16 v[68:71], a[16:19], v[52:55], v[68:71]\n" \
  "ds_read_b128 v[44:47], v10 offset:26624\n" \
  "v_mfma_f32_16x16x32_bf16 v[60:63], a[96:99], v[48:51], v[60:63]\n" \
  "s_add_u32 s72, s68, 4096\n" \
  "v_mfma_f32_16x16x32_bf16 v[72:75], a[96:99], v[52:55], v[72:75]\n" \
  "s_add_u32 s73, s68, 8192\n" \
  "v_mfma_f32_16x16x32_bf16 v[64:67], a[176:179], v[48:51], v[64:67]\n" \
  "s_add_u32 m0, s54, 40960\n" \
  "v_mfma_f32_16x16x32_bf16 v[76:79], a[176:179], v[52:55], v[76:79]\n" \
  "buffer_load_dwordx4 v1, s[36:39], s68 offen offset:0 sc1 lds\n" \
  "s_waitcnt lgkmcnt(2)\n" \
  "v_mfma_f32_16x16x32_bf16 v[56:59], a[20:23], v[32:35], v[56:59]\n" \
  "ds_read_b128 v[48:51], v10 offset:7168\n" \
  "v_mfma_f32_16x16x32_bf16 v[68:71], a[20:23], v[36:39], v[68:71]\n" \
  "ds_read_b128 v[52:55], v10 offset:27648\n" \
  "v_mfma_f32_16x16x32_bf16 v[60:63], a[100:103], v[32:35], v[60:63]\n" \
  "buffer_load_dwordx4 v1, s[36:39], s68 offen offset:1024 sc1 lds\n" \
  "v_mfma_f32_16x16x32_bf16 v[72:75], a[100:103], v[36:39], v[72:75]\n" \
  "v_mfma_f32_16x16x32_bf16 v[64:67], a[180:183], v[32:35], v[64:67]\n" \
  "v_mfma_f32_16x16x32_bf16 v[76:79], a[180:183], v[36:39], v[76:79]\n" \
  "s_waitcnt lgkmcnt(2)\n" \
  "v_mfma_f32_16x16x32_bf16 v[56:59], a[24:27], v[40:43], v[56:59]\n" \
  "ds_read_b128 v[32:35], v10 offset:8192\n" \
  "v_mfma_f32_16x16x32_bf16 v[68:71], a[24:27], v[44:47], v[68:71]\n" \
  "ds_read_b128 v[36:39], v10 offset:28672\n" \
  "v_mfma_f32_16x16x32_bf16 v[60:63], a[104:107], v[40:43], v[60:63]\n" \
  "buffer_load_dwordx4 v1, s[36:39], s68 offen offset:2048 sc1 lds\n" \
  "v_mfma_f32_16x16x32_bf16 v[72:75], a[104:107], v[44:47], v[72:75]\n" \
  "v_mfma_f32_16x16x32_bf16 v[64:67], a[184:187], v[40:43], v[64:67]\n" \
  "v_mfma_f32_16x16x32_bf16 v[76:79], a[184:187], v[44:47], v[76:79]\n" \
  "s_waitcnt lgkmcnt(2)\n" \
  "v_mfma_f32_16x16x32_bf16 v[56:59], a[28:31], v[48:51], v[56:59]\n" \
  "ds_read_b128 v[40:43], v10 offset:9216\n" \
  "v_mfma_f32_16x16x32_bf16 v[68:71], a[28:31], v[52:55], v[68:71]\n" \
  "ds_read_b128 v[44:47], v10 offset:29696\n" \
  "v_mfma_f32_16x16x32_bf16 v[60:63], a[108:111], v[48:51], v[60:63]\n" \
  "buffer_load_dwordx4 v1, s[36:39], s68 offen offset:3072 sc1 lds\n" \
  "v_mfma_f32_16x16x32_bf16 v[72:75], a[108:111], v[52:55], v[72:75]\n" \
  "v_mfma_f32_16x16x32_bf16 v[64:67], a[188:191], v[48:51], v[64:67]\n" \
  "v_mfma_f32_16x16x32_bf16 v[76:79], a[188:191], v[52:55], v[76:79]\n" \
  "s_waitcnt lgkmcnt(2)\n" \
  "v_mfma_f32_16x16x32_bf16 v[56:59], a[32:35], v[32:35], v[56:59]\n" \
  "ds_read_b128 v[48:51], v10 offset:10240\n" \
  "v_mfma_f32_16x16x32_bf16 v[68:71], a[32:35], v[36:39], v[68:71]\n" \
  "ds_read_b128 v[52:55], v10 offset:30720\n" \
  "v_mfma_f32_16x16x32_bf16 v[60:63], a[112:115], v[32:35], v[60:63]\n" \
  "s_add_u32 m0, s54, 45056\n" \
  "v_mfma_f32_16x16x32_bf16 v[72:75], a[112:115], v[36:39], v[72:75]\n" \
  "buffer_load_dwordx4 v1, s[36:39], s72 offen offset:0 sc1 lds\n" \
  "v_mfma_f32_16x16x32_bf16 v[64:67], a[192:195], v[32:35], v[64:67]\n" \
  "v_mfma_f32_16x16x32_bf16 v[76:79], a[192:195], v[36:39], v[76:79]\n" \
  "s_waitcnt lgkmcnt(2)\n" \
  "v_mfma_f32_16x16x32_bf16 v[56:59], a[36:39], v[40:43], v[56:59]\n" \
  "ds_read_b128 v[32:35], v10 offset:11264\n" \
  "v_mfma_f32_16x16x32_bf16 v[68:71], a[36:39], v[44:47], v[68:71]\n" \
  "ds_read_b128 v[36:39], v10 offset:31744\n" \
  "v_mfma_f32_16x16x32_bf16 v[60:63], a[116:119], v[40:43], v[60:63]\n" \
  "buffer_load_dwordx4 v1, s[36:39], s72 offen offset:1024 sc1 lds\n" \
  "v_mfma_f32_16x16x32_bf16 v[72:75], a[116:119], v[44:47], v[72:75]\n" \
  "v_mfma_f32_16x16x32_bf16 v[64:67], a[196:199], v[40:43], v[64:67]\n" \
  "v_mfma_f32_16x16x32_bf16 v[76:79], a[196:199], v[44:47], v[76:79]\n" \
  "s_waitcnt lgkmcnt(2)\n" \
  "v_mfma_f32_16x16x32_bf16 v[56:59], a[40:43], v[48:51], v[56:59]\n" \
  "ds_read_b128 v[40:43], v10 offset:12288\n" \
  "v_mfma_f32_16x16x32_bf16 v[68:71], a[40:43], v[52:55], v[68:71]\n" \
  "ds_read_b128 v[44:47], v10 offset:32768\n" \
  "v_mfma_f32_16x16x32_bf16 v[60:63], a[120:123], v[48:51], v[60:63]\n" \
  "buffer_load_dwordx4 v1, s[36:39], s72 offen offset:2048 sc1 lds\n" \
  "v_mfma_f32_16x16x32_bf16 v[72:75], a[120:123], v[52:55], v[72:75]\n" \
  "v_mfma_f32_16x16x32_bf16 v[64:67], a[200:203], v[48:51], v[64:67]\n" \
  "v_mfma_f32_16x16x32_bf16 v[76:79], a[200:203], v[52:55], v[76:79]\n" \
  "s_waitcnt lgkmcnt(2)\n" \
  "v_mfma_f32_16x16x32_bf16 v[56:59], a[44:47], v[32:35], v[56:59]\n" \
  "ds_read_b128 v[48:51], v10 offset:13312\n" \
  "v_mfma_f32_16x16x32_bf16 v[68:71], a[44:47], v[36:39], v[68:71]\n" \
  "ds_read_b128 v[52:55], v10 offset:33792\n" \
  "v_mfma_f32_16x16x32_bf16 v[60:63], a[124:127], v[32:35], v[60:63]\n" \
  "buffer_load_dwordx4 v1, s[36:39], s72 offen offset:3072 sc1 lds\n" \
  "v_mfma_f32_16x16x32_bf16 v[72:75], a[124:127], v[36:39], v[72:75]\n" \
  "v_mfma_f32_16x16x32_bf16 v[64:67], a[204:207], v[32:35], v[64:67]\n" \
  "v_mfma_f32_16x16x32_bf16 v[76:79], a[204:207], v[36:39], v[76:79]\n" \
  "s_waitcnt lgkmcnt(2)\n" \
  "v_mfma_f32_16x16x32_bf16 v[56:59], a[48:51], v[40:43], v[56:59]\n" \
  "ds_read_b128 v[32:35], v10 offset:14336\n" \
  "v_mfma_f32_16x16x32_bf16 v[68:71], a[48:51], v[44:47], v[68:71]\n" \
  "ds_read_b128 v[36:39], v10 offset:34816\n" \
  "v_mfma_f32_16x16x32_bf16 v[60:63], a[128:131], v[40:43], v[60:63]\n" \
  "s_add_u32 m0, s54, 49152\n" \
  "v_mfma_f32_16x16x32_bf16 v[72:75], a[128:131], v[44:47], v[72:75]\n" \
  "buffer_load_dwordx4 v1, s[36:39], s73 offen offset:0 sc1 lds\n" \
  "v_mfma_f32_16x16x32_bf16 v[64:67], a[208:211], v[40:43], v[64:67]\n" \
  "v_mfma_f32_16x16x32_bf16 v[76:79], a[208:211], v[44:47], v[76:79]\n" \
  "s_waitcnt lgkmcnt(2)\n" \
  "v_mfma_f32_16x16x32_bf16 v[56:59], a[52:55], v[48:51], v[56:59]\n" \
  "ds_read_b128 v[40:43], v10 offset:15360\n" \
  "v_mfma_f32_16x16x32_bf16 v[68:71], a[52:55], v[52:55], v[68:71]\n" \
  "ds_read_b128 v[44:47], v10 offset:35840\n" \
  "v_mfma_f32_16x16x32_bf16 v[60:63], a[132:135], v[48:51], v[60:63]\n" \
  "buffer_load_dwordx4 v1, s[36:39], s73 offen offset:1024 sc1 lds\n" \
  "v_mfma_f32_16x16x32_bf16 v[72:75], a[132:135], v[52:55], v[72:75]\n" \
  "v_mfma_f32_16x16x32_bf16 v[64:67], a[212:215], v[48:51], v[64:67]\n" \
  "v_mfma_f32_16x16x32_bf16 v[76:79], a[212:215], v[52:55], v[76:79]\n" \
  "s_waitcnt lgkmcnt(2)\n" \
  "v_mfma_f32_16x16x32_bf16 v[56:59], a[56:59], v[32:35], v[56:59]\n" \
  "ds_read_b128 v[48:51], v10 offset:16384\n" \
  "v_mfma_f32_16x16x32_bf16 v[68:71], a[56:59], v[36:39], v[68:71]\n" \
  "ds_read_b128 v[52:55], v10 offset:36864\n" \
  "v_mfma_f32_16x16x32_bf16 v[60:63], a[136:139], v[32:35], v[60:63]\n" \
  "s_add_u32 s68, s68, 40960\n" \
  "v_mfma_f32_16x16x32_bf16 v[72:75], a[136:139], v[36:39], v[72:75]\n" \
  "v_mfma_f32_16x16x32_bf16 v[64:67], a[216:219], v[32:35], v[64:67]\n" \
  "v_mfma_f32_16x16x32_bf16 v[76:79], a[216:219], v[36:39], v[76:79]\n" \
  "s_waitcnt lgkmcnt(2)\n" \
  "v_mfma_f32_16x16x32_bf16 v[56:59], a[60:63], v[40:43], v[56:59]\n" \
  "ds_read_b128 v[32:35], v10 offset:17408\n" \
  "v_mfma_f32_16x16x32_bf16 v[68:71], a[60:63], v[44:47], v[68:71]\n" \
  "ds_read_b128 v[36:39], v10 offset:37888\n" \
  "v_mfma_f32_16x16x32_bf16 v[60:63], a[140:143], v[40:43], v[60:63]\n" \
  "v_mov_b32 v11, s52\n" \
  "v_mfma_f32_16x16x32_bf16 v[72:75], a[140:143], v[44:47], v[72:75]\n" \
  "s_waitcnt vmcnt(10)\n" \
  "v_mfma_f32_16x16x32_bf16 v[64:67], a[220:223], v[40:43], v[64:67]\n" \
  "buffer_store_dword v11, v5, s[40:43], 0 offen sc1\n" \
  "v_mfma_f32_16x16x32_bf16 v[76:79], a[220:223], v[44:47], v[76:79]\n" \
  "s_waitcnt lgkmcnt(2)\n" \
  "v_mfma_f32_16x16x32_bf16 v[56:59], a[64:67], v[48:51], v[56:59]\n" \
  "ds_read_b128 v[40:43], v10 offset:18432\n" \
  "v_mfma_f32_16x16x32_bf16 v[68:71], a[64:67], v[52:55], v[68:71]\n" \
  "ds_read_b128 v[44:47], v10 offset:38912\n" \
  "v_mfma_f32_16x16x32_bf16 v[60:63], a[144:147], v[48:51], v[60:63]\n" \
  "v_mfma_f32_16x16x32_bf16 v[72:75], a[144:147], v[52:55], v[72:75]\n" \
  "v_mfma_f32_16x16x32_bf16 v[64:67], a[224:227], v[48:51], v[64:67]\n" \
  "v_mfma_f32_16x16x32_bf16 v[76:79], a[224:227], v[52:55], v[76:79]\n" \
  "s_waitcnt lgkmcnt(2)\n" \
  "v_mfma_f32_16x16x32_bf16 v[56:59], a[68:71], v[32:35], v[56:59]\n" \
  "ds_read_b128 v[48:51], v10 offset:19456\n" \
  "v_mfma_f32_16x16x32_bf16 v[68:71], a[68:71], v[36:39], v[68:71]\n" \
  "ds_read_b128 v[52:55], v10 offset:39936\n" \
  "v_mfma_f32_16x16x32_bf16 v[60:63], a[148:151], v[32:35], v[60:63]\n" \
  "v_readlane_b32 s69, v4, s52\n" \
  "v_mfma_f32_16x16x32_bf16 v[72:75], a[148:151], v[36:39], v[72:75]\n" \
  "v_readlane_b32 s71, v8, s52\n" \
  "v_mfma_f32_16x16x32_bf16 v[64:67], a[228:231], v[32:35], v[64:67]\n" \
  "s_add_u32 s52, s52, 1\n" \
  "v_mfma_f32_16x16x32_bf16 v[76:79], a[228:231], v[36:39], v[76:79]\n" \
  "s_waitcnt lgkmcnt(0)\n" \
  "s_barrier\n" \
  "v_mfma_f32_16x16x32_bf16 v[56:59], a[72:75], v[40:43], v[56:59]\n" \
  "ds_read_b128 v[32:35], v0 offset:0\n" \
  "v_mfma_f32_16x16x32_bf16 v[68:71], a[72:75], v[44:47], v[68:71]\n" \
  "ds_read_b128 v[36:39], v0 offset:20480\n" \
  "v_mfma_f32_16x16x32_bf16 v[60:63], a[152:155], v[40:43], v[60:63]\n" \
  "v_mfma_f32_16x16x32_bf16 v[72:75], a[152:155], v[44:47], v[72:75]\n" \
  "v_mfma_f32_16x16x32_bf16 v[64:67], a[232:235], v[40:43], v[64:67]\n" \
  "v_mfma_f32_16x16x32_bf16 v[76:79], a[232:235], v[44:47], v[76:79]\n" \
  "v_mfma_f32_16x16x32_bf16 v[56:59], a[76:79], v[48:51], v[56:59]\n" \
  "ds_read_b128 v[40:43], v0 offset:1024\n" \
  "v_mfma_f32_16x16x32_bf16 v[68:71], a[76:79], v[52:55], v[68:71]\n" \
  "ds_read_b128 v[44:47], v0 offset:21504\n" \
  "v_mfma_f32_16x16x32_bf16 v[60:63], a[156:159], v[48:51], v[60:63]\n" \
  "s_cmp_eq_u32 s52, s53\n" \
  "v_mfma_f32_16x16x32_bf16 v[72:75], a[156:159], v[52:55], v[72:75]\n" \
  "v_mfma_f32_16x16x32_bf16 v[64:67], a[236:239], v[48:51], v[64:67]\n" \
  "v_mfma_f32_16x16x32_bf16 v[76:79], a[236:239], v[52:55], v[76:79]\n" \
  "s_waitcnt lgkmcnt(2)\n" \
  "v_mfma_f32_16x16x32_bf16 v[100:103], a[0:3], v[32:35], 0\n" \
  "ds_read_b128 v[48:51], v0 offset:2048\n" \
  "v_mfma_f32_16x16x32_bf16 v[112:115], a[0:3], v[36:39], 0\n" \
  "ds_read_b128 v[52:55], v0 offset:22528\n" \
  "v_mfma_f32_16x16x32_bf16 v[104:107], a[80:83], v[32:35], 0\n" \
  "buffer_store_dwordx4 v[12:15], v7, s[40:43], s71 offen sc1\n" \
  "v_mfma_f32_16x16x32_bf16 v[116:119], a[80:83], v[36:39], 0\n" \
  "v_cvt_pk_bf16_f32 v16, v56, v57\n" \
  "v_mfma_f32_16x16x32_bf16 v[108:111], a[160:163], v[32:35], 0\n" \
  "v_cvt_pk_bf16_f32 v17, v58, v59\n" \
  "v_mfma_f32_16x16x32_bf16 v[120:123], a[160:163], v[36:39], 0\n" \
  "v_cvt_pk_bf16_f32 v18, v60, v61\n" \
  "s_waitcnt lgkmcnt(2)\n" \
  "v_mfma_f32_16x16x32_bf16 v[100:103], a[4:7], v[40:43], v[100:103]\n" \
  "ds_read_b128 v[32:35], v0 offset:3072\n" \
  "v_mfma_f32_16x16x32_bf16 v[112:115], a[4:7], v[44:47], v[112:115]\n" \
  "ds_read_b128 v[36:39], v0 offset:23552\n" \
  "v_mfma_f32_16x16x32_bf16 v[104:107], a[84:87], v[40:43], v[104:107]\n" \
  "v_cvt_pk_bf16_f32 v19, v62, v63\n" \
  "v_mfma_f32_16x16x32_bf16 v[116:119], a[84:87], v[44:47], v[116:119]\n" \
  "buffer_store_dwordx4 v[16:19], v2, s[40:43], s69 offen sc1\n" \
  "v_mfma_f32_16x16x32_bf16 v[108:111], a[164:167], v[40:43], v[108:111]\n" \
  "v_cvt_pk_bf16_f32 v20, v68, v69\n" \
  "v_mfma_f32_16x16x32_bf16 v[120:123], a[164:167], v[44:47], v[120:123]\n" \
  "v_cvt_pk_bf16_f32 v21, v70, v71\n" \
  "s_waitcnt lgkmcnt(2)\n" \
  "v_mfma_f32_16x16x32_bf16 v[100:103], a[8:11], v[48:51], v[100:103]\n" \
  "ds_read_b128 v[40:43], v0 offset:4096\n" \
  "v_mfma_f32_16x16x32_bf16 v[112:115], a[8:11], v[52:55], v[112:115]\n" \
  "ds_read_b128 v[44:47], v0 offset:24576\n" \
  "v_mfma_f32_16x16x32_bf16 v[104:107], a[88:91], v[48:51], v[104:107]\n" \
  "v_cvt_pk_bf16_f32 v22, v72, v73\n" \
  "v_mfma_f32_16x16x32_bf16 v[116:119], a[88:91], v[52:55], v[116:119]\n" \
  "v_cvt_pk_bf16_f32 v23, v74, v75\n" \
  "v_mfma_f32_16x16x32_bf16 v[108:111], a[168:171], v[48:51], v[108:111]\n" \
  "buffer_store_dwordx4 v[20:23], v2, s[40:43], s69 offen offset:1024 sc1\n" \
  "v_mfma_f32_16x16x32_bf16 v[120:123], a[168:171], v[52:55], v[120:123]\n" \
  "v_cvt_pk_bf16_f32 v24, v64, v65\n" \
  "s_waitcnt lgkmcnt(2)\n" \
  "v_mfma_f32_16x16x32_bf16 v[100:103], a[12:15], v[32:35], v[100:103]\n" \
  "ds_read_b128 v[48:51], v0 offset:5120\n" \
  "v_mfma_f32_16x16x32_bf16 v[112:115], a[12:15], v[36:39], v[112:115]\n" \
  "ds_read_b128 v[52:55], v0 offset:25600\n" \
  "v_mfma_f32_16x16x32_bf16 v[104:107], a[92:95], v[32:35], v[104:107]\n" \
  "v_cvt_pk_bf16_f32 v25, v66, v67\n" \
  "v_mfma_f32_16x16x32_bf16 v[116:119], a[92:95], v[36:39], v[116:119]\n" \
  "v_cvt_pk_bf16_f32 v26, v76, v77\n" \
  "v_mfma_f32_16x16x32_bf16 v[108:111], a[172:175], v[32:35], v[108:111]\n" \
  "v_cvt_pk_bf16_f32 v27, v78, v79\n" \
  "v_mfma_f32_16x16x32_bf16 v[120:123], a[172:175], v[36:39], v[120:123]\n" \
  "buffer_store_dwordx4 v[24:27], v3, s[40:43], s69 offen sc1\n" \
  "s_waitcnt lgkmcnt(2)\n" \
  "v_mfma_f32_16x16x32_bf16 v[100:103], a[16:19], v[40:43], v[100:103]\n" \
  "ds_read_b128 v[32:35], v0 offset:6144\n" \
  "v_mfma_f32_16x16x32_bf16 v[112:115], a[16:19], v[44:47], v[112:115]\n" \
  "ds_read_b128 v[36:39], v0 offset:26624\n" \
  "v_mfma_f32_16x16x32_bf16 v[104:107], a[96:99], v[40:43], v[104:107]\n" \
  "s_add_u32 s72, s68, 4096\n" \
  "v_mfma_f32_16x16x32_bf16 v[116:119], a[96:99], v[44:47], v[116:119]\n" \
  "s_add_u32 s73, s68, 8192\n" \
  "v_mfma_f32_16x16x32_bf16 v[108:111], a[176:179], v[40:43], v[108:111]\n" \
  "s_add_u32 m0, s54, 81920\n" \
  "v_mfma_f32_16x16x32_bf16 v[120:123], a[176:179], v[44:47], v[120:123]\n" \
  "buffer_load_dwordx4 v1, s[36:39], s68 offen offset:0 sc1 lds\n" \
  "s_waitcnt lgkmcnt(2)\n" \
  "v_mfma_f32_16x16x32_bf16 v[100:103], a[20:23], v[48:51], v[100:103]\n" \
  "ds_read_b128 v[40:43], v0 offset:7168\n" \
  "v_mfma_f32_16x16x32_bf16 v[112:115], a[20:23], v[52:55], v[112:115]\n" \
  "ds_read_b128 v[44:47], v0 offset:27648\n" \
  "v_mfma_f32_16x16x32_bf16 v[104:107], a[100:103], v[48:51], v[104:107]\n" \
  "buffer_load_dwordx4 v1, s[36:39], s68 offen offset:1024 sc1 lds\n" \
  "v_mfma_f32_16x16x32_bf16 v[116:119], a[100:103], v[52:55], v[116:119]\n" \
  "v_mfma_f32_16x16x32_bf16 v[108:111], a[180:183], v[48:51], v[108:111]\n" \
  "v_mfma_f32_16x16x32_bf16 v[120:123], a[180:183], v[52:55], v[120:123]\n" \
  "s_waitcnt lgkmcnt(2)\n" \
  "v_mfma_f32_16x16x32_bf16 v[100:103], a[24:27], v[32:35], v[100:103]\n" \
  "ds_read_b128 v[48:51], v0 offset:8192\n" \
  "v_mfma_f32_16x16x32_bf16 v[112:115], a[24:27], v[36:39], v[112:115]\n" \
  "ds_read_b128 v[52:55], v0 offset:28672\n" \
  "v_mfma_f32_16x16x32_bf16 v[104:107], a[104:107], v[32:35], v[104:107]\n" \
  "buffer_load_dwordx4 v1, s[36:39], s68 offen offset:2048 sc1 lds\n" \
  "v_mfma_f32_16x16x32_bf16 v[116:119], a[104:107], v[36:39], v[116:119]\n" \
  "v_mfma_f32_16x16x32_bf16 v[108:111], a[184:187], v[32:35], v[108:111]\n" \
  "v_mfma_f32_16x16x32_bf16 v[120:123], a[184:187], v[36:39], v[120:123]\n" \
  "s_waitcnt lgkmcnt(2)\n" \
  "v_mfma_f32_16x16x32_bf16 v[100:103], a[28:31], v[40:43], v[100:103]\n" \
  "ds_read_b128 v[32:35], v0 offset:9216\n" \
  "v_mfma_f32_16x16x32_bf16 v[112:115], a[28:31], v[44:47], v[112:115]\n" \
  "ds_read_b128 v[36:39], v0 offset:29696\n" \
  "v_mfma_f32_16x16x32_bf16 v[104:107], a[108:111], v[40:43], v[104:107]\n" \
  "buffer_load_dwordx4 v1, s[36:39], s68 offen offset:3072 sc1 lds\n" \
  "v_mfma_f32_16x16x32_bf16 v[116:119], a[108:111], v[44:47], v[116:119]\n" \
  "v_mfma_f32_16x16x32_bf16 v[108:111], a[188:191], v[40:43], v[108:111]\n" \
  "v_mfma_f32_16x16x32_bf16 v[120:123], a[188:191], v[44:47], v[120:123]\n" \
  "s_waitcnt lgkmcnt(2)\n" \
  "v_mfma_f32_16x16x32_bf16 v[100:103], a[32:35], v[48:51], v[100:103]\n" \
  "ds_read_b128 v[40:43], v0 offset:10240\n" \
  "v_mfma_f32_16x16x32_bf16 v[112:115], a[32:35], v[52:55], v[112:115]\n" \
  "ds_read_b128 v[44:47], v0 offset:30720\n" \
  "v_mfma_f32_16x16x32_bf16 v[104:107], a[112:115], v[48:51], v[104:107]\n" \
  "s_add_u32 m0, s54, 86016\n" \
  "v_mfma_f32_16x16x32_bf16 v[116:119], a[112:115], v[52:55], v[116:119]\n" \
  "buffer_load_dwordx4 v1, s[36:39], s72 offen offset:0 sc1 lds\n" \
  "v_mfma_f32_16x16x32_bf16 v[108:111], a[192:195], v[48:51], v[108:111]\n" \
  "v_mfma_f32_16x16x32_bf16 v[120:123], a[192:195], v[52:55], v[120:123]\n" \
  "s_waitcnt lgkmcnt(2)\n" \
  "v_mfma_f32_16x16x32_bf16 v[100:103], a[36:39], v[32:35], v[100:103]\n" \
  "ds_read_b128 v[48:51], v0 offset:11264\n" \
  "v_mfma_f32_16x16x32_bf16 v[112:115], a[36:39], v[36:39], v[112:115]\n" \
  "ds_read_b128 v[52:55], v0 offset:31744\n" \
  "v_mfma_f32_16x16x32_bf16 v[104:107], a[116:119], v[32:35], v[104:107]\n" \
  "buffer_load_dwordx4 v1, s[36:39], s72 offen offset:1024 sc1 lds\n" \
  "v_mfma_f32_16x16x32_bf16 v[116:119], a[116:119], v[36:39], v[116:119]\n" \
  "v_mfma_f32_16x16x32_bf16 v[108:111], a[196:199], v[32:35], v[108:111]\n" \
  "v_mfma_f32_16x16x32_bf16 v[120:123], a[196:199], v[36:39], v[120:123]\n" \
  "s_waitcnt lgkmcnt(2)\n" \
  "v_mfma_f32_16x16x32_bf16 v[100:103], a[40:43], v[40:43], v[100:103]\n" \
  "ds_read_b128 v[32:35], v0 offset:12288\n" \
  "v_mfma_f32_16x16x32_bf16 v[112:115], a[40:43], v[44:47], v[112:115]\n" \
  "ds_read_b128 v[36:39], v0 offset:32768\n" \
  "v_mfma_f32_16x16x32_bf16 v[104:107], a[120:123], v[40:43], v[104:107]\n" \
  "buffer_load_dwordx4 v1, s[36:39], s72 offen offset:2048 sc1 lds\n" \
  "v_mfma_f32_16x16x32_bf16 v[116:119], a[120:123], v[44:47], v[116:119]\n" \
  "v_mfma_f32_16x16x32_bf16 v[108:111], a[200:203], v[40:43], v[108:111]\n" \
  "v_mfma_f32_16x16x32_bf16 v[120:123], a[200:203], v[44:47], v[120:123]\n" \
  "s_waitcnt lgkmcnt(2)\n" \
  "v_mfma_f32_16x16x32_bf16 v[100:103], a[44:47], v[48:51], v[100:103]\n" \
  "ds_read_b128 v[40:43], v0 offset:13312\n" \
  "v_mfma_f32_16x16x32_bf16 v[112:115], a[44:47], v[52:55], v[112:115]\n" \
  "ds_read_b128 v[44:47], v0 offset:33792\n" \
  "v_mfma_f32_16x16x32_bf16 v[104:107], a[124:127], v[48:51], v[104:107]\n" \
  "buffer_load_dwordx4 v1, s[36:39], s72 offen offset:3072 sc1 lds\n" \
  "v_mfma_f32_16x16x32_bf16 v[116:119], a[124:127], v[52:55], v[116:119]\n" \
  "v_mfma_f32_16x16x32_bf16 v[108:111], a[204:207], v[48:51], v[108:111]\n" \
  "v_mfma_f32_16x16x32_bf16 v[120:123], a[204:207], v[52:55], v[120:123]\n" \
  "s_waitcnt lgkmcnt(2)\n" \
  "v_mfma_f32_16x16x32_bf16 v[100:103], a[48:51], v[32:35], v[100:103]\n" \
  "ds_read_b128 v[48:51], v0 offset:14336\n" \
  "v_mfma_f32_16x16x32_bf16 v[112:115], a[48:51], v[36:39], v[112:115]\n" \
  "ds_read_b128 v[52:55], v0 offset:34816\n" \
  "v_mfma_f32_16x16x32_bf16 v[104:107], a[128:131], v[32:35], v[104:107]\n" \
  "s_add_u32 m0, s54, 90112\n" \
  "v_mfma_f32_16x16x32_bf16 v[116:119], a[128:131], v[36:39], v[116:119]\n" \
  "buffer_load_dwordx4 v1, s[36:39], s73 offen offset:0 sc1 lds\n" \
  "v_mfma_f32_16x16x32_bf16 v[108:111], a[208:211], v[32:35], v[108:111]\n" \
  "v_mfma_f32_16x16x32_bf16 v[120:123], a[208:211], v[36:39], v[120:123]\n" \
  "s_waitcnt lgkmcnt(2)\n" \
  "v_mfma_f32_16x16x32_bf16 v[100:103], a[52:55], v[40:43], v[100:103]\n" \
  "ds_read_b128 v[32:35], v0 offset:15360\n" \
  "v_mfma_f32_16x16x32_bf16 v[112:115], a[52:55], v[44:47], v[112:115]\n" \
  "ds_read_b128 v[36:39], v0 offset:35840\n" \
  "v_mfma_f32_16x16x32_bf16 v[104:107], a[132:135], v[40:43], v[104:107]\n" \
  "buffer_load_dwordx4 v1, s[36:39], s73 offen offset:1024 sc1 lds\n" \
  "v_mfma_f32_16x16x32_bf16 v[116:119], a[132:135], v[44:47], v[116:119]\n" \
  "v_mfma_f32_16x16x32_bf16 v[108:111], a[212:215], v[40:43], v[108:111]\n" \
  "v_mfma_f32_16x16x32_bf16 v[120:123], a[212:215], v[44:47], v[120:123]\n" \
  "s_waitcnt lgkmcnt(2)\n" \
  "v_mfma_f32_16x16x32_bf16 v[100:103], a[56:59], v[48:51], v[100:103]\n" \
  "ds_read_b128 v[40:43], v0 offset:16384\n" \
  "v_mfma_f32_16x16x32_bf16 v[112:115], a[56:59], v[52:55], v[112:115]\n" \
  "ds_read_b128 v[44:47], v0 offset:36864\n" \
  "v_mfma_f32_16x16x32_bf16 v[104:107], a[136:139], v[48:51], v[104:107]\n" \
  "s_add_u32 s68, s68, 40960\n" \
  "v_mfma_f32_16x16x32_bf16 v[116:119], a[136:139], v[52:55], v[116:119]\n" \
  "v_mfma_f32_16x16x32_bf16 v[108:111], a[216:219], v[48:51], v[108:111]\n" \
  "v_mfma_f32_16x16x32_bf16 v[120:123], a[216:219], v[52:55], v[120:123]\n" \
  "s_waitcnt lgkmcnt(2)\n" \
  "v_mfma_f32_16x16x32_bf16 v[100:103], a[60:63], v[32:35], v[100:103]\n" \
  "ds_read_b128 v[48:51], v0 offset:17408\n" \
  "v_mfma_f32_16x16x32_bf16 v[112:115], a[60:63], v[36:39], v[112:115]\n" \
  "ds_read_b128 v[52:55], v0 offset:37888\n" \
  "v_mfma_f32_16x16x32_bf16 v[104:107], a[140:143], v[32:35], v[104:107]\n" \
  "v_mov_b32 v11, s52\n" \
  "v_mfma_f32_16x16x32_bf16 v[116:119], a[140:143], v[36:39], v[116:119]\n" \
  "s_waitcnt vmcnt(10)\n" \
  "v_mfma_f32_16x16x32_bf16 v[108:111], a[220:223], v[32:35], v[108:111]\n" \
  "buffer_store_dword v11, v5, s[40:43], 0 offen sc1\n" \
  "v_mfma_f32_16x16x32_bf16 v[120:123], a[220:223], v[36:39], v[120:123]\n" \
  "s_waitcnt lgkmcnt(2)\n" \
  "v_mfma_f32_16x16x32_bf16 v[100:103], a[64:67], v[40:43], v[100:103]\n" \
  "ds_read_b128 v[32:35], v0 offset:18432\n" \
  "v_mfma_f32_16x16x32_bf16 v[112:115], a[64:67], v[44:47], v[112:115]\n" \
  "ds_read_b128 v[36:39], v0 offset:38912\n" \
  "v_mfma_f32_16x16x32_bf16 v[104:107], a[144:147], v[40:43], v[104:107]\n" \
  "v_mfma_f32_16x16x32_bf16 v[116:119], a[144:147], v[44:47], v[116:119]\n" \
  "v_mfma_f32_16x16x32_bf16 v[108:111], a[224:227], v[40:43], v[108:111]\n" \
  "v_mfma_f32_16x16x32_bf16 v[120:123], a[224:227], v[44:47], v[120:123]\n" \
  "s_waitcnt lgkmcnt(2)\n" \
  "v_mfma_f32_16x16x32_bf16 v[100:103], a[68:71], v[48:51], v[100:103]\n" \
  "ds_read_b128 v[40:43], v0 offset:19456\n" \
  "v_mfma_f32_16x16x32_bf16 v[112:115], a[68:71], v[52:55], v[112:115]\n" \
  "ds_read_b128 v[44:47], v0 offset:39936\n" \
  "v_mfma_f32_16x16x32_bf16 v[104:107], a[148:151], v[48:51], v[104:107]\n" \
  "v_readlane_b32 s69, v4, s52\n" \
  "v_mfma_f32_16x16x32_bf16 v[116:119], a[148:151], v[52:55], v[116:119]\n" \
  "v_readlane_b32 s71, v8, s52\n" \
  "v_mfma_f32_16x16x32_bf16 v[108:111], a[228:231], v[48:51], v[108:111]\n" \
  "s_add_u32 s52, s52, 1\n" \
  "v_mfma_f32_16x16x32_bf16 v[120:123], a[228:231], v[52:55], v[120:123]\n" \
  "s_waitcnt lgkmcnt(0)\n" \
  "s_barrier\n" \
  "v_mfma_f32_16x16x32_bf16 v[100:103], a[72:75], v[32:35], v[100:103]\n" \
  "ds_read_b128 v[48:51], v9 offset:0\n" \
  "v_mfma_f32_16x16x32_bf16 v[112:115], a[72:75], v[36:39], v[112:115]\n" \
  "ds_read_b128 v[52:55], v9 offset:20480\n" \
  "v_mfma_f32_16x16x32_bf16 v[104:107], a[152:155], v[32:35], v[104:107]\n" \
  "v_mfma_f32_16x16x32_bf16 v[116:119], a[152:155], v[36:39], v[116:119]\n" \
  "v_mfma_f32_16x16x32_bf16 v[108:111], a[232:235], v[32:35], v[108:111]\n" \
  "v_mfma_f32_16x16x32_bf16 v[120:123], a[232:235], v[36:39], v[120:123]\n" \
  "v_mfma_f32_16x16x32_bf16 v[100:103], a[76:79], v[40:43], v[100:103]\n" \
  "ds_read_b128 v[32:35], v9 offset:1024\n" \
  "v_mfma_f32_16x16x32_bf16 v[112:115], a[76:79], v[44:47], v[112:115]\n" \
  "ds_read_b128 v[36:39], v9 offset:21504\n" \
  "v_mfma_f32_16x16x32_bf16 v[104:107], a[156:159], v[40:43], v[104:107]\n" \
  "s_cmp_eq_u32 s52, s53\n" \
  "v_mfma_f32_16x16x32_bf16 v[116:119], a[156:159], v[44:47], v[116:119]\n" \
  "v_mfma_f32_16x16x32_bf16 v[108:111], a[236:239], v[40:43], v[108:111]\n" \
  "v_mfma_f32_16x16x32_bf16 v[120:123], a[236:239], v[44:47], v[120:123]\n" \
  "s_cbranch_scc1 LPE_%=\n" \
  "s_waitcnt lgkmcnt(2)\n" \
  "v_mfma_f32_16x16x32_bf16 v[56:59], a[0:3], v[48:51], 0\n" \
  "ds_read_b128 v[40:43], v9 offset:2048\n" \
  "v_mfma_f32_16x16x32_bf16 v[68:71], a[0:3], v[52:55], 0\n" \
  "ds_read_b128 v[44:47], v9 offset:22528\n" \
  "v_mfma_f32_16x16x32_bf16 v[60:63], a[80:83], v[48:51], 0\n" \
  "buffer_store_dwordx4 v[12:15], v7, s[40:43], s71 offen sc1\n" \
  "v_mfma_f32_16x16x32_bf16 v[72:75], a[80:83], v[52:55], 0\n" \
  "v_cvt_pk_bf16_f32 v16, v100, v101\n" \
  "v_mfma_f32_16x16x32_bf16 v[64:67], a[160:163], v[48:51], 0\n" \
  "v_cvt_pk_bf16_f32 v17, v102, v103\n" \
  "v_mfma_f32_16x16x32_bf16 v[76:79], a[160:163], v[52:55], 0\n" \
  "v_cvt_pk_bf16_f32 v18, v104, v105\n" \
  "s_waitcnt lgkmcnt(2)\n" \
  "v_mfma_f32_16x16x32_bf16 v[56:59], a[4:7], v[32:35], v[56:59]\n" \
  "ds_read_b128 v[48:51], v9 offset:3072\n" \
  "v_mfma_f32_16x16x32_bf16 v[68:71], a[4:7], v[36:39], v[68:71]\n" \
  "ds_read_b128 v[52:55], v9 offset:23552\n" \
  "v_mfma_f32_16x16x32_bf16 v[60:63], a[84:87], v[32:35], v[60:63]\n" \
  "v_cvt_pk_bf16_f32 v19, v106, v107\n" \
  "v_mfma_f32_16x16x32_bf16 v[72:75], a[84:87], v[36:39], v[72:75]\n" \
  "buffer_store_dwordx4 v[16:19], v2, s[40:43], s69 offen sc1\n" \
  "v_mfma_f32_16x16x32_bf16 v[64:67], a[164:167], v[32:35], v[64:67]\n" \
  "v_cvt_pk_bf16_f32 v20, v112, v113\n" \
  "v_mfma_f32_16x16x32_bf16 v[76:79], a[164:167], v[36:39], v[76:79]\n" \
  "v_cvt_pk_bf16_f32 v21, v114, v115\n" \
  "s_waitcnt lgkmcnt(2)\n" \
  "v_mfma_f32_16x16x32_bf16 v[56:59], a[8:11], v[40:43], v[56:59]\n" \
  "ds_read_b128 v[32:35], v9 offset:4096\n" \
  "v_mfma_f32_16x16x32_bf16 v[68:71], a[8:11], v[44:47], v[68:71]\n" \
  "ds_read_b128 v[36:39], v9 offset:24576\n" \
  "v_mfma_f32_16x16x32_bf16 v[60:63], a[88:91], v[40:43], v[60:63]\n" \
  "v_cvt_pk_bf16_f32 v22, v116, v117\n" \
  "v_mfma_f32_16x16x32_bf16 v[72:75], a[88:91], v[44:47], v[72:75]\n" \
  "v_cvt_pk_bf16_f32 v23, v118, v119\n" \
  "v_mfma_f32_16x16x32_bf16 v[64:67], a[168:171], v[40:43], v[64:67]\n" \
  "buffer_store_dwordx4 v[20:23], v2, s[40:43], s69 offen offset:1024 sc1\n" \
  "v_mfma_f32_16x16x32_bf16 v[76:79], a[168:171], v[44:47], v[76:79]\n" \
  "v_cvt_pk_bf16_f32 v24, v108, v109\n" \
  "s_waitcnt lgkmcnt(2)\n" \
  "v_mfma_f32_16x16x32_bf16 v[56:59], a[12:15], v[48:51], v[56:59]\n" \
  "ds_read_b128 v[40:43], v9 offset:5120\n" \
  "v_mfma_f32_16x16x32_bf16 v[68:71], a[12:15], v[52:55], v[68:71]\n" \
  "ds_read_b128 v[44:47], v9 offset:25600\n" \
  "v_mfma_f32_16x16x32_bf16 v[60:63], a[92:95], v[48:51], v[60:63]\n" \
  "v_cvt_pk_bf16_f32 v25, v110, v111\n" \
  "v_mfma_f32_16x16x32_bf16 v[72:75], a[92:95], v[52:55], v[72:75]\n" \
  "v_cvt_pk_bf16_f32 v26, v120, v121\n" \
  "v_mfma_f32_16x16x32_bf16 v[64:67], a[172:175], v[48:51], v[64:67]\n" \
  "v_cvt_pk_bf16_f32 v27, v122, v123\n" \
  "v_mfma_f32_16x16x32_bf16 v[76:79], a[172:175], v[52:55], v[76:79]\n" \
  "buffer_store_dwordx4 v[24:27], v3, s[40:43], s69 offen sc1\n" \
  "s_waitcnt lgkmcnt(2)\n" \
  "v_mfma_f32_16x16x32_bf16 v[56:59], a[16:19], v[32:35], v[56:59]\n" \
  "ds_read_b128 v[48:51], v9 offset:6144\n" \
  "v_mfma_f32_16x16x32_bf16 v[68:71], a[16:19], v[36:39], v[68:71]\n" \
  "ds_read_b128 v[52:55], v9 offset:26624\n" \
  "v_mfma_f32_16x16x32_bf16 v[60:63], a[96:99], v[32:35], v[60:63]\n" \
  "s_add_u32 s72, s68, 4096\n" \
  "v_mfma_f32_16x16x32_bf16 v[72:75], a[96:99], v[36:39], v[72:75]\n" \
  "s_add_u32 s73, s68, 8192\n" \
  "v_mfma_f32_16x16x32_bf16 v[64:67], a[176:179], v[32:35], v[64:67]\n" \
  "s_add_u32 m0, s54, 0\n" \
  "v_mfma_f32_16x16x32_bf16 v[76:79], a[176:179], v[36:39], v[76:79]\n" \
  "buffer_load_dwordx4 v1, s[36:39], s68 offen offset:0 sc1 lds\n" \
  "s_waitcnt lgkmcnt(2)\n" \
  "v_mfma_f32_16x16x32_bf16 v[56:59], a[20:23], v[40:43], v[56:59]\n" \
  "ds_read_b128 v[32:35], v9 offset:7168\n" \
  "v_mfma_f32_16x16x32_bf16 v[68:71], a[20:23], v[44:47], v[68:71]\n" \
  "ds_read_b128 v[36:39], v9 offset:27648\n" \
  "v_mfma_f32_16x16x32_bf16 v[60:63], a[100:103], v[40:43], v[60:63]\n" \
  "buffer_load_dwordx4 v1, s[36:39], s68 offen offset:1024 sc1 lds\n" \
  "v_mfma_f32_16x16x32_bf16 v[72:75], a[100:103], v[44:47], v[72:75]\n" \
  "v_mfma_f32_16x16x32_bf16 v[64:67], a[180:183], v[40:43], v[64:67]\n" \
  "v_mfma_f32_16x16x32_bf16 v[76:79], a[180:183], v[44:47], v[76:79]\n" \
  "s_waitcnt lgkmcnt(2)\n" \
  "v_mfma_f32_16x16x32_bf16 v[56:59], a[24:27], v[48:51], v[56:59]\n" \
  "ds_read_b128 v[40:43], v9 offset:8192\n" \
  "v_mfma_f32_16x16x32_bf16 v[68:71], a[24:27], v[52:55], v[68:71]\n" \
  "ds_read_b128 v[44:47], v9 offset:28672\n" \
  "v_mfma_f32_16x16x32_bf16 v[60:63], a[104:107], v[48:51], v[60:63]\n" \
  "buffer_load_dwordx4 v1, s[36:39], s68 offen offset:2048 sc1 lds\n" \
  "v_mfma_f32_16x16x32_bf16 v[72:75], a[104:107], v[52:55], v[72:75]\n" \
  "v_mfma_f32_16x16x32_bf16 v[64:67], a[184:187], v[48:51], v[64:67]\n" \
  "v_mfma_f32_16x16x32_bf16 v[76:79], a[184:187], v[52:55], v[76:79]\n" \
  "s_waitcnt lgkmcnt(2)\n" \
  "v_mfma_f32_16x16x32_bf16 v[56:59], a[28:31], v[32:35], v[56:59]\n" \
  "ds_read_b128 v[48:51], v9 offset:9216\n" \
  "v_mfma_f32_16x16x32_bf16 v[68:71], a[28:31], v[36:39], v[68:71]\n" \
  "ds_read_b128 v[52:55], v9 offset:29696\n" \
  "v_mfma_f32_16x16x32_bf16 v[60:63], a[108:111], v[32:35], v[60:63]\n" \
  "buffer_load_dwordx4 v1, s[36:39], s68 offen offset:3072 sc1 lds\n" \
  "v_mfma_f32_16x16x32_bf16 v[72:75], a[108:111], v[36:39], v[72:75]\n" \
  "v_mfma_f32_16x16x32_bf16 v[64:67], a[188:191], v[32:35], v[64:67]\n" \
  "v_mfma_f32_16x16x32_bf16 v[76:79], a[188:191], v[36:39], v[76:79]\n" \
  "s_waitcnt lgkmcnt(2)\n" \
  "v_mfma_f32_16x16x32_bf16 v[56:59], a[32:35], v[40:43], v[56:59]\n" \
  "ds_read_b128 v[32:35], v9 offset:10240\n" \
  "v_mfma_f32_16x16x32_bf16 v[68:71], a[32:35], v[44:47], v[68:71]\n" \
  "ds_read_b128 v[36:39], v9 offset:30720\n" \
  "v_mfma_f32_16x16x32_bf16 v[60:63], a[112:115], v[40:43], v[60:63]\n" \
  "s_add_u32 m0, s54, 4096\n" \
  "v_mfma_f32_16x16x32_bf16 v[72:75], a[112:115], v[44:47], v[72:75]\n" \
  "buffer_load_dwordx4 v1, s[36:39], s72 offen offset:0 sc1 lds\n" \
  "v_mfma_f32_16x16x32_bf16 v[64:67], a[192:195], v[40:43], v[64:67]\n" \
  "v_mfma_f32_16x16x32_bf16 v[76:79], a[192:195], v[44:47], v[76:79]\n" \
  "s_waitcnt lgkmcnt(2)\n" \
  "v_mfma_f32_16x16x32_bf16 v[56:59], a[36:39], v[48:51], v[56:59]\n" \
  "ds_read_b128 v[40:43], v9 offset:11264\n" \
  "v_mfma_f32_16x16x32_bf16 v[68:71], a[36:39], v[52:55], v[68:71]\n" \
  "ds_read_b128 v[44:47], v9 offset:31744\n" \
  "v_mfma_f32_16x16x32_bf16 v[60:63], a[116:119], v[48:51], v[60:63]\n" \
  "buffer_load_dwordx4 v1, s[36:39], s72 offen offset:1024 sc1 lds\n" \
  "v_mfma_f32_16x16x32_bf16 v[72:75], a[116:119], v[52:55], v[72:75]\n" \
  "v_mfma_f32_16x16x32_bf16 v[64:67], a[196:199], v[48:51], v[64:67]\n" \
  "v_mfma_f32_16x16x32_bf16 v[76:79], a[196:199], v[52:55], v[76:79]\n" \
  "s_waitcnt lgkmcnt(2)\n" \
  "v_mfma_f32_16x16x32_bf16 v[56:59], a[40:43], v[32:35], v[56:59]\n" \
  "ds_read_b128 v[48:51], v9 offset:12288\n" \
  "v_mfma_f32_16x16x32_bf16 v[68:71], a[40:43], v[36:39], v[68:71]\n" \
  "ds_read_b128 v[52:55], v9 offset:32768\n" \
  "v_mfma_f32_16x16x32_bf16 v[60:63], a[120:123], v[32:35], v[60:63]\n" \
  "buffer_load_dwordx4 v1, s[36:39], s72 offen offset:2048 sc1 lds\n" \
  "v_mfma_f32_16x16x32_bf16 v[72:75], a[120:123], v[36:39], v[72:75]\n" \
  "v_mfma_f32_16x16x32_bf16 v[64:67], a[200:203], v[32:35], v[64:67]\n" \
  "v_mfma_f32_16x16x32_bf16 v[76:79], a[200:203], v[36:39], v[76:79]\n" \
  "s_waitcnt lgkmcnt(2)\n" \
  "v_mfma_f32_16x16x32_bf16 v[56:59], a[44:47], v[40:43], v[56:59]\n" \
  "ds_read_b128 v[32:35], v9 offset:13312\n" \
  "v_mfma_f32_16x16x32_bf16 v[68:71], a[44:47], v[44:47], v[68:71]\n" \
  "ds_read_b128 v[36:39], v9 offset:33792\n" \
  "v_mfma_f32_16x16x32_bf16 v[60:63], a[124:127], v[40:43], v[60:63]\n" \
  "buffer_load_dwordx4 v1, s[36:39], s72 offen offset:3072 sc1 lds\n" \
  "v_mfma_f32_16x16x32_bf16 v[72:75], a[124:127], v[44:47], v[72:75]\n" \
  "v_mfma_f32_16x16x32_bf16 v[64:67], a[204:207], v[40:43], v[64:67]\n" \
  "v_mfma_f32_16x16x32_bf16 v[76:79], a[204:207], v[44:47], v[76:79]\n" \
  "s_waitcnt lgkmcnt(2)\n" \
  "v_mfma_f32_16x16x32_bf16 v[56:59], a[48:51], v[48:51], v[56:59]\n" \
  "ds_read_b128 v[40:43], v9 offset:14336\n" \
  "v_mfma_f32_16x16x32_bf16 v[68:71], a[48:51], v[52:55], v[68:71]\n" \
  "ds_read_b128 v[44:47], v9 offset:34816\n" \
  "v_mfma_f32_16x16x32_bf16 v[60:63], a[128:131], v[48:51], v[60:63]\n" \
  "s_add_u32 m0, s54, 8192\n" \
  "v_mfma_f32_16x16x32_bf16 v[72:75], a[128:131], v[52:55], v[72:75]\n" \
  "buffer_load_dwordx4 v1, s[36:39], s73 offen offset:0 sc1 lds\n" \
  "v_mfma_f32_16x16x32_bf16 v[64:67], a[208:211], v[48:51], v[64:67]\n" \
  "v_mfma_f32_16x16x32_bf16 v[76:79], a[208:211], v[52:55], v[76:79]\n" \
  "s_waitcnt lgkmcnt(2)\n" \
  "v_mfma_f32_16x16x32_bf16 v[56:59], a[52:55], v[32:35], v[56:59]\n" \
  "ds_read_b128 v[48:51], v9 offset:15360\n" \
  "v_mfma_f32_16x16x32_bf16 v[68:71], a[52:55], v[36:39], v[68:71]\n" \
  "ds_read_b128 v[52:55], v9 offset:35840\n" \
  "v_mfma_f32_16x16x32_bf16 v[60:63], a[132:135], v[32:35], v[60:63]\n" \
  "buffer_load_dwordx4 v1, s[36:39], s73 offen offset:1024 sc1 lds\n" \
  "v_mfma_f32_16x16x32_bf16 v[72:75], a[132:135], v[36:39], v[72:75]\n" \
  "v_mfma_f32_16x16x32_bf16 v[64:67], a[212:215], v[32:35], v[64:67]\n" \
  "v_mfma_f32_16x16x32_bf16 v[76:79], a[212:215], v[36:39], v[76:79]\n" \
  "s_waitcnt lgkmcnt(2)\n" \
  "v_mfma_f32_16x16x32_bf16 v[56:59], a[56:59], v[40:43], v[56:59]\n" \
  "ds_read_b128 v[32:35], v9 offset:16384\n" \
  "v_mfma_f32_16x16x32_bf16 v[68:71], a[56:59], v[44:47], v[68:71]\n" \
  "ds_read_b128 v[36:39], v9 offset:36864\n" \
  "v_mfma_f32_16x16x32_bf16 v[60:63], a[136:139], v[40:43], v[60:63]\n" \
  "s_add_u32 s68, s68, 40960\n" \
  "v_mfma_f32_16x16x32_bf16 v[72:75], a[136:139], v[44:47], v[72:75]\n" \
  "v_mfma_f32_16x16x32_bf16 v[64:67], a[216:219], v[40:43], v[64:67]\n" \
  "v_mfma_f32_16x16x32_bf16 v[76:79], a[216:219], v[44:47], v[76:79]\n" \
  "s_waitcnt lgkmcnt(2)\n" \
  "v_mfma_f32_16x16x32_bf16 v[56:59], a[60:63], v[48:51], v[56:59]\n" \
  "ds_read_b128 v[40:43], v9 offset:17408\n" \
  "v_mfma_f32_16x16x32_bf16 v[68:71], a[60:63], v[52:55], v[68:71]\n" \
  "ds_read_b128 v[44:47], v9 offset:37888\n" \
  "v_mfma_f32_16x16x32_bf16 v[60:63], a[140:143], v[48:51], v[60:63]\n" \
  "v_mov_b32 v11, s52\n" \
  "v_mfma_f32_16x16x32_bf16 v[72:75], a[140:143], v[52:55], v[72:75]\n" \
  "s_waitcnt vmcnt(10)\n" \
  "v_mfma_f32_16x16x32_bf16 v[64:67], a[220:223], v[48:51], v[64:67]\n" \
  "buffer_store_dword v11, v5, s[40:43], 0 offen sc1\n" \
  "v_mfma_f32_16x16x32_bf16 v[76:79], a[220:223], v[52:55], v[76:79]\n" \
  "s_waitcnt lgkmcnt(2)\n" \
  "v_mfma_f32_16x16x32_bf16 v[56:59], a[64:67], v[32:35], v[56:59]\n" \
  "ds_read_b128 v[48:51], v9 offset:18432\n" \
  "v_mfma_f32_16x16x32_bf16 v[68:71], a[64:67], v[36:39], v[68:71]\n" \
  "ds_read_b128 v[52:55], v9 offset:38912\n" \
  "v_mfma_f32_16x16x32_bf16 v[60:63], a[144:147], v[32:35], v[60:63]\n" \
  "v_mfma_f32_16x16x32_bf16 v[72:75], a[144:147], v[36:39], v[72:75]\n" \
  "v_mfma_f32_16x16x32_bf16 v[64:67], a[224:227], v[32:35], v[64:67]\n" \
  "v_mfma_f32_16x16x32_bf16 v[76:79], a[224:227], v[36:39], v[76:79]\n" \
  "s_waitcnt lgkmcnt(2)\n" \
  "v_mfma_f32_16x16x32_bf16 v[56:59], a[68:71], v[40:43], v[56:59]\n" \
  "ds_read_b128 v[32:35], v9 offset:19456\n" \
  "v_mfma_f32_16x16x32_bf16 v[68:71], a[68:71], v[44:47], v[68:71]\n" \
  "ds_read_b128 v[36:39], v9 offset:39936\n" \
  "v_mfma_f32_16x16x32_bf16 v[60:63], a[148:151], v[40:43], v[60:63]\n" \
  "v_readlane_b32 s69, v4, s52\n" \
  "v_mfma_f32_16x16x32_bf16 v[72:75], a[148:151], v[44:47], v[72:75]\n" \
  "v_readlane_b32 s71, v8, s52\n" \
  "v_mfma_f32_16x16x32_bf16 v[64:67], a[228:231], v[40:43], v[64:67]\n" \
  "s_add_u32 s52, s52, 1\n" \
  "v_mfma_f32_16x16x32_bf16 v[76:79], a[228:231], v[44:47], v[76:79]\n" \
  "s_waitcnt lgkmcnt(0)\n" \
  "s_barrier\n" \
  "v_mfma_f32_16x16x32_bf16 v[56:59], a[72:75], v[48:51], v[56:59]\n" \
  "ds_read_b128 v[40:43], v10 offset:0\n" \
  "v_mfma_f32_16x16x32_bf16 v[68:71], a[72:75], v[52:55], v[68:71]\n" \
  "ds_read_b128 v[44:47], v10 offset:20480\n" \
  "v_mfma_f32_16x16x32_bf16 v[60:63], a[152:155], v[48:51], v[60:63]\n" \
  "v_mfma_f32_16x16x32_bf16 v[72:75], a[152:155], v[52:55], v[72:75]\n" \
  "v_mfma_f32_16x16x32_bf16 v[64:67], a[232:235], v[48:51], v[64:67]\n" \
  "v_mfma_f32_16x16x32_bf16 v[76:79], a[232:235], v[52:55], v[76:79]\n" \
  "v_mfma_f32_16x16x32_bf16 v[56:59], a[76:79], v[32:35], v[56:59]\n" \
  "ds_read_b128 v[48:51], v10 offset:1024\n" \
  "v_mfma_f32_16x16x32_bf16 v[68:71], a[76:79], v[36:39], v[68:71]\n" \
  "ds_read_b128 v[52:55], v10 offset:21504\n" \
  "v_mfma_f32_16x16x32_bf16 v[60:63], a[156:159], v[32:35], v[60:63]\n" \
  "s_cmp_eq_u32 s52, s53\n" \
  "v_mfma_f32_16x16x32_bf16 v[72:75], a[156:159], v[36:39], v[72:75]\n" \
  "v_mfma_f32_16x16x32_bf16 v[64:67], a[236:239], v[32:35], v[64:67]\n" \
  "v_mfma_f32_16x16x32_bf16 v[76:79], a[236:239], v[36:39], v[76:79]\n" \
  "s_waitcnt lgkmcnt(2)\n" \
  "v_mfma_f32_16x16x32_bf16 v[100:103], a[0:3], v[40:43], 0\n" \
  "ds_read_b128 v[32:35], v10 offset:2048\n" \
  "v_mfma_f32_16x16x32_bf16 v[112:115], a[0:3], v[44:47], 0\n" \
  "ds_read_b128 v[36:39], v10 offset:22528\n" \
  "v_mfma_f32_16x16x32_bf16 v[104:107], a[80:83], v[40:43], 0\n" \
  "buffer_store_dwordx4 v[12:15], v7, s[40:43], s71 offen sc1\n" \
  "v_mfma_f32_16x16x32_bf16 v[116:119], a[80:83], v[44:47], 0\n" \
  "v_cvt_pk_bf16_f32 v16, v56, v57\n" \
  "v_mfma_f32_16x16x32_bf16 v[108:111], a[160:163], v[40:43], 0\n" \
  "v_cvt_pk_bf16_f32 v17, v58, v59\n" \
  "v_mfma_f32_16x16x32_bf16 v[120:123], a[160:163], v[44:47], 0\n" \
  "v_cvt_pk_bf16_f32 v18, v60, v61\n" \
  "s_waitcnt lgkmcnt(2)\n" \
  "v_mfma_f32_16x16x32_bf16 v[100:103], a[4:7], v[48:51], v[100:103]\n" \
  "ds_read_b128 v[40:43], v10 offset:3072\n" \
  "v_mfma_f32_16x16x32_bf16 v[112:115], a[4:7], v[52:55], v[112:115]\n" \
  "ds_read_b128 v[44:47], v10 offset:23552\n" \
  "v_mfma_f32_16x16x32_bf16 v[104:107], a[84:87], v[48:51], v[104:107]\n" \
  "v_cvt_pk_bf16_f32 v19, v62, v63\n" \
  "v_mfma_f32_16x16x32_bf16 v[116:119], a[84:87], v[52:55], v[116:119]\n" \
  "buffer_store_dwordx4 v[16:19], v2, s[40:43], s69 offen sc1\n" \
  "v_mfma_f32_16x16x32_bf16 v[108:111], a[164:167], v[48:51], v[108:111]\n" \
  "v_cvt_pk_bf16_f32 v20, v68, v69\n" \
  "v_mfma_f32_16x16x32_bf16 v[120:123], a[164:167], v[52:55], v[120:123]\n" \
  "v_cvt_pk_bf16_f32 v21, v70, v71\n" \
  "s_waitcnt lgkmcnt(2)\n" \
  "v_mfma_f32_16x16x32_bf16 v[100:103], a[8:11], v[32:35], v[100:103]\n" \
  "ds_read_b128 v[48:51], v10 offset:4096\n" \
  "v_mfma_f32_16x16x32_bf16 v[112:115], a[8:11], v[36:39], v[112:115]\n" \
  "ds_read_b128 v[52:55], v10 offset:24576\n" \
  "v_mfma_f32_16x16x32_bf16 v[104:107], a[88:91], v[32:35], v[104:107]\n" \
  "v_cvt_pk_bf16_f32 v22, v72, v73\n" \
  "v_mfma_f32_16x16x32_bf16 v[116:119], a[88:91], v[36:39], v[116:119]\n" \
  "v_cvt_pk_bf16_f32 v23, v74, v75\n" \
  "v_mfma_f32_16x16x32_bf16 v[108:111], a[168:171], v[32:35], v[108:111]\n" \
  "buffer_store_dwordx4 v[20:23], v2, s[40:43], s69 offen offset:1024 sc1\n" \
  "v_mfma_f32_16x16x32_bf16 v[120:123], a[168:171], v[36:39], v[120:123]\n" \
  "v_cvt_pk_bf16_f32 v24, v64, v65\n" \
  "s_waitcnt lgkmcnt(2)\n" \
  "v_mfma_f32_16x16x32_bf16 v[100:103], a[12:15], v[40:43], v[100:103]\n" \
  "ds_read_b128 v[32:35], v10 offset:5120\n" \
  "v_mfma_f32_16x16x32_bf16 v[112:115], a[12:15], v[44:47], v[112:115]\n" \
  "ds_read_b128 v[36:39], v10 offset:25600\n" \
  "v_mfma_f32_16x16x32_bf16 v[104:107], a[92:95], v[40:43], v[104:107]\n" \
  "v_cvt_pk_bf16_f32 v25, v66, v67\n" \
  "v_mfma_f32_16x16x32_bf16 v[116:119], a[92:95], v[44:47], v[116:119]\n" \
  "v_cvt_pk_bf16_f32 v26, v76, v77\n" \
  "v_mfma_f32_16x16x32_bf16 v[108:111], a[172:175], v[40:43], v[108:111]\n" \
  "v_cvt_pk_bf16_f32 v27, v78, v79\n" \
  "v_mfma_f32_16x16x32_bf16 v[120:123], a[172:175], v[44:47], v[120:123]\n" \
  "buffer_store_dwordx4 v[24:27], v3, s[40:43], s69 offen sc1\n" \
  "s_waitcnt lgkmcnt(2)\n" \
  "v_mfma_f32_16x16x32_bf16 v[100:103], a[16:19], v[48:51], v[100:103]\n" \
  "ds_read_b128 v[40:43], v10 offset:6144\n" \
  "v_mfma_f32_16x16x32_bf16 v[112:115], a[16:19], v[52:55], v[112:115]\n" \
  "ds_read_b128 v[44:47], v10 offset:26624\n" \
  "v_mfma_f32_16x16x32_bf16 v[104:107], a[96:99], v[48:51], v[104:107]\n" \
  "s_add_u32 s72, s68, 4096\n" \
  "v_mfma_f32_16x16x32_bf16 v[116:119], a[96:99], v[52:55], v[116:119]\n" \
  "s_add_u32 s73, s68, 8192\n" \
  "v_mfma_f32_16x16x32_bf16 v[108:111], a[176:179], v[48:51], v[108:111]\n" \
  "s_add_u32 m0, s54, 40960\n" \
  "v_mfma_f32_16x16x32_bf16 v[120:123], a[176:179], v[52:55], v[120:123]\n" \
  "buffer_load_dwordx4 v1, s[36:39], s68 offen offset:0 sc1 lds\n" \
  "s_waitcnt lgkmcnt(2)\n" \
  "v_mfma_f32_16x16x32_bf16 v[100:103], a[20:23], v[32:35], v[100:103]\n" \
  "ds_read_b128 v[48:51], v10 offset:7168\n" \
  "v_mfma_f32_16x16x32_bf16 v[112:115], a[20:23], v[36:39], v[112:115]\n" \
  "ds_read_b128 v[52:55], v10 offset:27648\n" \
  "v_mfma_f32_16x16x32_bf16 v[104:107], a[100:103], v[32:35], v[104:107]\n" \
  "buffer_load_dwordx4 v1, s[36:39], s68 offen offset:1024 sc1 lds\n" \
  "v_mfma_f32_16x16x32_bf16 v[116:119], a[100:103], v[36:39], v[116:119]\n" \
  "v_mfma_f32_16x16x32_bf16 v[108:111], a[180:183], v[32:35], v[108:111]\n" \
  "v_mfma_f32_16x16x32_bf16 v[120:123], a[180:183], v[36:39], v[120:123]\n" \
  "s_waitcnt lgkmcnt(2)\n" \
  "v_mfma_f32_16x16x32_bf16 v[100:103], a[24:27], v[40:43], v[100:103]\n" \
  "ds_read_b128 v[32:35], v10 offset:8192\n" \
  "v_mfma_f32_16x16x32_bf16 v[112:115], a[24:27], v[44:47], v[112:115]\n" \
  "ds_read_b128 v[36:39], v10 offset:28672\n" \
  "v_mfma_f32_16x16x32_bf16 v[104:107], a[104:107], v[40:43], v[104:107]\n" \
  "buffer_load_dwordx4 v1, s[36:39], s68 offen offset:2048 sc1 lds\n" \
  "v_mfma_f32_16x16x32_bf16 v[116:119], a[104:107], v[44:47], v[116:119]\n" \
  "v_mfma_f32_16x16x32_bf16 v[108:111], a[184:187], v[40:43], v[108:111]\n" \
  "v_mfma_f32_16x16x32_bf16 v[120:123], a[184:187], v[44:47], v[120:123]\n" \
  "s_waitcnt lgkmcnt(2)\n" \
  "v_mfma_f32_16x16x32_bf16 v[100:103], a[28:31], v[48:51], v[100:103]\n" \
  "ds_read_b128 v[40:43], v10 offset:9216\n" \
  "v_mfma_f32_16x16x32_bf16 v[112:115], a[28:31], v[52:55], v[112:115]\n" \
  "ds_read_b128 v[44:47], v10 offset:29696\n" \
  "v_mfma_f32_16x16x32_bf16 v[104:107], a[108:111], v[48:51], v[104:107]\n" \
  "buffer_load_dwordx4 v1, s[36:39], s68 offen offset:3072 sc1 lds\n" \
  "v_mfma_f32_16x16x32_bf16 v[116:119], a[108:111], v[52:55], v[116:119]\n" \
  "v_mfma_f32_16x16x32_bf16 v[108:111], a[188:191], v[48:51], v[108:111]\n" \
  "v_mfma_f32_16x16x32_bf16 v[120:123], a[188:191], v[52:55], v[120:123]\n" \
  "s_waitcnt lgkmcnt(2)\n" \
  "v_mfma_f32_16x16x32_bf16 v[100:103], a[32:35], v[32:35], v[100:103]\n" \
  "ds_read_b128 v[48:51], v10 offset:10240\n" \
  "v_mfma_f32_16x16x32_bf16 v[112:115], a[32:35], v[36:39], v[112:115]\n" \
  "ds_read_b128 v[52:55], v10 offset:30720\n" \
  "v_mfma_f32_16x16x32_bf16 v[104:107], a[112:115], v[32:35], v[104:107]\n" \
  "s_add_u32 m0, s54, 45056\n" \
  "v_mfma_f32_16x16x32_bf16 v[116:119], a[112:115], v[36:39], v[116:119]\n" \
  "buffer_load_dwordx4 v1, s[36:39], s72 offen offset:0 sc1 lds\n" \
  "v_mfma_f32_16x16x32_bf16 v[108:111], a[192:195], v[32:35], v[108:111]\n" \
  "v_mfma_f32_16x16x32_bf16 v[120:123], a[192:195], v[36:39], v[120:123]\n" \
  "s_waitcnt lgkmcnt(2)\n" \
  "v_mfma_f32_16x16x32_bf16 v[100:103], a[36:39], v[40:43], v[100:103]\n" \
  "ds_read_b128 v[32:35], v10 offset:11264\n" \
  "v_mfma_f32_16x16x32_bf16 v[112:115], a[36:39], v[44:47], v[112:115]\n" \
  "ds_read_b128 v[36:39], v10 offset:31744\n" \
  "v_mfma_f32_16x16x32_bf16 v[104:107], a[116:119], v[40:43], v[104:107]\n" \
  "buffer_load_dwordx4 v1, s[36:39], s72 offen offset:1024 sc1 lds\n" \
  "v_mfma_f32_16x16x32_bf16 v[116:119], a[116:119], v[44:47], v[116:119]\n" \
  "v_mfma_f32_16x16x32_bf16 v[108:111], a[196:199], v[40:43], v[108:111]\n" \
  "v_mfma_f32_16x16x32_bf16 v[120:123], a[196:199], v[44:47], v[120:123]\n" \
  "s_waitcnt lgkmcnt(2)\n" \
  "v_mfma_f32_16x16x32_bf16 v[100:103], a[40:43], v[48:51], v[100:103]\n" \
  "ds_read_b128 v[40:43], v10 offset:12288\n" \
  "v_mfma_f32_16x16x32_bf16 v[112:115], a[40:43], v[52:55], v[112:115]\n" \
  "ds_read_b128 v[44:47], v10 offset:32768\n" \
  "v_mfma_f32_16x16x32_bf16 v[104:107], a[120:123], v[48:51], v[104:107]\n" \
  "buffer_load_dwordx4 v1, s[36:39], s72 offen offset:2048 sc1 lds\n" \
  "v_mfma_f32_16x16x32_bf16 v[116:119], a[120:123], v[52:55], v[116:119]\n" \
  "v_mfma_f32_16x16x32_bf16 v[108:111], a[200:203], v[48:51], v[108:111]\n" \
  "v_mfma_f32_16x16x32_bf16 v[120:123], a[200:203], v[52:55], v[120:123]\n" \
  "s_waitcnt lgkmcnt(2)\n" \
  "v_mfma_f32_16x16x32_bf16 v[100:103], a[44:47], v[32:35], v[100:103]\n" \
  "ds_read_b128 v[48:51], v10 offset:13312\n" \
  "v_mfma_f32_16x16x32_bf16 v[112:115], a[44:47], v[36:39], v[112:115]\n" \
  "ds_read_b128 v[52:55], v10 offset:33792\n" \
  "v_mfma_f32_16x16x32_bf16 v[104:107], a[124:127], v[32:35], v[104:107]\n" \
  "buffer_load_dwordx4 v1, s[36:39], s72 offen offset:3072 sc1 lds\n" \
  "v_mfma_f32_16x16x32_bf16 v[116:119], a[124:127], v[36:39], v[116:119]\n" \
  "v_mfma_f32_16x16x32_bf16 v[108:111], a[204:207], v[32:35], v[108:111]\n" \
  "v_mfma_f32_16x16x32_bf16 v[120:123], a[204:207], v[36:39], v[120:123]\n" \
  "s_waitcnt lgkmcnt(2)\n" \
  "v_mfma_f32_16x16x32_bf16 v[100:103], a[48:51], v[40:43], v[100:103]\n" \
  "ds_read_b128 v[32:35], v10 offset:14336\n" \
  "v_mfma_f32_16x16x32_bf16 v[112:115], a[48:51], v[44:47], v[112:115]\n" \
  "ds_read_b128 v[36:39], v10 offset:34816\n" \
  "v_mfma_f32_16x16x32_bf16 v[104:107], a[128:131], v[40:43], v[104:107]\n" \
  "s_add_u32 m0, s54, 49152\n" \
  "v_mfma_f32_16x16x32_bf16 v[116:119], a[128:131], v[44:47], v[116:119]\n" \
  "buffer_load_dwordx4 v1, s[36:39], s73 offen offset:0 sc1 lds\n" \
  "v_mfma_f32_16x16x32_bf16 v[108:111], a[208:211], v[40:43], v[108:111]\n" \
  "v_mfma_f32_16x16x32_bf16 v[120:123], a[208:211], v[44:47], v[120:123]\n" \
  "s_waitcnt lgkmcnt(2)\n" \
  "v_mfma_f32_16x16x32_bf16 v[100:103], a[52:55], v[48:51], v[100:103]\n" \
  "ds_read_b128 v[40:43], v10 offset:15360\n" \
  "v_mfma_f32_16x16x32_bf16 v[112:115], a[52:55], v[52:55], v[112:115]\n" \
  "ds_read_b128 v[44:47], v10 offset:35840\n" \
  "v_mfma_f32_16x16x32_bf16 v[104:107], a[132:135], v[48:51], v[104:107]\n" \
  "buffer_load_dwordx4 v1, s[36:39], s73 offen offset:1024 sc1 lds\n" \
  "v_mfma_f32_16x16x32_bf16 v[116:119], a[132:135], v[52:55], v[116:119]\n" \
  "v_mfma_f32_16x16x32_bf16 v[108:111], a[212:215], v[48:51], v[108:111]\n" \
  "v_mfma_f32_16x16x32_bf16 v[120:123], a[212:215], v[52:55], v[120:123]\n" \
  "s_waitcnt lgkmcnt(2)\n" \
  "v_mfma_f32_16x16x32_bf16 v[100:103], a[56:59], v[32:35], v[100:103]\n" \
  "ds_read_b128 v[48:51], v10 offset:16384\n" \
  "v_mfma_f32_16x16x32_bf16 v[112:115], a[56:59], v[36:39], v[112:115]\n" \
  "ds_read_b128 v[52:55], v10 offset:36864\n" \
  "v_mfma_f32_16x16x32_bf16 v[104:107], a[136:139], v[32:35], v[104:107]\n" \
  "s_add_u32 s68, s68, 40960\n" \
  "v_mfma_f32_16x16x32_bf16 v[116:119], a[136:139], v[36:39], v[116:119]\n" \
  "v_mfma_f32_16x16x32_bf16 v[108:111], a[216:219], v[32:35], v[108:111]\n" \
  "v_mfma_f32_16x16x32_bf16 v[120:123], a[216:219], v[36:39], v[120:123]\n" \
  "s_waitcnt lgkmcnt(2)\n" \
  "v_mfma_f32_16x16x32_bf16 v[100:103], a[60:63], v[40:43], v[100:103]\n" \
  "ds_read_b128 v[32:35], v10 offset:17408\n" \
  "v_mfma_f32_16x16x32_bf16 v[112:115], a[60:63], v[44:47], v[112:115]\n" \
  "ds_read_b128 v[36:39], v10 offset:37888\n" \
  "v_mfma_f32_16x16x32_bf16 v[104:107], a[140:143], v[40:43], v[104:107]\n" \
  "v_mov_b32 v11, s52\n" \
  "v_mfma_f32_16x16x32_bf16 v[116:119], a[140:143], v[44:47], v[116:119]\n" \
  "s_waitcnt vmcnt(10)\n" \
  "v_mfma_f32_16x16x32_bf16 v[108:111], a[220:223], v[40:43], v[108:111]\n" \
  "buffer_store_dword v11, v5, s[40:43], 0 offen sc1\n" \
  "v_mfma_f32_16x16x32_bf16 v[120:123], a[220:223], v[44:47], v[120:123]\n" \
  "s_waitcnt lgkmcnt(2)\n" \
  "v_mfma_f32_16x16x32_bf16 v[100:103], a[64:67], v[48:51], v[100:103]\n" \
  "ds_read_b128 v[40:43], v10 offset:18432\n" \
  "v_mfma_f32_16x16x32_bf16 v[112:115], a[64:67], v[52:55], v[112:115]\n" \
  "ds_read_b128 v[44:47], v10 offset:38912\n" \
  "v_mfma_f32_16x16x32_bf16 v[104:107], a[144:147], v[48:51], v[104:107]\n" \
  "v_mfma_f32_16x16x32_bf16 v[116:119], a[144:147], v[52:55], v[116:119]\n" \
  "v_mfma_f32_16x16x32_bf16 v[108:111], a[224:227], v[48:51], v[108:111]\n" \
  "v_mfma_f32_16x16x32_bf16 v[120:123], a[224:227], v[52:55], v[120:123]\n" \
  "s_waitcnt lgkmcnt(2)\n" \
  "v_mfma_f32_16x16x32_bf16 v[100:103], a[68:71], v[32:35], v[100:103]\n" \
  "ds_read_b128 v[48:51], v10 offset:19456\n" \
  "v_mfma_f32_16x16x32_bf16 v[112:115], a[68:71], v[36:39], v[112:115]\n" \
  "ds_read_b128 v[52:55], v10 offset:39936\n" \
  "v_mfma_f32_16x16x32_bf16 v[104:107], a[148:151], v[32:35], v[104:107]\n" \
  "v_readlane_b32 s69, v4, s52\n" \
  "v_mfma_f32_16x16x32_bf16 v[116:119], a[148:151], v[36:39], v[116:119]\n" \
  "v_readlane_b32 s71, v8, s52\n" \
  "v_mfma_f32_16x16x32_bf16 v[108:111], a[228:231], v[32:35], v[108:111]\n" \
  "s_add_u32 s52, s52, 1\n" \
  "v_mfma_f32_16x16x32_bf16 v[120:123], a[228:231], v[36:39], v[120:123]\n" \
  "s_waitcnt lgkmcnt(0)\n" \
  "s_barrier\n" \
  "v_mfma_f32_16x16x32_bf16 v[100:103], a[72:75], v[40:43], v[100:103]\n" \
  "ds_read_b128 v[32:35], v0 offset:0\n" \
  "v_mfma_f32_16x16x32_bf16 v[112:115], a[72:75], v[44:47], v[112:115]\n" \
  "ds_read_b128 v[36:39], v0 offset:20480\n" \
  "v_mfma_f32_16x16x32_bf16 v[104:107], a[152:155], v[40:43], v[104:107]\n" \
  "v_mfma_f32_16x16x32_bf16 v[116:119], a[152:155], v[44:47], v[116:119]\n" \
  "v_mfma_f32_16x16x32_bf16 v[108:111], a[232:235], v[40:43], v[108:111]\n" \
  "v_mfma_f32_16x16x32_bf16 v[120:123], a[232:235], v[44:47], v[120:123]\n" \
  "v_mfma_f32_16x16x32_bf16 v[100:103], a[76:79], v[48:51], v[100:103]\n" \
  "ds_read_b128 v[40:43], v0 offset:1024\n" \
  "v_mfma_f32_16x16x32_bf16 v[112:115], a[76:79], v[52:55], v[112:115]\n" \
  "ds_read_b128 v[44:47], v0 offset:21504\n" \
  "v_mfma_f32_16x16x32_bf16 v[104:107], a[156:159], v[48:51], v[104:107]\n" \
  "s_cmp_eq_u32 s52, s53\n" \
  "v_mfma_f32_16x16x32_bf16 v[116:119], a[156:159], v[52:55], v[116:119]\n" \
  "v_mfma_f32_16x16x32_bf16 v[108:111], a[236:239], v[48:51], v[108:111]\n" \
  "v_mfma_f32_16x16x32_bf16 v[120:123], a[236:239], v[52:55], v[120:123]\n" \
  "s_cbranch_scc0 LPL_%=\n" \
  "LPE_%=:\n" \
  "buffer_store_dwordx4 v[12:15], v7, s[40:43], s71 offen sc1\n" \
  "s_nop 6\n" \
  "v_cvt_pk_bf16_f32 v16, v100, v101\n" \
  "v_cvt_pk_bf16_f32 v17, v102, v103\n" \
  "v_cvt_pk_bf16_f32 v18, v104, v105\n" \
  "v_cvt_pk_bf16_f32 v19, v106, v107\n" \
  "v_cvt_pk_bf16_f32 v20, v112, v113\n" \
  "v_cvt_pk_bf16_f32 v21, v114, v115\n" \
  "v_cvt_pk_bf16_f32 v22, v116, v117\n" \
  "v_cvt_pk_bf16_f32 v23, v118, v119\n" \
  "v_cvt_pk_bf16_f32 v24, v108, v109\n" \
  "v_cvt_pk_bf16_f32 v25, v110, v111\n" \
  "v_cvt_pk_bf16_f32 v26, v120, v121\n" \
  "v_cvt_pk_bf16_f32 v27, v122, v123\n" \
  "buffer_store_dwordx4 v[16:19], v2, s[40:43], s69 offen sc1\n" \
  "buffer_store_dwordx4 v[20:23], v2, s[40:43], s69 offen offset:1024 sc1\n" \
  "buffer_store_dwordx4 v[24:27], v3, s[40:43], s69 offen sc1\n" \
  "v_mov_b32 v11, s52\n" \
  "s_waitcnt vmcnt(0) lgkmcnt(0)\n" \
  "buffer_store_dword v11, v5, s[40:43], 0 offen sc1\n"
#define VO_CLOBBERS \
  "v16", "v17", "v18", "v19", "v20", "v21", "v22", "v23", "v24", "v25", "v26", "v27", "v28", "v29", "v30", "v31", "v32", "v33", "v34", "v35", "v36", \
  "v37", "v38", "v39", "v40", "v41", "v42", "v43", "v44", "v45", "v46", "v47", "v48", "v49", "v50", "v51", "v52", "v53", "v54", "v55", "v56", "v57", \
  "v58", "v59", "v60", "v61", "v62", "v63", "v64", "v65", "v66", "v67", "v68", "v69", "v70", "v71", "v72", "v73", "v74", "v75", "v76", "v77", "v78", \
  "v79", "v80", "v81", "v82", "v83", "v84", "v85", "v86", "v87", "v88", "v89", "v90", "v91", "v92", "v93", "v94", "v95", "v96", "v97", "v98", "v99", \
  "v100", "v101", "v102", "v103", "v104", "v105", "v106", "v107", "v108", "v109", "v110", "v111", "v112", "v113", "v114", "v115", "v116", "v117", \
  "v118", "v119", "v120", "v121", "v122", "v123", "v124", "v125", "v126", "v127", "v224", "v225", "v226", "v227", "v228", "v229", "v230", "v231", \
  "v232", "v233", "v234", "v235", "v236", "v237", "v238", "v239", "v240", "v241", "v242", "v243", "v244", "v245", "v246", "v247", "v248", "v249", \
  "v250", "v251", "v252", "v253", "v254", "v255", "s68", "s69", "s70", "s71", "s72", "s73", "s74", "s75", "s76", "s77"
#define VO_ASM \
  "s_lshl_b32 s73, s54, 11\n" \
  "s_lshl_b32 s74, s54, 10\n" \
  "s_add_u32 s74, s74, 8192\n" \
  "s_mov_b32 s75, 0\n" \
  "s_mov_b32 s76, s52\n" \
  "s_mov_b32 s69, 0x40000000\n" \
  "s_barrier\n" \
  "v_mov_b32 v12, v0\n" \
  "LVPS0_%=:\n" \
  "buffer_load_dword v11, v9, s[36:39], 0 offen sc1\n" \
  "s_waitcnt vmcnt(0)\n" \
  "v_cmp_ge_u32 vcc, s76, v11\n" \
  "s_nop 3\n" \
  "s_cbranch_vccz LVS0_%=\n" \
  "s_add_u32 s75, s75, 1\n" \
  "s_cmp_gt_u32 s75, 200000\n" \
  "s_cbranch_scc1 LVS0_%=\n" \
  "s_sleep 2\n" \
  "s_branch LVPS0_%=\n" \
  "LVS0_%=:\n" \
  "v_readlane_b32 s71, v6, s76\n" \
  "s_add_u32 m0, s73, 0\n" \
  "s_nop 4\n" \
  "buffer_load_dwordx4 v1, s[36:39], s71 offen offset:0 sc1 lds\n" \
  "buffer_load_dwordx4 v1, s[36:39], s71 offen offset:1024 sc1 lds\n" \
  "s_add_u32 m0, s74, 0\n" \
  "s_add_u32 s76, s76, 1\n" \
  "buffer_load_dwordx4 v2, s[36:39], s71 offen sc1 lds\n" \
  "s_min_u32 s76, s76, 63\n" \
  "LVPS1_%=:\n" \
  "buffer_load_dword v11, v9, s[36:39], 0 offen sc1\n" \
  "s_waitcnt vmcnt(0)\n" \
  "v_cmp_ge_u32 vcc, s76, v11\n" \
  "s_nop 3\n" \
  "s_cbranch_vccz LVS1_%=\n" \
  "s_add_u32 s75, s75, 1\n" \
  "s_cmp_gt_u32 s75, 200000\n" \
  "s_cbranch_scc1 LVS1_%=\n" \
  "s_sleep 2\n" \
  "s_branch LVPS1_%=\n" \
  "LVS1_%=:\n" \
  "v_readlane_b32 s71, v6, s76\n" \
  "s_add_u32 m0, s73, 16384\n" \
  "s_nop 4\n" \
  "buffer_load_dwordx4 v1, s[36:39], s71 offen offset:0 sc1 lds\n" \
  "buffer_load_dwordx4 v1, s[36:39], s71 offen offset:1024 sc1 lds\n" \
  "s_add_u32 m0, s74, 16384\n" \
  "s_add_u32 s76, s76, 1\n" \
  "buffer_load_dwordx4 v2, s[36:39], s71 offen sc1 lds\n" \
  "s_min_u32 s76, s76, 63\n" \
  "s_xor_b32 s73, s73, 0x8000\n" \
  "s_xor_b32 s74, s74, 0x8000\n" \
  "buffer_load_dword v11, v9, s[36:39], 0 offen sc1\n" \
  "s_waitcnt vmcnt(0)\n" \
  "s_barrier\n" \
  "ds_read_b128 v[32:35], v12 offset:0\n" \
  "ds_read_b128 v[56:59], v12 offset:1024\n" \
  "ds_read_b128 v[36:39], v12 offset:2048\n" \
  "ds_read_b128 v[60:63], v12 offset:3072\n" \
  "ds_read_b128 v[40:43], v12 offset:4096\n" \
  "ds_read_b128 v[64:67], v12 offset:5120\n" \
  "ds_read_b128 v[44:47], v12 offset:6144\n" \
  "ds_read_b128 v[68:71], v12 offset:7168\n" \
  "s_waitcnt lgkmcnt(4)\n" \
  "ds_read_b64 v[48:49], v12 offset:8192\n" \
  "ds_read_b64 v[72:73], v12 offset:8200\n" \
  "ds_read_b64 v[50:51], v12 offset:9216\n" \
  "ds_read_b64 v[74:75], v12 offset:9224\n" \
  "ds_read_b64 v[52:53], v12 offset:10240\n" \
  "ds_read_b64 v[76:77], v12 offset:10248\n" \
  "ds_read_b64 v[54:55], v12 offset:11264\n" \
  "ds_read_b64 v[78:79], v12 offset:11272\n" \
  "s_waitcnt lgkmcnt(0)\n" \
  "LVL_%=:\n" \
  "s_mov_b32 s77, s69\n" \
  "v_readlane_b32 s68, v4, s52\n" \
  "v_readlane_b32 s69, v5, s52\n" \
  "v_mfma_f32_16x16x32_bf16 v[224:227], v[32:35], a[0:3], 0\n" \
  "v_mfma_f32_16x16x32_bf16 v[228:231], v[56:59], a[0:3], 0\n" \
  "v_mfma_f32_16x16x32_bf16 v[224:227], v[36:39], a[4:7], v[224:227]\n" \
  "v_cvt_pk_bf16_f32 v252, v232, v233\n" \
  "v_mfma_f32_16x16x32_bf16 v[228:231], v[60:63], a[4:7], v[228:231]\n" \
  "v_cvt_pk_bf16_f32 v253, v234, v235\n" \
  "v_mfma_f32_16x16x32_bf16 v[224:227], v[40:43], a[8:11], v[224:227]\n" \
  "v_cvt_pk_bf16_f32 v254, v236, v237\n" \
  "v_mfma_f32_16x16x32_bf16 v[228:231], v[64:67], a[8:11], v[228:231]\n" \
  "v_cvt_pk_bf16_f32 v255, v238, v239\n" \
  "v_mfma_f32_16x16x32_bf16 v[224:227], v[44:47], a[12:15], v[224:227]\n" \
  "s_add_u32 s72, s77, 589824\n" \
  "v_mfma_f32_16x16x32_bf16 v[228:231], v[68:71], a[12:15], v[228:231]\n" \
  "buffer_store_dwordx4 v[252:255], v3, s[36:39], s72 offen sc1\n" \
  "v_mfma_f32_16x16x32_bf16 v[224:227], v[48:51], a[16:19], v[224:227]\n" \
  "s_waitcnt vmcnt(7)\n" \
  "v_mfma_f32_16x16x32_bf16 v[228:231], v[72:75], a[16:19], v[228:231]\n" \
  "v_cmp_ge_u32 vcc, s76, v11\n" \
  "v_mfma_f32_16x16x32_bf16 v[224:227], v[52:55], a[20:23], v[224:227]\n" \
  "v_mfma_f32_16x16x32_bf16 v[228:231], v[76:79], a[20:23], v[228:231]\n" \
  "s_cbranch_vccnz LVP0_%=\n" \
  "LVK0_%=:\n" \
  "v_mfma_f32_16x16x32_bf16 v[232:235], v[32:35], a[24:27], 0\n" \
  "v_mfma_f32_16x16x32_bf16 v[236:239], v[56:59], a[24:27], 0\n" \
  "v_cvt_pk_bf16_f32 v248, v224, v225\n" \
  "v_mfma_f32_16x16x32_bf16 v[232:235], v[36:39], a[28:31], v[232:235]\n" \
  "v_cvt_pk_bf16_f32 v249, v226, v227\n" \
  "v_mfma_f32_16x16x32_bf16 v[236:239], v[60:63], a[28:31], v[236:239]\n" \
  "v_cvt_pk_bf16_f32 v250, v228, v229\n" \
  "v_mfma_f32_16x16x32_bf16 v[232:235], v[40:43], a[32:35], v[232:235]\n" \
  "v_cvt_pk_bf16_f32 v251, v230, v231\n" \
  "v_mfma_f32_16x16x32_bf16 v[236:239], v[64:67], a[32:35], v[236:239]\n" \
  "s_add_u32 s72, s68, 0\n" \
  "v_mfma_f32_16x16x32_bf16 v[232:235], v[44:47], a[36:39], v[232:235]\n" \
  "buffer_store_dwordx4 v[248:251], v3, s[36:39], s72 offen sc1\n" \
  "v_mfma_f32_16x16x32_bf16 v[236:239], v[68:71], a[36:39], v[236:239]\n" \
  "v_readlane_b32 s71, v6, s76\n" \
  "v_mfma_f32_16x16x32_bf16 v[232:235], v[48:51], a[40:43], v[232:235]\n" \
  "s_add_u32 m0, s73, 0\n" \
  "v_mfma_f32_16x16x32_bf16 v[236:239], v[72:75], a[40:43], v[236:239]\n" \
  "s_nop 2\n" \
  "v_mfma_f32_16x16x32_bf16 v[232:235], v[52:55], a[44:47], v[232:235]\n" \
  "buffer_load_dwordx4 v1, s[36:39], s71 offen offset:0 sc1 lds\n" \
  "v_mfma_f32_16x16x32_bf16 v[236:239], v[76:79], a[44:47], v[236:239]\n" \
  "buffer_load_dwordx4 v1, s[36:39], s71 offen offset:1024 sc1 lds\n" \
  "v_mfma_f32_16x16x32_bf16 v[224:227], v[32:35], a[48:51], 0\n" \
  "v_mfma_f32_16x16x32_bf16 v[228:231], v[56:59], a[48:51], 0\n" \
  "v_mfma_f32_16x16x32_bf16 v[224:227], v[36:39], a[52:55], v[224:227]\n" \
  "v_cvt_pk_bf16_f32 v252, v232, v233\n" \
  "v_mfma_f32_16x16x32_bf16 v[228:231], v[60:63], a[52:55], v[228:231]\n" \
  "v_cvt_pk_bf16_f32 v253, v234, v235\n" \
  "v_mfma_f32_16x16x32_bf16 v[224:227], v[40:43], a[56:59], v[224:227]\n" \
  "v_cvt_pk_bf16_f32 v254, v236, v237\n" \
  "v_mfma_f32_16x16x32_bf16 v[228:231], v[64:67], a[56:59], v[228:231]\n" \
  "v_cvt_pk_bf16_f32 v255, v238, v239\n" \
  "v_mfma_f32_16x16x32_bf16 v[224:227], v[44:47], a[60:63], v[224:227]\n" \
  "s_add_u32 s72, s68, 65536\n" \
  "v_mfma_f32_16x16x32_bf16 v[228:231], v[68:71], a[60:63], v[228:231]\n" \
  "buffer_store_dwordx4 v[252:255], v3, s[36:39], s72 offen sc1\n" \
  "v_mfma_f32_16x16x32_bf16 v[224:227], v[48:51], a[64:67], v[224:227]\n" \
  "s_add_u32 m0, s74, 0\n" \
  "v_mfma_f32_16x16x32_bf16 v[228:231], v[72:75], a[64:67], v[228:231]\n" \
  "s_add_u32 s76, s76, 1\n" \
  "v_mfma_f32_16x16x32_bf16 v[224:227], v[52:55], a[68:71], v[224:227]\n" \
  "buffer_load_dwordx4 v2, s[36:39], s71 offen sc1 lds\n" \
  "v_mfma_f32_16x16x32_bf16 v[228:231], v[76:79], a[68:71], v[228:231]\n" \
  "s_min_u32 s76, s76, 63\n" \
  "v_mfma_f32_16x16x32_bf16 v[232:235], v[32:35], a[72:75], 0\n" \
  "v_mfma_f32_16x16x32_bf16 v[236:239], v[56:59], a[72:75], 0\n" \
  "v_mfma_f32_16x16x32_bf16 v[232:235], v[36:39], a[76:79], v[232:235]\n" \
  "v_cvt_pk_bf16_f32 v248, v224, v225\n" \
  "v_mfma_f32_16x16x32_bf16 v[236:239], v[60:63], a[76:79], v[236:239]\n" \
  "v_cvt_pk_bf16_f32 v249, v226, v227\n" \
  "v_mfma_f32_16x16x32_bf16 v[232:235], v[40:43], a[80:83], v[232:235]\n" \
  "v_cvt_pk_bf16_f32 v250, v228, v229\n" \
  "v_mfma_f32_16x16x32_bf16 v[236:239], v[64:67], a[80:83], v[236:239]\n" \
  "v_cvt_pk_bf16_f32 v251, v230, v231\n" \
  "v_mfma_f32_16x16x32_bf16 v[232:235], v[44:47], a[84:87], v[232:235]\n" \
  "s_add_u32 s72, s69, 131072\n" \
  "v_mfma_f32_16x16x32_bf16 v[236:239], v[68:71], a[84:87], v[236:239]\n" \
  "buffer_store_dwordx4 v[248:251], v3, s[36:39], s72 offen sc1\n" \
  "v_mfma_f32_16x16x32_bf16 v[232:235], v[48:51], a[88:91], v[232:235]\n" \
  "v_mfma_f32_16x16x32_bf16 v[236:239], v[72:75], a[88:91], v[236:239]\n" \
  "v_mfma_f32_16x16x32_bf16 v[232:235], v[52:55], a[92:95], v[232:235]\n" \
  "v_mfma_f32_16x16x32_bf16 v[236:239], v[76:79], a[92:95], v[236:239]\n" \
  "v_mfma_f32_16x16x32_bf16 v[224:227], v[32:35], a[96:99], 0\n" \
  "v_mfma_f32_16x16x32_bf16 v[228:231], v[56:59], a[96:99], 0\n" \
  "v_mfma_f32_16x16x32_bf16 v[224:227], v[36:39], a[100:103], v[224:227]\n" \
  "v_cvt_pk_bf16_f32 v252, v232, v233\n" \
  "v_mfma_f32_16x16x32_bf16 v[228:231], v[60:63], a[100:103], v[228:231]\n" \
  "v_cvt_pk_bf16_f32 v253, v234, v235\n" \
  "v_mfma_f32_16x16x32_bf16 v[224:227], v[40:43], a[104:107], v[224:227]\n" \
  "v_cvt_pk_bf16_f32 v254, v236, v237\n" \
  "v_mfma_f32_16x16x32_bf16 v[228:231], v[64:67], a[104:107], v[228:231]\n" \
  "v_cvt_pk_bf16_f32 v255, v238, v239\n" \
  "v_mfma_f32_16x16x32_bf16 v[224:227], v[44:47], a[108:111], v[224:227]\n" \
  "s_add_u32 s72, s69, 196608\n" \
  "v_mfma_f32_16x16x32_bf16 v[228:231], v[68:71], a[108:111], v[228:231]\n" \
  "buffer_store_dwordx4 v[252:255], v3, s[36:39], s72 offen sc1\n" \
  "v_mfma_f32_16x16x32_bf16 v[224:227], v[48:51], a[112:115], v[224:227]\n" \
  "buffer_load_dword v11, v9, s[36:39], 0 offen sc1\n" \
  "v_mfma_f32_16x16x32_bf16 v[228:231], v[72:75], a[112:115], v[228:231]\n" \
  "v_mfma_f32_16x16x32_bf16 v[224:227], v[52:55], a[116:119], v[224:227]\n" \
  "v_mfma_f32_16x16x32_bf16 v[228:231], v[76:79], a[116:119], v[228:231]\n" \
  "v_mfma_f32_16x16x32_bf16 v[232:235], v[32:35], a[120:123], 0\n" \
  "v_mfma_f32_16x16x32_bf16 v[236:239], v[56:59], a[120:123], 0\n" \
  "v_mfma_f32_16x16x32_bf16 v[232:235], v[36:39], a[124:127], v[232:235]\n" \
  "v_cvt_pk_bf16_f32 v248, v224, v225\n" \
  "v_mfma_f32_16x16x32_bf16 v[236:239], v[60:63], a[124:127], v[236:239]\n" \
  "v_cvt_pk_bf16_f32 v249, v226, v227\n" \
  "v_mfma_f32_16x16x32_bf16 v[232:235], v[40:43], a[128:131], v[232:235]\n" \
  "v_cvt_pk_bf16_f32 v250, v228, v229\n" \
  "v_mfma_f32_16x16x32_bf16 v[236:239], v[64:67], a[128:131], v[236:239]\n" \
  "v_cvt_pk_bf16_f32 v251, v230, v231\n" \
  "v_mfma_f32_16x16x32_bf16 v[232:235], v[44:47], a[132:135], v[232:235]\n" \
  "s_add_u32 s72, s69, 262144\n" \
  "v_mfma_f32_16x16x32_bf16 v[236:239], v[68:71], a[132:135], v[236:239]\n" \
  "buffer_store_dwordx4 v[248:251], v3, s[36:39], s72 offen sc1\n" \
  "v_mfma_f32_16x16x32_bf16 v[232:235], v[48:51], a[136:139], v[232:235]\n" \
  "v_mfma_f32_16x16x32_bf16 v[236:239], v[72:75], a[136:139], v[236:239]\n" \
  "v_mfma_f32_16x16x32_bf16 v[232:235], v[52:55], a[140:143], v[232:235]\n" \
  "v_mfma_f32_16x16x32_bf16 v[236:239], v[76:79], a[140:143], v[236:239]\n" \
  "v_mfma_f32_16x16x32_bf16 v[224:227], v[32:35], a[144:147], 0\n" \
  "v_mfma_f32_16x16x32_bf16 v[228:231], v[56:59], a[144:147], 0\n" \
  "v_mfma_f32_16x16x32_bf16 v[224:227], v[36:39], a[148:151], v[224:227]\n" \
  "v_cvt_pk_bf16_f32 v252, v232, v233\n" \
  "v_mfma_f32_16x16x32_bf16 v[228:231], v[60:63], a[148:151], v[228:231]\n" \
  "v_cvt_pk_bf16_f32 v253, v234, v235\n" \
  "v_mfma_f32_16x16x32_bf16 v[224:227], v[40:43], a[152:155], v[224:227]\n" \
  "v_cvt_pk_bf16_f32 v254, v236, v237\n" \
  "v_mfma_f32_16x16x32_bf16 v[228:231], v[64:67], a[152:155], v[228:231]\n" \
  "v_cvt_pk_bf16_f32 v255, v238, v239\n" \
  "v_mfma_f32_16x16x32_bf16 v[224:227], v[44:47], a[156:159], v[224:227]\n" \
  "s_add_u32 s72, s69, 327680\n" \
  "v_mfma_f32_16x16x32_bf16 v[228:231], v[68:71], a[156:159], v[228:231]\n" \
  "buffer_store_dwordx4 v[252:255], v3, s[36:39], s72 offen sc1\n" \
  "v_mfma_f32_16x16x32_bf16 v[224:227], v[48:51], a[160:163], v[224:227]\n" \
  "v_mov_b32 v10, s52\n" \
  "v_mfma_f32_16x16x32_bf16 v[228:231], v[72:75], a[160:163], v[228:231]\n" \
  "s_waitcnt vmcnt(10)\n" \
  "v_mfma_f32_16x16x32_bf16 v[224:227], v[52:55], a[164:167], v[224:227]\n" \
  "buffer_store_dword v10, v7, s[36:39], 0 offen sc1\n" \
  "v_mfma_f32_16x16x32_bf16 v[228:231], v[76:79], a[164:167], v[228:231]\n" \
  "s_barrier\n" \
  "v_mfma_f32_16x16x32_bf16 v[232:235], v[32:35], a[168:171], 0\n" \
  "v_mfma_f32_16x16x32_bf16 v[236:239], v[56:59], a[168:171], 0\n" \
  "v_cvt_pk_bf16_f32 v248, v224, v225\n" \
  "v_cvt_pk_bf16_f32 v249, v226, v227\n" \
  "v_mfma_f32_16x16x32_bf16 v[232:235], v[36:39], a[172:175], v[232:235]\n" \
  "v_cvt_pk_bf16_f32 v250, v228, v229\n" \
  "v_cvt_pk_bf16_f32 v251, v230, v231\n" \
  "v_mfma_f32_16x16x32_bf16 v[236:239], v[60:63], a[172:175], v[236:239]\n" \
  "s_add_u32 s72, s69, 393216\n" \
  "v_mfma_f32_16x16x32_bf16 v[232:235], v[40:43], a[176:179], v[232:235]\n" \
  "buffer_store_dwordx4 v[248:251], v3, s[36:39], s72 offen sc1\n" \
  "v_mfma_f32_16x16x32_bf16 v[236:239], v[64:67], a[176:179], v[236:239]\n" \
  "ds_read_b128 v[80:83], v12 offset:16384\n" \
  "v_mfma_f32_16x16x32_bf16 v[232:235], v[44:47], a[180:183], v[232:235]\n" \
  "ds_read_b128 v[104:107], v12 offset:17408\n" \
  "v_mfma_f32_16x16x32_bf16 v[236:239], v[68:71], a[180:183], v[236:239]\n" \
  "ds_read_b128 v[84:87], v12 offset:18432\n" \
  "v_mfma_f32_16x16x32_bf16 v[232:235], v[48:51], a[184:187], v[232:235]\n" \
  "ds_read_b128 v[108:111], v12 offset:19456\n" \
  "v_mfma_f32_16x16x32_bf16 v[236:239], v[72:75], a[184:187], v[236:239]\n" \
  "ds_read_b128 v[88:91], v12 offset:20480\n" \
  "v_mfma_f32_16x16x32_bf16 v[232:235], v[52:55], a[188:191], v[232:235]\n" \
  "ds_read_b128 v[112:115], v12 offset:21504\n" \
  "v_mfma_f32_16x16x32_bf16 v[236:239], v[76:79], a[188:191], v[236:239]\n" \
  "ds_read_b128 v[92:95], v12 offset:22528\n" \
  "v_mfma_f32_16x16x32_bf16 v[224:227], v[32:35], a[192:195], 0\n" \
  "v_mfma_f32_16x16x32_bf16 v[228:231], v[56:59], a[192:195], 0\n" \
  "v_cvt_pk_bf16_f32 v252, v232, v233\n" \
  "v_cvt_pk_bf16_f32 v253, v234, v235\n" \
  "v_mfma_f32_16x16x32_bf16 v[224:227], v[36:39], a[196:199], v[224:227]\n" \
  "v_cvt_pk_bf16_f32 v254, v236, v237\n" \
  "v_cvt_pk_bf16_f32 v255, v238, v239\n" \
  "v_mfma_f32_16x16x32_bf16 v[228:231], v[60:63], a[196:199], v[228:231]\n" \
  "s_add_u32 s72, s69, 458752\n" \
  "v_mfma_f32_16x16x32_bf16 v[224:227], v[40:43], a[200:203], v[224:227]\n" \
  "buffer_store_dwordx4 v[252:255], v3, s[36:39], s72 offen sc1\n" \
  "v_mfma_f32_16x16x32_bf16 v[228:231], v[64:67], a[200:203], v[228:231]\n" \
  "ds_read_b128 v[116:119], v12 offset:23552\n" \
  "v_mfma_f32_16x16x32_bf16 v[224:227], v[44:47], a[204:207], v[224:227]\n" \
  "ds_read_b64 v[96:97], v12 offset:24576\n" \
  "v_mfma_f32_16x16x32_bf16 v[228:231], v[68:71], a[204:207], v[228:231]\n" \
  "ds_read_b64 v[120:121], v12 offset:24584\n" \
  "v_mfma_f32_16x16x32_bf16 v[224:227], v[48:51], a[208:211], v[224:227]\n" \
  "ds_read_b64 v[98:99], v12 offset:25600\n" \
  "v_mfma_f32_16x16x32_bf16 v[228:231], v[72:75], a[208:211], v[228:231]\n" \
  "ds_read_b64 v[122:123], v12 offset:25608\n" \
  "v_mfma_f32_16x16x32_bf16 v[224:227], v[52:55], a[212:215], v[224:227]\n" \
  "ds_read_b64 v[100:101], v12 offset:26624\n" \
  "v_mfma_f32_16x16x32_bf16 v[228:231], v[76:79], a[212:215], v[228:231]\n" \
  "ds_read_b64 v[124:125], v12 offset:26632\n" \
  "v_mfma_f32_16x16x32_bf16 v[232:235], v[32:35], a[216:219], 0\n" \
  "v_mfma_f32_16x16x32_bf16 v[236:239], v[56:59], a[216:219], 0\n" \
  "v_mfma_f32_16x16x32_bf16 v[232:235], v[36:39], a[220:223], v[232:235]\n" \
  "v_cvt_pk_bf16_f32 v248, v224, v225\n" \
  "v_mfma_f32_16x16x32_bf16 v[236:239], v[60:63], a[220:223], v[236:239]\n" \
  "v_cvt_pk_bf16_f32 v249, v226, v227\n" \
  "v_mfma_f32_16x16x32_bf16 v[232:235], v[40:43], a[224:227], v[232:235]\n" \
  "v_cvt_pk_bf16_f32 v250, v228, v229\n" \
  "v_mfma_f32_16x16x32_bf16 v[236:239], v[64:67], a[224:227], v[236:239]\n" \
  "v_cvt_pk_bf16_f32 v251, v230, v231\n" \
  "v_mfma_f32_16x16x32_bf16 v[232:235], v[44:47], a[228:231], v[232:235]\n" \
  "s_add_u32 s72, s69, 524288\n" \
  "v_mfma_f32_16x16x32_bf16 v[236:239], v[68:71], a[228:231], v[236:239]\n" \
  "buffer_store_dwordx4 v[248:251], v3, s[36:39], s72 offen sc1\n" \
  "v_mfma_f32_16x16x32_bf16 v[232:235], v[48:51], a[232:235], v[232:235]\n" \
  "ds_read_b64 v[102:103], v12 offset:27648\n" \
  "v_mfma_f32_16x16x32_bf16 v[236:239], v[72:75], a[232:235], v[236:239]\n" \
  "ds_read_b64 v[126:127], v12 offset:27656\n" \
  "v_mfma_f32_16x16x32_bf16 v[232:235], v[52:55], a[236:239], v[232:235]\n" \
  "s_add_u32 s52, s52, 1\n" \
  "v_mfma_f32_16x16x32_bf16 v[236:239], v[76:79], a[236:239], v[236:239]\n" \
  "s_cmp_eq_u32 s52, s53\n" \
  "s_waitcnt lgkmcnt(0)\n" \
  "s_mov_b32 s77, s69\n" \
  "v_readlane_b32 s68, v4, s52\n" \
  "v_readlane_b32 s69, v5, s52\n" \
  "v_mfma_f32_16x16x32_bf16 v[224:227], v[80:83], a[0:3], 0\n" \
  "v_mfma_f32_16x16x32_bf16 v[228:231], v[104:107], a[0:3], 0\n" \
  "v_mfma_f32_16x16x32_bf16 v[224:227], v[84:87], a[4:7], v[224:227]\n" \
  "v_cvt_pk_bf16_f32 v252, v232, v233\n" \
  "v_mfma_f32_16x16x32_bf16 v[228:231], v[108:111], a[4:7], v[228:231]\n" \
  "v_cvt_pk_bf16_f32 v253, v234, v235\n" \
  "v_mfma_f32_16x16x32_bf16 v[224:227], v[88:91], a[8:11], v[224:227]\n" \
  "v_cvt_pk_bf16_f32 v254, v236, v237\n" \
  "v_mfma_f32_16x16x32_bf16 v[228:231], v[112:115], a[8:11], v[228:231]\n" \
  "v_cvt_pk_bf16_f32 v255, v238, v239\n" \
  "v_mfma_f32_16x16x32_bf16 v[224:227], v[92:95], a[12:15], v[224:227]\n" \
  "s_add_u32 s72, s77, 589824\n" \
  "v_mfma_f32_16x16x32_bf16 v[228:231], v[116:119], a[12:15], v[228:231]\n" \
  "buffer_store_dwordx4 v[252:255], v3, s[36:39], s72 offen sc1\n" \
  "v_mfma_f32_16x16x32_bf16 v[224:227], v[96:99], a[16:19], v[224:227]\n" \
  "s_waitcnt vmcnt(7)\n" \
  "v_mfma_f32_16x16x32_bf16 v[228:231], v[120:123], a[16:19], v[228:231]\n" \
  "v_cmp_ge_u32 vcc, s76, v11\n" \
  "v_mfma_f32_16x16x32_bf16 v[224:227], v[100:103], a[20:23], v[224:227]\n" \
  "v_mfma_f32_16x16x32_bf16 v[228:231], v[124:127], a[20:23], v[228:231]\n" \
  "s_cbranch_vccnz LVP1_%=\n" \
  "LVK1_%=:\n" \
  "v_mfma_f32_16x16x32_bf16 v[232:235], v[80:83], a[24:27], 0\n" \
  "v_mfma_f32_16x16x32_bf16 v[236:239], v[104:107], a[24:27], 0\n" \
  "v_cvt_pk_bf16_f32 v248, v224, v225\n" \
  "v_mfma_f32_16x16x32_bf16 v[232:235], v[84:87], a[28:31], v[232:235]\n" \
  "v_cvt_pk_bf16_f32 v249, v226, v227\n" \
  "v_mfma_f32_16x16x32_bf16 v[236:239], v[108:111], a[28:31], v[236:239]\n" \
  "v_cvt_pk_bf16_f32 v250, v228, v229\n" \
  "v_mfma_f32_16x16x32_bf16 v[232:235], v[88:91], a[32:35], v[232:235]\n" \
  "v_cvt_pk_bf16_f32 v251, v230, v231\n" \
  "v_mfma_f32_16x16x32_bf16 v[236:239], v[112:115], a[32:35], v[236:239]\n" \
  "s_add_u32 s72, s68, 0\n" \
  "v_mfma_f32_16x16x32_bf16 v[232:235], v[92:95], a[36:39], v[232:235]\n" \
  "buffer_store_dwordx4 v[248:251], v3, s[36:39], s72 offen sc1\n" \
  "v_mfma_f32_16x16x32_bf16 v[236:239], v[116:119], a[36:39], v[236:239]\n" \
  "v_readlane_b32 s71, v6, s76\n" \
  "v_mfma_f32_16x16x32_bf16 v[232:235], v[96:99], a[40:43], v[232:235]\n" \
  "s_add_u32 m0, s73, 16384\n" \
  "v_mfma_f32_16x16x32_bf16 v[236:239], v[120:123], a[40:43], v[236:239]\n" \
  "s_nop 2\n" \
  "v_mfma_f32_16x16x32_bf16 v[232:235], v[100:103], a[44:47], v[232:235]\n" \
  "buffer_load_dwordx4 v1, s[36:39], s71 offen offset:0 sc1 lds\n" \
  "v_mfma_f32_16x16x32_bf16 v[236:239], v[124:127], a[44:47], v[236:239]\n" \
  "buffer_load_dwordx4 v1, s[36:39], s71 offen offset:1024 sc1 lds\n" \
  "v_mfma_f32_16x16x32_bf16 v[224:227], v[80:83], a[48:51], 0\n" \
  "v_mfma_f32_16x16x32_bf16 v[228:231], v[104:107], a[48:51], 0\n" \
  "v_mfma_f32_16x16x32_bf16 v[224:227], v[84:87], a[52:55], v[224:227]\n" \
  "v_cvt_pk_bf16_f32 v252, v232, v233\n" \
  "v_mfma_f32_16x16x32_bf16 v[228:231], v[108:111], a[52:55], v[228:231]\n" \
  "v_cvt_pk_bf16_f32 v253, v234, v235\n" \
  "v_mfma_f32_16x16x32_bf16 v[224:227], v[88:91], a[56:59], v[224:227]\n" \
  "v_cvt_pk_bf16_f32 v254, v236, v237\n" \
  "v_mfma_f32_16x16x32_bf16 v[228:231], v[112:115], a[56:59], v[228:231]\n" \
  "v_cvt_pk_bf16_f32 v255, v238, v239\n" \
  "v_mfma_f32_16x16x32_bf16 v[224:227], v[92:95], a[60:63], v[224:227]\n" \
  "s_add_u32 s72, s68, 65536\n" \
  "v_mfma_f32_16x16x32_bf16 v[228:231], v[116:119], a[60:63], v[228:231]\n" \
  "buffer_store_dwordx4 v[252:255], v3, s[36:39], s72 offen sc1\n" \
  "v_mfma_f32_16x16x32_bf16 v[224:227], v[96:99], a[64:67], v[224:227]\n" \
  "s_add_u32 m0, s74, 16384\n" \
  "v_mfma_f32_16x16x32_bf16 v[228:231], v[120:123], a[64:67], v[228:231]\n" \
  "s_add_u32 s76, s76, 1\n" \
  "v_mfma_f32_16x16x32_bf16 v[224:227], v[100:103], a[68:71], v[224:227]\n" \
  "buffer_load_dwordx4 v2, s[36:39], s71 offen sc1 lds\n" \
  "v_mfma_f32_16x16x32_bf16 v[228:231], v[124:127], a[68:71], v[228:231]\n" \
  "s_min_u32 s76, s76, 63\n" \
  "v_mfma_f32_16x16x32_bf16 v[232:235], v[80:83], a[72:75], 0\n" \
  "v_mfma_f32_16x16x32_bf16 v[236:239], v[104:107], a[72:75], 0\n" \
  "v_mfma_f32_16x16x32_bf16 v[232:235], v[84:87], a[76:79], v[232:235]\n" \
  "v_cvt_pk_bf16_f32 v248, v224, v225\n" \
  "v_mfma_f32_16x16x32_bf16 v[236:239], v[108:111], a[76:79], v[236:239]\n" \
  "v_cvt_pk_bf16_f32 v249, v226, v227\n" \
  "v_mfma_f32_16x16x32_bf16 v[232:235], v[88:91], a[80:83], v[232:235]\n" \
  "v_cvt_pk_bf16_f32 v250, v228, v229\n" \
  "v_mfma_f32_16x16x32_bf16 v[236:239], v[112:115], a[80:83], v[236:239]\n" \
  "v_cvt_pk_bf16_f32 v251, v230, v231\n" \
  "v_mfma_f32_16x16x32_bf16 v[232:235], v[92:95], a[84:87], v[232:235]\n" \
  "s_add_u32 s72, s69, 131072\n" \
  "v_mfma_f32_16x16x32_bf16 v[236:239], v[116:119], a[84:87], v[236:239]\n" \
  "buffer_store_dwordx4 v[248:251], v3, s[36:39], s72 offen sc1\n" \
  "v_mfma_f32_16x16x32_bf16 v[232:235], v[96:99], a[88:91], v[232:235]\n" \
  "v_mfma_f32_16x16x32_bf16 v[236:239], v[120:123], a[88:91], v[236:239]\n" \
  "v_mfma_f32_16x16x32_bf16 v[232:235], v[100:103], a[92:95], v[232:235]\n" \
  "v_mfma_f32_16x16x32_bf16 v[236:239], v[124:127], a[92:95], v[236:239]\n" \
  "v_mfma_f32_16x16x32_bf16 v[224:227], v[80:83], a[96:99], 0\n" \
  "v_mfma_f32_16x16x32_bf16 v[228:231], v[104:107], a[96:99], 0\n" \
  "v_mfma_f32_16x16x32_bf16 v[224:227], v[84:87], a[100:103], v[224:227]\n" \
  "v_cvt_pk_bf16_f32 v252, v232, v233\n" \
  "v_mfma_f32_16x16x32_bf16 v[228:231], v[108:111], a[100:103], v[228:231]\n" \
  "v_cvt_pk_bf16_f32 v253, v234, v235\n" \
  "v_mfma_f32_16x16x32_bf16 v[224:227], v[88:91], a[104:107], v[224:227]\n" \
  "v_cvt_pk_bf16_f32 v254, v236, v237\n" \
  "v_mfma_f32_16x16x32_bf16 v[228:231], v[112:115], a[104:107], v[228:231]\n" \
  "v_cvt_pk_bf16_f32 v255, v238, v239\n" \
  "v_mfma_f32_16x16x32_bf16 v[224:227], v[92:95], a[108:111], v[224:227]\n" \
  "s_add_u32 s72, s69, 196608\n" \
  "v_mfma_f32_16x16x32_bf16 v[228:231], v[116:119], a[108:111], v[228:231]\n" \
  "buffer_store_dwordx4 v[252:255], v3, s[36:39], s72 offen sc1\n" \
  "v_mfma_f32_16x16x32_bf16 v[224:227], v[96:99], a[112:115], v[224:227]\n" \
  "buffer_load_dword v11, v9, s[36:39], 0 offen sc1\n" \
  "v_mfma_f32_16x16x32_bf16 v[228:231], v[120:123], a[112:115], v[228:231]\n" \
  "v_mfma_f32_16x16x32_bf16 v[224:227], v[100:103], a[116:119], v[224:227]\n" \
  "v_mfma_f32_16x16x32_bf16 v[228:231], v[124:127], a[116:119], v[228:231]\n" \
  "v_mfma_f32_16x16x32_bf16 v[232:235], v[80:83], a[120:123], 0\n" \
  "v_mfma_f32_16x16x32_bf16 v[236:239], v[104:107], a[120:123], 0\n" \
  "v_mfma_f32_16x16x32_bf16 v[232:235], v[84:87], a[124:127], v[232:235]\n" \
  "v_cvt_pk_bf16_f32 v248, v224, v225\n" \
  "v_mfma_f32_16x16x32_bf16 v[236:239], v[108:111], a[124:127], v[236:239]\n" \
  "v_cvt_pk_bf16_f32 v249, v226, v227\n" \
  "v_mfma_f32_16x16x32_bf16 v[232:235], v[88:91], a[128:131], v[232:235]\n" \
  "v_cvt_pk_bf16_f32 v250, v228, v229\n" \
  "v_mfma_f32_16x16x32_bf16 v[236:239], v[112:115], a[128:131], v[236:239]\n" \
  "v_cvt_pk_bf16_f32 v251, v230, v231\n" \
  "v_mfma_f32_16x16x32_bf16 v[232:235], v[92:95], a[132:135], v[232:235]\n" \
  "s_add_u32 s72, s69, 262144\n" \
  "v_mfma_f32_16x16x32_bf16 v[236:239], v[116:119], a[132:135], v[236:239]\n" \
  "buffer_store_dwordx4 v[248:251], v3, s[36:39], s72 offen sc1\n" \
  "v_mfma_f32_16x16x32_bf16 v[232:235], v[96:99], a[136:139], v[232:235]\n" \
  "s_xor_b32 s73, s73, 0x8000\n" \
  "v_mfma_f32_16x16x32_bf16 v[236:239], v[120:123], a[136:139], v[236:239]\n" \
  "s_xor_b32 s74, s74, 0x8000\n" \
  "v_mfma_f32_16x16x32_bf16 v[232:235], v[100:103], a[140:143], v[232:235]\n" \
  "v_xor_b32 v12, 0x8000, v12\n" \
  "v_mfma_f32_16x16x32_bf16 v[236:239], v[124:127], a[140:143], v[236:239]\n" \
  "v_mfma_f32_16x16x32_bf16 v[224:227], v[80:83], a[144:147], 0\n" \
  "v_mfma_f32_16x16x32_bf16 v[228:231], v[104:107], a[144:147], 0\n" \
  "v_mfma_f32_16x16x32_bf16 v[224:227], v[84:87], a[148:151], v[224:227]\n" \
  "v_cvt_pk_bf16_f32 v252, v232, v233\n" \
  "v_mfma_f32_16x16x32_bf16 v[228:231], v[108:111], a[148:151], v[228:231]\n" \
  "v_cvt_pk_bf16_f32 v253, v234, v235\n" \
  "v_mfma_f32_16x16x32_bf16 v[224:227], v[88:91], a[152:155], v[224:227]\n" \
  "v_cvt_pk_bf16_f32 v254, v236, v237\n" \
  "v_mfma_f32_16x16x32_bf16 v[228:231], v[112:115], a[152:155], v[228:231]\n" \
  "v_cvt_pk_bf16_f32 v255, v238, v239\n" \
  "v_mfma_f32_16x16x32_bf16 v[224:227], v[92:95], a[156:159], v[224:227]\n" \
  "s_add_u32 s72, s69, 327680\n" \
  "v_mfma_f32_16x16x32_bf16 v[228:231], v[116:119], a[156:159], v[228:231]\n" \
  "buffer_store_dwordx4 v[252:255], v3, s[36:39], s72 offen sc1\n" \
  "v_mfma_f32_16x16x32_bf16 v[224:227], v[96:99], a[160:163], v[224:227]\n" \
  "v_mov_b32 v10, s52\n" \
  "v_mfma_f32_16x16x32_bf16 v[228:231], v[120:123], a[160:163], v[228:231]\n" \
  "s_waitcnt vmcnt(10)\n" \
  "v_mfma_f32_16x16x32_bf16 v[224:227], v[100:103], a[164:167], v[224:227]\n" \
  "buffer_store_dword v10, v7, s[36:39], 0 offen sc1\n" \
  "v_mfma_f32_16x16x32_bf16 v[228:231], v[124:127], a[164:167], v[228:231]\n" \
  "s_barrier\n" \
  "v_mfma_f32_16x16x32_bf16 v[232:235], v[80:83], a[168:171], 0\n" \
  "v_mfma_f32_16x16x32_bf16 v[236:239], v[104:107], a[168:171], 0\n" \
  "v_cvt_pk_bf16_f32 v248, v224, v225\n" \
  "v_cvt_pk_bf16_f32 v249, v226, v227\n" \
  "v_mfma_f32_16x16x32_bf16 v[232:235], v[84:87], a[172:175], v[232:235]\n" \
  "v_cvt_pk_bf16_f32 v250, v228, v229\n" \
  "v_cvt_pk_bf16_f32 v251, v230, v231\n" \
  "v_mfma_f32_16x16x32_bf16 v[236:239], v[108:111], a[172:175], v[236:239]\n" \
  "s_add_u32 s72, s69, 393216\n" \
  "v_mfma_f32_16x16x32_bf16 v[232:235], v[88:91], a[176:179], v[232:235]\n" \
  "buffer_store_dwordx4 v[248:251], v3, s[36:39], s72 offen sc1\n" \
  "v_mfma_f32_16x16x32_bf16 v[236:239], v[112:115], a[176:179], v[236:239]\n" \
  "ds_read_b128 v[32:35], v12 offset:0\n" \
  "v_mfma_f32_16x16x32_bf16 v[232:235], v[92:95], a[180:183], v[232:235]\n" \
  "ds_read_b128 v[56:59], v12 offset:1024\n" \
  "v_mfma_f32_16x16x32_bf16 v[236:239], v[116:119], a[180:183], v[236:239]\n" \
  "ds_read_b128 v[36:39], v12 offset:2048\n" \
  "v_mfma_f32_16x16x32_bf16 v[232:235], v[96:99], a[184:187], v[232:235]\n" \
  "ds_read_b128 v[60:63], v12 offset:3072\n" \
  "v_mfma_f32_16x16x32_bf16 v[236:239], v[120:123], a[184:187], v[236:239]\n" \
  "ds_read_b128 v[40:43], v12 offset:4096\n" \
  "v_mfma_f32_16x16x32_bf16 v[232:235], v[100:103], a[188:191], v[232:235]\n" \
  "ds_read_b128 v[64:67], v12 offset:5120\n" \
  "v_mfma_f32_16x16x32_bf16 v[236:239], v[124:127], a[188:191], v[236:239]\n" \
  "ds_read_b128 v[44:47], v12 offset:6144\n" \
  "v_mfma_f32_16x16x32_bf16 v[224:227], v[80:83], a[192:195], 0\n" \
  "v_mfma_f32_16x16x32_bf16 v[228:231], v[104:107], a[192:195], 0\n" \
  "v_cvt_pk_bf16_f32 v252, v232, v233\n" \
  "v_cvt_pk_bf16_f32 v253, v234, v235\n" \
  "v_mfma_f32_16x16x32_bf16 v[224:227], v[84:87], a[196:199], v[224:227]\n" \
  "v_cvt_pk_bf16_f32 v254, v236, v237\n" \
  "v_cvt_pk_bf16_f32 v255, v238, v239\n" \
  "v_mfma_f32_16x16x32_bf16 v[228:231], v[108:111], a[196:199], v[228:231]\n" \
  "s_add_u32 s72, s69, 458752\n" \
  "v_mfma_f32_16x16x32_bf16 v[224:227], v[88:91], a[200:203], v[224:227]\n" \
  "buffer_store_dwordx4 v[252:255], v3, s[36:39], s72 offen sc1\n" \
  "v_mfma_f32_16x16x32_bf16 v[228:231], v[112:115], a[200:203], v[228:231]\n" \
  "ds_read_b128 v[68:71], v12 offset:7168\n" \
  "v_mfma_f32_16x16x32_bf16 v[224:227], v[92:95], a[204:207], v[224:227]\n" \
  "ds_read_b64 v[48:49], v12 offset:8192\n" \
  "v_mfma_f32_16x16x32_bf16 v[228:231], v[116:119], a[204:207], v[228:231]\n" \
  "ds_read_b64 v[72:73], v12 offset:8200\n" \
  "v_mfma_f32_16x16x32_bf16 v[224:227], v[96:99], a[208:211], v[224:227]\n" \
  "ds_read_b64 v[50:51], v12 offset:9216\n" \
  "v_mfma_f32_16x16x32_bf16 v[228:231], v[120:123], a[208:211], v[228:231]\n" \
  "ds_read_b64 v[74:75], v12 offset:9224\n" \
  "v_mfma_f32_16x16x32_bf16 v[224:227], v[100:103], a[212:215], v[224:227]\n" \
  "ds_read_b64 v[52:53], v12 offset:10240\n" \
  "v_mfma_f32_16x16x32_bf16 v[228:231], v[124:127], a[212:215], v[228:231]\n" \
  "ds_read_b64 v[76:77], v12 offset:10248\n" \
  "v_mfma_f32_16x16x32_bf16 v[232:235], v[80:83], a[216:219], 0\n" \
  "v_mfma_f32_16x16x32_bf16 v[236:239], v[104:107], a[216:219], 0\n" \
  "v_mfma_f32_16x16x32_bf16 v[232:235], v[84:87], a[220:223], v[232:235]\n" \
  "v_cvt_pk_bf16_f32 v248, v224, v225\n" \
  "v_mfma_f32_16x16x32_bf16 v[236:239], v[108:111], a[220:223], v[236:239]\n" \
  "v_cvt_pk_bf16_f32 v249, v226, v227\n" \
  "v_mfma_f32_16x16x32_bf16 v[232:235], v[88:91], a[224:227], v[232:235]\n" \
  "v_cvt_pk_bf16_f32 v250, v228, v229\n" \
  "v_mfma_f32_16x16x32_bf16 v[236:239], v[112:115], a[224:227], v[236:239]\n" \
  "v_cvt_pk_bf16_f32 v251, v230, v231\n" \
  "v_mfma_f32_16x16x32_bf16 v[232:235], v[92:95], a[228:231], v[232:235]\n" \
  "s_add_u32 s72, s69, 524288\n" \
  "v_mfma_f32_16x16x32_bf16 v[236:239], v[116:119], a[228:231], v[236:239]\n" \
  "buffer_store_dwordx4 v[248:251], v3, s[36:39], s72 offen sc1\n" \
  "v_mfma_f32_16x16x32_bf16 v[232:235], v[96:99], a[232:235], v[232:235]\n" \
  "ds_read_b64 v[54:55], v12 offset:11264\n" \
  "v_mfma_f32_16x16x32_bf16 v[236:239], v[120:123], a[232:235], v[236:239]\n" \
  "ds_read_b64 v[78:79], v12 offset:11272\n" \
  "v_mfma_f32_16x16x32_bf16 v[232:235], v[100:103], a[236:239], v[232:235]\n" \
  "s_add_u32 s52, s52, 1\n" \
  "v_mfma_f32_16x16x32_bf16 v[236:239], v[124:127], a[236:239], v[236:239]\n" \
  "s_cmp_eq_u32 s52, s53\n" \
  "s_waitcnt lgkmcnt(0)\n" \
  "s_cbranch_scc0 LVL_%=\n" \
  "s_mov_b32 s77, s69\n" \
  "s_nop 7\n" \
  "v_cvt_pk_bf16_f32 v252, v232, v233\n" \
  "v_cvt_pk_bf16_f32 v253, v234, v235\n" \
  "v_cvt_pk_bf16_f32 v254, v236, v237\n" \
  "v_cvt_pk_bf16_f32 v255, v238, v239\n" \
  "s_add_u32 s72, s77, 589824\n" \
  "buffer_store_dwordx4 v[252:255], v3, s[36:39], s72 offen sc1\n" \
  "v_mov_b32 v10, s52\n" \
  "s_waitcnt vmcnt(0)\n" \
  "buffer_store_dword v10, v7, s[36:39], 0 offen sc1\n" \
  "s_branch LVD_%=\n" \
  "LVP0_%=:\n" \
  "buffer_load_dword v11, v9, s[36:39], 0 offen sc1\n" \
  "s_waitcnt vmcnt(0)\n" \
  "v_cmp_ge_u32 vcc, s76, v11\n" \
  "s_nop 3\n" \
  "s_cbranch_vccz LVK0_%=\n" \
  "s_add_u32 s75, s75, 1\n" \
  "s_cmp_gt_u32 s75, 200000\n" \
  "s_cbranch_scc1 LVK0_%=\n" \
  "s_sleep 2\n" \
  "s_branch LVP0_%=\n" \
  "LVP1_%=:\n" \
  "buffer_load_dword v11, v9, s[36:39], 0 offen sc1\n" \
  "s_waitcnt vmcnt(0)\n" \
  "v_cmp_ge_u32 vcc, s76, v11\n" \
  "s_nop 3\n" \
  "s_cbranch_vccz LVK1_%=\n" \
  "s_add_u32 s75, s75, 1\n" \
  "s_cmp_gt_u32 s75, 200000\n" \
  "s_cbranch_scc1 LVK1_%=\n" \
  "s_sleep 2\n" \
  "s_branch LVP1_%=\n" \
  "LVD_%=:\n"
#define UG_CLOBBERS \
  "v32", "v33", "v34", "v35", "v36", "v37", "v38", "v39", "v40", "v41", "v42", "v43", "v44", "v45", "v46", "v47", "v48", "v49", "v50", "v51", "v52", \
  "v53", "v54", "v55", "v56", "v57", "v58", "v59", "v60", "v61", "v62", "v63", "v64", "v65", "v66", "v67", "v68", "v69", "v70", "v71", "v72", "v73", \
  "v74", "v75", "v76", "v77", "v78", "v79", "v80", "v81", "v82", "v83", "v84", "v85", "v86", "v87", "v88", "v89", "v90", "v91", "v92", "v93", "v94", \
  "v95", "v96", "v97", "v98", "v99", "v100", "v101", "v102", "v103", "v104", "v105", "v106", "v107", "v108", "v109", "v110", "v111", "v112", "v113", \
  "v114", "v115", "v116", "v117", "v118", "v119", "v120", "v121", "v122", "v123", "v124", "v125", "v126", "v127", "v128", "v129", "v130", "v131", \
  "v132", "v133", "v134", "v135", "v136", "v137", "v138", "v139", "v140", "v141", "v142", "v143", "v144", "v145", "v146", "v147", "v148", "v149", \
  "v150", "v151", "v152", "v153", "v154", "v155", "v156", "v157", "v158", "v159", "v160", "v161", "v162", "v163", "v164", "v165", "v166", "v167", \
  "v168", "v169", "v170", "v171", "v172", "v173", "v174", "v175", "v176", "v177", "v178", "v179", "v180", "v181", "v182", "v183", "v184", "v185", \
  "v186", "v187", "v188", "v189", "v190", "v191", "v192", "v193", "v194", "v195", "v196", "v197", "v198", "v199", "v200", "v201", "v202", "v203", \
  "v204", "v205", "v206", "v207", "s68", "s69", "s70", "s71", "s72", "s73", "s74", "s75", "s76", "s77"
#define UG_ASM \
  "s_and_b32 s73, s52, 1\n" \
  "s_lshr_b32 s74, s52, 1\n" \
  "s_mul_i32 s68, s73, 10\n" \
  "s_add_u32 s77, s68, 10\n" \
  "s_add_u32 s75, s68, s74\n" \
  "s_mul_i32 s75, s75, 393216\n" \
  "s_lshl_b32 s73, s73, 1\n" \
  "s_add_u32 s73, s73, s74\n" \
  "s_mul_i32 s76, s73, 12288\n" \
  "s_add_u32 s76, s76, 86016\n" \
  "s_add_u32 s75, s75, 786432\n" \
  "v_add_u32 v27, 43008, v7\n" \
  "v_add_u32 v28, 43008, v26\n" \
  "v_mov_b32 v0, 1\n" \
  "s_waitcnt vmcnt(0)\n" \
  "s_barrier\n" \
  "ds_read_b128 v[32:35], v20 offset:0\n" \
  "ds_read_b128 v[56:59], v20 offset:768\n" \
  "ds_read_b128 v[80:83], v20 offset:12288\n" \
  "ds_read_b128 v[104:107], v20 offset:13056\n" \
  "ds_read_b64_tr_b16 v[128:129], v7 offset:0\n" \
  "ds_read_b64_tr_b16 v[130:131], v7 offset:1792\n" \
  "ds_read_b128 v[36:39], v21 offset:0\n" \
  "ds_read_b128 v[60:63], v21 offset:768\n" \
  "ds_read_b128 v[84:87], v21 offset:12288\n" \
  "ds_read_b128 v[108:111], v21 offset:13056\n" \
  "ds_read_b64_tr_b16 v[132:133], v7 offset:14336\n" \
  "ds_read_b64_tr_b16 v[134:135], v7 offset:16128\n" \
  "s_waitcnt lgkmcnt(2)\n" \
  "ds_read_b128 v[40:43], v20 offset:128\n" \
  "ds_read_b128 v[64:67], v20 offset:896\n" \
  "ds_read_b128 v[88:91], v20 offset:12416\n" \
  "ds_read_b128 v[112:115], v20 offset:13184\n" \
  "ds_read_b64_tr_b16 v[136:137], v7 offset:28672\n" \
  "ds_read_b64_tr_b16 v[138:139], v7 offset:30464\n" \
  "ds_read_b128 v[44:47], v21 offset:128\n" \
  "ds_read_b128 v[68:71], v21 offset:896\n" \
  "ds_read_b128 v[92:95], v21 offset:12416\n" \
  "ds_read_b128 v[116:119], v21 offset:13184\n" \
  "ds_read_b64_tr_b16 v[140:141], v27 offset:0\n" \
  "ds_read_b64_tr_b16 v[142:143], v27 offset:1792\n" \
  "s_waitcnt lgkmcnt(2)\n" \
  "ds_read_b128 v[48:51], v20 offset:256\n" \
  "ds_read_b128 v[72:75], v20 offset:1024\n" \
  "ds_read_b128 v[96:99], v20 offset:12544\n" \
  "ds_read_b128 v[120:123], v20 offset:13312\n" \
  "ds_read_b64_tr_b16 v[144:145], v27 offset:14336\n" \
  "ds_read_b64_tr_b16 v[146:147], v27 offset:16128\n" \
  "ds_read_b128 v[52:55], v21 offset:256\n" \
  "ds_read_b128 v[76:79], v21 offset:1024\n" \
  "ds_read_b128 v[100:103], v21 offset:12544\n" \
  "ds_read_b128 v[124:127], v21 offset:13312\n" \
  "s_waitcnt lgkmcnt(2)\n" \
  "s_mov_b32 s71, 0x40000000\n" \
  "s_mov_b32 s72, 0x40000000\n" \
  "s_waitcnt lgkmcnt(0)\n" \
  "LUR_%=:\n" \
  "v_mfma_f32_16x16x32_bf16 v[152:155], v[32:35], v[128:131], 0\n" \
  "ds_read_b64_tr_b16 v[148:149], v27 offset:28672\n" \
  "v_mfma_f32_16x16x32_bf16 v[156:159], v[56:59], v[128:131], 0\n" \
  "s_waitcnt lgkmcnt(8)\n" \
  "ds_read_b64_tr_b16 v[150:151], v27 offset:30464\n" \
  "v_mfma_f32_16x16x32_bf16 v[160:163], v[80:83], v[128:131], 0\n" \
  "v_cvt_pk_bf16_f32 v200, v184, v185\n" \
  "v_cvt_pk_bf16_f32 v201, v186, v187\n" \
  "v_mfma_f32_16x16x32_bf16 v[164:167], v[104:107], v[128:131], 0\n" \
  "v_cvt_pk_bf16_f32 v202, v188, v189\n" \
  "v_cvt_pk_bf16_f32 v203, v190, v191\n" \
  "v_mfma_f32_16x16x32_bf16 v[152:155], v[36:39], v[132:135], v[152:155]\n" \
  "ds_read_b64_tr_b16 v[128:129], v26 offset:0\n" \
  "v_mfma_f32_16x16x32_bf16 v[156:159], v[60:63], v[132:135], v[156:159]\n" \
  "ds_read_b64_tr_b16 v[130:131], v26 offset:1792\n" \
  "v_mfma_f32_16x16x32_bf16 v[160:163], v[84:87], v[132:135], v[160:163]\n" \
  "s_add_u32 s73, s71, 393216\n" \
  "v_mfma_f32_16x16x32_bf16 v[164:167], v[108:111], v[132:135], v[164:167]\n" \
  "buffer_store_dwordx4 v[200:203], v22, s[36:39], s73 offen sc1\n" \
  "v_mfma_f32_16x16x32_bf16 v[152:155], v[40:43], v[136:139], v[152:155]\n" \
  "ds_read_b64_tr_b16 v[132:133], v26 offset:14336\n" \
  "v_mfma_f32_16x16x32_bf16 v[156:159], v[64:67], v[136:139], v[156:159]\n" \
  "ds_read_b64_tr_b16 v[134:135], v26 offset:16128\n" \
  "v_mfma_f32_16x16x32_bf16 v[160:163], v[88:91], v[136:139], v[160:163]\n" \
  "v_cvt_pk_bf16_f32 v204, v192, v193\n" \
  "v_cvt_pk_bf16_f32 v205, v194, v195\n" \
  "v_mfma_f32_16x16x32_bf16 v[164:167], v[112:115], v[136:139], v[164:167]\n" \
  "v_cvt_pk_bf16_f32 v206, v196, v197\n" \
  "v_cvt_pk_bf16_f32 v207, v198, v199\n" \
  "v_mfma_f32_16x16x32_bf16 v[152:155], v[44:47], v[140:143], v[152:155]\n" \
  "ds_read_b64_tr_b16 v[136:137], v26 offset:28672\n" \
  "v_mfma_f32_16x16x32_bf16 v[156:159], v[68:71], v[140:143], v[156:159]\n" \
  "ds_read_b64_tr_b16 v[138:139], v26 offset:30464\n" \
  "v_mfma_f32_16x16x32_bf16 v[160:163], v[92:95], v[140:143], v[160:163]\n" \
  "s_add_u32 s73, s72, 393216\n" \
  "v_mfma_f32_16x16x32_bf16 v[164:167], v[116:119], v[140:143], v[164:167]\n" \
  "buffer_store_dwordx4 v[204:207], v22, s[36:39], s73 offen sc1\n" \
  "s_waitcnt lgkmcnt(10)\n" \
  "v_mfma_f32_16x16x32_bf16 v[152:155], v[48:51], v[144:147], v[152:155]\n" \
  "ds_read_b64_tr_b16 v[140:141], v28 offset:0\n" \
  "v_mfma_f32_16x16x32_bf16 v[156:159], v[72:75], v[144:147], v[156:159]\n" \
  "ds_read_b64_tr_b16 v[142:143], v28 offset:1792\n" \
  "v_mfma_f32_16x16x32_bf16 v[160:163], v[96:99], v[144:147], v[160:163]\n" \
  "v_readlane_b32 s69, v23, s68\n" \
  "v_readlane_b32 s71, v24, s68\n" \
  "s_add_u32 s68, s68, 1\n" \
  "v_mfma_f32_16x16x32_bf16 v[164:167], v[120:123], v[144:147], v[164:167]\n" \
  "v_readlane_b32 s70, v23, s68\n" \
  "v_readlane_b32 s72, v24, s68\n" \
  "s_add_u32 s68, s68, 1\n" \
  "s_waitcnt lgkmcnt(8)\n" \
  "v_mfma_f32_16x16x32_bf16 v[152:155], v[52:55], v[148:151], v[152:155]\n" \
  "ds_read_b64_tr_b16 v[144:145], v28 offset:14336\n" \
  "v_mfma_f32_16x16x32_bf16 v[156:159], v[76:79], v[148:151], v[156:159]\n" \
  "ds_read_b64_tr_b16 v[146:147], v28 offset:16128\n" \
  "v_mfma_f32_16x16x32_bf16 v[160:163], v[100:103], v[148:151], v[160:163]\n" \
  "s_cmp_eq_u32 s68, s77\n" \
  "s_cselect_b32 s75, 0x40000000, s75\n" \
  "v_mfma_f32_16x16x32_bf16 v[164:167], v[124:127], v[148:151], v[164:167]\n" \
  "s_barrier\n" \
  "s_waitcnt lgkmcnt(8)\n" \
  "v_mfma_f32_16x16x32_bf16 v[168:171], v[32:35], v[128:131], 0\n" \
  "ds_read_b64_tr_b16 v[148:149], v28 offset:28672\n" \
  "v_mfma_f32_16x16x32_bf16 v[172:175], v[56:59], v[128:131], 0\n" \
  "ds_read_b64_tr_b16 v[150:151], v28 offset:30464\n" \
  "v_mfma_f32_16x16x32_bf16 v[176:179], v[80:83], v[128:131], 0\n" \
  "v_cvt_pk_bf16_f32 v200, v152, v153\n" \
  "v_cvt_pk_bf16_f32 v201, v154, v155\n" \
  "v_mfma_f32_16x16x32_bf16 v[180:183], v[104:107], v[128:131], 0\n" \
  "s_add_u32 m0, s76, 0\n" \
  "s_nop 0\n" \
  "buffer_load_dwordx4 v8, s[40:43], s75 offen offset:0 lds\n" \
  "s_waitcnt lgkmcnt(8)\n" \
  "v_mfma_f32_16x16x32_bf16 v[168:171], v[36:39], v[132:135], v[168:171]\n" \
  "ds_read_b64_tr_b16 v[128:129], v7 offset:64\n" \
  "v_mfma_f32_16x16x32_bf16 v[172:175], v[60:63], v[132:135], v[172:175]\n" \
  "ds_read_b64_tr_b16 v[130:131], v7 offset:1856\n" \
  "v_mfma_f32_16x16x32_bf16 v[176:179], v[84:87], v[132:135], v[176:179]\n" \
  "v_cvt_pk_bf16_f32 v202, v156, v157\n" \
  "v_cvt_pk_bf16_f32 v203, v158, v159\n" \
  "v_mfma_f32_16x16x32_bf16 v[180:183], v[108:111], v[132:135], v[180:183]\n" \
  "s_add_u32 s73, s69, 0\n" \
  "s_waitcnt lgkmcnt(8)\n" \
  "v_mfma_f32_16x16x32_bf16 v[168:171], v[40:43], v[136:139], v[168:171]\n" \
  "ds_read_b64_tr_b16 v[132:133], v7 offset:14400\n" \
  "v_mfma_f32_16x16x32_bf16 v[172:175], v[64:67], v[136:139], v[172:175]\n" \
  "ds_read_b64_tr_b16 v[134:135], v7 offset:16192\n" \
  "v_mfma_f32_16x16x32_bf16 v[176:179], v[88:91], v[136:139], v[176:179]\n" \
  "buffer_load_dwordx4 v9, s[40:43], s75 offen offset:1024 lds\n" \
  "v_mfma_f32_16x16x32_bf16 v[180:183], v[112:115], v[136:139], v[180:183]\n" \
  "buffer_store_dwordx4 v[200:203], v22, s[36:39], s73 offen sc1\n" \
  "s_waitcnt lgkmcnt(8)\n" \
  "v_mfma_f32_16x16x32_bf16 v[168:171], v[44:47], v[140:143], v[168:171]\n" \
  "ds_read_b64_tr_b16 v[136:137], v7 offset:28736\n" \
  "v_mfma_f32_16x16x32_bf16 v[172:175], v[68:71], v[140:143], v[172:175]\n" \
  "ds_read_b64_tr_b16 v[138:139], v7 offset:30528\n" \
  "v_mfma_f32_16x16x32_bf16 v[176:179], v[92:95], v[140:143], v[176:179]\n" \
  "v_cvt_pk_bf16_f32 v204, v160, v161\n" \
  "v_cvt_pk_bf16_f32 v205, v162, v163\n" \
  "v_mfma_f32_16x16x32_bf16 v[180:183], v[116:119], v[140:143], v[180:183]\n" \
  "buffer_load_dwordx4 v10, s[40:43], s75 offen offset:2048 lds\n" \
  "s_waitcnt lgkmcnt(8)\n" \
  "v_mfma_f32_16x16x32_bf16 v[168:171], v[48:51], v[144:147], v[168:171]\n" \
  "ds_read_b64_tr_b16 v[140:141], v27 offset:64\n" \
  "v_mfma_f32_16x16x32_bf16 v[172:175], v[72:75], v[144:147], v[172:175]\n" \
  "ds_read_b64_tr_b16 v[142:143], v27 offset:1856\n" \
  "v_mfma_f32_16x16x32_bf16 v[176:179], v[96:99], v[144:147], v[176:179]\n" \
  "v_cvt_pk_bf16_f32 v206, v164, v165\n" \
  "v_cvt_pk_bf16_f32 v207, v166, v167\n" \
  "v_mfma_f32_16x16x32_bf16 v[180:183], v[120:123], v[144:147], v[180:183]\n" \
  "s_add_u32 s73, s70, 0\n" \
  "s_waitcnt lgkmcnt(8)\n" \
  "v_mfma_f32_16x16x32_bf16 v[168:171], v[52:55], v[148:151], v[168:171]\n" \
  "ds_read_b64_tr_b16 v[144:145], v27 offset:14400\n" \
  "v_mfma_f32_16x16x32_bf16 v[172:175], v[76:79], v[148:151], v[172:175]\n" \
  "ds_read_b64_tr_b16 v[146:147], v27 offset:16192\n" \
  "v_mfma_f32_16x16x32_bf16 v[176:179], v[100:103], v[148:151], v[176:179]\n" \
  "buffer_load_dwordx4 v11, s[40:43], s75 offen offset:3072 lds\n" \
  "v_mfma_f32_16x16x32_bf16 v[180:183], v[124:127], v[148:151], v[180:183]\n" \
  "buffer_store_dwordx4 v[204:207], v22, s[36:39], s73 offen sc1\n" \
  "s_waitcnt lgkmcnt(8)\n" \
  "v_mfma_f32_16x16x32_bf16 v[152:155], v[32:35], v[128:131], 0\n" \
  "ds_read_b64_tr_b16 v[148:149], v27 offset:28736\n" \
  "v_mfma_f32_16x16x32_bf16 v[156:159], v[56:59], v[128:131], 0\n" \
  "ds_read_b64_tr_b16 v[150:151], v27 offset:30528\n" \
  "v_mfma_f32_16x16x32_bf16 v[160:163], v[80:83], v[128:131], 0\n" \
  "v_cvt_pk_bf16_f32 v200, v168, v169\n" \
  "v_cvt_pk_bf16_f32 v201, v170, v171\n" \
  "v_mfma_f32_16x16x32_bf16 v[164:167], v[104:107], v[128:131], 0\n" \
  "s_add_u32 m0, s76, 4096\n" \
  "s_nop 0\n" \
  "buffer_load_dwordx4 v12, s[40:43], s75 offen offset:0 lds\n" \
  "s_waitcnt lgkmcnt(8)\n" \
  "v_mfma_f32_16x16x32_bf16 v[152:155], v[36:39], v[132:135], v[152:155]\n" \
  "ds_read_b64_tr_b16 v[128:129], v26 offset:64\n" \
  "v_mfma_f32_16x16x32_bf16 v[156:159], v[60:63], v[132:135], v[156:159]\n" \
  "ds_read_b64_tr_b16 v[130:131], v26 offset:1856\n" \
  "v_mfma_f32_16x16x32_bf16 v[160:163], v[84:87], v[132:135], v[160:163]\n" \
  "v_cvt_pk_bf16_f32 v202, v172, v173\n" \
  "v_cvt_pk_bf16_f32 v203, v174, v175\n" \
  "v_mfma_f32_16x16x32_bf16 v[164:167], v[108:111], v[132:135], v[164:167]\n" \
  "s_add_u32 s73, s69, 65536\n" \
  "s_waitcnt lgkmcnt(8)\n" \
  "v_mfma_f32_16x16x32_bf16 v[152:155], v[40:43], v[136:139], v[152:155]\n" \
  "ds_read_b64_tr_b16 v[132:133], v26 offset:14400\n" \
  "v_mfma_f32_16x16x32_bf16 v[156:159], v[64:67], v[136:139], v[156:159]\n" \
  "ds_read_b64_tr_b16 v[134:135], v26 offset:16192\n" \
  "v_mfma_f32_16x16x32_bf16 v[160:163], v[88:91], v[136:139], v[160:163]\n" \
  "buffer_load_dwordx4 v13, s[40:43], s75 offen offset:1024 lds\n" \
  "v_mfma_f32_16x16x32_bf16 v[164:167], v[112:115], v[136:139], v[164:167]\n" \
  "buffer_store_dwordx4 v[200:203], v22, s[36:39], s73 offen sc1\n" \
  "s_waitcnt lgkmcnt(8)\n" \
  "v_mfma_f32_16x16x32_bf16 v[152:155], v[44:47], v[140:143], v[152:155]\n" \
  "ds_read_b64_tr_b16 v[136:137], v26 offset:28736\n" \
  "v_mfma_f32_16x16x32_bf16 v[156:159], v[68:71], v[140:143], v[156:159]\n" \
  "ds_read_b64_tr_b16 v[138:139], v26 offset:30528\n" \
  "v_mfma_f32_16x16x32_bf16 v[160:163], v[92:95], v[140:143], v[160:163]\n" \
  "v_cvt_pk_bf16_f32 v204, v176, v177\n" \
  "v_cvt_pk_bf16_f32 v205, v178, v179\n" \
  "v_mfma_f32_16x16x32_bf16 v[164:167], v[116:119], v[140:143], v[164:167]\n" \
  "buffer_load_dwordx4 v14, s[40:43], s75 offen offset:2048 lds\n" \
  "s_waitcnt lgkmcnt(8)\n" \
  "v_mfma_f32_16x16x32_bf16 v[152:155], v[48:51], v[144:147], v[152:155]\n" \
  "ds_read_b64_tr_b16 v[140:141], v28 offset:64\n" \
  "v_mfma_f32_16x16x32_bf16 v[156:159], v[72:75], v[144:147], v[156:159]\n" \
  "ds_read_b64_tr_b16 v[142:143], v28 offset:1856\n" \
  "v_mfma_f32_16x16x32_bf16 v[160:163], v[96:99], v[144:147], v[160:163]\n" \
  "v_cvt_pk_bf16_f32 v206, v180, v181\n" \
  "v_cvt_pk_bf16_f32 v207, v182, v183\n" \
  "v_mfma_f32_16x16x32_bf16 v[164:167], v[120:123], v[144:147], v[164:167]\n" \
  "s_add_u32 s73, s70, 65536\n" \
  "s_waitcnt lgkmcnt(8)\n" \
  "v_mfma_f32_16x16x32_bf16 v[152:155], v[52:55], v[148:151], v[152:155]\n" \
  "ds_read_b64_tr_b16 v[144:145], v28 offset:14400\n" \
  "v_mfma_f32_16x16x32_bf16 v[156:159], v[76:79], v[148:151], v[156:159]\n" \
  "ds_read_b64_tr_b16 v[146:147], v28 offset:16192\n" \
  "v_mfma_f32_16x16x32_bf16 v[160:163], v[100:103], v[148:151], v[160:163]\n" \
  "buffer_load_dwordx4 v15, s[40:43], s75 offen offset:3072 lds\n" \
  "v_mfma_f32_16x16x32_bf16 v[164:167], v[124:127], v[148:151], v[164:167]\n" \
  "buffer_store_dwordx4 v[204:207], v22, s[36:39], s73 offen sc1\n" \
  "s_waitcnt lgkmcnt(8)\n" \
  "v_mfma_f32_16x16x32_bf16 v[168:171], v[32:35], v[128:131], 0\n" \
  "ds_read_b64_tr_b16 v[148:149], v28 offset:28736\n" \
  "v_mfma_f32_16x16x32_bf16 v[172:175], v[56:59], v[128:131], 0\n" \
  "ds_read_b64_tr_b16 v[150:151], v28 offset:30528\n" \
  "v_mfma_f32_16x16x32_bf16 v[176:179], v[80:83], v[128:131], 0\n" \
  "v_cvt_pk_bf16_f32 v200, v152, v153\n" \
  "v_cvt_pk_bf16_f32 v201, v154, v155\n" \
  "v_mfma_f32_16x16x32_bf16 v[180:183], v[104:107], v[128:131], 0\n" \
  "s_add_u32 m0, s76, 8192\n" \
  "s_nop 0\n" \
  "buffer_load_dwordx4 v16, s[40:43], s75 offen offset:0 lds\n" \
  "s_waitcnt lgkmcnt(8)\n" \
  "v_mfma_f32_16x16x32_bf16 v[168:171], v[36:39], v[132:135], v[168:171]\n" \
  "ds_read_b64_tr_b16 v[128:129], v7 offset:128\n" \
  "v_mfma_f32_16x16x32_bf16 v[172:175], v[60:63], v[132:135], v[172:175]\n" \
  "ds_read_b64_tr_b16 v[130:131], v7 offset:1920\n" \
  "v_mfma_f32_16x16x32_bf16 v[176:179], v[84:87], v[132:135], v[176:179]\n" \
  "v_cvt_pk_bf16_f32 v202, v156, v157\n" \
  "v_cvt_pk_bf16_f32 v203, v158, v159\n" \
  "v_mfma_f32_16x16x32_bf16 v[180:183], v[108:111], v[132:135], v[180:183]\n" \
  "s_add_u32 s73, s69, 131072\n" \
  "s_waitcnt lgkmcnt(8)\n" \
  "v_mfma_f32_16x16x32_bf16 v[168:171], v[40:43], v[136:139], v[168:171]\n" \
  "ds_read_b64_tr_b16 v[132:133], v7 offset:14464\n" \
  "v_mfma_f32_16x16x32_bf16 v[172:175], v[64:67], v[136:139], v[172:175]\n" \
  "ds_read_b64_tr_b16 v[134:135], v7 offset:16256\n" \
  "v_mfma_f32_16x16x32_bf16 v[176:179], v[88:91], v[136:139], v[176:179]\n" \
  "buffer_load_dwordx4 v17, s[40:43], s75 offen offset:1024 lds\n" \
  "v_mfma_f32_16x16x32_bf16 v[180:183], v[112:115], v[136:139], v[180:183]\n" \
  "buffer_store_dwordx4 v[200:203], v22, s[36:39], s73 offen sc1\n" \
  "s_waitcnt lgkmcnt(8)\n" \
  "v_mfma_f32_16x16x32_bf16 v[168:171], v[44:47], v[140:143], v[168:171]\n" \
  "ds_read_b64_tr_b16 v[136:137], v7 offset:28800\n" \
  "v_mfma_f32_16x16x32_bf16 v[172:175], v[68:71], v[140:143], v[172:175]\n" \
  "ds_read_b64_tr_b16 v[138:139], v7 offset:30592\n" \
  "v_mfma_f32_16x16x32_bf16 v[176:179], v[92:95], v[140:143], v[176:179]\n" \
  "v_cvt_pk_bf16_f32 v204, v160, v161\n" \
  "v_cvt_pk_bf16_f32 v205, v162, v163\n" \
  "v_mfma_f32_16x16x32_bf16 v[180:183], v[116:119], v[140:143], v[180:183]\n" \
  "buffer_load_dwordx4 v18, s[40:43], s75 offen offset:2048 lds\n" \
  "s_waitcnt lgkmcnt(8)\n" \
  "v_mfma_f32_16x16x32_bf16 v[168:171], v[48:51], v[144:147], v[168:171]\n" \
  "ds_read_b64_tr_b16 v[140:141], v27 offset:128\n" \
  "v_mfma_f32_16x16x32_bf16 v[172:175], v[72:75], v[144:147], v[172:175]\n" \
  "ds_read_b64_tr_b16 v[142:143], v27 offset:1920\n" \
  "v_mfma_f32_16x16x32_bf16 v[176:179], v[96:99], v[144:147], v[176:179]\n" \
  "v_cvt_pk_bf16_f32 v206, v164, v165\n" \
  "v_cvt_pk_bf16_f32 v207, v166, v167\n" \
  "v_mfma_f32_16x16x32_bf16 v[180:183], v[120:123], v[144:147], v[180:183]\n" \
  "s_add_u32 s73, s70, 131072\n" \
  "s_waitcnt lgkmcnt(8)\n" \
  "v_mfma_f32_16x16x32_bf16 v[168:171], v[52:55], v[148:151], v[168:171]\n" \
  "ds_read_b64_tr_b16 v[144:145], v27 offset:14464\n" \
  "v_mfma_f32_16x16x32_bf16 v[172:175], v[76:79], v[148:151], v[172:175]\n" \
  "ds_read_b64_tr_b16 v[146:147], v27 offset:16256\n" \
  "v_mfma_f32_16x16x32_bf16 v[176:179], v[100:103], v[148:151], v[176:179]\n" \
  "buffer_load_dwordx4 v19, s[40:43], s75 offen offset:3072 lds\n" \
  "s_add_u32 s75, s75, 786432\n" \
  "v_mfma_f32_16x16x32_bf16 v[180:183], v[124:127], v[148:151], v[180:183]\n" \
  "buffer_store_dwordx4 v[204:207], v22, s[36:39], s73 offen sc1\n" \
  "s_waitcnt lgkmcnt(8)\n" \
  "v_mfma_f32_16x16x32_bf16 v[152:155], v[32:35], v[128:131], 0\n" \
  "ds_read_b64_tr_b16 v[148:149], v27 offset:28800\n" \
  "v_mfma_f32_16x16x32_bf16 v[156:159], v[56:59], v[128:131], 0\n" \
  "ds_read_b64_tr_b16 v[150:151], v27 offset:30592\n" \
  "v_mfma_f32_16x16x32_bf16 v[160:163], v[80:83], v[128:131], 0\n" \
  "v_cvt_pk_bf16_f32 v200, v168, v169\n" \
  "v_cvt_pk_bf16_f32 v201, v170, v171\n" \
  "v_mfma_f32_16x16x32_bf16 v[164:167], v[104:107], v[128:131], 0\n" \
  "v_cvt_pk_bf16_f32 v202, v172, v173\n" \
  "v_cvt_pk_bf16_f32 v203, v174, v175\n" \
  "s_waitcnt lgkmcnt(8)\n" \
  "v_mfma_f32_16x16x32_bf16 v[152:155], v[36:39], v[132:135], v[152:155]\n" \
  "ds_read_b64_tr_b16 v[128:129], v26 offset:128\n" \
  "v_mfma_f32_16x16x32_bf16 v[156:159], v[60:63], v[132:135], v[156:159]\n" \
  "ds_read_b64_tr_b16 v[130:131], v26 offset:1920\n" \
  "v_mfma_f32_16x16x32_bf16 v[160:163], v[84:87], v[132:135], v[160:163]\n" \
  "s_add_u32 s73, s69, 196608\n" \
  "v_mfma_f32_16x16x32_bf16 v[164:167], v[108:111], v[132:135], v[164:167]\n" \
  "buffer_store_dwordx4 v[200:203], v22, s[36:39], s73 offen sc1\n" \
  "s_waitcnt lgkmcnt(8)\n" \
  "v_mfma_f32_16x16x32_bf16 v[152:155], v[40:43], v[136:139], v[152:155]\n" \
  "ds_read_b64_tr_b16 v[132:133], v26 offset:14464\n" \
  "v_mfma_f32_16x16x32_bf16 v[156:159], v[64:67], v[136:139], v[156:159]\n" \
  "ds_read_b64_tr_b16 v[134:135], v26 offset:16256\n" \
  "v_mfma_f32_16x16x32_bf16 v[160:163], v[88:91], v[136:139], v[160:163]\n" \
  "v_cvt_pk_bf16_f32 v204, v176, v177\n" \
  "v_cvt_pk_bf16_f32 v205, v178, v179\n" \
  "v_mfma_f32_16x16x32_bf16 v[164:167], v[112:115], v[136:139], v[164:167]\n" \
  "v_cvt_pk_bf16_f32 v206, v180, v181\n" \
  "v_cvt_pk_bf16_f32 v207, v182, v183\n" \
  "s_waitcnt lgkmcnt(8)\n" \
  "v_mfma_f32_16x16x32_bf16 v[152:155], v[44:47], v[140:143], v[152:155]\n" \
  "ds_read_b64_tr_b16 v[136:137], v26 offset:28800\n" \
  "v_mfma_f32_16x16x32_bf16 v[156:159], v[68:71], v[140:143], v[156:159]\n" \
  "ds_read_b64_tr_b16 v[138:139], v26 offset:30592\n" \
  "v_mfma_f32_16x16x32_bf16 v[160:163], v[92:95], v[140:143], v[160:163]\n" \
  "s_add_u32 s73, s70, 196608\n" \
  "v_mfma_f32_16x16x32_bf16 v[164:167], v[116:119], v[140:143], v[164:167]\n" \
  "buffer_store_dwordx4 v[204:207], v22, s[36:39], s73 offen sc1\n" \
  "s_waitcnt lgkmcnt(8)\n" \
  "v_mfma_f32_16x16x32_bf16 v[152:155], v[48:51], v[144:147], v[152:155]\n" \
  "ds_read_b64_tr_b16 v[140:141], v28 offset:128\n" \
  "v_mfma_f32_16x16x32_bf16 v[156:159], v[72:75], v[144:147], v[156:159]\n" \
  "ds_read_b64_tr_b16 v[142:143], v28 offset:1920\n" \
  "v_mfma_f32_16x16x32_bf16 v[160:163], v[96:99], v[144:147], v[160:163]\n" \
  "v_mfma_f32_16x16x32_bf16 v[164:167], v[120:123], v[144:147], v[164:167]\n" \
  "s_waitcnt lgkmcnt(8)\n" \
  "v_mfma_f32_16x16x32_bf16 v[152:155], v[52:55], v[148:151], v[152:155]\n" \
  "ds_read_b64_tr_b16 v[144:145], v28 offset:14464\n" \
  "v_mfma_f32_16x16x32_bf16 v[156:159], v[76:79], v[148:151], v[156:159]\n" \
  "ds_read_b64_tr_b16 v[146:147], v28 offset:16256\n" \
  "v_mfma_f32_16x16x32_bf16 v[160:163], v[100:103], v[148:151], v[160:163]\n" \
  "v_mfma_f32_16x16x32_bf16 v[164:167], v[124:127], v[148:151], v[164:167]\n" \
  "s_waitcnt lgkmcnt(8)\n" \
  "v_mfma_f32_16x16x32_bf16 v[168:171], v[32:35], v[128:131], 0\n" \
  "ds_read_b64_tr_b16 v[148:149], v28 offset:28800\n" \
  "v_mfma_f32_16x16x32_bf16 v[172:175], v[56:59], v[128:131], 0\n" \
  "ds_read_b64_tr_b16 v[150:151], v28 offset:30592\n" \
  "v_mfma_f32_16x16x32_bf16 v[176:179], v[80:83], v[128:131], 0\n" \
  "v_cvt_pk_bf16_f32 v200, v152, v153\n" \
  "v_cvt_pk_bf16_f32 v201, v154, v155\n" \
  "v_mfma_f32_16x16x32_bf16 v[180:183], v[104:107], v[128:131], 0\n" \
  "v_cvt_pk_bf16_f32 v202, v156, v157\n" \
  "v_cvt_pk_bf16_f32 v203, v158, v159\n" \
  "s_waitcnt lgkmcnt(8)\n" \
  "v_mfma_f32_16x16x32_bf16 v[168:171], v[36:39], v[132:135], v[168:171]\n" \
  "ds_read_b64_tr_b16 v[128:129], v7 offset:192\n" \
  "v_mfma_f32_16x16x32_bf16 v[172:175], v[60:63], v[132:135], v[172:175]\n" \
  "ds_read_b64_tr_b16 v[130:131], v7 offset:1984\n" \
  "v_mfma_f32_16x16x32_bf16 v[176:179], v[84:87], v[132:135], v[176:179]\n" \
  "s_add_u32 s73, s69, 262144\n" \
  "v_mfma_f32_16x16x32_bf16 v[180:183], v[108:111], v[132:135], v[180:183]\n" \
  "buffer_store_dwordx4 v[200:203], v22, s[36:39], s73 offen sc1\n" \
  "s_waitcnt lgkmcnt(8)\n" \
  "v_mfma_f32_16x16x32_bf16 v[168:171], v[40:43], v[136:139], v[168:171]\n" \
  "ds_read_b64_tr_b16 v[132:133], v7 offset:14528\n" \
  "v_mfma_f32_16x16x32_bf16 v[172:175], v[64:67], v[136:139], v[172:175]\n" \
  "ds_read_b64_tr_b16 v[134:135], v7 offset:16320\n" \
  "v_mfma_f32_16x16x32_bf16 v[176:179], v[88:91], v[136:139], v[176:179]\n" \
  "v_cvt_pk_bf16_f32 v204, v160, v161\n" \
  "v_cvt_pk_bf16_f32 v205, v162, v163\n" \
  "v_mfma_f32_16x16x32_bf16 v[180:183], v[112:115], v[136:139], v[180:183]\n" \
  "v_cvt_pk_bf16_f32 v206, v164, v165\n" \
  "v_cvt_pk_bf16_f32 v207, v166, v167\n" \
  "s_waitcnt lgkmcnt(8)\n" \
  "v_mfma_f32_16x16x32_bf16 v[168:171], v[44:47], v[140:143], v[168:171]\n" \
  "ds_read_b64_tr_b16 v[136:137], v7 offset:28864\n" \
  "v_mfma_f32_16x16x32_bf16 v[172:175], v[68:71], v[140:143], v[172:175]\n" \
  "ds_read_b64_tr_b16 v[138:139], v7 offset:30656\n" \
  "v_mfma_f32_16x16x32_bf16 v[176:179], v[92:95], v[140:143], v[176:179]\n" \
  "s_add_u32 s73, s70, 262144\n" \
  "v_mfma_f32_16x16x32_bf16 v[180:183], v[116:119], v[140:143], v[180:183]\n" \
  "buffer_store_dwordx4 v[204:207], v22, s[36:39], s73 offen sc1\n" \
  "s_waitcnt lgkmcnt(8)\n" \
  "v_mfma_f32_16x16x32_bf16 v[168:171], v[48:51], v[144:147], v[168:171]\n" \
  "ds_read_b64_tr_b16 v[140:141], v27 offset:192\n" \
  "v_mfma_f32_16x16x32_bf16 v[172:175], v[72:75], v[144:147], v[172:175]\n" \
  "ds_read_b64_tr_b16 v[142:143], v27 offset:1984\n" \
  "v_mfma_f32_16x16x32_bf16 v[176:179], v[96:99], v[144:147], v[176:179]\n" \
  "v_mfma_f32_16x16x32_bf16 v[180:183], v[120:123], v[144:147], v[180:183]\n" \
  "s_waitcnt lgkmcnt(8)\n" \
  "v_mfma_f32_16x16x32_bf16 v[168:171], v[52:55], v[148:151], v[168:171]\n" \
  "ds_read_b64_tr_b16 v[144:145], v27 offset:14528\n" \
  "v_mfma_f32_16x16x32_bf16 v[172:175], v[76:79], v[148:151], v[172:175]\n" \
  "ds_read_b64_tr_b16 v[146:147], v27 offset:16320\n" \
  "v_mfma_f32_16x16x32_bf16 v[176:179], v[100:103], v[148:151], v[176:179]\n" \
  "v_mfma_f32_16x16x32_bf16 v[180:183], v[124:127], v[148:151], v[180:183]\n" \
  "s_waitcnt vmcnt(5)\n" \
  "s_barrier\n" \
  "s_waitcnt lgkmcnt(8)\n" \
  "v_mfma_f32_16x16x32_bf16 v[184:187], v[32:35], v[128:131], 0\n" \
  "ds_read_b128 v[32:35], v20 offset:0\n" \
  "ds_read_b64_tr_b16 v[148:149], v27 offset:28864\n" \
  "v_mfma_f32_16x16x32_bf16 v[188:191], v[56:59], v[128:131], 0\n" \
  "ds_read_b128 v[56:59], v20 offset:768\n" \
  "ds_read_b64_tr_b16 v[150:151], v27 offset:30656\n" \
  "v_mfma_f32_16x16x32_bf16 v[192:195], v[80:83], v[128:131], 0\n" \
  "ds_read_b128 v[80:83], v20 offset:12288\n" \
  "v_cvt_pk_bf16_f32 v200, v168, v169\n" \
  "v_cvt_pk_bf16_f32 v201, v170, v171\n" \
  "v_mfma_f32_16x16x32_bf16 v[196:199], v[104:107], v[128:131], 0\n" \
  "ds_read_b128 v[104:107], v20 offset:13056\n" \
  "v_cvt_pk_bf16_f32 v202, v172, v173\n" \
  "v_cvt_pk_bf16_f32 v203, v174, v175\n" \
  "s_waitcnt lgkmcnt(12)\n" \
  "v_mfma_f32_16x16x32_bf16 v[184:187], v[36:39], v[132:135], v[184:187]\n" \
  "ds_read_b128 v[36:39], v21 offset:0\n" \
  "ds_read_b64_tr_b16 v[128:129], v7 offset:0\n" \
  "v_mfma_f32_16x16x32_bf16 v[188:191], v[60:63], v[132:135], v[188:191]\n" \
  "ds_read_b128 v[60:63], v21 offset:768\n" \
  "s_waitcnt lgkmcnt(8)\n" \
  "ds_read_b64_tr_b16 v[130:131], v7 offset:1792\n" \
  "v_mfma_f32_16x16x32_bf16 v[192:195], v[84:87], v[132:135], v[192:195]\n" \
  "ds_read_b128 v[84:87], v21 offset:12288\n" \
  "s_add_u32 s73, s69, 327680\n" \
  "v_mfma_f32_16x16x32_bf16 v[196:199], v[108:111], v[132:135], v[196:199]\n" \
  "ds_read_b128 v[108:111], v21 offset:13056\n" \
  "buffer_store_dwordx4 v[200:203], v22, s[36:39], s73 offen sc1\n" \
  "v_mfma_f32_16x16x32_bf16 v[184:187], v[40:43], v[136:139], v[184:187]\n" \
  "ds_read_b128 v[40:43], v20 offset:128\n" \
  "ds_read_b64_tr_b16 v[132:133], v7 offset:14336\n" \
  "v_mfma_f32_16x16x32_bf16 v[188:191], v[64:67], v[136:139], v[188:191]\n" \
  "ds_read_b128 v[64:67], v20 offset:896\n" \
  "ds_read_b64_tr_b16 v[134:135], v7 offset:16128\n" \
  "v_mfma_f32_16x16x32_bf16 v[192:195], v[88:91], v[136:139], v[192:195]\n" \
  "s_waitcnt lgkmcnt(8)\n" \
  "ds_read_b128 v[88:91], v20 offset:12416\n" \
  "v_cvt_pk_bf16_f32 v204, v176, v177\n" \
  "v_cvt_pk_bf16_f32 v205, v178, v179\n" \
  "v_mfma_f32_16x16x32_bf16 v[196:199], v[112:115], v[136:139], v[196:199]\n" \
  "ds_read_b128 v[112:115], v20 offset:13184\n" \
  "v_cvt_pk_bf16_f32 v206, v180, v181\n" \
  "v_cvt_pk_bf16_f32 v207, v182, v183\n" \
  "v_mfma_f32_16x16x32_bf16 v[184:187], v[44:47], v[140:143], v[184:187]\n" \
  "ds_read_b128 v[44:47], v21 offset:128\n" \
  "ds_read_b64_tr_b16 v[136:137], v7 offset:28672\n" \
  "v_mfma_f32_16x16x32_bf16 v[188:191], v[68:71], v[140:143], v[188:191]\n" \
  "ds_read_b128 v[68:71], v21 offset:896\n" \
  "ds_read_b64_tr_b16 v[138:139], v7 offset:30464\n" \
  "v_mfma_f32_16x16x32_bf16 v[192:195], v[92:95], v[140:143], v[192:195]\n" \
  "ds_read_b128 v[92:95], v21 offset:12416\n" \
  "s_add_u32 s73, s70, 327680\n" \
  "v_mfma_f32_16x16x32_bf16 v[196:199], v[116:119], v[140:143], v[196:199]\n" \
  "s_waitcnt lgkmcnt(8)\n" \
  "ds_read_b128 v[116:119], v21 offset:13184\n" \
  "buffer_store_dwordx4 v[204:207], v22, s[36:39], s73 offen sc1\n" \
  "v_mfma_f32_16x16x32_bf16 v[184:187], v[48:51], v[144:147], v[184:187]\n" \
  "ds_read_b128 v[48:51], v20 offset:256\n" \
  "ds_read_b64_tr_b16 v[140:141], v27 offset:0\n" \
  "v_mfma_f32_16x16x32_bf16 v[188:191], v[72:75], v[144:147], v[188:191]\n" \
  "ds_read_b128 v[72:75], v20 offset:1024\n" \
  "ds_read_b64_tr_b16 v[142:143], v27 offset:1792\n" \
  "v_mfma_f32_16x16x32_bf16 v[192:195], v[96:99], v[144:147], v[192:195]\n" \
  "ds_read_b128 v[96:99], v20 offset:12544\n" \
  "s_cmp_lg_u32 s68, s77\n" \
  "v_mfma_f32_16x16x32_bf16 v[196:199], v[120:123], v[144:147], v[196:199]\n" \
  "ds_read_b128 v[120:123], v20 offset:13312\n" \
  "v_mfma_f32_16x16x32_bf16 v[184:187], v[52:55], v[148:151], v[184:187]\n" \
  "s_waitcnt lgkmcnt(8)\n" \
  "ds_read_b128 v[52:55], v21 offset:256\n" \
  "ds_read_b64_tr_b16 v[144:145], v27 offset:14336\n" \
  "v_mfma_f32_16x16x32_bf16 v[188:191], v[76:79], v[148:151], v[188:191]\n" \
  "ds_read_b128 v[76:79], v21 offset:1024\n" \
  "ds_read_b64_tr_b16 v[146:147], v27 offset:16128\n" \
  "v_mfma_f32_16x16x32_bf16 v[192:195], v[100:103], v[148:151], v[192:195]\n" \
  "ds_read_b128 v[100:103], v21 offset:12544\n" \
  "v_mfma_f32_16x16x32_bf16 v[196:199], v[124:127], v[148:151], v[196:199]\n" \
  "ds_read_b128 v[124:127], v21 offset:13312\n" \
  "s_cbranch_scc1 LUR_%=\n" \
  "s_nop 7\n" \
  "v_cvt_pk_bf16_f32 v200, v184, v185\n" \
  "v_cvt_pk_bf16_f32 v201, v186, v187\n" \
  "v_cvt_pk_bf16_f32 v202, v188, v189\n" \
  "v_cvt_pk_bf16_f32 v203, v190, v191\n" \
  "s_add_u32 s73, s71, 393216\n" \
  "buffer_store_dwordx4 v[200:203], v22, s[36:39], s73 offen sc1\n" \
  "v_cvt_pk_bf16_f32 v204, v192, v193\n" \
  "v_cvt_pk_bf16_f32 v205, v194, v195\n" \
  "v_cvt_pk_bf16_f32 v206, v196, v197\n" \
  "v_cvt_pk_bf16_f32 v207, v198, v199\n" \
  "s_add_u32 s73, s72, 393216\n" \
  "buffer_store_dwordx4 v[204:207], v22, s[36:39], s73 offen sc1\n" \
  "s_waitcnt vmcnt(0) lgkmcnt(0)\n" \
  "buffer_atomic_add v0, v25, s[36:39], 0 offen sc1\n" \
  "s_barrier\n"
#define UF_CLOBBERS \
  "s68", "s69", "s70", "s71", "s72", "s73", "s74", "s75", "s76", "s77"
#define UF_ASM \
  "s_mul_i32 s74, s52, 21504\n" \
  "s_mul_i32 s73, s52, 61440\n" \
  "s_add_u32 s77, s73, 61440\n" \
  "LUW_%=:\n" \
  "s_add_u32 m0, s74, 0\n" \
  "s_nop 0\n" \
  "buffer_load_dwordx4 v0, s[44:47], s73 offen offset:0 lds\n" \
  "buffer_load_dwordx4 v1, s[44:47], s73 offen offset:1024 lds\n" \
  "buffer_load_dwordx4 v2, s[44:47], s73 offen offset:2048 lds\n" \
  "buffer_load_dwordx4 v3, s[44:47], s73 offen offset:3072 lds\n" \
  "s_add_u32 m0, s74, 4096\n" \
  "s_nop 0\n" \
  "buffer_load_dwordx4 v4, s[44:47], s73 offen offset:0 lds\n" \
  "buffer_load_dwordx4 v5, s[44:47], s73 offen offset:1024 lds\n" \
  "buffer_load_dwordx4 v6, s[44:47], s73 offen offset:2048 lds\n" \
  "s_add_u32 s74, s74, 7168\n" \
  "s_add_u32 s73, s73, 20480\n" \
  "s_cmp_lg_u32 s73, s77\n" \
  "s_cbranch_scc1 LUW_%=\n" \
  "s_and_b32 s73, s52, 1\n" \
  "s_lshr_b32 s74, s52, 1\n" \
  "s_mul_i32 s68, s73, 10\n" \
  "s_add_u32 s77, s68, 10\n" \
  "s_add_u32 s75, s68, s74\n" \
  "s_mul_i32 s75, s75, 393216\n" \
  "s_lshl_b32 s73, s73, 1\n" \
  "s_add_u32 s73, s73, s74\n" \
  "s_mul_i32 s76, s73, 12288\n" \
  "s_add_u32 s76, s76, 86016\n" \
  "s_add_u32 m0, s76, 0\n" \
  "s_nop 0\n" \
  "buffer_load_dwordx4 v8, s[40:43], s75 offen offset:0 lds\n" \
  "buffer_load_dwordx4 v9, s[40:43], s75 offen offset:1024 lds\n" \
  "buffer_load_dwordx4 v10, s[40:43], s75 offen offset:2048 lds\n" \
  "buffer_load_dwordx4 v11, s[40:43], s75 offen offset:3072 lds\n" \
  "s_add_u32 m0, s76, 4096\n" \
  "s_nop 0\n" \
  "buffer_load_dwordx4 v12, s[40:43], s75 offen offset:0 lds\n" \
  "buffer_load_dwordx4 v13, s[40:43], s75 offen offset:1024 lds\n" \
  "buffer_load_dwordx4 v14, s[40:43], s75 offen offset:2048 lds\n" \
  "buffer_load_dwordx4 v15, s[40:43], s75 offen offset:3072 lds\n" \
  "s_add_u32 m0, s76, 8192\n" \
  "s_nop 0\n" \
  "buffer_load_dwordx4 v16, s[40:43], s75 offen offset:0 lds\n" \
  "buffer_load_dwordx4 v17, s[40:43], s75 offen offset:1024 lds\n" \
  "buffer_load_dwordx4 v18, s[40:43], s75 offen offset:2048 lds\n" \
  "buffer_load_dwordx4 v19, s[40:43], s75 offen offset:3072 lds\n" \
  "s_add_u32 s75, s75, 786432\n"
#define TAKE0_CLOBBERS \
  "v2", "v3"
#define TAKE0_ASM \
  "v_add_u32 v2, 61440, v0\n" \
  "v_add_u32 v3, 61440, v1\n" \
  "s_cmp_eq_u32 s52, 3\n" \
  "s_cbranch_scc1 LTV_%=\n" \
  "ds_read_b64_tr_b16 a[0:1], v0 offset:0\n" \
  "ds_read_b64_tr_b16 a[2:3], v0 offset:1536\n" \
  "ds_read_b64_tr_b16 a[80:81], v0 offset:8\n" \
  "ds_read_b64_tr_b16 a[82:83], v0 offset:1544\n" \
  "ds_read_b64_tr_b16 a[160:161], v1 offset:0\n" \
  "ds_read_b64_tr_b16 a[162:163], v1 offset:1536\n" \
  "ds_read_b64_tr_b16 a[4:5], v0 offset:12288\n" \
  "ds_read_b64_tr_b16 a[6:7], v0 offset:13824\n" \
  "ds_read_b64_tr_b16 a[84:85], v0 offset:12296\n" \
  "ds_read_b64_tr_b16 a[86:87], v0 offset:13832\n" \
  "ds_read_b64_tr_b16 a[164:165], v1 offset:12288\n" \
  "ds_read_b64_tr_b16 a[166:167], v1 offset:13824\n" \
  "ds_read_b64_tr_b16 a[8:9], v0 offset:24576\n" \
  "ds_read_b64_tr_b16 a[10:11], v0 offset:26112\n" \
  "ds_read_b64_tr_b16 a[88:89], v0 offset:24584\n" \
  "s_waitcnt lgkmcnt(8)\n" \
  "ds_read_b64_tr_b16 a[90:91], v0 offset:26120\n" \
  "ds_read_b64_tr_b16 a[168:169], v1 offset:24576\n" \
  "ds_read_b64_tr_b16 a[170:171], v1 offset:26112\n" \
  "ds_read_b64_tr_b16 a[12:13], v0 offset:36864\n" \
  "ds_read_b64_tr_b16 a[14:15], v0 offset:38400\n" \
  "ds_read_b64_tr_b16 a[92:93], v0 offset:36872\n" \
  "ds_read_b64_tr_b16 a[94:95], v0 offset:38408\n" \
  "s_waitcnt lgkmcnt(8)\n" \
  "ds_read_b64_tr_b16 a[172:173], v1 offset:36864\n" \
  "ds_read_b64_tr_b16 a[174:175], v1 offset:38400\n" \
  "ds_read_b64_tr_b16 a[16:17], v0 offset:49152\n" \
  "ds_read_b64_tr_b16 a[18:19], v0 offset:50688\n" \
  "ds_read_b64_tr_b16 a[96:97], v0 offset:49160\n" \
  "ds_read_b64_tr_b16 a[98:99], v0 offset:50696\n" \
  "ds_read_b64_tr_b16 a[176:177], v1 offset:49152\n" \
  "s_waitcnt lgkmcnt(8)\n" \
  "ds_read_b64_tr_b16 a[178:179], v1 offset:50688\n" \
  "ds_read_b64_tr_b16 a[20:21], v2 offset:0\n" \
  "ds_read_b64_tr_b16 a[22:23], v2 offset:1536\n" \
  "ds_read_b64_tr_b16 a[100:101], v2 offset:8\n" \
  "ds_read_b64_tr_b16 a[102:103], v2 offset:1544\n" \
  "ds_read_b64_tr_b16 a[180:181], v3 offset:0\n" \
  "ds_read_b64_tr_b16 a[182:183], v3 offset:1536\n" \
  "s_waitcnt lgkmcnt(8)\n" \
  "ds_read_b64_tr_b16 a[24:25], v2 offset:12288\n" \
  "ds_read_b64_tr_b16 a[26:27], v2 offset:13824\n" \
  "ds_read_b64_tr_b16 a[104:105], v2 offset:12296\n" \
  "ds_read_b64_tr_b16 a[106:107], v2 offset:13832\n" \
  "ds_read_b64_tr_b16 a[184:185], v3 offset:12288\n" \
  "ds_read_b64_tr_b16 a[186:187], v3 offset:13824\n" \
  "ds_read_b64_tr_b16 a[28:29], v2 offset:24576\n" \
  "s_waitcnt lgkmcnt(8)\n" \
  "ds_read_b64_tr_b16 a[30:31], v2 offset:26112\n" \
  "ds_read_b64_tr_b16 a[108:109], v2 offset:24584\n" \
  "ds_read_b64_tr_b16 a[110:111], v2 offset:26120\n" \
  "ds_read_b64_tr_b16 a[188:189], v3 offset:24576\n" \
  "ds_read_b64_tr_b16 a[190:191], v3 offset:26112\n" \
  "ds_read_b64_tr_b16 a[32:33], v2 offset:36864\n" \
  "ds_read_b64_tr_b16 a[34:35], v2 offset:38400\n" \
  "s_waitcnt lgkmcnt(8)\n" \
  "ds_read_b64_tr_b16 a[112:113], v2 offset:36872\n" \
  "ds_read_b64_tr_b16 a[114:115], v2 offset:38408\n" \
  "ds_read_b64_tr_b16 a[192:193], v3 offset:36864\n" \
  "ds_read_b64_tr_b16 a[194:195], v3 offset:38400\n" \
  "ds_read_b64_tr_b16 a[36:37], v2 offset:49152\n" \
  "ds_read_b64_tr_b16 a[38:39], v2 offset:50688\n" \
  "ds_read_b64_tr_b16 a[116:117], v2 offset:49160\n" \
  "s_waitcnt lgkmcnt(8)\n" \
  "ds_read_b64_tr_b16 a[118:119], v2 offset:50696\n" \
  "ds_read_b64_tr_b16 a[196:197], v3 offset:49152\n" \
  "ds_read_b64_tr_b16 a[198:199], v3 offset:50688\n" \
  "s_branch LTE_%=\n" \
  "LTV_%=:\n" \
  "ds_read_b64_tr_b16 a[0:1], v0 offset:0\n" \
  "ds_read_b64_tr_b16 a[2:3], v0 offset:5120\n" \
  "ds_read_b64_tr_b16 a[16:17], v1 offset:0\n" \
  "ds_read_b64_tr_b16 a[24:25], v0 offset:32\n" \
  "ds_read_b64_tr_b16 a[26:27], v0 offset:5152\n" \
  "ds_read_b64_tr_b16 a[40:41], v1 offset:32\n" \
  "ds_read_b64_tr_b16 a[48:49], v0 offset:64\n" \
  "ds_read_b64_tr_b16 a[50:51], v0 offset:5184\n" \
  "ds_read_b64_tr_b16 a[64:65], v1 offset:64\n" \
  "ds_read_b64_tr_b16 a[72:73], v0 offset:96\n" \
  "ds_read_b64_tr_b16 a[74:75], v0 offset:5216\n" \
  "ds_read_b64_tr_b16 a[88:89], v1 offset:96\n" \
  "ds_read_b64_tr_b16 a[96:97], v0 offset:128\n" \
  "ds_read_b64_tr_b16 a[98:99], v0 offset:5248\n" \
  "ds_read_b64_tr_b16 a[112:113], v1 offset:128\n" \
  "s_waitcnt lgkmcnt(8)\n" \
  "ds_read_b64_tr_b16 a[120:121], v0 offset:160\n" \
  "ds_read_b64_tr_b16 a[122:123], v0 offset:5280\n" \
  "ds_read_b64_tr_b16 a[136:137], v1 offset:160\n" \
  "ds_read_b64_tr_b16 a[144:145], v0 offset:192\n" \
  "ds_read_b64_tr_b16 a[146:147], v0 offset:5312\n" \
  "ds_read_b64_tr_b16 a[160:161], v1 offset:192\n" \
  "ds_read_b64_tr_b16 a[168:169], v0 offset:224\n" \
  "s_waitcnt lgkmcnt(8)\n" \
  "ds_read_b64_tr_b16 a[170:171], v0 offset:5344\n" \
  "ds_read_b64_tr_b16 a[184:185], v1 offset:224\n" \
  "ds_read_b64_tr_b16 a[192:193], v0 offset:256\n" \
  "ds_read_b64_tr_b16 a[194:195], v0 offset:5376\n" \
  "ds_read_b64_tr_b16 a[208:209], v1 offset:256\n" \
  "ds_read_b64_tr_b16 a[216:217], v0 offset:288\n" \
  "ds_read_b64_tr_b16 a[218:219], v0 offset:5408\n" \
  "s_waitcnt lgkmcnt(8)\n" \
  "ds_read_b64_tr_b16 a[232:233], v1 offset:288\n" \
  "ds_read_b64_tr_b16 a[4:5], v2 offset:0\n" \
  "ds_read_b64_tr_b16 a[6:7], v2 offset:5120\n" \
  "ds_read_b64_tr_b16 a[18:19], v3 offset:0\n" \
  "ds_read_b64_tr_b16 a[28:29], v2 offset:32\n" \
  "ds_read_b64_tr_b16 a[30:31], v2 offset:5152\n" \
  "ds_read_b64_tr_b16 a[42:43], v3 offset:32\n" \
  "s_waitcnt lgkmcnt(8)\n" \
  "ds_read_b64_tr_b16 a[52:53], v2 offset:64\n" \
  "ds_read_b64_tr_b16 a[54:55], v2 offset:5184\n" \
  "ds_read_b64_tr_b16 a[66:67], v3 offset:64\n" \
  "ds_read_b64_tr_b16 a[76:77], v2 offset:96\n" \
  "ds_read_b64_tr_b16 a[78:79], v2 offset:5216\n" \
  "ds_read_b64_tr_b16 a[90:91], v3 offset:96\n" \
  "ds_read_b64_tr_b16 a[100:101], v2 offset:128\n" \
  "s_waitcnt lgkmcnt(8)\n" \
  "ds_read_b64_tr_b16 a[102:103], v2 offset:5248\n" \
  "ds_read_b64_tr_b16 a[114:115], v3 offset:128\n" \
  "ds_read_b64_tr_b16 a[124:125], v2 offset:160\n" \
  "ds_read_b64_tr_b16 a[126:127], v2 offset:5280\n" \
  "ds_read_b64_tr_b16 a[138:139], v3 offset:160\n" \
  "ds_read_b64_tr_b16 a[148:149], v2 offset:192\n" \
  "ds_read_b64_tr_b16 a[150:151], v2 offset:5312\n" \
  "s_waitcnt lgkmcnt(8)\n" \
  "ds_read_b64_tr_b16 a[162:163], v3 offset:192\n" \
  "ds_read_b64_tr_b16 a[172:173], v2 offset:224\n" \
  "ds_read_b64_tr_b16 a[174:175], v2 offset:5344\n" \
  "ds_read_b64_tr_b16 a[186:187], v3 offset:224\n" \
  "ds_read_b64_tr_b16 a[196:197], v2 offset:256\n" \
  "ds_read_b64_tr_b16 a[198:199], v2 offset:5376\n" \
  "ds_read_b64_tr_b16 a[210:211], v3 offset:256\n" \
  "s_waitcnt lgkmcnt(8)\n" \
  "ds_read_b64_tr_b16 a[220:221], v2 offset:288\n" \
  "ds_read_b64_tr_b16 a[222:223], v2 offset:5408\n" \
  "ds_read_b64_tr_b16 a[234:235], v3 offset:288\n" \
  "LTE_%=:\n" \
  "s_waitcnt lgkmcnt(0)\n"
#define TAKE1_CLOBBERS \
  "v2", "v3"
#define TAKE1_ASM \
  "v_add_u32 v2, 61440, v0\n" \
  "v_add_u32 v3, 61440, v1\n" \
  "s_cmp_eq_u32 s52, 3\n" \
  "s_cbranch_scc1 LTV_%=\n" \
  "ds_read_b64_tr_b16 a[40:41], v0 offset:0\n" \
  "ds_read_b64_tr_b16 a[42:43], v0 offset:1536\n" \
  "ds_read_b64_tr_b16 a[120:121], v0 offset:8\n" \
  "ds_read_b64_tr_b16 a[122:123], v0 offset:1544\n" \
  "ds_read_b64_tr_b16 a[200:201], v1 offset:0\n" \
  "ds_read_b64_tr_b16 a[202:203], v1 offset:1536\n" \
  "ds_read_b64_tr_b16 a[44:45], v0 offset:12288\n" \
  "ds_read_b64_tr_b16 a[46:47], v0 offset:13824\n" \
  "ds_read_b64_tr_b16 a[124:125], v0 offset:12296\n" \
  "ds_read_b64_tr_b16 a[126:127], v0 offset:13832\n" \
  "ds_read_b64_tr_b16 a[204:205], v1 offset:12288\n" \
  "ds_read_b64_tr_b16 a[206:207], v1 offset:13824\n" \
  "ds_read_b64_tr_b16 a[48:49], v0 offset:24576\n" \
  "ds_read_b64_tr_b16 a[50:51], v0 offset:26112\n" \
  "ds_read_b64_tr_b16 a[128:129], v0 offset:24584\n" \
  "s_waitcnt lgkmcnt(8)\n" \
  "ds_read_b64_tr_b16 a[130:131], v0 offset:26120\n" \
  "ds_read_b64_tr_b16 a[208:209], v1 offset:24576\n" \
  "ds_read_b64_tr_b16 a[210:211], v1 offset:26112\n" \
  "ds_read_b64_tr_b16 a[52:53], v0 offset:36864\n" \
  "ds_read_b64_tr_b16 a[54:55], v0 offset:38400\n" \
  "ds_read_b64_tr_b16 a[132:133], v0 offset:36872\n" \
  "ds_read_b64_tr_b16 a[134:135], v0 offset:38408\n" \
  "s_waitcnt lgkmcnt(8)\n" \
  "ds_read_b64_tr_b16 a[212:213], v1 offset:36864\n" \
  "ds_read_b64_tr_b16 a[214:215], v1 offset:38400\n" \
  "ds_read_b64_tr_b16 a[56:57], v0 offset:49152\n" \
  "ds_read_b64_tr_b16 a[58:59], v0 offset:50688\n" \
  "ds_read_b64_tr_b16 a[136:137], v0 offset:49160\n" \
  "ds_read_b64_tr_b16 a[138:139], v0 offset:50696\n" \
  "ds_read_b64_tr_b16 a[216:217], v1 offset:49152\n" \
  "s_waitcnt lgkmcnt(8)\n" \
  "ds_read_b64_tr_b16 a[218:219], v1 offset:50688\n" \
  "ds_read_b64_tr_b16 a[60:61], v2 offset:0\n" \
  "ds_read_b64_tr_b16 a[62:63], v2 offset:1536\n" \
  "ds_read_b64_tr_b16 a[140:141], v2 offset:8\n" \
  "ds_read_b64_tr_b16 a[142:143], v2 offset:1544\n" \
  "ds_read_b64_tr_b16 a[220:221], v3 offset:0\n" \
  "ds_read_b64_tr_b16 a[222:223], v3 offset:1536\n" \
  "s_waitcnt lgkmcnt(8)\n" \
  "ds_read_b64_tr_b16 a[64:65], v2 offset:12288\n" \
  "ds_read_b64_tr_b16 a[66:67], v2 offset:13824\n" \
  "ds_read_b64_tr_b16 a[144:145], v2 offset:12296\n" \
  "ds_read_b64_tr_b16 a[146:147], v2 offset:13832\n" \
  "ds_read_b64_tr_b16 a[224:225], v3 offset:12288\n" \
  "ds_read_b64_tr_b16 a[226:227], v3 offset:13824\n" \
  "ds_read_b64_tr_b16 a[68:69], v2 offset:24576\n" \
  "s_waitcnt lgkmcnt(8)\n" \
  "ds_read_b64_tr_b16 a[70:71], v2 offset:26112\n" \
  "ds_read_b64_tr_b16 a[148:149], v2 offset:24584\n" \
  "ds_read_b64_tr_b16 a[150:151], v2 offset:26120\n" \
  "ds_read_b64_tr_b16 a[228:229], v3 offset:24576\n" \
  "ds_read_b64_tr_b16 a[230:231], v3 offset:26112\n" \
  "ds_read_b64_tr_b16 a[72:73], v2 offset:36864\n" \
  "ds_read_b64_tr_b16 a[74:75], v2 offset:38400\n" \
  "s_waitcnt lgkmcnt(8)\n" \
  "ds_read_b64_tr_b16 a[152:153], v2 offset:36872\n" \
  "ds_read_b64_tr_b16 a[154:155], v2 offset:38408\n" \
  "ds_read_b64_tr_b16 a[232:233], v3 offset:36864\n" \
  "ds_read_b64_tr_b16 a[234:235], v3 offset:38400\n" \
  "ds_read_b64_tr_b16 a[76:77], v2 offset:49152\n" \
  "ds_read_b64_tr_b16 a[78:79], v2 offset:50688\n" \
  "ds_read_b64_tr_b16 a[156:157], v2 offset:49160\n" \
  "s_waitcnt lgkmcnt(8)\n" \
  "ds_read_b64_tr_b16 a[158:159], v2 offset:50696\n" \
  "ds_read_b64_tr_b16 a[236:237], v3 offset:49152\n" \
  "ds_read_b64_tr_b16 a[238:239], v3 offset:50688\n" \
  "s_branch LTE_%=\n" \
  "LTV_%=:\n" \
  "ds_read_b64_tr_b16 a[8:9], v0 offset:0\n" \
  "ds_read_b64_tr_b16 a[10:11], v0 offset:5120\n" \
  "ds_read_b64_tr_b16 a[20:21], v1 offset:0\n" \
  "ds_read_b64_tr_b16 a[32:33], v0 offset:32\n" \
  "ds_read_b64_tr_b16 a[34:35], v0 offset:5152\n" \
  "ds_read_b64_tr_b16 a[44:45], v1 offset:32\n" \
  "ds_read_b64_tr_b16 a[56:57], v0 offset:64\n" \
  "ds_read_b64_tr_b16 a[58:59], v0 offset:5184\n" \
  "ds_read_b64_tr_b16 a[68:69], v1 offset:64\n" \
  "ds_read_b64_tr_b16 a[80:81], v0 offset:96\n" \
  "ds_read_b64_tr_b16 a[82:83], v0 offset:5216\n" \
  "ds_read_b64_tr_b16 a[92:93], v1 offset:96\n" \
  "ds_read_b64_tr_b16 a[104:105], v0 offset:128\n" \
  "ds_read_b64_tr_b16 a[106:107], v0 offset:5248\n" \
  "ds_read_b64_tr_b16 a[116:117], v1 offset:128\n" \
  "s_waitcnt lgkmcnt(8)\n" \
  "ds_read_b64_tr_b16 a[128:129], v0 offset:160\n" \
  "ds_read_b64_tr_b16 a[130:131], v0 offset:5280\n" \
  "ds_read_b64_tr_b16 a[140:141], v1 offset:160\n" \
  "ds_read_b64_tr_b16 a[152:153], v0 offset:192\n" \
  "ds_read_b64_tr_b16 a[154:155], v0 offset:5312\n" \
  "ds_read_b64_tr_b16 a[164:165], v1 offset:192\n" \
  "ds_read_b64_tr_b16 a[176:177], v0 offset:224\n" \
  "s_waitcnt lgkmcnt(8)\n" \
  "ds_read_b64_tr_b16 a[178:179], v0 offset:5344\n" \
  "ds_read_b64_tr_b16 a[188:189], v1 offset:224\n" \
  "ds_read_b64_tr_b16 a[200:201], v0 offset:256\n" \
  "ds_read_b64_tr_b16 a[202:203], v0 offset:5376\n" \
  "ds_read_b64_tr_b16 a[212:213], v1 offset:256\n" \
  "ds_read_b64_tr_b16 a[224:225], v0 offset:288\n" \
  "ds_read_b64_tr_b16 a[226:227], v0 offset:5408\n" \
  "s_waitcnt lgkmcnt(8)\n" \
  "ds_read_b64_tr_b16 a[236:237], v1 offset:288\n" \
  "ds_read_b64_tr_b16 a[12:13], v2 offset:0\n" \
  "ds_read_b64_tr_b16 a[14:15], v2 offset:5120\n" \
  "ds_read_b64_tr_b16 a[22:23], v3 offset:0\n" \
  "ds_read_b64_tr_b16 a[36:37], v2 offset:32\n" \
  "ds_read_b64_tr_b16 a[38:39], v2 offset:5152\n" \
  "ds_read_b64_tr_b16 a[46:47], v3 offset:32\n" \
  "s_waitcnt lgkmcnt(8)\n" \
  "ds_read_b64_tr_b16 a[60:61], v2 offset:64\n" \
  "ds_read_b64_tr_b16 a[62:63], v2 offset:5184\n" \
  "ds_read_b64_tr_b16 a[70:71], v3 offset:64\n" \
  "ds_read_b64_tr_b16 a[84:85], v2 offset:96\n" \
  "ds_read_b64_tr_b16 a[86:87], v2 offset:5216\n" \
  "ds_read_b64_tr_b16 a[94:95], v3 offset:96\n" \
  "ds_read_b64_tr_b16 a[108:109], v2 offset:128\n" \
  "s_waitcnt lgkmcnt(8)\n" \
  "ds_read_b64_tr_b16 a[110:111], v2 offset:5248\n" \
  "ds_read_b64_tr_b16 a[118:119], v3 offset:128\n" \
  "ds_read_b64_tr_b16 a[132:133], v2 offset:160\n" \
  "ds_read_b64_tr_b16 a[134:135], v2 offset:5280\n" \
  "ds_read_b64_tr_b16 a[142:143], v3 offset:160\n" \
  "ds_read_b64_tr_b16 a[156:157], v2 offset:192\n" \
  "ds_read_b64_tr_b16 a[158:159], v2 offset:5312\n" \
  "s_waitcnt lgkmcnt(8)\n" \
  "ds_read_b64_tr_b16 a[166:167], v3 offset:192\n" \
  "ds_read_b64_tr_b16 a[180:181], v2 offset:224\n" \
  "ds_read_b64_tr_b16 a[182:183], v2 offset:5344\n" \
  "ds_read_b64_tr_b16 a[190:191], v3 offset:224\n" \
  "ds_read_b64_tr_b16 a[204:205], v2 offset:256\n" \
  "ds_read_b64_tr_b16 a[206:207], v2 offset:5376\n" \
  "ds_read_b64_tr_b16 a[214:215], v3 offset:256\n" \
  "s_waitcnt lgkmcnt(8)\n" \
  "ds_read_b64_tr_b16 a[228:229], v2 offset:288\n" \
  "ds_read_b64_tr_b16 a[230:231], v2 offset:5408\n" \
  "ds_read_b64_tr_b16 a[238:239], v3 offset:288\n" \
  "LTE_%=:\n" \
  "s_waitcnt lgkmcnt(0)\n"

__device__ __forceinline__ int fresh_lane() {
  int ln;
  asm volatile("v_mbcnt_lo_u32_b32 %0, -1, 0\n\tv_mbcnt_hi_u32_b32 %0, -1, %0" : "=v"(ln));
  __builtin_assume(ln >= 0 && ln < 64);
  return ln;
}
__device__ __forceinline__ unsigned now_ticks() { return (unsigned)__builtin_amdgcn_s_memrealtime(); }
template <int SLEEP>
__device__ __forceinline__ void wait_ge(gu32* ctrl, long word, int need) {
#pragma unroll 1
  for (int spin = 0;; ++spin) {
    const int x = (int)__hip_atomic_load(ctrl + word, RLX_AGENT);
    if (__builtin_amdgcn_ballot_w64(x < need) == 0) return;
    if (spin > SPIN_CAP || ((spin & 1023) == 1023 && rfl(__hip_atomic_load(ctrl + C_ERR, RLX_AGENT)))) { __hip_atomic_store(ctrl + C_ERR, 1u, RLX_AGENT); return; }
    __builtin_amdgcn_s_sleep(SLEEP);
  }
}
template <typename VT>
__device__ __forceinline__ void set_rsrc(VT& sr, int i, const void* ptr, unsigned bytes) {
  const unsigned long long a = uni64((unsigned long long)ptr);
  sr[4 * i] = (unsigned)a; sr[4 * i + 1] = (unsigned)(a >> 32) & 0xffffu; sr[4 * i + 2] = bytes; sr[4 * i + 3] = 0x00020000u;
}
__device__ __forceinline__ int side_a(const Params& p, int blk) {
  const unsigned w = (p.m0 & -(unsigned)(blk < 32)) | (p.m1 & -(unsigned)(blk >> 5 == 1)) | (p.m2 & -(unsigned)(blk >= 64));
  return (blk << 21) | (int)((w >> (blk & 31) & 1) << 13);
}
__device__ __forceinline__ int hand_off(const Params& p, int s) { return side_a(p, HB + s % HRING / 6) + (s % 6) * 2 * H_STEP; }
__device__ __forceinline__ int pt_off(const Params& p, int s) { return side_a(p, PB + s / 12) + (s % 12) * 2 * PT_SEG; }
__device__ __forceinline__ int z_off(const Params& p, int s) { return (side_a(p, slot_blk(s) + 1) ^ 8192) + s / RING * 655360; }
template <typename VT> __device__ __forceinline__ void pin(VT& v) {
#pragma unroll
  for (unsigned i = 0; i < __builtin_vectorelements(VT); ++i) { unsigned r = rfl(v[i]); asm volatile("" : "+s"(r)); v[i] = r; }
}
__device__ __forceinline__ void pause(unsigned ticks) {
  const unsigned t0 = now_ticks();
#pragma unroll 1
  while (now_ticks() - t0 < ticks) __builtin_amdgcn_s_sleep(8);
}
#define OPAQUE(v) asm volatile("" : "=v"(v))
__device__ __forceinline__ const __attribute__((address_space(4))) Params* fresh_params() {
  const __attribute__((address_space(4))) Params* pk = (const __attribute__((address_space(4))) Params*)__builtin_amdgcn_kernarg_segment_ptr();
  asm volatile("" : "+s"(pk) :: "memory");
  return pk;
}

__device__ __forceinline__ v4u ld16s(rsrc_t r, int vo, int so) { return __builtin_amdgcn_raw_buffer_load_b128(r, vo, so, 16); }
__device__ __forceinline__ v2u ld8s(rsrc_t r, int vo, int so) { return __builtin_amdgcn_raw_buffer_load_b64(r, vo, so, 16); }
#define TS(cond, i)
#define W240_IN(w) "{a[0:31]}"(w[0]), "{a[32:63]}"(w[1]), "{a[64:95]}"(w[2]), "{a[96:127]}"(w[3]), "{a[128:159]}"(w[4]), "{a[160:191]}"(w[5]), \
                   "{a[192:223]}"(w[6]), "{a[224:239]}"(w##7)
#define W240_IO(m, w) m "{a[0:31]}"(w[0]), m "{a[32:63]}"(w[1]), m "{a[64:95]}"(w[2]), m "{a[96:127]}"(w[3]), m "{a[128:159]}"(w[4]), m "{a[160:191]}"(w[5]), \
                      m "{a[192:223]}"(w[6]), m "{a[224:239]}"(w##7)
constexpr int L_MISC = 122880;
#define FFN_WAIT 100
typedef unsigned v3u __attribute__((ext_vector_type(3)));
__device__ __forceinline__ Params load_params(v3u par) {
  const __attribute__((address_space(4))) Params* k = fresh_params();
  Params p;
  p.x = k->x; p.mem0 = k->mem0; p.wq = k->wq; p.wk = k->wk; p.wv = k->wv; p.wo = k->wo; p.wr = k->wr; p.wg = k->wg; p.wu = k->wu; p.wd = k->wd; p.c = k->c; p.wmod = k->wmod;
  p.out = k->out; p.ws = k->ws; p.dE = k->dE; p.dL = k->dL; p.dM = k->dM; p.dZ = k->dZ; p.dS = k->dS; p.lead = k->lead;
  p.m0 = par[0]; p.m1 = par[1]; p.m2 = par[2]; p.co = side_a(p, LB) ^ 8192;
  return p;
}
__device__ __forceinline__ int look_off(int lane) { return lmap(H_PLAN) + (lane & 3) * 4; }
__device__ __forceinline__ int flag_off(const Params& p, int h) { return side_a(p, HB) + lmap(H_FLAG + h * 128); }

#define FPACE 12
#define SEL_AT 61
__device__ __forceinline__ unsigned wave_max_u32(unsigned m) {
  m = max(m, (unsigned)__builtin_amdgcn_update_dpp(0, (int)m, 0xb1, 0xf, 0xf, true)); m = max(m, (unsigned)__builtin_amdgcn_update_dpp(0, (int)m, 0x4e, 0xf, 0xf, true));
  m = max(m, (unsigned)__builtin_amdgcn_update_dpp(0, (int)m, 0x124, 0xf, 0xf, true)); m = max(m, ror8u(m));
  auto a = __builtin_amdgcn_permlane16_swap(m, m, false, false); m = max(a[0], a[1]);
  auto b = __builtin_amdgcn_permlane32_swap(m, m, false, false); return max(b[0], b[1]);
}
__device__ __forceinline__ unsigned score_key(unsigned r, int row) {
  unsigned u = f2u(u2f(r) + 0.f);
  u = (u & 0x80000000u) ? ~u : (u | 0x80000000u);
  return (u & 0xffff0000u) | (unsigned)(S - 1 - row);
}
template <int N>
__device__ __forceinline__ unsigned top8(const unsigned* key, int lane) {
  unsigned mine = 0, bound = 0;
#pragma unroll 1
  for (int it = 0; it < TOPK; ++it) {
    unsigned m = 0;
#pragma unroll
    for (int i = 0; i < N; ++i) m = max(m, key[i] - bound);
    bound += wave_max_u32(m);
    if ((lane & 7) == it) mine = bound;
  }
  return mine;
}
__device__ __forceinline__ void select_rows(v3u par, int lane) {
  const Params p = load_params(par);
  gu32* ctrl = (gu32*)(p.ws + p.co);
  const rsrc_t rR = mkrsrc(p.ws + p.co + C_R * 4, S * 4);
  unsigned key[28], mine;
  wait_ge<32>(ctrl, C_G, SEL_AT);
  if (lane == 0) __hip_atomic_fetch_add((gu32*)(p.ws + EPOCH_OFF), 1u, RLX_AGENT);
#pragma unroll 1
  for (int spin = 0;; ++spin) {
    asm volatile("" ::: "memory");
    v4u rr[7];
#pragma unroll
    for (int i = 0; i < 7; ++i) rr[i] = ld16s(rR, lane * 16 + i * 1024, 0);
    __builtin_amdgcn_sched_barrier(0);
    unsigned m = 0;
#pragma unroll
    for (int i = 0; i < 28; ++i) { m = max(m, rr[i >> 2][i & 3]); key[i] = score_key(rr[i >> 2][i & 3], (i >> 2) * 256 + lane * 4 + (i & 3)); }
    if (__builtin_amdgcn_ballot_w64(m == ~0u) == 0) break;
    if (spin > SPIN_CAP / 4) { __hip_atomic_store(ctrl + C_ERR, 1u, RLX_AGENT); break; }
    __builtin_amdgcn_s_sleep(16);
  }
  mine = top8<28>(key, lane);
  __hip_atomic_store(ctrl + C_IDX + (lane >> 3) * 32 + (lane & 7), S - (mine & 0xffffu), RLX_AGENT);
  TS(lane == 0, 16)
  key[4] = lane < 8 ? mine : 0u;
#pragma unroll 1
  for (int spin = -1;; ++spin) {
    asm volatile("" ::: "memory");
    const v4u r = ld16s(rR, lane * 16 + 7 * 1024, 0);
    const bool all = __builtin_amdgcn_ballot_w64(max(max(r[0], r[1]), max(r[2], r[3])) == ~0u) == 0 || spin > SPIN_CAP;
    if (!all && spin >= 0) { __builtin_amdgcn_s_sleep(1); continue; }
    TS(lane == 0, 17)
#pragma unroll
    for (int i = 0; i < 4; ++i) key[i] = score_key(r[i], S - 256 + lane * 4 + i);
    unsigned fin = mine;
    if (spin < 0 || __builtin_amdgcn_ballot_w64(max(max(key[0], key[1]), max(key[2], key[3])) > (unsigned)__builtin_amdgcn_readlane(mine, 7))) fin = top8<5>(key, lane);
    if (!all) continue;
    __hip_atomic_store(ctrl + C_IDX + 256 + (lane >> 3) * 32 + (lane & 7), S - (fin & 0xffffu), RLX_AGENT);
    break;
  }
  TS(lane == 0, 18)
}

__device__ __forceinline__ void ffn_role(const Params& p, int role, int lane, int wave) {
  gu32* ctrl = (gu32*)(p.ws + p.co);
  const int g = lane >> 4, n = lane & 15, q = (lane >> 2) & 3, pp = lane & 3;
  const bool down = role >= 8;
  const int c = role - 8;
  v4u wa[20], wd[12];
  {
    const rsrc_t rg = mkrsrc(p.wg, DM * FF * 2), ru = mkrsrc(p.wu, DM * FF * 2), rd = mkrsrc(p.wd, FF * DM * 2);
    BAR();
#pragma unroll 1
    for (int i = 0; i < 10; ++i) {
      const int vo = ((wave * 160 + i * 16 + (lane >> 2)) * FF + role * 32) * 2 + (lane & 3) * 16;
      dma16(rg, vo, 0, wave * 32768 + i * 1024); dma16(ru, vo, 0, wave * 32768 + 10240 + i * 1024);
      __builtin_amdgcn_s_sleep(FPACE);
    }
#pragma unroll 1
    for (int i = 0; i < 12; ++i) { dma16(rd, down ? ((wave * 384 + i * 32 + (lane >> 1)) * DM + c * 16) * 2 + (lane & 1) * 16 : OOB, 0, wave * 32768 + 20480 + i * 1024); __builtin_amdgcn_s_sleep(FPACE); }
    VMCNT(0);
#pragma unroll
    for (int i = 0; i < 32; ++i) {
      const unsigned a = wave * 32768 + (i < 20 ? ((i / 5) & 1) * 10240 + (i % 5) * 2048 + (8 * g + q) * 64 + (i / 10) * 32 : i * 1024 + (8 * g + q) * 32) + pp * 8;
      const v2u lo = trread(a), hi = trread(a + (i < 20 ? 256 : 128));
      const v4u x = {lo[0], lo[1], hi[0], hi[1]};
      if (i < 20) wa[i] = x; else wd[i - 20] = x;
    }
  }
  const rsrc_t rT = mkrsrc(p.ws + X1_OFF, (unsigned)(WS_END - X1_OFF));
  constexpr int SO_H2 = DM * 2, SO_ACT = (int)(ACT_OFF - X1_OFF), RED = 131072, LW = RED + 24576;
  int row = -1;
  v2u res = {};
#pragma unroll 1
  for (int rnd = 0; rnd < 2; ++rnd) {
    if (wave == 0) {
      const int w = C_IDX + rnd * 256 + (role & 7) * 32 + (lane & 7);
      if (rnd) wait_ge<1>(ctrl, w, 1); else wait_ge<16>(ctrl, w, 1);
      if (lane < 8) *(LAS unsigned*)(LW + lane * 4) = __hip_atomic_load(ctrl + w, RLX_AGENT);
    }
    __syncthreads();
    const unsigned t0 = now_ticks();
    const int nr = (*(LAS int*)(LW + (n & 7) * 4) - 1) & (S - 1);
    if (__builtin_amdgcn_ballot_w64(nr != row) == 0) break;
    row = nr;
    v2u x1, h2, gb; unsigned rv;
    if (down && wave == 0) { gb = ld8s(mkrsrc(p.ws + MODB_OFF, 7680), (5 * DM + c * 16 + 4 * g) * 2, 0); x1 = ld8s(rT, (row * 2 * DM + c * 16 + 4 * g) * 2, 0); h2 = ld8s(rT, (row * 2 * DM + c * 16 + 4 * g) * 2, SO_H2); rv = __hip_atomic_load(ctrl + C_R + row, RLX_AGENT); }
    {
      v4u xb[5];
#pragma unroll
      for (int i = 0; i < 5; ++i) xb[i] = ld16s(rT, (row * 2 * DM + wave * 160 + i * 32 + 8 * g) * 2, SO_H2);
      __builtin_amdgcn_sched_barrier(0);
      f4 ac[4] = {};
#pragma unroll
      for (int i = 0; i < 20; ++i) ac[i & 3] = mfma32(wa[(i & 3) * 5 + (i >> 2)], xb[i >> 2], ac[i & 3]);
#pragma unroll
      for (int k = 0; k < 4; ++k) *(LAS f4*)(RED + wave * 4096 + k * 1024 + lane * 16) = ac[k];
      __syncthreads();
      if (wave >= 2) {
        f4 ag = {}, au = {};
#pragma unroll
        for (int w = 0; w < 4; ++w) { ag += *(LAS f4*)(RED + w * 4096 + (wave - 2) * 2048 + lane * 16); au += *(LAS f4*)(RED + w * 4096 + (wave - 2) * 2048 + 1024 + lane * 16); }
        float a[4];
#pragma unroll
        for (int r = 0; r < 4; ++r) { const float gs = rbf(ag[r]); a[r] = rbf(gs / (1.f + __expf(-gs))) * rbf(au[r]); }
        __builtin_amdgcn_raw_buffer_store_b64(v2u{unsent(pack2(a[0], a[1])), unsent(pack2(a[2], a[3]))}, rT,
                                              n < 8 ? rnd * 24576 + (role * 2 + wave - 2) * 256 + (g >> 1) * 128 + n * 16 + (g & 1) * 8 : OOB, SO_ACT, 16);
      }
    }
    if (!down) continue;
    while ((int)(now_ticks() - t0) < FFN_WAIT) __builtin_amdgcn_s_sleep(1);
    v4u ab[12];
#pragma unroll 1
    for (int spin = 0;; ++spin) {
      asm volatile("" ::: "memory");
      unsigned m = 0;
#pragma unroll
      for (int i = 0; i < 12; ++i) ab[i] = ld16s(rT, rnd * 24576 + (wave * 12 + i) * 512 + g * 128 + (n & 7) * 16, SO_ACT);
      __builtin_amdgcn_sched_barrier(0);
#pragma unroll
      for (int i = 0; i < 12; ++i) m = max(max(m, max(ab[i][0], ab[i][1])), max(ab[i][2], ab[i][3]));
      if (__builtin_amdgcn_ballot_w64(m == ~0u) == 0) break;
      if (spin > SPIN_CAP) { __hip_atomic_store(ctrl + C_ERR, 1u, RLX_AGENT); break; }
      __builtin_amdgcn_s_sleep(2);
    }
    f4 y = {};
#pragma unroll
    for (int i = 0; i < 12; ++i) y = mfma32(wd[i], ab[i], y);
    *(LAS f4*)(RED + 16384 + wave * 1024 + lane * 16) = y;
    __syncthreads();
    if (wave == 0) {
#pragma unroll
      for (int w = 1; w < 4; ++w) y += *(LAS f4*)(RED + 16384 + w * 1024 + lane * 16);
      const float rs = rbf(1.f / (1.f + __expf(-u2f(rv))));
      float o[4];
#pragma unroll
      for (int r = 0; r < 4; ++r) {
        const float hv = (r & 1) ? hi2f(h2[r >> 1]) : lo2f(h2[r >> 1]), xv = (r & 1) ? hi2f(x1[r >> 1]) : lo2f(x1[r >> 1]);
        o[r] = xv + rbf(((r & 1) ? hi2f(gb[r >> 1]) : lo2f(gb[r >> 1])) * rbf(hv + rbf(rbf(y[r]) * rs)));
      }
      res = v2u{pack2(o[0], o[1]), pack2(o[2], o[3])};
    }
    TS(role == 8 && wave == 0 && lane == 0, 14 + rnd)
  }
  if (down && wave == 0) __builtin_amdgcn_raw_buffer_store_b64(res, mkrsrc(p.out, S * DM * 2), n < 8 ? (row * DM + c * 16 + 4 * g) * 2 : OOB, 0, 16);
}

constexpr int L_LN = 123904;
__device__ __forceinline__ void ln1_fetch(const u16* x, int tile, int lane, int wave) {
  const rsrc_t rx = mkrsrc(x, S * DM * 2);
#pragma unroll
  for (int k = 0; k < 4; ++k) dma16(rx, lane * 16, (tile * 16 + wave * 4 + k) * 1280, L_LN + (wave * 4 + k) * 1040);
  dma16(rx, (lane >> 4) * 1280 + 1024 + (lane & 15) * 16, (tile * 16 + wave * 4) * 1280, L_LN + 16640 + wave * 1024);
}
__device__ __forceinline__ void ln1_rows(const Params& p, int tile, int lane, int wave) {
  const rsrc_t rh = mkrsrc(p.ws + HF_OFF, (unsigned)HF_BYTES), rm = mkrsrc(p.ws + MODB_OFF, 2560);
    const int r = wave * 8 + (lane & 7);
    v4u a[10], sh[10], sc[10];
#pragma unroll
    for (int i = 0; i < 10; ++i) a[i] = lds128(L_LN + (i < 8 ? r * 1040 : 16640 - 1024 + r * 256) + (lane >> 3) * 16 + i * 128);
#pragma unroll
    for (int i = 0; i < 10; ++i) { sh[i] = ld16s(rm, (lane >> 3) * 16 + i * 128, 0); sc[i] = ld16s(rm, (lane >> 3) * 16 + i * 128, 1280); }
    __builtin_amdgcn_sched_barrier(0);
    float s = 0.f, q = 0.f;
    const bf2 one = {(__bf16)1.f, (__bf16)1.f};
#pragma unroll
    for (int i = 0; i < 10; ++i)
#pragma unroll
      for (int k = 0; k < 4; ++k) { const unsigned w = a[i][k]; s = __builtin_amdgcn_fdot2_f32_bf16(__builtin_bit_cast(bf2, w), one, s, false); }
    s += ror8f(s); s = swapsum16(s, s); s = swapsum32(s, s);
    const float mean = s * (1.f / DM);
#pragma unroll
    for (int i = 0; i < 10; ++i)
#pragma unroll
      for (int k = 0; k < 4; ++k) { const float d0 = lo2f(a[i][k]) - mean, d1 = hi2f(a[i][k]) - mean; q = fmaf(d0, d0, fmaf(d1, d1, q)); }
    q += ror8f(q); q = swapsum16(q, q); q = swapsum32(q, q);
    const float mb = rbf(mean), isd = __builtin_amdgcn_rcpf(rbf(__builtin_amdgcn_sqrtf(rbf(rbf(q * (1.f / DM)) + 1e-5f))));
#pragma unroll
    for (int i = 0; i < 10; ++i) {
      v4u o;
#pragma unroll
      for (int k = 0; k < 4; ++k) {
        const unsigned d = pack2(lo2f(a[i][k]) - mb, hi2f(a[i][k]) - mb), n = pack2(lo2f(d) * isd, hi2f(d) * isd);
        const unsigned m = pack2(lo2f(n) * lo2f(sc[i][k]), hi2f(n) * hi2f(sc[i][k]));
        o[k] = pack2(lo2f(m) + lo2f(sh[i][k]), hi2f(m) + hi2f(sh[i][k]));
      }
      __builtin_amdgcn_raw_buffer_store_b128(o, rh, tile * 20480 + i * 2048 + (lane >> 3) * 256 + r * 16, 0, 16);
    }
}
__device__ __forceinline__ void w_issue(rsrc_t rw, int type, int h, int m, int slot, int i0, int cnt, int lane) {
#pragma unroll 1
  for (int i = i0; i < i0 + cnt; ++i) {
    const int c = i * 64 + lane;
    dma16(rw, type < 3 ? ((m * 160 + c / 24) * HD + h * DH) * 2 + (c % 24) * 16 : (h * DH + (i < 40 ? 32 * m : 96 + 16 * m)) * DM * 2 + c * 16, 0, slot * 61440 + i * 1024);
  }
}
__device__ __forceinline__ rsrc_t w_rsrc(int type) { const auto* k = fresh_params(); return mkrsrc(type == 0 ? k->wk : type == 1 ? k->wq : type == 2 ? k->wv : k->wo, DM * HD * 2); }
__device__ __forceinline__ void put_line(unsigned char* ws, int off, v4u v, int lanes, int lane) {
  __builtin_amdgcn_raw_buffer_store_b128(v, mkrsrc(ws, WS_BYTES), lane < lanes ? off + lane * 16 : OOB, 0, 16);
}
__device__ __forceinline__ void sum_table(unsigned char* ws, unsigned old, int lane) {
  const unsigned epoch = old + 1;
  typedef GAS unsigned long long gu64;
#pragma unroll 1
  for (int spin = 0;; ++spin) {
    const unsigned long long a = __hip_atomic_load((gu64*)(ws + TAB_OFF) + lane * 16, RLX_AGENT), b = __hip_atomic_load((gu64*)(ws + TAB_OFF) + min(64 + lane, NTAB - 1) * 16, RLX_AGENT);
    if (__builtin_amdgcn_ballot_w64((unsigned)a != epoch || (unsigned)b != epoch) == 0 || spin > SPIN_CAP) {
      LAS int* v = (LAS int*)1024;
      v[lane] = 0; v[128 + lane] = 0;
      LGKM0();
      v[16 + lane] = (int)(a >> 32); v[80 + lane] = (int)(b >> 32);
      LGKM0();
      int sa = 0, sb = 0;
#pragma unroll
      for (int k = -CALWIN; k < CALWIN; ++k) { sa += v[16 + lane + k]; sb += v[80 + lane + k]; }
      const unsigned long long lo = __builtin_amdgcn_ballot_w64((sa > 0) ^ (lane & 1)), hi = __builtin_amdgcn_ballot_w64((sb > 0) ^ (lane & 1));
      put_line(ws, SUM_OFF, lane & 1 ? v4u{epoch, (unsigned)hi, epoch, 0u} : v4u{epoch, (unsigned)lo, epoch, (unsigned)(lo >> 32)}, 64, lane);
      return;
    }
    __builtin_amdgcn_s_sleep(2);
  }
}
__device__ __forceinline__ v3u wait_table(unsigned char* ws, unsigned old, int lane) {
  const unsigned epoch = old + 1;
#pragma unroll 1
  for (int spin = 0;; ++spin) {
    const unsigned long long a = __hip_atomic_load((GAS unsigned long long*)(ws + SUM_OFF) + (blockIdx.x & 7) * 16 + (lane & 3), RLX_AGENT);
    if (__builtin_amdgcn_ballot_w64((unsigned)a != epoch) == 0 || spin > SPIN_CAP)
      return v3u{(unsigned)__builtin_amdgcn_readlane(a >> 32, 0), (unsigned)__builtin_amdgcn_readlane(a >> 32, 1), (unsigned)__builtin_amdgcn_readlane(a >> 32, 2)};
    __builtin_amdgcn_s_sleep(4);
  }
}
__device__ __forceinline__ void calibrate(unsigned char* ws, unsigned epoch, int blk, int lane) {
  const rsrc_t r = mkrsrc(ws, WS_BYTES);
  unsigned best[2] = {1u << 30, 1u << 30};
#pragma unroll 1
  for (int rep = -1; rep < CALREP; ++rep) {
    const int off = lane < (rep < 0 ? 16 : 8) ? (blk << 21) + 0x1FC000 + (rep < 0 ? lane >> 3 : rep & 1) * 8192 + (lane & 7) * 16 : OOB;
    const unsigned long long t1 = __builtin_amdgcn_s_memtime();
    LGKM0();
    __builtin_amdgcn_raw_buffer_store_b128(v4u{1u, 2u, 3u, 4u}, r, off, 0, 16);
    VMCNT(0);
    const unsigned d = rep < 0 ? 1u << 30 : (unsigned)(__builtin_amdgcn_s_memtime() - t1);
    if (rep & 1) best[1] = min(best[1], d); else best[0] = min(best[0], d);
  }
  const int d = min(max((int)(best[1] - best[0]), -250), 250);
  put_line(ws, TAB_OFF + blk * 128, v4u{epoch + 1, (unsigned)(blk & 1 ? -d : d), 0u, 0u}, 8, lane);
}
__device__ __forceinline__ v3u token_start(v32u (&wa)[7], v16u& wa7, unsigned epoch, int type, int h, int lane, int wave) {
  const auto* k = fresh_params();
  const int tile = type < 3 ? h * 3 + type : 96 + h, cb = type * 32 + h, g = lane >> 4, q = (lane >> 2) & 3, pp = lane & 3;
  const rsrc_t rw = w_rsrc(type);
  const int cal = wave * 32 + (h & 7) * 4 + (h >> 3);
  if (type == 3 && cal < NCAL) calibrate(k->ws, epoch, cal, lane);
  if (type == 3) BAR();
  LAS float* red = (LAS float*)(L_LN + 20736);
  if (wave < 2) {
    const rsrc_t rm = mkrsrc(k->wmod, DM * 6 * DM * 2), rc = mkrsrc(k->c, DM * 2);
    const int rg = wave * 16 + (lane >> 2);
    v4u w[20]; unsigned cv[20];
#pragma unroll
    for (int i = 0; i < 20; ++i) {
      w[i] = __builtin_amdgcn_raw_buffer_load_b128(rm, cb < 120 ? ((rg + 32 * i) * 3840 + cb * 32 + pp * 8) * 2 : OOB, 0, 2);
      cv[i] = __builtin_amdgcn_raw_buffer_load_b16(rc, (rg + 32 * i) * 2, 0, 0);
    }
    __builtin_amdgcn_sched_barrier(0);
    float acc[8] = {};
#pragma unroll
    for (int i = 0; i < 20; ++i) {
      const float c = bf2f((u16)cv[i]), sv = rbf(c * __builtin_amdgcn_rcpf(1.f + __expf(-c)));
#pragma unroll
      for (int j = 0; j < 4; ++j) { acc[2 * j] += sv * lo2f(w[i][j]); acc[2 * j + 1] += sv * hi2f(w[i][j]); }
    }
#pragma unroll
    for (int j = 0; j < 8; ++j) {
      float v = acc[j];
      v += u2f(__builtin_amdgcn_update_dpp(0, (int)f2u(v), 0x124, 0xf, 0xf, true)); v += ror8f(v); v = swapsum16(v, v); v = swapsum32(v, v);
      if (lane < 4) red[wave * 32 + lane * 8 + j] = v;
    }
    LGKM0();
  } else {
    __builtin_amdgcn_s_sleep(4);
    ln1_fetch(k->x, tile, lane, 2 * wave - 4); ln1_fetch(k->x, tile, lane, 2 * wave - 3);
    w_issue(rw, type, h, wave - 2, wave - 2, 0, 30, lane);
  }
  BAR();
  if (wave >= 2) w_issue(rw, type, h, wave - 2, wave - 2, 30, 30, lane);
  if (wave == 1) {
    const float sum = red[lane & 31] + red[32 + (lane & 31)];
    __builtin_amdgcn_raw_buffer_store_b16((u16)pack2((cb / 20) % 3 == 1 ? 1.f + rbf(sum) : sum, 0.f), mkrsrc(k->ws + MODB_OFF, 7680), lane < 32 ? (cb * 32 + lane) * 2 : OOB, 0, 16);
  }
  const v3u par = wait_table(k->ws, epoch, lane);
  const Params p = load_params(par);
  gu32* ctrl = (gu32*)(p.ws + p.co);
  TS(wave == 0 && lane == 0 && cb == 5, 60)
  VMCNT(0);
  if (wave == 1 && cb < 120 && lane == 0) __hip_atomic_fetch_add(ctrl + C_MOD + (cb < 40 ? 0 : 64) + (cb & 1) * 32, 1u, RLX_AGENT);
  const v2u tb = type < 3 ? v2u{(unsigned)((8 * g + q) * 384 + (32 * wave + 8 * pp) * 2), (unsigned)((8 * g + q) * 384 + (128 + 16 * wave + 4 * pp) * 2)}
                          : v2u{(unsigned)((8 * g + q) * 1280 + (160 * wave + 4 * pp) * 2), (unsigned)((32 + 4 * g + q) * 1280 + (160 * wave + 4 * pp) * 2)};
  BAR();
  asm volatile(TAKE0_ASM : W240_IO("=", wa) : "{v[0:1]}"(tb), "{s52}"(type) : "memory", "scc", TAKE0_CLOBBERS);
  BAR();
  TS(wave == 0 && lane == 0 && cb == 5, 62)
  if (wave >= 2) w_issue(rw, type, h, wave, wave - 2, 0, 60, lane);
  else {
    wait_ge<2>(ctrl, C_MOD + (lane & 1) * 32, 20);
    TS(wave == 0 && lane == 0 && cb == 5, 61)
    ln1_rows(p, tile, lane, wave);
    VMCNT(0);
    if (lane == 0) __hip_atomic_fetch_add(ctrl + C_LN + (type < 3 ? tile % 6 : 6 + (h & 1)) * 32, 1u, RLX_AGENT);
    TS(wave == 0 && lane == 0 && type == 0, 640 + h)
  }
  VMCNT(0); BAR();
  asm volatile(TAKE1_ASM : W240_IO("+", wa) : "{v[0:1]}"(tb), "{s52}"(type) : "memory", "scc", TAKE1_CLOBBERS);
  if (wave == 2 && type < 3) wait_ge<2>(ctrl, C_LN + (lane % 6) * 32, 32);
  BAR();
  TS(wave == 0 && lane == 0, 512 + cb)
  return par;
}
__device__ __forceinline__ v3u score_start(unsigned epoch, int h, int lane, int wave) {
  const auto* k = fresh_params();
  const rsrc_t r = mkrsrc(k->ws, WS_BYTES);
  const v4u z = {0u, 0u, 0u, 0u}, m = {~0u, ~0u, ~0u, ~0u};
  const int tid = wave * 64 + lane, i = h * 256 + tid;
  __builtin_amdgcn_raw_buffer_store_b128(z, r, i < ZERO_BYTES / 16 ? CTRL_OFF + i * 16 : i < ZERO_BYTES / 16 + 512 ? (HB << 21) + lmap(H_FLAG) + (i & 255) * 16 + (i & 256) * 32 : OOB, 0, 16);
  VMCNT(0); BAR();
  if (wave == 0 && h < 14) put_line(k->ws, TAB_OFF + (NCAL + h) * 128, v4u{epoch + 1, 0u, 0u, 0u}, 8, lane);
  if (wave == 0 && h == 31) { pause(SUMWAIT); sum_table(k->ws, epoch, lane); }
  const v3u par = wait_table(k->ws, epoch, lane);
  const Params p = load_params(par);
  const int h0 = side_a(p, HB);
#pragma unroll 1
  for (int j = i; j < 4 * 2560; j += 8192) {
    const int o = j % 2560;
    v4u val = m;
    if (o < 640) { const v4u x = *(const GAS v4u*)(p.mem0 + (o & 7) * DM + (o >> 3) * 8); val = v4u{unsent(x[0]), unsent(x[1]), unsent(x[2]), unsent(x[3])}; }
    __builtin_amdgcn_raw_buffer_store_b128(val, r, o < 640 || o >= 1024 ? h0 + lmap(j * 16) : OOB, 0, 16);
  }
  __builtin_amdgcn_raw_buffer_store_b128(m, r, i < 512 ? p.co + C_R * 4 + i * 16 : i < 512 + TOPK * FF / 4 ? ACT_OFF + (i - 512) * 16 : OOB, 0, 16);
#pragma unroll 1
  for (int j = i; j < (NSEG - OSPLIT) * 32 * 80; j += 8192) __builtin_amdgcn_raw_buffer_store_b128(m, r, X1_OFF + (OSPLIT * 32 + j / 80) * 2560 + (j % 80) * 16, 0, 16);
  return par;
}

__device__ __forceinline__ v3u u_role(unsigned epoch, int h, int th, int lane, int wave) {
  const auto* kp = fresh_params();
  const int g = lane >> 4, n = lane & 15, q = (lane >> 2) & 3, pp = lane & 3;
  v32u vc; v12u sr; v16u sc = {};
  sc[0] = wave;
  OPAQUE(vc);
#pragma unroll
  for (int k = 0; k < 7; ++k) { const int c = k * 64 + lane; vc[k] = ((h * DH + c / 28) * DM + 208 * th) * 2 + ((c % 28) ^ (c / 28 >> 2 & 2)) * 16 - (k & 3) * 1024; }
  const int ch = wave >> 1;
  vc[7] = (8 * g + q) * 448 + pp * 8 + ((7 * ch) ^ (g & 1)) * 32;
  vc[26] = (8 * g + q) * 448 + pp * 8 + ((7 * ch + 1) ^ (g & 1)) * 32;
#pragma unroll
  for (int k = 0; k < 12; ++k) { const int c = k * 64 + lane, r = c / 24; vc[8 + k] = (r * HD + h * DH) * 2 + (((c % 24) ^ (r >> 2)) * 16) - (k & 3) * 1024; }
  const int row = 16 * (n >> 3) + 8 * ((n >> 1) & 1) + 4 * ((n >> 2) & 1) + (n & 1);
  vc[20] = 86016 + (wave & 1) * 24576 + row * 384 + (g ^ (row >> 2)) * 16;
  vc[21] = vc[20] ^ 64;
  vc[22] = (26 * th + 14 * ch + (n >> 3)) * 32768 + lmap(h * 512) + g * 128 + (n & 7) * 16;
  set_rsrc(sr, 0, kp->ws, WS_BYTES); set_rsrc(sr, 1, kp->wv, DM * HD * 2); set_rsrc(sr, 2, kp->wo, DM * HD * 2);
  pin(sc); pin(sr);
  asm volatile(UF_ASM : "+{s[52:67]}"(sc) : "{v[0:31]}"(vc), "{s[36:47]}"(sr) : "memory", "scc", UF_CLOBBERS);
  const v3u par = wait_table(kp->ws, epoch, lane);
  const Params p = load_params(par);
  vc[23] = side_a(p, 2 * (USLOT + lane % 20));
  vc[24] = th == 2 ? side_a(p, 2 * (USLOT + lane % 20) + 1) - (1 << 21) : vc[23];
  if (ch) vc[23] = vc[24];
  vc[25] = lane == 0 ? p.co + (C_U + (th * 4 + wave) * 32) * 4 : OOB;
  asm volatile(UG_ASM : "+{v[0:31]}"(vc), "+{s[52:67]}"(sc) : "{s[36:47]}"(sr) : "memory", "vcc", "scc", UG_CLOBBERS);
  return par;
}

__device__ __forceinline__ void proj_role(const v32u (&wa)[7], const v16u& wa7, const Params& p, int type, int head, int lane, int wave) {
  gu32* ctrl = (gu32*)(p.ws + p.co);
  {
    const rsrc_t rh = mkrsrc(p.ws + HF_OFF, (unsigned)HF_BYTES);
#pragma unroll
    for (int k = 0; k < 20; ++k) dma16_sc1(rh, wave * 10240 + lane * 16, (k / 10) * 40960 + (k % 10) * 1024, (k / 10) * 40960 + wave * 10240 + (k % 10) * 1024);
  }
  v12u vc; v8u sr;
  vc[0] = lane * 16; vc[1] = wave * 10240 + lane * 16;
  if (type < 2) {
    vc[2] = lmap(KQ_OFF + head * 24576 + type * 8192) + wave * 2048 + lane * 16;
    vc[3] = lmap(KQ_OFF + head * 24576 + 16384) + type * 4096 + wave * 1024 + lane * 16;
    vc[4] = side_a(p, slot_blk(lane) + 1);
    vc[5] = lane == 0 ? flag_off(p, head) + (type * 4 + wave) * 4 : OOB;
  } else {
    vc[2] = head * 32768 + wave * 2048 + lane * 16;
    vc[3] = head * 32768 + 16384 + wave * 1024 + lane * 16;
    vc[4] = side_a(p, slot_blk(lane)) ^ 8192;
    vc[5] = lane == 0 ? p.co + (C_VF + head * 32 + wave) * 4 : OOB;
  }
  vc[6] = 0;
  {
    const int pc = wave * 64 + type * 32 + head, k = pc % 34, o = k < 10 ? k : k + 6;
    vc[7] = type < 2 && pc < 136 ? (pc / 34) * 2 * H_COPY + lmap(o * 1024) + lane * 16 : OOB;
    vc[8] = lane < 63 ? hand_off(p, lane + 1) : OOB;
  }
  vc[9] = 0; vc[10] = 0; vc[11] = 0;
  set_rsrc(sr, 0, p.ws + HF_OFF, (unsigned)HF_BYTES); set_rsrc(sr, 1, p.ws, WS_BYTES);
#pragma unroll 1
  for (int k = 0; k < 11; ++k) {
    if (wave == 0 && k > 1) {
      if (k == 2) wait_ge<8>(ctrl, C_LN + (6 + (lane & 1)) * 32, 32);
      wait_ge<8>(ctrl, C_G + (head & 15) * 32, max(6 * k - AHEAD, 1));
      if (6 * k + 5 >= RING) wait_ge<8>(ctrl, C_AF + min(max(6 * k + lane % 6 - RING, 0), NSEG - 1 - RING) * 32, 2);
    }
    v4u sc = {6u * k, min(6u * k + 6u, 64u), (unsigned)wave * 10240u, 0u};
    v12u vt = vc;
    pin(sc);
    asm volatile(PROJ_ASM : "+{s[52:55]}"(sc), "+{v[0:11]}"(vt) : "{s[36:43]}"(sr), W240_IN(wa) : "memory", "vcc", "scc", PROJ_CLOBBERS);
    TS(wave == 0 && lane == 0 && head == 0 && (k == 4 || k == 7 || k == 10), 20 + type * 4 + (k - 4) / 3)
  }
}

__device__ __forceinline__ void vo_role(const v32u (&wa)[7], const v16u& wa7, const Params& p, int h, int lane, int wave) {
  const int g = lane >> 4, n = lane & 15, q = (lane >> 2) & 3, pp = lane & 3;
  v16u vc; v4u sr;
  vc[0] = lane * 16;
  vc[1] = h * 32768 + wave * 2048 + lane * 16;
  vc[2] = h * 32768 + 16384 + wave * 1024 + lane * 16;
  vc[3] = (20 * wave + (n >> 3)) * 32768 + lmap(h * 512) + g * 128 + (n & 7) * 16;
  vc[4] = side_a(p, slot_blk(lane));
  vc[5] = wave == 3 ? side_a(p, slot_blk(lane) + 1) - (1 << 21) : vc[4];
  vc[6] = vc[4] ^ 8192;
  vc[7] = lane == 0 ? flag_off(p, h) + (8 + wave) * 4 : OOB;
  vc[8] = 0;
  vc[9] = p.co + (C_VF + h * 32 + pp) * 4;
#pragma unroll
  for (int k = 10; k < 16; ++k) vc[k] = 0;
  set_rsrc(sr, 0, p.ws, WS_BYTES);
  v4u sc = {0u, 64u, (unsigned)wave, 0u};
  pin(sc); pin(sr);
  asm volatile(VO_ASM : "+{s[52:55]}"(sc), "+{v[0:15]}"(vc) : "{s[36:39]}"(sr), W240_IN(wa) : "memory", "vcc", "scc", VO_CLOBBERS);
}

typedef float f2 __attribute__((ext_vector_type(2)));
__device__ __forceinline__ f2 up2(unsigned w) { return f2{lo2f(w), hi2f(w)}; }
constexpr int L_PAR = 123904;
__device__ __forceinline__ void row_params(v3u par, int wave) {
  const Params p = load_params(par);
  const int tid = wave * 64 + fresh_lane();
  const rsrc_t rm = mkrsrc(p.ws + MODB_OFF + 2560, 5120), rr = mkrsrc(p.wr, 1280);
  wait_ge<8>((gu32*)(p.ws + p.co), C_MOD + 64 + (tid & 1) * 32, 40);
  unsigned w[5];
#pragma unroll
  for (int k = 0; k < 5; ++k) w[k] = __builtin_amdgcn_raw_buffer_load_b32(rm, (k * 256 + tid) * 4, 0, 16);
  const v2u r = __builtin_amdgcn_raw_buffer_load_b64(rr, tid * 8, 0, 0);
  BAR();
#pragma unroll
  for (int k = 0; k < 5; ++k) *(LAS f2*)(L_PAR + (k * 256 + tid) * 8) = up2(w[k]);
  *(LAS v2u*)(L_PAR + 10240 + tid * 8) = r;
  LGKM0(); BAR();
}
__device__ __forceinline__ float sum16(float v) {
  v += u2f(__builtin_amdgcn_update_dpp(0, (int)f2u(v), 0xb1, 0xf, 0xf, true)); v += u2f(__builtin_amdgcn_update_dpp(0, (int)f2u(v), 0x4e, 0xf, 0xf, true));
  v += u2f(__builtin_amdgcn_update_dpp(0, (int)f2u(v), 0x124, 0xf, 0xf, true));
  return v + ror8f(v);
}
struct RowIn { v4u x[5], y[5]; };
__device__ __forceinline__ void rows_load(RowIn& r, int row0) {
  const auto* k = fresh_params();
  const int lane = fresh_lane(), row = row0 + (lane >> 4), co = (lane & 15) * 16;
  const rsrc_t rx = mkrsrc(k->x, S * DM * 2), rT = mkrsrc(k->ws + X1_OFF, 2 * HF_BYTES);
#pragma unroll
  for (int i = 0; i < 5; ++i) { r.y[i] = ld16s(rT, row * 2560 + co + i * 256, 0); r.x[i] = __builtin_amdgcn_raw_buffer_load_b128(rx, row * 1280 + co + i * 256, 0, 0); }
}
__device__ __forceinline__ unsigned rows_do(const RowIn& r, int row0) {
  const auto* kp = fresh_params();
  const int lane = fresh_lane(), row = row0 + (lane >> 4), co = (lane & 15) * 16;
  const rsrc_t rT = mkrsrc(kp->ws + X1_OFF, (unsigned)(WS_END - X1_OFF)), rO = mkrsrc(kp->out, S * DM * 2);
  auto par = [&](int a, int i, int k) { const f4 v = *(LAS f4*)(L_PAR + a * 2560 + co * 2 + i * 512 + (k >> 1) * 16); return k & 1 ? f2{v[2], v[3]} : f2{v[0], v[1]}; };
  f2 x1[20], s = {}, q = {};
  float rs = 0.f;
#pragma unroll
  for (int i = 0; i < 5; ++i) {
    v4u w;
#pragma unroll
    for (int k = 0; k < 4; ++k) {
      const f2 t = __builtin_elementwise_fma(par(0, i, k), up2(r.y[i][k]), up2(r.x[i][k]));
      w[k] = pack2(t[0], t[1]); x1[4 * i + k] = up2(w[k]); s += x1[4 * i + k];
    }
    __builtin_amdgcn_raw_buffer_store_b128(w, rT, row * 2560 + co + i * 256, 0, 16);
  }
  const float mean = sum16(s[0] + s[1]) * (1.f / DM);
#pragma unroll
  for (int i = 0; i < 20; ++i) { const f2 d = x1[i] - mean; q = __builtin_elementwise_fma(d, d, q); }
  const float isd = __builtin_amdgcn_rcpf(rbf(__builtin_amdgcn_sqrtf(rbf(rbf(sum16(q[0] + q[1]) * (1.f / DM)) + 1e-5f)))), mi = -rbf(mean) * isd;
#pragma unroll
  for (int i = 0; i < 5; ++i) {
    const v4u wr = lds128(L_PAR + 10240 + co + i * 256);
    v4u h, o;
#pragma unroll
    for (int k = 0; k < 4; ++k) {
      const f2 hh = __builtin_elementwise_fma(__builtin_elementwise_fma(x1[4 * i + k], f2{isd, isd}, f2{mi, mi}), par(2, i, k), par(1, i, k));
      const f2 oo = __builtin_elementwise_fma(par(3, i, k), hh, x1[4 * i + k]);
      const unsigned hb = pack2(hh[0], hh[1]), wk = wr[k];
      h[k] = hb; o[k] = pack2(oo[0], oo[1]);
      rs = __builtin_amdgcn_fdot2_f32_bf16(__builtin_bit_cast(bf2, hb), __builtin_bit_cast(bf2, wk), rs, false);
    }
    __builtin_amdgcn_raw_buffer_store_b128(h, rT, row * 2560 + 1280 + co + i * 256, 0, 16);
    __builtin_amdgcn_raw_buffer_store_b128(o, rO, row * 1280 + co + i * 256, 0, 16);
  }
  return f2u(rbf(sum16(rs)));
}
__device__ __forceinline__ void row_one(v3u par, int row, const unsigned* x) {
  const Params p = load_params(par);
  const int lane = fresh_lane();
  const rsrc_t rT = mkrsrc(p.ws + X1_OFF, (unsigned)(WS_END - X1_OFF)), rO = mkrsrc(p.out, S * DM * 2);
  f2 pr[4][5]; unsigned wr[5], y[5];
#pragma unroll
  for (int i = 0; i < 5; ++i) {
#pragma unroll
    for (int a = 0; a < 4; ++a) pr[a][i] = *(LAS f2*)(L_PAR + a * 2560 + i * 512 + lane * 8);
    wr[i] = *(LAS unsigned*)(L_PAR + 10240 + i * 256 + lane * 4);
  }
#pragma unroll 1
  for (int spin = 0;; ++spin) {
    asm volatile("" ::: "memory");
#pragma unroll
    for (int i = 0; i < 5; ++i) y[i] = __builtin_amdgcn_raw_buffer_load_b32(rT, row * 2560 + i * 256 + lane * 4, 0, 16);
    if (__builtin_amdgcn_ballot_w64(max(max(max(y[0], y[1]), max(y[2], y[3])), y[4]) == ~0u) == 0) break;
    if (spin > SPIN_CAP) { __hip_atomic_store((gu32*)(p.ws + p.co) + C_ERR, 1u, RLX_AGENT); break; }
    __builtin_amdgcn_s_sleep(1);
  }
  f2 x1[5], s = {}, q = {};
#pragma unroll
  for (int i = 0; i < 5; ++i) {
    const f2 t = __builtin_elementwise_fma(pr[0][i], up2(y[i]), up2(x[i]));
    const unsigned w = pack2(t[0], t[1]);
    x1[i] = up2(w); s += x1[i];
    __builtin_amdgcn_raw_buffer_store_b32(w, rT, row * 2560 + i * 256 + lane * 4, 0, 16);
  }
  const float mean = wave_sum(s[0] + s[1]) * (1.f / DM);
#pragma unroll
  for (int i = 0; i < 5; ++i) { const f2 d = x1[i] - mean; q = __builtin_elementwise_fma(d, d, q); }
  const float isd = __builtin_amdgcn_rcpf(rbf(__builtin_amdgcn_sqrtf(rbf(rbf(wave_sum(q[0] + q[1]) * (1.f / DM)) + 1e-5f)))), mi = -rbf(mean) * isd;
  float rs = 0.f;
#pragma unroll
  for (int i = 0; i < 5; ++i) {
    const f2 hh = __builtin_elementwise_fma(__builtin_elementwise_fma(x1[i], f2{isd, isd}, f2{mi, mi}), pr[2][i], pr[1][i]);
    const f2 oo = __builtin_elementwise_fma(pr[3][i], hh, x1[i]);
    const unsigned hb = pack2(hh[0], hh[1]), wk = wr[i];
    __builtin_amdgcn_raw_buffer_store_b32(hb, rT, row * 2560 + 1280 + i * 256 + lane * 4, 0, 16);
    __builtin_amdgcn_raw_buffer_store_b32(pack2(oo[0], oo[1]), rO, row * 1280 + i * 256 + lane * 4, 0, 16);
    rs = __builtin_amdgcn_fdot2_f32_bf16(__builtin_bit_cast(bf2, hb), __builtin_bit_cast(bf2, wk), rs, false);
  }
  const unsigned sc = f2u(rbf(wave_sum(rs)));
  VMCNT(0);
  if (lane == 0) __hip_atomic_store((gu32*)(p.ws + p.co) + C_R + row, sc, RLX_AGENT);
}
#define OGATE (seg < 58 ? seg + 2 : 60 + (seg - 58) / 2)
template <int NT>
__device__ __forceinline__ void o_unit(const Params& p, int seg, int t0, int rep, int lane, int wave) {
  gu32* ctrl = (gu32*)(p.ws + p.co);
  constexpr int NB = NT < 4 ? NT : 4;
  const rsrc_t rH = mkrsrc(p.ws, WS_BYTES);
  const int g = lane >> 4, n = lane & 15;
  const int sa = rfl(side_a(p, slot_blk(seg))), sb = rfl(side_a(p, slot_blk(seg) + 1)), sp = rfl(pt_off(p, seg));
  if (wave == 0) {
    const long w = ((long)flag_off(p, lane & 31) - p.co) / 4 + 8 + (lane >> 5) * 2;
    wait_ge<16>(ctrl, C_G + rep * 32, OGATE);
    if (OGATE < seg + 1) { wait_ge<32>(ctrl, w, seg + 1); wait_ge<32>(ctrl, w + 1, seg + 1); }
  }
  BAR();
  const int vy = (n >> 3) * 32768 + lmap(wave * 4096) + g * 128 + (n & 7) * 16, vz = (n >> 3) * 4096 + wave * 1024 + g * 128 + (n & 7) * 16;
  v4u y[NB][8], z[NB][2], pt[2][8], pm[2][2];
  auto ldy = [&](int t) {
    const int T = t0 + t, so = T < 32 ? sa + T * 65536 : sb + (T - 32) * 65536;
#pragma unroll
    for (int j = 0; j < 8; ++j) y[t % NB][j] = ld16s(rH, vy + j * 512, so);
  };
  auto ldz = [&](int t) {
#pragma unroll
    for (int j = 0; j < 2; ++j) z[t % NB][j] = ld16s(rH, vz + j * 512, (sb ^ 8192) + seg / RING * 655360 + (t0 + t) * 16384);
  };
#pragma unroll
  for (int t = 0; t < NB; ++t) ldy(t);
  __builtin_amdgcn_sched_barrier(0);
  if (wave == 0) {
    if (seg < NSEG - 3) wait_ge<8>(ctrl, C_G + rep * 32, seg + 4);
    else wait_ge<2>(ctrl, C_PT + (lane & 15) * 32, lane & 8 ? 10 : 8);
  }
  BAR();
#pragma unroll
  for (int j = 0; j < 16; ++j) pt[j & 1][j >> 1] = ld16s(rH, lane * 16 + (j & 3) * 1024, sp + wave * 32768 + (j >> 3) * 16384 + ((j >> 2) & 1) * 4096);
#pragma unroll
  for (int j = 0; j < 4; ++j) pm[j & 1][j >> 1] = ld16s(rH, g * 512 + n * 16 + (j & 1) * 256 + (j >> 1) * 2048, sp + lmap(65536 + wave * 4096));
#pragma unroll
  for (int t = 0; t < NB; ++t) ldz(t);
  __builtin_amdgcn_sched_barrier(0);
#pragma unroll
  for (int t = 0; t < NT; ++t) {
    f4 acc[2] = {};
#pragma unroll
    for (int j = 0; j < 8; ++j) { acc[0] = mfma32(y[t % NB][j], pt[0][j], acc[0]); acc[1] = mfma32(y[t % NB][j], pt[1][j], acc[1]); }
#pragma unroll
    for (int j = 0; j < 2; ++j) { acc[0] = mfma32(z[t % NB][j], pm[0][j], acc[0]); acc[1] = mfma32(z[t % NB][j], pm[1][j], acc[1]); }
    __builtin_amdgcn_sched_barrier(0);
    if (t + NB < NT) { ldy(t + NB); ldz(t + NB); }
#pragma unroll
    for (int mt = 0; mt < 2; ++mt) *(LAS f4*)(wave * (32 * (NT * 64 + 16)) + (mt * 16 + n) * (NT * 64 + 16) + t * 64 + g * 16) = acc[mt];
    __builtin_amdgcn_sched_barrier(0);
  }
  LGKM0(); BAR();
  const rsrc_t rT = mkrsrc(p.ws + X1_OFF, (unsigned)(WS_END - X1_OFF));
#pragma unroll
  for (int i = 0; i < (NT + 3) / 4; ++i) {
    const int it = i * 256 + wave * 64 + lane, tok = (it / (2 * NT)) & 31, ch = it % (2 * NT);
    f4 a = {}, b = {};
#pragma unroll
    for (int w = 0; w < 4; ++w) { const unsigned ad = w * (32 * (NT * 64 + 16)) + tok * (NT * 64 + 16) + ch * 32; a += *(LAS f4*)ad; b += *(LAS f4*)(ad + 16); }
    __builtin_amdgcn_raw_buffer_store_b128(v4u{unsent(pack2(a[0], a[1])), unsent(pack2(a[2], a[3])), unsent(pack2(b[0], b[1])), unsent(pack2(b[2], b[3]))}, rT,
                                           it < 64 * NT ? (seg * 32 + tok) * 2560 + t0 * 32 + ch * 16 : OOB, 0, 16);
  }
}
static_assert(OSPLIT <= 2 * RING && OSPLIT >= NSEG - RING, "Z offset, arrival counts");
__device__ __forceinline__ void o_stream(v3u par, int id, int nb, int rep, int wave) {
  const Params p = load_params(par);
  gu32* ctrl = (gu32*)(p.ws + p.co);
  const int lane = fresh_lane(), g = lane >> 4, n = lane & 15;
  const rsrc_t rH = mkrsrc(p.ws, WS_BYTES);
  const int ta = side_a(p, slot_blk(lane)), tb = side_a(p, slot_blk(lane) + 1), tp = pt_off(p, lane);
  const int vy = (n >> 3) * 32768 + g * 128 + (n & 7) * 16, vz = (n >> 3) * 4096 + g * 128 + (n & 7) * 16;
  v4u y[5][8];
  auto ld = [&](int u, int it, int t) {
    const int sa = __builtin_amdgcn_readlane(ta, u >> 1), sb = __builtin_amdgcn_readlane(tb, u >> 1), T = (u & 1) * 20 + wave * 5 + t;
    const int so = it < 4 ? (T < 32 ? sa : sb - (1 << 21)) + T * 65536 : (sb ^ 8192) + (u >= 2 * RING ? 655360 : 0) + T * 16384;
    const int vo = it < 4 ? vy + lmap(((it + wave) & 3) * 4096) : vz;
#pragma unroll
    for (int j = 0; j < 8; ++j) y[t][j] = ld16s(rH, vo + j * 512, so);
  };
  if (wave == 0) wait_ge<8>(ctrl, C_G + rep * 32, (id >> 1) + 1);
  BAR();
#pragma unroll
  for (int t = 0; t < 5; ++t) ld(id, 0, t);
  int up = -1, srow = OOB;
  unsigned score = 0;
  RowIn ri;
  auto put_score = [&]() { __builtin_amdgcn_raw_buffer_store_b32(score, rH, n == 0 ? srow : OOB, 0, 16); };
#pragma unroll 1
  for (int u = id; u < 2 * OSPLIT; u += nb) {
    const int seg = u >> 1, un = min(u + nb, 2 * OSPLIT - 1);
    TS(id == 3 && wave == 0 && lane == 0 && u == id + 2 * nb, 40)
    if (wave == 0) wait_ge<8>(ctrl, C_G + rep * 32, seg + 4);
    BAR();
    TS(id == 3 && wave == 0 && lane == 0 && u == id + 2 * nb, 41)
    const int sp = __builtin_amdgcn_readlane(tp, seg);
#pragma unroll 1
    for (int k = 0; k < 20; ++k) dma16_sc1(rH, lane * 16, sp + lmap((wave * 20 + k) * 1024), (wave * 20 + k) * 1024);
    VMCNT(0); BAR();
    put_score();
    if (up >= 0 && wave == 1 && lane == 0) __hip_atomic_fetch_add(ctrl + C_AF + (up >> 1) * 32, 1u, RLX_AGENT);
    TS(id == 3 && wave == 0 && lane == 0 && u == id + 2 * nb, 42)
    f4 acc[2][5] = {};
#pragma unroll
    for (int it = 0; it < 5; ++it) {
      if (it == 4) {
        if (wave == 0) wait_ge<8>(ctrl, lane & 1 ? C_G + rep * 32 : C_AF + (max(up, 0) >> 1) * 32, lane & 1 ? (un >> 1) + 1 : up >= 0 ? 2 : 0);
        BAR();
        rows_load(ri, up * 16 + wave * 4);
      }
      v4u pf[2][8];
      const unsigned lp = it < 4 ? ((it + wave) & 3) * 16384 + lane * 16 : 65536 + g * 512 + n * 16;
#pragma unroll
      for (int j = 0; j < 16; ++j) pf[j & 1][j >> 1] = lds128(lp + (j >> 1) * 2048 + (j & 1) * (it < 4 ? 1024 : 256));
#pragma unroll
      for (int t = 0; t < 5; ++t) {
#pragma unroll
        for (int j = 0; j < 8; ++j) { acc[0][t] = mfma32(y[t][j], pf[0][j], acc[0][t]); acc[1][t] = mfma32(y[t][j], pf[1][j], acc[1][t]); }
        __builtin_amdgcn_sched_barrier(0);
        if (it < 4) ld(u, it + 1, t); else ld(un, 0, t);
        __builtin_amdgcn_sched_barrier(0);
      }
    }
#pragma unroll
    for (int k = 0; k < 10; ++k) {
      const f4 a = acc[k & 1][k >> 1];
      *(LAS v2u*)(81920 + ((k & 1) * 16 + n) * 656 + (wave * 5 + (k >> 1)) * 32 + g * 8) = v2u{pack2(a[0], a[1]), pack2(a[2], a[3])};
    }
    LGKM0(); BAR();
#pragma unroll
    for (int k = 0; k < 5; ++k) {
      const int c = k * 64 + lane, r = wave * 8 + c / 40, o = (c % 40) * 16;
      __builtin_amdgcn_raw_buffer_store_b128(lds128(81920 + r * 656 + o), rH, X1_OFF + (seg * 32 + r) * 2560 + (u & 1) * 640 + o, 0, 16);
    }
    __builtin_amdgcn_sched_barrier(0);
    TS(id == 3 && wave == 0 && lane == 0 && u == id + 2 * nb, 43)
    if (up >= 0) { score = rows_do(ri, up * 16 + wave * 4); srow = p.co + (C_R + up * 16 + wave * 4 + g) * 4; }
    TS(id == 3 && wave == 0 && lane == 0 && u == id + 2 * nb, 44)
    TS(up >= 0 && wave == 0 && lane == 0, 704 + (up >> 1))
    up = u;
  }
  VMCNT(0); BAR();
  put_score();
  if (wave == 0) {
    if (lane == 0) __hip_atomic_fetch_add(ctrl + C_AF + (up >> 1) * 32, 1u, RLX_AGENT);
    wait_ge<4>(ctrl, C_AF + (up >> 1) * 32, 2);
  }
  BAR();
  rows_load(ri, up * 16 + wave * 4);
  score = rows_do(ri, up * 16 + wave * 4); srow = p.co + (C_R + up * 16 + wave * 4 + g) * 4;
  VMCNT(0);
  put_score();
  TS(wave == 0 && lane == 0, 704 + (up >> 1))
}
__device__ __forceinline__ void o_role(v3u par, int u, int rep, int wave) {
#pragma unroll 1
  for (int seg = OSPLIT + u / 10; seg < NSEG; seg += 6) {
    const Params p = load_params(par);
    const int lane = fresh_lane(), row = seg * 32 + (u % 10) * 4 + wave;
    unsigned x[5];
#pragma unroll
    for (int i = 0; i < 5; ++i) x[i] = __builtin_amdgcn_raw_buffer_load_b32(mkrsrc(p.x, S * DM * 2), row * 1280 + i * 256 + lane * 4, 0, 0);
    o_unit<4>(p, seg, (u % 10) * 4, rep, lane, wave);
    TS(u % 10 == 0 && wave == 0 && lane == 0, 1500 + seg)
    if (u % 10 < 8) row_one(par, row, x);
    TS(u % 10 == 0 && wave == 0 && lane == 0, 704 + seg)
  }
}

__device__ __forceinline__ void s_role(const Params& p, int h, int rep, int lane, int wave) {
  constexpr int L_K = 24576, L_D = 30720, L_Q = 57344, L_MEM = 63488, L_RES = 73728;
  const int g = lane >> 4, n = lane & 15, q = (lane >> 2) & 3, pp = lane & 3, j0 = 4 * (g & 1) + 2 * (g >> 1);
  v32u vc; v12u sr; v16u sc;
  vc[0] = n < 8 ? 0x4444 : 0xEEEE;
  vc[1] = lane * 16;
  vc[2] = L_MEM + lane * 16;
  vc[3] = L_RES + wave * 22528 + lane * 16;
  vc[4] = 0;
  vc[5] = L_Q + g * 1024 + q * 128 + (4 * g + q) * 8 + pp * 2;
  vc[6] = L_K + g * 128 + (n ^ (8 * (g >> 1))) * 8;
  vc[7] = lane < 63 ? side_a(p, slot_blk(lane + 1) + 1) : OOB;
  vc[8] = lmap(KQ_OFF + h * 24576) + lane * 16;
  vc[9] = L_K + pp * 128 + ((4 * g + q) ^ (8 * (pp >> 1))) * 8;
  vc[10] = 0;
  vc[11] = hand_off(p, lane) + rep * 2 * H_COPY;
  vc[12] = L_D + lane * 4;
  const int kt = wave == 2, tok = (g * 16 + 8 * kt + 4 * (q >> 1) + pp) * 16;
  if (wave == 1 || wave == 2) {
    vc[13] = n - 4 * g;
    vc[14] = lane == 0 ? p.co + (C_PT + (h & 7) * 32) * 4 : OOB;
    vc[15] = pt_off(p, lane);
    vc[16] = lmap(h * 2048 + kt * 1024 + lane * 16);
    vc[17] = lmap(65536 + h * 512 + (16 * kt + n) * 16 + j0 * 2);
    vc[18] = lane * 16;
  } else {
    vc[13] = lmap(H_P) + (n < 8 ? h * 128 + n * 16 + j0 * 2 : 4096 + (h >> 3) * 1024 + ((2 * (((h >> 1) ^ h) & 1) + (g & 1)) * 16 + (h & 1) * 8 + n - 8) * 16 + ((h >> 2) & 1) * 8 + (g >> 1) * 4);
    vc[14] = j0 == (n & 7) ? 0x3f800000u : 0u;
    vc[15] = j0 + 1 == (n & 7) ? 0x3f800000u : 0u;
    vc[16] = (h & 7) == 0 && lane < 8 ? p.co + (C_G0 + rep * 32) * 4 + lane * 16 : OOB; vc[17] = 0;
    vc[18] = flag_off(p, h) + (lane % 12) * 4;
  }
  if (wave == 3) { vc[13] = lane < 4 ? lane * 2 * H_COPY + lmap(H_NOTE + rep * 128) : OOB; vc[14] = look_off(lane); }
  vc[19] = 0;
  vc[20] = (q & 1) * 1024 + tok;
  vc[21] = 16384 + (q & 1) * 8 + tok;
  vc[22] = lmap(H_E + h * 512 + (2 * kt + (g >> 1)) * 128 + (n & 7) * 16 + (g & 1) * 8 + (n >> 3) * 4);
  vc[23] = 0;
#pragma unroll
  for (int k = 0; k < 3; ++k) { const int c = k * 64 + lane; vc[24 + k] = ((16 * wave + c / 24) * HD + h * DH) * 2 + ((c % 24) ^ (c / 24 & 4)) * 16 - k * 1024; }
  vc[27] = (16 * (g >> 1) + 8 * (q >> 1) + 4 * (g & 1) + (q & 1)) * 384 + (((32 * wave + 8 * pp) * 2) ^ (64 * (g & 1)));
  vc[28] = lane == 0 ? p.co + (C_ANCH + 128 + rep * 32) * 4 : OOB;
  vc[29] = p.co + (C_ANCH + rep * 32 + lane % 3) * 4;
  vc[30] = (16 * (g >> 1) + 8 * (q >> 1) + 4 * (g & 1) + (q & 1)) * 384 + (((128 + 16 * wave + 4 * pp) * 2) ^ (64 * (g & 1))); vc[31] = 0;
  set_rsrc(sr, 0, p.ws, WS_BYTES); set_rsrc(sr, 1, p.wq, DM * HD * 2); set_rsrc(sr, 2, p.wk, DM * HD * 2);
#pragma unroll
  for (int i = 4; i < 16; ++i) sc[i] = 0;
  sc[0] = wave; sc[1] = p.dL + p.dM; sc[2] = p.dS; sc[3] = rfl(side_a(p, 1)); sc[9] = p.dL * 23 / 4 + KQ_DELAY - 250; sc[10] = rfl(rep * 2 * H_COPY);
#pragma unroll
  for (int k = 1; k < 4; ++k) sc[5 + k] = rfl((((rep + k) & 3) - rep) * 2 * H_COPY);
  pin(sc); pin(sr);
  asm volatile(SROLE_ASM : "+{s[52:67]}"(sc), "+{v[0:31]}"(vc), "+{s[36:47]}"(sr) :: "memory", "vcc", "scc", SROLE_CLOBBERS);
  gu32* ctrl = (gu32*)(fresh_params()->ws + p.co);
  if (fresh_lane() == 0) {
    if (sc[1] > 20000u || sc[4] > 20000u) __hip_atomic_store(ctrl + C_ERR, 1u, RLX_AGENT);
  }
}

__device__ __forceinline__ void c_role(const Params& p, int c8, int rep, int lane, int wave) {
  constexpr int L_Z = 10240;
  const int g = lane >> 4, n = lane & 15, j0 = 4 * (g & 1) + 2 * (g >> 1), hi = c8 >> 6;
  v32u vc; v4u sr; v16u sc;
  OPAQUE(vc);
  vc[0] = n < 8 ? 0x4444 : 0xEEEE;
  vc[1] = lane * 16;
  vc[2] = L_Z + (g >> 1) * 256 + n * 16 + (g & 1) * 8;
  vc[3] = L_Z + n * 16 + j0 * 2;
  vc[4] = hand_off(p, lane) + rep * 2 * H_COPY;
  vc[5] = (c8 & 63) * 32768 + (n >> 3) * 512 + g * 128 + (n & 7) * 16;
  vc[6] = side_a(p, 2 * (USLOT + lane % 20) + hi);
  vc[7] = lane == 0 && (wave == 0 || wave == 3) ? p.co + (C_ANCH + 128 + rep * 32) * 4 : OOB;
  vc[8] = p.co + (C_ANCH + rep * 32 + lane % 3) * 4;
  if (wave == 3) {
    vc[9] = side_a(p, slot_blk(lane) + hi);
    vc[10] = (n >> 3) * 512 + g * 128 + (n & 7) * 16;
    vc[11] = vc[10] ^ 512;
  } else {
    vc[9] = z_off(p, lane);
    vc[10] = lmap(c8 * 4096) + lane * 16;
    vc[11] = lane == 0 ? p.co + (C_Z + (c8 & 7) * 32) * 4 : OOB;
  }
  vc[12] = ((n & 7) >> 1) - 2 * (g & 1);
  vc[13] = lmap(c8 * 128) + (n & 7) * 16 + j0 * 2 + ((rep + (n >> 3)) & 3) * 2 * H_COPY;
  if (wave == 3) vc[2] = lmap(c8 * 128) + (n & 7) * 16 + j0 * 2 + ((rep + 2 + (n >> 3)) & 3) * 2 * H_COPY;
  vc[14] = p.co + (C_U + (lane % 12) * 32) * 4;
  vc[15] = 32;
#pragma unroll
  for (int i = 0; i < 5; ++i) vc[18 + i] = vc[5] + lmap((wave * 5 + i) * 1024);
  vc[16] = lane < 4 ? lane * 2 * H_COPY + lmap(H_NOTE + (4 + rep) * 128) : OOB; vc[17] = look_off(lane);
  set_rsrc(sr, 0, p.ws, WS_BYTES);
#pragma unroll
  for (int i = 4; i < 16; ++i) sc[i] = 0;
  sc[0] = wave; sc[1] = p.dL + p.dM; sc[2] = p.dS; sc[3] = p.dZ; sc[8] = rfl(rep * 2 * H_COPY);
  pin(sc); pin(sr);
  asm volatile(CROLE_ASM : "+{s[52:67]}"(sc), "+{v[0:31]}"(vc) : "{s[36:39]}"(sr) : "memory", "vcc", "scc", CROLE_CLOBBERS);
  gu32* ctrl = (gu32*)(fresh_params()->ws + p.co);
  if (fresh_lane() == 0) {
    if (sc[1] > 20000u || sc[5] > 20000u) __hip_atomic_store(ctrl + C_ERR, 1u, RLX_AGENT);
  }
}

__device__ __forceinline__ void start_chain(const Params& p, int lane) {
  gu32* ctrl = (gu32*)(p.ws + p.co);
  const int ho = hand_off(p, lane);
  const rsrc_t rH = mkrsrc(p.ws, WS_BYTES);
  wait_ge<1>(ctrl, C_ANCH + 128 + (lane & 3) * 32, 28);
  unsigned A = now_ticks() * 4 + p.lead;
  int x = XSTART, lev = 0;
  unsigned due = 0;
  float inv = 1.f / FSTART;
  auto plan = [&](int s) {
    const float sl = inv - 1.f / 23.f;
    const int dE = p.dE + lev + (int)(4.f * SLOPE_E * sl), dL = p.dL + lev + (int)(4.f * SLOPE_L * sl), dM = p.dM + lev / 2 + (int)(4.f * SLOPE_M * sl);
    const v4u v = {A | (s == 0), (A + x + dE) | (s == 0), (A + x + dL) | (s == 0), 0u};
    const int hs = __builtin_amdgcn_readlane(ho, (s - 1) & 63);
    __builtin_amdgcn_raw_buffer_store_b128(v, rH, lane >= 32 ? OOB : (s ? hs + (lane >> 3) * 2 * H_COPY + lmap(H_PLAN) : p.co + (C_ANCH + (lane >> 3) * 32) * 4) + (lane & 7) * 16, 0, 16);
    if (lane == s) due = A + x + dL;
    A += dL + dM + 2 * x; x >>= 1;
  };
#pragma unroll 1
  for (int s = 0; s <= HORIZON; ++s) plan(s);
  unsigned e1 = 0, a1 = 0, e2 = 0, a2 = 0;
#pragma unroll 1
  for (int g = 1, n1 = 0, n2 = 0, n3 = 0, calm = 0; g <= NSEG; ++g) {
    v4u w;
    {
      const unsigned t = __builtin_amdgcn_readlane(due, g - 1) + n1 * p.dS + GLOOK;
#pragma unroll 1
      while ((unsigned)(t - now_ticks() * 4) - 1u < 4000u) __builtin_amdgcn_s_sleep(1);
    }
#pragma unroll 1
    for (int spin = 0;; ++spin) {
      asm volatile("" ::: "memory");
      w = ld16s(rH, p.co + (C_G0 + (lane & 3) * 32) * 4, 0);
      if (__builtin_amdgcn_ballot_w64((int)w[0] < g << 16) == 0) break;
      if (spin > SPIN_CAP) { __hip_atomic_store(ctrl + C_ERR, 1u, RLX_AGENT); break; }
      __builtin_amdgcn_s_sleep(GPOLL);
    }
    if (lane < 16) __hip_atomic_store(ctrl + C_G + lane * 32, (unsigned)g, RLX_AGENT);
    const int n = rfl(w[0]) & 0xffff;
    if (n > n1 && n1 > n2 && n2 > n3 && g >= calm && g < NSEG) {
      const int hs = __builtin_amdgcn_readlane(ho, g);
      if (lane < 4) __hip_atomic_store(ctrl + ((long)hs + lane * 2 * H_COPY + lmap(H_PLAN) - p.co) / 4 + 3, (unsigned)(n + MEET), RLX_AGENT);
      calm = g + 4;
    }
    if (n > n1 && n1 == n2) lev += LEVUP;
    if (g > 2 && n == n2) {
      float r = 0.25f * (float)(w[2] - a2) / (float)(w[1] - e2);
      r = fmaxf(r, u2f(__builtin_amdgcn_update_dpp(0, (int)f2u(r), 0xb1, 0xf, 0xf, true))); r = fmaxf(r, u2f(__builtin_amdgcn_update_dpp(0, (int)f2u(r), 0x4e, 0xf, 0xf, true)));
      r = fminf(fmaxf(r, 1.f / 25.f), 1.f / 15.f);
      inv = fmaxf(r, inv + (r - inv) * FDECAY);
    }
    n3 = n2; n2 = n1; n1 = n; e2 = e1; a2 = a1; e1 = w[1]; a1 = w[2];
    if (g + HORIZON < NSEG) plan(g + HORIZON);
  }
}

__global__ void __launch_bounds__(256, 1) k_main(Params p0) {
  extern __shared__ __attribute__((aligned(16))) unsigned char smem[];
  int job = (blockIdx.x & 7) * 32 + (blockIdx.x >> 3) | rfl(threadIdx.x >> 6) << 8;
  unsigned epoch = __hip_atomic_load((gu32*)(p0.ws + EPOCH_OFF), RLX_AGENT);
  {
    unsigned long long pc;
    asm volatile("s_getpc_b64 s[40:41]\n\ts_add_u32 s40, s40, _Z6k_main6Params@rel32@lo+4\n\ts_addc_u32 s41, s41, _Z6k_main6Params@rel32@hi+12" : "={s[40:41]}"(pc) :: "scc");
    const int lane = fresh_lane(), wv = job >> 8;
    const rsrc_t r = wv == 3 ? mkrsrc((const void*)pc, CODE_BYTES) : mkrsrc(p0.ws, WS_BYTES);
    const int vo = wv == 3 ? (lane * 32 + (job & 31)) * 64 : wv < 2 ? ((wv * 64 + lane) << 21) + 0x1FF000 + (job & 31) * 128 : OOB;
    asm volatile("s_mov_b32 m0, %0\n\ts_nop 3\n\tbuffer_load_dword %1, %2, 0 offen lds" ::"s"(MAIN_LDS - 256), "v"(vo), "s"(r) : "memory");
  }
  v3u par;
#define JOB() job = rfl(job); asm volatile("" : "+s"(job)); const int wv = job >> 8, jb = job & 255, jx = jb >> 5, jt = jb & 31, type = jb & 3, head = (jb - 128) >> 2; (void)jx; (void)jt; (void)type; (void)head;
#define TSJ(i) { JOB() const Params p = load_params(par); TS(wv == 0 && fresh_lane() == 0, (i) + jb) }
  {
    JOB()
    if (jb >= 128) {
      v32u wa[7]; v16u wa7;
      par = token_start(wa, wa7, epoch, type, head, fresh_lane(), wv);
      pin(par);
      if (type < 3) proj_role(wa, wa7, load_params(par), type, head, fresh_lane(), wv);
      else vo_role(wa, wa7, load_params(par), head, fresh_lane(), wv);
    } else if (jt < 8) par = score_start(epoch, jx * 8 + jt, fresh_lane(), wv);
    else par = u_role(epoch, ((jx + (jt - 8) / 3) & 3) * 8 + (jt - 8) / 3, (jt - 8) % 3, fresh_lane(), wv);
    pin(par);
    TSJ(256)
  }
  JOB()
  if (jb < 128 && jt < 28) {
    if (jt < 8) s_role(load_params(par), jx * 8 + jt, jx, fresh_lane(), wv);
    else c_role(load_params(par), jx * 20 + jt - 8, jx, fresh_lane(), wv);
  } else {
    if (jb == 127) { if (wv == 0) start_chain(load_params(par), fresh_lane()); else if (wv == 1) select_rows(par, fresh_lane()); }
    JOB()
    const int u = type * 32 + head;
    if (jb < 127 || (jb > 127 && u < 60)) {
      row_params(par, wv);
      if (jb < 127) o_stream(par, jx * 4 + jt - 28, 15, jb & 15, wv); else o_role(par, u, jb & 15, wv);
    } else if (jb > 127 && u < 108) ffn_role(load_params(par), u - 60, fresh_lane(), wv);
  }
  TSJ(1024)
}

#define SCHED_DE 380
#define SCHED_DL 452
#define SCHED_DM 180
#define SCHED_DZ 96
#define SCHED_DS 400
#define SCHED_LEAD 600

extern "C" void kernel_launch(void* const* d_in, const int* in_sizes, int n_in, void* d_out, int out_size, void* d_ws, size_t ws_size,
                              hipStream_t stream) {
  Params p;
  p.x = (const u16*)d_in[0]; p.mem0 = (const u16*)d_in[1]; p.wq = (const u16*)d_in[2]; p.wk = (const u16*)d_in[3];
  p.wv = (const u16*)d_in[4]; p.wo = (const u16*)d_in[5]; p.wr = (const u16*)d_in[8]; p.wg = (const u16*)d_in[9];
  p.wu = (const u16*)d_in[10]; p.wd = (const u16*)d_in[11]; p.c = (const u16*)d_in[12]; p.wmod = (const u16*)d_in[13];
  p.out = (u16*)d_out;
  p.ws = (unsigned char*)(((uintptr_t)d_ws + ws_size - WS_BYTES) & ~(uintptr_t)(2 * MB - 1));
  (void)hipFuncSetAttribute((const void*)k_main, hipFuncAttributeMaxDynamicSharedMemorySize, MAIN_LDS);
  p.dE = SCHED_DE; p.dL = SCHED_DL; p.dM = SCHED_DM; p.dZ = SCHED_DZ; p.dS = SCHED_DS; p.lead = SCHED_LEAD;
  p.m0 = 0; p.m1 = 0; p.m2 = 0; p.co = 0;
  k_main<<<256, 256, MAIN_LDS, stream>>>(p);
}
```

```cpp
#include <hip/hip_runtime.h>
#include <hip/hip_bf16.h>
#include <stdint.h>
#include <type_traits>

#define GAS __attribute__((address_space(1)))
#define LAS __attribute__((address_space(3)))
#define RLX_AGENT __ATOMIC_RELAXED, __HIP_MEMORY_SCOPE_AGENT
typedef unsigned short u16;
typedef unsigned v4u __attribute__((ext_vector_type(4)));
typedef unsigned v2u __attribute__((ext_vector_type(2)));
typedef unsigned v8u __attribute__((ext_vector_type(8)));
typedef float f4 __attribute__((ext_vector_type(4)));
typedef __bf16 bf2 __attribute__((ext_vector_type(2)));
typedef __bf16 bf8 __attribute__((ext_vector_type(8)));
typedef __bf16 bf16v __attribute__((ext_vector_type(16)));
typedef short s4 __attribute__((ext_vector_type(4)));
typedef GAS unsigned gu32;
typedef __amdgpu_buffer_rsrc_t rsrc_t;

constexpr int S = 2048, DM = 640, NH = 32, DH = 192, HD = NH * DH, NSEG = 64, FF = 1536, TOPK = 8;

constexpr size_t MB = 1u << 20;
constexpr int RING = 32, HRING = 24, USLOT = 12, HB = 64, PB = 68, LB = 74;
__host__ __device__ constexpr int slot_blk(int s) { return 2 * (s % RING); }
constexpr int NCAL = LB + 1, NBLK = LB + 6;
constexpr unsigned WS_BYTES = (unsigned)NBLK << 21;
constexpr int CTRL_OFF = LB << 21;
constexpr int CTRL_BYTES = 131072;
constexpr int MODB_OFF = CTRL_OFF + CTRL_BYTES;
constexpr int MODG_OFF = MODB_OFF + 8192;
constexpr int HF_OFF = CTRL_OFF + (1 << 21);
constexpr int HF_BYTES = S * DM * 2;
constexpr int X1_OFF = HF_OFF + HF_BYTES;
constexpr int ACT_OFF = X1_OFF + 2 * HF_BYTES;
constexpr int WS_END = ACT_OFF + 2 * TOPK * FF * 2;
constexpr int TRACE_OFF = (LB + 5) << 21;
static_assert(WS_END <= TRACE_OFF, "workspace");
constexpr int H_COPY = 40960, H_STEP = 4 * H_COPY, H_FLAG = 10240, H_NOTE = 14336, H_PLAN = 15360, H_E = 16384, H_P = 32768, KQ_OFF = 262144, PT_SEG = 81920;

constexpr int C_ANCH = 0;
constexpr int C_IDX = 256;
constexpr int C_LN = 1152;
constexpr int C_MOD = 1408;
constexpr int C_PT = 1536;
constexpr int C_Z = 1792;
constexpr int C_G = 4096;
constexpr int C_G0 = 4608;
constexpr int C_ERR = 4736;
constexpr int C_VF = 5120;
constexpr int C_AF = 8192;
constexpr int C_U = 12288;
constexpr int C_R = 16384;
constexpr int ZERO_BYTES = 65536;
constexpr int TAB_OFF = CTRL_OFF + 0x1C000, SUM_OFF = TAB_OFF + 0x3800, EPOCH_OFF = TAB_OFF + 0x3F00, NTAB = NCAL + 32;
static_assert(NTAB <= 112 && NCAL <= 96, "table");
constexpr int DBG_OFF = CTRL_OFF + 262144, C_STAT = 0, C_DBG = 2048;
__host__ __device__ constexpr int lmap(int off) { return ((off >> 13) << 14) | (off & 8191); }
constexpr int OOB = 0x40000000;

__device__ __forceinline__ float u2f(unsigned v) { return __builtin_bit_cast(float, v); }
__device__ __forceinline__ unsigned f2u(float v) { return __builtin_bit_cast(unsigned, v); }
__device__ __forceinline__ float bf2f(u16 v) { return __builtin_bit_cast(float, (unsigned)v << 16); }
__device__ __forceinline__ float lo2f(unsigned v) { return __builtin_bit_cast(float, v << 16); }
__device__ __forceinline__ float hi2f(unsigned v) { return __builtin_bit_cast(float, v & 0xffff0000u); }
__device__ __forceinline__ unsigned pack2(float a, float b) { bf2 v = {(__bf16)a, (__bf16)b}; return __builtin_bit_cast(unsigned, v); }
__device__ __forceinline__ float rbf(float a) { return (float)(__bf16)a; }
__device__ __forceinline__ unsigned long long uni64(unsigned long long a) {
  return (unsigned)__builtin_amdgcn_readfirstlane((unsigned)a) | (unsigned long long)(unsigned)__builtin_amdgcn_readfirstlane((unsigned)(a >> 32)) << 32;
}
__device__ __forceinline__ rsrc_t mkrsrc(const void* p, unsigned bytes) { return __builtin_amdgcn_make_buffer_rsrc((void*)uni64((unsigned long long)p), 0, bytes, 0x00020000); }
__device__ __forceinline__ unsigned xcc_id() { return (unsigned)__builtin_amdgcn_s_getreg(20 | (3 << 11)) & 0xfu; }
__device__ __forceinline__ int rfl(int v) { return __builtin_amdgcn_readfirstlane(v); }
__device__ __forceinline__ f4 mfma32(v4u a, v4u b, f4 c) { return __builtin_amdgcn_mfma_f32_16x16x32_bf16(__builtin_bit_cast(bf8, a), __builtin_bit_cast(bf8, b), c, 0, 0, 0); }
__device__ __forceinline__ f4 mfma16(v2u a, v2u b, f4 c) { return mfma32(v4u{a[0], a[1], 0u, 0u}, v4u{b[0], b[1], 0u, 0u}, c); }
__device__ __forceinline__ f4 smfma(v4u a, v8u b, f4 c, int idx) { return __builtin_amdgcn_smfmac_f32_16x16x64_bf16(__builtin_bit_cast(bf8, a), __builtin_bit_cast(bf16v, b), c, idx, 0, 0); }
__device__ __forceinline__ v2u trread(unsigned addr) { return __builtin_bit_cast(v2u, __builtin_amdgcn_ds_read_tr16_b64_v4i16((LAS s4*)addr)); }
__device__ __forceinline__ v4u lds128(unsigned addr) { return *(LAS v4u*)addr; }
__device__ __forceinline__ v2u lds64(unsigned addr) { return *(LAS v2u*)addr; }
__device__ __forceinline__ void dma16(rsrc_t r, int voff, int soff, unsigned ldsaddr) {
  asm volatile("s_mov_b32 m0, %0\n\ts_nop 3\n\tbuffer_load_dwordx4 %1, %2, %3 offen lds" ::"s"(ldsaddr), "v"(voff), "s"(r), "s"(soff) : "memory");
}
__device__ __forceinline__ void dma16_sc1(rsrc_t r, int voff, int soff, unsigned ldsaddr) {
  asm volatile("s_mov_b32 m0, %0\n\ts_nop 3\n\tbuffer_load_dwordx4 %1, %2, %3 offen sc1 lds" ::"s"(ldsaddr), "v"(voff), "s"(r), "s"(soff) : "memory");
}
#define VMCNT_(n) asm volatile("s_waitcnt vmcnt(" #n ")" ::: "memory")
#define VMCNT(n) VMCNT_(n)
#define LGKM0() asm volatile("s_waitcnt lgkmcnt(0)" ::: "memory")
#define BAR() asm volatile("s_barrier" ::: "memory")
__device__ __forceinline__ float swapsum32(float a, float b) {
  auto r = __builtin_amdgcn_permlane32_swap(__builtin_bit_cast(unsigned, a), __builtin_bit_cast(unsigned, b), false, false);
  return u2f(r[0]) + u2f(r[1]);
}
__device__ __forceinline__ float swapsum16(float a, float b) {
  auto r = __builtin_amdgcn_permlane16_swap(__builtin_bit_cast(unsigned, a), __builtin_bit_cast(unsigned, b), false, false);
  return u2f(r[0]) + u2f(r[1]);
}
__device__ __forceinline__ float max_x32(float a) {
  auto r = __builtin_amdgcn_permlane32_swap(__builtin_bit_cast(unsigned, a), __builtin_bit_cast(unsigned, a), false, false);
  return fmaxf(u2f(r[0]), u2f(r[1]));
}
__device__ __forceinline__ float max_x16(float a) {
  auto r = __builtin_amdgcn_permlane16_swap(__builtin_bit_cast(unsigned, a), __builtin_bit_cast(unsigned, a), false, false);
  return fmaxf(u2f(r[0]), u2f(r[1]));
}
__device__ __forceinline__ unsigned ror8u(unsigned v) { return (unsigned)__builtin_amdgcn_update_dpp(0, (int)v, 0x128, 0xf, 0xf, true); }
__device__ __forceinline__ float ror8f(float v) { return __builtin_bit_cast(float, ror8u(__builtin_bit_cast(unsigned, v))); }
__device__ __forceinline__ float wave_sum(float v) {
  v += u2f(__builtin_amdgcn_update_dpp(0, (int)f2u(v), 0xb1, 0xf, 0xf, true)); v += u2f(__builtin_amdgcn_update_dpp(0, (int)f2u(v), 0x4e, 0xf, 0xf, true));
  v += u2f(__builtin_amdgcn_update_dpp(0, (int)f2u(v), 0x124, 0xf, 0xf, true)); v += ror8f(v);
  v = swapsum16(v, v);
  return swapsum32(v, v);
}
typedef unsigned short us2 __attribute__((ext_vector_type(2)));
__device__ __forceinline__ unsigned unsent(unsigned w) { return __builtin_bit_cast(unsigned, __builtin_elementwise_min(__builtin_bit_cast(us2, w), us2{0xfffe, 0xfffe})); }

constexpr int OSPLIT = 52;
constexpr unsigned CODE_BYTES = 126976;
struct Params {
  const u16 *x, *mem0, *wq, *wk, *wv, *wo, *wr, *wg, *wu, *wd, *c, *wmod;
  u16* out;
  unsigned char* ws;
  int dE, dL, dM, dZ, dS, lead;
  unsigned m0, m1, m2;
  int co;
};

#define XSTART 32
#define MEET 3
#define AHEAD 11
static_assert(AHEAD + 9 <= HRING, "hand-off ring");
#define SDELAY 0
#define KDELAY 0
#define FSTART 21.f
#define FDECAY 0.2f
#define LEVUP 4
#define SLOPE_E 1600.f
#define SLOPE_L 1900.f
#define SLOPE_M 500.f
#define HORIZON 2
#define GLOOK 60
#define CALREP 4
#define SUMWAIT 60
#define CALWIN 14
#define MODAUX 2
#define WARMAT 0
#define GPOLL 12
#define KQ_DELAY -100
constexpr int MAIN_LDS = 163840;
constexpr int SPIN_CAP = 1 << 18;
typedef unsigned v12u __attribute__((ext_vector_type(12)));
typedef unsigned v16u __attribute__((ext_vector_type(16)));
typedef unsigned v32u __attribute__((ext_vector_type(32)));

#define SROLE_CLOBBERS \
  "v32", "v33", "v34", "v35", "v36", "v37", "v38", "v39", "v40", "v41", "v42", "v43", "v44", "v45", "v46", "v47", "v48", "v49", "v50", "v51", "v52", \
  "v53", "v54", "v55", "v56", "v57", "v58", "v59", "v60", "v61", "v62", "v63", "v64", "v65", "v66", "v67", "v68", "v69", "v70", "v71", "v72", "v73", \
  "v74", "v75", "v76", "v77", "v78", "v79", "v80", "v81", "v82", "v83", "v84", "v85", "v86", "v87", "v88", "v89", "v90", "v91", "v92", "v93", "v94", \
  "v95", "v96", "v97", "v98", "v99", "v100", "v101", "v102", "v103", "v104", "v105", "v106", "v107", "v108", "v109", "v110", "v111", "v112", "v113", \
  "v114", "v115", "v116", "v117", "v118", "v119", "v120", "v121", "v122", "v123", "v124", "v125", "v126", "v127", "v128", "v129", "v130", "v131", \
  "v132", "v133", "v134", "v135", "v136", "v137", "v138", "v139", "v140", "v141", "v142", "v143", "v144", "v145", "v146", "v147", "v148", "v149", \
  "v150", "v151", "v152", "v153", "v154", "v155", "v156", "v157", "v158", "v159", "v160", "v161", "v162", "v163", "v164", "v165", "v166", "v167", \
  "v168", "v169", "v170", "v171", "v172", "v173", "v174", "v175", "v176", "v177", "v178", "v179", "v180", "v181", "v182", "v183", "v184", "v185", \
  "v186", "v187", "v188", "v189", "v190", "v191", "v192", "v193", "v194", "v195", "v196", "v197", "v198", "v199", "v200", "v201", "v202", "v203", \
  "v204", "v205", "v206", "v207", "v208", "v209", "v210", "v211", "v212", "v213", "v214", "v215", "v216", "v217", "v218", "v219", "v220", "v221", \
  "v222", "v223", "v224", "v225", "v226", "v227", "v228", "v229", "v230", "v231", "v232", "v233", "v234", "v235", "v236", "v237", "v238", "v239", \
  "v240", "v241", "v242", "v243", "v244", "v245", "v246", "v247", "v248", "v249", "v250", "v251", "v252", "v253", "v254", "v255", "s34", "s35", "s48", \
  "s49", "s50", "s51", "s68", "s69", "s70", "s71", "s72", "s73", "s74", "s75", "s76", "s77", "s78", "s79", "s80", "s81", "s82", "s83", "s84", "s85", \
  "s86", "s87", "s88", "s89", "s90", "s93", "s94", "s95", "s96", "s97", "s98", "a0", "a1", "a2", "a3", "a4", "a5", "a6", "a7", "a8", "a9", "a10", "a11", \
  "a12", "a13", "a14", "a15", "a16", "a17", "a18", "a19", "a20", "a21", "a22", "a23", "a24", "a25", "a26", "a27", "a28", "a29", "a30", "a31", "a32", \
  "a33", "a34", "a35", "a36", "a37", "a38", "a39", "a40", "a41", "a42", "a43", "a44", "a45", "a46", "a47", "a48", "a49", "a50", "a51", "a52", "a53", \
  "a54", "a55", "a56", "a57", "a58", "a59", "a60", "a61", "a62", "a63", "a64", "a65", "a66", "a67", "a68", "a69", "a70", "a71", "a72", "a73", "a74", \
  "a75", "a76", "a77", "a78", "a79", "a80", "a81", "a82", "a83", "a84", "a85", "a86", "a87", "a88", "a89", "a90", "a91", "a92", "a93", "a94", "a95", \
  "a96", "a97", "a98", "a99", "a100", "a101", "a102", "a103", "a104", "a105", "a106", "a107", "a108", "a109", "a110", "a111", "a112", "a113", "a114", \
  "a115", "a116", "a117", "a118", "a119", "a120", "a121", "a122", "a123", "a124", "a125", "a126", "a127", "a128", "a129", "a130", "a131", "a132", \
  "a133", "a134", "a135", "a136", "a137", "a138", "a139", "a140", "a141", "a142", "a143", "a144", "a145", "a146", "a147", "a148", "a149", "a150", \
  "a151", "a152", "a153", "a154", "a155", "a156", "a157", "a158", "a159", "a160", "a161", "a162", "a163", "a164", "a165", "a166", "a167", "a168", \
  "a169", "a170", "a171", "a172", "a173", "a174", "a175", "a176", "a177", "a178", "a179", "a180", "a181", "a182", "a183", "a184", "a185", "a186", \
  "a187", "a188", "a189", "a190", "a191", "a192", "a193", "a194", "a195", "a196", "a197", "a198", "a199", "a200", "a201", "a202", "a203", "a204", \
  "a205", "a206", "a207", "a208", "a209", "a210", "a211", "a212", "a213", "a214", "a215", "a216", "a217", "a218", "a219", "a220", "a221", "a222", \
  "a223", "a224", "a225", "a226", "a227", "a228", "a229", "a230", "a231", "a232", "a233", "a234", "a235", "a236", "a237", "a238", "a239", "a240", \
  "a241", "a242", "a243", "a244", "a245", "a246", "a247", "a248", "a249", "a250", "a251", "a252", "a253", "a254", "a255"
#define SROLE_ASM \
  "s_mov_b32 s34, 0x00ff00ff\n" \
  "s_mov_b32 s35, 0x00ff00ff\n" \
  "s_mov_b32 s68, 0\n" \
  "s_mov_b32 s73, 0\n" \
  "s_mov_b32 s56, 0\n" \
  "s_mov_b32 s57, 0\n" \
  "s_mov_b32 s51, 0\n" \
  "s_mov_b32 s71, 0\n" \
  "s_mov_b32 s50, 5900\n" \
  "v_mov_b32 v4, 0x3dd53b94\n" \
  "s_mov_b32 s93, s58\n" \
  "s_mov_b32 s94, s59\n" \
  "s_mov_b32 s95, s60\n" \
  "s_mov_b32 s96, s61\n" \
  "s_mov_b32 s58, 0\n" \
  "s_mov_b32 s59, 0\n" \
  "s_mov_b32 s60, 0\n" \
  "s_mov_b32 s61, 0\n" \
  "s_mov_b32 s62, 0\n" \
  "s_mov_b32 s63, 0\n" \
  "s_mov_b32 s64, 0\n" \
  "s_mov_b32 s65, 0\n" \
  "s_mov_b32 s66, 0\n" \
  "s_mov_b32 s67, 0\n" \
  "s_memrealtime s[76:77]\n" \
  "s_memtime s[78:79]\n" \
  "s_waitcnt lgkmcnt(0)\n" \
  "s_mov_b32 s88, s76\n" \
  "s_mov_b32 s89, s78\n" \
  "s_mul_i32 s72, s52, 6144\n" \
  "s_add_u32 m0, s72, 0\n" \
  "s_mov_b32 s74, 0\n" \
  "buffer_load_dwordx4 v24, s[40:43], s74 offen offset:0 lds\n" \
  "buffer_load_dwordx4 v25, s[40:43], s74 offen offset:1024 lds\n" \
  "buffer_load_dwordx4 v26, s[40:43], s74 offen offset:2048 lds\n" \
  "s_add_u32 m0, s72, 3072\n" \
  "s_mov_b32 s74, 98304\n" \
  "buffer_load_dwordx4 v24, s[40:43], s74 offen offset:0 lds\n" \
  "buffer_load_dwordx4 v25, s[40:43], s74 offen offset:1024 lds\n" \
  "buffer_load_dwordx4 v26, s[40:43], s74 offen offset:2048 lds\n" \
  "s_add_u32 m0, s72, 24576\n" \
  "s_mov_b32 s74, 0\n" \
  "buffer_load_dwordx4 v24, s[44:47], s74 offen offset:0 lds\n" \
  "buffer_load_dwordx4 v25, s[44:47], s74 offen offset:1024 lds\n" \
  "buffer_load_dwordx4 v26, s[44:47], s74 offen offset:2048 lds\n" \
  "s_add_u32 m0, s72, 27648\n" \
  "s_mov_b32 s74, 98304\n" \
  "buffer_load_dwordx4 v24, s[44:47], s74 offen offset:0 lds\n" \
  "buffer_load_dwordx4 v25, s[44:47], s74 offen offset:1024 lds\n" \
  "buffer_load_dwordx4 v26, s[44:47], s74 offen offset:2048 lds\n" \
  "s_add_u32 m0, s72, 49152\n" \
  "s_mov_b32 s74, 786432\n" \
  "buffer_load_dwordx4 v24, s[40:43], s74 offen offset:0 lds\n" \
  "buffer_load_dwordx4 v25, s[40:43], s74 offen offset:1024 lds\n" \
  "buffer_load_dwordx4 v26, s[40:43], s74 offen offset:2048 lds\n" \
  "s_add_u32 m0, s72, 52224\n" \
  "s_mov_b32 s74, 884736\n" \
  "buffer_load_dwordx4 v24, s[40:43], s74 offen offset:0 lds\n" \
  "buffer_load_dwordx4 v25, s[40:43], s74 offen offset:1024 lds\n" \
  "buffer_load_dwordx4 v26, s[40:43], s74 offen offset:2048 lds\n" \
  "s_add_u32 m0, s72, 73728\n" \
  "s_mov_b32 s74, 786432\n" \
  "buffer_load_dwordx4 v24, s[44:47], s74 offen offset:0 lds\n" \
  "buffer_load_dwordx4 v25, s[44:47], s74 offen offset:1024 lds\n" \
  "buffer_load_dwordx4 v26, s[44:47], s74 offen offset:2048 lds\n" \
  "s_add_u32 m0, s72, 76800\n" \
  "s_mov_b32 s74, 884736\n" \
  "buffer_load_dwordx4 v24, s[44:47], s74 offen offset:0 lds\n" \
  "buffer_load_dwordx4 v25, s[44:47], s74 offen offset:1024 lds\n" \
  "buffer_load_dwordx4 v26, s[44:47], s74 offen offset:2048 lds\n" \
  "s_add_u32 m0, s72, 98304\n" \
  "s_mov_b32 s74, 1572864\n" \
  "buffer_load_dwordx4 v24, s[40:43], s74 offen offset:0 lds\n" \
  "buffer_load_dwordx4 v25, s[40:43], s74 offen offset:1024 lds\n" \
  "buffer_load_dwordx4 v26, s[40:43], s74 offen offset:2048 lds\n" \
  "s_add_u32 m0, s72, 101376\n" \
  "s_mov_b32 s74, 1671168\n" \
  "buffer_load_dwordx4 v24, s[40:43], s74 offen offset:0 lds\n" \
  "buffer_load_dwordx4 v25, s[40:43], s74 offen offset:1024 lds\n" \
  "buffer_load_dwordx4 v26, s[40:43], s74 offen offset:2048 lds\n" \
  "v_add_u32 v31, 73728, v27\n" \
  "v_add_u32 v23, 73728, v30\n" \
  "s_waitcnt vmcnt(24) lgkmcnt(0)\n" \
  "s_barrier\n" \
  "s_add_u32 m0, s72, 122880\n" \
  "s_mov_b32 s74, 1572864\n" \
  "buffer_load_dwordx4 v24, s[44:47], s74 offen offset:0 lds\n" \
  "buffer_load_dwordx4 v25, s[44:47], s74 offen offset:1024 lds\n" \
  "buffer_load_dwordx4 v26, s[44:47], s74 offen offset:2048 lds\n" \
  "s_add_u32 m0, s72, 125952\n" \
  "s_mov_b32 s74, 1671168\n" \
  "buffer_load_dwordx4 v24, s[44:47], s74 offen offset:0 lds\n" \
  "buffer_load_dwordx4 v25, s[44:47], s74 offen offset:1024 lds\n" \
  "buffer_load_dwordx4 v26, s[44:47], s74 offen offset:2048 lds\n" \
  "ds_read_b64_tr_b16 v[64:65], v27 offset:0\n" \
  "ds_read_b64_tr_b16 v[66:67], v27 offset:768\n" \
  "ds_read_b64_tr_b16 v[68:69], v27 offset:12288\n" \
  "ds_read_b64_tr_b16 v[70:71], v27 offset:13056\n" \
  "ds_read_b64_tr_b16 v[72:73], v27 offset:8\n" \
  "ds_read_b64_tr_b16 v[74:75], v27 offset:776\n" \
  "ds_read_b64_tr_b16 v[76:77], v27 offset:12296\n" \
  "ds_read_b64_tr_b16 v[78:79], v27 offset:13064\n" \
  "ds_read_b64_tr_b16 a[0:1], v30 offset:0\n" \
  "ds_read_b64_tr_b16 a[2:3], v30 offset:768\n" \
  "ds_read_b64_tr_b16 a[4:5], v30 offset:12288\n" \
  "ds_read_b64_tr_b16 a[6:7], v30 offset:13056\n" \
  "s_waitcnt vmcnt(24) lgkmcnt(0)\n" \
  "s_barrier\n" \
  "s_add_u32 m0, s72, 0\n" \
  "s_mov_b32 s74, 2359296\n" \
  "buffer_load_dwordx4 v24, s[40:43], s74 offen offset:0 lds\n" \
  "buffer_load_dwordx4 v25, s[40:43], s74 offen offset:1024 lds\n" \
  "buffer_load_dwordx4 v26, s[40:43], s74 offen offset:2048 lds\n" \
  "s_add_u32 m0, s72, 3072\n" \
  "s_mov_b32 s74, 2457600\n" \
  "buffer_load_dwordx4 v24, s[40:43], s74 offen offset:0 lds\n" \
  "buffer_load_dwordx4 v25, s[40:43], s74 offen offset:1024 lds\n" \
  "buffer_load_dwordx4 v26, s[40:43], s74 offen offset:2048 lds\n" \
  "ds_read_b64_tr_b16 a[8:9], v27 offset:24576\n" \
  "ds_read_b64_tr_b16 a[10:11], v27 offset:25344\n" \
  "ds_read_b64_tr_b16 a[12:13], v27 offset:36864\n" \
  "ds_read_b64_tr_b16 a[14:15], v27 offset:37632\n" \
  "ds_read_b64_tr_b16 a[16:17], v27 offset:24584\n" \
  "ds_read_b64_tr_b16 a[18:19], v27 offset:25352\n" \
  "ds_read_b64_tr_b16 a[20:21], v27 offset:36872\n" \
  "ds_read_b64_tr_b16 a[22:23], v27 offset:37640\n" \
  "ds_read_b64_tr_b16 v[32:33], v30 offset:24576\n" \
  "ds_read_b64_tr_b16 v[34:35], v30 offset:25344\n" \
  "ds_read_b64_tr_b16 v[36:37], v30 offset:36864\n" \
  "ds_read_b64_tr_b16 v[38:39], v30 offset:37632\n" \
  "s_waitcnt vmcnt(24) lgkmcnt(0)\n" \
  "s_barrier\n" \
  "s_add_u32 m0, s72, 24576\n" \
  "s_mov_b32 s74, 2359296\n" \
  "buffer_load_dwordx4 v24, s[44:47], s74 offen offset:0 lds\n" \
  "buffer_load_dwordx4 v25, s[44:47], s74 offen offset:1024 lds\n" \
  "buffer_load_dwordx4 v26, s[44:47], s74 offen offset:2048 lds\n" \
  "s_add_u32 m0, s72, 27648\n" \
  "s_mov_b32 s74, 2457600\n" \
  "buffer_load_dwordx4 v24, s[44:47], s74 offen offset:0 lds\n" \
  "buffer_load_dwordx4 v25, s[44:47], s74 offen offset:1024 lds\n" \
  "buffer_load_dwordx4 v26, s[44:47], s74 offen offset:2048 lds\n" \
  "ds_read_b64_tr_b16 a[24:25], v27 offset:49152\n" \
  "ds_read_b64_tr_b16 a[26:27], v27 offset:49920\n" \
  "ds_read_b64_tr_b16 a[28:29], v27 offset:61440\n" \
  "ds_read_b64_tr_b16 a[30:31], v27 offset:62208\n" \
  "ds_read_b64_tr_b16 a[32:33], v27 offset:49160\n" \
  "ds_read_b64_tr_b16 a[34:35], v27 offset:49928\n" \
  "ds_read_b64_tr_b16 a[36:37], v27 offset:61448\n" \
  "ds_read_b64_tr_b16 a[38:39], v27 offset:62216\n" \
  "ds_read_b64_tr_b16 a[40:41], v30 offset:49152\n" \
  "ds_read_b64_tr_b16 a[42:43], v30 offset:49920\n" \
  "ds_read_b64_tr_b16 a[44:45], v30 offset:61440\n" \
  "ds_read_b64_tr_b16 a[46:47], v30 offset:62208\n" \
  "s_waitcnt vmcnt(24) lgkmcnt(0)\n" \
  "s_barrier\n" \
  "s_add_u32 m0, s72, 49152\n" \
  "s_mov_b32 s74, 3145728\n" \
  "buffer_load_dwordx4 v24, s[40:43], s74 offen offset:0 lds\n" \
  "buffer_load_dwordx4 v25, s[40:43], s74 offen offset:1024 lds\n" \
  "buffer_load_dwordx4 v26, s[40:43], s74 offen offset:2048 lds\n" \
  "s_add_u32 m0, s72, 52224\n" \
  "s_mov_b32 s74, 3244032\n" \
  "buffer_load_dwordx4 v24, s[40:43], s74 offen offset:0 lds\n" \
  "buffer_load_dwordx4 v25, s[40:43], s74 offen offset:1024 lds\n" \
  "buffer_load_dwordx4 v26, s[40:43], s74 offen offset:2048 lds\n" \
  "ds_read_b64_tr_b16 a[48:49], v31 offset:0\n" \
  "ds_read_b64_tr_b16 a[50:51], v31 offset:768\n" \
  "ds_read_b64_tr_b16 a[52:53], v31 offset:12288\n" \
  "ds_read_b64_tr_b16 a[54:55], v31 offset:13056\n" \
  "ds_read_b64_tr_b16 a[56:57], v31 offset:8\n" \
  "ds_read_b64_tr_b16 a[58:59], v31 offset:776\n" \
  "ds_read_b64_tr_b16 a[60:61], v31 offset:12296\n" \
  "ds_read_b64_tr_b16 a[62:63], v31 offset:13064\n" \
  "ds_read_b64_tr_b16 v[40:41], v23 offset:0\n" \
  "ds_read_b64_tr_b16 v[42:43], v23 offset:768\n" \
  "ds_read_b64_tr_b16 v[44:45], v23 offset:12288\n" \
  "ds_read_b64_tr_b16 v[46:47], v23 offset:13056\n" \
  "s_waitcnt vmcnt(24) lgkmcnt(0)\n" \
  "s_barrier\n" \
  "s_add_u32 m0, s72, 73728\n" \
  "s_mov_b32 s74, 3145728\n" \
  "buffer_load_dwordx4 v24, s[44:47], s74 offen offset:0 lds\n" \
  "buffer_load_dwordx4 v25, s[44:47], s74 offen offset:1024 lds\n" \
  "buffer_load_dwordx4 v26, s[44:47], s74 offen offset:2048 lds\n" \
  "s_add_u32 m0, s72, 76800\n" \
  "s_mov_b32 s74, 3244032\n" \
  "buffer_load_dwordx4 v24, s[44:47], s74 offen offset:0 lds\n" \
  "buffer_load_dwordx4 v25, s[44:47], s74 offen offset:1024 lds\n" \
  "buffer_load_dwordx4 v26, s[44:47], s74 offen offset:2048 lds\n" \
  "ds_read_b64_tr_b16 a[64:65], v31 offset:24576\n" \
  "ds_read_b64_tr_b16 a[66:67], v31 offset:25344\n" \
  "ds_read_b64_tr_b16 a[68:69], v31 offset:36864\n" \
  "ds_read_b64_tr_b16 a[70:71], v31 offset:37632\n" \
  "ds_read_b64_tr_b16 a[72:73], v31 offset:24584\n" \
  "ds_read_b64_tr_b16 a[74:75], v31 offset:25352\n" \
  "ds_read_b64_tr_b16 a[76:77], v31 offset:36872\n" \
  "ds_read_b64_tr_b16 a[78:79], v31 offset:37640\n" \
  "ds_read_b64_tr_b16 a[80:81], v23 offset:24576\n" \
  "ds_read_b64_tr_b16 a[82:83], v23 offset:25344\n" \
  "ds_read_b64_tr_b16 a[84:85], v23 offset:36864\n" \
  "ds_read_b64_tr_b16 a[86:87], v23 offset:37632\n" \
  "s_waitcnt vmcnt(24) lgkmcnt(0)\n" \
  "s_barrier\n" \
  "s_add_u32 m0, s72, 98304\n" \
  "s_mov_b32 s74, 3932160\n" \
  "buffer_load_dwordx4 v24, s[40:43], s74 offen offset:0 lds\n" \
  "buffer_load_dwordx4 v25, s[40:43], s74 offen offset:1024 lds\n" \
  "buffer_load_dwordx4 v26, s[40:43], s74 offen offset:2048 lds\n" \
  "s_add_u32 m0, s72, 101376\n" \
  "s_mov_b32 s74, 4030464\n" \
  "buffer_load_dwordx4 v24, s[40:43], s74 offen offset:0 lds\n" \
  "buffer_load_dwordx4 v25, s[40:43], s74 offen offset:1024 lds\n" \
  "buffer_load_dwordx4 v26, s[40:43], s74 offen offset:2048 lds\n" \
  "ds_read_b64_tr_b16 a[88:89], v31 offset:49152\n" \
  "ds_read_b64_tr_b16 a[90:91], v31 offset:49920\n" \
  "ds_read_b64_tr_b16 a[92:93], v31 offset:61440\n" \
  "ds_read_b64_tr_b16 a[94:95], v31 offset:62208\n" \
  "ds_read_b64_tr_b16 a[96:97], v31 offset:49160\n" \
  "ds_read_b64_tr_b16 a[98:99], v31 offset:49928\n" \
  "ds_read_b64_tr_b16 a[100:101], v31 offset:61448\n" \
  "ds_read_b64_tr_b16 a[102:103], v31 offset:62216\n" \
  "ds_read_b64_tr_b16 v[48:49], v23 offset:49152\n" \
  "ds_read_b64_tr_b16 v[50:51], v23 offset:49920\n" \
  "ds_read_b64_tr_b16 v[52:53], v23 offset:61440\n" \
  "ds_read_b64_tr_b16 v[54:55], v23 offset:62208\n" \
  "s_waitcnt vmcnt(24) lgkmcnt(0)\n" \
  "s_barrier\n" \
  "s_add_u32 m0, s72, 122880\n" \
  "s_mov_b32 s74, 3932160\n" \
  "buffer_load_dwordx4 v24, s[44:47], s74 offen offset:0 lds\n" \
  "buffer_load_dwordx4 v25, s[44:47], s74 offen offset:1024 lds\n" \
  "buffer_load_dwordx4 v26, s[44:47], s74 offen offset:2048 lds\n" \
  "s_add_u32 m0, s72, 125952\n" \
  "s_mov_b32 s74, 4030464\n" \
  "buffer_load_dwordx4 v24, s[44:47], s74 offen offset:0 lds\n" \
  "buffer_load_dwordx4 v25, s[44:47], s74 offen offset:1024 lds\n" \
  "buffer_load_dwordx4 v26, s[44:47], s74 offen offset:2048 lds\n" \
  "ds_read_b64_tr_b16 a[104:105], v27 offset:0\n" \
  "ds_read_b64_tr_b16 a[106:107], v27 offset:768\n" \
  "ds_read_b64_tr_b16 a[108:109], v27 offset:12288\n" \
  "ds_read_b64_tr_b16 a[110:111], v27 offset:13056\n" \
  "ds_read_b64_tr_b16 v[80:81], v27 offset:8\n" \
  "ds_read_b64_tr_b16 v[82:83], v27 offset:776\n" \
  "ds_read_b64_tr_b16 v[84:85], v27 offset:12296\n" \
  "ds_read_b64_tr_b16 v[86:87], v27 offset:13064\n" \
  "ds_read_b64_tr_b16 a[112:113], v30 offset:0\n" \
  "ds_read_b64_tr_b16 a[114:115], v30 offset:768\n" \
  "ds_read_b64_tr_b16 a[116:117], v30 offset:12288\n" \
  "ds_read_b64_tr_b16 a[118:119], v30 offset:13056\n" \
  "s_waitcnt vmcnt(24) lgkmcnt(0)\n" \
  "s_barrier\n" \
  "s_add_u32 m0, s72, 0\n" \
  "s_mov_b32 s74, 4718592\n" \
  "buffer_load_dwordx4 v24, s[40:43], s74 offen offset:0 lds\n" \
  "buffer_load_dwordx4 v25, s[40:43], s74 offen offset:1024 lds\n" \
  "buffer_load_dwordx4 v26, s[40:43], s74 offen offset:2048 lds\n" \
  "s_add_u32 m0, s72, 3072\n" \
  "s_mov_b32 s74, 4816896\n" \
  "buffer_load_dwordx4 v24, s[40:43], s74 offen offset:0 lds\n" \
  "buffer_load_dwordx4 v25, s[40:43], s74 offen offset:1024 lds\n" \
  "buffer_load_dwordx4 v26, s[40:43], s74 offen offset:2048 lds\n" \
  "ds_read_b64_tr_b16 a[120:121], v27 offset:24576\n" \
  "ds_read_b64_tr_b16 a[122:123], v27 offset:25344\n" \
  "ds_read_b64_tr_b16 a[124:125], v27 offset:36864\n" \
  "ds_read_b64_tr_b16 a[126:127], v27 offset:37632\n" \
  "ds_read_b64_tr_b16 a[128:129], v27 offset:24584\n" \
  "ds_read_b64_tr_b16 a[130:131], v27 offset:25352\n" \
  "ds_read_b64_tr_b16 a[132:133], v27 offset:36872\n" \
  "ds_read_b64_tr_b16 a[134:135], v27 offset:37640\n" \
  "ds_read_b64_tr_b16 v[56:57], v30 offset:24576\n" \
  "ds_read_b64_tr_b16 v[58:59], v30 offset:25344\n" \
  "ds_read_b64_tr_b16 v[60:61], v30 offset:36864\n" \
  "ds_read_b64_tr_b16 v[62:63], v30 offset:37632\n" \
  "s_waitcnt vmcnt(24) lgkmcnt(0)\n" \
  "s_barrier\n" \
  "s_add_u32 m0, s72, 24576\n" \
  "s_mov_b32 s74, 4718592\n" \
  "buffer_load_dwordx4 v24, s[44:47], s74 offen offset:0 lds\n" \
  "buffer_load_dwordx4 v25, s[44:47], s74 offen offset:1024 lds\n" \
  "buffer_load_dwordx4 v26, s[44:47], s74 offen offset:2048 lds\n" \
  "s_add_u32 m0, s72, 27648\n" \
  "s_mov_b32 s74, 4816896\n" \
  "buffer_load_dwordx4 v24, s[44:47], s74 offen offset:0 lds\n" \
  "buffer_load_dwordx4 v25, s[44:47], s74 offen offset:1024 lds\n" \
  "buffer_load_dwordx4 v26, s[44:47], s74 offen offset:2048 lds\n" \
  "ds_read_b64_tr_b16 v[88:89], v27 offset:49152\n" \
  "ds_read_b64_tr_b16 v[90:91], v27 offset:49920\n" \
  "ds_read_b64_tr_b16 v[92:93], v27 offset:61440\n" \
  "ds_read_b64_tr_b16 v[94:95], v27 offset:62208\n" \
  "ds_read_b64_tr_b16 a[136:137], v27 offset:49160\n" \
  "ds_read_b64_tr_b16 a[138:139], v27 offset:49928\n" \
  "ds_read_b64_tr_b16 a[140:141], v27 offset:61448\n" \
  "ds_read_b64_tr_b16 a[142:143], v27 offset:62216\n" \
  "ds_read_b64_tr_b16 a[144:145], v30 offset:49152\n" \
  "ds_read_b64_tr_b16 a[146:147], v30 offset:49920\n" \
  "ds_read_b64_tr_b16 a[148:149], v30 offset:61440\n" \
  "ds_read_b64_tr_b16 a[150:151], v30 offset:62208\n" \
  "s_waitcnt vmcnt(24) lgkmcnt(0)\n" \
  "s_barrier\n" \
  "s_add_u32 m0, s72, 49152\n" \
  "s_mov_b32 s74, 5505024\n" \
  "buffer_load_dwordx4 v24, s[40:43], s74 offen offset:0 lds\n" \
  "buffer_load_dwordx4 v25, s[40:43], s74 offen offset:1024 lds\n" \
  "buffer_load_dwordx4 v26, s[40:43], s74 offen offset:2048 lds\n" \
  "s_add_u32 m0, s72, 52224\n" \
  "s_mov_b32 s74, 5603328\n" \
  "buffer_load_dwordx4 v24, s[40:43], s74 offen offset:0 lds\n" \
  "buffer_load_dwordx4 v25, s[40:43], s74 offen offset:1024 lds\n" \
  "buffer_load_dwordx4 v26, s[40:43], s74 offen offset:2048 lds\n" \
  "ds_read_b64_tr_b16 a[152:153], v31 offset:0\n" \
  "ds_read_b64_tr_b16 a[154:155], v31 offset:768\n" \
  "ds_read_b64_tr_b16 a[156:157], v31 offset:12288\n" \
  "ds_read_b64_tr_b16 a[158:159], v31 offset:13056\n" \
  "ds_read_b64_tr_b16 a[160:161], v31 offset:8\n" \
  "ds_read_b64_tr_b16 a[162:163], v31 offset:776\n" \
  "ds_read_b64_tr_b16 a[164:165], v31 offset:12296\n" \
  "ds_read_b64_tr_b16 a[166:167], v31 offset:13064\n" \
  "ds_read_b64_tr_b16 a[168:169], v23 offset:0\n" \
  "ds_read_b64_tr_b16 a[170:171], v23 offset:768\n" \
  "ds_read_b64_tr_b16 a[172:173], v23 offset:12288\n" \
  "ds_read_b64_tr_b16 a[174:175], v23 offset:13056\n" \
  "s_waitcnt vmcnt(24) lgkmcnt(0)\n" \
  "s_barrier\n" \
  "s_add_u32 m0, s72, 73728\n" \
  "s_mov_b32 s74, 5505024\n" \
  "buffer_load_dwordx4 v24, s[44:47], s74 offen offset:0 lds\n" \
  "buffer_load_dwordx4 v25, s[44:47], s74 offen offset:1024 lds\n" \
  "buffer_load_dwordx4 v26, s[44:47], s74 offen offset:2048 lds\n" \
  "s_add_u32 m0, s72, 76800\n" \
  "s_mov_b32 s74, 5603328\n" \
  "buffer_load_dwordx4 v24, s[44:47], s74 offen offset:0 lds\n" \
  "buffer_load_dwordx4 v25, s[44:47], s74 offen offset:1024 lds\n" \
  "buffer_load_dwordx4 v26, s[44:47], s74 offen offset:2048 lds\n" \
  "ds_read_b64_tr_b16 v[96:97], v31 offset:24576\n" \
  "ds_read_b64_tr_b16 v[98:99], v31 offset:25344\n" \
  "ds_read_b64_tr_b16 v[100:101], v31 offset:36864\n" \
  "ds_read_b64_tr_b16 v[102:103], v31 offset:37632\n" \
  "ds_read_b64_tr_b16 a[176:177], v31 offset:24584\n" \
  "ds_read_b64_tr_b16 a[178:179], v31 offset:25352\n" \
  "ds_read_b64_tr_b16 a[180:181], v31 offset:36872\n" \
  "ds_read_b64_tr_b16 a[182:183], v31 offset:37640\n" \
  "ds_read_b64_tr_b16 a[184:185], v23 offset:24576\n" \
  "ds_read_b64_tr_b16 a[186:187], v23 offset:25344\n" \
  "ds_read_b64_tr_b16 a[188:189], v23 offset:36864\n" \
  "ds_read_b64_tr_b16 a[190:191], v23 offset:37632\n" \
  "s_waitcnt vmcnt(24) lgkmcnt(0)\n" \
  "s_barrier\n" \
  "s_add_u32 m0, s72, 98304\n" \
  "s_mov_b32 s74, 6291456\n" \
  "buffer_load_dwordx4 v24, s[40:43], s74 offen offset:0 lds\n" \
  "buffer_load_dwordx4 v25, s[40:43], s74 offen offset:1024 lds\n" \
  "buffer_load_dwordx4 v26, s[40:43], s74 offen offset:2048 lds\n" \
  "s_add_u32 m0, s72, 101376\n" \
  "s_mov_b32 s74, 6389760\n" \
  "buffer_load_dwordx4 v24, s[40:43], s74 offen offset:0 lds\n" \
  "buffer_load_dwordx4 v25, s[40:43], s74 offen offset:1024 lds\n" \
  "buffer_load_dwordx4 v26, s[40:43], s74 offen offset:2048 lds\n" \
  "ds_read_b64_tr_b16 a[192:193], v31 offset:49152\n" \
  "ds_read_b64_tr_b16 a[194:195], v31 offset:49920\n" \
  "ds_read_b64_tr_b16 a[196:197], v31 offset:61440\n" \
  "ds_read_b64_tr_b16 a[198:199], v31 offset:62208\n" \
  "ds_read_b64_tr_b16 a[200:201], v31 offset:49160\n" \
  "ds_read_b64_tr_b16 a[202:203], v31 offset:49928\n" \
  "ds_read_b64_tr_b16 a[204:205], v31 offset:61448\n" \
  "ds_read_b64_tr_b16 a[206:207], v31 offset:62216\n" \
  "ds_read_b64_tr_b16 a[208:209], v23 offset:49152\n" \
  "ds_read_b64_tr_b16 a[210:211], v23 offset:49920\n" \
  "ds_read_b64_tr_b16 a[212:213], v23 offset:61440\n" \
  "ds_read_b64_tr_b16 a[214:215], v23 offset:62208\n" \
  "s_waitcnt vmcnt(24) lgkmcnt(0)\n" \
  "s_barrier\n" \
  "s_add_u32 m0, s72, 122880\n" \
  "s_mov_b32 s74, 6291456\n" \
  "buffer_load_dwordx4 v24, s[44:47], s74 offen offset:0 lds\n" \
  "buffer_load_dwordx4 v25, s[44:47], s74 offen offset:1024 lds\n" \
  "buffer_load_dwordx4 v26, s[44:47], s74 offen offset:2048 lds\n" \
  "s_add_u32 m0, s72, 125952\n" \
  "s_mov_b32 s74, 6389760\n" \
  "buffer_load_dwordx4 v24, s[44:47], s74 offen offset:0 lds\n" \
  "buffer_load_dwordx4 v25, s[44:47], s74 offen offset:1024 lds\n" \
  "buffer_load_dwordx4 v26, s[44:47], s74 offen offset:2048 lds\n" \
  "ds_read_b64_tr_b16 a[216:217], v27 offset:0\n" \
  "ds_read_b64_tr_b16 a[218:219], v27 offset:768\n" \
  "ds_read_b64_tr_b16 a[220:221], v27 offset:12288\n" \
  "ds_read_b64_tr_b16 a[222:223], v27 offset:13056\n" \
  "ds_read_b64_tr_b16 a[224:225], v27 offset:8\n" \
  "ds_read_b64_tr_b16 a[226:227], v27 offset:776\n" \
  "ds_read_b64_tr_b16 a[228:229], v27 offset:12296\n" \
  "ds_read_b64_tr_b16 a[230:231], v27 offset:13064\n" \
  "ds_read_b64_tr_b16 a[232:233], v30 offset:0\n" \
  "ds_read_b64_tr_b16 a[234:235], v30 offset:768\n" \
  "ds_read_b64_tr_b16 a[236:237], v30 offset:12288\n" \
  "ds_read_b64_tr_b16 a[238:239], v30 offset:13056\n" \
  "s_waitcnt vmcnt(24) lgkmcnt(0)\n" \
  "s_barrier\n" \
  "s_add_u32 m0, s72, 0\n" \
  "s_mov_b32 s74, 7077888\n" \
  "buffer_load_dwordx4 v24, s[40:43], s74 offen offset:0 lds\n" \
  "buffer_load_dwordx4 v25, s[40:43], s74 offen offset:1024 lds\n" \
  "buffer_load_dwordx4 v26, s[40:43], s74 offen offset:2048 lds\n" \
  "s_add_u32 m0, s72, 3072\n" \
  "s_mov_b32 s74, 7176192\n" \
  "buffer_load_dwordx4 v24, s[40:43], s74 offen offset:0 lds\n" \
  "buffer_load_dwordx4 v25, s[40:43], s74 offen offset:1024 lds\n" \
  "buffer_load_dwordx4 v26, s[40:43], s74 offen offset:2048 lds\n" \
  "ds_read_b64_tr_b16 a[240:241], v27 offset:24576\n" \
  "ds_read_b64_tr_b16 a[242:243], v27 offset:25344\n" \
  "ds_read_b64_tr_b16 a[244:245], v27 offset:36864\n" \
  "ds_read_b64_tr_b16 a[246:247], v27 offset:37632\n" \
  "ds_read_b64_tr_b16 a[248:249], v27 offset:24584\n" \
  "ds_read_b64_tr_b16 a[250:251], v27 offset:25352\n" \
  "ds_read_b64_tr_b16 a[252:253], v27 offset:36872\n" \
  "ds_read_b64_tr_b16 a[254:255], v27 offset:37640\n" \
  "ds_read_b64_tr_b16 v[120:121], v30 offset:24576\n" \
  "ds_read_b64_tr_b16 v[122:123], v30 offset:25344\n" \
  "ds_read_b64_tr_b16 v[124:125], v30 offset:36864\n" \
  "ds_read_b64_tr_b16 v[126:127], v30 offset:37632\n" \
  "s_waitcnt vmcnt(24) lgkmcnt(0)\n" \
  "s_barrier\n" \
  "s_add_u32 m0, s72, 24576\n" \
  "s_mov_b32 s74, 7077888\n" \
  "buffer_load_dwordx4 v24, s[44:47], s74 offen offset:0 lds\n" \
  "buffer_load_dwordx4 v25, s[44:47], s74 offen offset:1024 lds\n" \
  "buffer_load_dwordx4 v26, s[44:47], s74 offen offset:2048 lds\n" \
  "s_add_u32 m0, s72, 27648\n" \
  "s_mov_b32 s74, 7176192\n" \
  "buffer_load_dwordx4 v24, s[44:47], s74 offen offset:0 lds\n" \
  "buffer_load_dwordx4 v25, s[44:47], s74 offen offset:1024 lds\n" \
  "buffer_load_dwordx4 v26, s[44:47], s74 offen offset:2048 lds\n" \
  "ds_read_b64_tr_b16 v[128:129], v27 offset:49152\n" \
  "ds_read_b64_tr_b16 v[130:131], v27 offset:49920\n" \
  "ds_read_b64_tr_b16 v[132:133], v27 offset:61440\n" \
  "ds_read_b64_tr_b16 v[134:135], v27 offset:62208\n" \
  "ds_read_b64_tr_b16 v[112:113], v27 offset:49160\n" \
  "ds_read_b64_tr_b16 v[114:115], v27 offset:49928\n" \
  "ds_read_b64_tr_b16 v[116:117], v27 offset:61448\n" \
  "ds_read_b64_tr_b16 v[118:119], v27 offset:62216\n" \
  "ds_read_b64_tr_b16 v[136:137], v30 offset:49152\n" \
  "ds_read_b64_tr_b16 v[138:139], v30 offset:49920\n" \
  "ds_read_b64_tr_b16 v[140:141], v30 offset:61440\n" \
  "ds_read_b64_tr_b16 v[142:143], v30 offset:62208\n" \
  "s_waitcnt vmcnt(24) lgkmcnt(0)\n" \
  "s_barrier\n" \
  "ds_read_b64_tr_b16 v[144:145], v31 offset:0\n" \
  "ds_read_b64_tr_b16 v[146:147], v31 offset:768\n" \
  "ds_read_b64_tr_b16 v[148:149], v31 offset:12288\n" \
  "ds_read_b64_tr_b16 v[150:151], v31 offset:13056\n" \
  "ds_read_b64_tr_b16 v[152:153], v31 offset:8\n" \
  "ds_read_b64_tr_b16 v[154:155], v31 offset:776\n" \
  "ds_read_b64_tr_b16 v[156:157], v31 offset:12296\n" \
  "ds_read_b64_tr_b16 v[158:159], v31 offset:13064\n" \
  "ds_read_b64_tr_b16 v[160:161], v23 offset:0\n" \
  "ds_read_b64_tr_b16 v[162:163], v23 offset:768\n" \
  "ds_read_b64_tr_b16 v[164:165], v23 offset:12288\n" \
  "ds_read_b64_tr_b16 v[166:167], v23 offset:13056\n" \
  "s_waitcnt vmcnt(18) lgkmcnt(0)\n" \
  "s_barrier\n" \
  "s_getpc_b64 s[82:83]\n" \
  "LCF1_%=:\n" \
  "s_add_u32 s82, s82, LSTARTs0_%=-LCF1_%=\n" \
  "s_addc_u32 s83, s83, 0\n" \
  "v_mbcnt_lo_u32_b32 v5, -1, 0\n" \
  "v_mbcnt_hi_u32_b32 v5, -1, v5\n" \
  "v_lshlrev_b32 v5, 6, v5\n" \
  "global_load_dword v10, v5, s[82:83]\n" \
  "v_add_u32 v5, 4096, v5\n" \
  "global_load_dword v10, v5, s[82:83]\n" \
  "v_add_u32 v5, 4096, v5\n" \
  "global_load_dword v10, v5, s[82:83]\n" \
  "s_getpc_b64 s[82:83]\n" \
  "LCF2_%=:\n" \
  "s_add_u32 s82, s82, LDONE_%=-4096-LCF2_%=\n" \
  "s_addc_u32 s83, s83, 0\n" \
  "v_mbcnt_lo_u32_b32 v5, -1, 0\n" \
  "v_mbcnt_hi_u32_b32 v5, -1, v5\n" \
  "v_lshlrev_b32 v5, 6, v5\n" \
  "global_load_dword v10, v5, s[82:83]\n" \
  "ds_read_b64_tr_b16 v[168:169], v31 offset:24576\n" \
  "ds_read_b64_tr_b16 v[170:171], v31 offset:25344\n" \
  "ds_read_b64_tr_b16 v[172:173], v31 offset:36864\n" \
  "ds_read_b64_tr_b16 v[174:175], v31 offset:37632\n" \
  "ds_read_b64_tr_b16 v[176:177], v31 offset:24584\n" \
  "ds_read_b64_tr_b16 v[178:179], v31 offset:25352\n" \
  "ds_read_b64_tr_b16 v[180:181], v31 offset:36872\n" \
  "ds_read_b64_tr_b16 v[182:183], v31 offset:37640\n" \
  "ds_read_b64_tr_b16 v[184:185], v23 offset:24576\n" \
  "ds_read_b64_tr_b16 v[186:187], v23 offset:25344\n" \
  "ds_read_b64_tr_b16 v[188:189], v23 offset:36864\n" \
  "ds_read_b64_tr_b16 v[190:191], v23 offset:37632\n" \
  "s_waitcnt vmcnt(16) lgkmcnt(0)\n" \
  "s_barrier\n" \
  "ds_read_b64_tr_b16 v[192:193], v31 offset:49152\n" \
  "ds_read_b64_tr_b16 v[194:195], v31 offset:49920\n" \
  "ds_read_b64_tr_b16 v[196:197], v31 offset:61440\n" \
  "ds_read_b64_tr_b16 v[198:199], v31 offset:62208\n" \
  "ds_read_b64_tr_b16 v[200:201], v31 offset:49160\n" \
  "ds_read_b64_tr_b16 v[202:203], v31 offset:49928\n" \
  "ds_read_b64_tr_b16 v[204:205], v31 offset:61448\n" \
  "ds_read_b64_tr_b16 v[206:207], v31 offset:62216\n" \
  "ds_read_b64_tr_b16 v[208:209], v23 offset:49152\n" \
  "ds_read_b64_tr_b16 v[210:211], v23 offset:49920\n" \
  "ds_read_b64_tr_b16 v[212:213], v23 offset:61440\n" \
  "ds_read_b64_tr_b16 v[214:215], v23 offset:62208\n" \
  "s_waitcnt vmcnt(10) lgkmcnt(0)\n" \
  "s_barrier\n" \
  "ds_read_b64_tr_b16 v[104:105], v27 offset:0\n" \
  "ds_read_b64_tr_b16 v[106:107], v27 offset:768\n" \
  "ds_read_b64_tr_b16 v[108:109], v27 offset:12288\n" \
  "ds_read_b64_tr_b16 v[110:111], v27 offset:13056\n" \
  "ds_read_b64_tr_b16 v[216:217], v27 offset:8\n" \
  "ds_read_b64_tr_b16 v[218:219], v27 offset:776\n" \
  "ds_read_b64_tr_b16 v[220:221], v27 offset:12296\n" \
  "ds_read_b64_tr_b16 v[222:223], v27 offset:13064\n" \
  "ds_read_b64_tr_b16 v[224:225], v30 offset:0\n" \
  "ds_read_b64_tr_b16 v[226:227], v30 offset:768\n" \
  "ds_read_b64_tr_b16 v[228:229], v30 offset:12288\n" \
  "ds_read_b64_tr_b16 v[230:231], v30 offset:13056\n" \
  "s_waitcnt vmcnt(0) lgkmcnt(0)\n" \
  "s_barrier\n" \
  "ds_read_b64_tr_b16 v[232:233], v27 offset:24576\n" \
  "ds_read_b64_tr_b16 v[234:235], v27 offset:25344\n" \
  "ds_read_b64_tr_b16 v[236:237], v27 offset:36864\n" \
  "ds_read_b64_tr_b16 v[238:239], v27 offset:37632\n" \
  "ds_read_b64_tr_b16 v[240:241], v27 offset:24584\n" \
  "ds_read_b64_tr_b16 v[242:243], v27 offset:25352\n" \
  "ds_read_b64_tr_b16 v[244:245], v27 offset:36872\n" \
  "ds_read_b64_tr_b16 v[246:247], v27 offset:37640\n" \
  "ds_read_b64_tr_b16 v[248:249], v30 offset:24576\n" \
  "ds_read_b64_tr_b16 v[250:251], v30 offset:25344\n" \
  "ds_read_b64_tr_b16 v[252:253], v30 offset:36864\n" \
  "ds_read_b64_tr_b16 v[254:255], v30 offset:37632\n" \
  "s_waitcnt lgkmcnt(0)\n" \
  "s_barrier\n" \
  "ds_write_b128 v3, v[32:35] offset:0\n" \
  "ds_write_b128 v3, v[36:39] offset:1024\n" \
  "ds_write_b128 v3, v[40:43] offset:2048\n" \
  "ds_write_b128 v3, v[44:47] offset:3072\n" \
  "ds_write_b128 v3, v[48:51] offset:4096\n" \
  "ds_write_b128 v3, v[52:55] offset:5120\n" \
  "ds_write_b128 v3, v[56:59] offset:6144\n" \
  "ds_write_b128 v3, v[60:63] offset:7168\n" \
  "ds_write_b128 v3, v[64:67] offset:8192\n" \
  "ds_write_b128 v3, v[68:71] offset:9216\n" \
  "ds_write_b128 v3, v[72:75] offset:10240\n" \
  "ds_write_b128 v3, v[76:79] offset:11264\n" \
  "ds_write_b128 v3, v[80:83] offset:12288\n" \
  "ds_write_b128 v3, v[84:87] offset:13312\n" \
  "ds_write_b128 v3, v[88:91] offset:14336\n" \
  "s_waitcnt lgkmcnt(8)\n" \
  "ds_write_b128 v3, v[92:95] offset:15360\n" \
  "ds_write_b128 v3, v[96:99] offset:16384\n" \
  "ds_write_b128 v3, v[100:103] offset:17408\n" \
  "ds_write_b128 v3, v[104:107] offset:18432\n" \
  "ds_write_b128 v3, v[108:111] offset:19456\n" \
  "ds_write_b128 v3, v[112:115] offset:20480\n" \
  "ds_write_b128 v3, v[116:119] offset:21504\n" \
  "s_waitcnt lgkmcnt(0)\n" \
  "s_cmp_eq_u32 s52, 1\n" \
  "s_cbranch_scc1 LSTARTs1_%=\n" \
  "s_cmp_eq_u32 s52, 2\n" \
  "s_cbranch_scc1 LSTARTs2_%=\n" \
  "s_cmp_eq_u32 s52, 3\n" \
  "s_cbranch_scc1 LSTARTs3_%=\n" \
  "LSTARTs0_%=:\n" \
  "v_mov_b32 v5, v20\n" \
  "v_mov_b32 v10, v21\n" \
  "v_mov_b32 v16, v22\n" \
  "s_mov_b32 s79, 0\n" \
  "LSDs0_%=:\n" \
  "buffer_load_dwordx2 v[64:65], v19, s[36:39], 0 offen sc1\n" \
  "s_waitcnt vmcnt(0)\n" \
  "v_readlane_b32 s76, v64, 3\n" \
  "s_nop 0\n" \
  "s_add_u32 s76, s76, 1\n" \
  "v_cmp_ne_u32 vcc, s76, v64\n" \
  "s_nop 3\n" \
  "s_and_b32 s76, vcc_lo, 7\n" \
  "s_cbranch_scc0 LSFs0_%=\n" \
  "s_add_u32 s79, s79, 1\n" \
  "s_cmp_gt_u32 s79, 200000\n" \
  "s_cbranch_scc1 LSFs0_%=\n" \
  "s_sleep 4\n" \
  "s_branch LSDs0_%=\n" \
  "LSFs0_%=:\n" \
  "v_readlane_b32 s76, v65, 0\n" \
  "v_readlane_b32 s77, v65, 1\n" \
  "v_readlane_b32 s78, v65, 2\n" \
  "s_nop 1\n" \
  "v_lshrrev_b32 v64, 21, v7\n" \
  "v_lshrrev_b64 v[66:67], v64, s[76:77]\n" \
  "v_lshrrev_b32_e64 v68, v64, s78\n" \
  "v_cmp_gt_u32 vcc, 64, v64\n" \
  "s_nop 1\n" \
  "v_cndmask_b32 v66, v68, v66, vcc\n" \
  "v_and_b32 v66, 1, v66\n" \
  "v_lshlrev_b32 v66, 13, v66\n" \
  "v_xor_b32 v7, v7, v66\n" \
  "v_lshrrev_b32 v64, 21, v11\n" \
  "v_lshrrev_b64 v[66:67], v64, s[76:77]\n" \
  "v_lshrrev_b32_e64 v68, v64, s78\n" \
  "v_cmp_gt_u32 vcc, 64, v64\n" \
  "s_nop 1\n" \
  "v_cndmask_b32 v66, v68, v66, vcc\n" \
  "v_and_b32 v66, 1, v66\n" \
  "v_lshlrev_b32 v66, 13, v66\n" \
  "v_xor_b32 v11, v11, v66\n" \
  "v_lshrrev_b32 v64, 21, v28\n" \
  "v_lshrrev_b64 v[66:67], v64, s[76:77]\n" \
  "v_lshrrev_b32_e64 v68, v64, s78\n" \
  "v_cmp_gt_u32 vcc, 64, v64\n" \
  "s_nop 1\n" \
  "v_cndmask_b32 v66, v68, v66, vcc\n" \
  "v_and_b32 v66, 1, v66\n" \
  "v_lshlrev_b32 v66, 13, v66\n" \
  "v_xor_b32 v28, v28, v66\n" \
  "v_lshrrev_b32 v64, 21, v18\n" \
  "v_lshrrev_b64 v[66:67], v64, s[76:77]\n" \
  "v_lshrrev_b32_e64 v68, v64, s78\n" \
  "v_cmp_gt_u32 vcc, 64, v64\n" \
  "s_nop 1\n" \
  "v_cndmask_b32 v66, v68, v66, vcc\n" \
  "v_and_b32 v66, 1, v66\n" \
  "v_lshlrev_b32 v66, 13, v66\n" \
  "v_xor_b32 v18, v18, v66\n" \
  "s_lshl_b32 s74, s76, 12\n" \
  "s_and_b32 s74, s74, 0x2000\n" \
  "s_or_b32 s55, s55, s74\n" \
  "v_mov_b32 v17, v28\n" \
  "s_mov_b32 s47, s38\n" \
  "s_mov_b32 s38, 0\n" \
  "s_mov_b32 s68, 63\n" \
  "s_mov_b32 s73, 20001\n" \
  "s_mov_b32 s56, 19999\n" \
  "s_mov_b32 s97, s54\n" \
  "s_mov_b32 s54, 0\n" \
  "s_mov_b32 s98, s96\n" \
  "s_mov_b32 s96, 0\n" \
  "LPASSs0_%=:\n" \
  "s_mov_b32 s40, 0\n" \
  "s_mov_b32 s42, 0x40000000\n" \
  "v_and_b32 v5, 0x7fff, v5\n" \
  "v_and_b32 v10, 0x7fff, v10\n" \
  "v_mov_b32 v64, 0\n" \
  "ds_write_b32 v12, v64 offset:512\n" \
  "s_waitcnt lgkmcnt(0)\n" \
  "LQGs0_%=:\n" \
  "buffer_load_dword v19, v18, s[36:39], 0 offen sc1\n" \
  "s_waitcnt vmcnt(0)\n" \
  "v_cmp_ge_u32 vcc, 1, v19\n" \
  "s_nop 3\n" \
  "s_cbranch_vccz LRGs0_%=\n" \
  "s_add_u32 s73, s73, 1\n" \
  "s_cmp_gt_u32 s73, 20000\n" \
  "s_cbranch_scc1 LRGs0_%=\n" \
  "s_sleep 4\n" \
  "s_branch LQGs0_%=\n" \
  "LRGs0_%=:\n" \
  "v_mov_b32 v64, 1\n" \
  "buffer_atomic_add v64, v17, s[36:39], 0 offen sc1\n" \
  "s_barrier\n" \
  "s_add_u32 m0, s40, 0\n" \
  "s_add_u32 s75, s55, 0\n" \
  "buffer_load_dwordx4 v8, s[36:39], s75 offen offset:0 sc1 lds\n" \
  "buffer_load_dwordx4 v8, s[36:39], s75 offen offset:1024 sc1 lds\n" \
  "buffer_load_dwordx4 v8, s[36:39], s75 offen offset:2048 sc1 lds\n" \
  "buffer_load_dwordx4 v8, s[36:39], s75 offen offset:3072 sc1 lds\n" \
  "s_add_u32 m0, s40, 4096\n" \
  "s_add_u32 s75, s55, 4096\n" \
  "buffer_load_dwordx4 v8, s[36:39], s75 offen offset:0 sc1 lds\n" \
  "buffer_load_dwordx4 v8, s[36:39], s75 offen offset:1024 sc1 lds\n" \
  "s_xor_b32 s40, s40, 0x8000\n" \
  "v_readlane_b32 s72, v11, 0\n" \
  "s_nop 0\n" \
  "s_add_u32 s69, s72, 2048\n" \
  "s_add_u32 s70, s72, 6144\n" \
  "s_add_u32 s44, s72, s93\n" \
  "s_add_u32 s45, s72, s94\n" \
  "s_add_u32 s46, s72, s95\n" \
  "v_readlane_b32 s41, v7, 0\n" \
  "s_nop 1\n" \
  "ds_read_b128 v[88:91], v3 offset:8192\n" \
  "ds_read_b128 v[92:95], v3 offset:9216\n" \
  "ds_read_b128 v[96:99], v3 offset:10240\n" \
  "ds_read_b128 v[100:103], v3 offset:11264\n" \
  "ds_read_b128 v[104:107], v3 offset:0\n" \
  "ds_read_b128 v[108:111], v3 offset:1024\n" \
  "ds_read_b128 v[112:115], v3 offset:22528\n" \
  "ds_read_b128 v[116:119], v3 offset:23552\n" \
  "v_mov_b32 v60, -1\n" \
  "v_mov_b32 v61, -1\n" \
  "ds_write_b64 v6, v[60:61] offset:32768\n" \
  "ds_write_b64 v6, v[60:61] offset:33280\n" \
  "ds_write_b64 v6, v[60:61] offset:36864\n" \
  "ds_write_b64 v2, v[60:61] offset:0\n" \
  "v_mov_b32 v20, 0\n" \
  "v_mov_b32 v21, 0\n" \
  "v_mov_b32 v22, 0\n" \
  "v_mov_b32 v23, 0\n" \
  "v_mov_b32 v64, 0\n" \
  "v_mov_b32 v65, 0\n" \
  "v_mov_b32 v66, 0\n" \
  "v_mov_b32 v67, 0\n" \
  "v_mov_b32 v68, 0\n" \
  "v_mov_b32 v69, 0\n" \
  "v_mov_b32 v70, 0\n" \
  "v_mov_b32 v71, 0\n" \
  "v_mov_b32 v72, 0\n" \
  "v_mov_b32 v73, 0\n" \
  "v_mov_b32 v74, 0\n" \
  "v_mov_b32 v75, 0\n" \
  "v_mov_b32 v76, 0\n" \
  "v_mov_b32 v77, 0\n" \
  "v_mov_b32 v78, 0\n" \
  "v_mov_b32 v79, 0\n" \
  "v_mov_b32 v80, 0\n" \
  "v_mov_b32 v81, 0\n" \
  "v_mov_b32 v82, 0\n" \
  "v_mov_b32 v83, 0\n" \
  "v_mov_b32 v84, 0\n" \
  "v_mov_b32 v85, 0\n" \
  "v_mov_b32 v86, 0\n" \
  "v_mov_b32 v87, 0\n" \
  "s_waitcnt lgkmcnt(0)\n" \
  "s_cmp_eq_u32 s38, 0\n" \
  "s_cselect_b32 s74, -1, 0\n" \
  "v_mov_b32 v64, s74\n" \
  "LOOPs0_%=:\n" \
  "s_barrier\n" \
  "buffer_load_dwordx4 v[24:27], v1, s[36:39], s72 offen offset:0 sc1\n" \
  "s_mov_b32 m0, 65536\n" \
  "s_nop 0\n" \
  "buffer_load_dwordx4 v1, s[36:39], s69 offen sc1 lds\n" \
  "buffer_load_dwordx4 v[28:31], v1, s[36:39], s72 offen offset:1024 sc1\n" \
  "s_mov_b32 m0, 69632\n" \
  "s_nop 0\n" \
  "buffer_load_dwordx4 v1, s[36:39], s70 offen sc1 lds\n" \
  "ds_write_b64 v6, v[60:61] offset:0\n" \
  "ds_write_b64 v6, v[60:61] offset:512\n" \
  "ds_write_b64 v6, v[60:61] offset:4096\n" \
  "s_add_u32 s74, s68, 2\n" \
  "s_min_u32 s43, s74, 63\n" \
  "s_add_u32 s68, s68, 1\n" \
  "LMs0_%=:\n" \
  "s_memtime s[76:77]\n" \
  "s_waitcnt vmcnt(3)\n" \
  "v_smfmac_f32_16x16x64_bf16 v[64:67], v[24:27], v[88:95], v0\n" \
  "ds_read_b128 v[88:91], v3 offset:2048\n" \
  "v_smfmac_f32_16x16x64_bf16 v[68:71], v[24:27], v[96:103], v0\n" \
  "ds_read_b128 v[92:95], v3 offset:3072\n" \
  "v_smfmac_f32_16x16x64_bf16 v[72:75], v[24:27], a[0:7], v0\n" \
  "ds_read_b128 v[96:99], v3 offset:24576\n" \
  "v_smfmac_f32_16x16x64_bf16 v[76:79], v[24:27], a[8:15], v0\n" \
  "ds_read_b128 v[100:103], v3 offset:25600\n" \
  "v_smfmac_f32_16x16x64_bf16 v[80:83], v[24:27], a[16:23], v0\n" \
  "v_smfmac_f32_16x16x64_bf16 v[84:87], v[24:27], v[104:111], v0\n" \
  "ds_read_b128 v[104:107], v3 offset:12288\n" \
  "v_smfmac_f32_16x16x64_bf16 v[20:23], v[24:27], v[112:119], v0\n" \
  "s_waitcnt vmcnt(2)\n" \
  "s_waitcnt lgkmcnt(5)\n" \
  "s_barrier\n" \
  "ds_read_b128 v[32:35], v2 offset:2048\n" \
  "s_waitcnt vmcnt(1)\n" \
  "v_smfmac_f32_16x16x64_bf16 v[64:67], v[28:31], a[24:31], v0\n" \
  "ds_read_b128 v[36:39], v2 offset:3072\n" \
  "v_smfmac_f32_16x16x64_bf16 v[68:71], v[28:31], a[32:39], v0\n" \
  "ds_read_b128 v[40:43], v2 offset:4096\n" \
  "v_smfmac_f32_16x16x64_bf16 v[72:75], v[28:31], a[40:47], v0\n" \
  "ds_read_b128 v[44:47], v2 offset:5120\n" \
  "s_waitcnt lgkmcnt(7)\n" \
  "v_smfmac_f32_16x16x64_bf16 v[84:87], v[28:31], v[88:95], v0\n" \
  "ds_read_b128 v[108:111], v3 offset:13312\n" \
  "s_waitcnt lgkmcnt(6)\n" \
  "v_smfmac_f32_16x16x64_bf16 v[20:23], v[28:31], v[96:103], v0\n" \
  "ds_read_b128 v[112:115], v3 offset:14336\n" \
  "s_waitcnt lgkmcnt(5)\n" \
  "v_smfmac_f32_16x16x64_bf16 v[64:67], v[32:35], a[64:71], v0\n" \
  "ds_read_b128 v[116:119], v3 offset:15360\n" \
  "v_smfmac_f32_16x16x64_bf16 v[68:71], v[32:35], a[72:79], v0\n" \
  "ds_read_b128 v[88:91], v3 offset:16384\n" \
  "v_smfmac_f32_16x16x64_bf16 v[72:75], v[32:35], a[80:87], v0\n" \
  "ds_read_b128 v[92:95], v3 offset:17408\n" \
  "s_waitcnt lgkmcnt(7)\n" \
  "v_smfmac_f32_16x16x64_bf16 v[64:67], v[36:39], a[104:111], v0\n" \
  "ds_read_b128 v[96:99], v3 offset:18432\n" \
  "s_waitcnt lgkmcnt(5)\n" \
  "v_smfmac_f32_16x16x64_bf16 v[68:71], v[36:39], v[104:111], v0\n" \
  "s_waitcnt vmcnt(0)\n" \
  "s_barrier\n" \
  "v_smfmac_f32_16x16x64_bf16 v[72:75], v[36:39], a[112:119], v0\n" \
  "ds_read_b128 v[48:51], v2 offset:6144\n" \
  "s_waitcnt lgkmcnt(4)\n" \
  "v_smfmac_f32_16x16x64_bf16 v[64:67], v[40:43], v[112:119], v0\n" \
  "ds_read_b128 v[52:55], v2 offset:7168\n" \
  "v_smfmac_f32_16x16x64_bf16 v[68:71], v[40:43], a[136:143], v0\n" \
  "ds_read_b128 v[56:59], v2 offset:8192\n" \
  "v_smfmac_f32_16x16x64_bf16 v[72:75], v[40:43], a[144:151], v0\n" \
  "ds_read_b128 v[60:63], v2 offset:9216\n" \
  "s_waitcnt lgkmcnt(5)\n" \
  "v_smfmac_f32_16x16x64_bf16 v[64:67], v[44:47], v[88:95], v0\n" \
  "ds_read_b128 v[100:103], v3 offset:19456\n" \
  "v_smfmac_f32_16x16x64_bf16 v[68:71], v[44:47], a[176:183], v0\n" \
  "ds_read_b128 v[104:107], v3 offset:20480\n" \
  "v_smfmac_f32_16x16x64_bf16 v[72:75], v[44:47], a[184:191], v0\n" \
  "ds_read_b128 v[108:111], v3 offset:21504\n" \
  "s_waitcnt lgkmcnt(6)\n" \
  "v_smfmac_f32_16x16x64_bf16 v[64:67], v[48:51], a[216:223], v0\n" \
  "ds_read_b128 v[112:115], v3 offset:26624\n" \
  "s_waitcnt lgkmcnt(6)\n" \
  "v_smfmac_f32_16x16x64_bf16 v[64:67], v[52:55], v[128:135], v0\n" \
  "ds_read_b128 v[116:119], v3 offset:27648\n" \
  "s_waitcnt lgkmcnt(6)\n" \
  "v_smfmac_f32_16x16x64_bf16 v[64:67], v[56:59], v[168:175], v0\n" \
  "ds_read_b128 v[88:91], v3 offset:28672\n" \
  "s_waitcnt lgkmcnt(5)\n" \
  "v_smfmac_f32_16x16x64_bf16 v[64:67], v[60:63], v[96:103], v0\n" \
  "ds_read_b128 v[92:95], v3 offset:29696\n" \
  "v_smfmac_f32_16x16x64_bf16 v[68:71], v[48:51], a[224:231], v0\n" \
  "ds_read_b128 v[96:99], v3 offset:4096\n" \
  "s_waitcnt lgkmcnt(5)\n" \
  "v_smfmac_f32_16x16x64_bf16 v[68:71], v[52:55], v[104:111], v0\n" \
  "buffer_load_dword v19, v18, s[36:39], 0 offen sc1\n" \
  "v_cvt_pk_bf16_f32 v64, v64, v65\n" \
  "v_smfmac_f32_16x16x64_bf16 v[68:71], v[56:59], v[176:183], v0\n" \
  "ds_read_b128 v[100:103], v3 offset:5120\n" \
  "v_cvt_pk_bf16_f32 v65, v66, v67\n" \
  "v_smfmac_f32_16x16x64_bf16 v[68:71], v[60:63], v[216:223], v0\n" \
  "ds_write_b64 v6, v[64:65] offset:32768\n" \
  "v_cmp_u_f32 vcc, v64, v65\n" \
  "v_smfmac_f32_16x16x64_bf16 v[72:75], v[48:51], a[232:239], v0\n" \
  "s_cbranch_vccnz LMQs0_%=\n" \
  "v_smfmac_f32_16x16x64_bf16 v[72:75], v[52:55], v[136:143], v0\n" \
  "ds_read_b128 v[104:107], v3 offset:6144\n" \
  "v_cvt_pk_bf16_f32 v68, v68, v69\n" \
  "v_smfmac_f32_16x16x64_bf16 v[72:75], v[56:59], v[184:191], v0\n" \
  "ds_read_b128 v[108:111], v3 offset:7168\n" \
  "v_cvt_pk_bf16_f32 v69, v70, v71\n" \
  "v_smfmac_f32_16x16x64_bf16 v[72:75], v[60:63], v[224:231], v0\n" \
  "ds_write_b64 v6, v[68:69] offset:33280\n" \
  "v_smfmac_f32_16x16x64_bf16 v[76:79], v[28:31], a[48:55], v0\n" \
  "v_smfmac_f32_16x16x64_bf16 v[80:83], v[28:31], a[56:63], v0\n" \
  "s_waitcnt lgkmcnt(8)\n" \
  "v_cvt_pk_bf16_f32 v72, v72, v73\n" \
  "v_smfmac_f32_16x16x64_bf16 v[20:23], v[32:35], v[112:119], v0\n" \
  "ds_read_b64_tr_b16 v[24:25], v9 offset:32768\n" \
  "s_waitcnt lgkmcnt(7)\n" \
  "v_cvt_pk_bf16_f32 v73, v74, v75\n" \
  "v_smfmac_f32_16x16x64_bf16 v[20:23], v[36:39], v[88:95], v0\n" \
  "ds_write_b64 v6, v[72:73] offset:36864\n" \
  "s_waitcnt lgkmcnt(6)\n" \
  "v_smfmac_f32_16x16x64_bf16 v[84:87], v[32:35], v[96:103], v0\n" \
  "ds_read_b64_tr_b16 v[28:29], v9 offset:33792\n" \
  "s_waitcnt lgkmcnt(4)\n" \
  "v_smfmac_f32_16x16x64_bf16 v[84:87], v[36:39], v[104:111], v0\n" \
  "ds_write_b128 v2, v[20:23] offset:0\n" \
  "v_smfmac_f32_16x16x64_bf16 v[76:79], v[32:35], a[88:95], v0\n" \
  "ds_read_b64_tr_b16 v[112:113], v9 offset:34816\n" \
  "v_smfmac_f32_16x16x64_bf16 v[76:79], v[36:39], a[120:127], v0\n" \
  "ds_read_b64_tr_b16 v[64:65], v9 offset:35840\n" \
  "v_smfmac_f32_16x16x64_bf16 v[76:79], v[40:43], a[152:159], v0\n" \
  "ds_read_b64_tr_b16 v[26:27], v9 offset:33280\n" \
  "v_smfmac_f32_16x16x64_bf16 v[76:79], v[44:47], a[192:199], v0\n" \
  "ds_read_b64_tr_b16 v[30:31], v9 offset:34304\n" \
  "v_smfmac_f32_16x16x64_bf16 v[76:79], v[48:51], a[240:247], v0\n" \
  "ds_read_b64_tr_b16 v[114:115], v9 offset:35328\n" \
  "v_smfmac_f32_16x16x64_bf16 v[76:79], v[52:55], v[144:151], v0\n" \
  "ds_read_b64_tr_b16 v[66:67], v9 offset:36352\n" \
  "v_smfmac_f32_16x16x64_bf16 v[76:79], v[56:59], v[192:199], v0\n" \
  "ds_read_b64_tr_b16 v[68:69], v9 offset:36864\n" \
  "v_smfmac_f32_16x16x64_bf16 v[76:79], v[60:63], v[232:239], v0\n" \
  "ds_read_b64_tr_b16 v[70:71], v9 offset:37376\n" \
  "v_smfmac_f32_16x16x64_bf16 v[80:83], v[32:35], a[96:103], v0\n" \
  "ds_read_b64_tr_b16 v[72:73], v9 offset:37888\n" \
  "v_smfmac_f32_16x16x64_bf16 v[80:83], v[36:39], a[128:135], v0\n" \
  "ds_read_b64_tr_b16 v[74:75], v9 offset:38400\n" \
  "v_cvt_pk_bf16_f32 v76, v76, v77\n" \
  "v_smfmac_f32_16x16x64_bf16 v[80:83], v[40:43], a[160:167], v0\n" \
  "v_cvt_pk_bf16_f32 v77, v78, v79\n" \
  "v_smfmac_f32_16x16x64_bf16 v[80:83], v[44:47], a[200:207], v0\n" \
  "s_waitcnt lgkmcnt(8)\n" \
  "ds_write_b64 v6, v[76:77] offset:0\n" \
  "v_smfmac_f32_16x16x64_bf16 v[80:83], v[48:51], a[248:255], v0\n" \
  "v_smfmac_f32_16x16x64_bf16 v[80:83], v[52:55], v[152:159], v0\n" \
  "v_smfmac_f32_16x16x64_bf16 v[80:83], v[56:59], v[200:207], v0\n" \
  "v_smfmac_f32_16x16x64_bf16 v[80:83], v[60:63], v[240:247], v0\n" \
  "ds_read_b64_tr_b16 v[88:89], v9 offset:0\n" \
  "v_smfmac_f32_16x16x64_bf16 v[84:87], v[40:43], a[168:175], v0\n" \
  "ds_read_b64_tr_b16 v[92:93], v9 offset:1024\n" \
  "v_smfmac_f32_16x16x64_bf16 v[84:87], v[44:47], a[208:215], v0\n" \
  "ds_read_b64_tr_b16 v[96:97], v9 offset:2048\n" \
  "v_cvt_pk_bf16_f32 v80, v80, v81\n" \
  "v_smfmac_f32_16x16x64_bf16 v[84:87], v[48:51], v[120:127], v0\n" \
  "ds_read_b64_tr_b16 v[100:101], v9 offset:3072\n" \
  "v_cvt_pk_bf16_f32 v81, v82, v83\n" \
  "v_smfmac_f32_16x16x64_bf16 v[84:87], v[52:55], v[160:167], v0\n" \
  "ds_write_b64 v6, v[80:81] offset:512\n" \
  "v_smfmac_f32_16x16x64_bf16 v[84:87], v[56:59], v[208:215], v0\n" \
  "v_smfmac_f32_16x16x64_bf16 v[84:87], v[60:63], v[248:255], v0\n" \
  "s_waitcnt lgkmcnt(6)\n" \
  "ds_read_b64_tr_b16 v[94:95], v9 offset:1536\n" \
  "ds_read_b64_tr_b16 v[98:99], v9 offset:2560\n" \
  "ds_read_b64_tr_b16 v[90:91], v9 offset:512\n" \
  "s_nop 3\n" \
  "v_cvt_pk_bf16_f32 v84, v84, v85\n" \
  "v_cvt_pk_bf16_f32 v85, v86, v87\n" \
  "ds_write_b64 v6, v[84:85] offset:4096\n" \
  "ds_read_b64_tr_b16 v[102:103], v9 offset:3584\n" \
  "s_barrier ; timing only\n" \
  "ds_read_b64_tr_b16 v[104:105], v9 offset:4096\n" \
  "ds_read_b64_tr_b16 v[106:107], v9 offset:4608\n" \
  "ds_read_b64_tr_b16 v[108:109], v9 offset:5120\n" \
  "ds_read_b64_tr_b16 v[110:111], v9 offset:5632\n" \
  "s_waitcnt lgkmcnt(8)\n" \
  "ds_read2_b32 v[34:35], v12 offset0:0 offset1:64\n" \
  "s_waitcnt lgkmcnt(7)\n" \
  "v_mfma_f32_16x16x32_bf16 v[116:119], v[88:91], v[24:27], 0\n" \
  "v_mfma_f32_16x16x32_bf16 v[116:119], v[92:95], v[28:31], v[116:119]\n" \
  "v_mfma_f32_16x16x32_bf16 v[116:119], v[96:99], v[112:115], v[116:119]\n" \
  "s_waitcnt lgkmcnt(5)\n" \
  "v_mfma_f32_16x16x32_bf16 v[116:119], v[100:103], v[64:67], v[116:119]\n" \
  "s_waitcnt lgkmcnt(3)\n" \
  "v_mfma_f32_16x16x32_bf16 v[116:119], v[104:107], v[68:71], v[116:119]\n" \
  "s_waitcnt lgkmcnt(1)\n" \
  "v_mfma_f32_16x16x32_bf16 v[116:119], v[108:111], v[72:75], v[116:119]\n" \
  "s_waitcnt lgkmcnt(0)\n" \
  "v_add_f32 v34, v34, v35\n" \
  "s_nop 1\n" \
  "v_add_f32_dpp v34, v34, v34 row_ror:8 row_mask:0xf bank_mask:0xf\n" \
  "s_nop 2\n" \
  "v_permlane32_swap_b32 v116, v118\n" \
  "v_permlane32_swap_b32 v117, v119\n" \
  "v_add_f32 v118, v116, v118\n" \
  "v_add_f32 v119, v117, v119\n" \
  "v_mul_f32 v116, v4, v118\n" \
  "v_mul_f32 v117, v4, v119\n" \
  "v_fmac_f32_dpp v116, v118, v4 row_ror:8 row_mask:0xf bank_mask:0xf\n" \
  "v_fmac_f32_dpp v117, v119, v4 row_ror:8 row_mask:0xf bank_mask:0xf\n" \
  "v_exp_f32 v32, v116\n" \
  "v_exp_f32 v33, v117\n" \
  "v_add_f32 v34, v34, v32\n" \
  "v_add_f32 v36, v34, v33\n" \
  "v_add_f32 v37, v34, v33\n" \
  "v_cndmask_b32 v32, v14, v32, s[34:35]\n" \
  "v_cndmask_b32 v33, v15, v33, s[34:35]\n" \
  "v_permlane16_swap_b32 v36, v37\n" \
  "v_add_f32 v34, v36, v37\n" \
  "v_add_f32 v35, v36, v37\n" \
  "s_nop 1\n" \
  "v_permlane32_swap_b32 v34, v35\n" \
  "v_add_f32 v34, v34, v35\n" \
  "v_rcp_f32 v38, v34\n" \
  "v_subrev_u32 v34, 0x1f800000, v34\n" \
  "v_cmp_lt_u32 vcc, 0x40000000, v34\n" \
  "v_mul_f32 v32, v32, v38\n" \
  "v_mul_f32 v33, v33, v38\n" \
  "v_cvt_pk_bf16_f32 v32, v32, v33\n" \
  "s_cbranch_vccnz LMXs0_%=\n" \
  "buffer_store_dword v32, v13, s[36:39], s72 offen offset:0 sc1\n" \
  "buffer_store_dword v32, v13, s[36:39], s44 offen offset:0 sc1\n" \
  "buffer_store_dword v32, v13, s[36:39], s45 offen offset:0 sc1\n" \
  "buffer_store_dword v32, v13, s[36:39], s46 offen offset:0 sc1\n" \
  "s_barrier\n" \
  "LMYs0_%=:\n" \
  "s_waitcnt vmcnt(4)\n" \
  "v_cmp_ge_u32 vcc, s43, v19\n" \
  "s_nop 3\n" \
  "s_cbranch_vccnz LFFs0_%=\n" \
  "LFOKs0_%=:\n" \
  "v_xor_b32 v5, 0x8000, v5\n" \
  "v_xor_b32 v10, 0x8000, v10\n" \
  "ds_read_b128 v[88:91], v3 offset:8192\n" \
  "ds_read_b128 v[92:95], v3 offset:9216\n" \
  "ds_read_b128 v[96:99], v3 offset:10240\n" \
  "ds_read_b128 v[100:103], v3 offset:11264\n" \
  "ds_read_b128 v[104:107], v3 offset:0\n" \
  "ds_read_b128 v[108:111], v3 offset:1024\n" \
  "ds_read_b128 v[112:115], v3 offset:22528\n" \
  "ds_read_b128 v[116:119], v3 offset:23552\n" \
  "v_mov_b32 v60, -1\n" \
  "v_mov_b32 v61, -1\n" \
  "ds_write_b64 v6, v[60:61] offset:32768\n" \
  "ds_write_b64 v6, v[60:61] offset:33280\n" \
  "ds_write_b64 v6, v[60:61] offset:36864\n" \
  "ds_write_b64 v2, v[60:61] offset:0\n" \
  "v_mov_b32 v20, 0\n" \
  "v_mov_b32 v21, 0\n" \
  "v_mov_b32 v22, 0\n" \
  "v_mov_b32 v23, 0\n" \
  "v_mov_b32 v64, 0\n" \
  "v_mov_b32 v65, 0\n" \
  "v_mov_b32 v66, 0\n" \
  "v_mov_b32 v67, 0\n" \
  "v_mov_b32 v68, 0\n" \
  "v_mov_b32 v69, 0\n" \
  "v_mov_b32 v70, 0\n" \
  "v_mov_b32 v71, 0\n" \
  "v_mov_b32 v72, 0\n" \
  "v_mov_b32 v73, 0\n" \
  "v_mov_b32 v74, 0\n" \
  "v_mov_b32 v75, 0\n" \
  "v_mov_b32 v76, 0\n" \
  "v_mov_b32 v77, 0\n" \
  "v_mov_b32 v78, 0\n" \
  "v_mov_b32 v79, 0\n" \
  "v_mov_b32 v80, 0\n" \
  "v_mov_b32 v81, 0\n" \
  "v_mov_b32 v82, 0\n" \
  "v_mov_b32 v83, 0\n" \
  "v_mov_b32 v84, 0\n" \
  "v_mov_b32 v85, 0\n" \
  "v_mov_b32 v86, 0\n" \
  "v_mov_b32 v87, 0\n" \
  "s_waitcnt lgkmcnt(0)\n" \
  "s_add_u32 s49, s76, s96\n" \
  "LUKs0_%=:\n" \
  "s_memtime s[78:79]\n" \
  "s_waitcnt lgkmcnt(0)\n" \
  "s_sub_i32 s74, s49, s78\n" \
  "s_cmp_gt_i32 s74, 0\n" \
  "s_cbranch_scc1 LUKs0_%=\n" \
  "s_add_u32 m0, s40, 0\n" \
  "s_add_u32 s75, s41, 0\n" \
  "buffer_load_dwordx4 v8, s[36:39], s75 offen offset:0 sc1 lds\n" \
  "buffer_load_dwordx4 v8, s[36:39], s75 offen offset:1024 sc1 lds\n" \
  "buffer_load_dwordx4 v8, s[36:39], s75 offen offset:2048 sc1 lds\n" \
  "buffer_load_dwordx4 v8, s[36:39], s75 offen offset:3072 sc1 lds\n" \
  "s_add_u32 m0, s40, 4096\n" \
  "s_add_u32 s75, s41, 4096\n" \
  "buffer_load_dwordx4 v8, s[36:39], s75 offen offset:0 sc1 lds\n" \
  "buffer_load_dwordx4 v8, s[36:39], s75 offen offset:1024 sc1 lds\n" \
  "s_add_u32 m0, s40, 4096\n" \
  "s_add_u32 s75, s41, 4096\n" \
  "buffer_load_dwordx4 v8, s[36:39], s75 offen offset:2048 sc1 lds\n" \
  "buffer_load_dwordx4 v8, s[36:39], s75 offen offset:3072 sc1 lds\n" \
  "s_add_u32 m0, s40, 8192\n" \
  "s_add_u32 s75, s41, 16384\n" \
  "buffer_load_dwordx4 v8, s[36:39], s75 offen offset:0 sc1 lds\n" \
  "buffer_load_dwordx4 v8, s[36:39], s75 offen offset:1024 sc1 lds\n" \
  "buffer_load_dwordx4 v8, s[36:39], s75 offen offset:2048 sc1 lds\n" \
  "buffer_load_dwordx4 v8, s[36:39], s75 offen offset:3072 sc1 lds\n" \
  "s_add_u32 m0, s40, 12288\n" \
  "s_add_u32 s75, s41, 20480\n" \
  "buffer_load_dwordx4 v8, s[36:39], s75 offen offset:0 sc1 lds\n" \
  "buffer_load_dwordx4 v8, s[36:39], s75 offen offset:1024 sc1 lds\n" \
  "buffer_load_dwordx4 v8, s[36:39], s75 offen offset:2048 sc1 lds\n" \
  "buffer_load_dwordx4 v8, s[36:39], s75 offen offset:3072 sc1 lds\n" \
  "s_add_u32 m0, s40, 16384\n" \
  "s_add_u32 s75, s41, 32768\n" \
  "buffer_load_dwordx4 v8, s[36:39], s75 offen offset:0 sc1 lds\n" \
  "buffer_load_dwordx4 v8, s[36:39], s75 offen offset:1024 sc1 lds\n" \
  "s_add_u32 m0, s40, 16384\n" \
  "s_add_u32 s75, s41, 32768\n" \
  "buffer_load_dwordx4 v8, s[36:39], s75 offen offset:2048 sc1 lds\n" \
  "buffer_load_dwordx4 v8, s[36:39], s75 offen offset:3072 sc1 lds\n" \
  "s_add_u32 m0, s40, 20480\n" \
  "s_add_u32 s75, s41, 36864\n" \
  "buffer_load_dwordx4 v8, s[36:39], s75 offen offset:0 sc1 lds\n" \
  "buffer_load_dwordx4 v8, s[36:39], s75 offen offset:1024 sc1 lds\n" \
  "buffer_load_dwordx4 v8, s[36:39], s75 offen offset:2048 sc1 lds\n" \
  "buffer_load_dwordx4 v8, s[36:39], s75 offen offset:3072 sc1 lds\n" \
  "s_xor_b32 s40, s40, 0x8000\n" \
  "s_cmp_eq_u32 s68, 64\n" \
  "s_cbranch_scc1 LENDs0_%=\n" \
  "v_readlane_b32 s72, v11, s68\n" \
  "s_nop 0\n" \
  "s_add_u32 s69, s72, 2048\n" \
  "s_add_u32 s70, s72, 6144\n" \
  "s_add_u32 s44, s72, s93\n" \
  "s_add_u32 s45, s72, s94\n" \
  "s_add_u32 s46, s72, s95\n" \
  "v_readlane_b32 s41, v7, s68\n" \
  "s_nop 1\n" \
  "s_branch LOOPs0_%=\n" \
  "LFFs0_%=:\n" \
  "LQFs0_%=:\n" \
  "buffer_load_dword v19, v18, s[36:39], 0 offen sc1\n" \
  "s_waitcnt vmcnt(0)\n" \
  "v_cmp_ge_u32 vcc, s43, v19\n" \
  "s_nop 3\n" \
  "s_cbranch_vccz LRFs0_%=\n" \
  "s_add_u32 s73, s73, 1\n" \
  "s_cmp_gt_u32 s73, 20000\n" \
  "s_cbranch_scc1 LRFs0_%=\n" \
  "s_sleep 16\n" \
  "s_branch LQFs0_%=\n" \
  "LRFs0_%=:\n" \
  "s_branch LFOKs0_%=\n" \
  "LMFs0_%=:\n" \
  "ds_read_b128 v[88:91], v3 offset:8192\n" \
  "ds_read_b128 v[92:95], v3 offset:9216\n" \
  "ds_read_b128 v[96:99], v3 offset:10240\n" \
  "ds_read_b128 v[100:103], v3 offset:11264\n" \
  "ds_read_b128 v[104:107], v3 offset:0\n" \
  "ds_read_b128 v[108:111], v3 offset:1024\n" \
  "ds_read_b128 v[112:115], v3 offset:22528\n" \
  "ds_read_b128 v[116:119], v3 offset:23552\n" \
  "v_mov_b32 v60, -1\n" \
  "v_mov_b32 v61, -1\n" \
  "ds_write_b64 v6, v[60:61] offset:32768\n" \
  "ds_write_b64 v6, v[60:61] offset:33280\n" \
  "ds_write_b64 v6, v[60:61] offset:36864\n" \
  "ds_write_b64 v2, v[60:61] offset:0\n" \
  "v_mov_b32 v20, 0\n" \
  "v_mov_b32 v21, 0\n" \
  "v_mov_b32 v22, 0\n" \
  "v_mov_b32 v23, 0\n" \
  "v_mov_b32 v64, 0\n" \
  "v_mov_b32 v65, 0\n" \
  "v_mov_b32 v66, 0\n" \
  "v_mov_b32 v67, 0\n" \
  "v_mov_b32 v68, 0\n" \
  "v_mov_b32 v69, 0\n" \
  "v_mov_b32 v70, 0\n" \
  "v_mov_b32 v71, 0\n" \
  "v_mov_b32 v72, 0\n" \
  "v_mov_b32 v73, 0\n" \
  "v_mov_b32 v74, 0\n" \
  "v_mov_b32 v75, 0\n" \
  "v_mov_b32 v76, 0\n" \
  "v_mov_b32 v77, 0\n" \
  "v_mov_b32 v78, 0\n" \
  "v_mov_b32 v79, 0\n" \
  "v_mov_b32 v80, 0\n" \
  "v_mov_b32 v81, 0\n" \
  "v_mov_b32 v82, 0\n" \
  "v_mov_b32 v83, 0\n" \
  "v_mov_b32 v84, 0\n" \
  "v_mov_b32 v85, 0\n" \
  "v_mov_b32 v86, 0\n" \
  "v_mov_b32 v87, 0\n" \
  "s_waitcnt lgkmcnt(0)\n" \
  "s_barrier\n" \
  "buffer_load_dwordx4 v[24:27], v1, s[36:39], s72 offen offset:0 sc1\n" \
  "s_mov_b32 m0, 65536\n" \
  "s_nop 0\n" \
  "buffer_load_dwordx4 v1, s[36:39], s69 offen sc1 lds\n" \
  "buffer_load_dwordx4 v[28:31], v1, s[36:39], s72 offen offset:1024 sc1\n" \
  "s_mov_b32 m0, 69632\n" \
  "s_nop 0\n" \
  "buffer_load_dwordx4 v1, s[36:39], s70 offen sc1 lds\n" \
  "ds_write_b64 v6, v[60:61] offset:0\n" \
  "ds_write_b64 v6, v[60:61] offset:512\n" \
  "ds_write_b64 v6, v[60:61] offset:4096\n" \
  "s_branch LMs0_%=\n" \
  "LMQs0_%=:\n" \
  "s_waitcnt lgkmcnt(0)\n" \
  "s_barrier\n" \
  "v_mov_b32 v38, -1\n" \
  "LMXs0_%=:\n" \
  "v_cmp_u_f32 vcc, v38, v38\n" \
  "s_nop 3\n" \
  "s_cmp_lg_u64 vcc, 0\n" \
  "s_cselect_b32 s74, 2, 1\n" \
  "s_cmp_gt_u32 s56, 20000\n" \
  "s_cselect_b32 s74, 1, s74\n" \
  "v_mov_b32 v34, s74\n" \
  "ds_write_b32 v12, v34 offset:512\n" \
  "s_waitcnt lgkmcnt(0)\n" \
  "s_barrier\n" \
  "s_barrier\n" \
  "v_mov_b32 v34, 0\n" \
  "ds_write_b32 v12, v34 offset:512\n" \
  "s_lshr_b32 s74, s74, 1\n" \
  "s_add_u32 s56, s56, s74\n" \
  "s_cmp_lg_u32 s74, 0\n" \
  "s_cbranch_scc1 LMFs0_%=\n" \
  "s_add_u32 s57, s57, 1\n" \
  "v_mov_b32 v46, v116\n" \
  "v_mov_b32 v47, v117\n" \
  "ds_read_b128 v[88:91], v5 offset:0\n" \
  "ds_read_b128 v[92:95], v5 offset:2048\n" \
  "ds_read_b128 v[96:99], v5 offset:4096\n" \
  "ds_read_b128 v[100:103], v5 offset:6144\n" \
  "ds_read_b64 v[104:105], v10 offset:0\n" \
  "ds_read_b64 v[106:107], v10 offset:1024\n" \
  "ds_read_b64 v[108:109], v10 offset:2048\n" \
  "ds_read_b64 v[110:111], v10 offset:3072\n" \
  "s_waitcnt lgkmcnt(7)\n" \
  "v_mfma_f32_16x16x32_bf16 v[48:51], v[88:91], v[24:27], 0\n" \
  "s_waitcnt lgkmcnt(6)\n" \
  "v_mfma_f32_16x16x32_bf16 v[48:51], v[92:95], v[28:31], v[48:51]\n" \
  "s_waitcnt lgkmcnt(5)\n" \
  "v_mfma_f32_16x16x32_bf16 v[48:51], v[96:99], v[112:115], v[48:51]\n" \
  "s_waitcnt lgkmcnt(4)\n" \
  "v_mfma_f32_16x16x32_bf16 v[48:51], v[100:103], v[64:67], v[48:51]\n" \
  "s_waitcnt lgkmcnt(2)\n" \
  "v_mfma_f32_16x16x32_bf16 v[48:51], v[104:107], v[68:71], v[48:51]\n" \
  "s_waitcnt lgkmcnt(0)\n" \
  "v_mfma_f32_16x16x32_bf16 v[48:51], v[108:111], v[72:75], v[48:51]\n" \
  "s_nop 7\n" \
  "ds_read_b128 v[88:91], v5 offset:128\n" \
  "ds_read_b128 v[92:95], v5 offset:2176\n" \
  "ds_read_b128 v[96:99], v5 offset:4224\n" \
  "ds_read_b128 v[100:103], v5 offset:6272\n" \
  "ds_read_b64 v[104:105], v10 offset:128\n" \
  "ds_read_b64 v[106:107], v10 offset:1152\n" \
  "ds_read_b64 v[108:109], v10 offset:2176\n" \
  "ds_read_b64 v[110:111], v10 offset:3200\n" \
  "s_waitcnt lgkmcnt(7)\n" \
  "v_mfma_f32_16x16x32_bf16 v[52:55], v[88:91], v[24:27], 0\n" \
  "s_waitcnt lgkmcnt(6)\n" \
  "v_mfma_f32_16x16x32_bf16 v[52:55], v[92:95], v[28:31], v[52:55]\n" \
  "s_waitcnt lgkmcnt(5)\n" \
  "v_mfma_f32_16x16x32_bf16 v[52:55], v[96:99], v[112:115], v[52:55]\n" \
  "s_waitcnt lgkmcnt(4)\n" \
  "v_mfma_f32_16x16x32_bf16 v[52:55], v[100:103], v[64:67], v[52:55]\n" \
  "s_waitcnt lgkmcnt(2)\n" \
  "v_mfma_f32_16x16x32_bf16 v[52:55], v[104:107], v[68:71], v[52:55]\n" \
  "s_waitcnt lgkmcnt(0)\n" \
  "v_mfma_f32_16x16x32_bf16 v[52:55], v[108:111], v[72:75], v[52:55]\n" \
  "s_nop 7\n" \
  "v_add_f32_dpp v48, v48, v48 row_ror:8 row_mask:0xf bank_mask:0xf\n" \
  "v_mul_f32 v48, v4, v48\n" \
  "v_add_f32_dpp v49, v49, v49 row_ror:8 row_mask:0xf bank_mask:0xf\n" \
  "v_mul_f32 v49, v4, v49\n" \
  "v_add_f32_dpp v50, v50, v50 row_ror:8 row_mask:0xf bank_mask:0xf\n" \
  "v_mul_f32 v50, v4, v50\n" \
  "v_add_f32_dpp v51, v51, v51 row_ror:8 row_mask:0xf bank_mask:0xf\n" \
  "v_mul_f32 v51, v4, v51\n" \
  "v_add_f32_dpp v52, v52, v52 row_ror:8 row_mask:0xf bank_mask:0xf\n" \
  "v_mul_f32 v52, v4, v52\n" \
  "v_add_f32_dpp v53, v53, v53 row_ror:8 row_mask:0xf bank_mask:0xf\n" \
  "v_mul_f32 v53, v4, v53\n" \
  "v_add_f32_dpp v54, v54, v54 row_ror:8 row_mask:0xf bank_mask:0xf\n" \
  "v_mul_f32 v54, v4, v54\n" \
  "v_add_f32_dpp v55, v55, v55 row_ror:8 row_mask:0xf bank_mask:0xf\n" \
  "v_mul_f32 v55, v4, v55\n" \
  "v_max_f32 v32, v46, v47\n" \
  "v_max3_f32 v32, v32, v48, v49\n" \
  "v_max3_f32 v32, v32, v50, v51\n" \
  "v_max3_f32 v32, v32, v52, v53\n" \
  "v_max3_f32 v32, v32, v54, v55\n" \
  "v_mov_b32 v33, v32\n" \
  "s_nop 1\n" \
  "v_permlane16_swap_b32 v32, v33\n" \
  "v_max_f32 v32, v32, v33\n" \
  "v_mov_b32 v33, v32\n" \
  "s_nop 1\n" \
  "v_permlane32_swap_b32 v32, v33\n" \
  "v_max_f32 v32, v32, v33\n" \
  "v_sub_f32 v46, v46, v32\n" \
  "v_sub_f32 v47, v47, v32\n" \
  "v_sub_f32 v48, v48, v32\n" \
  "v_sub_f32 v49, v49, v32\n" \
  "v_sub_f32 v50, v50, v32\n" \
  "v_sub_f32 v51, v51, v32\n" \
  "v_sub_f32 v52, v52, v32\n" \
  "v_sub_f32 v53, v53, v32\n" \
  "v_sub_f32 v54, v54, v32\n" \
  "v_sub_f32 v55, v55, v32\n" \
  "v_exp_f32 v46, v46\n" \
  "v_exp_f32 v47, v47\n" \
  "v_exp_f32 v48, v48\n" \
  "v_exp_f32 v49, v49\n" \
  "v_exp_f32 v50, v50\n" \
  "v_exp_f32 v51, v51\n" \
  "v_exp_f32 v52, v52\n" \
  "v_exp_f32 v53, v53\n" \
  "v_exp_f32 v54, v54\n" \
  "v_exp_f32 v55, v55\n" \
  "v_add_f32 v32, v46, v47\n" \
  "v_add_f32 v32, v32, v48\n" \
  "v_add_f32 v32, v32, v49\n" \
  "v_add_f32 v32, v32, v50\n" \
  "v_add_f32 v32, v32, v51\n" \
  "v_add_f32 v32, v32, v52\n" \
  "v_add_f32 v32, v32, v53\n" \
  "v_add_f32 v32, v32, v54\n" \
  "v_add_f32 v32, v32, v55\n" \
  "v_mov_b32 v33, v32\n" \
  "s_nop 1\n" \
  "v_permlane16_swap_b32 v32, v33\n" \
  "v_add_f32 v32, v32, v33\n" \
  "v_mov_b32 v33, v32\n" \
  "s_nop 1\n" \
  "v_permlane32_swap_b32 v32, v33\n" \
  "v_add_f32 v32, v32, v33\n" \
  "v_rcp_f32 v32, v32\n" \
  "v_cvt_pk_bf16_f32 v34, v48, v49\n" \
  "v_pk_min_u16 v34, v34, -2 op_sel_hi:[1,0]\n" \
  "v_cvt_pk_bf16_f32 v35, v50, v51\n" \
  "v_pk_min_u16 v35, v35, -2 op_sel_hi:[1,0]\n" \
  "v_cvt_pk_bf16_f32 v36, v52, v53\n" \
  "v_pk_min_u16 v36, v36, -2 op_sel_hi:[1,0]\n" \
  "v_cvt_pk_bf16_f32 v37, v54, v55\n" \
  "v_pk_min_u16 v37, v37, -2 op_sel_hi:[1,0]\n" \
  "v_cndmask_b32 v34, v35, v34, s[34:35]\n" \
  "buffer_store_dword v34, v16, s[36:39], s72 offen offset:0 sc1\n" \
  "buffer_store_dword v34, v16, s[36:39], s44 offen offset:0 sc1\n" \
  "buffer_store_dword v34, v16, s[36:39], s45 offen offset:0 sc1\n" \
  "buffer_store_dword v34, v16, s[36:39], s46 offen offset:0 sc1\n" \
  "v_cndmask_b32 v36, v37, v36, s[34:35]\n" \
  "buffer_store_dword v36, v16, s[36:39], s72 offen offset:256 sc1\n" \
  "buffer_store_dword v36, v16, s[36:39], s44 offen offset:256 sc1\n" \
  "buffer_store_dword v36, v16, s[36:39], s45 offen offset:256 sc1\n" \
  "buffer_store_dword v36, v16, s[36:39], s46 offen offset:256 sc1\n" \
  "v_cndmask_b32 v46, v14, v46, s[34:35]\n" \
  "v_cndmask_b32 v47, v15, v47, s[34:35]\n" \
  "v_mul_f32 v46, v46, v32\n" \
  "v_mul_f32 v47, v47, v32\n" \
  "v_cvt_pk_bf16_f32 v46, v46, v47\n" \
  "v_pk_min_u16 v46, v46, -2 op_sel_hi:[1,0]\n" \
  "v_or_b32 v33, v14, v15\n" \
  "v_cmp_eq_u32 vcc, 0, v33\n" \
  "v_mov_b32 v33, 0xfffe\n" \
  "s_nop 3\n" \
  "s_andn2_b64 vcc, vcc, s[34:35]\n" \
  "s_nop 3\n" \
  "v_cndmask_b32 v46, v46, v33, vcc\n" \
  "s_waitcnt vmcnt(0) lgkmcnt(0)\n" \
  "buffer_store_dword v46, v13, s[36:39], s72 offen offset:0 sc1\n" \
  "buffer_store_dword v46, v13, s[36:39], s44 offen offset:0 sc1\n" \
  "buffer_store_dword v46, v13, s[36:39], s45 offen offset:0 sc1\n" \
  "buffer_store_dword v46, v13, s[36:39], s46 offen offset:0 sc1\n" \
  "s_branch LMYs0_%=\n" \
  "LENDs0_%=:\n" \
  "s_cmp_lg_u32 s38, 0\n" \
  "s_cbranch_scc1 LFINs0_%=\n" \
  "s_waitcnt vmcnt(0) lgkmcnt(0)\n" \
  "s_mov_b32 s38, s47\n" \
  "s_mov_b32 s54, s97\n" \
  "s_mov_b32 s96, s98\n" \
  "s_mov_b32 s68, 0\n" \
  "s_mov_b32 s73, 0\n" \
  "s_mov_b32 s56, 0\n" \
  "s_mov_b32 s57, 0\n" \
  "s_branch LPASSs0_%=\n" \
  "LFINs0_%=:\n" \
  "s_branch LDONE_%=\n" \
  "LSTARTs1_%=:\n" \
  "s_mov_b32 s79, 0\n" \
  "LSDs1_%=:\n" \
  "buffer_load_dwordx2 v[64:65], v19, s[36:39], 0 offen sc1\n" \
  "s_waitcnt vmcnt(0)\n" \
  "v_readlane_b32 s76, v64, 3\n" \
  "s_nop 0\n" \
  "s_add_u32 s76, s76, 1\n" \
  "v_cmp_ne_u32 vcc, s76, v64\n" \
  "s_nop 3\n" \
  "s_and_b32 s76, vcc_lo, 7\n" \
  "s_cbranch_scc0 LSFs1_%=\n" \
  "s_add_u32 s79, s79, 1\n" \
  "s_cmp_gt_u32 s79, 200000\n" \
  "s_cbranch_scc1 LSFs1_%=\n" \
  "s_sleep 4\n" \
  "s_branch LSDs1_%=\n" \
  "LSFs1_%=:\n" \
  "v_readlane_b32 s76, v65, 0\n" \
  "v_readlane_b32 s77, v65, 1\n" \
  "v_readlane_b32 s78, v65, 2\n" \
  "s_nop 1\n" \
  "v_lshrrev_b32 v64, 21, v7\n" \
  "v_lshrrev_b64 v[66:67], v64, s[76:77]\n" \
  "v_lshrrev_b32_e64 v68, v64, s78\n" \
  "v_cmp_gt_u32 vcc, 64, v64\n" \
  "s_nop 1\n" \
  "v_cndmask_b32 v66, v68, v66, vcc\n" \
  "v_and_b32 v66, 1, v66\n" \
  "v_lshlrev_b32 v66, 13, v66\n" \
  "v_xor_b32 v7, v7, v66\n" \
  "v_lshrrev_b32 v64, 21, v11\n" \
  "v_lshrrev_b64 v[66:67], v64, s[76:77]\n" \
  "v_lshrrev_b32_e64 v68, v64, s78\n" \
  "v_cmp_gt_u32 vcc, 64, v64\n" \
  "s_nop 1\n" \
  "v_cndmask_b32 v66, v68, v66, vcc\n" \
  "v_and_b32 v66, 1, v66\n" \
  "v_lshlrev_b32 v66, 13, v66\n" \
  "v_xor_b32 v11, v11, v66\n" \
  "v_lshrrev_b32 v64, 21, v28\n" \
  "v_lshrrev_b64 v[66:67], v64, s[76:77]\n" \
  "v_lshrrev_b32_e64 v68, v64, s78\n" \
  "v_cmp_gt_u32 vcc, 64, v64\n" \
  "s_nop 1\n" \
  "v_cndmask_b32 v66, v68, v66, vcc\n" \
  "v_and_b32 v66, 1, v66\n" \
  "v_lshlrev_b32 v66, 13, v66\n" \
  "v_xor_b32 v28, v28, v66\n" \
  "v_lshrrev_b32 v64, 21, v14\n" \
  "v_lshrrev_b64 v[66:67], v64, s[76:77]\n" \
  "v_lshrrev_b32_e64 v68, v64, s78\n" \
  "v_cmp_gt_u32 vcc, 64, v64\n" \
  "s_nop 1\n" \
  "v_cndmask_b32 v66, v68, v66, vcc\n" \
  "v_and_b32 v66, 1, v66\n" \
  "v_lshlrev_b32 v66, 13, v66\n" \
  "v_xor_b32 v14, v14, v66\n" \
  "v_lshrrev_b32 v64, 21, v15\n" \
  "v_lshrrev_b64 v[66:67], v64, s[76:77]\n" \
  "v_lshrrev_b32_e64 v68, v64, s78\n" \
  "v_cmp_gt_u32 vcc, 64, v64\n" \
  "s_nop 1\n" \
  "v_cndmask_b32 v66, v68, v66, vcc\n" \
  "v_and_b32 v66, 1, v66\n" \
  "v_lshlrev_b32 v66, 13, v66\n" \
  "v_xor_b32 v15, v15, v66\n" \
  "s_lshl_b32 s74, s76, 12\n" \
  "s_and_b32 s74, s74, 0x2000\n" \
  "s_or_b32 s55, s55, s74\n" \
  "s_mov_b32 s47, s38\n" \
  "s_mov_b32 s38, 0\n" \
  "s_mov_b32 s68, 63\n" \
  "s_mov_b32 s73, 20001\n" \
  "s_mov_b32 s56, 19999\n" \
  "s_mov_b32 s97, s54\n" \
  "s_mov_b32 s54, 0\n" \
  "s_mov_b32 s98, s96\n" \
  "s_mov_b32 s96, 0\n" \
  "LPASSs1_%=:\n" \
  "s_mov_b32 s40, 0\n" \
  "s_mov_b32 s42, 0x40000000\n" \
  "v_and_b32 v20, 0x7fff, v20\n" \
  "v_and_b32 v21, 0x7fff, v21\n" \
  "v_and_b32 v18, 0x7fff, v18\n" \
  "s_barrier\n" \
  "s_add_u32 m0, s40, 4096\n" \
  "s_add_u32 s75, s55, 4096\n" \
  "buffer_load_dwordx4 v8, s[36:39], s75 offen offset:2048 sc1 lds\n" \
  "buffer_load_dwordx4 v8, s[36:39], s75 offen offset:3072 sc1 lds\n" \
  "s_add_u32 m0, s40, 8192\n" \
  "s_add_u32 s75, s55, 16384\n" \
  "buffer_load_dwordx4 v8, s[36:39], s75 offen offset:0 sc1 lds\n" \
  "buffer_load_dwordx4 v8, s[36:39], s75 offen offset:1024 sc1 lds\n" \
  "buffer_load_dwordx4 v8, s[36:39], s75 offen offset:2048 sc1 lds\n" \
  "buffer_load_dwordx4 v8, s[36:39], s75 offen offset:3072 sc1 lds\n" \
  "s_xor_b32 s40, s40, 0x8000\n" \
  "v_readlane_b32 s72, v11, 0\n" \
  "s_nop 0\n" \
  "s_add_u32 s69, s72, 3072\n" \
  "s_add_u32 s70, s72, 7168\n" \
  "s_add_u32 s44, s72, s93\n" \
  "s_add_u32 s45, s72, s94\n" \
  "s_add_u32 s46, s72, s95\n" \
  "v_readlane_b32 s41, v7, 0\n" \
  "s_nop 1\n" \
  "LOOPs1_%=:\n" \
  "v_mov_b32 v60, -1\n" \
  "v_mov_b32 v61, -1\n" \
  "v_mov_b32 v64, 0\n" \
  "v_mov_b32 v65, 0\n" \
  "v_mov_b32 v66, 0\n" \
  "v_mov_b32 v67, 0\n" \
  "v_mov_b32 v68, 0\n" \
  "v_mov_b32 v69, 0\n" \
  "v_mov_b32 v70, 0\n" \
  "v_mov_b32 v71, 0\n" \
  "v_mov_b32 v72, 0\n" \
  "v_mov_b32 v73, 0\n" \
  "v_mov_b32 v74, 0\n" \
  "v_mov_b32 v75, 0\n" \
  "v_mov_b32 v76, 0\n" \
  "v_mov_b32 v77, 0\n" \
  "v_mov_b32 v78, 0\n" \
  "v_mov_b32 v79, 0\n" \
  "v_mov_b32 v80, 0\n" \
  "v_mov_b32 v81, 0\n" \
  "v_mov_b32 v82, 0\n" \
  "v_mov_b32 v83, 0\n" \
  "s_barrier\n" \
  "buffer_load_dwordx4 v[24:27], v1, s[36:39], s72 offen offset:0 sc1\n" \
  "s_mov_b32 m0, 66560\n" \
  "s_nop 0\n" \
  "buffer_load_dwordx4 v1, s[36:39], s69 offen sc1 lds\n" \
  "buffer_load_dwordx4 v[28:31], v1, s[36:39], s72 offen offset:1024 sc1\n" \
  "s_mov_b32 m0, 70656\n" \
  "s_nop 0\n" \
  "buffer_load_dwordx4 v1, s[36:39], s70 offen sc1 lds\n" \
  "ds_read_b128 v[88:91], v3 offset:8192\n" \
  "ds_read_b128 v[92:95], v3 offset:9216\n" \
  "ds_read_b128 v[96:99], v3 offset:10240\n" \
  "ds_read_b128 v[100:103], v3 offset:11264\n" \
  "ds_read_b128 v[104:107], v3 offset:12288\n" \
  "ds_read_b128 v[108:111], v3 offset:13312\n" \
  "ds_read_b128 v[112:115], v3 offset:14336\n" \
  "ds_read_b128 v[116:119], v3 offset:15360\n" \
  "ds_write_b64 v6, v[60:61] offset:33792\n" \
  "ds_write_b64 v6, v[60:61] offset:34304\n" \
  "ds_write_b64 v6, v[60:61] offset:37376\n" \
  "ds_write_b64 v6, v[60:61] offset:1024\n" \
  "ds_write_b64 v6, v[60:61] offset:1536\n" \
  "ds_write_b64 v6, v[60:61] offset:4608\n" \
  "ds_write_b32 v12, v60 offset:0\n" \
  "s_add_u32 s68, s68, 1\n" \
  "LMs1_%=:\n" \
  "buffer_store_dwordx4 v[52:55], v16, s[36:39], s42 offen sc1\n" \
  "buffer_store_dword v56, v17, s[36:39], s42 offen sc1\n" \
  "s_mov_b32 s42, 0x40000000\n" \
  "s_waitcnt vmcnt(5)\n" \
  "s_waitcnt lgkmcnt(13)\n" \
  "v_smfmac_f32_16x16x64_bf16 v[64:67], v[24:27], v[88:95], v0\n" \
  "ds_read_b128 v[88:91], v3 offset:16384\n" \
  "s_waitcnt lgkmcnt(12)\n" \
  "v_smfmac_f32_16x16x64_bf16 v[68:71], v[24:27], v[96:103], v0\n" \
  "ds_read_b128 v[92:95], v3 offset:17408\n" \
  "v_smfmac_f32_16x16x64_bf16 v[72:75], v[24:27], a[0:7], v0\n" \
  "ds_read_b128 v[96:99], v3 offset:18432\n" \
  "v_smfmac_f32_16x16x64_bf16 v[76:79], v[24:27], a[8:15], v0\n" \
  "ds_read_b128 v[100:103], v3 offset:19456\n" \
  "v_smfmac_f32_16x16x64_bf16 v[80:83], v[24:27], a[16:23], v0\n" \
  "s_waitcnt vmcnt(4)\n" \
  "s_waitcnt lgkmcnt(4)\n" \
  "s_barrier\n" \
  "ds_read_b128 v[32:35], v2 offset:2048\n" \
  "s_waitcnt vmcnt(3)\n" \
  "v_smfmac_f32_16x16x64_bf16 v[64:67], v[28:31], a[24:31], v0\n" \
  "ds_read_b128 v[36:39], v2 offset:3072\n" \
  "v_smfmac_f32_16x16x64_bf16 v[68:71], v[28:31], a[32:39], v0\n" \
  "ds_read_b128 v[40:43], v2 offset:4096\n" \
  "v_smfmac_f32_16x16x64_bf16 v[72:75], v[28:31], a[40:47], v0\n" \
  "ds_read_b128 v[44:47], v2 offset:5120\n" \
  "v_smfmac_f32_16x16x64_bf16 v[76:79], v[28:31], a[48:55], v0\n" \
  "v_smfmac_f32_16x16x64_bf16 v[80:83], v[28:31], a[56:63], v0\n" \
  "s_waitcnt lgkmcnt(3)\n" \
  "v_smfmac_f32_16x16x64_bf16 v[64:67], v[32:35], a[64:71], v0\n" \
  "v_smfmac_f32_16x16x64_bf16 v[68:71], v[32:35], a[72:79], v0\n" \
  "v_smfmac_f32_16x16x64_bf16 v[72:75], v[32:35], a[80:87], v0\n" \
  "s_waitcnt lgkmcnt(2)\n" \
  "v_smfmac_f32_16x16x64_bf16 v[64:67], v[36:39], a[104:111], v0\n" \
  "v_smfmac_f32_16x16x64_bf16 v[68:71], v[36:39], v[104:111], v0\n" \
  "s_waitcnt vmcnt(2)\n" \
  "s_barrier\n" \
  "v_smfmac_f32_16x16x64_bf16 v[72:75], v[36:39], a[112:119], v0\n" \
  "ds_read_b128 v[48:51], v2 offset:6144\n" \
  "s_waitcnt lgkmcnt(2)\n" \
  "v_smfmac_f32_16x16x64_bf16 v[64:67], v[40:43], v[112:119], v0\n" \
  "ds_read_b128 v[52:55], v2 offset:7168\n" \
  "v_smfmac_f32_16x16x64_bf16 v[68:71], v[40:43], a[136:143], v0\n" \
  "ds_read_b128 v[56:59], v2 offset:8192\n" \
  "v_smfmac_f32_16x16x64_bf16 v[72:75], v[40:43], a[144:151], v0\n" \
  "ds_read_b128 v[60:63], v2 offset:9216\n" \
  "s_waitcnt lgkmcnt(4)\n" \
  "v_smfmac_f32_16x16x64_bf16 v[64:67], v[44:47], v[88:95], v0\n" \
  "ds_read_b128 v[104:107], v3 offset:20480\n" \
  "v_smfmac_f32_16x16x64_bf16 v[68:71], v[44:47], a[176:183], v0\n" \
  "ds_read_b128 v[108:111], v3 offset:21504\n" \
  "v_smfmac_f32_16x16x64_bf16 v[72:75], v[44:47], a[184:191], v0\n" \
  "ds_read_b128 v[88:91], v20 offset:0\n" \
  "s_waitcnt lgkmcnt(6)\n" \
  "v_smfmac_f32_16x16x64_bf16 v[64:67], v[48:51], a[216:223], v0\n" \
  "ds_read_b128 v[92:95], v20 offset:2048\n" \
  "s_waitcnt lgkmcnt(6)\n" \
  "v_smfmac_f32_16x16x64_bf16 v[64:67], v[52:55], v[128:135], v0\n" \
  "s_waitcnt lgkmcnt(5)\n" \
  "v_smfmac_f32_16x16x64_bf16 v[64:67], v[56:59], v[168:175], v0\n" \
  "s_waitcnt lgkmcnt(4)\n" \
  "v_smfmac_f32_16x16x64_bf16 v[64:67], v[60:63], v[96:103], v0\n" \
  "ds_read_b128 v[96:99], v20 offset:4096\n" \
  "v_smfmac_f32_16x16x64_bf16 v[68:71], v[48:51], a[224:231], v0\n" \
  "ds_read_b128 v[100:103], v20 offset:6144\n" \
  "s_waitcnt lgkmcnt(4)\n" \
  "v_smfmac_f32_16x16x64_bf16 v[68:71], v[52:55], v[104:111], v0\n" \
  "ds_read_b64 v[104:105], v21 offset:0\n" \
  "v_cvt_pk_bf16_f32 v64, v64, v65\n" \
  "v_smfmac_f32_16x16x64_bf16 v[68:71], v[56:59], v[176:183], v0\n" \
  "ds_read_b64 v[106:107], v21 offset:1024\n" \
  "v_cvt_pk_bf16_f32 v65, v66, v67\n" \
  "v_smfmac_f32_16x16x64_bf16 v[68:71], v[60:63], v[216:223], v0\n" \
  "ds_write_b64 v6, v[64:65] offset:33792\n" \
  "v_cmp_u_f32 vcc, v64, v65\n" \
  "v_smfmac_f32_16x16x64_bf16 v[72:75], v[48:51], a[232:239], v0\n" \
  "s_cbranch_vccnz LMQs1_%=\n" \
  "v_smfmac_f32_16x16x64_bf16 v[72:75], v[52:55], v[136:143], v0\n" \
  "ds_read_b64 v[108:109], v21 offset:2048\n" \
  "v_cvt_pk_bf16_f32 v68, v68, v69\n" \
  "v_smfmac_f32_16x16x64_bf16 v[72:75], v[56:59], v[184:191], v0\n" \
  "ds_read_b64 v[110:111], v21 offset:3072\n" \
  "v_cvt_pk_bf16_f32 v69, v70, v71\n" \
  "v_smfmac_f32_16x16x64_bf16 v[72:75], v[60:63], v[224:231], v0\n" \
  "ds_write_b64 v6, v[68:69] offset:34304\n" \
  "v_smfmac_f32_16x16x64_bf16 v[76:79], v[32:35], a[88:95], v0\n" \
  "ds_read_b64_tr_b16 v[24:25], v9 offset:32768\n" \
  "v_smfmac_f32_16x16x64_bf16 v[76:79], v[36:39], a[120:127], v0\n" \
  "ds_read_b64_tr_b16 v[28:29], v9 offset:33792\n" \
  "v_cvt_pk_bf16_f32 v72, v72, v73\n" \
  "v_smfmac_f32_16x16x64_bf16 v[76:79], v[40:43], a[152:159], v0\n" \
  "ds_read_b64_tr_b16 v[112:113], v9 offset:34816\n" \
  "v_cvt_pk_bf16_f32 v73, v74, v75\n" \
  "v_smfmac_f32_16x16x64_bf16 v[76:79], v[44:47], a[192:199], v0\n" \
  "ds_write_b64 v6, v[72:73] offset:37376\n" \
  "v_smfmac_f32_16x16x64_bf16 v[76:79], v[48:51], a[240:247], v0\n" \
  "ds_read_b64_tr_b16 v[64:65], v9 offset:35840\n" \
  "v_smfmac_f32_16x16x64_bf16 v[76:79], v[52:55], v[144:151], v0\n" \
  "s_waitcnt lgkmcnt(8)\n" \
  "ds_read_b64_tr_b16 v[26:27], v9 offset:33280\n" \
  "v_smfmac_f32_16x16x64_bf16 v[76:79], v[56:59], v[192:199], v0\n" \
  "ds_read_b64_tr_b16 v[30:31], v9 offset:34304\n" \
  "v_smfmac_f32_16x16x64_bf16 v[76:79], v[60:63], v[232:239], v0\n" \
  "ds_read_b64_tr_b16 v[114:115], v9 offset:35328\n" \
  "v_smfmac_f32_16x16x64_bf16 v[80:83], v[32:35], a[96:103], v0\n" \
  "ds_read_b64_tr_b16 v[66:67], v9 offset:36352\n" \
  "v_smfmac_f32_16x16x64_bf16 v[80:83], v[36:39], a[128:135], v0\n" \
  "ds_read_b64_tr_b16 v[68:69], v9 offset:36864\n" \
  "v_cvt_pk_bf16_f32 v76, v76, v77\n" \
  "v_smfmac_f32_16x16x64_bf16 v[80:83], v[40:43], a[160:167], v0\n" \
  "ds_read_b64_tr_b16 v[70:71], v9 offset:37376\n" \
  "v_cvt_pk_bf16_f32 v77, v78, v79\n" \
  "v_smfmac_f32_16x16x64_bf16 v[80:83], v[44:47], a[200:207], v0\n" \
  "ds_write_b64 v6, v[76:77] offset:1024\n" \
  "v_smfmac_f32_16x16x64_bf16 v[80:83], v[48:51], a[248:255], v0\n" \
  "s_waitcnt lgkmcnt(8)\n" \
  "ds_read_b64_tr_b16 v[72:73], v9 offset:37888\n" \
  "s_waitcnt lgkmcnt(7)\n" \
  "v_mfma_f32_16x16x32_bf16 v[116:119], v[88:91], v[24:27], 0\n" \
  "ds_read_b64_tr_b16 v[74:75], v9 offset:38400\n" \
  "s_waitcnt lgkmcnt(7)\n" \
  "v_mfma_f32_16x16x32_bf16 v[116:119], v[92:95], v[28:31], v[116:119]\n" \
  "s_waitcnt lgkmcnt(6)\n" \
  "v_mfma_f32_16x16x32_bf16 v[116:119], v[96:99], v[112:115], v[116:119]\n" \
  "ds_read_b128 v[84:87], v2 offset:0\n" \
  "s_waitcnt lgkmcnt(6)\n" \
  "v_mfma_f32_16x16x32_bf16 v[116:119], v[100:103], v[64:67], v[116:119]\n" \
  "v_smfmac_f32_16x16x64_bf16 v[80:83], v[52:55], v[152:159], v0\n" \
  "v_smfmac_f32_16x16x64_bf16 v[80:83], v[56:59], v[200:207], v0\n" \
  "v_smfmac_f32_16x16x64_bf16 v[80:83], v[60:63], v[240:247], v0\n" \
  "s_waitcnt lgkmcnt(4)\n" \
  "v_mfma_f32_16x16x32_bf16 v[116:119], v[104:107], v[68:71], v[116:119]\n" \
  "s_waitcnt lgkmcnt(1)\n" \
  "v_mfma_f32_16x16x32_bf16 v[116:119], v[108:111], v[72:75], v[116:119]\n" \
  "s_waitcnt lgkmcnt(0)\n" \
  "v_cvt_pk_bf16_f32 v80, v80, v81\n" \
  "v_cvt_pk_bf16_f32 v81, v82, v83\n" \
  "v_smfmac_f32_16x16x64_bf16 v[84:87], v[40:43], a[168:175], v0\n" \
  "ds_write_b64 v6, v[80:81] offset:1536\n" \
  "v_smfmac_f32_16x16x64_bf16 v[84:87], v[44:47], a[208:215], v0\n" \
  "v_add_f32_dpp v88, v116, v116 row_ror:8 row_mask:0xf bank_mask:0x3\n" \
  "v_add_f32_dpp v89, v117, v117 row_ror:8 row_mask:0xf bank_mask:0x3\n" \
  "v_smfmac_f32_16x16x64_bf16 v[84:87], v[48:51], v[120:127], v0\n" \
  "v_add_f32_dpp v88, v118, v118 row_ror:8 row_mask:0xf bank_mask:0xc\n" \
  "v_add_f32_dpp v89, v119, v119 row_ror:8 row_mask:0xf bank_mask:0xc\n" \
  "v_smfmac_f32_16x16x64_bf16 v[84:87], v[52:55], v[160:167], v0\n" \
  "v_mul_f32 v88, v4, v88\n" \
  "v_mul_f32 v89, v4, v89\n" \
  "v_smfmac_f32_16x16x64_bf16 v[84:87], v[56:59], v[208:215], v0\n" \
  "v_exp_f32 v88, v88\n" \
  "v_smfmac_f32_16x16x64_bf16 v[84:87], v[60:63], v[248:255], v0\n" \
  "v_exp_f32 v89, v89\n" \
  "s_nop 0\n" \
  "v_add_f32 v90, v88, v89\n" \
  "ds_write_b32 v12, v90 offset:0\n" \
  "v_cmp_o_f32 vcc, v88, v89\n" \
  "v_cvt_pk_bf16_f32 v91, v88, v89\n" \
  "s_nop 1\n" \
  "v_cvt_pk_bf16_f32 v84, v84, v85\n" \
  "v_cvt_pk_bf16_f32 v85, v86, v87\n" \
  "ds_write_b64 v6, v[84:85] offset:4608\n" \
  "s_barrier ; timing only\n" \
  "s_mov_b64 exec, vcc\n" \
  "buffer_store_dword v91, v22, s[36:39], s72 offen offset:0 sc1\n" \
  "buffer_store_dword v91, v22, s[36:39], s44 offen offset:0 sc1\n" \
  "buffer_store_dword v91, v22, s[36:39], s45 offen offset:0 sc1\n" \
  "buffer_store_dword v91, v22, s[36:39], s46 offen offset:0 sc1\n" \
  "s_mov_b64 exec, -1\n" \
  "v_mov_b32 v61, 0xf149f2ca\n" \
  "v_cmp_gt_i32 vcc, 0, v13\n" \
  "s_nop 1\n" \
  "v_cndmask_b32 v112, 0, v61, vcc\n" \
  "v_cmp_gt_i32 vcc, 1, v13\n" \
  "s_nop 1\n" \
  "v_cndmask_b32 v113, 0, v61, vcc\n" \
  "v_cmp_gt_i32 vcc, 2, v13\n" \
  "s_nop 1\n" \
  "v_cndmask_b32 v114, 0, v61, vcc\n" \
  "v_cmp_gt_i32 vcc, 3, v13\n" \
  "s_nop 1\n" \
  "v_cndmask_b32 v115, 0, v61, vcc\n" \
  "ds_read_b128 v[24:27], v18 offset:8192\n" \
  "ds_read_b128 v[64:67], v18 offset:0\n" \
  "ds_read_b64_tr_b16 v[48:49], v9 offset:0\n" \
  "ds_read_b64_tr_b16 v[50:51], v9 offset:512\n" \
  "ds_read_b128 v[28:31], v18 offset:10240\n" \
  "ds_read_b128 v[68:71], v18 offset:2048\n" \
  "ds_read_b64_tr_b16 v[52:53], v9 offset:1024\n" \
  "ds_read_b64_tr_b16 v[54:55], v9 offset:1536\n" \
  "ds_read_b128 v[32:35], v18 offset:12288\n" \
  "ds_read_b128 v[72:75], v18 offset:4096\n" \
  "ds_read_b64_tr_b16 v[56:57], v9 offset:2048\n" \
  "ds_read_b64_tr_b16 v[58:59], v9 offset:2560\n" \
  "s_waitcnt lgkmcnt(10)\n" \
  "v_mfma_f32_16x16x32_bf16 v[112:115], v[64:67], v[24:27], v[112:115]\n" \
  "s_waitcnt lgkmcnt(8)\n" \
  "v_mfma_f32_16x16x32_bf16 v[108:111], v[48:51], v[24:27], 0\n" \
  "ds_read_b128 v[36:39], v18 offset:14336\n" \
  "ds_read_b128 v[76:79], v18 offset:6144\n" \
  "ds_read_b64_tr_b16 v[48:49], v9 offset:3072\n" \
  "ds_read_b64_tr_b16 v[50:51], v9 offset:3584\n" \
  "s_waitcnt lgkmcnt(10)\n" \
  "v_mfma_f32_16x16x32_bf16 v[112:115], v[68:71], v[28:31], v[112:115]\n" \
  "s_waitcnt lgkmcnt(8)\n" \
  "v_mfma_f32_16x16x32_bf16 v[108:111], v[52:55], v[28:31], v[108:111]\n" \
  "ds_read_b64 v[40:41], v18 offset:20480\n" \
  "ds_read_b64 v[42:43], v18 offset:21504\n" \
  "ds_read_b64 v[80:81], v18 offset:16384\n" \
  "ds_read_b64 v[82:83], v18 offset:17408\n" \
  "ds_read_b64_tr_b16 v[52:53], v9 offset:4096\n" \
  "ds_read_b64_tr_b16 v[54:55], v9 offset:4608\n" \
  "s_waitcnt lgkmcnt(12)\n" \
  "v_mfma_f32_16x16x32_bf16 v[112:115], v[72:75], v[32:35], v[112:115]\n" \
  "s_waitcnt lgkmcnt(10)\n" \
  "v_mfma_f32_16x16x32_bf16 v[108:111], v[56:59], v[32:35], v[108:111]\n" \
  "ds_read_b64 v[44:45], v18 offset:22528\n" \
  "ds_read_b64 v[46:47], v18 offset:23552\n" \
  "ds_read_b64 v[84:85], v18 offset:18432\n" \
  "ds_read_b64 v[86:87], v18 offset:19456\n" \
  "ds_read_b64_tr_b16 v[56:57], v9 offset:5120\n" \
  "s_waitcnt lgkmcnt(8)\n" \
  "ds_read_b64_tr_b16 v[58:59], v9 offset:5632\n" \
  "v_mfma_f32_16x16x32_bf16 v[112:115], v[76:79], v[36:39], v[112:115]\n" \
  "v_mfma_f32_16x16x32_bf16 v[108:111], v[48:51], v[36:39], v[108:111]\n" \
  "s_waitcnt lgkmcnt(8)\n" \
  "v_mfma_f32_16x16x32_bf16 v[112:115], v[80:83], v[40:43], v[112:115]\n" \
  "s_waitcnt lgkmcnt(6)\n" \
  "v_mfma_f32_16x16x32_bf16 v[108:111], v[52:55], v[40:43], v[108:111]\n" \
  "s_barrier\n" \
  "ds_read_b32 v60, v12 offset:512\n" \
  "s_waitcnt lgkmcnt(3)\n" \
  "v_mfma_f32_16x16x32_bf16 v[112:115], v[84:87], v[44:47], v[112:115]\n" \
  "s_waitcnt lgkmcnt(1)\n" \
  "v_mfma_f32_16x16x32_bf16 v[108:111], v[56:59], v[44:47], v[108:111]\n" \
  "s_waitcnt lgkmcnt(0)\n" \
  "v_cmp_ne_u32 vcc, 0, v60\n" \
  "s_nop 3\n" \
  "s_cbranch_vccnz LMXs1_%=\n" \
  "LMYs1_%=:\n" \
  "s_nop 0\n" \
  "v_cmp_u_f32 vcc, v108, v109\n" \
  "v_cmp_u_f32 s[82:83], v110, v111\n" \
  "s_nop 1\n" \
  "s_or_b64 vcc, vcc, s[82:83]\n" \
  "s_cbranch_scc1 LAR1_%=\n" \
  "LAK1_%=:\n" \
  "v_permlane32_swap_b32 v108, v110\n" \
  "v_permlane32_swap_b32 v109, v111\n" \
  "v_pk_add_f32 v[110:111], v[108:109], v[110:111]\n" \
  "v_mov_b32 v32, 0x3d93cd3a\n" \
  "v_mov_b32 v34, 0x3fb8aa3b\n" \
  "v_cvt_pk_bf16_f32 v24, v110, v111\n" \
  "v_cvt_pk_bf16_f32 v25, v112, v113\n" \
  "v_cvt_pk_bf16_f32 v26, v114, v115\n" \
  "v_lshlrev_b32 v110, 16, v24\n" \
  "v_and_b32 v111, 0xffff0000, v24\n" \
  "v_pk_mul_f32 v[110:111], v[110:111], v[32:33] op_sel_hi:[1,0]\n" \
  "v_cvt_pk_bf16_f32 v24, v110, v111\n" \
  "v_lshlrev_b32 v110, 16, v24\n" \
  "v_and_b32 v111, 0xffff0000, v24\n" \
  "v_lshlrev_b32 v112, 16, v25\n" \
  "v_and_b32 v113, 0xffff0000, v25\n" \
  "v_pk_mul_f32 v[112:113], v[112:113], v[32:33] op_sel_hi:[1,0]\n" \
  "v_cvt_pk_bf16_f32 v25, v112, v113\n" \
  "v_lshlrev_b32 v112, 16, v25\n" \
  "v_and_b32 v113, 0xffff0000, v25\n" \
  "v_lshlrev_b32 v114, 16, v26\n" \
  "v_and_b32 v115, 0xffff0000, v26\n" \
  "v_pk_mul_f32 v[114:115], v[114:115], v[32:33] op_sel_hi:[1,0]\n" \
  "v_cvt_pk_bf16_f32 v26, v114, v115\n" \
  "v_lshlrev_b32 v114, 16, v26\n" \
  "v_and_b32 v115, 0xffff0000, v26\n" \
  "v_max_f32 v24, v110, v111\n" \
  "v_max3_f32 v24, v24, v112, v113\n" \
  "v_max3_f32 v24, v24, v114, v115\n" \
  "v_mov_b32 v25, v24\n" \
  "s_nop 1\n" \
  "v_permlane16_swap_b32 v24, v25\n" \
  "v_max_f32 v24, v24, v25\n" \
  "v_mov_b32 v25, v24\n" \
  "s_nop 1\n" \
  "v_permlane32_swap_b32 v24, v25\n" \
  "v_max_f32 v24, v24, v25\n" \
  "v_mul_f32 v24, v24, v34\n" \
  "v_pk_fma_f32 v[110:111], v[110:111], v[34:35], v[24:25] op_sel_hi:[1,0,0] neg_lo:[0,0,1] neg_hi:[0,0,1]\n" \
  "v_pk_fma_f32 v[112:113], v[112:113], v[34:35], v[24:25] op_sel_hi:[1,0,0] neg_lo:[0,0,1] neg_hi:[0,0,1]\n" \
  "v_pk_fma_f32 v[114:115], v[114:115], v[34:35], v[24:25] op_sel_hi:[1,0,0] neg_lo:[0,0,1] neg_hi:[0,0,1]\n" \
  "v_exp_f32 v110, v110\n" \
  "v_exp_f32 v111, v111\n" \
  "v_exp_f32 v112, v112\n" \
  "v_exp_f32 v113, v113\n" \
  "v_exp_f32 v114, v114\n" \
  "v_exp_f32 v115, v115\n" \
  "v_pk_add_f32 v[24:25], v[110:111], v[112:113]\n" \
  "v_pk_add_f32 v[24:25], v[24:25], v[114:115]\n" \
  "v_add_f32 v24, v24, v25\n" \
  "v_mov_b32 v25, v24\n" \
  "s_nop 1\n" \
  "v_permlane16_swap_b32 v24, v25\n" \
  "v_add_f32 v24, v24, v25\n" \
  "v_mov_b32 v25, v24\n" \
  "s_nop 1\n" \
  "v_permlane32_swap_b32 v24, v25\n" \
  "v_add_f32 v24, v24, v25\n" \
  "v_rcp_f32 v24, v24\n" \
  "v_mov_b32 v54, 0\n" \
  "v_mov_b32 v55, 0\n" \
  "v_pk_mul_f32 v[110:111], v[110:111], v[24:25] op_sel_hi:[1,0]\n" \
  "v_pk_mul_f32 v[112:113], v[112:113], v[24:25] op_sel_hi:[1,0]\n" \
  "v_pk_mul_f32 v[114:115], v[114:115], v[24:25] op_sel_hi:[1,0]\n" \
  "v_cvt_pk_bf16_f32 v52, v112, v113\n" \
  "v_cvt_pk_bf16_f32 v53, v114, v115\n" \
  "v_cvt_pk_bf16_f32 v56, v110, v111\n" \
  "s_sub_u32 s74, s68, 1\n" \
  "v_readlane_b32 s42, v15, s74\n" \
  "v_xor_b32 v20, 0x8000, v20\n" \
  "v_xor_b32 v21, 0x8000, v21\n" \
  "v_xor_b32 v18, 0x8000, v18\n" \
  "s_cmp_eq_u32 s68, 64\n" \
  "s_cbranch_scc1 LENDs1_%=\n" \
  "v_readlane_b32 s72, v11, s68\n" \
  "s_nop 0\n" \
  "s_add_u32 s69, s72, 3072\n" \
  "s_add_u32 s70, s72, 7168\n" \
  "s_add_u32 s44, s72, s93\n" \
  "s_add_u32 s45, s72, s94\n" \
  "s_add_u32 s46, s72, s95\n" \
  "v_readlane_b32 s41, v7, s68\n" \
  "s_nop 1\n" \
  "s_branch LOOPs1_%=\n" \
  "LAR1_%=:\n" \
  "ds_read_b64_tr_b16 v[64:65], v9 offset:0\n" \
  "ds_read_b64_tr_b16 v[66:67], v9 offset:512\n" \
  "ds_read_b64_tr_b16 v[68:69], v9 offset:1024\n" \
  "ds_read_b64_tr_b16 v[70:71], v9 offset:1536\n" \
  "ds_read_b64_tr_b16 v[72:73], v9 offset:2048\n" \
  "ds_read_b64_tr_b16 v[74:75], v9 offset:2560\n" \
  "ds_read_b64_tr_b16 v[76:77], v9 offset:3072\n" \
  "ds_read_b64_tr_b16 v[78:79], v9 offset:3584\n" \
  "ds_read_b64_tr_b16 v[80:81], v9 offset:4096\n" \
  "ds_read_b64_tr_b16 v[82:83], v9 offset:4608\n" \
  "ds_read_b64_tr_b16 v[84:85], v9 offset:5120\n" \
  "ds_read_b64_tr_b16 v[86:87], v9 offset:5632\n" \
  "s_waitcnt lgkmcnt(10)\n" \
  "v_mfma_f32_16x16x32_bf16 v[108:111], v[64:67], v[24:27], 0\n" \
  "s_waitcnt lgkmcnt(8)\n" \
  "v_mfma_f32_16x16x32_bf16 v[108:111], v[68:71], v[28:31], v[108:111]\n" \
  "s_waitcnt lgkmcnt(6)\n" \
  "v_mfma_f32_16x16x32_bf16 v[108:111], v[72:75], v[32:35], v[108:111]\n" \
  "s_waitcnt lgkmcnt(4)\n" \
  "v_mfma_f32_16x16x32_bf16 v[108:111], v[76:79], v[36:39], v[108:111]\n" \
  "s_waitcnt lgkmcnt(2)\n" \
  "v_mfma_f32_16x16x32_bf16 v[108:111], v[80:83], v[40:43], v[108:111]\n" \
  "s_waitcnt lgkmcnt(0)\n" \
  "v_mfma_f32_16x16x32_bf16 v[108:111], v[84:87], v[44:47], v[108:111]\n" \
  "s_nop 7\n" \
  "s_branch LAK1_%=\n" \
  "LMFs1_%=:\n" \
  "ds_read_b128 v[88:91], v3 offset:8192\n" \
  "ds_read_b128 v[92:95], v3 offset:9216\n" \
  "ds_read_b128 v[96:99], v3 offset:10240\n" \
  "ds_read_b128 v[100:103], v3 offset:11264\n" \
  "ds_read_b128 v[104:107], v3 offset:12288\n" \
  "ds_read_b128 v[108:111], v3 offset:13312\n" \
  "ds_read_b128 v[112:115], v3 offset:14336\n" \
  "ds_read_b128 v[116:119], v3 offset:15360\n" \
  "v_mov_b32 v60, -1\n" \
  "v_mov_b32 v61, -1\n" \
  "ds_write_b64 v6, v[60:61] offset:33792\n" \
  "ds_write_b64 v6, v[60:61] offset:34304\n" \
  "ds_write_b64 v6, v[60:61] offset:37376\n" \
  "v_mov_b32 v64, 0\n" \
  "v_mov_b32 v65, 0\n" \
  "v_mov_b32 v66, 0\n" \
  "v_mov_b32 v67, 0\n" \
  "v_mov_b32 v68, 0\n" \
  "v_mov_b32 v69, 0\n" \
  "v_mov_b32 v70, 0\n" \
  "v_mov_b32 v71, 0\n" \
  "v_mov_b32 v72, 0\n" \
  "v_mov_b32 v73, 0\n" \
  "v_mov_b32 v74, 0\n" \
  "v_mov_b32 v75, 0\n" \
  "v_mov_b32 v76, 0\n" \
  "v_mov_b32 v77, 0\n" \
  "v_mov_b32 v78, 0\n" \
  "v_mov_b32 v79, 0\n" \
  "v_mov_b32 v80, 0\n" \
  "v_mov_b32 v81, 0\n" \
  "v_mov_b32 v82, 0\n" \
  "v_mov_b32 v83, 0\n" \
  "s_waitcnt lgkmcnt(0)\n" \
  "s_barrier\n" \
  "buffer_load_dwordx4 v[24:27], v1, s[36:39], s72 offen offset:0 sc1\n" \
  "s_mov_b32 m0, 66560\n" \
  "s_nop 0\n" \
  "buffer_load_dwordx4 v1, s[36:39], s69 offen sc1 lds\n" \
  "buffer_load_dwordx4 v[28:31], v1, s[36:39], s72 offen offset:1024 sc1\n" \
  "s_mov_b32 m0, 70656\n" \
  "s_nop 0\n" \
  "buffer_load_dwordx4 v1, s[36:39], s70 offen sc1 lds\n" \
  "ds_write_b64 v6, v[60:61] offset:1024\n" \
  "ds_write_b64 v6, v[60:61] offset:1536\n" \
  "ds_write_b64 v6, v[60:61] offset:4608\n" \
  "ds_write_b32 v12, v60 offset:0\n" \
  "s_branch LMs1_%=\n" \
  "LMQs1_%=:\n" \
  "s_waitcnt lgkmcnt(0)\n" \
  "s_barrier\n" \
  "s_barrier\n" \
  "ds_read_b32 v60, v12 offset:512\n" \
  "s_waitcnt lgkmcnt(0)\n" \
  "LMXs1_%=:\n" \
  "v_cmp_eq_u32 vcc, 2, v60\n" \
  "s_waitcnt lgkmcnt(0)\n" \
  "s_nop 2\n" \
  "s_cbranch_vccnz LMVs1_%=\n" \
  "s_waitcnt vmcnt(0)\n" \
  "LMVs1_%=:\n" \
  "s_barrier\n" \
  "s_cbranch_vccnz LMFs1_%=\n" \
  "s_branch LMYs1_%=\n" \
  "LENDs1_%=:\n" \
  "s_cmp_lg_u32 s38, 0\n" \
  "s_cbranch_scc1 LFINs1_%=\n" \
  "s_waitcnt vmcnt(0) lgkmcnt(0)\n" \
  "s_mov_b32 s38, s47\n" \
  "s_mov_b32 s54, s97\n" \
  "s_mov_b32 s96, s98\n" \
  "s_mov_b32 s68, 0\n" \
  "s_mov_b32 s73, 0\n" \
  "s_mov_b32 s56, 0\n" \
  "s_mov_b32 s57, 0\n" \
  "s_branch LPASSs1_%=\n" \
  "LFINs1_%=:\n" \
  "s_nop 1\n" \
  "buffer_store_dwordx4 v[52:55], v16, s[36:39], s42 offen sc1\n" \
  "buffer_store_dword v56, v17, s[36:39], s42 offen sc1\n" \
  "s_waitcnt vmcnt(0)\n" \
  "v_mov_b32 v19, 1\n" \
  "buffer_atomic_add v19, v14, s[36:39], 0 offen sc1\n" \
  "s_branch LDONE_%=\n" \
  "LSTARTs2_%=:\n" \
  "s_mov_b32 s79, 0\n" \
  "LSDs2_%=:\n" \
  "buffer_load_dwordx2 v[64:65], v19, s[36:39], 0 offen sc1\n" \
  "s_waitcnt vmcnt(0)\n" \
  "v_readlane_b32 s76, v64, 3\n" \
  "s_nop 0\n" \
  "s_add_u32 s76, s76, 1\n" \
  "v_cmp_ne_u32 vcc, s76, v64\n" \
  "s_nop 3\n" \
  "s_and_b32 s76, vcc_lo, 7\n" \
  "s_cbranch_scc0 LSFs2_%=\n" \
  "s_add_u32 s79, s79, 1\n" \
  "s_cmp_gt_u32 s79, 200000\n" \
  "s_cbranch_scc1 LSFs2_%=\n" \
  "s_sleep 4\n" \
  "s_branch LSDs2_%=\n" \
  "LSFs2_%=:\n" \
  "v_readlane_b32 s76, v65, 0\n" \
  "v_readlane_b32 s77, v65, 1\n" \
  "v_readlane_b32 s78, v65, 2\n" \
  "s_nop 1\n" \
  "v_lshrrev_b32 v64, 21, v7\n" \
  "v_lshrrev_b64 v[66:67], v64, s[76:77]\n" \
  "v_lshrrev_b32_e64 v68, v64, s78\n" \
  "v_cmp_gt_u32 vcc, 64, v64\n" \
  "s_nop 1\n" \
  "v_cndmask_b32 v66, v68, v66, vcc\n" \
  "v_and_b32 v66, 1, v66\n" \
  "v_lshlrev_b32 v66, 13, v66\n" \
  "v_xor_b32 v7, v7, v66\n" \
  "v_lshrrev_b32 v64, 21, v11\n" \
  "v_lshrrev_b64 v[66:67], v64, s[76:77]\n" \
  "v_lshrrev_b32_e64 v68, v64, s78\n" \
  "v_cmp_gt_u32 vcc, 64, v64\n" \
  "s_nop 1\n" \
  "v_cndmask_b32 v66, v68, v66, vcc\n" \
  "v_and_b32 v66, 1, v66\n" \
  "v_lshlrev_b32 v66, 13, v66\n" \
  "v_xor_b32 v11, v11, v66\n" \
  "v_lshrrev_b32 v64, 21, v28\n" \
  "v_lshrrev_b64 v[66:67], v64, s[76:77]\n" \
  "v_lshrrev_b32_e64 v68, v64, s78\n" \
  "v_cmp_gt_u32 vcc, 64, v64\n" \
  "s_nop 1\n" \
  "v_cndmask_b32 v66, v68, v66, vcc\n" \
  "v_and_b32 v66, 1, v66\n" \
  "v_lshlrev_b32 v66, 13, v66\n" \
  "v_xor_b32 v28, v28, v66\n" \
  "v_lshrrev_b32 v64, 21, v14\n" \
  "v_lshrrev_b64 v[66:67], v64, s[76:77]\n" \
  "v_lshrrev_b32_e64 v68, v64, s78\n" \
  "v_cmp_gt_u32 vcc, 64, v64\n" \
  "s_nop 1\n" \
  "v_cndmask_b32 v66, v68, v66, vcc\n" \
  "v_and_b32 v66, 1, v66\n" \
  "v_lshlrev_b32 v66, 13, v66\n" \
  "v_xor_b32 v14, v14, v66\n" \
  "v_lshrrev_b32 v64, 21, v15\n" \
  "v_lshrrev_b64 v[66:67], v64, s[76:77]\n" \
  "v_lshrrev_b32_e64 v68, v64, s78\n" \
  "v_cmp_gt_u32 vcc, 64, v64\n" \
  "s_nop 1\n" \
  "v_cndmask_b32 v66, v68, v66, vcc\n" \
  "v_and_b32 v66, 1, v66\n" \
  "v_lshlrev_b32 v66, 13, v66\n" \
  "v_xor_b32 v15, v15, v66\n" \
  "s_lshl_b32 s74, s76, 12\n" \
  "s_and_b32 s74, s74, 0x2000\n" \
  "s_or_b32 s55, s55, s74\n" \
  "s_mov_b32 s47, s38\n" \
  "s_mov_b32 s38, 0\n" \
  "s_mov_b32 s68, 63\n" \
  "s_mov_b32 s73, 20001\n" \
  "s_mov_b32 s56, 19999\n" \
  "s_mov_b32 s97, s54\n" \
  "s_mov_b32 s54, 0\n" \
  "s_mov_b32 s98, s96\n" \
  "s_mov_b32 s96, 0\n" \
  "LPASSs2_%=:\n" \
  "s_mov_b32 s40, 0\n" \
  "s_mov_b32 s42, 0x40000000\n" \
  "v_and_b32 v20, 0x7fff, v20\n" \
  "v_and_b32 v21, 0x7fff, v21\n" \
  "v_and_b32 v18, 0x7fff, v18\n" \
  "s_barrier\n" \
  "s_add_u32 m0, s40, 12288\n" \
  "s_add_u32 s75, s55, 20480\n" \
  "buffer_load_dwordx4 v8, s[36:39], s75 offen offset:0 sc1 lds\n" \
  "buffer_load_dwordx4 v8, s[36:39], s75 offen offset:1024 sc1 lds\n" \
  "buffer_load_dwordx4 v8, s[36:39], s75 offen offset:2048 sc1 lds\n" \
  "buffer_load_dwordx4 v8, s[36:39], s75 offen offset:3072 sc1 lds\n" \
  "s_add_u32 m0, s40, 16384\n" \
  "s_add_u32 s75, s55, 32768\n" \
  "buffer_load_dwordx4 v8, s[36:39], s75 offen offset:0 sc1 lds\n" \
  "buffer_load_dwordx4 v8, s[36:39], s75 offen offset:1024 sc1 lds\n" \
  "s_xor_b32 s40, s40, 0x8000\n" \
  "v_readlane_b32 s72, v11, 0\n" \
  "s_nop 0\n" \
  "s_add_u32 s69, s72, 4096\n" \
  "s_add_u32 s70, s72, 16384\n" \
  "s_add_u32 s44, s72, s93\n" \
  "s_add_u32 s45, s72, s94\n" \
  "s_add_u32 s46, s72, s95\n" \
  "v_readlane_b32 s41, v7, 0\n" \
  "s_nop 1\n" \
  "LOOPs2_%=:\n" \
  "v_mov_b32 v60, -1\n" \
  "v_mov_b32 v61, -1\n" \
  "v_mov_b32 v64, 0\n" \
  "v_mov_b32 v65, 0\n" \
  "v_mov_b32 v66, 0\n" \
  "v_mov_b32 v67, 0\n" \
  "v_mov_b32 v68, 0\n" \
  "v_mov_b32 v69, 0\n" \
  "v_mov_b32 v70, 0\n" \
  "v_mov_b32 v71, 0\n" \
  "v_mov_b32 v72, 0\n" \
  "v_mov_b32 v73, 0\n" \
  "v_mov_b32 v74, 0\n" \
  "v_mov_b32 v75, 0\n" \
  "v_mov_b32 v76, 0\n" \
  "v_mov_b32 v77, 0\n" \
  "v_mov_b32 v78, 0\n" \
  "v_mov_b32 v79, 0\n" \
  "v_mov_b32 v80, 0\n" \
  "v_mov_b32 v81, 0\n" \
  "v_mov_b32 v82, 0\n" \
  "v_mov_b32 v83, 0\n" \
  "s_barrier\n" \
  "buffer_load_dwordx4 v[24:27], v1, s[36:39], s72 offen offset:0 sc1\n" \
  "s_mov_b32 m0, 67584\n" \
  "s_nop 0\n" \
  "buffer_load_dwordx4 v1, s[36:39], s69 offen sc1 lds\n" \
  "buffer_load_dwordx4 v[28:31], v1, s[36:39], s72 offen offset:1024 sc1\n" \
  "s_mov_b32 m0, 71680\n" \
  "s_nop 0\n" \
  "buffer_load_dwordx4 v1, s[36:39], s70 offen sc1 lds\n" \
  "ds_read_b128 v[88:91], v3 offset:8192\n" \
  "ds_read_b128 v[92:95], v3 offset:9216\n" \
  "ds_read_b128 v[96:99], v3 offset:10240\n" \
  "ds_read_b128 v[100:103], v3 offset:11264\n" \
  "ds_read_b128 v[104:107], v3 offset:12288\n" \
  "ds_read_b128 v[108:111], v3 offset:13312\n" \
  "ds_read_b128 v[112:115], v3 offset:14336\n" \
  "ds_read_b128 v[116:119], v3 offset:15360\n" \
  "ds_write_b64 v6, v[60:61] offset:34816\n" \
  "ds_write_b64 v6, v[60:61] offset:35328\n" \
  "ds_write_b64 v6, v[60:61] offset:37888\n" \
  "ds_write_b64 v6, v[60:61] offset:2048\n" \
  "ds_write_b64 v6, v[60:61] offset:2560\n" \
  "ds_write_b64 v6, v[60:61] offset:5120\n" \
  "ds_write_b32 v12, v60 offset:256\n" \
  "s_add_u32 s68, s68, 1\n" \
  "LMs2_%=:\n" \
  "buffer_store_dwordx4 v[52:55], v16, s[36:39], s42 offen sc1\n" \
  "buffer_store_dword v56, v17, s[36:39], s42 offen sc1\n" \
  "s_mov_b32 s42, 0x40000000\n" \
  "s_waitcnt vmcnt(5)\n" \
  "s_waitcnt lgkmcnt(13)\n" \
  "v_smfmac_f32_16x16x64_bf16 v[64:67], v[24:27], v[88:95], v0\n" \
  "ds_read_b128 v[88:91], v3 offset:16384\n" \
  "s_waitcnt lgkmcnt(12)\n" \
  "v_smfmac_f32_16x16x64_bf16 v[68:71], v[24:27], v[96:103], v0\n" \
  "ds_read_b128 v[92:95], v3 offset:17408\n" \
  "v_smfmac_f32_16x16x64_bf16 v[72:75], v[24:27], a[0:7], v0\n" \
  "ds_read_b128 v[96:99], v3 offset:18432\n" \
  "v_smfmac_f32_16x16x64_bf16 v[76:79], v[24:27], a[8:15], v0\n" \
  "ds_read_b128 v[100:103], v3 offset:19456\n" \
  "v_smfmac_f32_16x16x64_bf16 v[80:83], v[24:27], a[16:23], v0\n" \
  "s_waitcnt vmcnt(4)\n" \
  "s_waitcnt lgkmcnt(4)\n" \
  "s_barrier\n" \
  "ds_read_b128 v[32:35], v2 offset:2048\n" \
  "s_waitcnt vmcnt(3)\n" \
  "v_smfmac_f32_16x16x64_bf16 v[64:67], v[28:31], a[24:31], v0\n" \
  "ds_read_b128 v[36:39], v2 offset:3072\n" \
  "v_smfmac_f32_16x16x64_bf16 v[68:71], v[28:31], a[32:39], v0\n" \
  "ds_read_b128 v[40:43], v2 offset:4096\n" \
  "v_smfmac_f32_16x16x64_bf16 v[72:75], v[28:31], a[40:47], v0\n" \
  "ds_read_b128 v[44:47], v2 offset:5120\n" \
  "v_smfmac_f32_16x16x64_bf16 v[76:79], v[28:31], a[48:55], v0\n" \
  "v_smfmac_f32_16x16x64_bf16 v[80:83], v[28:31], a[56:63], v0\n" \
  "s_waitcnt lgkmcnt(3)\n" \
  "v_smfmac_f32_16x16x64_bf16 v[64:67], v[32:35], a[64:71], v0\n" \
  "v_smfmac_f32_16x16x64_bf16 v[68:71], v[32:35], a[72:79], v0\n" \
  "v_smfmac_f32_16x16x64_bf16 v[72:75], v[32:35], a[80:87], v0\n" \
  "s_waitcnt lgkmcnt(2)\n" \
  "v_smfmac_f32_16x16x64_bf16 v[64:67], v[36:39], a[104:111], v0\n" \
  "v_smfmac_f32_16x16x64_bf16 v[68:71], v[36:39], v[104:111], v0\n" \
  "s_waitcnt vmcnt(2)\n" \
  "s_barrier\n" \
  "v_smfmac_f32_16x16x64_bf16 v[72:75], v[36:39], a[112:119], v0\n" \
  "ds_read_b128 v[48:51], v2 offset:6144\n" \
  "s_waitcnt lgkmcnt(2)\n" \
  "v_smfmac_f32_16x16x64_bf16 v[64:67], v[40:43], v[112:119], v0\n" \
  "ds_read_b128 v[52:55], v2 offset:7168\n" \
  "v_smfmac_f32_16x16x64_bf16 v[68:71], v[40:43], a[136:143], v0\n" \
  "ds_read_b128 v[56:59], v2 offset:8192\n" \
  "v_smfmac_f32_16x16x64_bf16 v[72:75], v[40:43], a[144:151], v0\n" \
  "ds_read_b128 v[60:63], v2 offset:9216\n" \
  "s_waitcnt lgkmcnt(4)\n" \
  "v_smfmac_f32_16x16x64_bf16 v[64:67], v[44:47], v[88:95], v0\n" \
  "ds_read_b128 v[104:107], v3 offset:20480\n" \
  "v_smfmac_f32_16x16x64_bf16 v[68:71], v[44:47], a[176:183], v0\n" \
  "ds_read_b128 v[108:111], v3 offset:21504\n" \
  "v_smfmac_f32_16x16x64_bf16 v[72:75], v[44:47], a[184:191], v0\n" \
  "ds_read_b128 v[88:91], v20 offset:0\n" \
  "s_waitcnt lgkmcnt(6)\n" \
  "v_smfmac_f32_16x16x64_bf16 v[64:67], v[48:51], a[216:223], v0\n" \
  "ds_read_b128 v[92:95], v20 offset:2048\n" \
  "s_waitcnt lgkmcnt(6)\n" \
  "v_smfmac_f32_16x16x64_bf16 v[64:67], v[52:55], v[128:135], v0\n" \
  "s_waitcnt lgkmcnt(5)\n" \
  "v_smfmac_f32_16x16x64_bf16 v[64:67], v[56:59], v[168:175], v0\n" \
  "s_waitcnt lgkmcnt(4)\n" \
  "v_smfmac_f32_16x16x64_bf16 v[64:67], v[60:63], v[96:103], v0\n" \
  "ds_read_b128 v[96:99], v20 offset:4096\n" \
  "v_smfmac_f32_16x16x64_bf16 v[68:71], v[48:51], a[224:231], v0\n" \
  "ds_read_b128 v[100:103], v20 offset:6144\n" \
  "s_waitcnt lgkmcnt(4)\n" \
  "v_smfmac_f32_16x16x64_bf16 v[68:71], v[52:55], v[104:111], v0\n" \
  "ds_read_b64 v[104:105], v21 offset:0\n" \
  "v_cvt_pk_bf16_f32 v64, v64, v65\n" \
  "v_smfmac_f32_16x16x64_bf16 v[68:71], v[56:59], v[176:183], v0\n" \
  "ds_read_b64 v[106:107], v21 offset:1024\n" \
  "v_cvt_pk_bf16_f32 v65, v66, v67\n" \
  "v_smfmac_f32_16x16x64_bf16 v[68:71], v[60:63], v[216:223], v0\n" \
  "ds_write_b64 v6, v[64:65] offset:34816\n" \
  "v_cmp_u_f32 vcc, v64, v65\n" \
  "v_smfmac_f32_16x16x64_bf16 v[72:75], v[48:51], a[232:239], v0\n" \
  "s_cbranch_vccnz LMQs2_%=\n" \
  "v_smfmac_f32_16x16x64_bf16 v[72:75], v[52:55], v[136:143], v0\n" \
  "ds_read_b64 v[108:109], v21 offset:2048\n" \
  "v_cvt_pk_bf16_f32 v68, v68, v69\n" \
  "v_smfmac_f32_16x16x64_bf16 v[72:75], v[56:59], v[184:191], v0\n" \
  "ds_read_b64 v[110:111], v21 offset:3072\n" \
  "v_cvt_pk_bf16_f32 v69, v70, v71\n" \
  "v_smfmac_f32_16x16x64_bf16 v[72:75], v[60:63], v[224:231], v0\n" \
  "ds_write_b64 v6, v[68:69] offset:35328\n" \
  "v_smfmac_f32_16x16x64_bf16 v[76:79], v[32:35], a[88:95], v0\n" \
  "ds_read_b64_tr_b16 v[24:25], v9 offset:32768\n" \
  "v_smfmac_f32_16x16x64_bf16 v[76:79], v[36:39], a[120:127], v0\n" \
  "ds_read_b64_tr_b16 v[28:29], v9 offset:33792\n" \
  "v_cvt_pk_bf16_f32 v72, v72, v73\n" \
  "v_smfmac_f32_16x16x64_bf16 v[76:79], v[40:43], a[152:159], v0\n" \
  "ds_read_b64_tr_b16 v[112:113], v9 offset:34816\n" \
  "v_cvt_pk_bf16_f32 v73, v74, v75\n" \
  "v_smfmac_f32_16x16x64_bf16 v[76:79], v[44:47], a[192:199], v0\n" \
  "ds_write_b64 v6, v[72:73] offset:37888\n" \
  "v_smfmac_f32_16x16x64_bf16 v[76:79], v[48:51], a[240:247], v0\n" \
  "ds_read_b64_tr_b16 v[64:65], v9 offset:35840\n" \
  "v_smfmac_f32_16x16x64_bf16 v[76:79], v[52:55], v[144:151], v0\n" \
  "s_waitcnt lgkmcnt(8)\n" \
  "ds_read_b64_tr_b16 v[26:27], v9 offset:33280\n" \
  "v_smfmac_f32_16x16x64_bf16 v[76:79], v[56:59], v[192:199], v0\n" \
  "ds_read_b64_tr_b16 v[30:31], v9 offset:34304\n" \
  "v_smfmac_f32_16x16x64_bf16 v[76:79], v[60:63], v[232:239], v0\n" \
  "ds_read_b64_tr_b16 v[114:115], v9 offset:35328\n" \
  "v_smfmac_f32_16x16x64_bf16 v[80:83], v[32:35], a[96:103], v0\n" \
  "ds_read_b64_tr_b16 v[66:67], v9 offset:36352\n" \
  "v_smfmac_f32_16x16x64_bf16 v[80:83], v[36:39], a[128:135], v0\n" \
  "ds_read_b64_tr_b16 v[68:69], v9 offset:36864\n" \
  "v_cvt_pk_bf16_f32 v76, v76, v77\n" \
  "v_smfmac_f32_16x16x64_bf16 v[80:83], v[40:43], a[160:167], v0\n" \
  "ds_read_b64_tr_b16 v[70:71], v9 offset:37376\n" \
  "v_cvt_pk_bf16_f32 v77, v78, v79\n" \
  "v_smfmac_f32_16x16x64_bf16 v[80:83], v[44:47], a[200:207], v0\n" \
  "ds_write_b64 v6, v[76:77] offset:2048\n" \
  "v_smfmac_f32_16x16x64_bf16 v[80:83], v[48:51], a[248:255], v0\n" \
  "s_waitcnt lgkmcnt(8)\n" \
  "ds_read_b64_tr_b16 v[72:73], v9 offset:37888\n" \
  "s_waitcnt lgkmcnt(7)\n" \
  "v_mfma_f32_16x16x32_bf16 v[116:119], v[88:91], v[24:27], 0\n" \
  "ds_read_b64_tr_b16 v[74:75], v9 offset:38400\n" \
  "s_waitcnt lgkmcnt(7)\n" \
  "v_mfma_f32_16x16x32_bf16 v[116:119], v[92:95], v[28:31], v[116:119]\n" \
  "s_waitcnt lgkmcnt(6)\n" \
  "v_mfma_f32_16x16x32_bf16 v[116:119], v[96:99], v[112:115], v[116:119]\n" \
  "ds_read_b128 v[84:87], v2 offset:1024\n" \
  "s_waitcnt lgkmcnt(6)\n" \
  "v_mfma_f32_16x16x32_bf16 v[116:119], v[100:103], v[64:67], v[116:119]\n" \
  "v_smfmac_f32_16x16x64_bf16 v[80:83], v[52:55], v[152:159], v0\n" \
  "v_smfmac_f32_16x16x64_bf16 v[80:83], v[56:59], v[200:207], v0\n" \
  "v_smfmac_f32_16x16x64_bf16 v[80:83], v[60:63], v[240:247], v0\n" \
  "s_waitcnt lgkmcnt(4)\n" \
  "v_mfma_f32_16x16x32_bf16 v[116:119], v[104:107], v[68:71], v[116:119]\n" \
  "s_waitcnt lgkmcnt(1)\n" \
  "v_mfma_f32_16x16x32_bf16 v[116:119], v[108:111], v[72:75], v[116:119]\n" \
  "s_waitcnt lgkmcnt(0)\n" \
  "v_cvt_pk_bf16_f32 v80, v80, v81\n" \
  "v_cvt_pk_bf16_f32 v81, v82, v83\n" \
  "v_smfmac_f32_16x16x64_bf16 v[84:87], v[40:43], a[168:175], v0\n" \
  "ds_write_b64 v6, v[80:81] offset:2560\n" \
  "v_smfmac_f32_16x16x64_bf16 v[84:87], v[44:47], a[208:215], v0\n" \
  "v_add_f32_dpp v88, v116, v116 row_ror:8 row_mask:0xf bank_mask:0x3\n" \
  "v_add_f32_dpp v89, v117, v117 row_ror:8 row_mask:0xf bank_mask:0x3\n" \
  "v_smfmac_f32_16x16x64_bf16 v[84:87], v[48:51], v[120:127], v0\n" \
  "v_add_f32_dpp v88, v118, v118 row_ror:8 row_mask:0xf bank_mask:0xc\n" \
  "v_add_f32_dpp v89, v119, v119 row_ror:8 row_mask:0xf bank_mask:0xc\n" \
  "v_smfmac_f32_16x16x64_bf16 v[84:87], v[52:55], v[160:167], v0\n" \
  "v_mul_f32 v88, v4, v88\n" \
  "v_mul_f32 v89, v4, v89\n" \
  "v_smfmac_f32_16x16x64_bf16 v[84:87], v[56:59], v[208:215], v0\n" \
  "v_exp_f32 v88, v88\n" \
  "v_smfmac_f32_16x16x64_bf16 v[84:87], v[60:63], v[248:255], v0\n" \
  "v_exp_f32 v89, v89\n" \
  "s_nop 0\n" \
  "v_add_f32 v90, v88, v89\n" \
  "ds_write_b32 v12, v90 offset:256\n" \
  "v_cmp_o_f32 vcc, v88, v89\n" \
  "v_cvt_pk_bf16_f32 v91, v88, v89\n" \
  "s_nop 1\n" \
  "v_cvt_pk_bf16_f32 v84, v84, v85\n" \
  "v_cvt_pk_bf16_f32 v85, v86, v87\n" \
  "ds_write_b64 v6, v[84:85] offset:5120\n" \
  "s_barrier ; timing only\n" \
  "s_mov_b64 exec, vcc\n" \
  "buffer_store_dword v91, v22, s[36:39], s72 offen offset:0 sc1\n" \
  "buffer_store_dword v91, v22, s[36:39], s44 offen offset:0 sc1\n" \
  "buffer_store_dword v91, v22, s[36:39], s45 offen offset:0 sc1\n" \
  "buffer_store_dword v91, v22, s[36:39], s46 offen offset:0 sc1\n" \
  "s_mov_b64 exec, -1\n" \
  "v_mov_b32 v61, 0xf149f2ca\n" \
  "v_cmp_gt_i32 vcc, 0, v13\n" \
  "s_nop 1\n" \
  "v_cndmask_b32 v116, 0, v61, vcc\n" \
  "v_cmp_gt_i32 vcc, 1, v13\n" \
  "s_nop 1\n" \
  "v_cndmask_b32 v117, 0, v61, vcc\n" \
  "v_cmp_gt_i32 vcc, 2, v13\n" \
  "s_nop 1\n" \
  "v_cndmask_b32 v118, 0, v61, vcc\n" \
  "v_cmp_gt_i32 vcc, 3, v13\n" \
  "s_nop 1\n" \
  "v_cndmask_b32 v119, 0, v61, vcc\n" \
  "ds_read_b128 v[24:27], v18 offset:9216\n" \
  "ds_read_b128 v[64:67], v18 offset:0\n" \
  "ds_read_b128 v[88:91], v18 offset:1024\n" \
  "ds_read_b64_tr_b16 v[48:49], v9 offset:0\n" \
  "ds_read_b64_tr_b16 v[50:51], v9 offset:512\n" \
  "ds_read_b128 v[28:31], v18 offset:11264\n" \
  "ds_read_b128 v[68:71], v18 offset:2048\n" \
  "ds_read_b128 v[92:95], v18 offset:3072\n" \
  "ds_read_b64_tr_b16 v[52:53], v9 offset:1024\n" \
  "ds_read_b64_tr_b16 v[54:55], v9 offset:1536\n" \
  "ds_read_b128 v[32:35], v18 offset:13312\n" \
  "ds_read_b128 v[72:75], v18 offset:4096\n" \
  "s_waitcnt lgkmcnt(8)\n" \
  "ds_read_b128 v[96:99], v18 offset:5120\n" \
  "ds_read_b64_tr_b16 v[56:57], v9 offset:2048\n" \
  "ds_read_b64_tr_b16 v[58:59], v9 offset:2560\n" \
  "v_mfma_f32_16x16x32_bf16 v[112:115], v[64:67], v[24:27], 0\n" \
  "v_mfma_f32_16x16x32_bf16 v[116:119], v[88:91], v[24:27], v[116:119]\n" \
  "s_waitcnt lgkmcnt(10)\n" \
  "v_mfma_f32_16x16x32_bf16 v[108:111], v[48:51], v[24:27], 0\n" \
  "ds_read_b128 v[36:39], v18 offset:15360\n" \
  "ds_read_b128 v[76:79], v18 offset:6144\n" \
  "ds_read_b128 v[100:103], v18 offset:7168\n" \
  "ds_read_b64_tr_b16 v[48:49], v9 offset:3072\n" \
  "ds_read_b64_tr_b16 v[50:51], v9 offset:3584\n" \
  "s_waitcnt lgkmcnt(13)\n" \
  "v_mfma_f32_16x16x32_bf16 v[112:115], v[68:71], v[28:31], v[112:115]\n" \
  "s_waitcnt lgkmcnt(12)\n" \
  "v_mfma_f32_16x16x32_bf16 v[116:119], v[92:95], v[28:31], v[116:119]\n" \
  "s_waitcnt lgkmcnt(10)\n" \
  "v_mfma_f32_16x16x32_bf16 v[108:111], v[52:55], v[28:31], v[108:111]\n" \
  "ds_read_b64 v[40:41], v18 offset:20488\n" \
  "ds_read_b64 v[42:43], v18 offset:21512\n" \
  "ds_read_b64 v[80:81], v18 offset:16384\n" \
  "ds_read_b64 v[82:83], v18 offset:17408\n" \
  "ds_read_b64 v[104:105], v18 offset:16392\n" \
  "s_waitcnt lgkmcnt(8)\n" \
  "ds_read_b64 v[106:107], v18 offset:17416\n" \
  "ds_read_b64_tr_b16 v[52:53], v9 offset:4096\n" \
  "ds_read_b64_tr_b16 v[54:55], v9 offset:4608\n" \
  "v_mfma_f32_16x16x32_bf16 v[112:115], v[72:75], v[32:35], v[112:115]\n" \
  "v_mfma_f32_16x16x32_bf16 v[116:119], v[96:99], v[32:35], v[116:119]\n" \
  "v_mfma_f32_16x16x32_bf16 v[108:111], v[56:59], v[32:35], v[108:111]\n" \
  "ds_read_b64 v[44:45], v18 offset:22536\n" \
  "ds_read_b64 v[46:47], v18 offset:23560\n" \
  "ds_read_b64 v[84:85], v18 offset:18432\n" \
  "ds_read_b64 v[86:87], v18 offset:19456\n" \
  "s_waitcnt lgkmcnt(8)\n" \
  "ds_read_b64 v[88:89], v18 offset:18440\n" \
  "ds_read_b64 v[90:91], v18 offset:19464\n" \
  "ds_read_b64_tr_b16 v[56:57], v9 offset:5120\n" \
  "ds_read_b64_tr_b16 v[58:59], v9 offset:5632\n" \
  "s_barrier\n" \
  "ds_read_b32 v60, v12 offset:512\n" \
  "v_mfma_f32_16x16x32_bf16 v[112:115], v[76:79], v[36:39], v[112:115]\n" \
  "v_mfma_f32_16x16x32_bf16 v[116:119], v[100:103], v[36:39], v[116:119]\n" \
  "v_mfma_f32_16x16x32_bf16 v[108:111], v[48:51], v[36:39], v[108:111]\n" \
  "v_mfma_f32_16x16x32_bf16 v[112:115], v[80:83], v[40:43], v[112:115]\n" \
  "s_waitcnt lgkmcnt(11)\n" \
  "v_mfma_f32_16x16x32_bf16 v[116:119], v[104:107], v[40:43], v[116:119]\n" \
  "s_waitcnt lgkmcnt(9)\n" \
  "v_mfma_f32_16x16x32_bf16 v[108:111], v[52:55], v[40:43], v[108:111]\n" \
  "s_waitcnt lgkmcnt(5)\n" \
  "v_mfma_f32_16x16x32_bf16 v[112:115], v[84:87], v[44:47], v[112:115]\n" \
  "s_waitcnt lgkmcnt(3)\n" \
  "v_mfma_f32_16x16x32_bf16 v[116:119], v[88:91], v[44:47], v[116:119]\n" \
  "s_waitcnt lgkmcnt(1)\n" \
  "v_mfma_f32_16x16x32_bf16 v[108:111], v[56:59], v[44:47], v[108:111]\n" \
  "s_waitcnt lgkmcnt(0)\n" \
  "v_cmp_ne_u32 vcc, 0, v60\n" \
  "s_nop 3\n" \
  "s_cbranch_vccnz LMXs2_%=\n" \
  "LMYs2_%=:\n" \
  "s_nop 0\n" \
  "v_cmp_u_f32 vcc, v108, v109\n" \
  "v_cmp_u_f32 s[82:83], v110, v111\n" \
  "s_nop 1\n" \
  "s_or_b64 vcc, vcc, s[82:83]\n" \
  "s_cbranch_scc1 LAR2_%=\n" \
  "LAK2_%=:\n" \
  "v_permlane32_swap_b32 v108, v110\n" \
  "v_permlane32_swap_b32 v109, v111\n" \
  "v_pk_add_f32 v[110:111], v[108:109], v[110:111]\n" \
  "v_mov_b32 v32, 0x3d93cd3a\n" \
  "v_mov_b32 v34, 0x3fb8aa3b\n" \
  "v_cvt_pk_bf16_f32 v24, v110, v111\n" \
  "v_cvt_pk_bf16_f32 v25, v112, v113\n" \
  "v_cvt_pk_bf16_f32 v26, v114, v115\n" \
  "v_cvt_pk_bf16_f32 v27, v116, v117\n" \
  "v_cvt_pk_bf16_f32 v28, v118, v119\n" \
  "v_lshlrev_b32 v110, 16, v24\n" \
  "v_and_b32 v111, 0xffff0000, v24\n" \
  "v_pk_mul_f32 v[110:111], v[110:111], v[32:33] op_sel_hi:[1,0]\n" \
  "v_cvt_pk_bf16_f32 v24, v110, v111\n" \
  "v_lshlrev_b32 v110, 16, v24\n" \
  "v_and_b32 v111, 0xffff0000, v24\n" \
  "v_lshlrev_b32 v112, 16, v25\n" \
  "v_and_b32 v113, 0xffff0000, v25\n" \
  "v_pk_mul_f32 v[112:113], v[112:113], v[32:33] op_sel_hi:[1,0]\n" \
  "v_cvt_pk_bf16_f32 v25, v112, v113\n" \
  "v_lshlrev_b32 v112, 16, v25\n" \
  "v_and_b32 v113, 0xffff0000, v25\n" \
  "v_lshlrev_b32 v114, 16, v26\n" \
  "v_and_b32 v115, 0xffff0000, v26\n" \
  "v_pk_mul_f32 v[114:115], v[114:115], v[32:33] op_sel_hi:[1,0]\n" \
  "v_cvt_pk_bf16_f32 v26, v114, v115\n" \
  "v_lshlrev_b32 v114, 16, v26\n" \
  "v_and_b32 v115, 0xffff0000, v26\n" \
  "v_lshlrev_b32 v116, 16, v27\n" \
  "v_and_b32 v117, 0xffff0000, v27\n" \
  "v_pk_mul_f32 v[116:117], v[116:117], v[32:33] op_sel_hi:[1,0]\n" \
  "v_cvt_pk_bf16_f32 v27, v116, v117\n" \
  "v_lshlrev_b32 v116, 16, v27\n" \
  "v_and_b32 v117, 0xffff0000, v27\n" \
  "v_lshlrev_b32 v118, 16, v28\n" \
  "v_and_b32 v119, 0xffff0000, v28\n" \
  "v_pk_mul_f32 v[118:119], v[118:119], v[32:33] op_sel_hi:[1,0]\n" \
  "v_cvt_pk_bf16_f32 v28, v118, v119\n" \
  "v_lshlrev_b32 v118, 16, v28\n" \
  "v_and_b32 v119, 0xffff0000, v28\n" \
  "v_max_f32 v24, v110, v111\n" \
  "v_max3_f32 v24, v24, v112, v113\n" \
  "v_max3_f32 v24, v24, v114, v115\n" \
  "v_max3_f32 v24, v24, v116, v117\n" \
  "v_max3_f32 v24, v24, v118, v119\n" \
  "v_mov_b32 v25, v24\n" \
  "s_nop 1\n" \
  "v_permlane16_swap_b32 v24, v25\n" \
  "v_max_f32 v24, v24, v25\n" \
  "v_mov_b32 v25, v24\n" \
  "s_nop 1\n" \
  "v_permlane32_swap_b32 v24, v25\n" \
  "v_max_f32 v24, v24, v25\n" \
  "v_mul_f32 v24, v24, v34\n" \
  "v_pk_fma_f32 v[110:111], v[110:111], v[34:35], v[24:25] op_sel_hi:[1,0,0] neg_lo:[0,0,1] neg_hi:[0,0,1]\n" \
  "v_pk_fma_f32 v[112:113], v[112:113], v[34:35], v[24:25] op_sel_hi:[1,0,0] neg_lo:[0,0,1] neg_hi:[0,0,1]\n" \
  "v_pk_fma_f32 v[114:115], v[114:115], v[34:35], v[24:25] op_sel_hi:[1,0,0] neg_lo:[0,0,1] neg_hi:[0,0,1]\n" \
  "v_pk_fma_f32 v[116:117], v[116:117], v[34:35], v[24:25] op_sel_hi:[1,0,0] neg_lo:[0,0,1] neg_hi:[0,0,1]\n" \
  "v_pk_fma_f32 v[118:119], v[118:119], v[34:35], v[24:25] op_sel_hi:[1,0,0] neg_lo:[0,0,1] neg_hi:[0,0,1]\n" \
  "v_exp_f32 v110, v110\n" \
  "v_exp_f32 v111, v111\n" \
  "v_exp_f32 v112, v112\n" \
  "v_exp_f32 v113, v113\n" \
  "v_exp_f32 v114, v114\n" \
  "v_exp_f32 v115, v115\n" \
  "v_exp_f32 v116, v116\n" \
  "v_exp_f32 v117, v117\n" \
  "v_exp_f32 v118, v118\n" \
  "v_exp_f32 v119, v119\n" \
  "v_pk_add_f32 v[24:25], v[110:111], v[112:113]\n" \
  "v_pk_add_f32 v[24:25], v[24:25], v[114:115]\n" \
  "v_pk_add_f32 v[24:25], v[24:25], v[116:117]\n" \
  "v_pk_add_f32 v[24:25], v[24:25], v[118:119]\n" \
  "v_add_f32 v24, v24, v25\n" \
  "v_mov_b32 v25, v24\n" \
  "s_nop 1\n" \
  "v_permlane16_swap_b32 v24, v25\n" \
  "v_add_f32 v24, v24, v25\n" \
  "v_mov_b32 v25, v24\n" \
  "s_nop 1\n" \
  "v_permlane32_swap_b32 v24, v25\n" \
  "v_add_f32 v24, v24, v25\n" \
  "v_rcp_f32 v24, v24\n" \
  "s_nop 0\n" \
  "v_pk_mul_f32 v[110:111], v[110:111], v[24:25] op_sel_hi:[1,0]\n" \
  "v_pk_mul_f32 v[112:113], v[112:113], v[24:25] op_sel_hi:[1,0]\n" \
  "v_pk_mul_f32 v[114:115], v[114:115], v[24:25] op_sel_hi:[1,0]\n" \
  "v_pk_mul_f32 v[116:117], v[116:117], v[24:25] op_sel_hi:[1,0]\n" \
  "v_pk_mul_f32 v[118:119], v[118:119], v[24:25] op_sel_hi:[1,0]\n" \
  "v_cvt_pk_bf16_f32 v52, v112, v113\n" \
  "v_cvt_pk_bf16_f32 v53, v114, v115\n" \
  "v_cvt_pk_bf16_f32 v54, v116, v117\n" \
  "v_cvt_pk_bf16_f32 v55, v118, v119\n" \
  "v_cvt_pk_bf16_f32 v56, v110, v111\n" \
  "s_sub_u32 s74, s68, 1\n" \
  "v_readlane_b32 s42, v15, s74\n" \
  "v_xor_b32 v20, 0x8000, v20\n" \
  "v_xor_b32 v21, 0x8000, v21\n" \
  "v_xor_b32 v18, 0x8000, v18\n" \
  "s_cmp_eq_u32 s68, 64\n" \
  "s_cbranch_scc1 LENDs2_%=\n" \
  "v_readlane_b32 s72, v11, s68\n" \
  "s_nop 0\n" \
  "s_add_u32 s69, s72, 4096\n" \
  "s_add_u32 s70, s72, 16384\n" \
  "s_add_u32 s44, s72, s93\n" \
  "s_add_u32 s45, s72, s94\n" \
  "s_add_u32 s46, s72, s95\n" \
  "v_readlane_b32 s41, v7, s68\n" \
  "s_nop 1\n" \
  "s_branch LOOPs2_%=\n" \
  "LAR2_%=:\n" \
  "ds_read_b64_tr_b16 v[64:65], v9 offset:0\n" \
  "ds_read_b64_tr_b16 v[66:67], v9 offset:512\n" \
  "ds_read_b64_tr_b16 v[68:69], v9 offset:1024\n" \
  "ds_read_b64_tr_b16 v[70:71], v9 offset:1536\n" \
  "ds_read_b64_tr_b16 v[72:73], v9 offset:2048\n" \
  "ds_read_b64_tr_b16 v[74:75], v9 offset:2560\n" \
  "ds_read_b64_tr_b16 v[76:77], v9 offset:3072\n" \
  "ds_read_b64_tr_b16 v[78:79], v9 offset:3584\n" \
  "ds_read_b64_tr_b16 v[80:81], v9 offset:4096\n" \
  "ds_read_b64_tr_b16 v[82:83], v9 offset:4608\n" \
  "ds_read_b64_tr_b16 v[84:85], v9 offset:5120\n" \
  "ds_read_b64_tr_b16 v[86:87], v9 offset:5632\n" \
  "s_waitcnt lgkmcnt(10)\n" \
  "v_mfma_f32_16x16x32_bf16 v[108:111], v[64:67], v[24:27], 0\n" \
  "s_waitcnt lgkmcnt(8)\n" \
  "v_mfma_f32_16x16x32_bf16 v[108:111], v[68:71], v[28:31], v[108:111]\n" \
  "s_waitcnt lgkmcnt(6)\n" \
  "v_mfma_f32_16x16x32_bf16 v[108:111], v[72:75], v[32:35], v[108:111]\n" \
  "s_waitcnt lgkmcnt(4)\n" \
  "v_mfma_f32_16x16x32_bf16 v[108:111], v[76:79], v[36:39], v[108:111]\n" \
  "s_waitcnt lgkmcnt(2)\n" \
  "v_mfma_f32_16x16x32_bf16 v[108:111], v[80:83], v[40:43], v[108:111]\n" \
  "s_waitcnt lgkmcnt(0)\n" \
  "v_mfma_f32_16x16x32_bf16 v[108:111], v[84:87], v[44:47], v[108:111]\n" \
  "s_nop 7\n" \
  "s_branch LAK2_%=\n" \
  "LMFs2_%=:\n" \
  "ds_read_b128 v[88:91], v3 offset:8192\n" \
  "ds_read_b128 v[92:95], v3 offset:9216\n" \
  "ds_read_b128 v[96:99], v3 offset:10240\n" \
  "ds_read_b128 v[100:103], v3 offset:11264\n" \
  "ds_read_b128 v[104:107], v3 offset:12288\n" \
  "ds_read_b128 v[108:111], v3 offset:13312\n" \
  "ds_read_b128 v[112:115], v3 offset:14336\n" \
  "ds_read_b128 v[116:119], v3 offset:15360\n" \
  "v_mov_b32 v60, -1\n" \
  "v_mov_b32 v61, -1\n" \
  "ds_write_b64 v6, v[60:61] offset:34816\n" \
  "ds_write_b64 v6, v[60:61] offset:35328\n" \
  "ds_write_b64 v6, v[60:61] offset:37888\n" \
  "v_mov_b32 v64, 0\n" \
  "v_mov_b32 v65, 0\n" \
  "v_mov_b32 v66, 0\n" \
  "v_mov_b32 v67, 0\n" \
  "v_mov_b32 v68, 0\n" \
  "v_mov_b32 v69, 0\n" \
  "v_mov_b32 v70, 0\n" \
  "v_mov_b32 v71, 0\n" \
  "v_mov_b32 v72, 0\n" \
  "v_mov_b32 v73, 0\n" \
  "v_mov_b32 v74, 0\n" \
  "v_mov_b32 v75, 0\n" \
  "v_mov_b32 v76, 0\n" \
  "v_mov_b32 v77, 0\n" \
  "v_mov_b32 v78, 0\n" \
  "v_mov_b32 v79, 0\n" \
  "v_mov_b32 v80, 0\n" \
  "v_mov_b32 v81, 0\n" \
  "v_mov_b32 v82, 0\n" \
  "v_mov_b32 v83, 0\n" \
  "s_waitcnt lgkmcnt(0)\n" \
  "s_barrier\n" \
  "buffer_load_dwordx4 v[24:27], v1, s[36:39], s72 offen offset:0 sc1\n" \
  "s_mov_b32 m0, 67584\n" \
  "s_nop 0\n" \
  "buffer_load_dwordx4 v1, s[36:39], s69 offen sc1 lds\n" \
  "buffer_load_dwordx4 v[28:31], v1, s[36:39], s72 offen offset:1024 sc1\n" \
  "s_mov_b32 m0, 71680\n" \
  "s_nop 0\n" \
  "buffer_load_dwordx4 v1, s[36:39], s70 offen sc1 lds\n" \
  "ds_write_b64 v6, v[60:61] offset:2048\n" \
  "ds_write_b64 v6, v[60:61] offset:2560\n" \
  "ds_write_b64 v6, v[60:61] offset:5120\n" \
  "ds_write_b32 v12, v60 offset:256\n" \
  "s_branch LMs2_%=\n" \
  "LMQs2_%=:\n" \
  "s_waitcnt lgkmcnt(0)\n" \
  "s_barrier\n" \
  "s_barrier\n" \
  "ds_read_b32 v60, v12 offset:512\n" \
  "s_waitcnt lgkmcnt(0)\n" \
  "LMXs2_%=:\n" \
  "v_cmp_eq_u32 vcc, 2, v60\n" \
  "s_waitcnt lgkmcnt(0)\n" \
  "s_nop 2\n" \
  "s_cbranch_vccnz LMVs2_%=\n" \
  "s_waitcnt vmcnt(0)\n" \
  "LMVs2_%=:\n" \
  "s_barrier\n" \
  "s_cbranch_vccnz LMFs2_%=\n" \
  "s_branch LMYs2_%=\n" \
  "LENDs2_%=:\n" \
  "s_cmp_lg_u32 s38, 0\n" \
  "s_cbranch_scc1 LFINs2_%=\n" \
  "s_waitcnt vmcnt(0) lgkmcnt(0)\n" \
  "s_mov_b32 s38, s47\n" \
  "s_mov_b32 s54, s97\n" \
  "s_mov_b32 s96, s98\n" \
  "s_mov_b32 s68, 0\n" \
  "s_mov_b32 s73, 0\n" \
  "s_mov_b32 s56, 0\n" \
  "s_mov_b32 s57, 0\n" \
  "s_branch LPASSs2_%=\n" \
  "LFINs2_%=:\n" \
  "s_nop 1\n" \
  "buffer_store_dwordx4 v[52:55], v16, s[36:39], s42 offen sc1\n" \
  "buffer_store_dword v56, v17, s[36:39], s42 offen sc1\n" \
  "s_waitcnt vmcnt(0)\n" \
  "v_mov_b32 v19, 1\n" \
  "buffer_atomic_add v19, v14, s[36:39], 0 offen sc1\n" \
  "s_branch LDONE_%=\n" \
  "LSTARTs3_%=:\n" \
  "s_mov_b32 s79, 0\n" \
  "LSDs3_%=:\n" \
  "buffer_load_dwordx2 v[64:65], v19, s[36:39], 0 offen sc1\n" \
  "s_waitcnt vmcnt(0)\n" \
  "v_readlane_b32 s76, v64, 3\n" \
  "s_nop 0\n" \
  "s_add_u32 s76, s76, 1\n" \
  "v_cmp_ne_u32 vcc, s76, v64\n" \
  "s_nop 3\n" \
  "s_and_b32 s76, vcc_lo, 7\n" \
  "s_cbranch_scc0 LSFs3_%=\n" \
  "s_add_u32 s79, s79, 1\n" \
  "s_cmp_gt_u32 s79, 200000\n" \
  "s_cbranch_scc1 LSFs3_%=\n" \
  "s_sleep 4\n" \
  "s_branch LSDs3_%=\n" \
  "LSFs3_%=:\n" \
  "v_readlane_b32 s76, v65, 0\n" \
  "v_readlane_b32 s77, v65, 1\n" \
  "v_readlane_b32 s78, v65, 2\n" \
  "s_nop 1\n" \
  "v_lshrrev_b32 v64, 21, v7\n" \
  "v_lshrrev_b64 v[66:67], v64, s[76:77]\n" \
  "v_lshrrev_b32_e64 v68, v64, s78\n" \
  "v_cmp_gt_u32 vcc, 64, v64\n" \
  "s_nop 1\n" \
  "v_cndmask_b32 v66, v68, v66, vcc\n" \
  "v_and_b32 v66, 1, v66\n" \
  "v_lshlrev_b32 v66, 13, v66\n" \
  "v_xor_b32 v7, v7, v66\n" \
  "v_lshrrev_b32 v64, 21, v11\n" \
  "v_lshrrev_b64 v[66:67], v64, s[76:77]\n" \
  "v_lshrrev_b32_e64 v68, v64, s78\n" \
  "v_cmp_gt_u32 vcc, 64, v64\n" \
  "s_nop 1\n" \
  "v_cndmask_b32 v66, v68, v66, vcc\n" \
  "v_and_b32 v66, 1, v66\n" \
  "v_lshlrev_b32 v66, 13, v66\n" \
  "v_xor_b32 v11, v11, v66\n" \
  "v_lshrrev_b32 v64, 21, v28\n" \
  "v_lshrrev_b64 v[66:67], v64, s[76:77]\n" \
  "v_lshrrev_b32_e64 v68, v64, s78\n" \
  "v_cmp_gt_u32 vcc, 64, v64\n" \
  "s_nop 1\n" \
  "v_cndmask_b32 v66, v68, v66, vcc\n" \
  "v_and_b32 v66, 1, v66\n" \
  "v_lshlrev_b32 v66, 13, v66\n" \
  "v_xor_b32 v28, v28, v66\n" \
  "v_lshrrev_b32 v64, 21, v16\n" \
  "v_lshrrev_b64 v[66:67], v64, s[76:77]\n" \
  "v_lshrrev_b32_e64 v68, v64, s78\n" \
  "v_cmp_gt_u32 vcc, 64, v64\n" \
  "s_nop 1\n" \
  "v_cndmask_b32 v66, v68, v66, vcc\n" \
  "v_and_b32 v66, 1, v66\n" \
  "v_lshlrev_b32 v66, 13, v66\n" \
  "v_xor_b32 v16, v16, v66\n" \
  "v_lshrrev_b32 v64, 21, v29\n" \
  "v_lshrrev_b64 v[66:67], v64, s[76:77]\n" \
  "v_lshrrev_b32_e64 v68, v64, s78\n" \
  "v_cmp_gt_u32 vcc, 64, v64\n" \
  "s_nop 1\n" \
  "v_cndmask_b32 v66, v68, v66, vcc\n" \
  "v_and_b32 v66, 1, v66\n" \
  "v_lshlrev_b32 v66, 13, v66\n" \
  "v_xor_b32 v29, v29, v66\n" \
  "s_lshl_b32 s74, s76, 12\n" \
  "s_and_b32 s74, s74, 0x2000\n" \
  "s_or_b32 s55, s55, s74\n" \
  "v_mov_b32 v17, v29\n" \
  "s_mov_b32 s47, s38\n" \
  "s_mov_b32 s38, 0\n" \
  "s_mov_b32 s68, 63\n" \
  "s_mov_b32 s73, 20001\n" \
  "s_mov_b32 s56, 19999\n" \
  "s_mov_b32 s97, s54\n" \
  "s_mov_b32 s54, 0\n" \
  "s_mov_b32 s98, s96\n" \
  "s_mov_b32 s96, 0\n" \
  "s_getpc_b64 s[82:83]\n" \
  "LPCAs3_%=:\n" \
  "s_add_u32 s84, s82, LEAs3_%=-LPCAs3_%=+24\n" \
  "s_addc_u32 s85, s83, 0\n" \
  "LPASSs3_%=:\n" \
  "s_mov_b32 s40, 0\n" \
  "s_mov_b32 s42, 0x40000000\n" \
  "s_barrier\n" \
  "s_add_u32 m0, s40, 16384\n" \
  "s_add_u32 s75, s55, 32768\n" \
  "buffer_load_dwordx4 v8, s[36:39], s75 offen offset:2048 sc1 lds\n" \
  "buffer_load_dwordx4 v8, s[36:39], s75 offen offset:3072 sc1 lds\n" \
  "s_add_u32 m0, s40, 20480\n" \
  "s_add_u32 s75, s55, 36864\n" \
  "buffer_load_dwordx4 v8, s[36:39], s75 offen offset:0 sc1 lds\n" \
  "buffer_load_dwordx4 v8, s[36:39], s75 offen offset:1024 sc1 lds\n" \
  "buffer_load_dwordx4 v8, s[36:39], s75 offen offset:2048 sc1 lds\n" \
  "buffer_load_dwordx4 v8, s[36:39], s75 offen offset:3072 sc1 lds\n" \
  "s_xor_b32 s40, s40, 0x8000\n" \
  "s_memrealtime s[76:77]\n" \
  "s_memtime s[78:79]\n" \
  "s_waitcnt lgkmcnt(0)\n" \
  "s_lshl_b32 s75, s76, 2\n" \
  "s_sub_i32 s75, s48, s75\n" \
  "s_mul_i32 s75, s75, s50\n" \
  "s_ashr_i32 s75, s75, 10\n" \
  "s_sub_i32 s74, s49, s78\n" \
  "s_min_i32 s74, s74, s75\n" \
  "s_sub_i32 s75, s75, 92\n" \
  "s_max_i32 s74, s74, s75\n" \
  "s_add_u32 s49, s74, s78\n" \
  "s_sub_u32 s74, s78, s89\n" \
  "s_sub_u32 s75, s76, s88\n" \
  "v_cvt_f32_u32 v24, s74\n" \
  "v_cvt_f32_u32 v25, s75\n" \
  "v_rcp_f32 v25, v25\n" \
  "v_mul_f32 v24, 0x43820000, v24\n" \
  "v_mul_f32 v24, v24, v25\n" \
  "v_cvt_u32_f32 v24, v24\n" \
  "s_nop 0\n" \
  "v_readfirstlane_b32 s74, v24\n" \
  "s_min_u32 s74, s74, 6400\n" \
  "s_cmp_gt_u32 s75, 150\n" \
  "s_cselect_b32 s50, s74, s50\n" \
  "s_cselect_b32 s88, s76, s88\n" \
  "s_cselect_b32 s89, s78, s89\n" \
  "v_mov_b32 v24, 0\n" \
  "v_mov_b32 v25, 0\n" \
  "v_mov_b32 v26, 0\n" \
  "v_mov_b32 v27, s50\n" \
  "buffer_store_dwordx4 v[24:27], v16, s[36:39], 0 offen sc1\n" \
  "LQAs3_%=:\n" \
  "buffer_load_dword v64, v17, s[36:39], 0 offen sc1\n" \
  "s_waitcnt vmcnt(0)\n" \
  "v_cmp_eq_u32 vcc, 0, v64\n" \
  "s_nop 3\n" \
  "s_cbranch_vccz LRAs3_%=\n" \
  "s_add_u32 s73, s73, 1\n" \
  "s_cmp_gt_u32 s73, 20000\n" \
  "s_cbranch_scc1 LRAs3_%=\n" \
  "s_sleep 2\n" \
  "s_branch LQAs3_%=\n" \
  "LRAs3_%=:\n" \
  "s_memrealtime s[76:77]\n" \
  "s_memtime s[78:79]\n" \
  "s_waitcnt lgkmcnt(0)\n" \
  "s_lshl_b32 s48, s76, 2\n" \
  "v_readlane_b32 s90, v64, 0\n" \
  "s_nop 1\n" \
  "s_sub_u32 s74, s90, s48\n" \
  "s_add_u32 s74, s74, 40000\n" \
  "s_cmp_gt_u32 s74, 80000\n" \
  "s_cselect_b32 s90, s48, s90\n" \
  "s_sub_u32 s74, s78, s89\n" \
  "s_sub_u32 s75, s76, s88\n" \
  "v_cvt_f32_u32 v24, s74\n" \
  "v_cvt_f32_u32 v25, s75\n" \
  "v_rcp_f32 v25, v25\n" \
  "v_mul_f32 v24, 0x43820000, v24\n" \
  "v_mul_f32 v24, v24, v25\n" \
  "v_cvt_u32_f32 v24, v24\n" \
  "s_nop 0\n" \
  "v_readfirstlane_b32 s74, v24\n" \
  "s_min_u32 s74, s74, 6400\n" \
  "s_cmp_gt_u32 s75, 150\n" \
  "s_cselect_b32 s50, s74, s50\n" \
  "s_cselect_b32 s88, s76, s88\n" \
  "s_cselect_b32 s89, s78, s89\n" \
  "s_add_u32 s49, s78, 0x1000000\n" \
  "s_mul_i32 s86, s54, 6\n" \
  "s_mov_b32 s87, -200\n" \
  "s_mov_b32 s51, 0\n" \
  "s_mov_b32 s71, 0\n" \
  "s_mul_i32 s74, s51, s54\n" \
  "s_add_u32 s74, s74, s90\n" \
  "s_sub_i32 s75, s74, s48\n" \
  "s_mov_b32 s48, s74\n" \
  "s_mul_i32 s75, s75, s50\n" \
  "s_ashr_i32 s75, s75, 10\n" \
  "s_add_u32 s49, s49, s75\n" \
  "s_cmp_eq_u32 s38, 0\n" \
  "s_cselect_b32 s87, 0x80000000, s87\n" \
  "v_readlane_b32 s72, v11, 0\n" \
  "s_nop 0\n" \
  "s_add_u32 s69, s72, 5120\n" \
  "s_add_u32 s70, s72, 17408\n" \
  "s_add_u32 s44, s72, s93\n" \
  "s_add_u32 s45, s72, s94\n" \
  "s_add_u32 s46, s72, s95\n" \
  "v_readlane_b32 s41, v7, 0\n" \
  "s_nop 1\n" \
  "ds_read_b128 v[88:91], v3 offset:8192\n" \
  "ds_read_b128 v[92:95], v3 offset:9216\n" \
  "ds_read_b128 v[96:99], v3 offset:10240\n" \
  "ds_read_b128 v[100:103], v3 offset:11264\n" \
  "ds_read_b128 v[104:107], v3 offset:0\n" \
  "ds_read_b128 v[108:111], v3 offset:1024\n" \
  "ds_read_b128 v[112:115], v3 offset:22528\n" \
  "ds_read_b128 v[116:119], v3 offset:23552\n" \
  "v_mov_b32 v60, -1\n" \
  "v_mov_b32 v61, -1\n" \
  "ds_write_b64 v6, v[60:61] offset:35840\n" \
  "ds_write_b64 v6, v[60:61] offset:36352\n" \
  "ds_write_b64 v6, v[60:61] offset:38400\n" \
  "ds_write_b64 v2, v[60:61] offset:1024\n" \
  "v_mov_b32 v20, 0\n" \
  "v_mov_b32 v21, 0\n" \
  "v_mov_b32 v22, 0\n" \
  "v_mov_b32 v23, 0\n" \
  "v_mov_b32 v64, 0\n" \
  "v_mov_b32 v65, 0\n" \
  "v_mov_b32 v66, 0\n" \
  "v_mov_b32 v67, 0\n" \
  "v_mov_b32 v68, 0\n" \
  "v_mov_b32 v69, 0\n" \
  "v_mov_b32 v70, 0\n" \
  "v_mov_b32 v71, 0\n" \
  "v_mov_b32 v72, 0\n" \
  "v_mov_b32 v73, 0\n" \
  "v_mov_b32 v74, 0\n" \
  "v_mov_b32 v75, 0\n" \
  "v_mov_b32 v76, 0\n" \
  "v_mov_b32 v77, 0\n" \
  "v_mov_b32 v78, 0\n" \
  "v_mov_b32 v79, 0\n" \
  "v_mov_b32 v80, 0\n" \
  "v_mov_b32 v81, 0\n" \
  "v_mov_b32 v82, 0\n" \
  "v_mov_b32 v83, 0\n" \
  "v_mov_b32 v84, 0\n" \
  "v_mov_b32 v85, 0\n" \
  "v_mov_b32 v86, 0\n" \
  "v_mov_b32 v87, 0\n" \
  "s_waitcnt lgkmcnt(0)\n" \
  "s_cmp_eq_u32 s38, 0\n" \
  "s_cselect_b32 s74, -1, 0\n" \
  "v_mov_b32 v64, s74\n" \
  "LOOPs3_%=:\n" \
  "LWAs3_%=:\n" \
  "s_memrealtime s[76:77]\n" \
  "s_memtime s[78:79]\n" \
  "s_waitcnt lgkmcnt(0)\n" \
  "s_lshl_b32 s75, s76, 2\n" \
  "s_sub_i32 s75, s48, s75\n" \
  "s_mul_i32 s75, s75, s50\n" \
  "s_ashr_i32 s75, s75, 10\n" \
  "s_sub_i32 s74, s49, s78\n" \
  "s_min_i32 s74, s74, s75\n" \
  "s_sub_i32 s75, s75, 92\n" \
  "s_max_i32 s74, s74, s75\n" \
  "s_add_u32 s49, s74, s78\n" \
  "s_cmp_gt_i32 s74, 168\n" \
  "s_cbranch_scc1 LWAs3_%=\n" \
  "s_cmp_lt_i32 s74, s87\n" \
  "s_cbranch_scc1 LSAs3_%=\n" \
  "s_max_i32 s74, s74, 48\n" \
  "s_lshr_b32 s74, s74, 3\n" \
  "s_lshl_b32 s74, s74, 2\n" \
  "s_sub_u32 s80, s84, s74\n" \
  "s_subb_u32 s81, s85, 0\n" \
  "s_setpc_b64 s[80:81]\n" \
  "s_nop 1\n" \
  "s_nop 1\n" \
  "s_nop 1\n" \
  "s_nop 1\n" \
  "s_nop 1\n" \
  "s_nop 1\n" \
  "s_nop 1\n" \
  "s_nop 1\n" \
  "s_nop 1\n" \
  "s_nop 1\n" \
  "s_nop 1\n" \
  "s_nop 1\n" \
  "s_nop 1\n" \
  "s_nop 1\n" \
  "s_nop 1\n" \
  "s_nop 1\n" \
  "LEAs3_%=:\n" \
  "s_barrier\n" \
  "buffer_load_dwordx4 v[24:27], v1, s[36:39], s72 offen offset:0 sc1\n" \
  "s_mov_b32 m0, 68608\n" \
  "s_nop 0\n" \
  "buffer_load_dwordx4 v1, s[36:39], s69 offen sc1 lds\n" \
  "buffer_load_dwordx4 v[28:31], v1, s[36:39], s72 offen offset:1024 sc1\n" \
  "s_mov_b32 m0, 72704\n" \
  "s_nop 0\n" \
  "buffer_load_dwordx4 v1, s[36:39], s70 offen sc1 lds\n" \
  "ds_write_b64 v6, v[60:61] offset:3072\n" \
  "ds_write_b64 v6, v[60:61] offset:3584\n" \
  "ds_write_b64 v6, v[60:61] offset:5632\n" \
  "s_add_u32 s68, s68, 1\n" \
  "LMs3_%=:\n" \
  "s_waitcnt vmcnt(3)\n" \
  "v_smfmac_f32_16x16x64_bf16 v[64:67], v[24:27], v[88:95], v0\n" \
  "ds_read_b128 v[88:91], v3 offset:2048\n" \
  "v_smfmac_f32_16x16x64_bf16 v[68:71], v[24:27], v[96:103], v0\n" \
  "ds_read_b128 v[92:95], v3 offset:3072\n" \
  "v_smfmac_f32_16x16x64_bf16 v[72:75], v[24:27], a[0:7], v0\n" \
  "ds_read_b128 v[96:99], v3 offset:24576\n" \
  "v_smfmac_f32_16x16x64_bf16 v[76:79], v[24:27], a[8:15], v0\n" \
  "ds_read_b128 v[100:103], v3 offset:25600\n" \
  "v_smfmac_f32_16x16x64_bf16 v[80:83], v[24:27], a[16:23], v0\n" \
  "v_smfmac_f32_16x16x64_bf16 v[84:87], v[24:27], v[104:111], v0\n" \
  "ds_read_b128 v[104:107], v3 offset:12288\n" \
  "v_smfmac_f32_16x16x64_bf16 v[20:23], v[24:27], v[112:119], v0\n" \
  "s_waitcnt vmcnt(2)\n" \
  "s_waitcnt lgkmcnt(5)\n" \
  "s_barrier\n" \
  "ds_read_b128 v[32:35], v2 offset:2048\n" \
  "s_waitcnt vmcnt(1)\n" \
  "v_smfmac_f32_16x16x64_bf16 v[64:67], v[28:31], a[24:31], v0\n" \
  "ds_read_b128 v[36:39], v2 offset:3072\n" \
  "v_smfmac_f32_16x16x64_bf16 v[68:71], v[28:31], a[32:39], v0\n" \
  "ds_read_b128 v[40:43], v2 offset:4096\n" \
  "v_smfmac_f32_16x16x64_bf16 v[72:75], v[28:31], a[40:47], v0\n" \
  "ds_read_b128 v[44:47], v2 offset:5120\n" \
  "s_waitcnt lgkmcnt(7)\n" \
  "v_smfmac_f32_16x16x64_bf16 v[84:87], v[28:31], v[88:95], v0\n" \
  "ds_read_b128 v[108:111], v3 offset:13312\n" \
  "s_waitcnt lgkmcnt(6)\n" \
  "v_smfmac_f32_16x16x64_bf16 v[20:23], v[28:31], v[96:103], v0\n" \
  "ds_read_b128 v[112:115], v3 offset:14336\n" \
  "s_waitcnt lgkmcnt(5)\n" \
  "v_smfmac_f32_16x16x64_bf16 v[64:67], v[32:35], a[64:71], v0\n" \
  "ds_read_b128 v[116:119], v3 offset:15360\n" \
  "v_smfmac_f32_16x16x64_bf16 v[68:71], v[32:35], a[72:79], v0\n" \
  "ds_read_b128 v[88:91], v3 offset:16384\n" \
  "v_smfmac_f32_16x16x64_bf16 v[72:75], v[32:35], a[80:87], v0\n" \
  "ds_read_b128 v[92:95], v3 offset:17408\n" \
  "s_waitcnt lgkmcnt(7)\n" \
  "v_smfmac_f32_16x16x64_bf16 v[64:67], v[36:39], a[104:111], v0\n" \
  "ds_read_b128 v[96:99], v3 offset:18432\n" \
  "s_waitcnt lgkmcnt(5)\n" \
  "v_smfmac_f32_16x16x64_bf16 v[68:71], v[36:39], v[104:111], v0\n" \
  "s_waitcnt vmcnt(0)\n" \
  "s_barrier\n" \
  "v_smfmac_f32_16x16x64_bf16 v[72:75], v[36:39], a[112:119], v0\n" \
  "ds_read_b128 v[48:51], v2 offset:6144\n" \
  "s_waitcnt lgkmcnt(4)\n" \
  "v_smfmac_f32_16x16x64_bf16 v[64:67], v[40:43], v[112:119], v0\n" \
  "ds_read_b128 v[52:55], v2 offset:7168\n" \
  "v_smfmac_f32_16x16x64_bf16 v[68:71], v[40:43], a[136:143], v0\n" \
  "ds_read_b128 v[56:59], v2 offset:8192\n" \
  "v_smfmac_f32_16x16x64_bf16 v[72:75], v[40:43], a[144:151], v0\n" \
  "ds_read_b128 v[60:63], v2 offset:9216\n" \
  "s_waitcnt lgkmcnt(5)\n" \
  "v_smfmac_f32_16x16x64_bf16 v[64:67], v[44:47], v[88:95], v0\n" \
  "ds_read_b128 v[100:103], v3 offset:19456\n" \
  "v_smfmac_f32_16x16x64_bf16 v[68:71], v[44:47], a[176:183], v0\n" \
  "ds_read_b128 v[104:107], v3 offset:20480\n" \
  "v_smfmac_f32_16x16x64_bf16 v[72:75], v[44:47], a[184:191], v0\n" \
  "ds_read_b128 v[108:111], v3 offset:21504\n" \
  "s_waitcnt lgkmcnt(6)\n" \
  "v_smfmac_f32_16x16x64_bf16 v[64:67], v[48:51], a[216:223], v0\n" \
  "ds_read_b128 v[112:115], v3 offset:26624\n" \
  "s_waitcnt lgkmcnt(6)\n" \
  "v_smfmac_f32_16x16x64_bf16 v[64:67], v[52:55], v[128:135], v0\n" \
  "ds_read_b128 v[116:119], v3 offset:27648\n" \
  "s_waitcnt lgkmcnt(6)\n" \
  "v_smfmac_f32_16x16x64_bf16 v[64:67], v[56:59], v[168:175], v0\n" \
  "ds_read_b128 v[88:91], v3 offset:28672\n" \
  "s_waitcnt lgkmcnt(5)\n" \
  "v_smfmac_f32_16x16x64_bf16 v[64:67], v[60:63], v[96:103], v0\n" \
  "ds_read_b128 v[92:95], v3 offset:29696\n" \
  "v_smfmac_f32_16x16x64_bf16 v[68:71], v[48:51], a[224:231], v0\n" \
  "ds_read_b128 v[96:99], v3 offset:4096\n" \
  "s_waitcnt lgkmcnt(5)\n" \
  "v_smfmac_f32_16x16x64_bf16 v[68:71], v[52:55], v[104:111], v0\n" \
  "ds_read_b128 v[100:103], v3 offset:5120\n" \
  "v_cvt_pk_bf16_f32 v64, v64, v65\n" \
  "v_smfmac_f32_16x16x64_bf16 v[68:71], v[56:59], v[176:183], v0\n" \
  "ds_read_b128 v[104:107], v3 offset:6144\n" \
  "v_cvt_pk_bf16_f32 v65, v66, v67\n" \
  "v_smfmac_f32_16x16x64_bf16 v[68:71], v[60:63], v[216:223], v0\n" \
  "ds_write_b64 v6, v[64:65] offset:35840\n" \
  "v_cmp_u_f32 vcc, v64, v65\n" \
  "v_smfmac_f32_16x16x64_bf16 v[72:75], v[48:51], a[232:239], v0\n" \
  "s_cbranch_vccnz LMQs3_%=\n" \
  "v_smfmac_f32_16x16x64_bf16 v[72:75], v[52:55], v[136:143], v0\n" \
  "ds_read_b128 v[108:111], v3 offset:7168\n" \
  "v_cvt_pk_bf16_f32 v68, v68, v69\n" \
  "v_smfmac_f32_16x16x64_bf16 v[72:75], v[56:59], v[184:191], v0\n" \
  "v_cvt_pk_bf16_f32 v69, v70, v71\n" \
  "v_smfmac_f32_16x16x64_bf16 v[72:75], v[60:63], v[224:231], v0\n" \
  "ds_write_b64 v6, v[68:69] offset:36352\n" \
  "v_smfmac_f32_16x16x64_bf16 v[76:79], v[28:31], a[48:55], v0\n" \
  "v_smfmac_f32_16x16x64_bf16 v[80:83], v[28:31], a[56:63], v0\n" \
  "s_waitcnt lgkmcnt(8)\n" \
  "v_cvt_pk_bf16_f32 v72, v72, v73\n" \
  "v_smfmac_f32_16x16x64_bf16 v[20:23], v[32:35], v[112:119], v0\n" \
  "s_waitcnt lgkmcnt(6)\n" \
  "v_cvt_pk_bf16_f32 v73, v74, v75\n" \
  "v_smfmac_f32_16x16x64_bf16 v[20:23], v[36:39], v[88:95], v0\n" \
  "ds_write_b64 v6, v[72:73] offset:38400\n" \
  "s_waitcnt lgkmcnt(5)\n" \
  "v_smfmac_f32_16x16x64_bf16 v[84:87], v[32:35], v[96:103], v0\n" \
  "s_waitcnt lgkmcnt(2)\n" \
  "v_smfmac_f32_16x16x64_bf16 v[84:87], v[36:39], v[104:111], v0\n" \
  "ds_write_b128 v2, v[20:23] offset:1024\n" \
  "v_smfmac_f32_16x16x64_bf16 v[76:79], v[32:35], a[88:95], v0\n" \
  "v_smfmac_f32_16x16x64_bf16 v[76:79], v[36:39], a[120:127], v0\n" \
  "v_smfmac_f32_16x16x64_bf16 v[76:79], v[40:43], a[152:159], v0\n" \
  "v_smfmac_f32_16x16x64_bf16 v[76:79], v[44:47], a[192:199], v0\n" \
  "v_smfmac_f32_16x16x64_bf16 v[76:79], v[48:51], a[240:247], v0\n" \
  "v_smfmac_f32_16x16x64_bf16 v[76:79], v[52:55], v[144:151], v0\n" \
  "v_smfmac_f32_16x16x64_bf16 v[76:79], v[56:59], v[192:199], v0\n" \
  "v_smfmac_f32_16x16x64_bf16 v[76:79], v[60:63], v[232:239], v0\n" \
  "v_smfmac_f32_16x16x64_bf16 v[80:83], v[32:35], a[96:103], v0\n" \
  "v_smfmac_f32_16x16x64_bf16 v[80:83], v[36:39], a[128:135], v0\n" \
  "v_cvt_pk_bf16_f32 v76, v76, v77\n" \
  "v_smfmac_f32_16x16x64_bf16 v[80:83], v[40:43], a[160:167], v0\n" \
  "v_cvt_pk_bf16_f32 v77, v78, v79\n" \
  "v_smfmac_f32_16x16x64_bf16 v[80:83], v[44:47], a[200:207], v0\n" \
  "ds_write_b64 v6, v[76:77] offset:3072\n" \
  "v_smfmac_f32_16x16x64_bf16 v[80:83], v[48:51], a[248:255], v0\n" \
  "v_smfmac_f32_16x16x64_bf16 v[80:83], v[52:55], v[152:159], v0\n" \
  "v_smfmac_f32_16x16x64_bf16 v[80:83], v[56:59], v[200:207], v0\n" \
  "v_smfmac_f32_16x16x64_bf16 v[80:83], v[60:63], v[240:247], v0\n" \
  "v_smfmac_f32_16x16x64_bf16 v[84:87], v[40:43], a[168:175], v0\n" \
  "v_smfmac_f32_16x16x64_bf16 v[84:87], v[44:47], a[208:215], v0\n" \
  "v_cvt_pk_bf16_f32 v80, v80, v81\n" \
  "v_smfmac_f32_16x16x64_bf16 v[84:87], v[48:51], v[120:127], v0\n" \
  "v_cvt_pk_bf16_f32 v81, v82, v83\n" \
  "v_smfmac_f32_16x16x64_bf16 v[84:87], v[52:55], v[160:167], v0\n" \
  "ds_write_b64 v6, v[80:81] offset:3584\n" \
  "v_smfmac_f32_16x16x64_bf16 v[84:87], v[56:59], v[208:215], v0\n" \
  "v_smfmac_f32_16x16x64_bf16 v[84:87], v[60:63], v[248:255], v0\n" \
  "s_nop 7\n" \
  "v_cvt_pk_bf16_f32 v84, v84, v85\n" \
  "v_cvt_pk_bf16_f32 v85, v86, v87\n" \
  "ds_write_b64 v6, v[84:85] offset:5632\n" \
  "s_barrier ; timing only\n" \
  "s_waitcnt lgkmcnt(0)\n" \
  "s_barrier\n" \
  "ds_read_b32 v60, v12 offset:512\n" \
  "s_waitcnt lgkmcnt(0)\n" \
  "v_cmp_ne_u32 vcc, 0, v60\n" \
  "s_nop 3\n" \
  "s_cbranch_vccnz LMXs3_%=\n" \
  "LMYs3_%=:\n" \
  "buffer_load_dword v15, v14, s[36:39], s72 offen sc1\n" \
  "s_lshl_b32 s74, s68, 16\n" \
  "s_or_b32 s74, s74, s51\n" \
  "v_mov_b32 v24, s74\n" \
  "v_mov_b32 v25, s49\n" \
  "v_mov_b32 v26, s48\n" \
  "v_mov_b32 v27, s50\n" \
  "buffer_store_dwordx4 v[24:27], v16, s[36:39], 0 offen sc1\n" \
  "ds_read_b128 v[88:91], v3 offset:8192\n" \
  "ds_read_b128 v[92:95], v3 offset:9216\n" \
  "ds_read_b128 v[96:99], v3 offset:10240\n" \
  "ds_read_b128 v[100:103], v3 offset:11264\n" \
  "ds_read_b128 v[104:107], v3 offset:0\n" \
  "ds_read_b128 v[108:111], v3 offset:1024\n" \
  "ds_read_b128 v[112:115], v3 offset:22528\n" \
  "ds_read_b128 v[116:119], v3 offset:23552\n" \
  "v_mov_b32 v60, -1\n" \
  "v_mov_b32 v61, -1\n" \
  "ds_write_b64 v6, v[60:61] offset:35840\n" \
  "ds_write_b64 v6, v[60:61] offset:36352\n" \
  "ds_write_b64 v6, v[60:61] offset:38400\n" \
  "ds_write_b64 v2, v[60:61] offset:1024\n" \
  "v_mov_b32 v20, 0\n" \
  "v_mov_b32 v21, 0\n" \
  "v_mov_b32 v22, 0\n" \
  "v_mov_b32 v23, 0\n" \
  "v_mov_b32 v64, 0\n" \
  "v_mov_b32 v65, 0\n" \
  "v_mov_b32 v66, 0\n" \
  "v_mov_b32 v67, 0\n" \
  "v_mov_b32 v68, 0\n" \
  "v_mov_b32 v69, 0\n" \
  "v_mov_b32 v70, 0\n" \
  "v_mov_b32 v71, 0\n" \
  "v_mov_b32 v72, 0\n" \
  "v_mov_b32 v73, 0\n" \
  "v_mov_b32 v74, 0\n" \
  "v_mov_b32 v75, 0\n" \
  "v_mov_b32 v76, 0\n" \
  "v_mov_b32 v77, 0\n" \
  "v_mov_b32 v78, 0\n" \
  "v_mov_b32 v79, 0\n" \
  "v_mov_b32 v80, 0\n" \
  "v_mov_b32 v81, 0\n" \
  "v_mov_b32 v82, 0\n" \
  "v_mov_b32 v83, 0\n" \
  "v_mov_b32 v84, 0\n" \
  "v_mov_b32 v85, 0\n" \
  "v_mov_b32 v86, 0\n" \
  "v_mov_b32 v87, 0\n" \
  "s_waitcnt lgkmcnt(0)\n" \
  "s_memrealtime s[76:77]\n" \
  "s_memtime s[78:79]\n" \
  "s_waitcnt lgkmcnt(0)\n" \
  "s_lshl_b32 s75, s76, 2\n" \
  "s_sub_i32 s75, s48, s75\n" \
  "s_mul_i32 s75, s75, s50\n" \
  "s_ashr_i32 s75, s75, 10\n" \
  "s_sub_i32 s74, s49, s78\n" \
  "s_min_i32 s74, s74, s75\n" \
  "s_sub_i32 s75, s75, 92\n" \
  "s_max_i32 s74, s74, s75\n" \
  "s_add_u32 s49, s74, s78\n" \
  "s_sub_u32 s74, s78, s89\n" \
  "s_sub_u32 s75, s76, s88\n" \
  "v_cvt_f32_u32 v24, s74\n" \
  "v_cvt_f32_u32 v25, s75\n" \
  "v_rcp_f32 v25, v25\n" \
  "v_mul_f32 v24, 0x43820000, v24\n" \
  "v_mul_f32 v24, v24, v25\n" \
  "v_cvt_u32_f32 v24, v24\n" \
  "s_nop 0\n" \
  "v_readfirstlane_b32 s74, v24\n" \
  "s_min_u32 s74, s74, 6400\n" \
  "s_cmp_gt_u32 s75, 900\n" \
  "s_cselect_b32 s50, s74, s50\n" \
  "s_cselect_b32 s88, s76, s88\n" \
  "s_cselect_b32 s89, s78, s89\n" \
  "s_waitcnt vmcnt(1)\n" \
  "v_readlane_b32 s74, v15, 3\n" \
  "s_nop 0\n" \
  "s_sub_u32 s75, s74, s51\n" \
  "s_cmp_lt_u32 s75, 16\n" \
  "s_cselect_b32 s51, s74, s51\n" \
  "v_readlane_b32 s74, v15, 0\n" \
  "s_add_u32 s90, s90, s53\n" \
  "s_sub_u32 s75, s74, s90\n" \
  "s_add_u32 s75, s75, 2000\n" \
  "s_cmp_lt_u32 s75, 4000\n" \
  "s_cselect_b32 s90, s74, s90\n" \
  "s_mul_i32 s74, s51, s54\n" \
  "s_add_u32 s74, s74, s90\n" \
  "s_sub_i32 s75, s74, s48\n" \
  "s_mov_b32 s48, s74\n" \
  "s_mul_i32 s75, s75, s50\n" \
  "s_ashr_i32 s75, s75, 10\n" \
  "s_add_u32 s49, s49, s75\n" \
  "s_mov_b32 s71, 0\n" \
  "s_cmp_eq_u32 s68, 64\n" \
  "s_cbranch_scc1 LENDs3_%=\n" \
  "v_readlane_b32 s72, v11, s68\n" \
  "s_nop 0\n" \
  "s_add_u32 s69, s72, 5120\n" \
  "s_add_u32 s70, s72, 17408\n" \
  "s_add_u32 s44, s72, s93\n" \
  "s_add_u32 s45, s72, s94\n" \
  "s_add_u32 s46, s72, s95\n" \
  "v_readlane_b32 s41, v7, s68\n" \
  "s_nop 1\n" \
  "s_branch LOOPs3_%=\n" \
  "LSAs3_%=:\n" \
  "s_add_i32 s71, s71, 48\n" \
  "s_cmp_gt_i32 s71, 0\n" \
  "s_cselect_b32 s74, s54, 0\n" \
  "s_addc_u32 s51, s51, 0\n" \
  "s_sub_i32 s71, s71, s74\n" \
  "s_mul_i32 s74, s51, s54\n" \
  "s_add_u32 s74, s74, s90\n" \
  "s_add_u32 s74, s74, s71\n" \
  "s_sub_i32 s75, s74, s48\n" \
  "s_mov_b32 s48, s74\n" \
  "s_mul_i32 s75, s75, s50\n" \
  "s_ashr_i32 s75, s75, 10\n" \
  "s_add_u32 s49, s49, s75\n" \
  "s_branch LWAs3_%=\n" \
  "LMFs3_%=:\n" \
  "ds_read_b128 v[88:91], v3 offset:8192\n" \
  "ds_read_b128 v[92:95], v3 offset:9216\n" \
  "ds_read_b128 v[96:99], v3 offset:10240\n" \
  "ds_read_b128 v[100:103], v3 offset:11264\n" \
  "ds_read_b128 v[104:107], v3 offset:0\n" \
  "ds_read_b128 v[108:111], v3 offset:1024\n" \
  "ds_read_b128 v[112:115], v3 offset:22528\n" \
  "ds_read_b128 v[116:119], v3 offset:23552\n" \
  "v_mov_b32 v60, -1\n" \
  "v_mov_b32 v61, -1\n" \
  "ds_write_b64 v6, v[60:61] offset:35840\n" \
  "ds_write_b64 v6, v[60:61] offset:36352\n" \
  "ds_write_b64 v6, v[60:61] offset:38400\n" \
  "ds_write_b64 v2, v[60:61] offset:1024\n" \
  "v_mov_b32 v20, 0\n" \
  "v_mov_b32 v21, 0\n" \
  "v_mov_b32 v22, 0\n" \
  "v_mov_b32 v23, 0\n" \
  "v_mov_b32 v64, 0\n" \
  "v_mov_b32 v65, 0\n" \
  "v_mov_b32 v66, 0\n" \
  "v_mov_b32 v67, 0\n" \
  "v_mov_b32 v68, 0\n" \
  "v_mov_b32 v69, 0\n" \
  "v_mov_b32 v70, 0\n" \
  "v_mov_b32 v71, 0\n" \
  "v_mov_b32 v72, 0\n" \
  "v_mov_b32 v73, 0\n" \
  "v_mov_b32 v74, 0\n" \
  "v_mov_b32 v75, 0\n" \
  "v_mov_b32 v76, 0\n" \
  "v_mov_b32 v77, 0\n" \
  "v_mov_b32 v78, 0\n" \
  "v_mov_b32 v79, 0\n" \
  "v_mov_b32 v80, 0\n" \
  "v_mov_b32 v81, 0\n" \
  "v_mov_b32 v82, 0\n" \
  "v_mov_b32 v83, 0\n" \
  "v_mov_b32 v84, 0\n" \
  "v_mov_b32 v85, 0\n" \
  "v_mov_b32 v86, 0\n" \
  "v_mov_b32 v87, 0\n" \
  "s_waitcnt lgkmcnt(0)\n" \
  "s_mov_b32 s71, 0\n" \
  "s_mul_i32 s74, s51, s54\n" \
  "s_add_u32 s74, s74, s90\n" \
  "s_sub_i32 s75, s74, s48\n" \
  "s_mov_b32 s48, s74\n" \
  "s_mul_i32 s75, s75, s50\n" \
  "s_ashr_i32 s75, s75, 10\n" \
  "s_add_u32 s49, s49, s75\n" \
  "LHMs3_%=:\n" \
  "s_add_u32 s48, s48, s54\n" \
  "s_add_u32 s49, s49, s86\n" \
  "s_add_u32 s51, s51, 1\n" \
  "LGMs3_%=:\n" \
  "s_memrealtime s[76:77]\n" \
  "s_memtime s[78:79]\n" \
  "s_waitcnt lgkmcnt(0)\n" \
  "s_lshl_b32 s75, s76, 2\n" \
  "s_sub_i32 s75, s48, s75\n" \
  "s_mul_i32 s75, s75, s50\n" \
  "s_ashr_i32 s75, s75, 10\n" \
  "s_sub_i32 s74, s49, s78\n" \
  "s_min_i32 s74, s74, s75\n" \
  "s_sub_i32 s75, s75, 92\n" \
  "s_max_i32 s74, s74, s75\n" \
  "s_add_u32 s49, s74, s78\n" \
  "s_cmp_gt_i32 s74, 20\n" \
  "s_cbranch_scc1 LGMs3_%=\n" \
  "s_cmp_lt_i32 s74, s87\n" \
  "s_cbranch_scc1 LHMs3_%=\n" \
  "s_barrier\n" \
  "buffer_load_dwordx4 v[24:27], v1, s[36:39], s72 offen offset:0 sc1\n" \
  "s_mov_b32 m0, 68608\n" \
  "s_nop 0\n" \
  "buffer_load_dwordx4 v1, s[36:39], s69 offen sc1 lds\n" \
  "buffer_load_dwordx4 v[28:31], v1, s[36:39], s72 offen offset:1024 sc1\n" \
  "s_mov_b32 m0, 72704\n" \
  "s_nop 0\n" \
  "buffer_load_dwordx4 v1, s[36:39], s70 offen sc1 lds\n" \
  "ds_write_b64 v6, v[60:61] offset:3072\n" \
  "ds_write_b64 v6, v[60:61] offset:3584\n" \
  "ds_write_b64 v6, v[60:61] offset:5632\n" \
  "s_branch LMs3_%=\n" \
  "LMQs3_%=:\n" \
  "s_waitcnt lgkmcnt(0)\n" \
  "s_barrier\n" \
  "s_barrier\n" \
  "ds_read_b32 v60, v12 offset:512\n" \
  "s_waitcnt lgkmcnt(0)\n" \
  "LMXs3_%=:\n" \
  "v_cmp_eq_u32 vcc, 2, v60\n" \
  "s_waitcnt lgkmcnt(0)\n" \
  "s_nop 2\n" \
  "s_barrier\n" \
  "s_cbranch_vccnz LMFs3_%=\n" \
  "s_branch LMYs3_%=\n" \
  "LENDs3_%=:\n" \
  "s_cmp_lg_u32 s38, 0\n" \
  "s_cbranch_scc1 LFINs3_%=\n" \
  "s_waitcnt vmcnt(0) lgkmcnt(0)\n" \
  "s_mov_b32 s38, s47\n" \
  "s_mov_b32 s54, s97\n" \
  "s_mov_b32 s96, s98\n" \
  "s_mov_b32 s68, 0\n" \
  "s_mov_b32 s73, 0\n" \
  "s_mov_b32 s56, 0\n" \
  "s_mov_b32 s57, 0\n" \
  "s_branch LPASSs3_%=\n" \
  "LFINs3_%=:\n" \
  "s_branch LDONE_%=\n" \
  "LDONE_%=:\n" \
  "s_waitcnt vmcnt(0) lgkmcnt(0)\n" \
  "s_mov_b32 s53, s73\n" \
  "s_mov_b32 s54, s51\n" \
  "s_mov_b32 s55, s50\n"
#define CROLE_CLOBBERS \
  "v32", "v33", "v34", "v35", "v36", "v37", "v38", "v39", "v40", "v41", "v42", "v43", "v44", "v45", "v46", "v47", "v48", "v49", "v50", "v51", "v52", \
  "v53", "v54", "v55", "v56", "v57", "v58", "v59", "v60", "v61", "v62", "v63", "v64", "v65", "v66", "v67", "v68", "v69", "v70", "v71", "v72", "v73", \
  "v74", "v75", "v76", "v77", "v78", "v79", "v80", "v81", "v82", "v83", "v84", "v85", "v86", "v87", "v88", "v89", "v90", "v91", "v92", "v93", "v94", \
  "v95", "v96", "v97", "v98", "v99", "v100", "v101", "v102", "v103", "v104", "v105", "v106", "v107", "v108", "v109", "v110", "v111", "v112", "v113", \
  "v114", "v115", "v116", "v117", "v118", "v119", "v120", "v121", "v122", "v123", "v124", "v125", "v126", "v127", "v128", "v129", "v130", "v131", \
  "v132", "v133", "v134", "v135", "v136", "v137", "v138", "v139", "v140", "v141", "v142", "v143", "v144", "v145", "v146", "v147", "v148", "v149", \
  "v150", "v151", "v152", "v153", "v154", "v155", "v156", "v157", "v158", "v159", "v160", "v161", "v162", "v163", "v164", "v165", "v166", "v167", \
  "v168", "v169", "v170", "v171", "v172", "v173", "v174", "v175", "v176", "v177", "v178", "v179", "v180", "v181", "v182", "v183", "v184", "v185", \
  "v186", "v187", "v188", "v189", "v190", "v191", "v192", "v193", "v194", "v195", "v196", "v197", "v198", "v199", "v200", "v201", "v202", "v203", \
  "v204", "v205", "v206", "v207", "v240", "v241", "v242", "v243", "v244", "v245", "v246", "v247", "v248", "v249", "v250", "v251", "v252", "v253", \
  "v254", "v255", "s34", "s35", "s48", "s49", "s50", "s51", "s68", "s69", "s70", "s71", "s72", "s73", "s74", "s75", "s76", "s77", "s78", "s79", "s80", \
  "s81", "s82", "s83", "s84", "s85", "s86", "s87", "s88", "s89", "s90", "s91", "s93", "s94", "s95", "s96", "a0", "a1", "a2", "a3", "a4", "a5", "a6", \
  "a7", "a8", "a9", "a10", "a11", "a12", "a13", "a14", "a15", "a16", "a17", "a18", "a19", "a20", "a21", "a22", "a23", "a24", "a25", "a26", "a27", "a28", \
  "a29", "a30", "a31", "a32", "a33", "a34", "a35", "a36", "a37", "a38", "a39", "a40", "a41", "a42", "a43", "a44", "a45", "a46", "a47", "a48", "a49", \
  "a50", "a51", "a52", "a53", "a54", "a55", "a56", "a57", "a58", "a59", "a60", "a61", "a62", "a63", "a64", "a65", "a66", "a67", "a68", "a69", "a70", \
  "a71", "a72", "a73", "a74", "a75", "a76", "a77", "a78", "a79", "a80", "a81", "a82", "a83", "a84", "a85", "a86", "a87", "a88", "a89", "a90", "a91", \
  "a92", "a93", "a94", "a95", "a96", "a97", "a98", "a99", "a100", "a101", "a102", "a103", "a104", "a105", "a106", "a107", "a108", "a109", "a110", \
  "a111", "a112", "a113", "a114", "a115", "a116", "a117", "a118", "a119", "a120", "a121", "a122", "a123", "a124", "a125", "a126", "a127", "a128", \
  "a129", "a130", "a131", "a132", "a133", "a134", "a135", "a136", "a137", "a138", "a139", "a140", "a141", "a142", "a143", "a144", "a145", "a146", \
  "a147", "a148", "a149", "a150", "a151", "a152", "a153", "a154", "a155", "a156", "a157", "a158", "a159", "a160", "a161", "a162", "a163", "a164", \
  "a165", "a166", "a167", "a168", "a169", "a170", "a171", "a172", "a173", "a174", "a175", "a176", "a177", "a178", "a179", "a180", "a181", "a182", \
  "a183", "a184", "a185", "a186", "a187", "a188", "a189", "a190", "a191", "a192", "a193", "a194", "a195", "a196", "a197", "a198", "a199", "a200", \
  "a201", "a202", "a203", "a204", "a205", "a206", "a207", "a208", "a209", "a210", "a211", "a212", "a213", "a214", "a215", "a216", "a217", "a218", \
  "a219", "a220", "a221", "a222", "a223", "a224", "a225", "a226", "a227", "a228", "a229", "a230", "a231", "a232", "a233", "a234", "a235", "a236", \
  "a237", "a238", "a239", "a240", "a241", "a242", "a243", "a244", "a245", "a246", "a247", "a248", "a249", "a250", "a251", "a252", "a253", "a254", "a255"
#define CROLE_ASM \
  "s_cmp_lt_i32 s61, 0\n" \
  "s_cbranch_scc1 LXIT_%=\n" \
  "v_mov_b32 v0, v208\n" \
  "v_mov_b32 v1, v209\n" \
  "v_mov_b32 v2, v210\n" \
  "v_mov_b32 v3, v211\n" \
  "v_mov_b32 v4, v212\n" \
  "v_mov_b32 v5, v213\n" \
  "v_mov_b32 v6, v214\n" \
  "v_mov_b32 v7, v215\n" \
  "v_mov_b32 v8, v216\n" \
  "v_mov_b32 v9, v217\n" \
  "v_mov_b32 v10, v218\n" \
  "v_mov_b32 v11, v219\n" \
  "v_mov_b32 v12, v220\n" \
  "v_mov_b32 v13, v221\n" \
  "v_mov_b32 v14, v222\n" \
  "v_mov_b32 v15, v223\n" \
  "v_mov_b32 v16, v224\n" \
  "v_mov_b32 v17, v225\n" \
  "v_mov_b32 v18, v226\n" \
  "v_mov_b32 v19, v227\n" \
  "v_mov_b32 v20, v228\n" \
  "v_mov_b32 v21, v229\n" \
  "v_mov_b32 v22, v230\n" \
  "v_mov_b32 v23, v231\n" \
  "v_mov_b32 v24, v232\n" \
  "v_mov_b32 v25, v233\n" \
  "v_mov_b32 v26, v234\n" \
  "v_mov_b32 v27, v235\n" \
  "v_mov_b32 v28, v236\n" \
  "v_mov_b32 v29, v237\n" \
  "v_mov_b32 v30, v238\n" \
  "v_mov_b32 v31, v239\n" \
  "s_mov_b32 s34, 0x00ff00ff\n" \
  "s_mov_b32 s35, 0x00ff00ff\n" \
  "s_mov_b32 s40, 0x00ff00ff\n" \
  "s_mov_b32 s41, 0xff00ff00\n" \
  "s_mov_b32 s68, 0\n" \
  "s_mov_b32 s73, 0\n" \
  "s_mov_b32 s58, 0\n" \
  "s_mov_b32 s59, 0\n" \
  "s_mov_b32 s51, 0\n" \
  "s_mov_b32 s71, 0\n" \
  "s_mov_b32 s44, 0\n" \
  "s_mov_b32 s50, 5900\n" \
  "s_mov_b32 s42, 0x40000000\n" \
  "s_mov_b32 s93, s60\n" \
  "s_mov_b32 s60, 0\n" \
  "s_mov_b32 s61, 0\n" \
  "s_mov_b32 s62, 0\n" \
  "s_mov_b32 s63, 0\n" \
  "s_mov_b32 s64, 0\n" \
  "s_mov_b32 s65, 0\n" \
  "s_mov_b32 s66, 0\n" \
  "s_mov_b32 s67, 0\n" \
  "s_cmp_eq_u32 s52, 3\n" \
  "s_cbranch_scc1 LSTARTc3_%=\n" \
  "LSTARTc0_%=:\n" \
  "LQU0c0_%=:\n" \
  "buffer_load_dword v92, v14, s[36:39], 0 offen offset:0 sc1\n" \
  "s_waitcnt vmcnt(0)\n" \
  "v_cmp_ne_u32 vcc, 1, v92\n" \
  "s_nop 3\n" \
  "s_cbranch_vccz LRU0c0_%=\n" \
  "s_add_u32 s73, s73, 1\n" \
  "s_cmp_gt_u32 s73, 20000\n" \
  "s_cbranch_scc1 LRU0c0_%=\n" \
  "s_sleep 1\n" \
  "s_branch LQU0c0_%=\n" \
  "LRU0c0_%=:\n" \
  "v_readlane_b32 s46, v16, 6\n" \
  "s_nop 4\n" \
  "buffer_load_dwordx4 a[240:243], v18, s[36:39], s46 offen sc1\n" \
  "buffer_load_dwordx4 a[248:251], v19, s[36:39], s46 offen sc1\n" \
  "buffer_load_dwordx4 v[112:115], v20, s[36:39], s46 offen sc1\n" \
  "buffer_load_dwordx4 v[120:123], v21, s[36:39], s46 offen sc1\n" \
  "buffer_load_dwordx4 v[128:131], v22, s[36:39], s46 offen sc1\n" \
  "s_barrier\n" \
  "v_readlane_b32 s46, v16, 7\n" \
  "s_nop 4\n" \
  "buffer_load_dwordx4 a[244:247], v18, s[36:39], s46 offen sc1\n" \
  "buffer_load_dwordx4 a[252:255], v19, s[36:39], s46 offen sc1\n" \
  "buffer_load_dwordx4 v[116:119], v20, s[36:39], s46 offen sc1\n" \
  "buffer_load_dwordx4 v[124:127], v21, s[36:39], s46 offen sc1\n" \
  "buffer_load_dwordx4 v[132:135], v22, s[36:39], s46 offen sc1\n" \
  "s_barrier\n" \
  "v_readlane_b32 s46, v16, 16\n" \
  "s_nop 4\n" \
  "buffer_load_dwordx4 v[136:139], v18, s[36:39], s46 offen sc1\n" \
  "buffer_load_dwordx4 v[144:147], v19, s[36:39], s46 offen sc1\n" \
  "buffer_load_dwordx4 v[152:155], v20, s[36:39], s46 offen sc1\n" \
  "buffer_load_dwordx4 v[160:163], v21, s[36:39], s46 offen sc1\n" \
  "buffer_load_dwordx4 v[168:171], v22, s[36:39], s46 offen sc1\n" \
  "v_readlane_b32 s46, v16, 17\n" \
  "s_nop 4\n" \
  "buffer_load_dwordx4 v[140:143], v18, s[36:39], s46 offen sc1\n" \
  "buffer_load_dwordx4 v[148:151], v19, s[36:39], s46 offen sc1\n" \
  "buffer_load_dwordx4 v[156:159], v20, s[36:39], s46 offen sc1\n" \
  "buffer_load_dwordx4 v[164:167], v21, s[36:39], s46 offen sc1\n" \
  "buffer_load_dwordx4 v[172:175], v22, s[36:39], s46 offen sc1\n" \
  "s_barrier\n" \
  "LQU4c0_%=:\n" \
  "buffer_load_dword v92, v14, s[36:39], 0 offen offset:1536 sc1\n" \
  "s_waitcnt vmcnt(0)\n" \
  "v_cmp_ne_u32 vcc, 1, v92\n" \
  "s_nop 3\n" \
  "s_cbranch_vccz LRU4c0_%=\n" \
  "s_add_u32 s73, s73, 1\n" \
  "s_cmp_gt_u32 s73, 20000\n" \
  "s_cbranch_scc1 LRU4c0_%=\n" \
  "s_sleep 1\n" \
  "s_branch LQU4c0_%=\n" \
  "LRU4c0_%=:\n" \
  "v_readlane_b32 s46, v16, 8\n" \
  "s_nop 4\n" \
  "buffer_load_dwordx4 v[176:179], v18, s[36:39], s46 offen sc1\n" \
  "buffer_load_dwordx4 v[184:187], v19, s[36:39], s46 offen sc1\n" \
  "buffer_load_dwordx4 v[192:195], v20, s[36:39], s46 offen sc1\n" \
  "buffer_load_dwordx4 v[200:203], v21, s[36:39], s46 offen sc1\n" \
  "buffer_load_dwordx4 v[208:211], v22, s[36:39], s46 offen sc1\n" \
  "buffer_atomic_add v15, v7, s[36:39], 0 offen sc1\n" \
  "v_readlane_b32 s46, v16, 9\n" \
  "s_nop 4\n" \
  "buffer_load_dwordx4 v[180:183], v18, s[36:39], s46 offen sc1\n" \
  "buffer_load_dwordx4 v[188:191], v19, s[36:39], s46 offen sc1\n" \
  "buffer_load_dwordx4 v[196:199], v20, s[36:39], s46 offen sc1\n" \
  "buffer_load_dwordx4 v[204:207], v21, s[36:39], s46 offen sc1\n" \
  "buffer_load_dwordx4 v[212:215], v22, s[36:39], s46 offen sc1\n" \
  "s_barrier\n" \
  "v_readlane_b32 s46, v16, 18\n" \
  "s_nop 4\n" \
  "buffer_load_dwordx4 v[216:219], v18, s[36:39], s46 offen sc1\n" \
  "buffer_load_dwordx4 v[224:227], v19, s[36:39], s46 offen sc1\n" \
  "buffer_load_dwordx4 v[232:235], v20, s[36:39], s46 offen sc1\n" \
  "buffer_load_dwordx4 v[240:243], v21, s[36:39], s46 offen sc1\n" \
  "buffer_load_dwordx4 v[248:251], v22, s[36:39], s46 offen sc1\n" \
  "v_readlane_b32 s46, v16, 19\n" \
  "s_nop 4\n" \
  "buffer_load_dwordx4 v[220:223], v18, s[36:39], s46 offen sc1\n" \
  "buffer_load_dwordx4 v[228:231], v19, s[36:39], s46 offen sc1\n" \
  "buffer_load_dwordx4 v[236:239], v20, s[36:39], s46 offen sc1\n" \
  "buffer_load_dwordx4 v[244:247], v21, s[36:39], s46 offen sc1\n" \
  "buffer_load_dwordx4 v[252:255], v22, s[36:39], s46 offen sc1\n" \
  "s_cmp_eq_u32 s52, 1\n" \
  "s_cbranch_scc1 LSTARTc1_%=\n" \
  "s_cmp_eq_u32 s52, 2\n" \
  "s_cbranch_scc1 LSTARTc2_%=\n" \
  "s_mov_b32 s57, 0\n" \
  "v_readlane_b32 s44, v4, 0\n" \
  "s_nop 3\n" \
  "LOOPc0_%=:\n" \
  "s_mov_b32 m0, 0\n" \
  "s_add_u32 s75, s44, 0\n" \
  "s_barrier\n" \
  "buffer_load_dwordx4 v1, s[36:39], s75 offen offset:0 sc1 lds\n" \
  "buffer_load_dwordx4 v1, s[36:39], s75 offen offset:1024 sc1 lds\n" \
  "buffer_load_dwordx4 v1, s[36:39], s75 offen offset:2048 sc1 lds\n" \
  "buffer_load_dwordx4 v1, s[36:39], s75 offen offset:3072 sc1 lds\n" \
  "v_mov_b32 v56, 0\n" \
  "v_mov_b32 v57, 0\n" \
  "v_mov_b32 v58, 0\n" \
  "v_mov_b32 v59, 0\n" \
  "v_mov_b32 v60, 0\n" \
  "v_mov_b32 v61, 0\n" \
  "v_mov_b32 v62, 0\n" \
  "v_mov_b32 v63, 0\n" \
  "v_mov_b32 v64, 0\n" \
  "v_mov_b32 v65, 0\n" \
  "v_mov_b32 v66, 0\n" \
  "v_mov_b32 v67, 0\n" \
  "v_mov_b32 v68, 0\n" \
  "v_mov_b32 v69, 0\n" \
  "v_mov_b32 v70, 0\n" \
  "v_mov_b32 v71, 0\n" \
  "v_mov_b32 v72, 0\n" \
  "v_mov_b32 v73, 0\n" \
  "v_mov_b32 v74, 0\n" \
  "v_mov_b32 v75, 0\n" \
  "s_add_u32 s68, s68, 1\n" \
  "s_min_u32 s74, s68, 63\n" \
  "v_readlane_b32 s45, v4, s74\n" \
  "s_waitcnt vmcnt(0)\n" \
  "s_barrier\n" \
  "buffer_store_dwordx4 v[76:79], v10, s[36:39], s42 offen offset:0 sc1\n" \
  "buffer_store_dwordx4 v[80:83], v10, s[36:39], s42 offen offset:1024 sc1\n" \
  "buffer_store_dwordx4 v[84:87], v10, s[36:39], s42 offen offset:2048 sc1\n" \
  "buffer_store_dwordx4 v[88:91], v10, s[36:39], s42 offen offset:3072 sc1\n" \
  "LMc0_%=:\n" \
  "ds_read_b128 v[16:19], v1 offset:0\n" \
  "ds_read_b128 v[20:23], v1 offset:1024\n" \
  "ds_read_b128 v[24:27], v1 offset:2048\n" \
  "ds_read_b128 v[28:31], v1 offset:3072\n" \
  "ds_read_b128 v[32:35], v1 offset:4096\n" \
  "ds_read_b128 v[36:39], v1 offset:5120\n" \
  "ds_read_b128 v[40:43], v1 offset:6144\n" \
  "ds_read_b128 v[44:47], v1 offset:7168\n" \
  "ds_read_b128 v[48:51], v1 offset:8192\n" \
  "ds_read_b128 v[52:55], v1 offset:9216\n" \
  "s_waitcnt lgkmcnt(9)\n" \
  "v_smfmac_f32_16x16x64_bf16 v[56:59], v[16:19], a[0:7], v0\n" \
  "s_waitcnt lgkmcnt(8)\n" \
  "v_smfmac_f32_16x16x64_bf16 v[56:59], v[20:23], a[80:87], v0\n" \
  "s_waitcnt lgkmcnt(7)\n" \
  "v_smfmac_f32_16x16x64_bf16 v[56:59], v[24:27], a[160:167], v0\n" \
  "s_waitcnt lgkmcnt(6)\n" \
  "v_smfmac_f32_16x16x64_bf16 v[56:59], v[28:31], a[240:247], v0\n" \
  "s_waitcnt lgkmcnt(5)\n" \
  "v_smfmac_f32_16x16x64_bf16 v[56:59], v[32:35], v[176:183], v0\n" \
  "s_waitcnt lgkmcnt(4)\n" \
  "v_smfmac_f32_16x16x64_bf16 v[56:59], v[36:39], a[40:47], v0\n" \
  "s_waitcnt lgkmcnt(3)\n" \
  "v_smfmac_f32_16x16x64_bf16 v[56:59], v[40:43], a[120:127], v0\n" \
  "s_waitcnt lgkmcnt(2)\n" \
  "v_smfmac_f32_16x16x64_bf16 v[56:59], v[44:47], a[200:207], v0\n" \
  "s_waitcnt lgkmcnt(1)\n" \
  "v_smfmac_f32_16x16x64_bf16 v[56:59], v[48:51], v[136:143], v0\n" \
  "s_waitcnt lgkmcnt(0)\n" \
  "v_smfmac_f32_16x16x64_bf16 v[56:59], v[52:55], v[216:223], v0\n" \
  "v_smfmac_f32_16x16x64_bf16 v[60:63], v[16:19], a[8:15], v0\n" \
  "v_smfmac_f32_16x16x64_bf16 v[64:67], v[16:19], a[16:23], v0\n" \
  "v_cmp_u_f32 vcc, v56, v57\n" \
  "v_cmp_u_f32 s[82:83], v58, v59\n" \
  "s_nop 1\n" \
  "s_or_b64 vcc, vcc, s[82:83]\n" \
  "s_cbranch_scc1 LMFc0_%=\n" \
  "LMOKc0_%=:\n" \
  "v_smfmac_f32_16x16x64_bf16 v[68:71], v[16:19], a[24:31], v0\n" \
  "v_smfmac_f32_16x16x64_bf16 v[72:75], v[16:19], a[32:39], v0\n" \
  "v_smfmac_f32_16x16x64_bf16 v[60:63], v[20:23], a[88:95], v0\n" \
  "v_smfmac_f32_16x16x64_bf16 v[64:67], v[20:23], a[96:103], v0\n" \
  "v_smfmac_f32_16x16x64_bf16 v[68:71], v[20:23], a[104:111], v0\n" \
  "v_smfmac_f32_16x16x64_bf16 v[72:75], v[20:23], a[112:119], v0\n" \
  "v_smfmac_f32_16x16x64_bf16 v[60:63], v[24:27], a[168:175], v0\n" \
  "v_smfmac_f32_16x16x64_bf16 v[64:67], v[24:27], a[176:183], v0\n" \
  "v_smfmac_f32_16x16x64_bf16 v[68:71], v[24:27], a[184:191], v0\n" \
  "v_smfmac_f32_16x16x64_bf16 v[72:75], v[24:27], a[192:199], v0\n" \
  "v_smfmac_f32_16x16x64_bf16 v[60:63], v[28:31], a[248:255], v0\n" \
  "v_smfmac_f32_16x16x64_bf16 v[64:67], v[28:31], v[112:119], v0\n" \
  "v_smfmac_f32_16x16x64_bf16 v[68:71], v[28:31], v[120:127], v0\n" \
  "v_smfmac_f32_16x16x64_bf16 v[72:75], v[28:31], v[128:135], v0\n" \
  "v_smfmac_f32_16x16x64_bf16 v[60:63], v[32:35], v[184:191], v0\n" \
  "v_smfmac_f32_16x16x64_bf16 v[64:67], v[32:35], v[192:199], v0\n" \
  "v_smfmac_f32_16x16x64_bf16 v[68:71], v[32:35], v[200:207], v0\n" \
  "v_smfmac_f32_16x16x64_bf16 v[72:75], v[32:35], v[208:215], v0\n" \
  "v_smfmac_f32_16x16x64_bf16 v[60:63], v[36:39], a[48:55], v0\n" \
  "v_smfmac_f32_16x16x64_bf16 v[64:67], v[36:39], a[56:63], v0\n" \
  "v_smfmac_f32_16x16x64_bf16 v[68:71], v[36:39], a[64:71], v0\n" \
  "v_smfmac_f32_16x16x64_bf16 v[72:75], v[36:39], a[72:79], v0\n" \
  "v_smfmac_f32_16x16x64_bf16 v[60:63], v[40:43], a[128:135], v0\n" \
  "v_smfmac_f32_16x16x64_bf16 v[64:67], v[40:43], a[136:143], v0\n" \
  "v_smfmac_f32_16x16x64_bf16 v[68:71], v[40:43], a[144:151], v0\n" \
  "v_smfmac_f32_16x16x64_bf16 v[72:75], v[40:43], a[152:159], v0\n" \
  "v_smfmac_f32_16x16x64_bf16 v[60:63], v[44:47], a[208:215], v0\n" \
  "v_smfmac_f32_16x16x64_bf16 v[64:67], v[44:47], a[216:223], v0\n" \
  "v_smfmac_f32_16x16x64_bf16 v[68:71], v[44:47], a[224:231], v0\n" \
  "v_smfmac_f32_16x16x64_bf16 v[72:75], v[44:47], a[232:239], v0\n" \
  "v_smfmac_f32_16x16x64_bf16 v[60:63], v[48:51], v[144:151], v0\n" \
  "v_smfmac_f32_16x16x64_bf16 v[64:67], v[48:51], v[152:159], v0\n" \
  "v_smfmac_f32_16x16x64_bf16 v[68:71], v[48:51], v[160:167], v0\n" \
  "v_smfmac_f32_16x16x64_bf16 v[72:75], v[48:51], v[168:175], v0\n" \
  "v_smfmac_f32_16x16x64_bf16 v[60:63], v[52:55], v[224:231], v0\n" \
  "v_smfmac_f32_16x16x64_bf16 v[64:67], v[52:55], v[232:239], v0\n" \
  "v_smfmac_f32_16x16x64_bf16 v[68:71], v[52:55], v[240:247], v0\n" \
  "v_smfmac_f32_16x16x64_bf16 v[72:75], v[52:55], v[248:255], v0\n" \
  "v_permlane32_swap_b32 v56, v60\n" \
  "v_permlane32_swap_b32 v57, v61\n" \
  "v_permlane32_swap_b32 v58, v62\n" \
  "v_permlane32_swap_b32 v59, v63\n" \
  "v_pk_add_f32 v[56:57], v[56:57], v[60:61]\n" \
  "v_pk_add_f32 v[58:59], v[58:59], v[62:63]\n" \
  "v_cvt_pk_bf16_f32 v56, v56, v57\n" \
  "v_cvt_pk_bf16_f32 v57, v58, v59\n" \
  "ds_write_b64 v2, v[56:57] offset:0\n" \
  "v_permlane32_swap_b32 v64, v68\n" \
  "v_permlane32_swap_b32 v65, v69\n" \
  "v_permlane32_swap_b32 v66, v70\n" \
  "v_permlane32_swap_b32 v67, v71\n" \
  "v_pk_add_f32 v[64:65], v[64:65], v[68:69]\n" \
  "v_pk_add_f32 v[66:67], v[66:67], v[70:71]\n" \
  "v_cvt_pk_bf16_f32 v64, v64, v65\n" \
  "v_cvt_pk_bf16_f32 v65, v66, v67\n" \
  "ds_write_b64 v2, v[64:65] offset:512\n" \
  "v_permlane32_swap_b32 v72, v74\n" \
  "v_permlane32_swap_b32 v73, v75\n" \
  "v_pk_add_f32 v[72:73], v[72:73], v[74:75]\n" \
  "v_cvt_pk_bf16_f32 v72, v72, v73\n" \
  "ds_write_b32 v3, v72 offset:1024\n" \
  "s_waitcnt lgkmcnt(0)\n" \
  "s_waitcnt vmcnt(0)\n" \
  "s_barrier\n" \
  "ds_read_b128 v[76:79], v1 offset:10240\n" \
  "ds_read_b128 v[80:83], v1 offset:11264\n" \
  "ds_read_b128 v[84:87], v1 offset:12288\n" \
  "ds_read_b128 v[88:91], v1 offset:13312\n" \
  "s_sub_u32 s74, s68, 1\n" \
  "v_readlane_b32 s42, v9, s74\n" \
  "s_waitcnt lgkmcnt(0)\n" \
  "s_mov_b32 s44, s45\n" \
  "s_barrier\n" \
  "s_cmp_eq_u32 s68, 64\n" \
  "s_cbranch_scc0 LOOPc0_%=\n" \
  "buffer_store_dwordx4 v[76:79], v10, s[36:39], s42 offen offset:0 sc1\n" \
  "buffer_store_dwordx4 v[80:83], v10, s[36:39], s42 offen offset:1024 sc1\n" \
  "buffer_store_dwordx4 v[84:87], v10, s[36:39], s42 offen offset:2048 sc1\n" \
  "buffer_store_dwordx4 v[88:91], v10, s[36:39], s42 offen offset:3072 sc1\n" \
  "s_waitcnt vmcnt(0)\n" \
  "buffer_atomic_add v15, v11, s[36:39], 0 offen sc1\n" \
  "s_branch LDONE_%=\n" \
  "LMFc0_%=:\n" \
  "v_max_u32 v92, v16, v17\n" \
  "v_max3_u32 v92, v92, v18, v19\n" \
  "v_max3_u32 v92, v92, v20, v21\n" \
  "v_max3_u32 v92, v92, v22, v23\n" \
  "v_max3_u32 v92, v92, v24, v25\n" \
  "v_max3_u32 v92, v92, v26, v27\n" \
  "v_max3_u32 v92, v92, v28, v29\n" \
  "v_max3_u32 v92, v92, v30, v31\n" \
  "v_max3_u32 v92, v92, v32, v33\n" \
  "v_max3_u32 v92, v92, v34, v35\n" \
  "v_max3_u32 v92, v92, v36, v37\n" \
  "v_max3_u32 v92, v92, v38, v39\n" \
  "v_max3_u32 v92, v92, v40, v41\n" \
  "v_max3_u32 v92, v92, v42, v43\n" \
  "v_max3_u32 v92, v92, v44, v45\n" \
  "v_max3_u32 v92, v92, v46, v47\n" \
  "v_max3_u32 v92, v92, v48, v49\n" \
  "v_max3_u32 v92, v92, v50, v51\n" \
  "v_max3_u32 v92, v92, v52, v53\n" \
  "v_max3_u32 v92, v92, v54, v55\n" \
  "v_cmp_eq_u32 vcc, -1, v92\n" \
  "s_nop 3\n" \
  "s_cbranch_vccz LMOKc0_%=\n" \
  "s_cmp_gt_u32 s57, 20000\n" \
  "s_cbranch_scc1 LMOKc0_%=\n" \
  "s_add_u32 s57, s57, 1\n" \
  "s_barrier\n" \
  "buffer_load_dwordx4 v1, s[36:39], s75 offen offset:0 sc1 lds\n" \
  "buffer_load_dwordx4 v1, s[36:39], s75 offen offset:1024 sc1 lds\n" \
  "buffer_load_dwordx4 v1, s[36:39], s75 offen offset:2048 sc1 lds\n" \
  "buffer_load_dwordx4 v1, s[36:39], s75 offen offset:3072 sc1 lds\n" \
  "v_mov_b32 v56, 0\n" \
  "v_mov_b32 v57, 0\n" \
  "v_mov_b32 v58, 0\n" \
  "v_mov_b32 v59, 0\n" \
  "v_mov_b32 v60, 0\n" \
  "v_mov_b32 v61, 0\n" \
  "v_mov_b32 v62, 0\n" \
  "v_mov_b32 v63, 0\n" \
  "v_mov_b32 v64, 0\n" \
  "v_mov_b32 v65, 0\n" \
  "v_mov_b32 v66, 0\n" \
  "v_mov_b32 v67, 0\n" \
  "v_mov_b32 v68, 0\n" \
  "v_mov_b32 v69, 0\n" \
  "v_mov_b32 v70, 0\n" \
  "v_mov_b32 v71, 0\n" \
  "v_mov_b32 v72, 0\n" \
  "v_mov_b32 v73, 0\n" \
  "v_mov_b32 v74, 0\n" \
  "v_mov_b32 v75, 0\n" \
  "s_waitcnt vmcnt(0)\n" \
  "s_barrier\n" \
  "s_branch LMc0_%=\n" \
  "LSTARTc1_%=:\n" \
  "s_mov_b32 s57, 0\n" \
  "v_readlane_b32 s44, v4, 0\n" \
  "s_nop 3\n" \
  "LOOPc1_%=:\n" \
  "s_mov_b32 m0, 4096\n" \
  "s_add_u32 s75, s44, 4096\n" \
  "s_barrier\n" \
  "buffer_load_dwordx4 v1, s[36:39], s75 offen offset:0 sc1 lds\n" \
  "buffer_load_dwordx4 v1, s[36:39], s75 offen offset:1024 sc1 lds\n" \
  "buffer_load_dwordx4 v1, s[36:39], s75 offen offset:2048 sc1 lds\n" \
  "buffer_load_dwordx4 v1, s[36:39], s75 offen offset:3072 sc1 lds\n" \
  "v_mov_b32 v56, 0\n" \
  "v_mov_b32 v57, 0\n" \
  "v_mov_b32 v58, 0\n" \
  "v_mov_b32 v59, 0\n" \
  "v_mov_b32 v60, 0\n" \
  "v_mov_b32 v61, 0\n" \
  "v_mov_b32 v62, 0\n" \
  "v_mov_b32 v63, 0\n" \
  "v_mov_b32 v64, 0\n" \
  "v_mov_b32 v65, 0\n" \
  "v_mov_b32 v66, 0\n" \
  "v_mov_b32 v67, 0\n" \
  "v_mov_b32 v68, 0\n" \
  "v_mov_b32 v69, 0\n" \
  "v_mov_b32 v70, 0\n" \
  "v_mov_b32 v71, 0\n" \
  "v_mov_b32 v72, 0\n" \
  "v_mov_b32 v73, 0\n" \
  "v_mov_b32 v74, 0\n" \
  "v_mov_b32 v75, 0\n" \
  "s_add_u32 s68, s68, 1\n" \
  "s_min_u32 s74, s68, 63\n" \
  "v_readlane_b32 s45, v4, s74\n" \
  "s_waitcnt vmcnt(0)\n" \
  "s_barrier\n" \
  "LMc1_%=:\n" \
  "ds_read_b128 v[16:19], v1 offset:0\n" \
  "ds_read_b128 v[20:23], v1 offset:1024\n" \
  "ds_read_b128 v[24:27], v1 offset:2048\n" \
  "ds_read_b128 v[28:31], v1 offset:3072\n" \
  "ds_read_b128 v[32:35], v1 offset:4096\n" \
  "ds_read_b128 v[36:39], v1 offset:5120\n" \
  "ds_read_b128 v[40:43], v1 offset:6144\n" \
  "ds_read_b128 v[44:47], v1 offset:7168\n" \
  "ds_read_b128 v[48:51], v1 offset:8192\n" \
  "ds_read_b128 v[52:55], v1 offset:9216\n" \
  "s_waitcnt lgkmcnt(9)\n" \
  "v_smfmac_f32_16x16x64_bf16 v[56:59], v[16:19], a[0:7], v0\n" \
  "s_waitcnt lgkmcnt(8)\n" \
  "v_smfmac_f32_16x16x64_bf16 v[56:59], v[20:23], a[80:87], v0\n" \
  "s_waitcnt lgkmcnt(7)\n" \
  "v_smfmac_f32_16x16x64_bf16 v[56:59], v[24:27], a[160:167], v0\n" \
  "s_waitcnt lgkmcnt(6)\n" \
  "v_smfmac_f32_16x16x64_bf16 v[56:59], v[28:31], a[240:247], v0\n" \
  "s_waitcnt lgkmcnt(5)\n" \
  "v_smfmac_f32_16x16x64_bf16 v[56:59], v[32:35], v[176:183], v0\n" \
  "s_waitcnt lgkmcnt(4)\n" \
  "v_smfmac_f32_16x16x64_bf16 v[56:59], v[36:39], a[40:47], v0\n" \
  "s_waitcnt lgkmcnt(3)\n" \
  "v_smfmac_f32_16x16x64_bf16 v[56:59], v[40:43], a[120:127], v0\n" \
  "s_waitcnt lgkmcnt(2)\n" \
  "v_smfmac_f32_16x16x64_bf16 v[56:59], v[44:47], a[200:207], v0\n" \
  "s_waitcnt lgkmcnt(1)\n" \
  "v_smfmac_f32_16x16x64_bf16 v[56:59], v[48:51], v[136:143], v0\n" \
  "s_waitcnt lgkmcnt(0)\n" \
  "v_smfmac_f32_16x16x64_bf16 v[56:59], v[52:55], v[216:223], v0\n" \
  "v_smfmac_f32_16x16x64_bf16 v[60:63], v[16:19], a[8:15], v0\n" \
  "v_smfmac_f32_16x16x64_bf16 v[64:67], v[16:19], a[16:23], v0\n" \
  "v_cmp_u_f32 vcc, v56, v57\n" \
  "v_cmp_u_f32 s[82:83], v58, v59\n" \
  "s_nop 1\n" \
  "s_or_b64 vcc, vcc, s[82:83]\n" \
  "s_cbranch_scc1 LMFc1_%=\n" \
  "LMOKc1_%=:\n" \
  "v_smfmac_f32_16x16x64_bf16 v[68:71], v[16:19], a[24:31], v0\n" \
  "v_smfmac_f32_16x16x64_bf16 v[72:75], v[16:19], a[32:39], v0\n" \
  "v_smfmac_f32_16x16x64_bf16 v[60:63], v[20:23], a[88:95], v0\n" \
  "v_smfmac_f32_16x16x64_bf16 v[64:67], v[20:23], a[96:103], v0\n" \
  "v_smfmac_f32_16x16x64_bf16 v[68:71], v[20:23], a[104:111], v0\n" \
  "v_smfmac_f32_16x16x64_bf16 v[72:75], v[20:23], a[112:119], v0\n" \
  "v_smfmac_f32_16x16x64_bf16 v[60:63], v[24:27], a[168:175], v0\n" \
  "v_smfmac_f32_16x16x64_bf16 v[64:67], v[24:27], a[176:183], v0\n" \
  "v_smfmac_f32_16x16x64_bf16 v[68:71], v[24:27], a[184:191], v0\n" \
  "v_smfmac_f32_16x16x64_bf16 v[72:75], v[24:27], a[192:199], v0\n" \
  "v_smfmac_f32_16x16x64_bf16 v[60:63], v[28:31], a[248:255], v0\n" \
  "v_smfmac_f32_16x16x64_bf16 v[64:67], v[28:31], v[112:119], v0\n" \
  "v_smfmac_f32_16x16x64_bf16 v[68:71], v[28:31], v[120:127], v0\n" \
  "v_smfmac_f32_16x16x64_bf16 v[72:75], v[28:31], v[128:135], v0\n" \
  "v_smfmac_f32_16x16x64_bf16 v[60:63], v[32:35], v[184:191], v0\n" \
  "v_smfmac_f32_16x16x64_bf16 v[64:67], v[32:35], v[192:199], v0\n" \
  "v_smfmac_f32_16x16x64_bf16 v[68:71], v[32:35], v[200:207], v0\n" \
  "v_smfmac_f32_16x16x64_bf16 v[72:75], v[32:35], v[208:215], v0\n" \
  "v_smfmac_f32_16x16x64_bf16 v[60:63], v[36:39], a[48:55], v0\n" \
  "v_smfmac_f32_16x16x64_bf16 v[64:67], v[36:39], a[56:63], v0\n" \
  "v_smfmac_f32_16x16x64_bf16 v[68:71], v[36:39], a[64:71], v0\n" \
  "v_smfmac_f32_16x16x64_bf16 v[72:75], v[36:39], a[72:79], v0\n" \
  "v_smfmac_f32_16x16x64_bf16 v[60:63], v[40:43], a[128:135], v0\n" \
  "v_smfmac_f32_16x16x64_bf16 v[64:67], v[40:43], a[136:143], v0\n" \
  "v_smfmac_f32_16x16x64_bf16 v[68:71], v[40:43], a[144:151], v0\n" \
  "v_smfmac_f32_16x16x64_bf16 v[72:75], v[40:43], a[152:159], v0\n" \
  "v_smfmac_f32_16x16x64_bf16 v[60:63], v[44:47], a[208:215], v0\n" \
  "v_smfmac_f32_16x16x64_bf16 v[64:67], v[44:47], a[216:223], v0\n" \
  "v_smfmac_f32_16x16x64_bf16 v[68:71], v[44:47], a[224:231], v0\n" \
  "v_smfmac_f32_16x16x64_bf16 v[72:75], v[44:47], a[232:239], v0\n" \
  "v_smfmac_f32_16x16x64_bf16 v[60:63], v[48:51], v[144:151], v0\n" \
  "v_smfmac_f32_16x16x64_bf16 v[64:67], v[48:51], v[152:159], v0\n" \
  "v_smfmac_f32_16x16x64_bf16 v[68:71], v[48:51], v[160:167], v0\n" \
  "v_smfmac_f32_16x16x64_bf16 v[72:75], v[48:51], v[168:175], v0\n" \
  "v_smfmac_f32_16x16x64_bf16 v[60:63], v[52:55], v[224:231], v0\n" \
  "v_smfmac_f32_16x16x64_bf16 v[64:67], v[52:55], v[232:239], v0\n" \
  "v_smfmac_f32_16x16x64_bf16 v[68:71], v[52:55], v[240:247], v0\n" \
  "v_smfmac_f32_16x16x64_bf16 v[72:75], v[52:55], v[248:255], v0\n" \
  "v_permlane32_swap_b32 v56, v60\n" \
  "v_permlane32_swap_b32 v57, v61\n" \
  "v_permlane32_swap_b32 v58, v62\n" \
  "v_permlane32_swap_b32 v59, v63\n" \
  "v_pk_add_f32 v[56:57], v[56:57], v[60:61]\n" \
  "v_pk_add_f32 v[58:59], v[58:59], v[62:63]\n" \
  "v_cvt_pk_bf16_f32 v56, v56, v57\n" \
  "v_cvt_pk_bf16_f32 v57, v58, v59\n" \
  "ds_write_b64 v2, v[56:57] offset:1280\n" \
  "v_permlane32_swap_b32 v64, v68\n" \
  "v_permlane32_swap_b32 v65, v69\n" \
  "v_permlane32_swap_b32 v66, v70\n" \
  "v_permlane32_swap_b32 v67, v71\n" \
  "v_pk_add_f32 v[64:65], v[64:65], v[68:69]\n" \
  "v_pk_add_f32 v[66:67], v[66:67], v[70:71]\n" \
  "v_cvt_pk_bf16_f32 v64, v64, v65\n" \
  "v_cvt_pk_bf16_f32 v65, v66, v67\n" \
  "ds_write_b64 v2, v[64:65] offset:1792\n" \
  "v_permlane32_swap_b32 v72, v74\n" \
  "v_permlane32_swap_b32 v73, v75\n" \
  "v_pk_add_f32 v[72:73], v[72:73], v[74:75]\n" \
  "v_cvt_pk_bf16_f32 v72, v72, v73\n" \
  "ds_write_b32 v3, v72 offset:2304\n" \
  "s_waitcnt lgkmcnt(0)\n" \
  "s_barrier\n" \
  "s_mov_b32 s44, s45\n" \
  "s_barrier\n" \
  "s_cmp_eq_u32 s68, 64\n" \
  "s_cbranch_scc0 LOOPc1_%=\n" \
  "s_branch LDONE_%=\n" \
  "LMFc1_%=:\n" \
  "v_max_u32 v92, v16, v17\n" \
  "v_max3_u32 v92, v92, v18, v19\n" \
  "v_max3_u32 v92, v92, v20, v21\n" \
  "v_max3_u32 v92, v92, v22, v23\n" \
  "v_max3_u32 v92, v92, v24, v25\n" \
  "v_max3_u32 v92, v92, v26, v27\n" \
  "v_max3_u32 v92, v92, v28, v29\n" \
  "v_max3_u32 v92, v92, v30, v31\n" \
  "v_max3_u32 v92, v92, v32, v33\n" \
  "v_max3_u32 v92, v92, v34, v35\n" \
  "v_max3_u32 v92, v92, v36, v37\n" \
  "v_max3_u32 v92, v92, v38, v39\n" \
  "v_max3_u32 v92, v92, v40, v41\n" \
  "v_max3_u32 v92, v92, v42, v43\n" \
  "v_max3_u32 v92, v92, v44, v45\n" \
  "v_max3_u32 v92, v92, v46, v47\n" \
  "v_max3_u32 v92, v92, v48, v49\n" \
  "v_max3_u32 v92, v92, v50, v51\n" \
  "v_max3_u32 v92, v92, v52, v53\n" \
  "v_max3_u32 v92, v92, v54, v55\n" \
  "v_cmp_eq_u32 vcc, -1, v92\n" \
  "s_nop 3\n" \
  "s_cbranch_vccz LMOKc1_%=\n" \
  "s_cmp_gt_u32 s57, 20000\n" \
  "s_cbranch_scc1 LMOKc1_%=\n" \
  "s_add_u32 s57, s57, 1\n" \
  "s_barrier\n" \
  "buffer_load_dwordx4 v1, s[36:39], s75 offen offset:0 sc1 lds\n" \
  "buffer_load_dwordx4 v1, s[36:39], s75 offen offset:1024 sc1 lds\n" \
  "buffer_load_dwordx4 v1, s[36:39], s75 offen offset:2048 sc1 lds\n" \
  "buffer_load_dwordx4 v1, s[36:39], s75 offen offset:3072 sc1 lds\n" \
  "v_mov_b32 v56, 0\n" \
  "v_mov_b32 v57, 0\n" \
  "v_mov_b32 v58, 0\n" \
  "v_mov_b32 v59, 0\n" \
  "v_mov_b32 v60, 0\n" \
  "v_mov_b32 v61, 0\n" \
  "v_mov_b32 v62, 0\n" \
  "v_mov_b32 v63, 0\n" \
  "v_mov_b32 v64, 0\n" \
  "v_mov_b32 v65, 0\n" \
  "v_mov_b32 v66, 0\n" \
  "v_mov_b32 v67, 0\n" \
  "v_mov_b32 v68, 0\n" \
  "v_mov_b32 v69, 0\n" \
  "v_mov_b32 v70, 0\n" \
  "v_mov_b32 v71, 0\n" \
  "v_mov_b32 v72, 0\n" \
  "v_mov_b32 v73, 0\n" \
  "v_mov_b32 v74, 0\n" \
  "v_mov_b32 v75, 0\n" \
  "s_waitcnt vmcnt(0)\n" \
  "s_barrier\n" \
  "s_branch LMc1_%=\n" \
  "LSTARTc2_%=:\n" \
  "s_mov_b32 s57, 0\n" \
  "v_readlane_b32 s44, v4, 0\n" \
  "s_nop 3\n" \
  "LOOPc2_%=:\n" \
  "s_mov_b32 m0, 8192\n" \
  "s_add_u32 s75, s44, 16384\n" \
  "s_barrier\n" \
  "buffer_load_dwordx4 v1, s[36:39], s75 offen offset:0 sc1 lds\n" \
  "buffer_load_dwordx4 v1, s[36:39], s75 offen offset:1024 sc1 lds\n" \
  "v_mov_b32 v56, 0\n" \
  "v_mov_b32 v57, 0\n" \
  "v_mov_b32 v58, 0\n" \
  "v_mov_b32 v59, 0\n" \
  "v_mov_b32 v60, 0\n" \
  "v_mov_b32 v61, 0\n" \
  "v_mov_b32 v62, 0\n" \
  "v_mov_b32 v63, 0\n" \
  "v_mov_b32 v64, 0\n" \
  "v_mov_b32 v65, 0\n" \
  "v_mov_b32 v66, 0\n" \
  "v_mov_b32 v67, 0\n" \
  "v_mov_b32 v68, 0\n" \
  "v_mov_b32 v69, 0\n" \
  "v_mov_b32 v70, 0\n" \
  "v_mov_b32 v71, 0\n" \
  "v_mov_b32 v72, 0\n" \
  "v_mov_b32 v73, 0\n" \
  "v_mov_b32 v74, 0\n" \
  "v_mov_b32 v75, 0\n" \
  "s_add_u32 s68, s68, 1\n" \
  "s_min_u32 s74, s68, 63\n" \
  "v_readlane_b32 s45, v4, s74\n" \
  "s_waitcnt vmcnt(0)\n" \
  "s_barrier\n" \
  "LMc2_%=:\n" \
  "ds_read_b128 v[16:19], v1 offset:0\n" \
  "ds_read_b128 v[20:23], v1 offset:1024\n" \
  "ds_read_b128 v[24:27], v1 offset:2048\n" \
  "ds_read_b128 v[28:31], v1 offset:3072\n" \
  "ds_read_b128 v[32:35], v1 offset:4096\n" \
  "ds_read_b128 v[36:39], v1 offset:5120\n" \
  "ds_read_b128 v[40:43], v1 offset:6144\n" \
  "ds_read_b128 v[44:47], v1 offset:7168\n" \
  "ds_read_b128 v[48:51], v1 offset:8192\n" \
  "ds_read_b128 v[52:55], v1 offset:9216\n" \
  "s_waitcnt lgkmcnt(9)\n" \
  "v_smfmac_f32_16x16x64_bf16 v[56:59], v[16:19], a[0:7], v0\n" \
  "s_waitcnt lgkmcnt(8)\n" \
  "v_smfmac_f32_16x16x64_bf16 v[56:59], v[20:23], a[80:87], v0\n" \
  "s_waitcnt lgkmcnt(7)\n" \
  "v_smfmac_f32_16x16x64_bf16 v[56:59], v[24:27], a[160:167], v0\n" \
  "s_waitcnt lgkmcnt(6)\n" \
  "v_smfmac_f32_16x16x64_bf16 v[56:59], v[28:31], a[240:247], v0\n" \
  "s_waitcnt lgkmcnt(5)\n" \
  "v_smfmac_f32_16x16x64_bf16 v[56:59], v[32:35], v[176:183], v0\n" \
  "s_waitcnt lgkmcnt(4)\n" \
  "v_smfmac_f32_16x16x64_bf16 v[56:59], v[36:39], a[40:47], v0\n" \
  "s_waitcnt lgkmcnt(3)\n" \
  "v_smfmac_f32_16x16x64_bf16 v[56:59], v[40:43], a[120:127], v0\n" \
  "s_waitcnt lgkmcnt(2)\n" \
  "v_smfmac_f32_16x16x64_bf16 v[56:59], v[44:47], a[200:207], v0\n" \
  "s_waitcnt lgkmcnt(1)\n" \
  "v_smfmac_f32_16x16x64_bf16 v[56:59], v[48:51], v[136:143], v0\n" \
  "s_waitcnt lgkmcnt(0)\n" \
  "v_smfmac_f32_16x16x64_bf16 v[56:59], v[52:55], v[216:223], v0\n" \
  "v_smfmac_f32_16x16x64_bf16 v[60:63], v[16:19], a[8:15], v0\n" \
  "v_smfmac_f32_16x16x64_bf16 v[64:67], v[16:19], a[16:23], v0\n" \
  "v_cmp_u_f32 vcc, v56, v57\n" \
  "v_cmp_u_f32 s[82:83], v58, v59\n" \
  "s_nop 1\n" \
  "s_or_b64 vcc, vcc, s[82:83]\n" \
  "s_cbranch_scc1 LMFc2_%=\n" \
  "LMOKc2_%=:\n" \
  "v_smfmac_f32_16x16x64_bf16 v[68:71], v[16:19], a[24:31], v0\n" \
  "v_smfmac_f32_16x16x64_bf16 v[72:75], v[16:19], a[32:39], v0\n" \
  "v_smfmac_f32_16x16x64_bf16 v[60:63], v[20:23], a[88:95], v0\n" \
  "v_smfmac_f32_16x16x64_bf16 v[64:67], v[20:23], a[96:103], v0\n" \
  "v_smfmac_f32_16x16x64_bf16 v[68:71], v[20:23], a[104:111], v0\n" \
  "v_smfmac_f32_16x16x64_bf16 v[72:75], v[20:23], a[112:119], v0\n" \
  "v_smfmac_f32_16x16x64_bf16 v[60:63], v[24:27], a[168:175], v0\n" \
  "v_smfmac_f32_16x16x64_bf16 v[64:67], v[24:27], a[176:183], v0\n" \
  "v_smfmac_f32_16x16x64_bf16 v[68:71], v[24:27], a[184:191], v0\n" \
  "v_smfmac_f32_16x16x64_bf16 v[72:75], v[24:27], a[192:199], v0\n" \
  "v_smfmac_f32_16x16x64_bf16 v[60:63], v[28:31], a[248:255], v0\n" \
  "v_smfmac_f32_16x16x64_bf16 v[64:67], v[28:31], v[112:119], v0\n" \
  "v_smfmac_f32_16x16x64_bf16 v[68:71], v[28:31], v[120:127], v0\n" \
  "v_smfmac_f32_16x16x64_bf16 v[72:75], v[28:31], v[128:135], v0\n" \
  "v_smfmac_f32_16x16x64_bf16 v[60:63], v[32:35], v[184:191], v0\n" \
  "v_smfmac_f32_16x16x64_bf16 v[64:67], v[32:35], v[192:199], v0\n" \
  "v_smfmac_f32_16x16x64_bf16 v[68:71], v[32:35], v[200:207], v0\n" \
  "v_smfmac_f32_16x16x64_bf16 v[72:75], v[32:35], v[208:215], v0\n" \
  "v_smfmac_f32_16x16x64_bf16 v[60:63], v[36:39], a[48:55], v0\n" \
  "v_smfmac_f32_16x16x64_bf16 v[64:67], v[36:39], a[56:63], v0\n" \
  "v_smfmac_f32_16x16x64_bf16 v[68:71], v[36:39], a[64:71], v0\n" \
  "v_smfmac_f32_16x16x64_bf16 v[72:75], v[36:39], a[72:79], v0\n" \
  "v_smfmac_f32_16x16x64_bf16 v[60:63], v[40:43], a[128:135], v0\n" \
  "v_smfmac_f32_16x16x64_bf16 v[64:67], v[40:43], a[136:143], v0\n" \
  "v_smfmac_f32_16x16x64_bf16 v[68:71], v[40:43], a[144:151], v0\n" \
  "v_smfmac_f32_16x16x64_bf16 v[72:75], v[40:43], a[152:159], v0\n" \
  "v_smfmac_f32_16x16x64_bf16 v[60:63], v[44:47], a[208:215], v0\n" \
  "v_smfmac_f32_16x16x64_bf16 v[64:67], v[44:47], a[216:223], v0\n" \
  "v_smfmac_f32_16x16x64_bf16 v[68:71], v[44:47], a[224:231], v0\n" \
  "v_smfmac_f32_16x16x64_bf16 v[72:75], v[44:47], a[232:239], v0\n" \
  "v_smfmac_f32_16x16x64_bf16 v[60:63], v[48:51], v[144:151], v0\n" \
  "v_smfmac_f32_16x16x64_bf16 v[64:67], v[48:51], v[152:159], v0\n" \
  "v_smfmac_f32_16x16x64_bf16 v[68:71], v[48:51], v[160:167], v0\n" \
  "v_smfmac_f32_16x16x64_bf16 v[72:75], v[48:51], v[168:175], v0\n" \
  "v_smfmac_f32_16x16x64_bf16 v[60:63], v[52:55], v[224:231], v0\n" \
  "v_smfmac_f32_16x16x64_bf16 v[64:67], v[52:55], v[232:239], v0\n" \
  "v_smfmac_f32_16x16x64_bf16 v[68:71], v[52:55], v[240:247], v0\n" \
  "v_smfmac_f32_16x16x64_bf16 v[72:75], v[52:55], v[248:255], v0\n" \
  "v_permlane32_swap_b32 v56, v60\n" \
  "v_permlane32_swap_b32 v57, v61\n" \
  "v_permlane32_swap_b32 v58, v62\n" \
  "v_permlane32_swap_b32 v59, v63\n" \
  "v_pk_add_f32 v[56:57], v[56:57], v[60:61]\n" \
  "v_pk_add_f32 v[58:59], v[58:59], v[62:63]\n" \
  "v_cvt_pk_bf16_f32 v56, v56, v57\n" \
  "v_cvt_pk_bf16_f32 v57, v58, v59\n" \
  "ds_write_b64 v2, v[56:57] offset:2560\n" \
  "v_permlane32_swap_b32 v64, v68\n" \
  "v_permlane32_swap_b32 v65, v69\n" \
  "v_permlane32_swap_b32 v66, v70\n" \
  "v_permlane32_swap_b32 v67, v71\n" \
  "v_pk_add_f32 v[64:65], v[64:65], v[68:69]\n" \
  "v_pk_add_f32 v[66:67], v[66:67], v[70:71]\n" \
  "v_cvt_pk_bf16_f32 v64, v64, v65\n" \
  "v_cvt_pk_bf16_f32 v65, v66, v67\n" \
  "ds_write_b64 v2, v[64:65] offset:3072\n" \
  "v_permlane32_swap_b32 v72, v74\n" \
  "v_permlane32_swap_b32 v73, v75\n" \
  "v_pk_add_f32 v[72:73], v[72:73], v[74:75]\n" \
  "v_cvt_pk_bf16_f32 v72, v72, v73\n" \
  "ds_write_b32 v3, v72 offset:3584\n" \
  "s_waitcnt lgkmcnt(0)\n" \
  "s_barrier\n" \
  "s_mov_b32 s44, s45\n" \
  "s_barrier\n" \
  "s_cmp_eq_u32 s68, 64\n" \
  "s_cbranch_scc0 LOOPc2_%=\n" \
  "s_branch LDONE_%=\n" \
  "LMFc2_%=:\n" \
  "v_max_u32 v92, v16, v17\n" \
  "v_max3_u32 v92, v92, v18, v19\n" \
  "v_max3_u32 v92, v92, v20, v21\n" \
  "v_max3_u32 v92, v92, v22, v23\n" \
  "v_max3_u32 v92, v92, v24, v25\n" \
  "v_max3_u32 v92, v92, v26, v27\n" \
  "v_max3_u32 v92, v92, v28, v29\n" \
  "v_max3_u32 v92, v92, v30, v31\n" \
  "v_max3_u32 v92, v92, v32, v33\n" \
  "v_max3_u32 v92, v92, v34, v35\n" \
  "v_max3_u32 v92, v92, v36, v37\n" \
  "v_max3_u32 v92, v92, v38, v39\n" \
  "v_max3_u32 v92, v92, v40, v41\n" \
  "v_max3_u32 v92, v92, v42, v43\n" \
  "v_max3_u32 v92, v92, v44, v45\n" \
  "v_max3_u32 v92, v92, v46, v47\n" \
  "v_max3_u32 v92, v92, v48, v49\n" \
  "v_max3_u32 v92, v92, v50, v51\n" \
  "v_max3_u32 v92, v92, v52, v53\n" \
  "v_max3_u32 v92, v92, v54, v55\n" \
  "v_cmp_eq_u32 vcc, -1, v92\n" \
  "s_nop 3\n" \
  "s_cbranch_vccz LMOKc2_%=\n" \
  "s_cmp_gt_u32 s57, 20000\n" \
  "s_cbranch_scc1 LMOKc2_%=\n" \
  "s_add_u32 s57, s57, 1\n" \
  "s_barrier\n" \
  "buffer_load_dwordx4 v1, s[36:39], s75 offen offset:0 sc1 lds\n" \
  "buffer_load_dwordx4 v1, s[36:39], s75 offen offset:1024 sc1 lds\n" \
  "v_mov_b32 v56, 0\n" \
  "v_mov_b32 v57, 0\n" \
  "v_mov_b32 v58, 0\n" \
  "v_mov_b32 v59, 0\n" \
  "v_mov_b32 v60, 0\n" \
  "v_mov_b32 v61, 0\n" \
  "v_mov_b32 v62, 0\n" \
  "v_mov_b32 v63, 0\n" \
  "v_mov_b32 v64, 0\n" \
  "v_mov_b32 v65, 0\n" \
  "v_mov_b32 v66, 0\n" \
  "v_mov_b32 v67, 0\n" \
  "v_mov_b32 v68, 0\n" \
  "v_mov_b32 v69, 0\n" \
  "v_mov_b32 v70, 0\n" \
  "v_mov_b32 v71, 0\n" \
  "v_mov_b32 v72, 0\n" \
  "v_mov_b32 v73, 0\n" \
  "v_mov_b32 v74, 0\n" \
  "v_mov_b32 v75, 0\n" \
  "s_waitcnt vmcnt(0)\n" \
  "s_barrier\n" \
  "s_branch LMc2_%=\n" \
  "LSTARTc3_%=:\n" \
  "v_mov_b32 v96, v16\n" \
  "v_mov_b32 v97, v17\n" \
  "v_mov_b32 v100, v18\n" \
  "s_memrealtime s[76:77]\n" \
  "s_memtime s[78:79]\n" \
  "s_waitcnt lgkmcnt(0)\n" \
  "s_mov_b32 s88, s76\n" \
  "s_mov_b32 s89, s78\n" \
  "s_mov_b32 s56, s38\n" \
  "s_mov_b32 s38, 0\n" \
  "s_mov_b32 s73, 20001\n" \
  "s_mov_b32 s57, 20001\n" \
  "s_getpc_b64 s[82:83]\n" \
  "LPCEc3_%=:\n" \
  "s_add_u32 s84, s82, LEEc3_%=-LPCEc3_%=+24\n" \
  "s_addc_u32 s85, s83, 0\n" \
  "s_getpc_b64 s[82:83]\n" \
  "LPCLc3_%=:\n" \
  "s_add_u32 s90, s82, LELc3_%=-LPCLc3_%=+24\n" \
  "s_addc_u32 s91, s83, 0\n" \
  "v_mov_b32 v92, 0\n" \
  "s_memrealtime s[76:77]\n" \
  "s_memtime s[78:79]\n" \
  "s_waitcnt lgkmcnt(0)\n" \
  "s_lshl_b32 s48, s76, 2\n" \
  "v_readlane_b32 s72, v92, 0\n" \
  "s_nop 1\n" \
  "s_sub_u32 s74, s72, s48\n" \
  "s_add_u32 s74, s74, 40000\n" \
  "s_cmp_gt_u32 s74, 80000\n" \
  "s_cselect_b32 s72, s48, s72\n" \
  "v_readlane_b32 s94, v92, 1\n" \
  "s_nop 1\n" \
  "s_sub_u32 s74, s94, s48\n" \
  "s_add_u32 s74, s74, 40000\n" \
  "s_cmp_gt_u32 s74, 80000\n" \
  "s_cselect_b32 s94, s48, s94\n" \
  "v_readlane_b32 s95, v92, 2\n" \
  "s_nop 1\n" \
  "s_sub_u32 s74, s95, s48\n" \
  "s_add_u32 s74, s74, 40000\n" \
  "s_cmp_gt_u32 s74, 80000\n" \
  "s_cselect_b32 s95, s48, s95\n" \
  "s_sub_u32 s74, s78, s89\n" \
  "s_sub_u32 s75, s76, s88\n" \
  "v_cvt_f32_u32 v92, s74\n" \
  "v_cvt_f32_u32 v93, s75\n" \
  "v_rcp_f32 v93, v93\n" \
  "v_mul_f32 v92, 0x43820000, v92\n" \
  "v_mul_f32 v92, v92, v93\n" \
  "v_cvt_u32_f32 v92, v92\n" \
  "s_nop 0\n" \
  "v_readfirstlane_b32 s74, v92\n" \
  "s_min_u32 s74, s74, 6400\n" \
  "s_cmp_gt_u32 s75, 150\n" \
  "s_cselect_b32 s50, s74, s50\n" \
  "s_cselect_b32 s88, s76, s88\n" \
  "s_cselect_b32 s89, s78, s89\n" \
  "s_add_u32 s49, s78, 0x1000000\n" \
  "s_mul_i32 s86, s54, 6\n" \
  "s_mov_b32 s87, -200\n" \
  "s_mov_b32 s51, 0\n" \
  "s_mov_b32 s96, 0\n" \
  "s_mul_i32 s74, s51, s54\n" \
  "s_add_u32 s74, s74, s72\n" \
  "s_sub_i32 s75, s74, s48\n" \
  "s_mov_b32 s48, s74\n" \
  "s_mul_i32 s75, s75, s50\n" \
  "s_ashr_i32 s75, s75, 10\n" \
  "s_add_u32 s49, s49, s75\n" \
  "s_mov_b32 s87, 0x80000000\n" \
  "v_accvgpr_write_b32 a88, -1\n" \
  "s_branch LOOPc3_%=\n" \
  "LPASSc3_%=:\n" \
  "s_waitcnt vmcnt(0)\n" \
  "s_mov_b32 s38, s56\n" \
  "s_mov_b32 s68, 0\n" \
  "s_mov_b32 s73, 0\n" \
  "s_mov_b32 s58, 0\n" \
  "s_mov_b32 s59, 0\n" \
  "LQU4c3_%=:\n" \
  "buffer_load_dword v92, v14, s[36:39], 0 offen offset:1536 sc1\n" \
  "s_waitcnt vmcnt(0)\n" \
  "v_cmp_ne_u32 vcc, 1, v92\n" \
  "s_nop 3\n" \
  "s_cbranch_vccz LRU4c3_%=\n" \
  "s_add_u32 s73, s73, 1\n" \
  "s_cmp_gt_u32 s73, 20000\n" \
  "s_cbranch_scc1 LRU4c3_%=\n" \
  "s_sleep 1\n" \
  "s_branch LQU4c3_%=\n" \
  "LRU4c3_%=:\n" \
  "v_readlane_b32 s46, v96, 6\n" \
  "s_nop 4\n" \
  "buffer_load_dwordx4 a[88:91], v100, s[36:39], s46 offen sc1\n" \
  "v_readlane_b32 s46, v96, 8\n" \
  "s_nop 4\n" \
  "buffer_load_dwordx4 a[104:107], v100, s[36:39], s46 offen sc1\n" \
  "v_readlane_b32 s46, v96, 7\n" \
  "s_nop 4\n" \
  "buffer_load_dwordx4 a[92:95], v100, s[36:39], s46 offen sc1\n" \
  "v_readlane_b32 s46, v96, 9\n" \
  "s_nop 4\n" \
  "buffer_load_dwordx4 a[108:111], v100, s[36:39], s46 offen sc1\n" \
  "v_readlane_b32 s46, v96, 16\n" \
  "s_nop 4\n" \
  "buffer_load_dwordx4 a[96:99], v100, s[36:39], s46 offen sc1\n" \
  "v_readlane_b32 s46, v96, 18\n" \
  "s_nop 4\n" \
  "buffer_load_dwordx4 a[112:115], v100, s[36:39], s46 offen sc1\n" \
  "v_readlane_b32 s46, v96, 17\n" \
  "s_nop 4\n" \
  "buffer_load_dwordx4 a[100:103], v100, s[36:39], s46 offen sc1\n" \
  "v_readlane_b32 s46, v96, 19\n" \
  "s_nop 4\n" \
  "buffer_load_dwordx4 a[116:119], v100, s[36:39], s46 offen sc1\n" \
  "LQAc3_%=:\n" \
  "buffer_load_dword v92, v8, s[36:39], 0 offen offset:0 sc1\n" \
  "s_waitcnt vmcnt(0)\n" \
  "v_cmp_eq_u32 vcc, 0, v92\n" \
  "s_nop 3\n" \
  "s_cbranch_vccz LRAc3_%=\n" \
  "s_add_u32 s73, s73, 1\n" \
  "s_cmp_gt_u32 s73, 20000\n" \
  "s_cbranch_scc1 LRAc3_%=\n" \
  "s_sleep 2\n" \
  "s_branch LQAc3_%=\n" \
  "LRAc3_%=:\n" \
  "s_memrealtime s[76:77]\n" \
  "s_memtime s[78:79]\n" \
  "s_waitcnt lgkmcnt(0)\n" \
  "s_lshl_b32 s48, s76, 2\n" \
  "v_readlane_b32 s72, v92, 0\n" \
  "s_nop 1\n" \
  "s_sub_u32 s74, s72, s48\n" \
  "s_add_u32 s74, s74, 40000\n" \
  "s_cmp_gt_u32 s74, 80000\n" \
  "s_cselect_b32 s72, s48, s72\n" \
  "v_readlane_b32 s94, v92, 1\n" \
  "s_nop 1\n" \
  "s_sub_u32 s74, s94, s48\n" \
  "s_add_u32 s74, s74, 40000\n" \
  "s_cmp_gt_u32 s74, 80000\n" \
  "s_cselect_b32 s94, s48, s94\n" \
  "v_readlane_b32 s95, v92, 2\n" \
  "s_nop 1\n" \
  "s_sub_u32 s74, s95, s48\n" \
  "s_add_u32 s74, s74, 40000\n" \
  "s_cmp_gt_u32 s74, 80000\n" \
  "s_cselect_b32 s95, s48, s95\n" \
  "s_sub_u32 s74, s78, s89\n" \
  "s_sub_u32 s75, s76, s88\n" \
  "v_cvt_f32_u32 v92, s74\n" \
  "v_cvt_f32_u32 v93, s75\n" \
  "v_rcp_f32 v93, v93\n" \
  "v_mul_f32 v92, 0x43820000, v92\n" \
  "v_mul_f32 v92, v92, v93\n" \
  "v_cvt_u32_f32 v92, v92\n" \
  "s_nop 0\n" \
  "v_readfirstlane_b32 s74, v92\n" \
  "s_min_u32 s74, s74, 6400\n" \
  "s_cmp_gt_u32 s75, 150\n" \
  "s_cselect_b32 s50, s74, s50\n" \
  "s_cselect_b32 s88, s76, s88\n" \
  "s_cselect_b32 s89, s78, s89\n" \
  "s_add_u32 s49, s78, 0x1000000\n" \
  "s_mul_i32 s86, s54, 6\n" \
  "s_mov_b32 s87, -200\n" \
  "s_mov_b32 s51, 0\n" \
  "s_mov_b32 s96, 0\n" \
  "s_mov_b32 s57, 0\n" \
  "v_readlane_b32 s44, v4, 0\n" \
  "s_nop 3\n" \
  "LOOPc3_%=:\n" \
  "LZWc3_%=:\n" \
  "s_memrealtime s[76:77]\n" \
  "s_memtime s[78:79]\n" \
  "s_waitcnt lgkmcnt(0)\n" \
  "s_lshl_b32 s75, s76, 2\n" \
  "s_sub_i32 s75, s48, s75\n" \
  "s_mul_i32 s75, s75, s50\n" \
  "s_ashr_i32 s75, s75, 10\n" \
  "s_sub_i32 s74, s49, s78\n" \
  "s_min_i32 s74, s74, s75\n" \
  "s_sub_i32 s75, s75, 92\n" \
  "s_max_i32 s74, s74, s75\n" \
  "s_add_u32 s49, s74, s78\n" \
  "s_cmp_gt_i32 s74, 60\n" \
  "s_cbranch_scc1 LZWc3_%=\n" \
  "s_barrier\n" \
  "v_mov_b32 v56, 0\n" \
  "v_mov_b32 v57, 0\n" \
  "v_mov_b32 v58, 0\n" \
  "v_mov_b32 v59, 0\n" \
  "v_readlane_b32 s74, v9, s68\n" \
  "s_nop 0\n" \
  "s_add_u32 s46, s74, 0\n" \
  "s_add_u32 s47, s74, 4096\n" \
  "s_add_u32 s69, s74, 16384\n" \
  "s_add_u32 s70, s74, 20480\n" \
  "s_add_u32 s68, s68, 1\n" \
  "s_min_u32 s74, s68, 63\n" \
  "v_readlane_b32 s45, v4, s74\n" \
  "s_nop 15\n" \
  "buffer_load_dwordx4 a[8:11], v5, s[36:39], s46 offen offset:0 sc1\n" \
  "buffer_load_dwordx4 a[12:15], v5, s[36:39], s46 offen offset:1024 sc1\n" \
  "buffer_load_dwordx4 a[16:19], v5, s[36:39], s46 offen offset:2048 sc1\n" \
  "buffer_load_dwordx4 a[20:23], v5, s[36:39], s46 offen offset:3072 sc1\n" \
  "buffer_load_dwordx4 a[24:27], v5, s[36:39], s47 offen offset:0 sc1\n" \
  "buffer_load_dwordx4 a[28:31], v5, s[36:39], s47 offen offset:1024 sc1\n" \
  "buffer_load_dwordx4 a[32:35], v5, s[36:39], s47 offen offset:2048 sc1\n" \
  "buffer_load_dwordx4 a[36:39], v5, s[36:39], s47 offen offset:3072 sc1\n" \
  "buffer_load_dwordx4 a[48:51], v5, s[36:39], s69 offen offset:0 sc1\n" \
  "buffer_load_dwordx4 a[52:55], v5, s[36:39], s69 offen offset:1024 sc1\n" \
  "buffer_load_dwordx4 a[56:59], v5, s[36:39], s69 offen offset:2048 sc1\n" \
  "buffer_load_dwordx4 a[60:63], v5, s[36:39], s69 offen offset:3072 sc1\n" \
  "buffer_load_dwordx4 a[64:67], v5, s[36:39], s70 offen offset:0 sc1\n" \
  "buffer_load_dwordx4 a[68:71], v5, s[36:39], s70 offen offset:1024 sc1\n" \
  "buffer_load_dwordx4 a[72:75], v5, s[36:39], s70 offen offset:2048 sc1\n" \
  "buffer_load_dwordx4 a[76:79], v5, s[36:39], s70 offen offset:3072 sc1\n" \
  "buffer_load_dword v98, v97, s[36:39], s44 offen sc1\n" \
  "s_barrier\n" \
  "LMc3_%=:\n" \
  "ds_read_b128 v[16:19], v1 offset:0\n" \
  "ds_read_b128 v[20:23], v1 offset:1024\n" \
  "ds_read_b128 v[24:27], v1 offset:2048\n" \
  "ds_read_b128 v[28:31], v1 offset:3072\n" \
  "ds_read_b128 v[32:35], v1 offset:4096\n" \
  "ds_read_b128 v[36:39], v1 offset:5120\n" \
  "ds_read_b128 v[40:43], v1 offset:6144\n" \
  "ds_read_b128 v[44:47], v1 offset:7168\n" \
  "ds_read_b128 v[48:51], v1 offset:8192\n" \
  "ds_read_b128 v[52:55], v1 offset:9216\n" \
  "s_waitcnt lgkmcnt(9)\n" \
  "v_smfmac_f32_16x16x64_bf16 v[56:59], v[16:19], a[0:7], v0\n" \
  "s_waitcnt lgkmcnt(8)\n" \
  "v_smfmac_f32_16x16x64_bf16 v[56:59], v[20:23], a[80:87], v0\n" \
  "s_waitcnt lgkmcnt(7)\n" \
  "v_smfmac_f32_16x16x64_bf16 v[56:59], v[24:27], a[160:167], v0\n" \
  "s_waitcnt lgkmcnt(6)\n" \
  "v_smfmac_f32_16x16x64_bf16 v[56:59], v[28:31], a[88:95], v0\n" \
  "s_waitcnt lgkmcnt(5)\n" \
  "v_smfmac_f32_16x16x64_bf16 v[56:59], v[32:35], a[104:111], v0\n" \
  "s_waitcnt lgkmcnt(4)\n" \
  "v_smfmac_f32_16x16x64_bf16 v[56:59], v[36:39], a[40:47], v0\n" \
  "s_waitcnt lgkmcnt(3)\n" \
  "v_smfmac_f32_16x16x64_bf16 v[56:59], v[40:43], a[120:127], v0\n" \
  "s_waitcnt lgkmcnt(2)\n" \
  "v_smfmac_f32_16x16x64_bf16 v[56:59], v[44:47], a[200:207], v0\n" \
  "s_waitcnt lgkmcnt(1)\n" \
  "v_smfmac_f32_16x16x64_bf16 v[56:59], v[48:51], a[96:103], v0\n" \
  "s_waitcnt lgkmcnt(0)\n" \
  "v_smfmac_f32_16x16x64_bf16 v[56:59], v[52:55], a[112:119], v0\n" \
  "s_nop 7\n" \
  "v_cmp_u_f32 vcc, v56, v57\n" \
  "v_cmp_u_f32 s[82:83], v58, v59\n" \
  "s_nop 1\n" \
  "s_or_b64 vcc, vcc, s[82:83]\n" \
  "s_cbranch_scc1 LMFc3_%=\n" \
  "LMOKc3_%=:\n" \
  "v_permlane32_swap_b32 v56, v58\n" \
  "v_permlane32_swap_b32 v57, v59\n" \
  "v_pk_add_f32 v[56:57], v[56:57], v[58:59]\n" \
  "v_cvt_pk_bf16_f32 v56, v56, v57\n" \
  "ds_write_b32 v3, v56 offset:3840\n" \
  "s_waitcnt lgkmcnt(0)\n" \
  "s_cmp_eq_u32 s68, 64\n" \
  "s_cbranch_scc1 LENDc3_%=\n" \
  "s_add_u32 s46, s44, 32768\n" \
  "s_add_u32 s47, s44, 36864\n" \
  "s_add_u32 s69, s44, 49152\n" \
  "s_add_u32 s70, s44, 53248\n" \
  "s_sub_u32 s43, s45, s93\n" \
  "s_mul_i32 s74, s51, s54\n" \
  "s_add_u32 s74, s74, s94\n" \
  "s_sub_i32 s75, s74, s48\n" \
  "s_mov_b32 s48, s74\n" \
  "s_mul_i32 s75, s75, s50\n" \
  "s_ashr_i32 s75, s75, 10\n" \
  "s_add_u32 s49, s49, s75\n" \
  "s_memrealtime s[76:77]\n" \
  "s_memtime s[78:79]\n" \
  "s_waitcnt lgkmcnt(0)\n" \
  "s_lshl_b32 s75, s76, 2\n" \
  "s_sub_i32 s75, s48, s75\n" \
  "s_mul_i32 s75, s75, s50\n" \
  "s_ashr_i32 s75, s75, 10\n" \
  "s_sub_i32 s74, s49, s78\n" \
  "s_min_i32 s74, s74, s75\n" \
  "s_sub_i32 s75, s75, 92\n" \
  "s_max_i32 s74, s74, s75\n" \
  "s_add_u32 s49, s74, s78\n" \
  "s_sub_u32 s74, s78, s89\n" \
  "s_sub_u32 s75, s76, s88\n" \
  "v_cvt_f32_u32 v92, s74\n" \
  "v_cvt_f32_u32 v93, s75\n" \
  "v_rcp_f32 v93, v93\n" \
  "v_mul_f32 v92, 0x43820000, v92\n" \
  "v_mul_f32 v92, v92, v93\n" \
  "v_cvt_u32_f32 v92, v92\n" \
  "s_nop 0\n" \
  "v_readfirstlane_b32 s74, v92\n" \
  "s_min_u32 s74, s74, 6400\n" \
  "s_cmp_gt_u32 s75, 900\n" \
  "s_cselect_b32 s50, s74, s50\n" \
  "s_cselect_b32 s88, s76, s88\n" \
  "s_cselect_b32 s89, s78, s89\n" \
  "LWEc3_%=:\n" \
  "s_memrealtime s[76:77]\n" \
  "s_memtime s[78:79]\n" \
  "s_waitcnt lgkmcnt(0)\n" \
  "s_lshl_b32 s75, s76, 2\n" \
  "s_sub_i32 s75, s48, s75\n" \
  "s_mul_i32 s75, s75, s50\n" \
  "s_ashr_i32 s75, s75, 10\n" \
  "s_sub_i32 s74, s49, s78\n" \
  "s_min_i32 s74, s74, s75\n" \
  "s_sub_i32 s75, s75, 92\n" \
  "s_max_i32 s74, s74, s75\n" \
  "s_add_u32 s49, s74, s78\n" \
  "s_cmp_gt_i32 s74, 168\n" \
  "s_cbranch_scc1 LWEc3_%=\n" \
  "s_cmp_lt_i32 s74, s87\n" \
  "s_cbranch_scc1 LSEc3_%=\n" \
  "s_max_i32 s74, s74, 48\n" \
  "s_lshr_b32 s74, s74, 3\n" \
  "s_lshl_b32 s74, s74, 2\n" \
  "s_sub_u32 s80, s84, s74\n" \
  "s_subb_u32 s81, s85, 0\n" \
  "s_setpc_b64 s[80:81]\n" \
  "s_nop 1\n" \
  "s_nop 1\n" \
  "s_nop 1\n" \
  "s_nop 1\n" \
  "s_nop 1\n" \
  "s_nop 1\n" \
  "s_nop 1\n" \
  "s_nop 1\n" \
  "s_nop 1\n" \
  "s_nop 1\n" \
  "s_nop 1\n" \
  "s_nop 1\n" \
  "s_nop 1\n" \
  "s_nop 1\n" \
  "s_nop 1\n" \
  "s_nop 1\n" \
  "LEEc3_%=:\n" \
  "buffer_load_dwordx4 v[112:115], v10, s[36:39], s46 offen offset:0 sc1\n" \
  "buffer_load_dwordx4 v[120:123], v10, s[36:39], s46 offen offset:2048 sc1\n" \
  "buffer_load_dwordx4 v[116:119], v6, s[36:39], s46 offen offset:1024 sc1\n" \
  "buffer_load_dwordx4 v[124:127], v6, s[36:39], s46 offen offset:3072 sc1\n" \
  "buffer_load_dwordx4 v[132:135], v6, s[36:39], s47 offen offset:1024 sc1\n" \
  "buffer_load_dwordx4 v[140:143], v6, s[36:39], s47 offen offset:3072 sc1\n" \
  "buffer_load_dwordx4 v[128:131], v10, s[36:39], s47 offen offset:0 sc1\n" \
  "buffer_load_dwordx4 v[136:139], v10, s[36:39], s47 offen offset:2048 sc1\n" \
  "buffer_load_dwordx4 v[144:147], v10, s[36:39], s69 offen offset:0 sc1\n" \
  "buffer_load_dwordx4 v[152:155], v10, s[36:39], s69 offen offset:2048 sc1\n" \
  "buffer_load_dwordx4 v[148:151], v6, s[36:39], s69 offen offset:1024 sc1\n" \
  "buffer_load_dwordx4 v[156:159], v6, s[36:39], s69 offen offset:3072 sc1\n" \
  "buffer_load_dwordx4 v[164:167], v6, s[36:39], s70 offen offset:1024 sc1\n" \
  "buffer_load_dwordx4 v[172:175], v6, s[36:39], s70 offen offset:3072 sc1\n" \
  "buffer_load_dwordx4 v[160:163], v10, s[36:39], s70 offen offset:0 sc1\n" \
  "buffer_load_dwordx4 v[168:171], v10, s[36:39], s70 offen offset:2048 sc1\n" \
  "s_add_u32 s46, s44, 65536\n" \
  "s_add_u32 s47, s44, 69632\n" \
  "s_mul_i32 s74, s51, s54\n" \
  "s_add_u32 s74, s74, s95\n" \
  "s_add_u32 s74, s74, s96\n" \
  "s_sub_i32 s75, s74, s48\n" \
  "s_mov_b32 s48, s74\n" \
  "s_mul_i32 s75, s75, s50\n" \
  "s_ashr_i32 s75, s75, 10\n" \
  "s_add_u32 s49, s49, s75\n" \
  "LULc3_%=:\n" \
  "s_memtime s[78:79]\n" \
  "s_waitcnt lgkmcnt(0)\n" \
  "s_sub_i32 s74, s49, s78\n" \
  "s_cmp_gt_i32 s74, 168\n" \
  "s_cbranch_scc1 LULc3_%=\n" \
  "s_max_i32 s74, s74, 48\n" \
  "s_lshr_b32 s74, s74, 3\n" \
  "s_lshl_b32 s74, s74, 2\n" \
  "s_sub_u32 s80, s90, s74\n" \
  "s_subb_u32 s81, s91, 0\n" \
  "s_setpc_b64 s[80:81]\n" \
  "s_nop 1\n" \
  "s_nop 1\n" \
  "s_nop 1\n" \
  "s_nop 1\n" \
  "s_nop 1\n" \
  "s_nop 1\n" \
  "s_nop 1\n" \
  "s_nop 1\n" \
  "s_nop 1\n" \
  "s_nop 1\n" \
  "s_nop 1\n" \
  "s_nop 1\n" \
  "s_nop 1\n" \
  "s_nop 1\n" \
  "s_nop 1\n" \
  "s_nop 1\n" \
  "LELc3_%=:\n" \
  "buffer_load_dwordx4 v[16:19], v1, s[36:39], s46 offen offset:0 sc1\n" \
  "buffer_load_dwordx4 v[20:23], v1, s[36:39], s46 offen offset:1024 sc1\n" \
  "buffer_load_dwordx4 v[24:27], v1, s[36:39], s46 offen offset:2048 sc1\n" \
  "buffer_load_dwordx4 v[28:31], v1, s[36:39], s46 offen offset:3072 sc1\n" \
  "buffer_load_dwordx4 v[32:35], v1, s[36:39], s47 offen offset:0 sc1\n" \
  "buffer_load_dwordx4 v[36:39], v1, s[36:39], s47 offen offset:1024 sc1\n" \
  "buffer_load_dwordx4 v[40:43], v1, s[36:39], s47 offen offset:2048 sc1\n" \
  "buffer_load_dwordx4 v[44:47], v1, s[36:39], s47 offen offset:3072 sc1\n" \
  "s_cmp_lg_u32 s71, 0\n" \
  "s_cbranch_scc1 LNBc3_%=\n" \
  "s_barrier\n" \
  "LNBc3_%=:\n" \
  "s_mov_b32 s71, 0\n" \
  "ds_read_b128 v[224:227], v1 offset:10240\n" \
  "ds_read_b128 v[228:231], v1 offset:11264\n" \
  "ds_read_b128 v[232:235], v1 offset:12288\n" \
  "ds_read_b128 v[236:239], v1 offset:13312\n" \
  "s_mov_b64 exec, s[40:41]\n" \
  "s_waitcnt vmcnt(23)\n" \
  "v_mfma_f32_16x16x32_bf16 v[176:179], v[112:115], a[8:11], 0\n" \
  "s_waitcnt vmcnt(22)\n" \
  "v_mfma_f32_16x16x32_bf16 v[184:187], v[120:123], a[16:19], 0\n" \
  "s_waitcnt vmcnt(20)\n" \
  "v_mfma_f32_16x16x32_bf16 v[180:183], v[116:119], a[12:15], 0\n" \
  "v_cvt_pk_bf16_f32 v208, v176, v177\n" \
  "v_cvt_pk_bf16_f32 v209, v178, v179\n" \
  "v_mfma_f32_16x16x32_bf16 v[188:191], v[124:127], a[20:23], 0\n" \
  "s_waitcnt vmcnt(18)\n" \
  "v_cvt_pk_bf16_f32 v210, v184, v185\n" \
  "v_cvt_pk_bf16_f32 v211, v186, v187\n" \
  "v_mfma_f32_16x16x32_bf16 v[196:199], v[132:135], a[28:31], 0\n" \
  "s_not_b64 exec, exec\n" \
  "v_cvt_pk_bf16_f32 v208, v180, v181\n" \
  "v_cvt_pk_bf16_f32 v209, v182, v183\n" \
  "v_mfma_f32_16x16x32_bf16 v[204:207], v[140:143], a[36:39], 0\n" \
  "s_waitcnt vmcnt(16)\n" \
  "v_cvt_pk_bf16_f32 v210, v188, v189\n" \
  "v_cvt_pk_bf16_f32 v211, v190, v191\n" \
  "v_mfma_f32_16x16x32_bf16 v[192:195], v[128:131], a[24:27], 0\n" \
  "v_cvt_pk_bf16_f32 v212, v196, v197\n" \
  "v_cvt_pk_bf16_f32 v213, v198, v199\n" \
  "v_mfma_f32_16x16x32_bf16 v[200:203], v[136:139], a[32:35], 0\n" \
  "s_waitcnt vmcnt(14)\n" \
  "v_cvt_pk_bf16_f32 v214, v204, v205\n" \
  "v_cvt_pk_bf16_f32 v215, v206, v207\n" \
  "v_mfma_f32_16x16x32_bf16 v[176:179], v[144:147], a[48:51], 0\n" \
  "s_not_b64 exec, exec\n" \
  "v_cvt_pk_bf16_f32 v212, v192, v193\n" \
  "v_cvt_pk_bf16_f32 v213, v194, v195\n" \
  "v_mfma_f32_16x16x32_bf16 v[184:187], v[152:155], a[56:59], 0\n" \
  "s_waitcnt vmcnt(12)\n" \
  "v_cvt_pk_bf16_f32 v214, v200, v201\n" \
  "v_cvt_pk_bf16_f32 v215, v202, v203\n" \
  "v_mfma_f32_16x16x32_bf16 v[180:183], v[148:151], a[52:55], 0\n" \
  "v_cvt_pk_bf16_f32 v216, v176, v177\n" \
  "v_cvt_pk_bf16_f32 v217, v178, v179\n" \
  "v_mfma_f32_16x16x32_bf16 v[188:191], v[156:159], a[60:63], 0\n" \
  "s_waitcnt vmcnt(10)\n" \
  "v_cvt_pk_bf16_f32 v218, v184, v185\n" \
  "v_cvt_pk_bf16_f32 v219, v186, v187\n" \
  "v_mfma_f32_16x16x32_bf16 v[196:199], v[164:167], a[68:71], 0\n" \
  "s_not_b64 exec, exec\n" \
  "v_cvt_pk_bf16_f32 v216, v180, v181\n" \
  "v_cvt_pk_bf16_f32 v217, v182, v183\n" \
  "v_mfma_f32_16x16x32_bf16 v[204:207], v[172:175], a[76:79], 0\n" \
  "s_waitcnt vmcnt(9)\n" \
  "v_cvt_pk_bf16_f32 v218, v188, v189\n" \
  "v_cvt_pk_bf16_f32 v219, v190, v191\n" \
  "v_mfma_f32_16x16x32_bf16 v[192:195], v[160:163], a[64:67], 0\n" \
  "s_waitcnt vmcnt(8)\n" \
  "v_cvt_pk_bf16_f32 v220, v196, v197\n" \
  "v_cvt_pk_bf16_f32 v221, v198, v199\n" \
  "v_mfma_f32_16x16x32_bf16 v[200:203], v[168:171], a[72:75], 0\n" \
  "v_cvt_pk_bf16_f32 v222, v204, v205\n" \
  "v_cvt_pk_bf16_f32 v223, v206, v207\n" \
  "s_waitcnt vmcnt(7)\n" \
  "s_waitcnt lgkmcnt(3)\n" \
  "v_mfma_f32_16x16x32_bf16 v[240:243], v[224:227], v[16:19], 0\n" \
  "s_not_b64 exec, exec\n" \
  "v_cvt_pk_bf16_f32 v220, v192, v193\n" \
  "v_cvt_pk_bf16_f32 v221, v194, v195\n" \
  "s_waitcnt vmcnt(6)\n" \
  "s_waitcnt lgkmcnt(2)\n" \
  "v_mfma_f32_16x16x32_bf16 v[240:243], v[228:231], v[20:23], v[240:243]\n" \
  "v_cvt_pk_bf16_f32 v222, v200, v201\n" \
  "v_cvt_pk_bf16_f32 v223, v202, v203\n" \
  "s_waitcnt vmcnt(5)\n" \
  "s_waitcnt lgkmcnt(1)\n" \
  "v_mfma_f32_16x16x32_bf16 v[240:243], v[232:235], v[24:27], v[240:243]\n" \
  "s_mov_b64 exec, -1\n" \
  "s_waitcnt vmcnt(4)\n" \
  "s_waitcnt lgkmcnt(0)\n" \
  "v_mfma_f32_16x16x32_bf16 v[240:243], v[236:239], v[28:31], v[240:243]\n" \
  "s_waitcnt vmcnt(3)\n" \
  "v_mfma_f32_16x16x32_bf16 v[240:243], v[208:211], v[32:35], v[240:243]\n" \
  "s_waitcnt vmcnt(2)\n" \
  "v_mfma_f32_16x16x32_bf16 v[240:243], v[212:215], v[36:39], v[240:243]\n" \
  "s_waitcnt vmcnt(1)\n" \
  "v_mfma_f32_16x16x32_bf16 v[240:243], v[216:219], v[40:43], v[240:243]\n" \
  "s_waitcnt vmcnt(0)\n" \
  "v_mfma_f32_16x16x32_bf16 v[240:243], v[220:223], v[44:47], v[240:243]\n" \
  "s_nop 7\n" \
  "v_permlane32_swap_b32 v240, v242\n" \
  "v_permlane32_swap_b32 v241, v243\n" \
  "s_nop 0\n" \
  "v_add_f32_dpp v240, v242, v240 row_ror:8 row_mask:0xf bank_mask:0xf\n" \
  "v_add_f32_dpp v241, v243, v241 row_ror:8 row_mask:0xf bank_mask:0xf\n" \
  "v_cmp_u_f32 vcc, v240, v241\n" \
  "v_cvt_pk_bf16_f32 v240, v240, v241\n" \
  "s_and_b64 vcc, vcc, s[34:35]\n" \
  "s_cbranch_scc1 LPFc3_%=\n" \
  "LPOKc3_%=:\n" \
  "v_mov_b32_dpp v240, v240 row_ror:8 row_mask:0xf bank_mask:0xc\n" \
  "buffer_store_dword v240, v13, s[36:39], s43 offen sc1\n" \
  "buffer_store_dword v240, v2, s[36:39], s43 offen sc1\n" \
  "v_readlane_b32 s74, v98, 3\n" \
  "s_nop 0\n" \
  "s_sub_u32 s75, s74, s51\n" \
  "s_cmp_lt_u32 s75, 16\n" \
  "s_cselect_b32 s51, s74, s51\n" \
  "v_readlane_b32 s74, v98, 0\n" \
  "s_add_u32 s72, s72, s53\n" \
  "s_sub_u32 s75, s74, s72\n" \
  "s_add_u32 s75, s75, 2000\n" \
  "s_cmp_lt_u32 s75, 4000\n" \
  "s_cselect_b32 s72, s74, s72\n" \
  "v_readlane_b32 s74, v98, 1\n" \
  "s_add_u32 s94, s94, s53\n" \
  "s_sub_u32 s75, s74, s94\n" \
  "s_add_u32 s75, s75, 2000\n" \
  "s_cmp_lt_u32 s75, 4000\n" \
  "s_cselect_b32 s94, s74, s94\n" \
  "v_readlane_b32 s74, v98, 2\n" \
  "s_add_u32 s95, s95, s53\n" \
  "s_sub_u32 s75, s74, s95\n" \
  "s_add_u32 s75, s75, 2000\n" \
  "s_cmp_lt_u32 s75, 4000\n" \
  "s_cselect_b32 s95, s74, s95\n" \
  "s_mul_i32 s74, s51, s54\n" \
  "s_add_u32 s74, s74, s72\n" \
  "s_add_u32 s74, s74, s55\n" \
  "s_sub_i32 s75, s74, s48\n" \
  "s_mov_b32 s48, s74\n" \
  "s_mul_i32 s75, s75, s50\n" \
  "s_ashr_i32 s75, s75, 10\n" \
  "s_add_u32 s49, s49, s75\n" \
  "s_mov_b32 s96, 0\n" \
  "s_barrier\n" \
  "s_mov_b32 s44, s45\n" \
  "s_cmp_lg_u32 s38, 0\n" \
  "s_cbranch_scc1 LOOPc3_%=\n" \
  "s_branch LPASSc3_%=\n" \
  "LSEc3_%=:\n" \
  "s_add_i32 s96, s96, 48\n" \
  "s_cmp_gt_i32 s96, 0\n" \
  "s_cselect_b32 s74, s54, 0\n" \
  "s_addc_u32 s51, s51, 0\n" \
  "s_sub_i32 s96, s96, s74\n" \
  "s_mul_i32 s74, s51, s54\n" \
  "s_add_u32 s74, s74, s94\n" \
  "s_add_u32 s74, s74, s96\n" \
  "s_sub_i32 s75, s74, s48\n" \
  "s_mov_b32 s48, s74\n" \
  "s_mul_i32 s75, s75, s50\n" \
  "s_ashr_i32 s75, s75, 10\n" \
  "s_add_u32 s49, s49, s75\n" \
  "s_branch LWEc3_%=\n" \
  "LENDc3_%=:\n" \
  "s_barrier\n" \
  "s_barrier\n" \
  "s_branch LDONE_%=\n" \
  "LPFc3_%=:\n" \
  "s_add_u32 s46, s44, 32768\n" \
  "s_add_u32 s47, s44, 36864\n" \
  "s_add_u32 s69, s44, 49152\n" \
  "s_add_u32 s70, s44, 53248\n" \
  "s_sub_i32 s71, s54, s96\n" \
  "s_sub_i32 s71, s71, 40\n" \
  "v_max_u32 v92, v112, v113\n" \
  "v_max3_u32 v92, v92, v114, v115\n" \
  "v_max3_u32 v92, v92, v116, v117\n" \
  "v_max3_u32 v92, v92, v118, v119\n" \
  "v_max3_u32 v92, v92, v120, v121\n" \
  "v_max3_u32 v92, v92, v122, v123\n" \
  "v_max3_u32 v92, v92, v124, v125\n" \
  "v_max3_u32 v92, v92, v126, v127\n" \
  "v_max3_u32 v92, v92, v128, v129\n" \
  "v_max3_u32 v92, v92, v130, v131\n" \
  "v_max3_u32 v92, v92, v132, v133\n" \
  "v_max3_u32 v92, v92, v134, v135\n" \
  "v_max3_u32 v92, v92, v136, v137\n" \
  "v_max3_u32 v92, v92, v138, v139\n" \
  "v_max3_u32 v92, v92, v140, v141\n" \
  "v_max3_u32 v92, v92, v142, v143\n" \
  "v_max3_u32 v92, v92, v144, v145\n" \
  "v_max3_u32 v92, v92, v146, v147\n" \
  "v_max3_u32 v92, v92, v148, v149\n" \
  "v_max3_u32 v92, v92, v150, v151\n" \
  "v_max3_u32 v92, v92, v152, v153\n" \
  "v_max3_u32 v92, v92, v154, v155\n" \
  "v_max3_u32 v92, v92, v156, v157\n" \
  "v_max3_u32 v92, v92, v158, v159\n" \
  "v_max3_u32 v92, v92, v160, v161\n" \
  "v_max3_u32 v92, v92, v162, v163\n" \
  "v_max3_u32 v92, v92, v164, v165\n" \
  "v_max3_u32 v92, v92, v166, v167\n" \
  "v_max3_u32 v92, v92, v168, v169\n" \
  "v_max3_u32 v92, v92, v170, v171\n" \
  "v_max3_u32 v92, v92, v172, v173\n" \
  "v_max3_u32 v92, v92, v174, v175\n" \
  "v_cmp_eq_u32 vcc, -1, v92\n" \
  "s_nop 3\n" \
  "s_cbranch_vccnz LPRc3_%=\n" \
  "s_mov_b32 s71, 256\n" \
  "s_add_u32 s59, s59, 1\n" \
  "v_max_u32 v92, v16, v17\n" \
  "v_max3_u32 v92, v92, v18, v19\n" \
  "v_max3_u32 v92, v92, v20, v21\n" \
  "v_max3_u32 v92, v92, v22, v23\n" \
  "v_max3_u32 v92, v92, v24, v25\n" \
  "v_max3_u32 v92, v92, v26, v27\n" \
  "v_max3_u32 v92, v92, v28, v29\n" \
  "v_max3_u32 v92, v92, v30, v31\n" \
  "v_max3_u32 v92, v92, v32, v33\n" \
  "v_max3_u32 v92, v92, v34, v35\n" \
  "v_max3_u32 v92, v92, v36, v37\n" \
  "v_max3_u32 v92, v92, v38, v39\n" \
  "v_max3_u32 v92, v92, v40, v41\n" \
  "v_max3_u32 v92, v92, v42, v43\n" \
  "v_max3_u32 v92, v92, v44, v45\n" \
  "v_max3_u32 v92, v92, v46, v47\n" \
  "v_cmp_eq_u32 vcc, -1, v92\n" \
  "s_nop 3\n" \
  "s_cbranch_vccz LPEc3_%=\n" \
  "LPRc3_%=:\n" \
  "s_add_u32 s73, s73, 1\n" \
  "s_cmp_gt_u32 s73, 20000\n" \
  "s_cbranch_scc1 LPEc3_%=\n" \
  "s_add_u32 s58, s58, 1\n" \
  "s_add_i32 s96, s96, s71\n" \
  "s_cmp_gt_i32 s96, 0\n" \
  "s_cselect_b32 s74, s54, 0\n" \
  "s_addc_u32 s51, s51, 0\n" \
  "s_sub_i32 s96, s96, s74\n" \
  "s_mul_i32 s74, s51, s54\n" \
  "s_add_u32 s74, s74, s94\n" \
  "s_add_u32 s74, s74, s96\n" \
  "s_sub_i32 s75, s74, s48\n" \
  "s_mov_b32 s48, s74\n" \
  "s_mul_i32 s75, s75, s50\n" \
  "s_ashr_i32 s75, s75, 10\n" \
  "s_add_u32 s49, s49, s75\n" \
  "s_branch LWEc3_%=\n" \
  "LPEc3_%=:\n" \
  "s_mov_b32 s71, 0\n" \
  "buffer_load_dwordx4 v[112:115], v10, s[36:39], s46 offen offset:0 sc1\n" \
  "buffer_load_dwordx4 v[120:123], v10, s[36:39], s46 offen offset:2048 sc1\n" \
  "buffer_load_dwordx4 v[116:119], v6, s[36:39], s46 offen offset:1024 sc1\n" \
  "buffer_load_dwordx4 v[124:127], v6, s[36:39], s46 offen offset:3072 sc1\n" \
  "buffer_load_dwordx4 v[132:135], v6, s[36:39], s47 offen offset:1024 sc1\n" \
  "buffer_load_dwordx4 v[140:143], v6, s[36:39], s47 offen offset:3072 sc1\n" \
  "buffer_load_dwordx4 v[128:131], v10, s[36:39], s47 offen offset:0 sc1\n" \
  "buffer_load_dwordx4 v[136:139], v10, s[36:39], s47 offen offset:2048 sc1\n" \
  "buffer_load_dwordx4 v[144:147], v10, s[36:39], s69 offen offset:0 sc1\n" \
  "buffer_load_dwordx4 v[152:155], v10, s[36:39], s69 offen offset:2048 sc1\n" \
  "buffer_load_dwordx4 v[148:151], v6, s[36:39], s69 offen offset:1024 sc1\n" \
  "buffer_load_dwordx4 v[156:159], v6, s[36:39], s69 offen offset:3072 sc1\n" \
  "buffer_load_dwordx4 v[164:167], v6, s[36:39], s70 offen offset:1024 sc1\n" \
  "buffer_load_dwordx4 v[172:175], v6, s[36:39], s70 offen offset:3072 sc1\n" \
  "buffer_load_dwordx4 v[160:163], v10, s[36:39], s70 offen offset:0 sc1\n" \
  "buffer_load_dwordx4 v[168:171], v10, s[36:39], s70 offen offset:2048 sc1\n" \
  "s_waitcnt vmcnt(0)\n" \
  "LPGc3_%=:\n" \
  "v_cmp_eq_u32 vcc, 0, v12\n" \
  "s_nop 1\n" \
  "v_cndmask_b32 v32, 0, v32, vcc\n" \
  "s_nop 1\n" \
  "v_cndmask_b32 v36, 0, v36, vcc\n" \
  "s_nop 1\n" \
  "v_cndmask_b32 v40, 0, v40, vcc\n" \
  "s_nop 1\n" \
  "v_cndmask_b32 v44, 0, v44, vcc\n" \
  "v_cmp_eq_u32 vcc, 1, v12\n" \
  "s_nop 1\n" \
  "v_cndmask_b32 v33, 0, v33, vcc\n" \
  "s_nop 1\n" \
  "v_cndmask_b32 v37, 0, v37, vcc\n" \
  "s_nop 1\n" \
  "v_cndmask_b32 v41, 0, v41, vcc\n" \
  "s_nop 1\n" \
  "v_cndmask_b32 v45, 0, v45, vcc\n" \
  "v_cmp_eq_u32 vcc, 0, v12\n" \
  "s_nop 1\n" \
  "v_cndmask_b32 v34, 0, v34, vcc\n" \
  "s_nop 1\n" \
  "v_cndmask_b32 v38, 0, v38, vcc\n" \
  "s_nop 1\n" \
  "v_cndmask_b32 v42, 0, v42, vcc\n" \
  "s_nop 1\n" \
  "v_cndmask_b32 v46, 0, v46, vcc\n" \
  "v_cmp_eq_u32 vcc, 1, v12\n" \
  "s_nop 1\n" \
  "v_cndmask_b32 v35, 0, v35, vcc\n" \
  "s_nop 1\n" \
  "v_cndmask_b32 v39, 0, v39, vcc\n" \
  "s_nop 1\n" \
  "v_cndmask_b32 v43, 0, v43, vcc\n" \
  "s_nop 1\n" \
  "v_cndmask_b32 v47, 0, v47, vcc\n" \
  "v_mfma_f32_16x16x32_bf16 v[176:179], v[112:115], a[8:11], 0\n" \
  "v_mfma_f32_16x16x32_bf16 v[180:183], v[116:119], a[12:15], 0\n" \
  "v_mfma_f32_16x16x32_bf16 v[184:187], v[120:123], a[16:19], 0\n" \
  "v_mfma_f32_16x16x32_bf16 v[188:191], v[124:127], a[20:23], 0\n" \
  "v_mfma_f32_16x16x32_bf16 v[192:195], v[128:131], a[24:27], 0\n" \
  "v_mfma_f32_16x16x32_bf16 v[196:199], v[132:135], a[28:31], 0\n" \
  "v_mfma_f32_16x16x32_bf16 v[200:203], v[136:139], a[32:35], 0\n" \
  "v_mfma_f32_16x16x32_bf16 v[204:207], v[140:143], a[36:39], 0\n" \
  "s_mov_b64 exec, s[40:41]\n" \
  "v_cvt_pk_bf16_f32 v208, v176, v177\n" \
  "v_cvt_pk_bf16_f32 v209, v178, v179\n" \
  "v_cvt_pk_bf16_f32 v210, v184, v185\n" \
  "v_cvt_pk_bf16_f32 v211, v186, v187\n" \
  "v_cvt_pk_bf16_f32 v212, v192, v193\n" \
  "v_cvt_pk_bf16_f32 v213, v194, v195\n" \
  "v_cvt_pk_bf16_f32 v214, v200, v201\n" \
  "v_cvt_pk_bf16_f32 v215, v202, v203\n" \
  "s_not_b64 exec, exec\n" \
  "v_cvt_pk_bf16_f32 v208, v180, v181\n" \
  "v_cvt_pk_bf16_f32 v209, v182, v183\n" \
  "v_cvt_pk_bf16_f32 v210, v188, v189\n" \
  "v_cvt_pk_bf16_f32 v211, v190, v191\n" \
  "v_cvt_pk_bf16_f32 v212, v196, v197\n" \
  "v_cvt_pk_bf16_f32 v213, v198, v199\n" \
  "v_cvt_pk_bf16_f32 v214, v204, v205\n" \
  "v_cvt_pk_bf16_f32 v215, v206, v207\n" \
  "s_mov_b64 exec, -1\n" \
  "v_mfma_f32_16x16x32_bf16 v[176:179], v[144:147], a[48:51], 0\n" \
  "v_mfma_f32_16x16x32_bf16 v[180:183], v[148:151], a[52:55], 0\n" \
  "v_mfma_f32_16x16x32_bf16 v[184:187], v[152:155], a[56:59], 0\n" \
  "v_mfma_f32_16x16x32_bf16 v[188:191], v[156:159], a[60:63], 0\n" \
  "v_mfma_f32_16x16x32_bf16 v[192:195], v[160:163], a[64:67], 0\n" \
  "v_mfma_f32_16x16x32_bf16 v[196:199], v[164:167], a[68:71], 0\n" \
  "v_mfma_f32_16x16x32_bf16 v[200:203], v[168:171], a[72:75], 0\n" \
  "v_mfma_f32_16x16x32_bf16 v[204:207], v[172:175], a[76:79], 0\n" \
  "s_mov_b64 exec, s[40:41]\n" \
  "v_cvt_pk_bf16_f32 v216, v176, v177\n" \
  "v_cvt_pk_bf16_f32 v217, v178, v179\n" \
  "v_cvt_pk_bf16_f32 v218, v184, v185\n" \
  "v_cvt_pk_bf16_f32 v219, v186, v187\n" \
  "v_cvt_pk_bf16_f32 v220, v192, v193\n" \
  "v_cvt_pk_bf16_f32 v221, v194, v195\n" \
  "v_cvt_pk_bf16_f32 v222, v200, v201\n" \
  "v_cvt_pk_bf16_f32 v223, v202, v203\n" \
  "s_not_b64 exec, exec\n" \
  "v_cvt_pk_bf16_f32 v216, v180, v181\n" \
  "v_cvt_pk_bf16_f32 v217, v182, v183\n" \
  "v_cvt_pk_bf16_f32 v218, v188, v189\n" \
  "v_cvt_pk_bf16_f32 v219, v190, v191\n" \
  "v_cvt_pk_bf16_f32 v220, v196, v197\n" \
  "v_cvt_pk_bf16_f32 v221, v198, v199\n" \
  "v_cvt_pk_bf16_f32 v222, v204, v205\n" \
  "v_cvt_pk_bf16_f32 v223, v206, v207\n" \
  "s_mov_b64 exec, -1\n" \
  "v_mfma_f32_16x16x32_bf16 v[240:243], v[224:227], v[16:19], 0\n" \
  "v_mfma_f32_16x16x32_bf16 v[240:243], v[228:231], v[20:23], v[240:243]\n" \
  "v_mfma_f32_16x16x32_bf16 v[240:243], v[232:235], v[24:27], v[240:243]\n" \
  "v_mfma_f32_16x16x32_bf16 v[240:243], v[236:239], v[28:31], v[240:243]\n" \
  "v_mfma_f32_16x16x32_bf16 v[240:243], v[208:211], v[32:35], v[240:243]\n" \
  "v_mfma_f32_16x16x32_bf16 v[240:243], v[212:215], v[36:39], v[240:243]\n" \
  "v_mfma_f32_16x16x32_bf16 v[240:243], v[216:219], v[40:43], v[240:243]\n" \
  "v_mfma_f32_16x16x32_bf16 v[240:243], v[220:223], v[44:47], v[240:243]\n" \
  "s_nop 7\n" \
  "v_permlane32_swap_b32 v240, v242\n" \
  "v_permlane32_swap_b32 v241, v243\n" \
  "s_nop 0\n" \
  "v_add_f32_dpp v240, v242, v240 row_ror:8 row_mask:0xf bank_mask:0xf\n" \
  "v_add_f32_dpp v241, v243, v241 row_ror:8 row_mask:0xf bank_mask:0xf\n" \
  "v_cvt_pk_bf16_f32 v240, v240, v241\n" \
  "v_pk_min_u16 v240, v240, -2 op_sel_hi:[1,0]\n" \
  "s_nop 1\n" \
  "s_branch LPOKc3_%=\n" \
  "LMFc3_%=:\n" \
  "v_max_u32 v92, v16, v17\n" \
  "v_max3_u32 v92, v92, v18, v19\n" \
  "v_max3_u32 v92, v92, v20, v21\n" \
  "v_max3_u32 v92, v92, v22, v23\n" \
  "v_max3_u32 v92, v92, v24, v25\n" \
  "v_max3_u32 v92, v92, v26, v27\n" \
  "v_max3_u32 v92, v92, v28, v29\n" \
  "v_max3_u32 v92, v92, v30, v31\n" \
  "v_max3_u32 v92, v92, v32, v33\n" \
  "v_max3_u32 v92, v92, v34, v35\n" \
  "v_max3_u32 v92, v92, v36, v37\n" \
  "v_max3_u32 v92, v92, v38, v39\n" \
  "v_max3_u32 v92, v92, v40, v41\n" \
  "v_max3_u32 v92, v92, v42, v43\n" \
  "v_max3_u32 v92, v92, v44, v45\n" \
  "v_max3_u32 v92, v92, v46, v47\n" \
  "v_max3_u32 v92, v92, v48, v49\n" \
  "v_max3_u32 v92, v92, v50, v51\n" \
  "v_max3_u32 v92, v92, v52, v53\n" \
  "v_max3_u32 v92, v92, v54, v55\n" \
  "v_cmp_eq_u32 vcc, -1, v92\n" \
  "s_nop 3\n" \
  "s_cbranch_vccz LMOKc3_%=\n" \
  "s_cmp_gt_u32 s57, 20000\n" \
  "s_cbranch_scc1 LMOKc3_%=\n" \
  "s_add_u32 s57, s57, 1\n" \
  "LHMc3_%=:\n" \
  "s_add_u32 s48, s48, s54\n" \
  "s_add_u32 s49, s49, s86\n" \
  "s_add_u32 s51, s51, 1\n" \
  "LGMc3_%=:\n" \
  "s_memrealtime s[76:77]\n" \
  "s_memtime s[78:79]\n" \
  "s_waitcnt lgkmcnt(0)\n" \
  "s_lshl_b32 s75, s76, 2\n" \
  "s_sub_i32 s75, s48, s75\n" \
  "s_mul_i32 s75, s75, s50\n" \
  "s_ashr_i32 s75, s75, 10\n" \
  "s_sub_i32 s74, s49, s78\n" \
  "s_min_i32 s74, s74, s75\n" \
  "s_sub_i32 s75, s75, 92\n" \
  "s_max_i32 s74, s74, s75\n" \
  "s_add_u32 s49, s74, s78\n" \
  "s_cmp_gt_i32 s74, 60\n" \
  "s_cbranch_scc1 LGMc3_%=\n" \
  "s_cmp_lt_i32 s74, s87\n" \
  "s_cbranch_scc1 LHMc3_%=\n" \
  "s_barrier\n" \
  "v_mov_b32 v56, 0\n" \
  "v_mov_b32 v57, 0\n" \
  "v_mov_b32 v58, 0\n" \
  "v_mov_b32 v59, 0\n" \
  "s_waitcnt vmcnt(0)\n" \
  "s_barrier\n" \
  "s_branch LMc3_%=\n" \
  "LDONE_%=:\n" \
  "s_waitcnt vmcnt(0) lgkmcnt(0)\n" \
  "s_mov_b32 s53, s73\n" \
  "s_mov_b32 s54, s51\n" \
  "s_mov_b32 s55, s50\n" \
  "LXIT_%=:\n"
#define PROJ_CLOBBERS \
  "v12", "v13", "v14", "v15", "v16", "v17", "v18", "v19", "v20", "v21", "v22", "v23", "v24", "v25", "v26", "v27", "v28", "v29", "v30", "v31", "v32", \
  "v33", "v34", "v35", "v36", "v37", "v38", "v39", "v40", "v41", "v42", "v43", "v44", "v45", "v46", "v47", "v48", "v49", "v50", "v51", "v52", "v53", \
  "v54", "v55", "v56", "v57", "v58", "v59", "v60", "v61", "v62", "v63", "v64", "v65", "v66", "v67", "v68", "v69", "v70", "v71", "v72", "v73", "v74", \
  "v75", "v76", "v77", "v78", "v79", "v100", "v101", "v102", "v103", "v104", "v105", "v106", "v107", "v108", "v109", "v110", "v111", "v112", "v113", \
  "v114", "v115", "v116", "v117", "v118", "v119", "v120", "v121", "v122", "v123", "s68", "s69", "s70", "s71", "s72", "s73", "s74", "s75", "s76", "s77"
#define PROJ_ASM \
  "s_getpc_b64 s[74:75]\n" \
  "LCF3_%=:\n" \
  "s_add_u32 s74, s74, LPL_%=-LCF3_%=\n" \
  "s_addc_u32 s75, s75, 0\n" \
  "v_mbcnt_lo_u32_b32 v12, -1, 0\n" \
  "v_mbcnt_hi_u32_b32 v12, -1, v12\n" \
  "v_lshlrev_b32 v12, 6, v12\n" \
  "global_load_dword v13, v12, s[74:75]\n" \
  "v_add_u32 v12, 4096, v12\n" \
  "global_load_dword v13, v12, s[74:75]\n" \
  "s_getpc_b64 s[74:75]\n" \
  "LCF4_%=:\n" \
  "s_add_u32 s74, s74, LPE_%=-4096-LCF4_%=\n" \
  "s_addc_u32 s75, s75, 0\n" \
  "v_mbcnt_lo_u32_b32 v12, -1, 0\n" \
  "v_mbcnt_hi_u32_b32 v12, -1, v12\n" \
  "v_lshlrev_b32 v12, 6, v12\n" \
  "global_load_dword v13, v12, s[74:75]\n" \
  "s_waitcnt vmcnt(0)\n" \
  "s_barrier\n" \
  "ds_read_b128 v[32:35], v0 offset:0\n" \
  "ds_read_b128 v[36:39], v0 offset:20480\n" \
  "ds_read_b128 v[40:43], v0 offset:1024\n" \
  "ds_read_b128 v[44:47], v0 offset:21504\n" \
  "v_add_u32 v9, 40960, v0\n" \
  "v_add_u32 v10, 81920, v0\n" \
  "s_mul_i32 s68, s52, 40960\n" \
  "s_add_u32 s68, s68, 81920\n" \
  "s_mov_b32 s69, 0x40000000\n" \
  "s_mov_b32 s71, 0x40000000\n" \
  "v_mov_b32 v12, -1\n" \
  "v_mov_b32 v13, -1\n" \
  "v_mov_b32 v14, -1\n" \
  "v_mov_b32 v15, -1\n" \
  "LPL_%=:\n" \
  "s_waitcnt lgkmcnt(2)\n" \
  "v_mfma_f32_16x16x32_bf16 v[56:59], a[0:3], v[32:35], 0\n" \
  "ds_read_b128 v[48:51], v0 offset:2048\n" \
  "v_mfma_f32_16x16x32_bf16 v[68:71], a[0:3], v[36:39], 0\n" \
  "ds_read_b128 v[52:55], v0 offset:22528\n" \
  "v_mfma_f32_16x16x32_bf16 v[60:63], a[80:83], v[32:35], 0\n" \
  "buffer_store_dwordx4 v[12:15], v7, s[40:43], s71 offen sc1\n" \
  "v_mfma_f32_16x16x32_bf16 v[72:75], a[80:83], v[36:39], 0\n" \
  "v_cvt_pk_bf16_f32 v16, v100, v101\n" \
  "v_mfma_f32_16x16x32_bf16 v[64:67], a[160:163], v[32:35], 0\n" \
  "v_cvt_pk_bf16_f32 v17, v102, v103\n" \
  "v_mfma_f32_16x16x32_bf16 v[76:79], a[160:163], v[36:39], 0\n" \
  "v_cvt_pk_bf16_f32 v18, v104, v105\n" \
  "s_waitcnt lgkmcnt(2)\n" \
  "v_mfma_f32_16x16x32_bf16 v[56:59], a[4:7], v[40:43], v[56:59]\n" \
  "ds_read_b128 v[32:35], v0 offset:3072\n" \
  "v_mfma_f32_16x16x32_bf16 v[68:71], a[4:7], v[44:47], v[68:71]\n" \
  "ds_read_b128 v[36:39], v0 offset:23552\n" \
  "v_mfma_f32_16x16x32_bf16 v[60:63], a[84:87], v[40:43], v[60:63]\n" \
  "v_cvt_pk_bf16_f32 v19, v106, v107\n" \
  "v_mfma_f32_16x16x32_bf16 v[72:75], a[84:87], v[44:47], v[72:75]\n" \
  "buffer_store_dwordx4 v[16:19], v2, s[40:43], s69 offen sc1\n" \
  "v_mfma_f32_16x16x32_bf16 v[64:67], a[164:167], v[40:43], v[64:67]\n" \
  "v_cvt_pk_bf16_f32 v20, v112, v113\n" \
  "v_mfma_f32_16x16x32_bf16 v[76:79], a[164:167], v[44:47], v[76:79]\n" \
  "v_cvt_pk_bf16_f32 v21, v114, v115\n" \
  "s_waitcnt lgkmcnt(2)\n" \
  "v_mfma_f32_16x16x32_bf16 v[56:59], a[8:11], v[48:51], v[56:59]\n" \
  "ds_read_b128 v[40:43], v0 offset:4096\n" \
  "v_mfma_f32_16x16x32_bf16 v[68:71], a[8:11], v[52:55], v[68:71]\n" \
  "ds_read_b128 v[44:47], v0 offset:24576\n" \
  "v_mfma_f32_16x16x32_bf16 v[60:63], a[88:91], v[48:51], v[60:63]\n" \
  "v_cvt_pk_bf16_f32 v22, v116, v117\n" \
  "v_mfma_f32_16x16x32_bf16 v[72:75], a[88:91], v[52:55], v[72:75]\n" \
  "v_cvt_pk_bf16_f32 v23, v118, v119\n" \
  "v_mfma_f32_16x16x32_bf16 v[64:67], a[168:171], v[48:51], v[64:67]\n" \
  "buffer_store_dwordx4 v[20:23], v2, s[40:43], s69 offen offset:1024 sc1\n" \
  "v_mfma_f32_16x16x32_bf16 v[76:79], a[168:171], v[52:55], v[76:79]\n" \
  "v_cvt_pk_bf16_f32 v24, v108, v109\n" \
  "s_waitcnt lgkmcnt(2)\n" \
  "v_mfma_f32_16x16x32_bf16 v[56:59], a[12:15], v[32:35], v[56:59]\n" \
  "ds_read_b128 v[48:51], v0 offset:5120\n" \
  "v_mfma_f32_16x16x32_bf16 v[68:71], a[12:15], v[36:39], v[68:71]\n" \
  "ds_read_b128 v[52:55], v0 offset:25600\n" \
  "v_mfma_f32_16x16x32_bf16 v[60:63], a[92:95], v[32:35], v[60:63]\n" \
  "v_cvt_pk_bf16_f32 v25, v110, v111\n" \
  "v_mfma_f32_16x16x32_bf16 v[72:75], a[92:95], v[36:39], v[72:75]\n" \
  "v_cvt_pk_bf16_f32 v26, v120, v121\n" \
  "v_mfma_f32_16x16x32_bf16 v[64:67], a[172:175], v[32:35], v[64:67]\n" \
  "v_cvt_pk_bf16_f32 v27, v122, v123\n" \
  "v_mfma_f32_16x16x32_bf16 v[76:79], a[172:175], v[36:39], v[76:79]\n" \
  "buffer_store_dwordx4 v[24:27], v3, s[40:43], s69 offen sc1\n" \
  "s_waitcnt lgkmcnt(2)\n" \
  "v_mfma_f32_16x16x32_bf16 v[56:59], a[16:19], v[40:43], v[56:59]\n" \
  "ds_read_b128 v[32:35], v0 offset:6144\n" \
  "v_mfma_f32_16x16x32_bf16 v[68:71], a[16:19], v[44:47], v[68:71]\n" \
  "ds_read_b128 v[36:39], v0 offset:26624\n" \
  "v_mfma_f32_16x16x32_bf16 v[60:63], a[96:99], v[40:43], v[60:63]\n" \
  "s_add_u32 s72, s68, 4096\n" \
  "v_mfma_f32_16x16x32_bf16 v[72:75], a[96:99], v[44:47], v[72:75]\n" \
  "s_add_u32 s73, s68, 8192\n" \
  "v_mfma_f32_16x16x32_bf16 v[64:67], a[176:179], v[40:43], v[64:67]\n" \
  "s_add_u32 m0, s54, 81920\n" \
  "v_mfma_f32_16x16x32_bf16 v[76:79], a[176:179], v[44:47], v[76:79]\n" \
  "buffer_load_dwordx4 v1, s[36:39], s68 offen offset:0 sc1 lds\n" \
  "s_waitcnt lgkmcnt(2)\n" \
  "v_mfma_f32_16x16x32_bf16 v[56:59], a[20:23], v[48:51], v[56:59]\n" \
  "ds_read_b128 v[40:43], v0 offset:7168\n" \
  "v_mfma_f32_16x16x32_bf16 v[68:71], a[20:23], v[52:55], v[68:71]\n" \
  "ds_read_b128 v[44:47], v0 offset:27648\n" \
  "v_mfma_f32_16x16x32_bf16 v[60:63], a[100:103], v[48:51], v[60:63]\n" \
  "buffer_load_dwordx4 v1, s[36:39], s68 offen offset:1024 sc1 lds\n" \
  "v_mfma_f32_16x16x32_bf16 v[72:75], a[100:103], v[52:55], v[72:75]\n" \
  "v_mfma_f32_16x16x32_bf16 v[64:67], a[180:183], v[48:51], v[64:67]\n" \
  "v_mfma_f32_16x16x32_bf16 v[76:79], a[180:183], v[52:55], v[76:79]\n" \
  "s_waitcnt lgkmcnt(2)\n" \
  "v_mfma_f32_16x16x32_bf16 v[56:59], a[24:27], v[32:35], v[56:59]\n" \
  "ds_read_b128 v[48:51], v0 offset:8192\n" \
  "v_mfma_f32_16x16x32_bf16 v[68:71], a[24:27], v[36:39], v[68:71]\n" \
  "ds_read_b128 v[52:55], v0 offset:28672\n" \
  "v_mfma_f32_16x16x32_bf16 v[60:63], a[104:107], v[32:35], v[60:63]\n" \
  "buffer_load_dwordx4 v1, s[36:39], s68 offen offset:2048 sc1 lds\n" \
  "v_mfma_f32_16x16x32_bf16 v[72:75], a[104:107], v[36:39], v[72:75]\n" \
  "v_mfma_f32_16x16x32_bf16 v[64:67], a[184:187], v[32:35], v[64:67]\n" \
  "v_mfma_f32_16x16x32_bf16 v[76:79], a[184:187], v[36:39], v[76:79]\n" \
  "s_waitcnt lgkmcnt(2)\n" \
  "v_mfma_f32_16x16x32_bf16 v[56:59], a[28:31], v[40:43], v[56:59]\n" \
  "ds_read_b128 v[32:35], v0 offset:9216\n" \
  "v_mfma_f32_16x16x32_bf16 v[68:71], a[28:31], v[44:47], v[68:71]\n" \
  "ds_read_b128 v[36:39], v0 offset:29696\n" \
  "v_mfma_f32_16x16x32_bf16 v[60:63], a[108:111], v[40:43], v[60:63]\n" \
  "buffer_load_dwordx4 v1, s[36:39], s68 offen offset:3072 sc1 lds\n" \
  "v_mfma_f32_16x16x32_bf16 v[72:75], a[108:111], v[44:47], v[72:75]\n" \
  "v_mfma_f32_16x16x32_bf16 v[64:67], a[188:191], v[40:43], v[64:67]\n" \
  "v_mfma_f32_16x16x32_bf16 v[76:79], a[188:191], v[44:47], v[76:79]\n" \
  "s_waitcnt lgkmcnt(2)\n" \
  "v_mfma_f32_16x16x32_bf16 v[56:59], a[32:35], v[48:51], v[56:59]\n" \
  "ds_read_b128 v[40:43], v0 offset:10240\n" \
  "v_mfma_f32_16x16x32_bf16 v[68:71], a[32:35], v[52:55], v[68:71]\n" \
  "ds_read_b128 v[44:47], v0 offset:30720\n" \
  "v_mfma_f32_16x16x32_bf16 v[60:63], a[112:115], v[48:51], v[60:63]\n" \
  "s_add_u32 m0, s54, 86016\n" \
  "v_mfma_f32_16x16x32_bf16 v[72:75], a[112:115], v[52:55], v[72:75]\n" \
  "buffer_load_dwordx4 v1, s[36:39], s72 offen offset:0 sc1 lds\n" \
  "v_mfma_f32_16x16x32_bf16 v[64:67], a[192:195], v[48:51], v[64:67]\n" \
  "v_mfma_f32_16x16x32_bf16 v[76:79], a[192:195], v[52:55], v[76:79]\n" \
  "s_waitcnt lgkmcnt(2)\n" \
  "v_mfma_f32_16x16x32_bf16 v[56:59], a[36:39], v[32:35], v[56:59]\n" \
  "ds_read_b128 v[48:51], v0 offset:11264\n" \
  "v_mfma_f32_16x16x32_bf16 v[68:71], a[36:39], v[36:39], v[68:71]\n" \
  "ds_read_b128 v[52:55], v0 offset:31744\n" \
  "v_mfma_f32_16x16x32_bf16 v[60:63], a[116:119], v[32:35], v[60:63]\n" \
  "buffer_load_dwordx4 v1, s[36:39], s72 offen offset:1024 sc1 lds\n" \
  "v_mfma_f32_16x16x32_bf16 v[72:75], a[116:119], v[36:39], v[72:75]\n" \
  "v_mfma_f32_16x16x32_bf16 v[64:67], a[196:199], v[32:35], v[64:67]\n" \
  "v_mfma_f32_16x16x32_bf16 v[76:79], a[196:199], v[36:39], v[76:79]\n" \
  "s_waitcnt lgkmcnt(2)\n" \
  "v_mfma_f32_16x16x32_bf16 v[56:59], a[40:43], v[40:43], v[56:59]\n" \
  "ds_read_b128 v[32:35], v0 offset:12288\n" \
  "v_mfma_f32_16x16x32_bf16 v[68:71], a[40:43], v[44:47], v[68:71]\n" \
  "ds_read_b128 v[36:39], v0 offset:32768\n" \
  "v_mfma_f32_16x16x32_bf16 v[60:63], a[120:123], v[40:43], v[60:63]\n" \
  "buffer_load_dwordx4 v1, s[36:39], s72 offen offset:2048 sc1 lds\n" \
  "v_mfma_f32_16x16x32_bf16 v[72:75], a[120:123], v[44:47], v[72:75]\n" \
  "v_mfma_f32_16x16x32_bf16 v[64:67], a[200:203], v[40:43], v[64:67]\n" \
  "v_mfma_f32_16x16x32_bf16 v[76:79], a[200:203], v[44:47], v[76:79]\n" \
  "s_waitcnt lgkmcnt(2)\n" \
  "v_mfma_f32_16x16x32_bf16 v[56:59], a[44:47], v[48:51], v[56:59]\n" \
  "ds_read_b128 v[40:43], v0 offset:13312\n" \
  "v_mfma_f32_16x16x32_bf16 v[68:71], a[44:47], v[52:55], v[68:71]\n" \
  "ds_read_b128 v[44:47], v0 offset:33792\n" \
  "v_mfma_f32_16x16x32_bf16 v[60:63], a[124:127], v[48:51], v[60:63]\n" \
  "buffer_load_dwordx4 v1, s[36:39], s72 offen offset:3072 sc1 lds\n" \
  "v_mfma_f32_16x16x32_bf16 v[72:75], a[124:127], v[52:55], v[72:75]\n" \
  "v_mfma_f32_16x16x32_bf16 v[64:67], a[204:207], v[48:51], v[64:67]\n" \
  "v_mfma_f32_16x16x32_bf16 v[76:79], a[204:207], v[52:55], v[76:79]\n" \
  "s_waitcnt lgkmcnt(2)\n" \
  "v_mfma_f32_16x16x32_bf16 v[56:59], a[48:51], v[32:35], v[56:59]\n" \
  "ds_read_b128 v[48:51], v0 offset:14336\n" \
  "v_mfma_f32_16x16x32_bf16 v[68:71], a[48:51], v[36:39], v[68:71]\n" \
  "ds_read_b128 v[52:55], v0 offset:34816\n" \
  "v_mfma_f32_16x16x32_bf16 v[60:63], a[128:131], v[32:35], v[60:63]\n" \
  "s_add_u32 m0, s54, 90112\n" \
  "v_mfma_f32_16x16x32_bf16 v[72:75], a[128:131], v[36:39], v[72:75]\n" \
  "buffer_load_dwordx4 v1, s[36:39], s73 offen offset:0 sc1 lds\n" \
  "v_mfma_f32_16x16x32_bf16 v[64:67], a[208:211], v[32:35], v[64:67]\n" \
  "v_mfma_f32_16x16x32_bf16 v[76:79], a[208:211], v[36:39], v[76:79]\n" \
  "s_waitcnt lgkmcnt(2)\n" \
  "v_mfma_f32_16x16x32_bf16 v[56:59], a[52:55], v[40:43], v[56:59]\n" \
  "ds_read_b128 v[32:35], v0 offset:15360\n" \
  "v_mfma_f32_16x16x32_bf16 v[68:71], a[52:55], v[44:47], v[68:71]\n" \
  "ds_read_b128 v[36:39], v0 offset:35840\n" \
  "v_mfma_f32_16x16x32_bf16 v[60:63], a[132:135], v[40:43], v[60:63]\n" \
  "buffer_load_dwordx4 v1, s[36:39], s73 offen offset:1024 sc1 lds\n" \
  "v_mfma_f32_16x16x32_bf16 v[72:75], a[132:135], v[44:47], v[72:75]\n" \
  "v_mfma_f32_16x16x32_bf16 v[64:67], a[212:215], v[40:43], v[64:67]\n" \
  "v_mfma_f32_16x16x32_bf16 v[76:79], a[212:215], v[44:47], v[76:79]\n" \
  "s_waitcnt lgkmcnt(2)\n" \
  "v_mfma_f32_16x16x32_bf16 v[56:59], a[56:59], v[48:51], v[56:59]\n" \
  "ds_read_b128 v[40:43], v0 offset:16384\n" \
  "v_mfma_f32_16x16x32_bf16 v[68:71], a[56:59], v[52:55], v[68:71]\n" \
  "ds_read_b128 v[44:47], v0 offset:36864\n" \
  "v_mfma_f32_16x16x32_bf16 v[60:63], a[136:139], v[48:51], v[60:63]\n" \
  "s_add_u32 s68, s68, 40960\n" \
  "v_mfma_f32_16x16x32_bf16 v[72:75], a[136:139], v[52:55], v[72:75]\n" \
  "v_mfma_f32_16x16x32_bf16 v[64:67], a[216:219], v[48:51], v[64:67]\n" \
  "v_mfma_f32_16x16x32_bf16 v[76:79], a[216:219], v[52:55], v[76:79]\n" \
  "s_waitcnt lgkmcnt(2)\n" \
  "v_mfma_f32_16x16x32_bf16 v[56:59], a[60:63], v[32:35], v[56:59]\n" \
  "ds_read_b128 v[48:51], v0 offset:17408\n" \
  "v_mfma_f32_16x16x32_bf16 v[68:71], a[60:63], v[36:39], v[68:71]\n" \
  "ds_read_b128 v[52:55], v0 offset:37888\n" \
  "v_mfma_f32_16x16x32_bf16 v[60:63], a[140:143], v[32:35], v[60:63]\n" \
  "v_mov_b32 v11, s52\n" \
  "v_mfma_f32_16x16x32_bf16 v[72:75], a[140:143], v[36:39], v[72:75]\n" \
  "s_waitcnt vmcnt(10)\n" \
  "v_mfma_f32_16x16x32_bf16 v[64:67], a[220:223], v[32:35], v[64:67]\n" \
  "buffer_store_dword v11, v5, s[40:43], 0 offen sc1\n" \
  "v_mfma_f32_16x16x32_bf16 v[76:79], a[220:223], v[36:39], v[76:79]\n" \
  "s_waitcnt lgkmcnt(2)\n" \
  "v_mfma_f32_16x16x32_bf16 v[56:59], a[64:67], v[40:43], v[56:59]\n" \
  "ds_read_b128 v[32:35], v0 offset:18432\n" \
  "v_mfma_f32_16x16x32_bf16 v[68:71], a[64:67], v[44:47], v[68:71]\n" \
  "ds_read_b128 v[36:39], v0 offset:38912\n" \
  "v_mfma_f32_16x16x32_bf16 v[60:63], a[144:147], v[40:43], v[60:63]\n" \
  "v_mfma_f32_16x16x32_bf16 v[72:75], a[144:147], v[44:47], v[72:75]\n" \
  "v_mfma_f32_16x16x32_bf16 v[64:67], a[224:227], v[40:43], v[64:67]\n" \
  "v_mfma_f32_16x16x32_bf16 v[76:79], a[224:227], v[44:47], v[76:79]\n" \
  "s_waitcnt lgkmcnt(2)\n" \
  "v_mfma_f32_16x16x32_bf16 v[56:59], a[68:71], v[48:51], v[56:59]\n" \
  "ds_read_b128 v[40:43], v0 offset:19456\n" \
  "v_mfma_f32_16x16x32_bf16 v[68:71], a[68:71], v[52:55], v[68:71]\n" \
  "ds_read_b128 v[44:47], v0 offset:39936\n" \
  "v_mfma_f32_16x16x32_bf16 v[60:63], a[148:151], v[48:51], v[60:63]\n" \
  "v_readlane_b32 s69, v4, s52\n" \
  "v_mfma_f32_16x16x32_bf16 v[72:75], a[148:151], v[52:55], v[72:75]\n" \
  "v_readlane_b32 s71, v8, s52\n" \
  "v_mfma_f32_16x16x32_bf16 v[64:67], a[228:231], v[48:51], v[64:67]\n" \
  "s_add_u32 s52, s52, 1\n" \
  "v_mfma_f32_16x16x32_bf16 v[76:79], a[228:231], v[52:55], v[76:79]\n" \
  "s_waitcnt lgkmcnt(0)\n" \
  "s_barrier\n" \
  "v_mfma_f32_16x16x32_bf16 v[56:59], a[72:75], v[32:35], v[56:59]\n" \
  "ds_read_b128 v[48:51], v9 offset:0\n" \
  "v_mfma_f32_16x16x32_bf16 v[68:71], a[72:75], v[36:39], v[68:71]\n" \
  "ds_read_b128 v[52:55], v9 offset:20480\n" \
  "v_mfma_f32_16x16x32_bf16 v[60:63], a[152:155], v[32:35], v[60:63]\n" \
  "v_mfma_f32_16x16x32_bf16 v[72:75], a[152:155], v[36:39], v[72:75]\n" \
  "v_mfma_f32_16x16x32_bf16 v[64:67], a[232:235], v[32:35], v[64:67]\n" \
  "v_mfma_f32_16x16x32_bf16 v[76:79], a[232:235], v[36:39], v[76:79]\n" \
  "v_mfma_f32_16x16x32_bf16 v[56:59], a[76:79], v[40:43], v[56:59]\n" \
  "ds_read_b128 v[32:35], v9 offset:1024\n" \
  "v_mfma_f32_16x16x32_bf16 v[68:71], a[76:79], v[44:47], v[68:71]\n" \
  "ds_read_b128 v[36:39], v9 offset:21504\n" \
  "v_mfma_f32_16x16x32_bf16 v[60:63], a[156:159], v[40:43], v[60:63]\n" \
  "s_cmp_eq_u32 s52, s53\n" \
  "v_mfma_f32_16x16x32_bf16 v[72:75], a[156:159], v[44:47], v[72:75]\n" \
  "v_mfma_f32_16x16x32_bf16 v[64:67], a[236:239], v[40:43], v[64:67]\n" \
  "v_mfma_f32_16x16x32_bf16 v[76:79], a[236:239], v[44:47], v[76:79]\n" \
  "s_waitcnt lgkmcnt(2)\n" \
  "v_mfma_f32_16x16x32_bf16 v[100:103], a[0:3], v[48:51], 0\n" \
  "ds_read_b128 v[40:43], v9 offset:2048\n" \
  "v_mfma_f32_16x16x32_bf16 v[112:115], a[0:3], v[52:55], 0\n" \
  "ds_read_b128 v[44:47], v9 offset:22528\n" \
  "v_mfma_f32_16x16x32_bf16 v[104:107], a[80:83], v[48:51], 0\n" \
  "buffer_store_dwordx4 v[12:15], v7, s[40:43], s71 offen sc1\n" \
  "v_mfma_f32_16x16x32_bf16 v[116:119], a[80:83], v[52:55], 0\n" \
  "v_cvt_pk_bf16_f32 v16, v56, v57\n" \
  "v_mfma_f32_16x16x32_bf16 v[108:111], a[160:163], v[48:51], 0\n" \
  "v_cvt_pk_bf16_f32 v17, v58, v59\n" \
  "v_mfma_f32_16x16x32_bf16 v[120:123], a[160:163], v[52:55], 0\n" \
  "v_cvt_pk_bf16_f32 v18, v60, v61\n" \
  "s_waitcnt lgkmcnt(2)\n" \
  "v_mfma_f32_16x16x32_bf16 v[100:103], a[4:7], v[32:35], v[100:103]\n" \
  "ds_read_b128 v[48:51], v9 offset:3072\n" \
  "v_mfma_f32_16x16x32_bf16 v[112:115], a[4:7], v[36:39], v[112:115]\n" \
  "ds_read_b128 v[52:55], v9 offset:23552\n" \
  "v_mfma_f32_16x16x32_bf16 v[104:107], a[84:87], v[32:35], v[104:107]\n" \
  "v_cvt_pk_bf16_f32 v19, v62, v63\n" \
  "v_mfma_f32_16x16x32_bf16 v[116:119], a[84:87], v[36:39], v[116:119]\n" \
  "buffer_store_dwordx4 v[16:19], v2, s[40:43], s69 offen sc1\n" \
  "v_mfma_f32_16x16x32_bf16 v[108:111], a[164:167], v[32:35], v[108:111]\n" \
  "v_cvt_pk_bf16_f32 v20, v68, v69\n" \
  "v_mfma_f32_16x16x32_bf16 v[120:123], a[164:167], v[36:39], v[120:123]\n" \
  "v_cvt_pk_bf16_f32 v21, v70, v71\n" \
  "s_waitcnt lgkmcnt(2)\n" \
  "v_mfma_f32_16x16x32_bf16 v[100:103], a[8:11], v[40:43], v[100:103]\n" \
  "ds_read_b128 v[32:35], v9 offset:4096\n" \
  "v_mfma_f32_16x16x32_bf16 v[112:115], a[8:11], v[44:47], v[112:115]\n" \
  "ds_read_b128 v[36:39], v9 offset:24576\n" \
  "v_mfma_f32_16x16x32_bf16 v[104:107], a[88:91], v[40:43], v[104:107]\n" \
  "v_cvt_pk_bf16_f32 v22, v72, v73\n" \
  "v_mfma_f32_16x16x32_bf16 v[116:119], a[88:91], v[44:47], v[116:119]\n" \
  "v_cvt_pk_bf16_f32 v23, v74, v75\n" \
  "v_mfma_f32_16x16x32_bf16 v[108:111], a[168:171], v[40:43], v[108:111]\n" \
  "buffer_store_dwordx4 v[20:23], v2, s[40:43], s69 offen offset:1024 sc1\n" \
  "v_mfma_f32_16x16x32_bf16 v[120:123], a[168:171], v[44:47], v[120:123]\n" \
  "v_cvt_pk_bf16_f32 v24, v64, v65\n" \
  "s_waitcnt lgkmcnt(2)\n" \
  "v_mfma_f32_16x16x32_bf16 v[100:103], a[12:15], v[48:51], v[100:103]\n" \
  "ds_read_b128 v[40:43], v9 offset:5120\n" \
  "v_mfma_f32_16x16x32_bf16 v[112:115], a[12:15], v[52:55], v[112:115]\n" \
  "ds_read_b128 v[44:47], v9 offset:25600\n" \
  "v_mfma_f32_16x16x32_bf16 v[104:107], a[92:95], v[48:51], v[104:107]\n" \
  "v_cvt_pk_bf16_f32 v25, v66, v67\n" \
  "v_mfma_f32_16x16x32_bf16 v[116:119], a[92:95], v[52:55], v[116:119]\n" \
  "v_cvt_pk_bf16_f32 v26, v76, v77\n" \
  "v_mfma_f32_16x16x32_bf16 v[108:111], a[172:175], v[48:51], v[108:111]\n" \
  "v_cvt_pk_bf16_f32 v27, v78, v79\n" \
  "v_mfma_f32_16x16x32_bf16 v[120:123], a[172:175], v[52:55], v[120:123]\n" \
  "buffer_store_dwordx4 v[24:27], v3, s[40:43], s69 offen sc1\n" \
  "s_waitcnt lgkmcnt(2)\n" \
  "v_mfma_f32_16x16x32_bf16 v[100:103], a[16:19], v[32:35], v[100:103]\n" \
  "ds_read_b128 v[48:51], v9 offset:6144\n" \
  "v_mfma_f32_16x16x32_bf16 v[112:115], a[16:19], v[36:39], v[112:115]\n" \
  "ds_read_b128 v[52:55], v9 offset:26624\n" \
  "v_mfma_f32_16x16x32_bf16 v[104:107], a[96:99], v[32:35], v[104:107]\n" \
  "s_add_u32 s72, s68, 4096\n" \
  "v_mfma_f32_16x16x32_bf16 v[116:119], a[96:99], v[36:39], v[116:119]\n" \
  "s_add_u32 s73, s68, 8192\n" \
  "v_mfma_f32_16x16x32_bf16 v[108:111], a[176:179], v[32:35], v[108:111]\n" \
  "s_add_u32 m0, s54, 0\n" \
  "v_mfma_f32_16x16x32_bf16 v[120:123], a[176:179], v[36:39], v[120:123]\n" \
  "buffer_load_dwordx4 v1, s[36:39], s68 offen offset:0 sc1 lds\n" \
  "s_waitcnt lgkmcnt(2)\n" \
  "v_mfma_f32_16x16x32_bf16 v[100:103], a[20:23], v[40:43], v[100:103]\n" \
  "ds_read_b128 v[32:35], v9 offset:7168\n" \
  "v_mfma_f32_16x16x32_bf16 v[112:115], a[20:23], v[44:47], v[112:115]\n" \
  "ds_read_b128 v[36:39], v9 offset:27648\n" \
  "v_mfma_f32_16x16x32_bf16 v[104:107], a[100:103], v[40:43], v[104:107]\n" \
  "buffer_load_dwordx4 v1, s[36:39], s68 offen offset:1024 sc1 lds\n" \
  "v_mfma_f32_16x16x32_bf16 v[116:119], a[100:103], v[44:47], v[116:119]\n" \
  "v_mfma_f32_16x16x32_bf16 v[108:111], a[180:183], v[40:43], v[108:111]\n" \
  "v_mfma_f32_16x16x32_bf16 v[120:123], a[180:183], v[44:47], v[120:123]\n" \
  "s_waitcnt lgkmcnt(2)\n" \
  "v_mfma_f32_16x16x32_bf16 v[100:103], a[24:27], v[48:51], v[100:103]\n" \
  "ds_read_b128 v[40:43], v9 offset:8192\n" \
  "v_mfma_f32_16x16x32_bf16 v[112:115], a[24:27], v[52:55], v[112:115]\n" \
  "ds_read_b128 v[44:47], v9 offset:28672\n" \
  "v_mfma_f32_16x16x32_bf16 v[104:107], a[104:107], v[48:51], v[104:107]\n" \
  "buffer_load_dwordx4 v1, s[36:39], s68 offen offset:2048 sc1 lds\n" \
  "v_mfma_f32_16x16x32_bf16 v[116:119], a[104:107], v[52:55], v[116:119]\n" \
  "v_mfma_f32_16x16x32_bf16 v[108:111], a[184:187], v[48:51], v[108:111]\n" \
  "v_mfma_f32_16x16x32_bf16 v[120:123], a[184:187], v[52:55], v[120:123]\n" \
  "s_waitcnt lgkmcnt(2)\n" \
  "v_mfma_f32_16x16x32_bf16 v[100:103], a[28:31], v[32:35], v[100:103]\n" \
  "ds_read_b128 v[48:51], v9 offset:9216\n" \
  "v_mfma_f32_16x16x32_bf16 v[112:115], a[28:31], v[36:39], v[112:115]\n" \
  "ds_read_b128 v[52:55], v9 offset:29696\n" \
  "v_mfma_f32_16x16x32_bf16 v[104:107], a[108:111], v[32:35], v[104:107]\n" \
  "buffer_load_dwordx4 v1, s[36:39], s68 offen offset:3072 sc1 lds\n" \
  "v_mfma_f32_16x16x32_bf16 v[116:119], a[108:111], v[36:39], v[116:119]\n" \
  "v_mfma_f32_16x16x32_bf16 v[108:111], a[188:191], v[32:35], v[108:111]\n" \
  "v_mfma_f32_16x16x32_bf16 v[120:123], a[188:191], v[36:39], v[120:123]\n" \
  "s_waitcnt lgkmcnt(2)\n" \
  "v_mfma_f32_16x16x32_bf16 v[100:103], a[32:35], v[40:43], v[100:103]\n" \
  "ds_read_b128 v[32:35], v9 offset:10240\n" \
  "v_mfma_f32_16x16x32_bf16 v[112:115], a[32:35], v[44:47], v[112:115]\n" \
  "ds_read_b128 v[36:39], v9 offset:30720\n" \
  "v_mfma_f32_16x16x32_bf16 v[104:107], a[112:115], v[40:43], v[104:107]\n" \
  "s_add_u32 m0, s54, 4096\n" \
  "v_mfma_f32_16x16x32_bf16 v[116:119], a[112:115], v[44:47], v[116:119]\n" \
  "buffer_load_dwordx4 v1, s[36:39], s72 offen offset:0 sc1 lds\n" \
  "v_mfma_f32_16x16x32_bf16 v[108:111], a[192:195], v[40:43], v[108:111]\n" \
  "v_mfma_f32_16x16x32_bf16 v[120:123], a[192:195], v[44:47], v[120:123]\n" \
  "s_waitcnt lgkmcnt(2)\n" \
  "v_mfma_f32_16x16x32_bf16 v[100:103], a[36:39], v[48:51], v[100:103]\n" \
  "ds_read_b128 v[40:43], v9 offset:11264\n" \
  "v_mfma_f32_16x16x32_bf16 v[112:115], a[36:39], v[52:55], v[112:115]\n" \
  "ds_read_b128 v[44:47], v9 offset:31744\n" \
  "v_mfma_f32_16x16x32_bf16 v[104:107], a[116:119], v[48:51], v[104:107]\n" \
  "buffer_load_dwordx4 v1, s[36:39], s72 offen offset:1024 sc1 lds\n" \
  "v_mfma_f32_16x16x32_bf16 v[116:119], a[116:119], v[52:55], v[116:119]\n" \
  "v_mfma_f32_16x16x32_bf16 v[108:111], a[196:199], v[48:51], v[108:111]\n" \
  "v_mfma_f32_16x16x32_bf16 v[120:123], a[196:199], v[52:55], v[120:123]\n" \
  "s_waitcnt lgkmcnt(2)\n" \
  "v_mfma_f32_16x16x32_bf16 v[100:103], a[40:43], v[32:35], v[100:103]\n" \
  "ds_read_b128 v[48:51], v9 offset:12288\n" \
  "v_mfma_f32_16x16x32_bf16 v[112:115], a[40:43], v[36:39], v[112:115]\n" \
  "ds_read_b128 v[52:55], v9 offset:32768\n" \
  "v_mfma_f32_16x16x32_bf16 v[104:107], a[120:123], v[32:35], v[104:107]\n" \
  "buffer_load_dwordx4 v1, s[36:39], s72 offen offset:2048 sc1 lds\n" \
  "v_mfma_f32_16x16x32_bf16 v[116:119], a[120:123], v[36:39], v[116:119]\n" \
  "v_mfma_f32_16x16x32_bf16 v[108:111], a[200:203], v[32:35], v[108:111]\n" \
  "v_mfma_f32_16x16x32_bf16 v[120:123], a[200:203], v[36:39], v[120:123]\n" \
  "s_waitcnt lgkmcnt(2)\n" \
  "v_mfma_f32_16x16x32_bf16 v[100:103], a[44:47], v[40:43], v[100:103]\n" \
  "ds_read_b128 v[32:35], v9 offset:13312\n" \
  "v_mfma_f32_16x16x32_bf16 v[112:115], a[44:47], v[44:47], v[112:115]\n" \
  "ds_read_b128 v[36:39], v9 offset:33792\n" \
  "v_mfma_f32_16x16x32_bf16 v[104:107], a[124:127], v[40:43], v[104:107]\n" \
  "buffer_load_dwordx4 v1, s[36:39], s72 offen offset:3072 sc1 lds\n" \
  "v_mfma_f32_16x16x32_bf16 v[116:119], a[124:127], v[44:47], v[116:119]\n" \
  "v_mfma_f32_16x16x32_bf16 v[108:111], a[204:207], v[40:43], v[108:111]\n" \
  "v_mfma_f32_16x16x32_bf16 v[120:123], a[204:207], v[44:47], v[120:123]\n" \
  "s_waitcnt lgkmcnt(2)\n" \
  "v_mfma_f32_16x16x32_bf16 v[100:103], a[48:51], v[48:51], v[100:103]\n" \
  "ds_read_b128 v[40:43], v9 offset:14336\n" \
  "v_mfma_f32_16x16x32_bf16 v[112:115], a[48:51], v[52:55], v[112:115]\n" \
  "ds_read_b128 v[44:47], v9 offset:34816\n" \
  "v_mfma_f32_16x16x32_bf16 v[104:107], a[128:131], v[48:51], v[104:107]\n" \
  "s_add_u32 m0, s54, 8192\n" \
  "v_mfma_f32_16x16x32_bf16 v[116:119], a[128:131], v[52:55], v[116:119]\n" \
  "buffer_load_dwordx4 v1, s[36:39], s73 offen offset:0 sc1 lds\n" \
  "v_mfma_f32_16x16x32_bf16 v[108:111], a[208:211], v[48:51], v[108:111]\n" \
  "v_mfma_f32_16x16x32_bf16 v[120:123], a[208:211], v[52:55], v[120:123]\n" \
  "s_waitcnt lgkmcnt(2)\n" \
  "v_mfma_f32_16x16x32_bf16 v[100:103], a[52:55], v[32:35], v[100:103]\n" \
  "ds_read_b128 v[48:51], v9 offset:15360\n" \
  "v_mfma_f32_16x16x32_bf16 v[112:115], a[52:55], v[36:39], v[112:115]\n" \
  "ds_read_b128 v[52:55], v9 offset:35840\n" \
  "v_mfma_f32_16x16x32_bf16 v[104:107], a[132:135], v[32:35], v[104:107]\n" \
  "buffer_load_dwordx4 v1, s[36:39], s73 offen offset:1024 sc1 lds\n" \
  "v_mfma_f32_16x16x32_bf16 v[116:119], a[132:135], v[36:39], v[116:119]\n" \
  "v_mfma_f32_16x16x32_bf16 v[108:111], a[212:215], v[32:35], v[108:111]\n" \
  "v_mfma_f32_16x16x32_bf16 v[120:123], a[212:215], v[36:39], v[120:123]\n" \
  "s_waitcnt lgkmcnt(2)\n" \
  "v_mfma_f32_16x16x32_bf16 v[100:103], a[56:59], v[40:43], v[100:103]\n" \
  "ds_read_b128 v[32:35], v9 offset:16384\n" \
  "v_mfma_f32_16x16x32_bf16 v[112:115], a[56:59], v[44:47], v[112:115]\n" \
  "ds_read_b128 v[36:39], v9 offset:36864\n" \
  "v_mfma_f32_16x16x32_bf16 v[104:107], a[136:139], v[40:43], v[104:107]\n" \
  "s_add_u32 s68, s68, 40960\n" \
  "v_mfma_f32_16x16x32_bf16 v[116:119], a[136:139], v[44:47], v[116:119]\n" \
  "v_mfma_f32_16x16x32_bf16 v[108:111], a[216:219], v[40:43], v[108:111]\n" \
  "v_mfma_f32_16x16x32_bf16 v[120:123], a[216:219], v[44:47], v[120:123]\n" \
  "s_waitcnt lgkmcnt(2)\n" \
  "v_mfma_f32_16x16x32_bf16 v[100:103], a[60:63], v[48:51], v[100:103]\n" \
  "ds_read_b128 v[40:43], v9 offset:17408\n" \
  "v_mfma_f32_16x16x32_bf16 v[112:115], a[60:63], v[52:55], v[112:115]\n" \
  "ds_read_b128 v[44:47], v9 offset:37888\n" \
  "v_mfma_f32_16x16x32_bf16 v[104:107], a[140:143], v[48:51], v[104:107]\n" \
  "v_mov_b32 v11, s52\n" \
  "v_mfma_f32_16x16x32_bf16 v[116:119], a[140:143], v[52:55], v[116:119]\n" \
  "s_waitcnt vmcnt(10)\n" \
  "v_mfma_f32_16x16x32_bf16 v[108:111], a[220:223], v[48:51], v[108:111]\n" \
  "buffer_store_dword v11, v5, s[40:43], 0 offen sc1\n" \
  "v_mfma_f32_16x16x32_bf16 v[120:123], a[220:223], v[52:55], v[120:123]\n" \
  "s_waitcnt lgkmcnt(2)\n" \
  "v_mfma_f32_16x16x32_bf16 v[100:103], a[64:67], v[32:35], v[100:103]\n" \
  "ds_read_b128 v[48:51], v9 offset:18432\n" \
  "v_mfma_f32_16x16x32_bf16 v[112:115], a[64:67], v[36:39], v[112:115]\n" \
  "ds_read_b128 v[52:55], v9 offset:38912\n" \
  "v_mfma_f32_16x16x32_bf16 v[104:107], a[144:147], v[32:35], v[104:107]\n" \
  "v_mfma_f32_16x16x32_bf16 v[116:119], a[144:147], v[36:39], v[116:119]\n" \
  "v_mfma_f32_16x16x32_bf16 v[108:111], a[224:227], v[32:35], v[108:111]\n" \
  "v_mfma_f32_16x16x32_bf16 v[120:123], a[224:227], v[36:39], v[120:123]\n" \
  "s_waitcnt lgkmcnt(2)\n" \
  "v_mfma_f32_16x16x32_bf16 v[100:103], a[68:71], v[40:43], v[100:103]\n" \
  "ds_read_b128 v[32:35], v9 offset:19456\n" \
  "v_mfma_f32_16x16x32_bf16 v[112:115], a[68:71], v[44:47], v[112:115]\n" \
  "ds_read_b128 v[36:39], v9 offset:39936\n" \
  "v_mfma_f32_16x16x32_bf16 v[104:107], a[148:151], v[40:43], v[104:107]\n" \
  "v_readlane_b32 s69, v4, s52\n" \
  "v_mfma_f32_16x16x32_bf16 v[116:119], a[148:151], v[44:47], v[116:119]\n" \
  "v_readlane_b32 s71, v8, s52\n" \
  "v_mfma_f32_16x16x32_bf16 v[108:111], a[228:231], v[40:43], v[108:111]\n" \
  "s_add_u32 s52, s52, 1\n" \
  "v_mfma_f32_16x16x32_bf16 v[120:123], a[228:231], v[44:47], v[120:123]\n" \
  "s_waitcnt lgkmcnt(0)\n" \
  "s_barrier\n" \
  "v_mfma_f32_16x16x32_bf16 v[100:103], a[72:75], v[48:51], v[100:103]\n" \
  "ds_read_b128 v[40:43], v10 offset:0\n" \
  "v_mfma_f32_16x16x32_bf16 v[112:115], a[72:75], v[52:55], v[112:115]\n" \
  "ds_read_b128 v[44:47], v10 offset:20480\n" \
  "v_mfma_f32_16x16x32_bf16 v[104:107], a[152:155], v[48:51], v[104:107]\n" \
  "v_mfma_f32_16x16x32_bf16 v[116:119], a[152:155], v[52:55], v[116:119]\n" \
  "v_mfma_f32_16x16x32_bf16 v[108:111], a[232:235], v[48:51], v[108:111]\n" \
  "v_mfma_f32_16x16x32_bf16 v[120:123], a[232:235], v[52:55], v[120:123]\n" \
  "v_mfma_f32_16x16x32_bf16 v[100:103], a[76:79], v[32:35], v[100:103]\n" \
  "ds_read_b128 v[48:51], v10 offset:1024\n" \
  "v_mfma_f32_16x16x32_bf16 v[112:115], a[76:79], v[36:39], v[112:115]\n" \
  "ds_read_b128 v[52:55], v10 offset:21504\n" \
  "v_mfma_f32_16x16x32_bf16 v[104:107], a[156:159], v[32:35], v[104:107]\n" \
  "s_cmp_eq_u32 s52, s53\n" \
  "v_mfma_f32_16x16x32_bf16 v[116:119], a[156:159], v[36:39], v[116:119]\n" \
  "v_mfma_f32_16x16x32_bf16 v[108:111], a[236:239], v[32:35], v[108:111]\n" \
  "v_mfma_f32_16x16x32_bf16 v[120:123], a[236:239], v[36:39], v[120:123]\n" \
  "s_waitcnt lgkmcnt(2)\n" \
  "v_mfma_f32_16x16x32_bf16 v[56:59], a[0:3], v[40:43], 0\n" \
  "ds_read_b128 v[32:35], v10 offset:2048\n" \
  "v_mfma_f32_16x16x32_bf16 v[68:71], a[0:3], v[44:47], 0\n" \
  "ds_read_b128 v[36:39], v10 offset:22528\n" \
  "v_mfma_f32_16x16x32_bf16 v[60:63], a[80:83], v[40:43], 0\n" \
  "buffer_store_dwordx4 v[12:15], v7, s[40:43], s71 offen sc1\n" \
  "v_mfma_f32_16x16x32_bf16 v[72:75], a[80:83], v[44:47], 0\n" \
  "v_cvt_pk_bf16_f32 v16, v100, v101\n" \
  "v_mfma_f32_16x16x32_bf16 v[64:67], a[160:163], v[40:43], 0\n" \
  "v_cvt_pk_bf16_f32 v17, v102, v103\n" \
  "v_mfma_f32_16x16x32_bf16 v[76:79], a[160:163], v[44:47], 0\n" \
  "v_cvt_pk_bf16_f32 v18, v104, v105\n" \
  "s_waitcnt lgkmcnt(2)\n" \
  "v_mfma_f32_16x16x32_bf16 v[56:59], a[4:7], v[48:51], v[56:59]\n" \
  "ds_read_b128 v[40:43], v10 offset:3072\n" \
  "v_mfma_f32_16x16x32_bf16 v[68:71], a[4:7], v[52:55], v[68:71]\n" \
  "ds_read_b128 v[44:47], v10 offset:23552\n" \
  "v_mfma_f32_16x16x32_bf16 v[60:63], a[84:87], v[48:51], v[60:63]\n" \
  "v_cvt_pk_bf16_f32 v19, v106, v107\n" \
  "v_mfma_f32_16x16x32_bf16 v[72:75], a[84:87], v[52:55], v[72:75]\n" \
  "buffer_store_dwordx4 v[16:19], v2, s[40:43], s69 offen sc1\n" \
  "v_mfma_f32_16x16x32_bf16 v[64:67], a[164:167], v[48:51], v[64:67]\n" \
  "v_cvt_pk_bf16_f32 v20, v112, v113\n" \
  "v_mfma_f32_16x16x32_bf16 v[76:79], a[164:167], v[52:55], v[76:79]\n" \
  "v_cvt_pk_bf16_f32 v21, v114, v115\n" \
  "s_waitcnt lgkmcnt(2)\n" \
  "v_mfma_f32_16x16x32_bf16 v[56:59], a[8:11], v[32:35], v[56:59]\n" \
  "ds_read_b128 v[48:51], v10 offset:4096\n" \
  "v_mfma_f32_16x16x32_bf16 v[68:71], a[8:11], v[36:39], v[68:71]\n" \
  "ds_read_b128 v[52:55], v10 offset:24576\n" \
  "v_mfma_f32_16x16x32_bf16 v[60:63], a[88:91], v[32:35], v[60:63]\n" \
  "v_cvt_pk_bf16_f32 v22, v116, v117\n" \
  "v_mfma_f32_16x16x32_bf16 v[72:75], a[88:91], v[36:39], v[72:75]\n" \
  "v_cvt_pk_bf16_f32 v23, v118, v119\n" \
  "v_mfma_f32_16x16x32_bf16 v[64:67], a[168:171], v[32:35], v[64:67]\n" \
  "buffer_store_dwordx4 v[20:23], v2, s[40:43], s69 offen offset:1024 sc1\n" \
  "v_mfma_f32_16x16x32_bf16 v[76:79], a[168:171], v[36:39], v[76:79]\n" \
  "v_cvt_pk_bf16_f32 v24, v108, v109\n" \
  "s_waitcnt lgkmcnt(2)\n" \
  "v_mfma_f32_16x16x32_bf16 v[56:59], a[12:15], v[40:43], v[56:59]\n" \
  "ds_read_b128 v[32:35], v10 offset:5120\n" \
  "v_mfma_f32_16x16x32_bf16 v[68:71], a[12:15], v[44:47], v[68:71]\n" \
  "ds_read_b128 v[36:39], v10 offset:25600\n" \
  "v_mfma_f32_16x16x32_bf16 v[60:63], a[92:95], v[40:43], v[60:63]\n" \
  "v_cvt_pk_bf16_f32 v25, v110, v111\n" \
  "v_mfma_f32_16x16x32_bf16 v[72:75], a[92:95], v[44:47], v[72:75]\n" \
  "v_cvt_pk_bf16_f32 v26, v120, v121\n" \
  "v_mfma_f32_16x16x32_bf16 v[64:67], a[172:175], v[40:43], v[64:67]\n" \
  "v_cvt_pk_bf16_f32 v27, v122, v123\n" \
  "v_mfma_f32_16x16x32_bf16 v[76:79], a[172:175], v[44:47], v[76:79]\n" \
  "buffer_store_dwordx4 v[24:27], v3, s[40:43], s69 offen sc1\n" \
  "s_waitcnt lgkmcnt(2)\n" \
  "v_mfma_f32_16x16x32_bf16 v[56:59], a[16:19], v[48:51], v[56:59]\n" \
  "ds_read_b128 v[40:43], v10 offset:6144\n" \
  "v_mfma_f32_16x16x32_bf16 v[68:71], a[16:19], v[52:55], v[68:71]\n" \
  "ds_read_b128 v[44:47], v10 offset:26624\n" \
  "v_mfma_f32_16x16x32_bf16 v[60:63], a[96:99], v[48:51], v[60:63]\n" \
  "s_add_u32 s72, s68, 4096\n" \
  "v_mfma_f32_16x16x32_bf16 v[72:75], a[96:99], v[52:55], v[72:75]\n" \
  "s_add_u32 s73, s68, 8192\n" \
  "v_mfma_f32_16x16x32_bf16 v[64:67], a[176:179], v[48:51], v[64:67]\n" \
  "s_add_u32 m0, s54, 40960\n" \
  "v_mfma_f32_16x16x32_bf16 v[76:79], a[176:179], v[52:55], v[76:79]\n" \
  "buffer_load_dwordx4 v1, s[36:39], s68 offen offset:0 sc1 lds\n" \
  "s_waitcnt lgkmcnt(2)\n" \
  "v_mfma_f32_16x16x32_bf16 v[56:59], a[20:23], v[32:35], v[56:59]\n" \
  "ds_read_b128 v[48:51], v10 offset:7168\n" \
  "v_mfma_f32_16x16x32_bf16 v[68:71], a[20:23], v[36:39], v[68:71]\n" \
  "ds_read_b128 v[52:55], v10 offset:27648\n" \
  "v_mfma_f32_16x16x32_bf16 v[60:63], a[100:103], v[32:35], v[60:63]\n" \
  "buffer_load_dwordx4 v1, s[36:39], s68 offen offset:1024 sc1 lds\n" \
  "v_mfma_f32_16x16x32_bf16 v[72:75], a[100:103], v[36:39], v[72:75]\n" \
  "v_mfma_f32_16x16x32_bf16 v[64:67], a[180:183], v[32:35], v[64:67]\n" \
  "v_mfma_f32_16x16x32_bf16 v[76:79], a[180:183], v[36:39], v[76:79]\n" \
  "s_waitcnt lgkmcnt(2)\n" \
  "v_mfma_f32_16x16x32_bf16 v[56:59], a[24:27], v[40:43], v[56:59]\n" \
  "ds_read_b128 v[32:35], v10 offset:8192\n" \
  "v_mfma_f32_16x16x32_bf16 v[68:71], a[24:27], v[44:47], v[68:71]\n" \
  "ds_read_b128 v[36:39], v10 offset:28672\n" \
  "v_mfma_f32_16x16x32_bf16 v[60:63], a[104:107], v[40:43], v[60:63]\n" \
  "buffer_load_dwordx4 v1, s[36:39], s68 offen offset:2048 sc1 lds\n" \
  "v_mfma_f32_16x16x32_bf16 v[72:75], a[104:107], v[44:47], v[72:75]\n" \
  "v_mfma_f32_16x16x32_bf16 v[64:67], a[184:187], v[40:43], v[64:67]\n" \
  "v_mfma_f32_16x16x32_bf16 v[76:79], a[184:187], v[44:47], v[76:79]\n" \
  "s_waitcnt lgkmcnt(2)\n" \
  "v_mfma_f32_16x16x32_bf16 v[56:59], a[28:31], v[48:51], v[56:59]\n" \
  "ds_read_b128 v[40:43], v10 offset:9216\n" \
  "v_mfma_f32_16x16x32_bf16 v[68:71], a[28:31], v[52:55], v[68:71]\n" \
  "ds_read_b128 v[44:47], v10 offset:29696\n" \
  "v_mfma_f32_16x16x32_bf16 v[60:63], a[108:111], v[48:51], v[60:63]\n" \
  "buffer_load_dwordx4 v1, s[36:39], s68 offen offset:3072 sc1 lds\n" \
  "v_mfma_f32_16x16x32_bf16 v[72:75], a[108:111], v[52:55], v[72:75]\n" \
  "v_mfma_f32_16x16x32_bf16 v[64:67], a[188:191], v[48:51], v[64:67]\n" \
  "v_mfma_f32_16x16x32_bf16 v[76:79], a[188:191], v[52:55], v[76:79]\n" \
  "s_waitcnt lgkmcnt(2)\n" \
  "v_mfma_f32_16x16x32_bf16 v[56:59], a[32:35], v[32:35], v[56:59]\n" \
  "ds_read_b128 v[48:51], v10 offset:10240\n" \
  "v_mfma_f32_16x16x32_bf16 v[68:71], a[32:35], v[36:39], v[68:71]\n" \
  "ds_read_b128 v[52:55], v10 offset:30720\n" \
  "v_mfma_f32_16x16x32_bf16 v[60:63], a[112:115], v[32:35], v[60:63]\n" \
  "s_add_u32 m0, s54, 45056\n" \
  "v_mfma_f32_16x16x32_bf16 v[72:75], a[112:115], v[36:39], v[72:75]\n" \
  "buffer_load_dwordx4 v1, s[36:39], s72 offen offset:0 sc1 lds\n" \
  "v_mfma_f32_16x16x32_bf16 v[64:67], a[192:195], v[32:35], v[64:67]\n" \
  "v_mfma_f32_16x16x32_bf16 v[76:79], a[192:195], v[36:39], v[76:79]\n" \
  "s_waitcnt lgkmcnt(2)\n" \
  "v_mfma_f32_16x16x32_bf16 v[56:59], a[36:39], v[40:43], v[56:59]\n" \
  "ds_read_b128 v[32:35], v10 offset:11264\n" \
  "v_mfma_f32_16x16x32_bf16 v[68:71], a[36:39], v[44:47], v[68:71]\n" \
  "ds_read_b128 v[36:39], v10 offset:31744\n" \
  "v_mfma_f32_16x16x32_bf16 v[60:63], a[116:119], v[40:43], v[60:63]\n" \
  "buffer_load_dwordx4 v1, s[36:39], s72 offen offset:1024 sc1 lds\n" \
  "v_mfma_f32_16x16x32_bf16 v[72:75], a[116:119], v[44:47], v[72:75]\n" \
  "v_mfma_f32_16x16x32_bf16 v[64:67], a[196:199], v[40:43], v[64:67]\n" \
  "v_mfma_f32_16x16x32_bf16 v[76:79], a[196:199], v[44:47], v[76:79]\n" \
  "s_waitcnt lgkmcnt(2)\n" \
  "v_mfma_f32_16x16x32_bf16 v[56:59], a[40:43], v[48:51], v[56:59]\n" \
  "ds_read_b128 v[40:43], v10 offset:12288\n" \
  "v_mfma_f32_16x16x32_bf16 v[68:71], a[40:43], v[52:55], v[68:71]\n" \
  "ds_read_b128 v[44:47], v10 offset:32768\n" \
  "v_mfma_f32_16x16x32_bf16 v[60:63], a[120:123], v[48:51], v[60:63]\n" \
  "buffer_load_dwordx4 v1, s[36:39], s72 offen offset:2048 sc1 lds\n" \
  "v_mfma_f32_16x16x32_bf16 v[72:75], a[120:123], v[52:55], v[72:75]\n" \
  "v_mfma_f32_16x16x32_bf16 v[64:67], a[200:203], v[48:51], v[64:67]\n" \
  "v_mfma_f32_16x16x32_bf16 v[76:79], a[200:203], v[52:55], v[76:79]\n" \
  "s_waitcnt lgkmcnt(2)\n" \
  "v_mfma_f32_16x16x32_bf16 v[56:59], a[44:47], v[32:35], v[56:59]\n" \
  "ds_read_b128 v[48:51], v10 offset:13312\n" \
  "v_mfma_f32_16x16x32_bf16 v[68:71], a[44:47], v[36:39], v[68:71]\n" \
  "ds_read_b128 v[52:55], v10 offset:33792\n" \
  "v_mfma_f32_16x16x32_bf16 v[60:63], a[124:127], v[32:35], v[60:63]\n" \
  "buffer_load_dwordx4 v1, s[36:39], s72 offen offset:3072 sc1 lds\n" \
  "v_mfma_f32_16x16x32_bf16 v[72:75], a[124:127], v[36:39], v[72:75]\n" \
  "v_mfma_f32_16x16x32_bf16 v[64:67], a[204:207], v[32:35], v[64:67]\n" \
  "v_mfma_f32_16x16x32_bf16 v[76:79], a[204:207], v[36:39], v[76:79]\n" \
  "s_waitcnt lgkmcnt(2)\n" \
  "v_mfma_f32_16x16x32_bf16 v[56:59], a[48:51], v[40:43], v[56:59]\n" \
  "ds_read_b128 v[32:35], v10 offset:14336\n" \
  "v_mfma_f32_16x16x32_bf16 v[68:71], a[48:51], v[44:47], v[68:71]\n" \
  "ds_read_b128 v[36:39], v10 offset:34816\n" \
  "v_mfma_f32_16x16x32_bf16 v[60:63], a[128:131], v[40:43], v[60:63]\n" \
  "s_add_u32 m0, s54, 49152\n" \
  "v_mfma_f32_16x16x32_bf16 v[72:75], a[128:131], v[44:47], v[72:75]\n" \
  "buffer_load_dwordx4 v1, s[36:39], s73 offen offset:0 sc1 lds\n" \
  "v_mfma_f32_16x16x32_bf16 v[64:67], a[208:211], v[40:43], v[64:67]\n" \
  "v_mfma_f32_16x16x32_bf16 v[76:79], a[208:211], v[44:47], v[76:79]\n" \
  "s_waitcnt lgkmcnt(2)\n" \
  "v_mfma_f32_16x16x32_bf16 v[56:59], a[52:55], v[48:51], v[56:59]\n" \
  "ds_read_b128 v[40:43], v10 offset:15360\n" \
  "v_mfma_f32_16x16x32_bf16 v[68:71], a[52:55], v[52:55], v[68:71]\n" \
  "ds_read_b128 v[44:47], v10 offset:35840\n" \
  "v_mfma_f32_16x16x32_bf16 v[60:63], a[132:135], v[48:51], v[60:63]\n" \
  "buffer_load_dwordx4 v1, s[36:39], s73 offen offset:1024 sc1 lds\n" \
  "v_mfma_f32_16x16x32_bf16 v[72:75], a[132:135], v[52:55], v[72:75]\n" \
  "v_mfma_f32_16x16x32_bf16 v[64:67], a[212:215], v[48:51], v[64:67]\n" \
  "v_mfma_f32_16x16x32_bf16 v[76:79], a[212:215], v[52:55], v[76:79]\n" \
  "s_waitcnt lgkmcnt(2)\n" \
  "v_mfma_f32_16x16x32_bf16 v[56:59], a[56:59], v[32:35], v[56:59]\n" \
  "ds_read_b128 v[48:51], v10 offset:16384\n" \
  "v_mfma_f32_16x16x32_bf16 v[68:71], a[56:59], v[36:39], v[68:71]\n" \
  "ds_read_b128 v[52:55], v10 offset:36864\n" \
  "v_mfma_f32_16x16x32_bf16 v[60:63], a[136:139], v[32:35], v[60:63]\n" \
  "s_add_u32 s68, s68, 40960\n" \
  "v_mfma_f32_16x16x32_bf16 v[72:75], a[136:139], v[36:39], v[72:75]\n" \
  "v_mfma_f32_16x16x32_bf16 v[64:67], a[216:219], v[32:35], v[64:67]\n" \
  "v_mfma_f32_16x16x32_bf16 v[76:79], a[216:219], v[36:39], v[76:79]\n" \
  "s_waitcnt lgkmcnt(2)\n" \
  "v_mfma_f32_16x16x32_bf16 v[56:59], a[60:63], v[40:43], v[56:59]\n" \
  "ds_read_b128 v[32:35], v10 offset:17408\n" \
  "v_mfma_f32_16x16x32_bf16 v[68:71], a[60:63], v[44:47], v[68:71]\n" \
  "ds_read_b128 v[36:39], v10 offset:37888\n" \
  "v_mfma_f32_16x16x32_bf16 v[60:63], a[140:143], v[40:43], v[60:63]\n" \
  "v_mov_b32 v11, s52\n" \
  "v_mfma_f32_16x16x32_bf16 v[72:75], a[140:143], v[44:47], v[72:75]\n" \
  "s_waitcnt vmcnt(10)\n" \
  "v_mfma_f32_16x16x32_bf16 v[64:67], a[220:223], v[40:43], v[64:67]\n" \
  "buffer_store_dword v11, v5, s[40:43], 0 offen sc1\n" \
  "v_mfma_f32_16x16x32_bf16 v[76:79], a[220:223], v[44:47], v[76:79]\n" \
  "s_waitcnt lgkmcnt(2)\n" \
  "v_mfma_f32_16x16x32_bf16 v[56:59], a[64:67], v[48:51], v[56:59]\n" \
  "ds_read_b128 v[40:43], v10 offset:18432\n" \
  "v_mfma_f32_16x16x32_bf16 v[68:71], a[64:67], v[52:55], v[68:71]\n" \
  "ds_read_b128 v[44:47], v10 offset:38912\n" \
  "v_mfma_f32_16x16x32_bf16 v[60:63], a[144:147], v[48:51], v[60:63]\n" \
  "v_mfma_f32_16x16x32_bf16 v[72:75], a[144:147], v[52:55], v[72:75]\n" \
  "v_mfma_f32_16x16x32_bf16 v[64:67], a[224:227], v[48:51], v[64:67]\n" \
  "v_mfma_f32_16x16x32_bf16 v[76:79], a[224:227], v[52:55], v[76:79]\n" \
  "s_waitcnt lgkmcnt(2)\n" \
  "v_mfma_f32_16x16x32_bf16 v[56:59], a[68:71], v[32:35], v[56:59]\n" \
  "ds_read_b128 v[48:51], v10 offset:19456\n" \
  "v_mfma_f32_16x16x32_bf16 v[68:71], a[68:71], v[36:39], v[68:71]\n" \
  "ds_read_b128 v[52:55], v10 offset:39936\n" \
  "v_mfma_f32_16x16x32_bf16 v[60:63], a[148:151], v[32:35], v[60:63]\n" \
  "v_readlane_b32 s69, v4, s52\n" \
  "v_mfma_f32_16x16x32_bf16 v[72:75], a[148:151], v[36:39], v[72:75]\n" \
  "v_readlane_b32 s71, v8, s52\n" \
  "v_mfma_f32_16x16x32_bf16 v[64:67], a[228:231], v[32:35], v[64:67]\n" \
  "s_add_u32 s52, s52, 1\n" \
  "v_mfma_f32_16x16x32_bf16 v[76:79], a[228:231], v[36:39], v[76:79]\n" \
  "s_waitcnt lgkmcnt(0)\n" \
  "s_barrier\n" \
  "v_mfma_f32_16x16x32_bf16 v[56:59], a[72:75], v[40:43], v[56:59]\n" \
  "ds_read_b128 v[32:35], v0 offset:0\n" \
  "v_mfma_f32_16x16x32_bf16 v[68:71], a[72:75], v[44:47], v[68:71]\n" \
  "ds_read_b128 v[36:39], v0 offset:20480\n" \
  "v_mfma_f32_16x16x32_bf16 v[60:63], a[152:155], v[40:43], v[60:63]\n" \
  "v_mfma_f32_16x16x32_bf16 v[72:75], a[152:155], v[44:47], v[72:75]\n" \
  "v_mfma_f32_16x16x32_bf16 v[64:67], a[232:235], v[40:43], v[64:67]\n" \
  "v_mfma_f32_16x16x32_bf16 v[76:79], a[232:235], v[44:47], v[76:79]\n" \
  "v_mfma_f32_16x16x32_bf16 v[56:59], a[76:79], v[48:51], v[56:59]\n" \
  "ds_read_b128 v[40:43], v0 offset:1024\n" \
  "v_mfma_f32_16x16x32_bf16 v[68:71], a[76:79], v[52:55], v[68:71]\n" \
  "ds_read_b128 v[44:47], v0 offset:21504\n" \
  "v_mfma_f32_16x16x32_bf16 v[60:63], a[156:159], v[48:51], v[60:63]\n" \
  "s_cmp_eq_u32 s52, s53\n" \
  "v_mfma_f32_16x16x32_bf16 v[72:75], a[156:159], v[52:55], v[72:75]\n" \
  "v_mfma_f32_16x16x32_bf16 v[64:67], a[236:239], v[48:51], v[64:67]\n" \
  "v_mfma_f32_16x16x32_bf16 v[76:79], a[236:239], v[52:55], v[76:79]\n" \
  "s_waitcnt lgkmcnt(2)\n" \
  "v_mfma_f32_16x16x32_bf16 v[100:103], a[0:3], v[32:35], 0\n" \
  "ds_read_b128 v[48:51], v0 offset:2048\n" \
  "v_mfma_f32_16x16x32_bf16 v[112:115], a[0:3], v[36:39], 0\n" \
  "ds_read_b128 v[52:55], v0 offset:22528\n" \
  "v_mfma_f32_16x16x32_bf16 v[104:107], a[80:83], v[32:35], 0\n" \
  "buffer_store_dwordx4 v[12:15], v7, s[40:43], s71 offen sc1\n" \
  "v_mfma_f32_16x16x32_bf16 v[116:119], a[80:83], v[36:39], 0\n" \
  "v_cvt_pk_bf16_f32 v16, v56, v57\n" \
  "v_mfma_f32_16x16x32_bf16 v[108:111], a[160:163], v[32:35], 0\n" \
  "v_cvt_pk_bf16_f32 v17, v58, v59\n" \
  "v_mfma_f32_16x16x32_bf16 v[120:123], a[160:163], v[36:39], 0\n" \
  "v_cvt_pk_bf16_f32 v18, v60, v61\n" \
  "s_waitcnt lgkmcnt(2)\n" \
  "v_mfma_f32_16x16x32_bf16 v[100:103], a[4:7], v[40:43], v[100:103]\n" \
  "ds_read_b128 v[32:35], v0 offset:3072\n" \
  "v_mfma_f32_16x16x32_bf16 v[112:115], a[4:7], v[44:47], v[112:115]\n" \
  "ds_read_b128 v[36:39], v0 offset:23552\n" \
  "v_mfma_f32_16x16x32_bf16 v[104:107], a[84:87], v[40:43], v[104:107]\n" \
  "v_cvt_pk_bf16_f32 v19, v62, v63\n" \
  "v_mfma_f32_16x16x32_bf16 v[116:119], a[84:87], v[44:47], v[116:119]\n" \
  "buffer_store_dwordx4 v[16:19], v2, s[40:43], s69 offen sc1\n" \
  "v_mfma_f32_16x16x32_bf16 v[108:111], a[164:167], v[40:43], v[108:111]\n" \
  "v_cvt_pk_bf16_f32 v20, v68, v69\n" \
  "v_mfma_f32_16x16x32_bf16 v[120:123], a[164:167], v[44:47], v[120:123]\n" \
  "v_cvt_pk_bf16_f32 v21, v70, v71\n" \
  "s_waitcnt lgkmcnt(2)\n" \
  "v_mfma_f32_16x16x32_bf16 v[100:103], a[8:11], v[48:51], v[100:103]\n" \
  "ds_read_b128 v[40:43], v0 offset:4096\n" \
  "v_mfma_f32_16x16x32_bf16 v[112:115], a[8:11], v[52:55], v[112:115]\n" \
  "ds_read_b128 v[44:47], v0 offset:24576\n" \
  "v_mfma_f32_16x16x32_bf16 v[104:107], a[88:91], v[48:51], v[104:107]\n" \
  "v_cvt_pk_bf16_f32 v22, v72, v73\n" \
  "v_mfma_f32_16x16x32_bf16 v[116:119], a[88:91], v[52:55], v[116:119]\n" \
  "v_cvt_pk_bf16_f32 v23, v74, v75\n" \
  "v_mfma_f32_16x16x32_bf16 v[108:111], a[168:171], v[48:51], v[108:111]\n" \
  "buffer_store_dwordx4 v[20:23], v2, s[40:43], s69 offen offset:1024 sc1\n" \
  "v_mfma_f32_16x16x32_bf16 v[120:123], a[168:171], v[52:55], v[120:123]\n" \
  "v_cvt_pk_bf16_f32 v24, v64, v65\n" \
  "s_waitcnt lgkmcnt(2)\n" \
  "v_mfma_f32_16x16x32_bf16 v[100:103], a[12:15], v[32:35], v[100:103]\n" \
  "ds_read_b128 v[48:51], v0 offset:5120\n" \
  "v_mfma_f32_16x16x32_bf16 v[112:115], a[12:15], v[36:39], v[112:115]\n" \
  "ds_read_b128 v[52:55], v0 offset:25600\n" \
  "v_mfma_f32_16x16x32_bf16 v[104:107], a[92:95], v[32:35], v[104:107]\n" \
  "v_cvt_pk_bf16_f32 v25, v66, v67\n" \
  "v_mfma_f32_16x16x32_bf16 v[116:119], a[92:95], v[36:39], v[116:119]\n" \
  "v_cvt_pk_bf16_f32 v26, v76, v77\n" \
  "v_mfma_f32_16x16x32_bf16 v[108:111], a[172:175], v[32:35], v[108:111]\n" \
  "v_cvt_pk_bf16_f32 v27, v78, v79\n" \
  "v_mfma_f32_16x16x32_bf16 v[120:123], a[172:175], v[36:39], v[120:123]\n" \
  "buffer_store_dwordx4 v[24:27], v3, s[40:43], s69 offen sc1\n" \
  "s_waitcnt lgkmcnt(2)\n" \
  "v_mfma_f32_16x16x32_bf16 v[100:103], a[16:19], v[40:43], v[100:103]\n" \
  "ds_read_b128 v[32:35], v0 offset:6144\n" \
  "v_mfma_f32_16x16x32_bf16 v[112:115], a[16:19], v[44:47], v[112:115]\n" \
  "ds_read_b128 v[36:39], v0 offset:26624\n" \
  "v_mfma_f32_16x16x32_bf16 v[104:107], a[96:99], v[40:43], v[104:107]\n" \
  "s_add_u32 s72, s68, 4096\n" \
  "v_mfma_f32_16x16x32_bf16 v[116:119], a[96:99], v[44:47], v[116:119]\n" \
  "s_add_u32 s73, s68, 8192\n" \
  "v_mfma_f32_16x16x32_bf16 v[108:111], a[176:179], v[40:43], v[108:111]\n" \
  "s_add_u32 m0, s54, 81920\n" \
  "v_mfma_f32_16x16x32_bf16 v[120:123], a[176:179], v[44:47], v[120:123]\n" \
  "buffer_load_dwordx4 v1, s[36:39], s68 offen offset:0 sc1 lds\n" \
  "s_waitcnt lgkmcnt(2)\n" \
  "v_mfma_f32_16x16x32_bf16 v[100:103], a[20:23], v[48:51], v[100:103]\n" \
  "ds_read_b128 v[40:43], v0 offset:7168\n" \
  "v_mfma_f32_16x16x32_bf16 v[112:115], a[20:23], v[52:55], v[112:115]\n" \
  "ds_read_b128 v[44:47], v0 offset:27648\n" \
  "v_mfma_f32_16x16x32_bf16 v[104:107], a[100:103], v[48:51], v[104:107]\n" \
  "buffer_load_dwordx4 v1, s[36:39], s68 offen offset:1024 sc1 lds\n" \
  "v_mfma_f32_16x16x32_bf16 v[116:119], a[100:103], v[52:55], v[116:119]\n" \
  "v_mfma_f32_16x16x32_bf16 v[108:111], a[180:183], v[48:51], v[108:111]\n" \
  "v_mfma_f32_16x16x32_bf16 v[120:123], a[180:183], v[52:55], v[120:123]\n" \
  "s_waitcnt lgkmcnt(2)\n" \
  "v_mfma_f32_16x16x32_bf16 v[100:103], a[24:27], v[32:35], v[100:103]\n" \
  "ds_read_b128 v[48:51], v0 offset:8192\n" \
  "v_mfma_f32_16x16x32_bf16 v[112:115], a[24:27], v[36:39], v[112:115]\n" \
  "ds_read_b128 v[52:55], v0 offset:28672\n" \
  "v_mfma_f32_16x16x32_bf16 v[104:107], a[104:107], v[32:35], v[104:107]\n" \
  "buffer_load_dwordx4 v1, s[36:39], s68 offen offset:2048 sc1 lds\n" \
  "v_mfma_f32_16x16x32_bf16 v[116:119], a[104:107], v[36:39], v[116:119]\n" \
  "v_mfma_f32_16x16x32_bf16 v[108:111], a[184:187], v[32:35], v[108:111]\n" \
  "v_mfma_f32_16x16x32_bf16 v[120:123], a[184:187], v[36:39], v[120:123]\n" \
  "s_waitcnt lgkmcnt(2)\n" \
  "v_mfma_f32_16x16x32_bf16 v[100:103], a[28:31], v[40:43], v[100:103]\n" \
  "ds_read_b128 v[32:35], v0 offset:9216\n" \
  "v_mfma_f32_16x16x32_bf16 v[112:115], a[28:31], v[44:47], v[112:115]\n" \
  "ds_read_b128 v[36:39], v0 offset:29696\n" \
  "v_mfma_f32_16x16x32_bf16 v[104:107], a[108:111], v[40:43], v[104:107]\n" \
  "buffer_load_dwordx4 v1, s[36:39], s68 offen offset:3072 sc1 lds\n" \
  "v_mfma_f32_16x16x32_bf16 v[116:119], a[108:111], v[44:47], v[116:119]\n" \
  "v_mfma_f32_16x16x32_bf16 v[108:111], a[188:191], v[40:43], v[108:111]\n" \
  "v_mfma_f32_16x16x32_bf16 v[120:123], a[188:191], v[44:47], v[120:123]\n" \
  "s_waitcnt lgkmcnt(2)\n" \
  "v_mfma_f32_16x16x32_bf16 v[100:103], a[32:35], v[48:51], v[100:103]\n" \
  "ds_read_b128 v[40:43], v0 offset:10240\n" \
  "v_mfma_f32_16x16x32_bf16 v[112:115], a[32:35], v[52:55], v[112:115]\n" \
  "ds_read_b128 v[44:47], v0 offset:30720\n" \
  "v_mfma_f32_16x16x32_bf16 v[104:107], a[112:115], v[48:51], v[104:107]\n" \
  "s_add_u32 m0, s54, 86016\n" \
  "v_mfma_f32_16x16x32_bf16 v[116:119], a[112:115], v[52:55], v[116:119]\n" \
  "buffer_load_dwordx4 v1, s[36:39], s72 offen offset:0 sc1 lds\n" \
  "v_mfma_f32_16x16x32_bf16 v[108:111], a[192:195], v[48:51], v[108:111]\n" \
  "v_mfma_f32_16x16x32_bf16 v[120:123], a[192:195], v[52:55], v[120:123]\n" \
  "s_waitcnt lgkmcnt(2)\n" \
  "v_mfma_f32_16x16x32_bf16 v[100:103], a[36:39], v[32:35], v[100:103]\n" \
  "ds_read_b128 v[48:51], v0 offset:11264\n" \
  "v_mfma_f32_16x16x32_bf16 v[112:115], a[36:39], v[36:39], v[112:115]\n" \
  "ds_read_b128 v[52:55], v0 offset:31744\n" \
  "v_mfma_f32_16x16x32_bf16 v[104:107], a[116:119], v[32:35], v[104:107]\n" \
  "buffer_load_dwordx4 v1, s[36:39], s72 offen offset:1024 sc1 lds\n" \
  "v_mfma_f32_16x16x32_bf16 v[116:119], a[116:119], v[36:39], v[116:119]\n" \
  "v_mfma_f32_16x16x32_bf16 v[108:111], a[196:199], v[32:35], v[108:111]\n" \
  "v_mfma_f32_16x16x32_bf16 v[120:123], a[196:199], v[36:39], v[120:123]\n" \
  "s_waitcnt lgkmcnt(2)\n" \
  "v_mfma_f32_16x16x32_bf16 v[100:103], a[40:43], v[40:43], v[100:103]\n" \
  "ds_read_b128 v[32:35], v0 offset:12288\n" \
  "v_mfma_f32_16x16x32_bf16 v[112:115], a[40:43], v[44:47], v[112:115]\n" \
  "ds_read_b128 v[36:39], v0 offset:32768\n" \
  "v_mfma_f32_16x16x32_bf16 v[104:107], a[120:123], v[40:43], v[104:107]\n" \
  "buffer_load_dwordx4 v1, s[36:39], s72 offen offset:2048 sc1 lds\n" \
  "v_mfma_f32_16x16x32_bf16 v[116:119], a[120:123], v[44:47], v[116:119]\n" \
  "v_mfma_f32_16x16x32_bf16 v[108:111], a[200:203], v[40:43], v[108:111]\n" \
  "v_mfma_f32_16x16x32_bf16 v[120:123], a[200:203], v[44:47], v[120:123]\n" \
  "s_waitcnt lgkmcnt(2)\n" \
  "v_mfma_f32_16x16x32_bf16 v[100:103], a[44:47], v[48:51], v[100:103]\n" \
  "ds_read_b128 v[40:43], v0 offset:13312\n" \
  "v_mfma_f32_16x16x32_bf16 v[112:115], a[44:47], v[52:55], v[112:115]\n" \
  "ds_read_b128 v[44:47], v0 offset:33792\n" \
  "v_mfma_f32_16x16x32_bf16 v[104:107], a[124:127], v[48:51], v[104:107]\n" \
  "buffer_load_dwordx4 v1, s[36:39], s72 offen offset:3072 sc1 lds\n" \
  "v_mfma_f32_16x16x32_bf16 v[116:119], a[124:127], v[52:55], v[116:119]\n" \
  "v_mfma_f32_16x16x32_bf16 v[108:111], a[204:207], v[48:51], v[108:111]\n" \
  "v_mfma_f32_16x16x32_bf16 v[120:123], a[204:207], v[52:55], v[120:123]\n" \
  "s_waitcnt lgkmcnt(2)\n" \
  "v_mfma_f32_16x16x32_bf16 v[100:103], a[48:51], v[32:35], v[100:103]\n" \
  "ds_read_b128 v[48:51], v0 offset:14336\n" \
  "v_mfma_f32_16x16x32_bf16 v[112:115], a[48:51], v[36:39], v[112:115]\n" \
  "ds_read_b128 v[52:55], v0 offset:34816\n" \
  "v_mfma_f32_16x16x32_bf16 v[104:107], a[128:131], v[32:35], v[104:107]\n" \
  "s_add_u32 m0, s54, 90112\n" \
  "v_mfma_f32_16x16x32_bf16 v[116:119], a[128:131], v[36:39], v[116:119]\n" \
  "buffer_load_dwordx4 v1, s[36:39], s73 offen offset:0 sc1 lds\n" \
  "v_mfma_f32_16x16x32_bf16 v[108:111], a[208:211], v[32:35], v[108:111]\n" \
  "v_mfma_f32_16x16x32_bf16 v[120:123], a[208:211], v[36:39], v[120:123]\n" \
  "s_waitcnt lgkmcnt(2)\n" \
  "v_mfma_f32_16x16x32_bf16 v[100:103], a[52:55], v[40:43], v[100:103]\n" \
  "ds_read_b128 v[32:35], v0 offset:15360\n" \
  "v_mfma_f32_16x16x32_bf16 v[112:115], a[52:55], v[44:47], v[112:115]\n" \
  "ds_read_b128 v[36:39], v0 offset:35840\n" \
  "v_mfma_f32_16x16x32_bf16 v[104:107], a[132:135], v[40:43], v[104:107]\n" \
  "buffer_load_dwordx4 v1, s[36:39], s73 offen offset:1024 sc1 lds\n" \
  "v_mfma_f32_16x16x32_bf16 v[116:119], a[132:135], v[44:47], v[116:119]\n" \
  "v_mfma_f32_16x16x32_bf16 v[108:111], a[212:215], v[40:43], v[108:111]\n" \
  "v_mfma_f32_16x16x32_bf16 v[120:123], a[212:215], v[44:47], v[120:123]\n" \
  "s_waitcnt lgkmcnt(2)\n" \
  "v_mfma_f32_16x16x32_bf16 v[100:103], a[56:59], v[48:51], v[100:103]\n" \
  "ds_read_b128 v[40:43], v0 offset:16384\n" \
  "v_mfma_f32_16x16x32_bf16 v[112:115], a[56:59], v[52:55], v[112:115]\n" \
  "ds_read_b128 v[44:47], v0 offset:36864\n" \
  "v_mfma_f32_16x16x32_bf16 v[104:107], a[136:139], v[48:51], v[104:107]\n" \
  "s_add_u32 s68, s68, 40960\n" \
  "v_mfma_f32_16x16x32_bf16 v[116:119], a[136:139], v[52:55], v[116:119]\n" \
  "v_mfma_f32_16x16x32_bf16 v[108:111], a[216:219], v[48:51], v[108:111]\n" \
  "v_mfma_f32_16x16x32_bf16 v[120:123], a[216:219], v[52:55], v[120:123]\n" \
  "s_waitcnt lgkmcnt(2)\n" \
  "v_mfma_f32_16x16x32_bf16 v[100:103], a[60:63], v[32:35], v[100:103]\n" \
  "ds_read_b128 v[48:51], v0 offset:17408\n" \
  "v_mfma_f32_16x16x32_bf16 v[112:115], a[60:63], v[36:39], v[112:115]\n" \
  "ds_read_b128 v[52:55], v0 offset:37888\n" \
  "v_mfma_f32_16x16x32_bf16 v[104:107], a[140:143], v[32:35], v[104:107]\n" \
  "v_mov_b32 v11, s52\n" \
  "v_mfma_f32_16x16x32_bf16 v[116:119], a[140:143], v[36:39], v[116:119]\n" \
  "s_waitcnt vmcnt(10)\n" \
  "v_mfma_f32_16x16x32_bf16 v[108:111], a[220:223], v[32:35], v[108:111]\n" \
  "buffer_store_dword v11, v5, s[40:43], 0 offen sc1\n" \
  "v_mfma_f32_16x16x32_bf16 v[120:123], a[220:223], v[36:39], v[120:123]\n" \
  "s_waitcnt lgkmcnt(2)\n" \
  "v_mfma_f32_16x16x32_bf16 v[100:103], a[64:67], v[40:43], v[100:103]\n" \
  "ds_read_b128 v[32:35], v0 offset:18432\n" \
  "v_mfma_f32_16x16x32_bf16 v[112:115], a[64:67], v[44:47], v[112:115]\n" \
  "ds_read_b128 v[36:39], v0 offset:38912\n" \
  "v_mfma_f32_16x16x32_bf16 v[104:107], a[144:147], v[40:43], v[104:107]\n" \
  "v_mfma_f32_16x16x32_bf16 v[116:119], a[144:147], v[44:47], v[116:119]\n" \
  "v_mfma_f32_16x16x32_bf16 v[108:111], a[224:227], v[40:43], v[108:111]\n" \
  "v_mfma_f32_16x16x32_bf16 v[120:123], a[224:227], v[44:47], v[120:123]\n" \
  "s_waitcnt lgkmcnt(2)\n" \
  "v_mfma_f32_16x16x32_bf16 v[100:103], a[68:71], v[48:51], v[100:103]\n" \
  "ds_read_b128 v[40:43], v0 offset:19456\n" \
  "v_mfma_f32_16x16x32_bf16 v[112:115], a[68:71], v[52:55], v[112:115]\n" \
  "ds_read_b128 v[44:47], v0 offset:39936\n" \
  "v_mfma_f32_16x16x32_bf16 v[104:107], a[148:151], v[48:51], v[104:107]\n" \
  "v_readlane_b32 s69, v4, s52\n" \
  "v_mfma_f32_16x16x32_bf16 v[116:119], a[148:151], v[52:55], v[116:119]\n" \
  "v_readlane_b32 s71, v8, s52\n" \
  "v_mfma_f32_16x16x32_bf16 v[108:111], a[228:231], v[48:51], v[108:111]\n" \
  "s_add_u32 s52, s52, 1\n" \
  "v_mfma_f32_16x16x32_bf16 v[120:123], a[228:231], v[52:55], v[120:123]\n" \
  "s_waitcnt lgkmcnt(0)\n" \
  "s_barrier\n" \
  "v_mfma_f32_16x16x32_bf16 v[100:103], a[72:75], v[32:35], v[100:103]\n" \
  "ds_read_b128 v[48:51], v9 offset:0\n" \
  "v_mfma_f32_16x16x32_bf16 v[112:115], a[72:75], v[36:39], v[112:115]\n" \
  "ds_read_b128 v[52:55], v9 offset:20480\n" \
  "v_mfma_f32_16x16x32_bf16 v[104:107], a[152:155], v[32:35], v[104:107]\n" \
  "v_mfma_f32_16x16x32_bf16 v[116:119], a[152:155], v[36:39], v[116:119]\n" \
  "v_mfma_f32_16x16x32_bf16 v[108:111], a[232:235], v[32:35], v[108:111]\n" \
  "v_mfma_f32_16x16x32_bf16 v[120:123], a[232:235], v[36:39], v[120:123]\n" \
  "v_mfma_f32_16x16x32_bf16 v[100:103], a[76:79], v[40:43], v[100:103]\n" \
  "ds_read_b128 v[32:35], v9 offset:1024\n" \
  "v_mfma_f32_16x16x32_bf16 v[112:115], a[76:79], v[44:47], v[112:115]\n" \
  "ds_read_b128 v[36:39], v9 offset:21504\n" \
  "v_mfma_f32_16x16x32_bf16 v[104:107], a[156:159], v[40:43], v[104:107]\n" \
  "s_cmp_eq_u32 s52, s53\n" \
  "v_mfma_f32_16x16x32_bf16 v[116:119], a[156:159], v[44:47], v[116:119]\n" \
  "v_mfma_f32_16x16x32_bf16 v[108:111], a[236:239], v[40:43], v[108:111]\n" \
  "v_mfma_f32_16x16x32_bf16 v[120:123], a[236:239], v[44:47], v[120:123]\n" \
  "s_cbranch_scc1 LPE_%=\n" \
  "s_waitcnt lgkmcnt(2)\n" \
  "v_mfma_f32_16x16x32_bf16 v[56:59], a[0:3], v[48:51], 0\n" \
  "ds_read_b128 v[40:43], v9 offset:2048\n" \
  "v_mfma_f32_16x16x32_bf16 v[68:71], a[0:3], v[52:55], 0\n" \
  "ds_read_b128 v[44:47], v9 offset:22528\n" \
  "v_mfma_f32_16x16x32_bf16 v[60:63], a[80:83], v[48:51], 0\n" \
  "buffer_store_dwordx4 v[12:15], v7, s[40:43], s71 offen sc1\n" \
  "v_mfma_f32_16x16x32_bf16 v[72:75], a[80:83], v[52:55], 0\n" \
  "v_cvt_pk_bf16_f32 v16, v100, v101\n" \
  "v_mfma_f32_16x16x32_bf16 v[64:67], a[160:163], v[48:51], 0\n" \
  "v_cvt_pk_bf16_f32 v17, v102, v103\n" \
  "v_mfma_f32_16x16x32_bf16 v[76:79], a[160:163], v[52:55], 0\n" \
  "v_cvt_pk_bf16_f32 v18, v104, v105\n" \
  "s_waitcnt lgkmcnt(2)\n" \
  "v_mfma_f32_16x16x32_bf16 v[56:59], a[4:7], v[32:35], v[56:59]\n" \
  "ds_read_b128 v[48:51], v9 offset:3072\n" \
  "v_mfma_f32_16x16x32_bf16 v[68:71], a[4:7], v[36:39], v[68:71]\n" \
  "ds_read_b128 v[52:55], v9 offset:23552\n" \
  "v_mfma_f32_16x16x32_bf16 v[60:63], a[84:87], v[32:35], v[60:63]\n" \
  "v_cvt_pk_bf16_f32 v19, v106, v107\n" \
  "v_mfma_f32_16x16x32_bf16 v[72:75], a[84:87], v[36:39], v[72:75]\n" \
  "buffer_store_dwordx4 v[16:19], v2, s[40:43], s69 offen sc1\n" \
  "v_mfma_f32_16x16x32_bf16 v[64:67], a[164:167], v[32:35], v[64:67]\n" \
  "v_cvt_pk_bf16_f32 v20, v112, v113\n" \
  "v_mfma_f32_16x16x32_bf16 v[76:79], a[164:167], v[36:39], v[76:79]\n" \
  "v_cvt_pk_bf16_f32 v21, v114, v115\n" \
  "s_waitcnt lgkmcnt(2)\n" \
  "v_mfma_f32_16x16x32_bf16 v[56:59], a[8:11], v[40:43], v[56:59]\n" \
  "ds_read_b128 v[32:35], v9 offset:4096\n" \
  "v_mfma_f32_16x16x32_bf16 v[68:71], a[8:11], v[44:47], v[68:71]\n" \
  "ds_read_b128 v[36:39], v9 offset:24576\n" \
  "v_mfma_f32_16x16x32_bf16 v[60:63], a[88:91], v[40:43], v[60:63]\n" \
  "v_cvt_pk_bf16_f32 v22, v116, v117\n" \
  "v_mfma_f32_16x16x32_bf16 v[72:75], a[88:91], v[44:47], v[72:75]\n" \
  "v_cvt_pk_bf16_f32 v23, v118, v119\n" \
  "v_mfma_f32_16x16x32_bf16 v[64:67], a[168:171], v[40:43], v[64:67]\n" \
  "buffer_store_dwordx4 v[20:23], v2, s[40:43], s69 offen offset:1024 sc1\n" \
  "v_mfma_f32_16x16x32_bf16 v[76:79], a[168:171], v[44:47], v[76:79]\n" \
  "v_cvt_pk_bf16_f32 v24, v108, v109\n" \
  "s_waitcnt lgkmcnt(2)\n" \
  "v_mfma_f32_16x16x32_bf16 v[56:59], a[12:15], v[48:51], v[56:59]\n" \
  "ds_read_b128 v[40:43], v9 offset:5120\n" \
  "v_mfma_f32_16x16x32_bf16 v[68:71], a[12:15], v[52:55], v[68:71]\n" \
  "ds_read_b128 v[44:47], v9 offset:25600\n" \
  "v_mfma_f32_16x16x32_bf16 v[60:63], a[92:95], v[48:51], v[60:63]\n" \
  "v_cvt_pk_bf16_f32 v25, v110, v111\n" \
  "v_mfma_f32_16x16x32_bf16 v[72:75], a[92:95], v[52:55], v[72:75]\n" \
  "v_cvt_pk_bf16_f32 v26, v120, v121\n" \
  "v_mfma_f32_16x16x32_bf16 v[64:67], a[172:175], v[48:51], v[64:67]\n" \
  "v_cvt_pk_bf16_f32 v27, v122, v123\n" \
  "v_mfma_f32_16x16x32_bf16 v[76:79], a[172:175], v[52:55], v[76:79]\n" \
  "buffer_store_dwordx4 v[24:27], v3, s[40:43], s69 offen sc1\n" \
  "s_waitcnt lgkmcnt(2)\n" \
  "v_mfma_f32_16x16x32_bf16 v[56:59], a[16:19], v[32:35], v[56:59]\n" \
  "ds_read_b128 v[48:51], v9 offset:6144\n" \
  "v_mfma_f32_16x16x32_bf16 v[68:71], a[16:19], v[36:39], v[68:71]\n" \
  "ds_read_b128 v[52:55], v9 offset:26624\n" \
  "v_mfma_f32_16x16x32_bf16 v[60:63], a[96:99], v[32:35], v[60:63]\n" \
  "s_add_u32 s72, s68, 4096\n" \
  "v_mfma_f32_16x16x32_bf16 v[72:75], a[96:99], v[36:39], v[72:75]\n" \
  "s_add_u32 s73, s68, 8192\n" \
  "v_mfma_f32_16x16x32_bf16 v[64:67], a[176:179], v[32:35], v[64:67]\n" \
  "s_add_u32 m0, s54, 0\n" \
  "v_mfma_f32_16x16x32_bf16 v[76:79], a[176:179], v[36:39], v[76:79]\n" \
  "buffer_load_dwordx4 v1, s[36:39], s68 offen offset:0 sc1 lds\n" \
  "s_waitcnt lgkmcnt(2)\n" \
  "v_mfma_f32_16x16x32_bf16 v[56:59], a[20:23], v[40:43], v[56:59]\n" \
  "ds_read_b128 v[32:35], v9 offset:7168\n" \
  "v_mfma_f32_16x16x32_bf16 v[68:71], a[20:23], v[44:47], v[68:71]\n" \
  "ds_read_b128 v[36:39], v9 offset:27648\n" \
  "v_mfma_f32_16x16x32_bf16 v[60:63], a[100:103], v[40:43], v[60:63]\n" \
  "buffer_load_dwordx4 v1, s[36:39], s68 offen offset:1024 sc1 lds\n" \
  "v_mfma_f32_16x16x32_bf16 v[72:75], a[100:103], v[44:47], v[72:75]\n" \
  "v_mfma_f32_16x16x32_bf16 v[64:67], a[180:183], v[40:43], v[64:67]\n" \
  "v_mfma_f32_16x16x32_bf16 v[76:79], a[180:183], v[44:47], v[76:79]\n" \
  "s_waitcnt lgkmcnt(2)\n" \
  "v_mfma_f32_16x16x32_bf16 v[56:59], a[24:27], v[48:51], v[56:59]\n" \
  "ds_read_b128 v[40:43], v9 offset:8192\n" \
  "v_mfma_f32_16x16x32_bf16 v[68:71], a[24:27], v[52:55], v[68:71]\n" \
  "ds_read_b128 v[44:47], v9 offset:28672\n" \
  "v_mfma_f32_16x16x32_bf16 v[60:63], a[104:107], v[48:51], v[60:63]\n" \
  "buffer_load_dwordx4 v1, s[36:39], s68 offen offset:2048 sc1 lds\n" \
  "v_mfma_f32_16x16x32_bf16 v[72:75], a[104:107], v[52:55], v[72:75]\n" \
  "v_mfma_f32_16x16x32_bf16 v[64:67], a[184:187], v[48:51], v[64:67]\n" \
  "v_mfma_f32_16x16x32_bf16 v[76:79], a[184:187], v[52:55], v[76:79]\n" \
  "s_waitcnt lgkmcnt(2)\n" \
  "v_mfma_f32_16x16x32_bf16 v[56:59], a[28:31], v[32:35], v[56:59]\n" \
  "ds_read_b128 v[48:51], v9 offset:9216\n" \
  "v_mfma_f32_16x16x32_bf16 v[68:71], a[28:31], v[36:39], v[68:71]\n" \
  "ds_read_b128 v[52:55], v9 offset:29696\n" \
  "v_mfma_f32_16x16x32_bf16 v[60:63], a[108:111], v[32:35], v[60:63]\n" \
  "buffer_load_dwordx4 v1, s[36:39], s68 offen offset:3072 sc1 lds\n" \
  "v_mfma_f32_16x16x32_bf16 v[72:75], a[108:111], v[36:39], v[72:75]\n" \
  "v_mfma_f32_16x16x32_bf16 v[64:67], a[188:191], v[32:35], v[64:67]\n" \
  "v_mfma_f32_16x16x32_bf16 v[76:79], a[188:191], v[36:39], v[76:79]\n" \
  "s_waitcnt lgkmcnt(2)\n" \
  "v_mfma_f32_16x16x32_bf16 v[56:59], a[32:35], v[40:43], v[56:59]\n" \
  "ds_read_b128 v[32:35], v9 offset:10240\n" \
  "v_mfma_f32_16x16x32_bf16 v[68:71], a[32:35], v[44:47], v[68:71]\n" \
  "ds_read_b128 v[36:39], v9 offset:30720\n" \
  "v_mfma_f32_16x16x32_bf16 v[60:63], a[112:115], v[40:43], v[60:63]\n" \
  "s_add_u32 m0, s54, 4096\n" \
  "v_mfma_f32_16x16x32_bf16 v[72:75], a[112:115], v[44:47], v[72:75]\n" \
  "buffer_load_dwordx4 v1, s[36:39], s72 offen offset:0 sc1 lds\n" \
  "v_mfma_f32_16x16x32_bf16 v[64:67], a[192:195], v[40:43], v[64:67]\n" \
  "v_mfma_f32_16x16x32_bf16 v[76:79], a[192:195], v[44:47], v[76:79]\n" \
  "s_waitcnt lgkmcnt(2)\n" \
  "v_mfma_f32_16x16x32_bf16 v[56:59], a[36:39], v[48:51], v[56:59]\n" \
  "ds_read_b128 v[40:43], v9 offset:11264\n" \
  "v_mfma_f32_16x16x32_bf16 v[68:71], a[36:39], v[52:55], v[68:71]\n" \
  "ds_read_b128 v[44:47], v9 offset:31744\n" \
  "v_mfma_f32_16x16x32_bf16 v[60:63], a[116:119], v[48:51], v[60:63]\n" \
  "buffer_load_dwordx4 v1, s[36:39], s72 offen offset:1024 sc1 lds\n" \
  "v_mfma_f32_16x16x32_bf16 v[72:75], a[116:119], v[52:55], v[72:75]\n" \
  "v_mfma_f32_16x16x32_bf16 v[64:67], a[196:199], v[48:51], v[64:67]\n" \
  "v_mfma_f32_16x16x32_bf16 v[76:79], a[196:199], v[52:55], v[76:79]\n" \
  "s_waitcnt lgkmcnt(2)\n" \
  "v_mfma_f32_16x16x32_bf16 v[56:59], a[40:43], v[32:35], v[56:59]\n" \
  "ds_read_b128 v[48:51], v9 offset:12288\n" \
  "v_mfma_f32_16x16x32_bf16 v[68:71], a[40:43], v[36:39], v[68:71]\n" \
  "ds_read_b128 v[52:55], v9 offset:32768\n" \
  "v_mfma_f32_16x16x32_bf16 v[60:63], a[120:123], v[32:35], v[60:63]\n" \
  "buffer_load_dwordx4 v1, s[36:39], s72 offen offset:2048 sc1 lds\n" \
  "v_mfma_f32_16x16x32_bf16 v[72:75], a[120:123], v[36:39], v[72:75]\n" \
  "v_mfma_f32_16x16x32_bf16 v[64:67], a[200:203], v[32:35], v[64:67]\n" \
  "v_mfma_f32_16x16x32_bf16 v[76:79], a[200:203], v[36:39], v[76:79]\n" \
  "s_waitcnt lgkmcnt(2)\n" \
  "v_mfma_f32_16x16x32_bf16 v[56:59], a[44:47], v[40:43], v[56:59]\n" \
  "ds_read_b128 v[32:35], v9 offset:13312\n" \
  "v_mfma_f32_16x16x32_bf16 v[68:71], a[44:47], v[44:47], v[68:71]\n" \
  "ds_read_b128 v[36:39], v9 offset:33792\n" \
  "v_mfma_f32_16x16x32_bf16 v[60:63], a[124:127], v[40:43], v[60:63]\n" \
  "buffer_load_dwordx4 v1, s[36:39], s72 offen offset:3072 sc1 lds\n" \
  "v_mfma_f32_16x16x32_bf16 v[72:75], a[124:127], v[44:47], v[72:75]\n" \
  "v_mfma_f32_16x16x32_bf16 v[64:67], a[204:207], v[40:43], v[64:67]\n" \
  "v_mfma_f32_16x16x32_bf16 v[76:79], a[204:207], v[44:47], v[76:79]\n" \
  "s_waitcnt lgkmcnt(2)\n" \
  "v_mfma_f32_16x16x32_bf16 v[56:59], a[48:51], v[48:51], v[56:59]\n" \
  "ds_read_b128 v[40:43], v9 offset:14336\n" \
  "v_mfma_f32_16x16x32_bf16 v[68:71], a[48:51], v[52:55], v[68:71]\n" \
  "ds_read_b128 v[44:47], v9 offset:34816\n" \
  "v_mfma_f32_16x16x32_bf16 v[60:63], a[128:131], v[48:51], v[60:63]\n" \
  "s_add_u32 m0, s54, 8192\n" \
  "v_mfma_f32_16x16x32_bf16 v[72:75], a[128:131], v[52:55], v[72:75]\n" \
  "buffer_load_dwordx4 v1, s[36:39], s73 offen offset:0 sc1 lds\n" \
  "v_mfma_f32_16x16x32_bf16 v[64:67], a[208:211], v[48:51], v[64:67]\n" \
  "v_mfma_f32_16x16x32_bf16 v[76:79], a[208:211], v[52:55], v[76:79]\n" \
  "s_waitcnt lgkmcnt(2)\n" \
  "v_mfma_f32_16x16x32_bf16 v[56:59], a[52:55], v[32:35], v[56:59]\n" \
  "ds_read_b128 v[48:51], v9 offset:15360\n" \
  "v_mfma_f32_16x16x32_bf16 v[68:71], a[52:55], v[36:39], v[68:71]\n" \
  "ds_read_b128 v[52:55], v9 offset:35840\n" \
  "v_mfma_f32_16x16x32_bf16 v[60:63], a[132:135], v[32:35], v[60:63]\n" \
  "buffer_load_dwordx4 v1, s[36:39], s73 offen offset:1024 sc1 lds\n" \
  "v_mfma_f32_16x16x32_bf16 v[72:75], a[132:135], v[36:39], v[72:75]\n" \
  "v_mfma_f32_16x16x32_bf16 v[64:67], a[212:215], v[32:35], v[64:67]\n" \
  "v_mfma_f32_16x16x32_bf16 v[76:79], a[212:215], v[36:39], v[76:79]\n" \
  "s_waitcnt lgkmcnt(2)\n" \
  "v_mfma_f32_16x16x32_bf16 v[56:59], a[56:59], v[40:43], v[56:59]\n" \
  "ds_read_b128 v[32:35], v9 offset:16384\n" \
  "v_mfma_f32_16x16x32_bf16 v[68:71], a[56:59], v[44:47], v[68:71]\n" \
  "ds_read_b128 v[36:39], v9 offset:36864\n" \
  "v_mfma_f32_16x16x32_bf16 v[60:63], a[136:139], v[40:43], v[60:63]\n" \
  "s_add_u32 s68, s68, 40960\n" \
  "v_mfma_f32_16x16x32_bf16 v[72:75], a[136:139], v[44:47], v[72:75]\n" \
  "v_mfma_f32_16x16x32_bf16 v[64:67], a[216:219], v[40:43], v[64:67]\n" \
  "v_mfma_f32_16x16x32_bf16 v[76:79], a[216:219], v[44:47], v[76:79]\n" \
  "s_waitcnt lgkmcnt(2)\n" \
  "v_mfma_f32_16x16x32_bf16 v[56:59], a[60:63], v[48:51], v[56:59]\n" \
  "ds_read_b128 v[40:43], v9 offset:17408\n" \
  "v_mfma_f32_16x16x32_bf16 v[68:71], a[60:63], v[52:55], v[68:71]\n" \
  "ds_read_b128 v[44:47], v9 offset:37888\n" \
  "v_mfma_f32_16x16x32_bf16 v[60:63], a[140:143], v[48:51], v[60:63]\n" \
  "v_mov_b32 v11, s52\n" \
  "v_mfma_f32_16x16x32_bf16 v[72:75], a[140:143], v[52:55], v[72:75]\n" \
  "s_waitcnt vmcnt(10)\n" \
  "v_mfma_f32_16x16x32_bf16 v[64:67], a[220:223], v[48:51], v[64:67]\n" \
  "buffer_store_dword v11, v5, s[40:43], 0 offen sc1\n" \
  "v_mfma_f32_16x16x32_bf16 v[76:79], a[220:223], v[52:55], v[76:79]\n" \
  "s_waitcnt lgkmcnt(2)\n" \
  "v_mfma_f32_16x16x32_bf16 v[56:59], a[64:67], v[32:35], v[56:59]\n" \
  "ds_read_b128 v[48:51], v9 offset:18432\n" \
  "v_mfma_f32_16x16x32_bf16 v[68:71], a[64:67], v[36:39], v[68:71]\n" \
  "ds_read_b128 v[52:55], v9 offset:38912\n" \
  "v_mfma_f32_16x16x32_bf16 v[60:63], a[144:147], v[32:35], v[60:63]\n" \
  "v_mfma_f32_16x16x32_bf16 v[72:75], a[144:147], v[36:39], v[72:75]\n" \
  "v_mfma_f32_16x16x32_bf16 v[64:67], a[224:227], v[32:35], v[64:67]\n" \
  "v_mfma_f32_16x16x32_bf16 v[76:79], a[224:227], v[36:39], v[76:79]\n" \
  "s_waitcnt lgkmcnt(2)\n" \
  "v_mfma_f32_16x16x32_bf16 v[56:59], a[68:71], v[40:43], v[56:59]\n" \
  "ds_read_b128 v[32:35], v9 offset:19456\n" \
  "v_mfma_f32_16x16x32_bf16 v[68:71], a[68:71], v[44:47], v[68:71]\n" \
  "ds_read_b128 v[36:39], v9 offset:39936\n" \
  "v_mfma_f32_16x16x32_bf16 v[60:63], a[148:151], v[40:43], v[60:63]\n" \
  "v_readlane_b32 s69, v4, s52\n" \
  "v_mfma_f32_16x16x32_bf16 v[72:75], a[148:151], v[44:47], v[72:75]\n" \
  "v_readlane_b32 s71, v8, s52\n" \
  "v_mfma_f32_16x16x32_bf16 v[64:67], a[228:231], v[40:43], v[64:67]\n" \
  "s_add_u32 s52, s52, 1\n" \
  "v_mfma_f32_16x16x32_bf16 v[76:79], a[228:231], v[44:47], v[76:79]\n" \
  "s_waitcnt lgkmcnt(0)\n" \
  "s_barrier\n" \
  "v_mfma_f32_16x16x32_bf16 v[56:59], a[72:75], v[48:51], v[56:59]\n" \
  "ds_read_b128 v[40:43], v10 offset:0\n" \
  "v_mfma_f32_16x16x32_bf16 v[68:71], a[72:75], v[52:55], v[68:71]\n" \
  "ds_read_b128 v[44:47], v10 offset:20480\n" \
  "v_mfma_f32_16x16x32_bf16 v[60:63], a[152:155], v[48:51], v[60:63]\n" \
  "v_mfma_f32_16x16x32_bf16 v[72:75], a[152:155], v[52:55], v[72:75]\n" \
  "v_mfma_f32_16x16x32_bf16 v[64:67], a[232:235], v[48:51], v[64:67]\n" \
  "v_mfma_f32_16x16x32_bf16 v[76:79], a[232:235], v[52:55], v[76:79]\n" \
  "v_mfma_f32_16x16x32_bf16 v[56:59], a[76:79], v[32:35], v[56:59]\n" \
  "ds_read_b128 v[48:51], v10 offset:1024\n" \
  "v_mfma_f32_16x16x32_bf16 v[68:71], a[76:79], v[36:39], v[68:71]\n" \
  "ds_read_b128 v[52:55], v10 offset:21504\n" \
  "v_mfma_f32_16x16x32_bf16 v[60:63], a[156:159], v[32:35], v[60:63]\n" \
  "s_cmp_eq_u32 s52, s53\n" \
  "v_mfma_f32_16x16x32_bf16 v[72:75], a[156:159], v[36:39], v[72:75]\n" \
  "v_mfma_f32_16x16x32_bf16 v[64:67], a[236:239], v[32:35], v[64:67]\n" \
  "v_mfma_f32_16x16x32_bf16 v[76:79], a[236:239], v[36:39], v[76:79]\n" \
  "s_waitcnt lgkmcnt(2)\n" \
  "v_mfma_f32_16x16x32_bf16 v[100:103], a[0:3], v[40:43], 0\n" \
  "ds_read_b128 v[32:35], v10 offset:2048\n" \
  "v_mfma_f32_16x16x32_bf16 v[112:115], a[0:3], v[44:47], 0\n" \
  "ds_read_b128 v[36:39], v10 offset:22528\n" \
  "v_mfma_f32_16x16x32_bf16 v[104:107], a[80:83], v[40:43], 0\n" \
  "buffer_store_dwordx4 v[12:15], v7, s[40:43], s71 offen sc1\n" \
  "v_mfma_f32_16x16x32_bf16 v[116:119], a[80:83], v[44:47], 0\n" \
  "v_cvt_pk_bf16_f32 v16, v56, v57\n" \
  "v_mfma_f32_16x16x32_bf16 v[108:111], a[160:163], v[40:43], 0\n" \
  "v_cvt_pk_bf16_f32 v17, v58, v59\n" \
  "v_mfma_f32_16x16x32_bf16 v[120:123], a[160:163], v[44:47], 0\n" \
  "v_cvt_pk_bf16_f32 v18, v60, v61\n" \
  "s_waitcnt lgkmcnt(2)\n" \
  "v_mfma_f32_16x16x32_bf16 v[100:103], a[4:7], v[48:51], v[100:103]\n" \
  "ds_read_b128 v[40:43], v10 offset:3072\n" \
  "v_mfma_f32_16x16x32_bf16 v[112:115], a[4:7], v[52:55], v[112:115]\n" \
  "ds_read_b128 v[44:47], v10 offset:23552\n" \
  "v_mfma_f32_16x16x32_bf16 v[104:107], a[84:87], v[48:51], v[104:107]\n" \
  "v_cvt_pk_bf16_f32 v19, v62, v63\n" \
  "v_mfma_f32_16x16x32_bf16 v[116:119], a[84:87], v[52:55], v[116:119]\n" \
  "buffer_store_dwordx4 v[16:19], v2, s[40:43], s69 offen sc1\n" \
  "v_mfma_f32_16x16x32_bf16 v[108:111], a[164:167], v[48:51], v[108:111]\n" \
  "v_cvt_pk_bf16_f32 v20, v68, v69\n" \
  "v_mfma_f32_16x16x32_bf16 v[120:123], a[164:167], v[52:55], v[120:123]\n" \
  "v_cvt_pk_bf16_f32 v21, v70, v71\n" \
  "s_waitcnt lgkmcnt(2)\n" \
  "v_mfma_f32_16x16x32_bf16 v[100:103], a[8:11], v[32:35], v[100:103]\n" \
  "ds_read_b128 v[48:51], v10 offset:4096\n" \
  "v_mfma_f32_16x16x32_bf16 v[112:115], a[8:11], v[36:39], v[112:115]\n" \
  "ds_read_b128 v[52:55], v10 offset:24576\n" \
  "v_mfma_f32_16x16x32_bf16 v[104:107], a[88:91], v[32:35], v[104:107]\n" \
  "v_cvt_pk_bf16_f32 v22, v72, v73\n" \
  "v_mfma_f32_16x16x32_bf16 v[116:119], a[88:91], v[36:39], v[116:119]\n" \
  "v_cvt_pk_bf16_f32 v23, v74, v75\n" \
  "v_mfma_f32_16x16x32_bf16 v[108:111], a[168:171], v[32:35], v[108:111]\n" \
  "buffer_store_dwordx4 v[20:23], v2, s[40:43], s69 offen offset:1024 sc1\n" \
  "v_mfma_f32_16x16x32_bf16 v[120:123], a[168:171], v[36:39], v[120:123]\n" \
  "v_cvt_pk_bf16_f32 v24, v64, v65\n" \
  "s_waitcnt lgkmcnt(2)\n" \
  "v_mfma_f32_16x16x32_bf16 v[100:103], a[12:15], v[40:43], v[100:103]\n" \
  "ds_read_b128 v[32:35], v10 offset:5120\n" \
  "v_mfma_f32_16x16x32_bf16 v[112:115], a[12:15], v[44:47], v[112:115]\n" \
  "ds_read_b128 v[36:39], v10 offset:25600\n" \
  "v_mfma_f32_16x16x32_bf16 v[104:107], a[92:95], v[40:43], v[104:107]\n" \
  "v_cvt_pk_bf16_f32 v25, v66, v67\n" \
  "v_mfma_f32_16x16x32_bf16 v[116:119], a[92:95], v[44:47], v[116:119]\n" \
  "v_cvt_pk_bf16_f32 v26, v76, v77\n" \
  "v_mfma_f32_16x16x32_bf16 v[108:111], a[172:175], v[40:43], v[108:111]\n" \
  "v_cvt_pk_bf16_f32 v27, v78, v79\n" \
  "v_mfma_f32_16x16x32_bf16 v[120:123], a[172:175], v[44:47], v[120:123]\n" \
  "buffer_store_dwordx4 v[24:27], v3, s[40:43], s69 offen sc1\n" \
  "s_waitcnt lgkmcnt(2)\n" \
  "v_mfma_f32_16x16x32_bf16 v[100:103], a[16:19], v[48:51], v[100:103]\n" \
  "ds_read_b128 v[40:43], v10 offset:6144\n" \
  "v_mfma_f32_16x16x32_bf16 v[112:115], a[16:19], v[52:55], v[112:115]\n" \
  "ds_read_b128 v[44:47], v10 offset:26624\n" \
  "v_mfma_f32_16x16x32_bf16 v[104:107], a[96:99], v[48:51], v[104:107]\n" \
  "s_add_u32 s72, s68, 4096\n" \
  "v_mfma_f32_16x16x32_bf16 v[116:119], a[96:99], v[52:55], v[116:119]\n" \
  "s_add_u32 s73, s68, 8192\n" \
  "v_mfma_f32_16x16x32_bf16 v[108:111], a[176:179], v[48:51], v[108:111]\n" \
  "s_add_u32 m0, s54, 40960\n" \
  "v_mfma_f32_16x16x32_bf16 v[120:123], a[176:179], v[52:55], v[120:123]\n" \
  "buffer_load_dwordx4 v1, s[36:39], s68 offen offset:0 sc1 lds\n" \
  "s_waitcnt lgkmcnt(2)\n" \
  "v_mfma_f32_16x16x32_bf16 v[100:103], a[20:23], v[32:35], v[100:103]\n" \
  "ds_read_b128 v[48:51], v10 offset:7168\n" \
  "v_mfma_f32_16x16x32_bf16 v[112:115], a[20:23], v[36:39], v[112:115]\n" \
  "ds_read_b128 v[52:55], v10 offset:27648\n" \
  "v_mfma_f32_16x16x32_bf16 v[104:107], a[100:103], v[32:35], v[104:107]\n" \
  "buffer_load_dwordx4 v1, s[36:39], s68 offen offset:1024 sc1 lds\n" \
  "v_mfma_f32_16x16x32_bf16 v[116:119], a[100:103], v[36:39], v[116:119]\n" \
  "v_mfma_f32_16x16x32_bf16 v[108:111], a[180:183], v[32:35], v[108:111]\n" \
  "v_mfma_f32_16x16x32_bf16 v[120:123], a[180:183], v[36:39], v[120:123]\n" \
  "s_waitcnt lgkmcnt(2)\n" \
  "v_mfma_f32_16x16x32_bf16 v[100:103], a[24:27], v[40:43], v[100:103]\n" \
  "ds_read_b128 v[32:35], v10 offset:8192\n" \
  "v_mfma_f32_16x16x32_bf16 v[112:115], a[24:27], v[44:47], v[112:115]\n" \
  "ds_read_b128 v[36:39], v10 offset:28672\n" \
  "v_mfma_f32_16x16x32_bf16 v[104:107], a[104:107], v[40:43], v[104:107]\n" \
  "buffer_load_dwordx4 v1, s[36:39], s68 offen offset:2048 sc1 lds\n" \
  "v_mfma_f32_16x16x32_bf16 v[116:119], a[104:107], v[44:47], v[116:119]\n" \
  "v_mfma_f32_16x16x32_bf16 v[108:111], a[184:187], v[40:43], v[108:111]\n" \
  "v_mfma_f32_16x16x32_bf16 v[120:123], a[184:187], v[44:47], v[120:123]\n" \
  "s_waitcnt lgkmcnt(2)\n" \
  "v_mfma_f32_16x16x32_bf16 v[100:103], a[28:31], v[48:51], v[100:103]\n" \
  "ds_read_b128 v[40:43], v10 offset:9216\n" \
  "v_mfma_f32_16x16x32_bf16 v[112:115], a[28:31], v[52:55], v[112:115]\n" \
  "ds_read_b128 v[44:47], v10 offset:29696\n" \
  "v_mfma_f32_16x16x32_bf16 v[104:107], a[108:111], v[48:51], v[104:107]\n" \
  "buffer_load_dwordx4 v1, s[36:39], s68 offen offset:3072 sc1 lds\n" \
  "v_mfma_f32_16x16x32_bf16 v[116:119], a[108:111], v[52:55], v[116:119]\n" \
  "v_mfma_f32_16x16x32_bf16 v[108:111], a[188:191], v[48:51], v[108:111]\n" \
  "v_mfma_f32_16x16x32_bf16 v[120:123], a[188:191], v[52:55], v[120:123]\n" \
  "s_waitcnt lgkmcnt(2)\n" \
  "v_mfma_f32_16x16x32_bf16 v[100:103], a[32:35], v[32:35], v[100:103]\n" \
  "ds_read_b128 v[48:51], v10 offset:10240\n" \
  "v_mfma_f32_16x16x32_bf16 v[112:115], a[32:35], v[36:39], v[112:115]\n" \
  "ds_read_b128 v[52:55], v10 offset:30720\n" \
  "v_mfma_f32_16x16x32_bf16 v[104:107], a[112:115], v[32:35], v[104:107]\n" \
  "s_add_u32 m0, s54, 45056\n" \
  "v_mfma_f32_16x16x32_bf16 v[116:119], a[112:115], v[36:39], v[116:119]\n" \
  "buffer_load_dwordx4 v1, s[36:39], s72 offen offset:0 sc1 lds\n" \
  "v_mfma_f32_16x16x32_bf16 v[108:111], a[192:195], v[32:35], v[108:111]\n" \
  "v_mfma_f32_16x16x32_bf16 v[120:123], a[192:195], v[36:39], v[120:123]\n" \
  "s_waitcnt lgkmcnt(2)\n" \
  "v_mfma_f32_16x16x32_bf16 v[100:103], a[36:39], v[40:43], v[100:103]\n" \
  "ds_read_b128 v[32:35], v10 offset:11264\n" \
  "v_mfma_f32_16x16x32_bf16 v[112:115], a[36:39], v[44:47], v[112:115]\n" \
  "ds_read_b128 v[36:39], v10 offset:31744\n" \
  "v_mfma_f32_16x16x32_bf16 v[104:107], a[116:119], v[40:43], v[104:107]\n" \
  "buffer_load_dwordx4 v1, s[36:39], s72 offen offset:1024 sc1 lds\n" \
  "v_mfma_f32_16x16x32_bf16 v[116:119], a[116:119], v[44:47], v[116:119]\n" \
  "v_mfma_f32_16x16x32_bf16 v[108:111], a[196:199], v[40:43], v[108:111]\n" \
  "v_mfma_f32_16x16x32_bf16 v[120:123], a[196:199], v[44:47], v[120:123]\n" \
  "s_waitcnt lgkmcnt(2)\n" \
  "v_mfma_f32_16x16x32_bf16 v[100:103], a[40:43], v[48:51], v[100:103]\n" \
  "ds_read_b128 v[40:43], v10 offset:12288\n" \
  "v_mfma_f32_16x16x32_bf16 v[112:115], a[40:43], v[52:55], v[112:115]\n" \
  "ds_read_b128 v[44:47], v10 offset:32768\n" \
  "v_mfma_f32_16x16x32_bf16 v[104:107], a[120:123], v[48:51], v[104:107]\n" \
  "buffer_load_dwordx4 v1, s[36:39], s72 offen offset:2048 sc1 lds\n" \
  "v_mfma_f32_16x16x32_bf16 v[116:119], a[120:123], v[52:55], v[116:119]\n" \
  "v_mfma_f32_16x16x32_bf16 v[108:111], a[200:203], v[48:51], v[108:111]\n" \
  "v_mfma_f32_16x16x32_bf16 v[120:123], a[200:203], v[52:55], v[120:123]\n" \
  "s_waitcnt lgkmcnt(2)\n" \
  "v_mfma_f32_16x16x32_bf16 v[100:103], a[44:47], v[32:35], v[100:103]\n" \
  "ds_read_b128 v[48:51], v10 offset:13312\n" \
  "v_mfma_f32_16x16x32_bf16 v[112:115], a[44:47], v[36:39], v[112:115]\n" \
  "ds_read_b128 v[52:55], v10 offset:33792\n" \
  "v_mfma_f32_16x16x32_bf16 v[104:107], a[124:127], v[32:35], v[104:107]\n" \
  "buffer_load_dwordx4 v1, s[36:39], s72 offen offset:3072 sc1 lds\n" \
  "v_mfma_f32_16x16x32_bf16 v[116:119], a[124:127], v[36:39], v[116:119]\n" \
  "v_mfma_f32_16x16x32_bf16 v[108:111], a[204:207], v[32:35], v[108:111]\n" \
  "v_mfma_f32_16x16x32_bf16 v[120:123], a[204:207], v[36:39], v[120:123]\n" \
  "s_waitcnt lgkmcnt(2)\n" \
  "v_mfma_f32_16x16x32_bf16 v[100:103], a[48:51], v[40:43], v[100:103]\n" \
  "ds_read_b128 v[32:35], v10 offset:14336\n" \
  "v_mfma_f32_16x16x32_bf16 v[112:115], a[48:51], v[44:47], v[112:115]\n" \
  "ds_read_b128 v[36:39], v10 offset:34816\n" \
  "v_mfma_f32_16x16x32_bf16 v[104:107], a[128:131], v[40:43], v[104:107]\n" \
  "s_add_u32 m0, s54, 49152\n" \
  "v_mfma_f32_16x16x32_bf16 v[116:119], a[128:131], v[44:47], v[116:119]\n" \
  "buffer_load_dwordx4 v1, s[36:39], s73 offen offset:0 sc1 lds\n" \
  "v_mfma_f32_16x16x32_bf16 v[108:111], a[208:211], v[40:43], v[108:111]\n" \
  "v_mfma_f32_16x16x32_bf16 v[120:123], a[208:211], v[44:47], v[120:123]\n" \
  "s_waitcnt lgkmcnt(2)\n" \
  "v_mfma_f32_16x16x32_bf16 v[100:103], a[52:55], v[48:51], v[100:103]\n" \
  "ds_read_b128 v[40:43], v10 offset:15360\n" \
  "v_mfma_f32_16x16x32_bf16 v[112:115], a[52:55], v[52:55], v[112:115]\n" \
  "ds_read_b128 v[44:47], v10 offset:35840\n" \
  "v_mfma_f32_16x16x32_bf16 v[104:107], a[132:135], v[48:51], v[104:107]\n" \
  "buffer_load_dwordx4 v1, s[36:39], s73 offen offset:1024 sc1 lds\n" \
  "v_mfma_f32_16x16x32_bf16 v[116:119], a[132:135], v[52:55], v[116:119]\n" \
  "v_mfma_f32_16x16x32_bf16 v[108:111], a[212:215], v[48:51], v[108:111]\n" \
  "v_mfma_f32_16x16x32_bf16 v[120:123], a[212:215], v[52:55], v[120:123]\n" \
  "s_waitcnt lgkmcnt(2)\n" \
  "v_mfma_f32_16x16x32_bf16 v[100:103], a[56:59], v[32:35], v[100:103]\n" \
  "ds_read_b128 v[48:51], v10 offset:16384\n" \
  "v_mfma_f32_16x16x32_bf16 v[112:115], a[56:59], v[36:39], v[112:115]\n" \
  "ds_read_b128 v[52:55], v10 offset:36864\n" \
  "v_mfma_f32_16x16x32_bf16 v[104:107], a[136:139], v[32:35], v[104:107]\n" \
  "s_add_u32 s68, s68, 40960\n" \
  "v_mfma_f32_16x16x32_bf16 v[116:119], a[136:139], v[36:39], v[116:119]\n" \
  "v_mfma_f32_16x16x32_bf16 v[108:111], a[216:219], v[32:35], v[108:111]\n" \
  "v_mfma_f32_16x16x32_bf16 v[120:123], a[216:219], v[36:39], v[120:123]\n" \
  "s_waitcnt lgkmcnt(2)\n" \
  "v_mfma_f32_16x16x32_bf16 v[100:103], a[60:63], v[40:43], v[100:103]\n" \
  "ds_read_b128 v[32:35], v10 offset:17408\n" \
  "v_mfma_f32_16x16x32_bf16 v[112:115], a[60:63], v[44:47], v[112:115]\n" \
  "ds_read_b128 v[36:39], v10 offset:37888\n" \
  "v_mfma_f32_16x16x32_bf16 v[104:107], a[140:143], v[40:43], v[104:107]\n" \
  "v_mov_b32 v11, s52\n" \
  "v_mfma_f32_16x16x32_bf16 v[116:119], a[140:143], v[44:47], v[116:119]\n" \
  "s_waitcnt vmcnt(10)\n" \
  "v_mfma_f32_16x16x32_bf16 v[108:111], a[220:223], v[40:43], v[108:111]\n" \
  "buffer_store_dword v11, v5, s[40:43], 0 offen sc1\n" \
  "v_mfma_f32_16x16x32_bf16 v[120:123], a[220:223], v[44:47], v[120:123]\n" \
  "s_waitcnt lgkmcnt(2)\n" \
  "v_mfma_f32_16x16x32_bf16 v[100:103], a[64:67], v[48:51], v[100:103]\n" \
  "ds_read_b128 v[40:43], v10 offset:18432\n" \
  "v_mfma_f32_16x16x32_bf16 v[112:115], a[64:67], v[52:55], v[112:115]\n" \
  "ds_read_b128 v[44:47], v10 offset:38912\n" \
  "v_mfma_f32_16x16x32_bf16 v[104:107], a[144:147], v[48:51], v[104:107]\n" \
  "v_mfma_f32_16x16x32_bf16 v[116:119], a[144:147], v[52:55], v[116:119]\n" \
  "v_mfma_f32_16x16x32_bf16 v[108:111], a[224:227], v[48:51], v[108:111]\n" \
  "v_mfma_f32_16x16x32_bf16 v[120:123], a[224:227], v[52:55], v[120:123]\n" \
  "s_waitcnt lgkmcnt(2)\n" \
  "v_mfma_f32_16x16x32_bf16 v[100:103], a[68:71], v[32:35], v[100:103]\n" \
  "ds_read_b128 v[48:51], v10 offset:19456\n" \
  "v_mfma_f32_16x16x32_bf16 v[112:115], a[68:71], v[36:39], v[112:115]\n" \
  "ds_read_b128 v[52:55], v10 offset:39936\n" \
  "v_mfma_f32_16x16x32_bf16 v[104:107], a[148:151], v[32:35], v[104:107]\n" \
  "v_readlane_b32 s69, v4, s52\n" \
  "v_mfma_f32_16x16x32_bf16 v[116:119], a[148:151], v[36:39], v[116:119]\n" \
  "v_readlane_b32 s71, v8, s52\n" \
  "v_mfma_f32_16x16x32_bf16 v[108:111], a[228:231], v[32:35], v[108:111]\n" \
  "s_add_u32 s52, s52, 1\n" \
  "v_mfma_f32_16x16x32_bf16 v[120:123], a[228:231], v[36:39], v[120:123]\n" \
  "s_waitcnt lgkmcnt(0)\n" \
  "s_barrier\n" \
  "v_mfma_f32_16x16x32_bf16 v[100:103], a[72:75], v[40:43], v[100:103]\n" \
  "ds_read_b128 v[32:35], v0 offset:0\n" \
  "v_mfma_f32_16x16x32_bf16 v[112:115], a[72:75], v[44:47], v[112:115]\n" \
  "ds_read_b128 v[36:39], v0 offset:20480\n" \
  "v_mfma_f32_16x16x32_bf16 v[104:107], a[152:155], v[40:43], v[104:107]\n" \
  "v_mfma_f32_16x16x32_bf16 v[116:119], a[152:155], v[44:47], v[116:119]\n" \
  "v_mfma_f32_16x16x32_bf16 v[108:111], a[232:235], v[40:43], v[108:111]\n" \
  "v_mfma_f32_16x16x32_bf16 v[120:123], a[232:235], v[44:47], v[120:123]\n" \
  "v_mfma_f32_16x16x32_bf16 v[100:103], a[76:79], v[48:51], v[100:103]\n" \
  "ds_read_b128 v[40:43], v0 offset:1024\n" \
  "v_mfma_f32_16x16x32_bf16 v[112:115], a[76:79], v[52:55], v[112:115]\n" \
  "ds_read_b128 v[44:47], v0 offset:21504\n" \
  "v_mfma_f32_16x16x32_bf16 v[104:107], a[156:159], v[48:51], v[104:107]\n" \
  "s_cmp_eq_u32 s52, s53\n" \
  "v_mfma_f32_16x16x32_bf16 v[116:119], a[156:159], v[52:55], v[116:119]\n" \
  "v_mfma_f32_16x16x32_bf16 v[108:111], a[236:239], v[48:51], v[108:111]\n" \
  "v_mfma_f32_16x16x32_bf16 v[120:123], a[236:239], v[52:55], v[120:123]\n" \
  "s_cbranch_scc0 LPL_%=\n" \
  "LPE_%=:\n" \
  "buffer_store_dwordx4 v[12:15], v7, s[40:43], s71 offen sc1\n" \
  "s_nop 6\n" \
  "v_cvt_pk_bf16_f32 v16, v100, v101\n" \
  "v_cvt_pk_bf16_f32 v17, v102, v103\n" \
  "v_cvt_pk_bf16_f32 v18, v104, v105\n" \
  "v_cvt_pk_bf16_f32 v19, v106, v107\n" \
  "v_cvt_pk_bf16_f32 v20, v112, v113\n" \
  "v_cvt_pk_bf16_f32 v21, v114, v115\n" \
  "v_cvt_pk_bf16_f32 v22, v116, v117\n" \
  "v_cvt_pk_bf16_f32 v23, v118, v119\n" \
  "v_cvt_pk_bf16_f32 v24, v108, v109\n" \
  "v_cvt_pk_bf16_f32 v25, v110, v111\n" \
  "v_cvt_pk_bf16_f32 v26, v120, v121\n" \
  "v_cvt_pk_bf16_f32 v27, v122, v123\n" \
  "buffer_store_dwordx4 v[16:19], v2, s[40:43], s69 offen sc1\n" \
  "buffer_store_dwordx4 v[20:23], v2, s[40:43], s69 offen offset:1024 sc1\n" \
  "buffer_store_dwordx4 v[24:27], v3, s[40:43], s69 offen sc1\n" \
  "v_mov_b32 v11, s52\n" \
  "s_waitcnt vmcnt(0) lgkmcnt(0)\n" \
  "buffer_store_dword v11, v5, s[40:43], 0 offen sc1\n"
#define VO_CLOBBERS \
  "v16", "v17", "v18", "v19", "v20", "v21", "v22", "v23", "v24", "v25", "v26", "v27", "v28", "v29", "v30", "v31", "v32", "v33", "v34", "v35", "v36", \
  "v37", "v38", "v39", "v40", "v41", "v42", "v43", "v44", "v45", "v46", "v47", "v48", "v49", "v50", "v51", "v52", "v53", "v54", "v55", "v56", "v57", \
  "v58", "v59", "v60", "v61", "v62", "v63", "v64", "v65", "v66", "v67", "v68", "v69", "v70", "v71", "v72", "v73", "v74", "v75", "v76", "v77", "v78", \
  "v79", "v80", "v81", "v82", "v83", "v84", "v85", "v86", "v87", "v88", "v89", "v90", "v91", "v92", "v93", "v94", "v95", "v96", "v97", "v98", "v99", \
  "v100", "v101", "v102", "v103", "v104", "v105", "v106", "v107", "v108", "v109", "v110", "v111", "v112", "v113", "v114", "v115", "v116", "v117", \
  "v118", "v119", "v120", "v121", "v122", "v123", "v124", "v125", "v126", "v127", "v224", "v225", "v226", "v227", "v228", "v229", "v230", "v231", \
  "v232", "v233", "v234", "v235", "v236", "v237", "v238", "v239", "v240", "v241", "v242", "v243", "v244", "v245", "v246", "v247", "v248", "v249", \
  "v250", "v251", "v252", "v253", "v254", "v255", "s68", "s69", "s70", "s71", "s72", "s73", "s74", "s75", "s76", "s77"
#define VO_ASM \
  "s_lshl_b32 s73, s54, 11\n" \
  "s_lshl_b32 s74, s54, 10\n" \
  "s_add_u32 s74, s74, 8192\n" \
  "s_mov_b32 s75, 0\n" \
  "s_mov_b32 s76, s52\n" \
  "s_mov_b32 s69, 0x40000000\n" \
  "s_getpc_b64 s[70:71]\n" \
  "LCF5_%=:\n" \
  "s_add_u32 s70, s70, LVF_%=-LCF5_%=\n" \
  "s_addc_u32 s71, s71, 0\n" \
  "v_mbcnt_lo_u32_b32 v16, -1, 0\n" \
  "v_mbcnt_hi_u32_b32 v16, -1, v16\n" \
  "v_lshlrev_b32 v16, 6, v16\n" \
  "global_load_dword v17, v16, s[70:71]\n" \
  "s_barrier\n" \
  "v_mov_b32 v12, v0\n" \
  "s_mov_b32 s77, s69\n" \
  "s_mov_b32 s68, s69\n" \
  "s_sub_u32 s52, s52, 1\n" \
  "s_branch LVT_%=\n" \
  "LVF_%=:\n" \
  "LVPF_%=:\n" \
  "buffer_load_dword v11, v9, s[36:39], 0 offen sc1\n" \
  "s_waitcnt vmcnt(0)\n" \
  "v_cmp_ge_u32 vcc, s76, v11\n" \
  "s_nop 3\n" \
  "s_cbranch_vccz LVG_%=\n" \
  "s_add_u32 s75, s75, 1\n" \
  "s_cmp_gt_u32 s75, 200000\n" \
  "s_cbranch_scc1 LVG_%=\n" \
  "s_sleep 2\n" \
  "s_branch LVPF_%=\n" \
  "LVG_%=:\n" \
  "v_readlane_b32 s71, v6, s76\n" \
  "s_add_u32 m0, s73, 0\n" \
  "s_nop 4\n" \
  "buffer_load_dwordx4 v1, s[36:39], s71 offen offset:0 sc1 lds\n" \
  "buffer_load_dwordx4 v1, s[36:39], s71 offen offset:1024 sc1 lds\n" \
  "s_add_u32 m0, s74, 0\n" \
  "s_add_u32 s76, s76, 1\n" \
  "buffer_load_dwordx4 v2, s[36:39], s71 offen sc1 lds\n" \
  "s_min_u32 s76, s76, 63\n" \
  "v_readlane_b32 s68, v4, s52\n" \
  "v_readlane_b32 s69, v5, s52\n" \
  "v_mov_b32 v10, s52\n" \
  "s_waitcnt vmcnt(0)\n" \
  "buffer_store_dword v10, v7, s[36:39], 0 offen sc1\n" \
  "s_barrier\n" \
  "ds_read_b128 v[32:35], v12 offset:0\n" \
  "ds_read_b128 v[56:59], v12 offset:1024\n" \
  "ds_read_b128 v[36:39], v12 offset:2048\n" \
  "ds_read_b128 v[60:63], v12 offset:3072\n" \
  "ds_read_b128 v[40:43], v12 offset:4096\n" \
  "ds_read_b128 v[64:67], v12 offset:5120\n" \
  "ds_read_b128 v[44:47], v12 offset:6144\n" \
  "ds_read_b128 v[68:71], v12 offset:7168\n" \
  "s_waitcnt lgkmcnt(4)\n" \
  "ds_read_b64 v[48:49], v12 offset:8192\n" \
  "ds_read_b64 v[72:73], v12 offset:8200\n" \
  "ds_read_b64 v[50:51], v12 offset:9216\n" \
  "ds_read_b64 v[74:75], v12 offset:9224\n" \
  "ds_read_b64 v[52:53], v12 offset:10240\n" \
  "ds_read_b64 v[76:77], v12 offset:10248\n" \
  "ds_read_b64 v[54:55], v12 offset:11264\n" \
  "ds_read_b64 v[78:79], v12 offset:11272\n" \
  "s_waitcnt lgkmcnt(0)\n" \
  "s_barrier\n" \
  "LVT_%=:\n" \
  "v_mfma_f32_16x16x32_bf16 v[224:227], v[32:35], a[0:3], 0\n" \
  "v_mfma_f32_16x16x32_bf16 v[228:231], v[56:59], a[0:3], 0\n" \
  "v_mfma_f32_16x16x32_bf16 v[224:227], v[36:39], a[4:7], v[224:227]\n" \
  "v_cvt_pk_bf16_f32 v252, v232, v233\n" \
  "v_mfma_f32_16x16x32_bf16 v[228:231], v[60:63], a[4:7], v[228:231]\n" \
  "v_cvt_pk_bf16_f32 v253, v234, v235\n" \
  "v_mfma_f32_16x16x32_bf16 v[224:227], v[40:43], a[8:11], v[224:227]\n" \
  "v_cvt_pk_bf16_f32 v254, v236, v237\n" \
  "v_mfma_f32_16x16x32_bf16 v[228:231], v[64:67], a[8:11], v[228:231]\n" \
  "v_cvt_pk_bf16_f32 v255, v238, v239\n" \
  "v_mfma_f32_16x16x32_bf16 v[224:227], v[44:47], a[12:15], v[224:227]\n" \
  "s_add_u32 s72, s77, 589824\n" \
  "v_mfma_f32_16x16x32_bf16 v[228:231], v[68:71], a[12:15], v[228:231]\n" \
  "buffer_store_dwordx4 v[252:255], v3, s[36:39], s72 offen sc1\n" \
  "v_mfma_f32_16x16x32_bf16 v[224:227], v[48:51], a[16:19], v[224:227]\n" \
  "v_mfma_f32_16x16x32_bf16 v[228:231], v[72:75], a[16:19], v[228:231]\n" \
  "v_mfma_f32_16x16x32_bf16 v[224:227], v[52:55], a[20:23], v[224:227]\n" \
  "v_mfma_f32_16x16x32_bf16 v[228:231], v[76:79], a[20:23], v[228:231]\n" \
  "v_mfma_f32_16x16x32_bf16 v[232:235], v[32:35], a[24:27], 0\n" \
  "v_mfma_f32_16x16x32_bf16 v[236:239], v[56:59], a[24:27], 0\n" \
  "v_mfma_f32_16x16x32_bf16 v[232:235], v[36:39], a[28:31], v[232:235]\n" \
  "v_cvt_pk_bf16_f32 v248, v224, v225\n" \
  "v_mfma_f32_16x16x32_bf16 v[236:239], v[60:63], a[28:31], v[236:239]\n" \
  "v_cvt_pk_bf16_f32 v249, v226, v227\n" \
  "v_mfma_f32_16x16x32_bf16 v[232:235], v[40:43], a[32:35], v[232:235]\n" \
  "v_cvt_pk_bf16_f32 v250, v228, v229\n" \
  "v_mfma_f32_16x16x32_bf16 v[236:239], v[64:67], a[32:35], v[236:239]\n" \
  "v_cvt_pk_bf16_f32 v251, v230, v231\n" \
  "v_mfma_f32_16x16x32_bf16 v[232:235], v[44:47], a[36:39], v[232:235]\n" \
  "s_add_u32 s72, s68, 0\n" \
  "v_mfma_f32_16x16x32_bf16 v[236:239], v[68:71], a[36:39], v[236:239]\n" \
  "buffer_store_dwordx4 v[248:251], v3, s[36:39], s72 offen sc1\n" \
  "v_mfma_f32_16x16x32_bf16 v[232:235], v[48:51], a[40:43], v[232:235]\n" \
  "v_mfma_f32_16x16x32_bf16 v[236:239], v[72:75], a[40:43], v[236:239]\n" \
  "v_mfma_f32_16x16x32_bf16 v[232:235], v[52:55], a[44:47], v[232:235]\n" \
  "v_mfma_f32_16x16x32_bf16 v[236:239], v[76:79], a[44:47], v[236:239]\n" \
  "v_mfma_f32_16x16x32_bf16 v[224:227], v[32:35], a[48:51], 0\n" \
  "v_mfma_f32_16x16x32_bf16 v[228:231], v[56:59], a[48:51], 0\n" \
  "v_mfma_f32_16x16x32_bf16 v[224:227], v[36:39], a[52:55], v[224:227]\n" \
  "v_cvt_pk_bf16_f32 v252, v232, v233\n" \
  "v_mfma_f32_16x16x32_bf16 v[228:231], v[60:63], a[52:55], v[228:231]\n" \
  "v_cvt_pk_bf16_f32 v253, v234, v235\n" \
  "v_mfma_f32_16x16x32_bf16 v[224:227], v[40:43], a[56:59], v[224:227]\n" \
  "v_cvt_pk_bf16_f32 v254, v236, v237\n" \
  "v_mfma_f32_16x16x32_bf16 v[228:231], v[64:67], a[56:59], v[228:231]\n" \
  "v_cvt_pk_bf16_f32 v255, v238, v239\n" \
  "v_mfma_f32_16x16x32_bf16 v[224:227], v[44:47], a[60:63], v[224:227]\n" \
  "s_add_u32 s72, s68, 65536\n" \
  "v_mfma_f32_16x16x32_bf16 v[228:231], v[68:71], a[60:63], v[228:231]\n" \
  "buffer_store_dwordx4 v[252:255], v3, s[36:39], s72 offen sc1\n" \
  "v_mfma_f32_16x16x32_bf16 v[224:227], v[48:51], a[64:67], v[224:227]\n" \
  "v_mfma_f32_16x16x32_bf16 v[228:231], v[72:75], a[64:67], v[228:231]\n" \
  "v_mfma_f32_16x16x32_bf16 v[224:227], v[52:55], a[68:71], v[224:227]\n" \
  "v_mfma_f32_16x16x32_bf16 v[228:231], v[76:79], a[68:71], v[228:231]\n" \
  "v_mfma_f32_16x16x32_bf16 v[232:235], v[32:35], a[72:75], 0\n" \
  "v_mfma_f32_16x16x32_bf16 v[236:239], v[56:59], a[72:75], 0\n" \
  "v_mfma_f32_16x16x32_bf16 v[232:235], v[36:39], a[76:79], v[232:235]\n" \
  "v_cvt_pk_bf16_f32 v248, v224, v225\n" \
  "v_mfma_f32_16x16x32_bf16 v[236:239], v[60:63], a[76:79], v[236:239]\n" \
  "v_cvt_pk_bf16_f32 v249, v226, v227\n" \
  "v_mfma_f32_16x16x32_bf16 v[232:235], v[40:43], a[80:83], v[232:235]\n" \
  "v_cvt_pk_bf16_f32 v250, v228, v229\n" \
  "v_mfma_f32_16x16x32_bf16 v[236:239], v[64:67], a[80:83], v[236:239]\n" \
  "v_cvt_pk_bf16_f32 v251, v230, v231\n" \
  "v_mfma_f32_16x16x32_bf16 v[232:235], v[44:47], a[84:87], v[232:235]\n" \
  "s_add_u32 s72, s69, 131072\n" \
  "v_mfma_f32_16x16x32_bf16 v[236:239], v[68:71], a[84:87], v[236:239]\n" \
  "buffer_store_dwordx4 v[248:251], v3, s[36:39], s72 offen sc1\n" \
  "v_mfma_f32_16x16x32_bf16 v[232:235], v[48:51], a[88:91], v[232:235]\n" \
  "v_mfma_f32_16x16x32_bf16 v[236:239], v[72:75], a[88:91], v[236:239]\n" \
  "v_mfma_f32_16x16x32_bf16 v[232:235], v[52:55], a[92:95], v[232:235]\n" \
  "v_mfma_f32_16x16x32_bf16 v[236:239], v[76:79], a[92:95], v[236:239]\n" \
  "v_mfma_f32_16x16x32_bf16 v[224:227], v[32:35], a[96:99], 0\n" \
  "v_mfma_f32_16x16x32_bf16 v[228:231], v[56:59], a[96:99], 0\n" \
  "v_mfma_f32_16x16x32_bf16 v[224:227], v[36:39], a[100:103], v[224:227]\n" \
  "v_cvt_pk_bf16_f32 v252, v232, v233\n" \
  "v_mfma_f32_16x16x32_bf16 v[228:231], v[60:63], a[100:103], v[228:231]\n" \
  "v_cvt_pk_bf16_f32 v253, v234, v235\n" \
  "v_mfma_f32_16x16x32_bf16 v[224:227], v[40:43], a[104:107], v[224:227]\n" \
  "v_cvt_pk_bf16_f32 v254, v236, v237\n" \
  "v_mfma_f32_16x16x32_bf16 v[228:231], v[64:67], a[104:107], v[228:231]\n" \
  "v_cvt_pk_bf16_f32 v255, v238, v239\n" \
  "v_mfma_f32_16x16x32_bf16 v[224:227], v[44:47], a[108:111], v[224:227]\n" \
  "s_add_u32 s72, s69, 196608\n" \
  "v_mfma_f32_16x16x32_bf16 v[228:231], v[68:71], a[108:111], v[228:231]\n" \
  "buffer_store_dwordx4 v[252:255], v3, s[36:39], s72 offen sc1\n" \
  "v_mfma_f32_16x16x32_bf16 v[224:227], v[48:51], a[112:115], v[224:227]\n" \
  "v_mfma_f32_16x16x32_bf16 v[228:231], v[72:75], a[112:115], v[228:231]\n" \
  "v_mfma_f32_16x16x32_bf16 v[224:227], v[52:55], a[116:119], v[224:227]\n" \
  "v_mfma_f32_16x16x32_bf16 v[228:231], v[76:79], a[116:119], v[228:231]\n" \
  "v_mfma_f32_16x16x32_bf16 v[232:235], v[32:35], a[120:123], 0\n" \
  "v_mfma_f32_16x16x32_bf16 v[236:239], v[56:59], a[120:123], 0\n" \
  "v_mfma_f32_16x16x32_bf16 v[232:235], v[36:39], a[124:127], v[232:235]\n" \
  "v_cvt_pk_bf16_f32 v248, v224, v225\n" \
  "v_mfma_f32_16x16x32_bf16 v[236:239], v[60:63], a[124:127], v[236:239]\n" \
  "v_cvt_pk_bf16_f32 v249, v226, v227\n" \
  "v_mfma_f32_16x16x32_bf16 v[232:235], v[40:43], a[128:131], v[232:235]\n" \
  "v_cvt_pk_bf16_f32 v250, v228, v229\n" \
  "v_mfma_f32_16x16x32_bf16 v[236:239], v[64:67], a[128:131], v[236:239]\n" \
  "v_cvt_pk_bf16_f32 v251, v230, v231\n" \
  "v_mfma_f32_16x16x32_bf16 v[232:235], v[44:47], a[132:135], v[232:235]\n" \
  "s_add_u32 s72, s69, 262144\n" \
  "v_mfma_f32_16x16x32_bf16 v[236:239], v[68:71], a[132:135], v[236:239]\n" \
  "buffer_store_dwordx4 v[248:251], v3, s[36:39], s72 offen sc1\n" \
  "v_mfma_f32_16x16x32_bf16 v[232:235], v[48:51], a[136:139], v[232:235]\n" \
  "v_mfma_f32_16x16x32_bf16 v[236:239], v[72:75], a[136:139], v[236:239]\n" \
  "v_mfma_f32_16x16x32_bf16 v[232:235], v[52:55], a[140:143], v[232:235]\n" \
  "v_mfma_f32_16x16x32_bf16 v[236:239], v[76:79], a[140:143], v[236:239]\n" \
  "v_mfma_f32_16x16x32_bf16 v[224:227], v[32:35], a[144:147], 0\n" \
  "v_mfma_f32_16x16x32_bf16 v[228:231], v[56:59], a[144:147], 0\n" \
  "v_mfma_f32_16x16x32_bf16 v[224:227], v[36:39], a[148:151], v[224:227]\n" \
  "v_cvt_pk_bf16_f32 v252, v232, v233\n" \
  "v_mfma_f32_16x16x32_bf16 v[228:231], v[60:63], a[148:151], v[228:231]\n" \
  "v_cvt_pk_bf16_f32 v253, v234, v235\n" \
  "v_mfma_f32_16x16x32_bf16 v[224:227], v[40:43], a[152:155], v[224:227]\n" \
  "v_cvt_pk_bf16_f32 v254, v236, v237\n" \
  "v_mfma_f32_16x16x32_bf16 v[228:231], v[64:67], a[152:155], v[228:231]\n" \
  "v_cvt_pk_bf16_f32 v255, v238, v239\n" \
  "v_mfma_f32_16x16x32_bf16 v[224:227], v[44:47], a[156:159], v[224:227]\n" \
  "s_add_u32 s72, s69, 327680\n" \
  "v_mfma_f32_16x16x32_bf16 v[228:231], v[68:71], a[156:159], v[228:231]\n" \
  "buffer_store_dwordx4 v[252:255], v3, s[36:39], s72 offen sc1\n" \
  "v_mfma_f32_16x16x32_bf16 v[224:227], v[48:51], a[160:163], v[224:227]\n" \
  "v_mfma_f32_16x16x32_bf16 v[228:231], v[72:75], a[160:163], v[228:231]\n" \
  "v_mfma_f32_16x16x32_bf16 v[224:227], v[52:55], a[164:167], v[224:227]\n" \
  "v_mfma_f32_16x16x32_bf16 v[228:231], v[76:79], a[164:167], v[228:231]\n" \
  "v_mfma_f32_16x16x32_bf16 v[232:235], v[32:35], a[168:171], 0\n" \
  "v_mfma_f32_16x16x32_bf16 v[236:239], v[56:59], a[168:171], 0\n" \
  "v_mfma_f32_16x16x32_bf16 v[232:235], v[36:39], a[172:175], v[232:235]\n" \
  "v_cvt_pk_bf16_f32 v248, v224, v225\n" \
  "v_mfma_f32_16x16x32_bf16 v[236:239], v[60:63], a[172:175], v[236:239]\n" \
  "v_cvt_pk_bf16_f32 v249, v226, v227\n" \
  "v_mfma_f32_16x16x32_bf16 v[232:235], v[40:43], a[176:179], v[232:235]\n" \
  "v_cvt_pk_bf16_f32 v250, v228, v229\n" \
  "v_mfma_f32_16x16x32_bf16 v[236:239], v[64:67], a[176:179], v[236:239]\n" \
  "v_cvt_pk_bf16_f32 v251, v230, v231\n" \
  "v_mfma_f32_16x16x32_bf16 v[232:235], v[44:47], a[180:183], v[232:235]\n" \
  "s_add_u32 s72, s69, 393216\n" \
  "v_mfma_f32_16x16x32_bf16 v[236:239], v[68:71], a[180:183], v[236:239]\n" \
  "buffer_store_dwordx4 v[248:251], v3, s[36:39], s72 offen sc1\n" \
  "v_mfma_f32_16x16x32_bf16 v[232:235], v[48:51], a[184:187], v[232:235]\n" \
  "v_mfma_f32_16x16x32_bf16 v[236:239], v[72:75], a[184:187], v[236:239]\n" \
  "v_mfma_f32_16x16x32_bf16 v[232:235], v[52:55], a[188:191], v[232:235]\n" \
  "v_mfma_f32_16x16x32_bf16 v[236:239], v[76:79], a[188:191], v[236:239]\n" \
  "v_mfma_f32_16x16x32_bf16 v[224:227], v[32:35], a[192:195], 0\n" \
  "v_mfma_f32_16x16x32_bf16 v[228:231], v[56:59], a[192:195], 0\n" \
  "v_mfma_f32_16x16x32_bf16 v[224:227], v[36:39], a[196:199], v[224:227]\n" \
  "v_cvt_pk_bf16_f32 v252, v232, v233\n" \
  "v_mfma_f32_16x16x32_bf16 v[228:231], v[60:63], a[196:199], v[228:231]\n" \
  "v_cvt_pk_bf16_f32 v253, v234, v235\n" \
  "v_mfma_f32_16x16x32_bf16 v[224:227], v[40:43], a[200:203], v[224:227]\n" \
  "v_cvt_pk_bf16_f32 v254, v236, v237\n" \
  "v_mfma_f32_16x16x32_bf16 v[228:231], v[64:67], a[200:203], v[228:231]\n" \
  "v_cvt_pk_bf16_f32 v255, v238, v239\n" \
  "v_mfma_f32_16x16x32_bf16 v[224:227], v[44:47], a[204:207], v[224:227]\n" \
  "s_add_u32 s72, s69, 458752\n" \
  "v_mfma_f32_16x16x32_bf16 v[228:231], v[68:71], a[204:207], v[228:231]\n" \
  "buffer_store_dwordx4 v[252:255], v3, s[36:39], s72 offen sc1\n" \
  "v_mfma_f32_16x16x32_bf16 v[224:227], v[48:51], a[208:211], v[224:227]\n" \
  "v_mfma_f32_16x16x32_bf16 v[228:231], v[72:75], a[208:211], v[228:231]\n" \
  "v_mfma_f32_16x16x32_bf16 v[224:227], v[52:55], a[212:215], v[224:227]\n" \
  "v_mfma_f32_16x16x32_bf16 v[228:231], v[76:79], a[212:215], v[228:231]\n" \
  "v_mfma_f32_16x16x32_bf16 v[232:235], v[32:35], a[216:219], 0\n" \
  "v_mfma_f32_16x16x32_bf16 v[236:239], v[56:59], a[216:219], 0\n" \
  "v_mfma_f32_16x16x32_bf16 v[232:235], v[36:39], a[220:223], v[232:235]\n" \
  "v_cvt_pk_bf16_f32 v248, v224, v225\n" \
  "v_mfma_f32_16x16x32_bf16 v[236:239], v[60:63], a[220:223], v[236:239]\n" \
  "v_cvt_pk_bf16_f32 v249, v226, v227\n" \
  "v_mfma_f32_16x16x32_bf16 v[232:235], v[40:43], a[224:227], v[232:235]\n" \
  "v_cvt_pk_bf16_f32 v250, v228, v229\n" \
  "v_mfma_f32_16x16x32_bf16 v[236:239], v[64:67], a[224:227], v[236:239]\n" \
  "v_cvt_pk_bf16_f32 v251, v230, v231\n" \
  "v_mfma_f32_16x16x32_bf16 v[232:235], v[44:47], a[228:231], v[232:235]\n" \
  "s_add_u32 s72, s69, 524288\n" \
  "v_mfma_f32_16x16x32_bf16 v[236:239], v[68:71], a[228:231], v[236:239]\n" \
  "buffer_store_dwordx4 v[248:251], v3, s[36:39], s72 offen sc1\n" \
  "v_mfma_f32_16x16x32_bf16 v[232:235], v[48:51], a[232:235], v[232:235]\n" \
  "v_mfma_f32_16x16x32_bf16 v[236:239], v[72:75], a[232:235], v[236:239]\n" \
  "v_mfma_f32_16x16x32_bf16 v[232:235], v[52:55], a[236:239], v[232:235]\n" \
  "v_mfma_f32_16x16x32_bf16 v[236:239], v[76:79], a[236:239], v[236:239]\n" \
  "s_nop 7\n" \
  "v_cvt_pk_bf16_f32 v252, v232, v233\n" \
  "v_cvt_pk_bf16_f32 v253, v234, v235\n" \
  "v_cvt_pk_bf16_f32 v254, v236, v237\n" \
  "v_cvt_pk_bf16_f32 v255, v238, v239\n" \
  "s_add_u32 s72, s69, 589824\n" \
  "buffer_store_dwordx4 v[252:255], v3, s[36:39], s72 offen sc1\n" \
  "s_add_u32 s52, s52, 1\n" \
  "s_cmp_lg_u32 s52, 2\n" \
  "s_cbranch_scc1 LVF_%=\n" \
  "v_mov_b32 v10, s52\n" \
  "s_waitcnt vmcnt(0)\n" \
  "buffer_store_dword v10, v7, s[36:39], 0 offen sc1\n" \
  "s_mov_b32 s69, s77\n" \
  "LVPS0_%=:\n" \
  "buffer_load_dword v11, v9, s[36:39], 0 offen sc1\n" \
  "s_waitcnt vmcnt(0)\n" \
  "v_cmp_ge_u32 vcc, s76, v11\n" \
  "s_nop 3\n" \
  "s_cbranch_vccz LVS0_%=\n" \
  "s_add_u32 s75, s75, 1\n" \
  "s_cmp_gt_u32 s75, 200000\n" \
  "s_cbranch_scc1 LVS0_%=\n" \
  "s_sleep 2\n" \
  "s_branch LVPS0_%=\n" \
  "LVS0_%=:\n" \
  "v_readlane_b32 s71, v6, s76\n" \
  "s_add_u32 m0, s73, 0\n" \
  "s_nop 4\n" \
  "buffer_load_dwordx4 v1, s[36:39], s71 offen offset:0 sc1 lds\n" \
  "buffer_load_dwordx4 v1, s[36:39], s71 offen offset:1024 sc1 lds\n" \
  "s_add_u32 m0, s74, 0\n" \
  "s_add_u32 s76, s76, 1\n" \
  "buffer_load_dwordx4 v2, s[36:39], s71 offen sc1 lds\n" \
  "s_min_u32 s76, s76, 63\n" \
  "LVPS1_%=:\n" \
  "buffer_load_dword v11, v9, s[36:39], 0 offen sc1\n" \
  "s_waitcnt vmcnt(0)\n" \
  "v_cmp_ge_u32 vcc, s76, v11\n" \
  "s_nop 3\n" \
  "s_cbranch_vccz LVS1_%=\n" \
  "s_add_u32 s75, s75, 1\n" \
  "s_cmp_gt_u32 s75, 200000\n" \
  "s_cbranch_scc1 LVS1_%=\n" \
  "s_sleep 2\n" \
  "s_branch LVPS1_%=\n" \
  "LVS1_%=:\n" \
  "v_readlane_b32 s71, v6, s76\n" \
  "s_add_u32 m0, s73, 16384\n" \
  "s_nop 4\n" \
  "buffer_load_dwordx4 v1, s[36:39], s71 offen offset:0 sc1 lds\n" \
  "buffer_load_dwordx4 v1, s[36:39], s71 offen offset:1024 sc1 lds\n" \
  "s_add_u32 m0, s74, 16384\n" \
  "s_add_u32 s76, s76, 1\n" \
  "buffer_load_dwordx4 v2, s[36:39], s71 offen sc1 lds\n" \
  "s_min_u32 s76, s76, 63\n" \
  "s_xor_b32 s73, s73, 0x8000\n" \
  "s_xor_b32 s74, s74, 0x8000\n" \
  "buffer_load_dword v11, v9, s[36:39], 0 offen sc1\n" \
  "s_waitcnt vmcnt(0)\n" \
  "s_barrier\n" \
  "ds_read_b128 v[32:35], v12 offset:0\n" \
  "ds_read_b128 v[56:59], v12 offset:1024\n" \
  "ds_read_b128 v[36:39], v12 offset:2048\n" \
  "ds_read_b128 v[60:63], v12 offset:3072\n" \
  "ds_read_b128 v[40:43], v12 offset:4096\n" \
  "ds_read_b128 v[64:67], v12 offset:5120\n" \
  "ds_read_b128 v[44:47], v12 offset:6144\n" \
  "ds_read_b128 v[68:71], v12 offset:7168\n" \
  "s_waitcnt lgkmcnt(4)\n" \
  "ds_read_b64 v[48:49], v12 offset:8192\n" \
  "ds_read_b64 v[72:73], v12 offset:8200\n" \
  "ds_read_b64 v[50:51], v12 offset:9216\n" \
  "ds_read_b64 v[74:75], v12 offset:9224\n" \
  "ds_read_b64 v[52:53], v12 offset:10240\n" \
  "ds_read_b64 v[76:77], v12 offset:10248\n" \
  "ds_read_b64 v[54:55], v12 offset:11264\n" \
  "ds_read_b64 v[78:79], v12 offset:11272\n" \
  "s_waitcnt lgkmcnt(0)\n" \
  "LVL_%=:\n" \
  "s_mov_b32 s77, s69\n" \
  "v_readlane_b32 s68, v4, s52\n" \
  "v_readlane_b32 s69, v5, s52\n" \
  "v_mfma_f32_16x16x32_bf16 v[224:227], v[32:35], a[0:3], 0\n" \
  "v_mfma_f32_16x16x32_bf16 v[228:231], v[56:59], a[0:3], 0\n" \
  "v_mfma_f32_16x16x32_bf16 v[224:227], v[36:39], a[4:7], v[224:227]\n" \
  "v_cvt_pk_bf16_f32 v252, v232, v233\n" \
  "v_mfma_f32_16x16x32_bf16 v[228:231], v[60:63], a[4:7], v[228:231]\n" \
  "v_cvt_pk_bf16_f32 v253, v234, v235\n" \
  "v_mfma_f32_16x16x32_bf16 v[224:227], v[40:43], a[8:11], v[224:227]\n" \
  "v_cvt_pk_bf16_f32 v254, v236, v237\n" \
  "v_mfma_f32_16x16x32_bf16 v[228:231], v[64:67], a[8:11], v[228:231]\n" \
  "v_cvt_pk_bf16_f32 v255, v238, v239\n" \
  "v_mfma_f32_16x16x32_bf16 v[224:227], v[44:47], a[12:15], v[224:227]\n" \
  "s_add_u32 s72, s77, 589824\n" \
  "v_mfma_f32_16x16x32_bf16 v[228:231], v[68:71], a[12:15], v[228:231]\n" \
  "buffer_store_dwordx4 v[252:255], v3, s[36:39], s72 offen sc1\n" \
  "v_mfma_f32_16x16x32_bf16 v[224:227], v[48:51], a[16:19], v[224:227]\n" \
  "s_waitcnt vmcnt(7)\n" \
  "v_mfma_f32_16x16x32_bf16 v[228:231], v[72:75], a[16:19], v[228:231]\n" \
  "v_cmp_ge_u32 vcc, s76, v11\n" \
  "v_mfma_f32_16x16x32_bf16 v[224:227], v[52:55], a[20:23], v[224:227]\n" \
  "v_mfma_f32_16x16x32_bf16 v[228:231], v[76:79], a[20:23], v[228:231]\n" \
  "s_cbranch_vccnz LVP0_%=\n" \
  "LVK0_%=:\n" \
  "v_mfma_f32_16x16x32_bf16 v[232:235], v[32:35], a[24:27], 0\n" \
  "v_mfma_f32_16x16x32_bf16 v[236:239], v[56:59], a[24:27], 0\n" \
  "v_cvt_pk_bf16_f32 v248, v224, v225\n" \
  "v_mfma_f32_16x16x32_bf16 v[232:235], v[36:39], a[28:31], v[232:235]\n" \
  "v_cvt_pk_bf16_f32 v249, v226, v227\n" \
  "v_mfma_f32_16x16x32_bf16 v[236:239], v[60:63], a[28:31], v[236:239]\n" \
  "v_cvt_pk_bf16_f32 v250, v228, v229\n" \
  "v_mfma_f32_16x16x32_bf16 v[232:235], v[40:43], a[32:35], v[232:235]\n" \
  "v_cvt_pk_bf16_f32 v251, v230, v231\n" \
  "v_mfma_f32_16x16x32_bf16 v[236:239], v[64:67], a[32:35], v[236:239]\n" \
  "s_add_u32 s72, s68, 0\n" \
  "v_mfma_f32_16x16x32_bf16 v[232:235], v[44:47], a[36:39], v[232:235]\n" \
  "buffer_store_dwordx4 v[248:251], v3, s[36:39], s72 offen sc1\n" \
  "v_mfma_f32_16x16x32_bf16 v[236:239], v[68:71], a[36:39], v[236:239]\n" \
  "v_readlane_b32 s71, v6, s76\n" \
  "v_mfma_f32_16x16x32_bf16 v[232:235], v[48:51], a[40:43], v[232:235]\n" \
  "s_add_u32 m0, s73, 0\n" \
  "v_mfma_f32_16x16x32_bf16 v[236:239], v[72:75], a[40:43], v[236:239]\n" \
  "s_nop 2\n" \
  "v_mfma_f32_16x16x32_bf16 v[232:235], v[52:55], a[44:47], v[232:235]\n" \
  "buffer_load_dwordx4 v1, s[36:39], s71 offen offset:0 sc1 lds\n" \
  "v_mfma_f32_16x16x32_bf16 v[236:239], v[76:79], a[44:47], v[236:239]\n" \
  "buffer_load_dwordx4 v1, s[36:39], s71 offen offset:1024 sc1 lds\n" \
  "v_mfma_f32_16x16x32_bf16 v[224:227], v[32:35], a[48:51], 0\n" \
  "v_mfma_f32_16x16x32_bf16 v[228:231], v[56:59], a[48:51], 0\n" \
  "v_mfma_f32_16x16x32_bf16 v[224:227], v[36:39], a[52:55], v[224:227]\n" \
  "v_cvt_pk_bf16_f32 v252, v232, v233\n" \
  "v_mfma_f32_16x16x32_bf16 v[228:231], v[60:63], a[52:55], v[228:231]\n" \
  "v_cvt_pk_bf16_f32 v253, v234, v235\n" \
  "v_mfma_f32_16x16x32_bf16 v[224:227], v[40:43], a[56:59], v[224:227]\n" \
  "v_cvt_pk_bf16_f32 v254, v236, v237\n" \
  "v_mfma_f32_16x16x32_bf16 v[228:231], v[64:67], a[56:59], v[228:231]\n" \
  "v_cvt_pk_bf16_f32 v255, v238, v239\n" \
  "v_mfma_f32_16x16x32_bf16 v[224:227], v[44:47], a[60:63], v[224:227]\n" \
  "s_add_u32 s72, s68, 65536\n" \
  "v_mfma_f32_16x16x32_bf16 v[228:231], v[68:71], a[60:63], v[228:231]\n" \
  "buffer_store_dwordx4 v[252:255], v3, s[36:39], s72 offen sc1\n" \
  "v_mfma_f32_16x16x32_bf16 v[224:227], v[48:51], a[64:67], v[224:227]\n" \
  "s_add_u32 m0, s74, 0\n" \
  "v_mfma_f32_16x16x32_bf16 v[228:231], v[72:75], a[64:67], v[228:231]\n" \
  "s_add_u32 s76, s76, 1\n" \
  "v_mfma_f32_16x16x32_bf16 v[224:227], v[52:55], a[68:71], v[224:227]\n" \
  "buffer_load_dwordx4 v2, s[36:39], s71 offen sc1 lds\n" \
  "v_mfma_f32_16x16x32_bf16 v[228:231], v[76:79], a[68:71], v[228:231]\n" \
  "s_min_u32 s76, s76, 63\n" \
  "v_mfma_f32_16x16x32_bf16 v[232:235], v[32:35], a[72:75], 0\n" \
  "v_mfma_f32_16x16x32_bf16 v[236:239], v[56:59], a[72:75], 0\n" \
  "v_mfma_f32_16x16x32_bf16 v[232:235], v[36:39], a[76:79], v[232:235]\n" \
  "v_cvt_pk_bf16_f32 v248, v224, v225\n" \
  "v_mfma_f32_16x16x32_bf16 v[236:239], v[60:63], a[76:79], v[236:239]\n" \
  "v_cvt_pk_bf16_f32 v249, v226, v227\n" \
  "v_mfma_f32_16x16x32_bf16 v[232:235], v[40:43], a[80:83], v[232:235]\n" \
  "v_cvt_pk_bf16_f32 v250, v228, v229\n" \
  "v_mfma_f32_16x16x32_bf16 v[236:239], v[64:67], a[80:83], v[236:239]\n" \
  "v_cvt_pk_bf16_f32 v251, v230, v231\n" \
  "v_mfma_f32_16x16x32_bf16 v[232:235], v[44:47], a[84:87], v[232:235]\n" \
  "s_add_u32 s72, s69, 131072\n" \
  "v_mfma_f32_16x16x32_bf16 v[236:239], v[68:71], a[84:87], v[236:239]\n" \
  "buffer_store_dwordx4 v[248:251], v3, s[36:39], s72 offen sc1\n" \
  "v_mfma_f32_16x16x32_bf16 v[232:235], v[48:51], a[88:91], v[232:235]\n" \
  "v_mfma_f32_16x16x32_bf16 v[236:239], v[72:75], a[88:91], v[236:239]\n" \
  "v_mfma_f32_16x16x32_bf16 v[232:235], v[52:55], a[92:95], v[232:235]\n" \
  "v_mfma_f32_16x16x32_bf16 v[236:239], v[76:79], a[92:95], v[236:239]\n" \
  "v_mfma_f32_16x16x32_bf16 v[224:227], v[32:35], a[96:99], 0\n" \
  "v_mfma_f32_16x16x32_bf16 v[228:231], v[56:59], a[96:99], 0\n" \
  "v_mfma_f32_16x16x32_bf16 v[224:227], v[36:39], a[100:103], v[224:227]\n" \
  "v_cvt_pk_bf16_f32 v252, v232, v233\n" \
  "v_mfma_f32_16x16x32_bf16 v[228:231], v[60:63], a[100:103], v[228:231]\n" \
  "v_cvt_pk_bf16_f32 v253, v234, v235\n" \
  "v_mfma_f32_16x16x32_bf16 v[224:227], v[40:43], a[104:107], v[224:227]\n" \
  "v_cvt_pk_bf16_f32 v254, v236, v237\n" \
  "v_mfma_f32_16x16x32_bf16 v[228:231], v[64:67], a[104:107], v[228:231]\n" \
  "v_cvt_pk_bf16_f32 v255, v238, v239\n" \
  "v_mfma_f32_16x16x32_bf16 v[224:227], v[44:47], a[108:111], v[224:227]\n" \
  "s_add_u32 s72, s69, 196608\n" \
  "v_mfma_f32_16x16x32_bf16 v[228:231], v[68:71], a[108:111], v[228:231]\n" \
  "buffer_store_dwordx4 v[252:255], v3, s[36:39], s72 offen sc1\n" \
  "v_mfma_f32_16x16x32_bf16 v[224:227], v[48:51], a[112:115], v[224:227]\n" \
  "buffer_load_dword v11, v9, s[36:39], 0 offen sc1\n" \
  "v_mfma_f32_16x16x32_bf16 v[228:231], v[72:75], a[112:115], v[228:231]\n" \
  "v_mfma_f32_16x16x32_bf16 v[224:227], v[52:55], a[116:119], v[224:227]\n" \
  "v_mfma_f32_16x16x32_bf16 v[228:231], v[76:79], a[116:119], v[228:231]\n" \
  "v_mfma_f32_16x16x32_bf16 v[232:235], v[32:35], a[120:123], 0\n" \
  "v_mfma_f32_16x16x32_bf16 v[236:239], v[56:59], a[120:123], 0\n" \
  "v_mfma_f32_16x16x32_bf16 v[232:235], v[36:39], a[124:127], v[232:235]\n" \
  "v_cvt_pk_bf16_f32 v248, v224, v225\n" \
  "v_mfma_f32_16x16x32_bf16 v[236:239], v[60:63], a[124:127], v[236:239]\n" \
  "v_cvt_pk_bf16_f32 v249, v226, v227\n" \
  "v_mfma_f32_16x16x32_bf16 v[232:235], v[40:43], a[128:131], v[232:235]\n" \
  "v_cvt_pk_bf16_f32 v250, v228, v229\n" \
  "v_mfma_f32_16x16x32_bf16 v[236:239], v[64:67], a[128:131], v[236:239]\n" \
  "v_cvt_pk_bf16_f32 v251, v230, v231\n" \
  "v_mfma_f32_16x16x32_bf16 v[232:235], v[44:47], a[132:135], v[232:235]\n" \
  "s_add_u32 s72, s69, 262144\n" \
  "v_mfma_f32_16x16x32_bf16 v[236:239], v[68:71], a[132:135], v[236:239]\n" \
  "buffer_store_dwordx4 v[248:251], v3, s[36:39], s72 offen sc1\n" \
  "v_mfma_f32_16x16x32_bf16 v[232:235], v[48:51], a[136:139], v[232:235]\n" \
  "v_mfma_f32_16x16x32_bf16 v[236:239], v[72:75], a[136:139], v[236:239]\n" \
  "v_mfma_f32_16x16x32_bf16 v[232:235], v[52:55], a[140:143], v[232:235]\n" \
  "v_mfma_f32_16x16x32_bf16 v[236:239], v[76:79], a[140:143], v[236:239]\n" \
  "v_mfma_f32_16x16x32_bf16 v[224:227], v[32:35], a[144:147], 0\n" \
  "v_mfma_f32_16x16x32_bf16 v[228:231], v[56:59], a[144:147], 0\n" \
  "v_mfma_f32_16x16x32_bf16 v[224:227], v[36:39], a[148:151], v[224:227]\n" \
  "v_cvt_pk_bf16_f32 v252, v232, v233\n" \
  "v_mfma_f32_16x16x32_bf16 v[228:231], v[60:63], a[148:151], v[228:231]\n" \
  "v_cvt_pk_bf16_f32 v253, v234, v235\n" \
  "v_mfma_f32_16x16x32_bf16 v[224:227], v[40:43], a[152:155], v[224:227]\n" \
  "v_cvt_pk_bf16_f32 v254, v236, v237\n" \
  "v_mfma_f32_16x16x32_bf16 v[228:231], v[64:67], a[152:155], v[228:231]\n" \
  "v_cvt_pk_bf16_f32 v255, v238, v239\n" \
  "v_mfma_f32_16x16x32_bf16 v[224:227], v[44:47], a[156:159], v[224:227]\n" \
  "s_add_u32 s72, s69, 327680\n" \
  "v_mfma_f32_16x16x32_bf16 v[228:231], v[68:71], a[156:159], v[228:231]\n" \
  "buffer_store_dwordx4 v[252:255], v3, s[36:39], s72 offen sc1\n" \
  "v_mfma_f32_16x16x32_bf16 v[224:227], v[48:51], a[160:163], v[224:227]\n" \
  "v_mov_b32 v10, s52\n" \
  "v_mfma_f32_16x16x32_bf16 v[228:231], v[72:75], a[160:163], v[228:231]\n" \
  "s_waitcnt vmcnt(10)\n" \
  "v_mfma_f32_16x16x32_bf16 v[224:227], v[52:55], a[164:167], v[224:227]\n" \
  "buffer_store_dword v10, v7, s[36:39], 0 offen sc1\n" \
  "v_mfma_f32_16x16x32_bf16 v[228:231], v[76:79], a[164:167], v[228:231]\n" \
  "s_barrier\n" \
  "v_mfma_f32_16x16x32_bf16 v[232:235], v[32:35], a[168:171], 0\n" \
  "v_mfma_f32_16x16x32_bf16 v[236:239], v[56:59], a[168:171], 0\n" \
  "v_cvt_pk_bf16_f32 v248, v224, v225\n" \
  "v_cvt_pk_bf16_f32 v249, v226, v227\n" \
  "v_mfma_f32_16x16x32_bf16 v[232:235], v[36:39], a[172:175], v[232:235]\n" \
  "v_cvt_pk_bf16_f32 v250, v228, v229\n" \
  "v_cvt_pk_bf16_f32 v251, v230, v231\n" \
  "v_mfma_f32_16x16x32_bf16 v[236:239], v[60:63], a[172:175], v[236:239]\n" \
  "s_add_u32 s72, s69, 393216\n" \
  "v_mfma_f32_16x16x32_bf16 v[232:235], v[40:43], a[176:179], v[232:235]\n" \
  "buffer_store_dwordx4 v[248:251], v3, s[36:39], s72 offen sc1\n" \
  "v_mfma_f32_16x16x32_bf16 v[236:239], v[64:67], a[176:179], v[236:239]\n" \
  "ds_read_b128 v[80:83], v12 offset:16384\n" \
  "v_mfma_f32_16x16x32_bf16 v[232:235], v[44:47], a[180:183], v[232:235]\n" \
  "ds_read_b128 v[104:107], v12 offset:17408\n" \
  "v_mfma_f32_16x16x32_bf16 v[236:239], v[68:71], a[180:183], v[236:239]\n" \
  "ds_read_b128 v[84:87], v12 offset:18432\n" \
  "v_mfma_f32_16x16x32_bf16 v[232:235], v[48:51], a[184:187], v[232:235]\n" \
  "ds_read_b128 v[108:111], v12 offset:19456\n" \
  "v_mfma_f32_16x16x32_bf16 v[236:239], v[72:75], a[184:187], v[236:239]\n" \
  "ds_read_b128 v[88:91], v12 offset:20480\n" \
  "v_mfma_f32_16x16x32_bf16 v[232:235], v[52:55], a[188:191], v[232:235]\n" \
  "ds_read_b128 v[112:115], v12 offset:21504\n" \
  "v_mfma_f32_16x16x32_bf16 v[236:239], v[76:79], a[188:191], v[236:239]\n" \
  "ds_read_b128 v[92:95], v12 offset:22528\n" \
  "v_mfma_f32_16x16x32_bf16 v[224:227], v[32:35], a[192:195], 0\n" \
  "v_mfma_f32_16x16x32_bf16 v[228:231], v[56:59], a[192:195], 0\n" \
  "v_cvt_pk_bf16_f32 v252, v232, v233\n" \
  "v_cvt_pk_bf16_f32 v253, v234, v235\n" \
  "v_mfma_f32_16x16x32_bf16 v[224:227], v[36:39], a[196:199], v[224:227]\n" \
  "v_cvt_pk_bf16_f32 v254, v236, v237\n" \
  "v_cvt_pk_bf16_f32 v255, v238, v239\n" \
  "v_mfma_f32_16x16x32_bf16 v[228:231], v[60:63], a[196:199], v[228:231]\n" \
  "s_add_u32 s72, s69, 458752\n" \
  "v_mfma_f32_16x16x32_bf16 v[224:227], v[40:43], a[200:203], v[224:227]\n" \
  "buffer_store_dwordx4 v[252:255], v3, s[36:39], s72 offen sc1\n" \
  "v_mfma_f32_16x16x32_bf16 v[228:231], v[64:67], a[200:203], v[228:231]\n" \
  "ds_read_b128 v[116:119], v12 offset:23552\n" \
  "v_mfma_f32_16x16x32_bf16 v[224:227], v[44:47], a[204:207], v[224:227]\n" \
  "ds_read_b64 v[96:97], v12 offset:24576\n" \
  "v_mfma_f32_16x16x32_bf16 v[228:231], v[68:71], a[204:207], v[228:231]\n" \
  "ds_read_b64 v[120:121], v12 offset:24584\n" \
  "v_mfma_f32_16x16x32_bf16 v[224:227], v[48:51], a[208:211], v[224:227]\n" \
  "ds_read_b64 v[98:99], v12 offset:25600\n" \
  "v_mfma_f32_16x16x32_bf16 v[228:231], v[72:75], a[208:211], v[228:231]\n" \
  "ds_read_b64 v[122:123], v12 offset:25608\n" \
  "v_mfma_f32_16x16x32_bf16 v[224:227], v[52:55], a[212:215], v[224:227]\n" \
  "ds_read_b64 v[100:101], v12 offset:26624\n" \
  "v_mfma_f32_16x16x32_bf16 v[228:231], v[76:79], a[212:215], v[228:231]\n" \
  "ds_read_b64 v[124:125], v12 offset:26632\n" \
  "v_mfma_f32_16x16x32_bf16 v[232:235], v[32:35], a[216:219], 0\n" \
  "v_mfma_f32_16x16x32_bf16 v[236:239], v[56:59], a[216:219], 0\n" \
  "v_mfma_f32_16x16x32_bf16 v[232:235], v[36:39], a[220:223], v[232:235]\n" \
  "v_cvt_pk_bf16_f32 v248, v224, v225\n" \
  "v_mfma_f32_16x16x32_bf16 v[236:239], v[60:63], a[220:223], v[236:239]\n" \
  "v_cvt_pk_bf16_f32 v249, v226, v227\n" \
  "v_mfma_f32_16x16x32_bf16 v[232:235], v[40:43], a[224:227], v[232:235]\n" \
  "v_cvt_pk_bf16_f32 v250, v228, v229\n" \
  "v_mfma_f32_16x16x32_bf16 v[236:239], v[64:67], a[224:227], v[236:239]\n" \
  "v_cvt_pk_bf16_f32 v251, v230, v231\n" \
  "v_mfma_f32_16x16x32_bf16 v[232:235], v[44:47], a[228:231], v[232:235]\n" \
  "s_add_u32 s72, s69, 524288\n" \
  "v_mfma_f32_16x16x32_bf16 v[236:239], v[68:71], a[228:231], v[236:239]\n" \
  "buffer_store_dwordx4 v[248:251], v3, s[36:39], s72 offen sc1\n" \
  "v_mfma_f32_16x16x32_bf16 v[232:235], v[48:51], a[232:235], v[232:235]\n" \
  "ds_read_b64 v[102:103], v12 offset:27648\n" \
  "v_mfma_f32_16x16x32_bf16 v[236:239], v[72:75], a[232:235], v[236:239]\n" \
  "ds_read_b64 v[126:127], v12 offset:27656\n" \
  "v_mfma_f32_16x16x32_bf16 v[232:235], v[52:55], a[236:239], v[232:235]\n" \
  "s_add_u32 s52, s52, 1\n" \
  "v_mfma_f32_16x16x32_bf16 v[236:239], v[76:79], a[236:239], v[236:239]\n" \
  "s_cmp_eq_u32 s52, s53\n" \
  "s_waitcnt lgkmcnt(0)\n" \
  "s_mov_b32 s77, s69\n" \
  "v_readlane_b32 s68, v4, s52\n" \
  "v_readlane_b32 s69, v5, s52\n" \
  "v_mfma_f32_16x16x32_bf16 v[224:227], v[80:83], a[0:3], 0\n" \
  "v_mfma_f32_16x16x32_bf16 v[228:231], v[104:107], a[0:3], 0\n" \
  "v_mfma_f32_16x16x32_bf16 v[224:227], v[84:87], a[4:7], v[224:227]\n" \
  "v_cvt_pk_bf16_f32 v252, v232, v233\n" \
  "v_mfma_f32_16x16x32_bf16 v[228:231], v[108:111], a[4:7], v[228:231]\n" \
  "v_cvt_pk_bf16_f32 v253, v234, v235\n" \
  "v_mfma_f32_16x16x32_bf16 v[224:227], v[88:91], a[8:11], v[224:227]\n" \
  "v_cvt_pk_bf16_f32 v254, v236, v237\n" \
  "v_mfma_f32_16x16x32_bf16 v[228:231], v[112:115], a[8:11], v[228:231]\n" \
  "v_cvt_pk_bf16_f32 v255, v238, v239\n" \
  "v_mfma_f32_16x16x32_bf16 v[224:227], v[92:95], a[12:15], v[224:227]\n" \
  "s_add_u32 s72, s77, 589824\n" \
  "v_mfma_f32_16x16x32_bf16 v[228:231], v[116:119], a[12:15], v[228:231]\n" \
  "buffer_store_dwordx4 v[252:255], v3, s[36:39], s72 offen sc1\n" \
  "v_mfma_f32_16x16x32_bf16 v[224:227], v[96:99], a[16:19], v[224:227]\n" \
  "s_waitcnt vmcnt(7)\n" \
  "v_mfma_f32_16x16x32_bf16 v[228:231], v[120:123], a[16:19], v[228:231]\n" \
  "v_cmp_ge_u32 vcc, s76, v11\n" \
  "v_mfma_f32_16x16x32_bf16 v[224:227], v[100:103], a[20:23], v[224:227]\n" \
  "v_mfma_f32_16x16x32_bf16 v[228:231], v[124:127], a[20:23], v[228:231]\n" \
  "s_cbranch_vccnz LVP1_%=\n" \
  "LVK1_%=:\n" \
  "v_mfma_f32_16x16x32_bf16 v[232:235], v[80:83], a[24:27], 0\n" \
  "v_mfma_f32_16x16x32_bf16 v[236:239], v[104:107], a[24:27], 0\n" \
  "v_cvt_pk_bf16_f32 v248, v224, v225\n" \
  "v_mfma_f32_16x16x32_bf16 v[232:235], v[84:87], a[28:31], v[232:235]\n" \
  "v_cvt_pk_bf16_f32 v249, v226, v227\n" \
  "v_mfma_f32_16x16x32_bf16 v[236:239], v[108:111], a[28:31], v[236:239]\n" \
  "v_cvt_pk_bf16_f32 v250, v228, v229\n" \
  "v_mfma_f32_16x16x32_bf16 v[232:235], v[88:91], a[32:35], v[232:235]\n" \
  "v_cvt_pk_bf16_f32 v251, v230, v231\n" \
  "v_mfma_f32_16x16x32_bf16 v[236:239], v[112:115], a[32:35], v[236:239]\n" \
  "s_add_u32 s72, s68, 0\n" \
  "v_mfma_f32_16x16x32_bf16 v[232:235], v[92:95], a[36:39], v[232:235]\n" \
  "buffer_store_dwordx4 v[248:251], v3, s[36:39], s72 offen sc1\n" \
  "v_mfma_f32_16x16x32_bf16 v[236:239], v[116:119], a[36:39], v[236:239]\n" \
  "v_readlane_b32 s71, v6, s76\n" \
  "v_mfma_f32_16x16x32_bf16 v[232:235], v[96:99], a[40:43], v[232:235]\n" \
  "s_add_u32 m0, s73, 16384\n" \
  "v_mfma_f32_16x16x32_bf16 v[236:239], v[120:123], a[40:43], v[236:239]\n" \
  "s_nop 2\n" \
  "v_mfma_f32_16x16x32_bf16 v[232:235], v[100:103], a[44:47], v[232:235]\n" \
  "buffer_load_dwordx4 v1, s[36:39], s71 offen offset:0 sc1 lds\n" \
  "v_mfma_f32_16x16x32_bf16 v[236:239], v[124:127], a[44:47], v[236:239]\n" \
  "buffer_load_dwordx4 v1, s[36:39], s71 offen offset:1024 sc1 lds\n" \
  "v_mfma_f32_16x16x32_bf16 v[224:227], v[80:83], a[48:51], 0\n" \
  "v_mfma_f32_16x16x32_bf16 v[228:231], v[104:107], a[48:51], 0\n" \
  "v_mfma_f32_16x16x32_bf16 v[224:227], v[84:87], a[52:55], v[224:227]\n" \
  "v_cvt_pk_bf16_f32 v252, v232, v233\n" \
  "v_mfma_f32_16x16x32_bf16 v[228:231], v[108:111], a[52:55], v[228:231]\n" \
  "v_cvt_pk_bf16_f32 v253, v234, v235\n" \
  "v_mfma_f32_16x16x32_bf16 v[224:227], v[88:91], a[56:59], v[224:227]\n" \
  "v_cvt_pk_bf16_f32 v254, v236, v237\n" \
  "v_mfma_f32_16x16x32_bf16 v[228:231], v[112:115], a[56:59], v[228:231]\n" \
  "v_cvt_pk_bf16_f32 v255, v238, v239\n" \
  "v_mfma_f32_16x16x32_bf16 v[224:227], v[92:95], a[60:63], v[224:227]\n" \
  "s_add_u32 s72, s68, 65536\n" \
  "v_mfma_f32_16x16x32_bf16 v[228:231], v[116:119], a[60:63], v[228:231]\n" \
  "buffer_store_dwordx4 v[252:255], v3, s[36:39], s72 offen sc1\n" \
  "v_mfma_f32_16x16x32_bf16 v[224:227], v[96:99], a[64:67], v[224:227]\n" \
  "s_add_u32 m0, s74, 16384\n" \
  "v_mfma_f32_16x16x32_bf16 v[228:231], v[120:123], a[64:67], v[228:231]\n" \
  "s_add_u32 s76, s76, 1\n" \
  "v_mfma_f32_16x16x32_bf16 v[224:227], v[100:103], a[68:71], v[224:227]\n" \
  "buffer_load_dwordx4 v2, s[36:39], s71 offen sc1 lds\n" \
  "v_mfma_f32_16x16x32_bf16 v[228:231], v[124:127], a[68:71], v[228:231]\n" \
  "s_min_u32 s76, s76, 63\n" \
  "v_mfma_f32_16x16x32_bf16 v[232:235], v[80:83], a[72:75], 0\n" \
  "v_mfma_f32_16x16x32_bf16 v[236:239], v[104:107], a[72:75], 0\n" \
  "v_mfma_f32_16x16x32_bf16 v[232:235], v[84:87], a[76:79], v[232:235]\n" \
  "v_cvt_pk_bf16_f32 v248, v224, v225\n" \
  "v_mfma_f32_16x16x32_bf16 v[236:239], v[108:111], a[76:79], v[236:239]\n" \
  "v_cvt_pk_bf16_f32 v249, v226, v227\n" \
  "v_mfma_f32_16x16x32_bf16 v[232:235], v[88:91], a[80:83], v[232:235]\n" \
  "v_cvt_pk_bf16_f32 v250, v228, v229\n" \
  "v_mfma_f32_16x16x32_bf16 v[236:239], v[112:115], a[80:83], v[236:239]\n" \
  "v_cvt_pk_bf16_f32 v251, v230, v231\n" \
  "v_mfma_f32_16x16x32_bf16 v[232:235], v[92:95], a[84:87], v[232:235]\n" \
  "s_add_u32 s72, s69, 131072\n" \
  "v_mfma_f32_16x16x32_bf16 v[236:239], v[116:119], a[84:87], v[236:239]\n" \
  "buffer_store_dwordx4 v[248:251], v3, s[36:39], s72 offen sc1\n" \
  "v_mfma_f32_16x16x32_bf16 v[232:235], v[96:99], a[88:91], v[232:235]\n" \
  "v_mfma_f32_16x16x32_bf16 v[236:239], v[120:123], a[88:91], v[236:239]\n" \
  "v_mfma_f32_16x16x32_bf16 v[232:235], v[100:103], a[92:95], v[232:235]\n" \
  "v_mfma_f32_16x16x32_bf16 v[236:239], v[124:127], a[92:95], v[236:239]\n" \
  "v_mfma_f32_16x16x32_bf16 v[224:227], v[80:83], a[96:99], 0\n" \
  "v_mfma_f32_16x16x32_bf16 v[228:231], v[104:107], a[96:99], 0\n" \
  "v_mfma_f32_16x16x32_bf16 v[224:227], v[84:87], a[100:103], v[224:227]\n" \
  "v_cvt_pk_bf16_f32 v252, v232, v233\n" \
  "v_mfma_f32_16x16x32_bf16 v[228:231], v[108:111], a[100:103], v[228:231]\n" \
  "v_cvt_pk_bf16_f32 v253, v234, v235\n" \
  "v_mfma_f32_16x16x32_bf16 v[224:227], v[88:91], a[104:107], v[224:227]\n" \
  "v_cvt_pk_bf16_f32 v254, v236, v237\n" \
  "v_mfma_f32_16x16x32_bf16 v[228:231], v[112:115], a[104:107], v[228:231]\n" \
  "v_cvt_pk_bf16_f32 v255, v238, v239\n" \
  "v_mfma_f32_16x16x32_bf16 v[224:227], v[92:95], a[108:111], v[224:227]\n" \
  "s_add_u32 s72, s69, 196608\n" \
  "v_mfma_f32_16x16x32_bf16 v[228:231], v[116:119], a[108:111], v[228:231]\n" \
  "buffer_store_dwordx4 v[252:255], v3, s[36:39], s72 offen sc1\n" \
  "v_mfma_f32_16x16x32_bf16 v[224:227], v[96:99], a[112:115], v[224:227]\n" \
  "buffer_load_dword v11, v9, s[36:39], 0 offen sc1\n" \
  "v_mfma_f32_16x16x32_bf16 v[228:231], v[120:123], a[112:115], v[228:231]\n" \
  "v_mfma_f32_16x16x32_bf16 v[224:227], v[100:103], a[116:119], v[224:227]\n" \
  "v_mfma_f32_16x16x32_bf16 v[228:231], v[124:127], a[116:119], v[228:231]\n" \
  "v_mfma_f32_16x16x32_bf16 v[232:235], v[80:83], a[120:123], 0\n" \
  "v_mfma_f32_16x16x32_bf16 v[236:239], v[104:107], a[120:123], 0\n" \
  "v_mfma_f32_16x16x32_bf16 v[232:235], v[84:87], a[124:127], v[232:235]\n" \
  "v_cvt_pk_bf16_f32 v248, v224, v225\n" \
  "v_mfma_f32_16x16x32_bf16 v[236:239], v[108:111], a[124:127], v[236:239]\n" \
  "v_cvt_pk_bf16_f32 v249, v226, v227\n" \
  "v_mfma_f32_16x16x32_bf16 v[232:235], v[88:91], a[128:131], v[232:235]\n" \
  "v_cvt_pk_bf16_f32 v250, v228, v229\n" \
  "v_mfma_f32_16x16x32_bf16 v[236:239], v[112:115], a[128:131], v[236:239]\n" \
  "v_cvt_pk_bf16_f32 v251, v230, v231\n" \
  "v_mfma_f32_16x16x32_bf16 v[232:235], v[92:95], a[132:135], v[232:235]\n" \
  "s_add_u32 s72, s69, 262144\n" \
  "v_mfma_f32_16x16x32_bf16 v[236:239], v[116:119], a[132:135], v[236:239]\n" \
  "buffer_store_dwordx4 v[248:251], v3, s[36:39], s72 offen sc1\n" \
  "v_mfma_f32_16x16x32_bf16 v[232:235], v[96:99], a[136:139], v[232:235]\n" \
  "s_xor_b32 s73, s73, 0x8000\n" \
  "v_mfma_f32_16x16x32_bf16 v[236:239], v[120:123], a[136:139], v[236:239]\n" \
  "s_xor_b32 s74, s74, 0x8000\n" \
  "v_mfma_f32_16x16x32_bf16 v[232:235], v[100:103], a[140:143], v[232:235]\n" \
  "v_xor_b32 v12, 0x8000, v12\n" \
  "v_mfma_f32_16x16x32_bf16 v[236:239], v[124:127], a[140:143], v[236:239]\n" \
  "v_mfma_f32_16x16x32_bf16 v[224:227], v[80:83], a[144:147], 0\n" \
  "v_mfma_f32_16x16x32_bf16 v[228:231], v[104:107], a[144:147], 0\n" \
  "v_mfma_f32_16x16x32_bf16 v[224:227], v[84:87], a[148:151], v[224:227]\n" \
  "v_cvt_pk_bf16_f32 v252, v232, v233\n" \
  "v_mfma_f32_16x16x32_bf16 v[228:231], v[108:111], a[148:151], v[228:231]\n" \
  "v_cvt_pk_bf16_f32 v253, v234, v235\n" \
  "v_mfma_f32_16x16x32_bf16 v[224:227], v[88:91], a[152:155], v[224:227]\n" \
  "v_cvt_pk_bf16_f32 v254, v236, v237\n" \
  "v_mfma_f32_16x16x32_bf16 v[228:231], v[112:115], a[152:155], v[228:231]\n" \
  "v_cvt_pk_bf16_f32 v255, v238, v239\n" \
  "v_mfma_f32_16x16x32_bf16 v[224:227], v[92:95], a[156:159], v[224:227]\n" \
  "s_add_u32 s72, s69, 327680\n" \
  "v_mfma_f32_16x16x32_bf16 v[228:231], v[116:119], a[156:159], v[228:231]\n" \
  "buffer_store_dwordx4 v[252:255], v3, s[36:39], s72 offen sc1\n" \
  "v_mfma_f32_16x16x32_bf16 v[224:227], v[96:99], a[160:163], v[224:227]\n" \
  "v_mov_b32 v10, s52\n" \
  "v_mfma_f32_16x16x32_bf16 v[228:231], v[120:123], a[160:163], v[228:231]\n" \
  "s_waitcnt vmcnt(10)\n" \
  "v_mfma_f32_16x16x32_bf16 v[224:227], v[100:103], a[164:167], v[224:227]\n" \
  "buffer_store_dword v10, v7, s[36:39], 0 offen sc1\n" \
  "v_mfma_f32_16x16x32_bf16 v[228:231], v[124:127], a[164:167], v[228:231]\n" \
  "s_barrier\n" \
  "v_mfma_f32_16x16x32_bf16 v[232:235], v[80:83], a[168:171], 0\n" \
  "v_mfma_f32_16x16x32_bf16 v[236:239], v[104:107], a[168:171], 0\n" \
  "v_cvt_pk_bf16_f32 v248, v224, v225\n" \
  "v_cvt_pk_bf16_f32 v249, v226, v227\n" \
  "v_mfma_f32_16x16x32_bf16 v[232:235], v[84:87], a[172:175], v[232:235]\n" \
  "v_cvt_pk_bf16_f32 v250, v228, v229\n" \
  "v_cvt_pk_bf16_f32 v251, v230, v231\n" \
  "v_mfma_f32_16x16x32_bf16 v[236:239], v[108:111], a[172:175], v[236:239]\n" \
  "s_add_u32 s72, s69, 393216\n" \
  "v_mfma_f32_16x16x32_bf16 v[232:235], v[88:91], a[176:179], v[232:235]\n" \
  "buffer_store_dwordx4 v[248:251], v3, s[36:39], s72 offen sc1\n" \
  "v_mfma_f32_16x16x32_bf16 v[236:239], v[112:115], a[176:179], v[236:239]\n" \
  "ds_read_b128 v[32:35], v12 offset:0\n" \
  "v_mfma_f32_16x16x32_bf16 v[232:235], v[92:95], a[180:183], v[232:235]\n" \
  "ds_read_b128 v[56:59], v12 offset:1024\n" \
  "v_mfma_f32_16x16x32_bf16 v[236:239], v[116:119], a[180:183], v[236:239]\n" \
  "ds_read_b128 v[36:39], v12 offset:2048\n" \
  "v_mfma_f32_16x16x32_bf16 v[232:235], v[96:99], a[184:187], v[232:235]\n" \
  "ds_read_b128 v[60:63], v12 offset:3072\n" \
  "v_mfma_f32_16x16x32_bf16 v[236:239], v[120:123], a[184:187], v[236:239]\n" \
  "ds_read_b128 v[40:43], v12 offset:4096\n" \
  "v_mfma_f32_16x16x32_bf16 v[232:235], v[100:103], a[188:191], v[232:235]\n" \
  "ds_read_b128 v[64:67], v12 offset:5120\n" \
  "v_mfma_f32_16x16x32_bf16 v[236:239], v[124:127], a[188:191], v[236:239]\n" \
  "ds_read_b128 v[44:47], v12 offset:6144\n" \
  "v_mfma_f32_16x16x32_bf16 v[224:227], v[80:83], a[192:195], 0\n" \
  "v_mfma_f32_16x16x32_bf16 v[228:231], v[104:107], a[192:195], 0\n" \
  "v_cvt_pk_bf16_f32 v252, v232, v233\n" \
  "v_cvt_pk_bf16_f32 v253, v234, v235\n" \
  "v_mfma_f32_16x16x32_bf16 v[224:227], v[84:87], a[196:199], v[224:227]\n" \
  "v_cvt_pk_bf16_f32 v254, v236, v237\n" \
  "v_cvt_pk_bf16_f32 v255, v238, v239\n" \
  "v_mfma_f32_16x16x32_bf16 v[228:231], v[108:111], a[196:199], v[228:231]\n" \
  "s_add_u32 s72, s69, 458752\n" \
  "v_mfma_f32_16x16x32_bf16 v[224:227], v[88:91], a[200:203], v[224:227]\n" \
  "buffer_store_dwordx4 v[252:255], v3, s[36:39], s72 offen sc1\n" \
  "v_mfma_f32_16x16x32_bf16 v[228:231], v[112:115], a[200:203], v[228:231]\n" \
  "ds_read_b128 v[68:71], v12 offset:7168\n" \
  "v_mfma_f32_16x16x32_bf16 v[224:227], v[92:95], a[204:207], v[224:227]\n" \
  "ds_read_b64 v[48:49], v12 offset:8192\n" \
  "v_mfma_f32_16x16x32_bf16 v[228:231], v[116:119], a[204:207], v[228:231]\n" \
  "ds_read_b64 v[72:73], v12 offset:8200\n" \
  "v_mfma_f32_16x16x32_bf16 v[224:227], v[96:99], a[208:211], v[224:227]\n" \
  "ds_read_b64 v[50:51], v12 offset:9216\n" \
  "v_mfma_f32_16x16x32_bf16 v[228:231], v[120:123], a[208:211], v[228:231]\n" \
  "ds_read_b64 v[74:75], v12 offset:9224\n" \
  "v_mfma_f32_16x16x32_bf16 v[224:227], v[100:103], a[212:215], v[224:227]\n" \
  "ds_read_b64 v[52:53], v12 offset:10240\n" \
  "v_mfma_f32_16x16x32_bf16 v[228:231], v[124:127], a[212:215], v[228:231]\n" \
  "ds_read_b64 v[76:77], v12 offset:10248\n" \
  "v_mfma_f32_16x16x32_bf16 v[232:235], v[80:83], a[216:219], 0\n" \
  "v_mfma_f32_16x16x32_bf16 v[236:239], v[104:107], a[216:219], 0\n" \
  "v_mfma_f32_16x16x32_bf16 v[232:235], v[84:87], a[220:223], v[232:235]\n" \
  "v_cvt_pk_bf16_f32 v248, v224, v225\n" \
  "v_mfma_f32_16x16x32_bf16 v[236:239], v[108:111], a[220:223], v[236:239]\n" \
  "v_cvt_pk_bf16_f32 v249, v226, v227\n" \
  "v_mfma_f32_16x16x32_bf16 v[232:235], v[88:91], a[224:227], v[232:235]\n" \
  "v_cvt_pk_bf16_f32 v250, v228, v229\n" \
  "v_mfma_f32_16x16x32_bf16 v[236:239], v[112:115], a[224:227], v[236:239]\n" \
  "v_cvt_pk_bf16_f32 v251, v230, v231\n" \
  "v_mfma_f32_16x16x32_bf16 v[232:235], v[92:95], a[228:231], v[232:235]\n" \
  "s_add_u32 s72, s69, 524288\n" \
  "v_mfma_f32_16x16x32_bf16 v[236:239], v[116:119], a[228:231], v[236:239]\n" \
  "buffer_store_dwordx4 v[248:251], v3, s[36:39], s72 offen sc1\n" \
  "v_mfma_f32_16x16x32_bf16 v[232:235], v[96:99], a[232:235], v[232:235]\n" \
  "ds_read_b64 v[54:55], v12 offset:11264\n" \
  "v_mfma_f32_16x16x32_bf16 v[236:239], v[120:123], a[232:235], v[236:239]\n" \
  "ds_read_b64 v[78:79], v12 offset:11272\n" \
  "v_mfma_f32_16x16x32_bf16 v[232:235], v[100:103], a[236:239], v[232:235]\n" \
  "s_add_u32 s52, s52, 1\n" \
  "v_mfma_f32_16x16x32_bf16 v[236:239], v[124:127], a[236:239], v[236:239]\n" \
  "s_cmp_eq_u32 s52, s53\n" \
  "s_waitcnt lgkmcnt(0)\n" \
  "s_cbranch_scc0 LVL_%=\n" \
  "s_mov_b32 s77, s69\n" \
  "s_nop 7\n" \
  "v_cvt_pk_bf16_f32 v252, v232, v233\n" \
  "v_cvt_pk_bf16_f32 v253, v234, v235\n" \
  "v_cvt_pk_bf16_f32 v254, v236, v237\n" \
  "v_cvt_pk_bf16_f32 v255, v238, v239\n" \
  "s_add_u32 s72, s77, 589824\n" \
  "buffer_store_dwordx4 v[252:255], v3, s[36:39], s72 offen sc1\n" \
  "v_mov_b32 v10, s52\n" \
  "s_waitcnt vmcnt(0)\n" \
  "buffer_store_dword v10, v7, s[36:39], 0 offen sc1\n" \
  "s_branch LVD_%=\n" \
  "LVP0_%=:\n" \
  "buffer_load_dword v11, v9, s[36:39], 0 offen sc1\n" \
  "s_waitcnt vmcnt(0)\n" \
  "v_cmp_ge_u32 vcc, s76, v11\n" \
  "s_nop 3\n" \
  "s_cbranch_vccz LVK0_%=\n" \
  "s_add_u32 s75, s75, 1\n" \
  "s_cmp_gt_u32 s75, 200000\n" \
  "s_cbranch_scc1 LVK0_%=\n" \
  "s_sleep 2\n" \
  "s_branch LVP0_%=\n" \
  "LVP1_%=:\n" \
  "buffer_load_dword v11, v9, s[36:39], 0 offen sc1\n" \
  "s_waitcnt vmcnt(0)\n" \
  "v_cmp_ge_u32 vcc, s76, v11\n" \
  "s_nop 3\n" \
  "s_cbranch_vccz LVK1_%=\n" \
  "s_add_u32 s75, s75, 1\n" \
  "s_cmp_gt_u32 s75, 200000\n" \
  "s_cbranch_scc1 LVK1_%=\n" \
  "s_sleep 2\n" \
  "s_branch LVP1_%=\n" \
  "LVD_%=:\n"
#define UG_CLOBBERS \
  "v32", "v33", "v34", "v35", "v36", "v37", "v38", "v39", "v40", "v41", "v42", "v43", "v44", "v45", "v46", "v47", "v48", "v49", "v50", "v51", "v52", \
  "v53", "v54", "v55", "v56", "v57", "v58", "v59", "v60", "v61", "v62", "v63", "v64", "v65", "v66", "v67", "v68", "v69", "v70", "v71", "v72", "v73", \
  "v74", "v75", "v76", "v77", "v78", "v79", "v80", "v81", "v82", "v83", "v84", "v85", "v86", "v87", "v88", "v89", "v90", "v91", "v92", "v93", "v94", \
  "v95", "v96", "v97", "v98", "v99", "v100", "v101", "v102", "v103", "v104", "v105", "v106", "v107", "v108", "v109", "v110", "v111", "v112", "v113", \
  "v114", "v115", "v116", "v117", "v118", "v119", "v120", "v121", "v122", "v123", "v124", "v125", "v126", "v127", "v128", "v129", "v130", "v131", \
  "v132", "v133", "v134", "v135", "v136", "v137", "v138", "v139", "v140", "v141", "v142", "v143", "v144", "v145", "v146", "v147", "v148", "v149", \
  "v150", "v151", "v152", "v153", "v154", "v155", "v156", "v157", "v158", "v159", "v160", "v161", "v162", "v163", "v164", "v165", "v166", "v167", \
  "v168", "v169", "v170", "v171", "v172", "v173", "v174", "v175", "v176", "v177", "v178", "v179", "v180", "v181", "v182", "v183", "v184", "v185", \
  "v186", "v187", "v188", "v189", "v190", "v191", "v192", "v193", "v194", "v195", "v196", "v197", "v198", "v199", "v200", "v201", "v202", "v203", \
  "v204", "v205", "v206", "v207", "s68", "s69", "s70", "s71", "s72", "s73", "s74", "s75", "s76", "s77", "s78", "s79", "s80", "s81"
#define UG_ASM \
  "s_mul_i32 s74, s52, 21504\n" \
  "s_mul_i32 s73, s52, 61440\n" \
  "s_add_u32 s77, s73, 61440\n" \
  "LUW_%=:\n" \
  "s_add_u32 m0, s74, 0\n" \
  "s_nop 0\n" \
  "buffer_load_dwordx4 v0, s[44:47], s73 offen offset:0 lds\n" \
  "buffer_load_dwordx4 v1, s[44:47], s73 offen offset:1024 lds\n" \
  "buffer_load_dwordx4 v2, s[44:47], s73 offen offset:2048 lds\n" \
  "buffer_load_dwordx4 v3, s[44:47], s73 offen offset:3072 lds\n" \
  "s_add_u32 m0, s74, 4096\n" \
  "s_nop 0\n" \
  "buffer_load_dwordx4 v4, s[44:47], s73 offen offset:0 lds\n" \
  "buffer_load_dwordx4 v5, s[44:47], s73 offen offset:1024 lds\n" \
  "buffer_load_dwordx4 v6, s[44:47], s73 offen offset:2048 lds\n" \
  "s_add_u32 s74, s74, 7168\n" \
  "s_add_u32 s73, s73, 20480\n" \
  "s_cmp_lg_u32 s73, s77\n" \
  "s_cbranch_scc1 LUW_%=\n" \
  "s_and_b32 s73, s52, 1\n" \
  "s_lshr_b32 s74, s52, 1\n" \
  "s_mul_i32 s68, s73, 10\n" \
  "s_add_u32 s77, s68, 10\n" \
  "s_add_u32 s75, s68, s74\n" \
  "s_mul_i32 s75, s75, 393216\n" \
  "s_lshl_b32 s73, s73, 1\n" \
  "s_add_u32 s73, s73, s74\n" \
  "s_mul_i32 s76, s73, 12288\n" \
  "s_add_u32 s76, s76, 86016\n" \
  "s_add_u32 m0, s76, 0\n" \
  "s_nop 0\n" \
  "buffer_load_dwordx4 v8, s[40:43], s75 offen offset:0 lds\n" \
  "buffer_load_dwordx4 v9, s[40:43], s75 offen offset:1024 lds\n" \
  "buffer_load_dwordx4 v10, s[40:43], s75 offen offset:2048 lds\n" \
  "buffer_load_dwordx4 v11, s[40:43], s75 offen offset:3072 lds\n" \
  "s_add_u32 m0, s76, 4096\n" \
  "s_nop 0\n" \
  "buffer_load_dwordx4 v12, s[40:43], s75 offen offset:0 lds\n" \
  "buffer_load_dwordx4 v13, s[40:43], s75 offen offset:1024 lds\n" \
  "buffer_load_dwordx4 v14, s[40:43], s75 offen offset:2048 lds\n" \
  "buffer_load_dwordx4 v15, s[40:43], s75 offen offset:3072 lds\n" \
  "s_add_u32 m0, s76, 8192\n" \
  "s_nop 0\n" \
  "buffer_load_dwordx4 v16, s[40:43], s75 offen offset:0 lds\n" \
  "buffer_load_dwordx4 v17, s[40:43], s75 offen offset:1024 lds\n" \
  "buffer_load_dwordx4 v18, s[40:43], s75 offen offset:2048 lds\n" \
  "buffer_load_dwordx4 v19, s[40:43], s75 offen offset:3072 lds\n" \
  "s_add_u32 s75, s75, 786432\n" \
  "v_mov_b32 v1, v231\n" \
  "v_mov_b32 v2, v232\n" \
  "v_mov_b32 v3, v233\n" \
  "v_mov_b32 v4, v234\n" \
  "v_mov_b32 v5, v235\n" \
  "v_mov_b32 v6, v236\n" \
  "v_mov_b32 v0, 1\n" \
  "s_waitcnt vmcnt(0)\n" \
  "s_barrier\n" \
  "buffer_load_dword v241, v27, s[40:43], 0 offen\n" \
  "buffer_load_dword v241, v28, s[40:43], 0 offen\n" \
  "buffer_load_dword v241, v29, s[40:43], 0 offen\n" \
  "v_add_u32 v27, 43008, v7\n" \
  "v_add_u32 v28, 43008, v26\n" \
  "ds_read_b128 v[32:35], v20 offset:0\n" \
  "ds_read_b128 v[56:59], v20 offset:768\n" \
  "ds_read_b128 v[80:83], v20 offset:12288\n" \
  "ds_read_b128 v[104:107], v20 offset:13056\n" \
  "ds_read_b64_tr_b16 v[128:129], v7 offset:0\n" \
  "ds_read_b64_tr_b16 v[130:131], v7 offset:1792\n" \
  "ds_read_b128 v[36:39], v21 offset:0\n" \
  "ds_read_b128 v[60:63], v21 offset:768\n" \
  "ds_read_b128 v[84:87], v21 offset:12288\n" \
  "ds_read_b128 v[108:111], v21 offset:13056\n" \
  "ds_read_b64_tr_b16 v[132:133], v7 offset:14336\n" \
  "ds_read_b64_tr_b16 v[134:135], v7 offset:16128\n" \
  "s_waitcnt lgkmcnt(2)\n" \
  "ds_read_b128 v[40:43], v20 offset:128\n" \
  "ds_read_b128 v[64:67], v20 offset:896\n" \
  "ds_read_b128 v[88:91], v20 offset:12416\n" \
  "ds_read_b128 v[112:115], v20 offset:13184\n" \
  "ds_read_b64_tr_b16 v[136:137], v7 offset:28672\n" \
  "ds_read_b64_tr_b16 v[138:139], v7 offset:30464\n" \
  "ds_read_b128 v[44:47], v21 offset:128\n" \
  "ds_read_b128 v[68:71], v21 offset:896\n" \
  "ds_read_b128 v[92:95], v21 offset:12416\n" \
  "ds_read_b128 v[116:119], v21 offset:13184\n" \
  "ds_read_b64_tr_b16 v[140:141], v27 offset:0\n" \
  "ds_read_b64_tr_b16 v[142:143], v27 offset:1792\n" \
  "s_waitcnt lgkmcnt(2)\n" \
  "ds_read_b128 v[48:51], v20 offset:256\n" \
  "ds_read_b128 v[72:75], v20 offset:1024\n" \
  "ds_read_b128 v[96:99], v20 offset:12544\n" \
  "ds_read_b128 v[120:123], v20 offset:13312\n" \
  "ds_read_b64_tr_b16 v[144:145], v27 offset:14336\n" \
  "ds_read_b64_tr_b16 v[146:147], v27 offset:16128\n" \
  "ds_read_b128 v[52:55], v21 offset:256\n" \
  "ds_read_b128 v[76:79], v21 offset:1024\n" \
  "ds_read_b128 v[100:103], v21 offset:12544\n" \
  "ds_read_b128 v[124:127], v21 offset:13312\n" \
  "s_waitcnt lgkmcnt(2)\n" \
  "s_mov_b32 s71, 0x40000000\n" \
  "s_mov_b32 s72, 0x40000000\n" \
  "s_mov_b32 s88, 0x7ffffa00\n" \
  "s_mov_b32 s81, 0\n" \
  "s_waitcnt lgkmcnt(0)\n" \
  "LUR_%=:\n" \
  "v_mfma_f32_16x16x32_bf16 v[152:155], v[32:35], v[128:131], 0\n" \
  "ds_read_b64_tr_b16 v[148:149], v27 offset:28672\n" \
  "v_mfma_f32_16x16x32_bf16 v[156:159], v[56:59], v[128:131], 0\n" \
  "s_waitcnt lgkmcnt(8)\n" \
  "ds_read_b64_tr_b16 v[150:151], v27 offset:30464\n" \
  "v_mfma_f32_16x16x32_bf16 v[160:163], v[80:83], v[128:131], 0\n" \
  "v_cvt_pk_bf16_f32 v200, v184, v185\n" \
  "v_cvt_pk_bf16_f32 v201, v186, v187\n" \
  "v_mfma_f32_16x16x32_bf16 v[164:167], v[104:107], v[128:131], 0\n" \
  "v_cvt_pk_bf16_f32 v202, v188, v189\n" \
  "v_cvt_pk_bf16_f32 v203, v190, v191\n" \
  "v_mfma_f32_16x16x32_bf16 v[152:155], v[36:39], v[132:135], v[152:155]\n" \
  "ds_read_b64_tr_b16 v[128:129], v26 offset:0\n" \
  "v_mfma_f32_16x16x32_bf16 v[156:159], v[60:63], v[132:135], v[156:159]\n" \
  "ds_read_b64_tr_b16 v[130:131], v26 offset:1792\n" \
  "v_mfma_f32_16x16x32_bf16 v[160:163], v[84:87], v[132:135], v[160:163]\n" \
  "s_add_u32 s73, s71, 393216\n" \
  "v_mfma_f32_16x16x32_bf16 v[164:167], v[108:111], v[132:135], v[164:167]\n" \
  "buffer_store_dwordx4 v[200:203], v22, s[36:39], s73 offen sc1\n" \
  "v_mfma_f32_16x16x32_bf16 v[152:155], v[40:43], v[136:139], v[152:155]\n" \
  "ds_read_b64_tr_b16 v[132:133], v26 offset:14336\n" \
  "v_mfma_f32_16x16x32_bf16 v[156:159], v[64:67], v[136:139], v[156:159]\n" \
  "ds_read_b64_tr_b16 v[134:135], v26 offset:16128\n" \
  "v_mfma_f32_16x16x32_bf16 v[160:163], v[88:91], v[136:139], v[160:163]\n" \
  "v_cvt_pk_bf16_f32 v204, v192, v193\n" \
  "v_cvt_pk_bf16_f32 v205, v194, v195\n" \
  "v_mfma_f32_16x16x32_bf16 v[164:167], v[112:115], v[136:139], v[164:167]\n" \
  "v_cvt_pk_bf16_f32 v206, v196, v197\n" \
  "v_cvt_pk_bf16_f32 v207, v198, v199\n" \
  "v_mfma_f32_16x16x32_bf16 v[152:155], v[44:47], v[140:143], v[152:155]\n" \
  "ds_read_b64_tr_b16 v[136:137], v26 offset:28672\n" \
  "v_mfma_f32_16x16x32_bf16 v[156:159], v[68:71], v[140:143], v[156:159]\n" \
  "ds_read_b64_tr_b16 v[138:139], v26 offset:30464\n" \
  "v_mfma_f32_16x16x32_bf16 v[160:163], v[92:95], v[140:143], v[160:163]\n" \
  "s_add_u32 s73, s72, 393216\n" \
  "v_mfma_f32_16x16x32_bf16 v[164:167], v[116:119], v[140:143], v[164:167]\n" \
  "buffer_store_dwordx4 v[204:207], v22, s[36:39], s73 offen sc1\n" \
  "s_waitcnt lgkmcnt(10)\n" \
  "v_mfma_f32_16x16x32_bf16 v[152:155], v[48:51], v[144:147], v[152:155]\n" \
  "ds_read_b64_tr_b16 v[140:141], v28 offset:0\n" \
  "v_mfma_f32_16x16x32_bf16 v[156:159], v[72:75], v[144:147], v[156:159]\n" \
  "ds_read_b64_tr_b16 v[142:143], v28 offset:1792\n" \
  "v_mfma_f32_16x16x32_bf16 v[160:163], v[96:99], v[144:147], v[160:163]\n" \
  "v_readlane_b32 s69, v23, s68\n" \
  "v_readlane_b32 s71, v24, s68\n" \
  "s_sub_u32 s69, s69, s62\n" \
  "s_sub_u32 s71, s71, s63\n" \
  "s_add_u32 s68, s68, 1\n" \
  "v_mfma_f32_16x16x32_bf16 v[164:167], v[120:123], v[144:147], v[164:167]\n" \
  "v_readlane_b32 s70, v23, s68\n" \
  "v_readlane_b32 s72, v24, s68\n" \
  "s_sub_u32 s70, s70, s62\n" \
  "s_sub_u32 s72, s72, s63\n" \
  "s_add_u32 s68, s68, 1\n" \
  "s_waitcnt lgkmcnt(8)\n" \
  "v_mfma_f32_16x16x32_bf16 v[152:155], v[52:55], v[148:151], v[152:155]\n" \
  "ds_read_b64_tr_b16 v[144:145], v28 offset:14336\n" \
  "v_mfma_f32_16x16x32_bf16 v[156:159], v[76:79], v[148:151], v[156:159]\n" \
  "ds_read_b64_tr_b16 v[146:147], v28 offset:16128\n" \
  "v_mfma_f32_16x16x32_bf16 v[160:163], v[100:103], v[148:151], v[160:163]\n" \
  "s_cmp_eq_u32 s68, s77\n" \
  "s_cselect_b32 s75, 0x40000000, s75\n" \
  "v_mfma_f32_16x16x32_bf16 v[164:167], v[124:127], v[148:151], v[164:167]\n" \
  "s_barrier\n" \
  "s_waitcnt lgkmcnt(8)\n" \
  "v_mfma_f32_16x16x32_bf16 v[168:171], v[32:35], v[128:131], 0\n" \
  "ds_read_b64_tr_b16 v[148:149], v28 offset:28672\n" \
  "v_mfma_f32_16x16x32_bf16 v[172:175], v[56:59], v[128:131], 0\n" \
  "ds_read_b64_tr_b16 v[150:151], v28 offset:30464\n" \
  "v_mfma_f32_16x16x32_bf16 v[176:179], v[80:83], v[128:131], 0\n" \
  "v_cvt_pk_bf16_f32 v200, v152, v153\n" \
  "v_cvt_pk_bf16_f32 v201, v154, v155\n" \
  "v_mfma_f32_16x16x32_bf16 v[180:183], v[104:107], v[128:131], 0\n" \
  "s_add_u32 m0, s76, 0\n" \
  "s_nop 0\n" \
  "buffer_load_dwordx4 v8, s[40:43], s75 offen offset:0 lds\n" \
  "s_waitcnt lgkmcnt(8)\n" \
  "v_mfma_f32_16x16x32_bf16 v[168:171], v[36:39], v[132:135], v[168:171]\n" \
  "ds_read_b64_tr_b16 v[128:129], v7 offset:64\n" \
  "v_mfma_f32_16x16x32_bf16 v[172:175], v[60:63], v[132:135], v[172:175]\n" \
  "ds_read_b64_tr_b16 v[130:131], v7 offset:1856\n" \
  "v_mfma_f32_16x16x32_bf16 v[176:179], v[84:87], v[132:135], v[176:179]\n" \
  "v_cvt_pk_bf16_f32 v202, v156, v157\n" \
  "v_cvt_pk_bf16_f32 v203, v158, v159\n" \
  "v_mfma_f32_16x16x32_bf16 v[180:183], v[108:111], v[132:135], v[180:183]\n" \
  "s_add_u32 s73, s69, 0\n" \
  "s_waitcnt lgkmcnt(8)\n" \
  "v_mfma_f32_16x16x32_bf16 v[168:171], v[40:43], v[136:139], v[168:171]\n" \
  "ds_read_b64_tr_b16 v[132:133], v7 offset:14400\n" \
  "v_mfma_f32_16x16x32_bf16 v[172:175], v[64:67], v[136:139], v[172:175]\n" \
  "ds_read_b64_tr_b16 v[134:135], v7 offset:16192\n" \
  "v_mfma_f32_16x16x32_bf16 v[176:179], v[88:91], v[136:139], v[176:179]\n" \
  "buffer_load_dwordx4 v9, s[40:43], s75 offen offset:1024 lds\n" \
  "v_mfma_f32_16x16x32_bf16 v[180:183], v[112:115], v[136:139], v[180:183]\n" \
  "buffer_store_dwordx4 v[200:203], v22, s[36:39], s73 offen sc1\n" \
  "s_waitcnt lgkmcnt(8)\n" \
  "v_mfma_f32_16x16x32_bf16 v[168:171], v[44:47], v[140:143], v[168:171]\n" \
  "ds_read_b64_tr_b16 v[136:137], v7 offset:28736\n" \
  "v_mfma_f32_16x16x32_bf16 v[172:175], v[68:71], v[140:143], v[172:175]\n" \
  "ds_read_b64_tr_b16 v[138:139], v7 offset:30528\n" \
  "v_mfma_f32_16x16x32_bf16 v[176:179], v[92:95], v[140:143], v[176:179]\n" \
  "v_cvt_pk_bf16_f32 v204, v160, v161\n" \
  "v_cvt_pk_bf16_f32 v205, v162, v163\n" \
  "v_mfma_f32_16x16x32_bf16 v[180:183], v[116:119], v[140:143], v[180:183]\n" \
  "buffer_load_dwordx4 v10, s[40:43], s75 offen offset:2048 lds\n" \
  "s_waitcnt lgkmcnt(8)\n" \
  "v_mfma_f32_16x16x32_bf16 v[168:171], v[48:51], v[144:147], v[168:171]\n" \
  "ds_read_b64_tr_b16 v[140:141], v27 offset:64\n" \
  "v_mfma_f32_16x16x32_bf16 v[172:175], v[72:75], v[144:147], v[172:175]\n" \
  "ds_read_b64_tr_b16 v[142:143], v27 offset:1856\n" \
  "v_mfma_f32_16x16x32_bf16 v[176:179], v[96:99], v[144:147], v[176:179]\n" \
  "v_cvt_pk_bf16_f32 v206, v164, v165\n" \
  "v_cvt_pk_bf16_f32 v207, v166, v167\n" \
  "v_mfma_f32_16x16x32_bf16 v[180:183], v[120:123], v[144:147], v[180:183]\n" \
  "s_add_u32 s73, s70, 0\n" \
  "s_waitcnt lgkmcnt(8)\n" \
  "v_mfma_f32_16x16x32_bf16 v[168:171], v[52:55], v[148:151], v[168:171]\n" \
  "ds_read_b64_tr_b16 v[144:145], v27 offset:14400\n" \
  "v_mfma_f32_16x16x32_bf16 v[172:175], v[76:79], v[148:151], v[172:175]\n" \
  "ds_read_b64_tr_b16 v[146:147], v27 offset:16192\n" \
  "v_mfma_f32_16x16x32_bf16 v[176:179], v[100:103], v[148:151], v[176:179]\n" \
  "buffer_load_dwordx4 v11, s[40:43], s75 offen offset:3072 lds\n" \
  "v_mfma_f32_16x16x32_bf16 v[180:183], v[124:127], v[148:151], v[180:183]\n" \
  "buffer_store_dwordx4 v[204:207], v22, s[36:39], s73 offen sc1\n" \
  "s_waitcnt lgkmcnt(8)\n" \
  "v_mfma_f32_16x16x32_bf16 v[152:155], v[32:35], v[128:131], 0\n" \
  "ds_read_b64_tr_b16 v[148:149], v27 offset:28736\n" \
  "v_mfma_f32_16x16x32_bf16 v[156:159], v[56:59], v[128:131], 0\n" \
  "ds_read_b64_tr_b16 v[150:151], v27 offset:30528\n" \
  "v_mfma_f32_16x16x32_bf16 v[160:163], v[80:83], v[128:131], 0\n" \
  "v_cvt_pk_bf16_f32 v200, v168, v169\n" \
  "v_cvt_pk_bf16_f32 v201, v170, v171\n" \
  "v_mfma_f32_16x16x32_bf16 v[164:167], v[104:107], v[128:131], 0\n" \
  "s_add_u32 m0, s76, 4096\n" \
  "s_nop 0\n" \
  "buffer_load_dwordx4 v12, s[40:43], s75 offen offset:0 lds\n" \
  "s_waitcnt lgkmcnt(8)\n" \
  "v_mfma_f32_16x16x32_bf16 v[152:155], v[36:39], v[132:135], v[152:155]\n" \
  "ds_read_b64_tr_b16 v[128:129], v26 offset:64\n" \
  "v_mfma_f32_16x16x32_bf16 v[156:159], v[60:63], v[132:135], v[156:159]\n" \
  "ds_read_b64_tr_b16 v[130:131], v26 offset:1856\n" \
  "v_mfma_f32_16x16x32_bf16 v[160:163], v[84:87], v[132:135], v[160:163]\n" \
  "v_cvt_pk_bf16_f32 v202, v172, v173\n" \
  "v_cvt_pk_bf16_f32 v203, v174, v175\n" \
  "v_mfma_f32_16x16x32_bf16 v[164:167], v[108:111], v[132:135], v[164:167]\n" \
  "s_add_u32 s73, s69, 65536\n" \
  "s_waitcnt lgkmcnt(8)\n" \
  "v_mfma_f32_16x16x32_bf16 v[152:155], v[40:43], v[136:139], v[152:155]\n" \
  "ds_read_b64_tr_b16 v[132:133], v26 offset:14400\n" \
  "v_mfma_f32_16x16x32_bf16 v[156:159], v[64:67], v[136:139], v[156:159]\n" \
  "ds_read_b64_tr_b16 v[134:135], v26 offset:16192\n" \
  "v_mfma_f32_16x16x32_bf16 v[160:163], v[88:91], v[136:139], v[160:163]\n" \
  "buffer_load_dwordx4 v13, s[40:43], s75 offen offset:1024 lds\n" \
  "v_mfma_f32_16x16x32_bf16 v[164:167], v[112:115], v[136:139], v[164:167]\n" \
  "buffer_store_dwordx4 v[200:203], v22, s[36:39], s73 offen sc1\n" \
  "s_waitcnt lgkmcnt(8)\n" \
  "v_mfma_f32_16x16x32_bf16 v[152:155], v[44:47], v[140:143], v[152:155]\n" \
  "ds_read_b64_tr_b16 v[136:137], v26 offset:28736\n" \
  "v_mfma_f32_16x16x32_bf16 v[156:159], v[68:71], v[140:143], v[156:159]\n" \
  "ds_read_b64_tr_b16 v[138:139], v26 offset:30528\n" \
  "v_mfma_f32_16x16x32_bf16 v[160:163], v[92:95], v[140:143], v[160:163]\n" \
  "v_cvt_pk_bf16_f32 v204, v176, v177\n" \
  "v_cvt_pk_bf16_f32 v205, v178, v179\n" \
  "v_mfma_f32_16x16x32_bf16 v[164:167], v[116:119], v[140:143], v[164:167]\n" \
  "buffer_load_dwordx4 v14, s[40:43], s75 offen offset:2048 lds\n" \
  "s_waitcnt lgkmcnt(8)\n" \
  "v_mfma_f32_16x16x32_bf16 v[152:155], v[48:51], v[144:147], v[152:155]\n" \
  "ds_read_b64_tr_b16 v[140:141], v28 offset:64\n" \
  "v_mfma_f32_16x16x32_bf16 v[156:159], v[72:75], v[144:147], v[156:159]\n" \
  "ds_read_b64_tr_b16 v[142:143], v28 offset:1856\n" \
  "v_mfma_f32_16x16x32_bf16 v[160:163], v[96:99], v[144:147], v[160:163]\n" \
  "v_cvt_pk_bf16_f32 v206, v180, v181\n" \
  "v_cvt_pk_bf16_f32 v207, v182, v183\n" \
  "v_mfma_f32_16x16x32_bf16 v[164:167], v[120:123], v[144:147], v[164:167]\n" \
  "s_add_u32 s73, s70, 65536\n" \
  "s_waitcnt lgkmcnt(8)\n" \
  "v_mfma_f32_16x16x32_bf16 v[152:155], v[52:55], v[148:151], v[152:155]\n" \
  "ds_read_b64_tr_b16 v[144:145], v28 offset:14400\n" \
  "v_mfma_f32_16x16x32_bf16 v[156:159], v[76:79], v[148:151], v[156:159]\n" \
  "ds_read_b64_tr_b16 v[146:147], v28 offset:16192\n" \
  "v_mfma_f32_16x16x32_bf16 v[160:163], v[100:103], v[148:151], v[160:163]\n" \
  "buffer_load_dwordx4 v15, s[40:43], s75 offen offset:3072 lds\n" \
  "v_mfma_f32_16x16x32_bf16 v[164:167], v[124:127], v[148:151], v[164:167]\n" \
  "buffer_store_dwordx4 v[204:207], v22, s[36:39], s73 offen sc1\n" \
  "s_waitcnt lgkmcnt(8)\n" \
  "v_mfma_f32_16x16x32_bf16 v[168:171], v[32:35], v[128:131], 0\n" \
  "ds_read_b64_tr_b16 v[148:149], v28 offset:28736\n" \
  "v_mfma_f32_16x16x32_bf16 v[172:175], v[56:59], v[128:131], 0\n" \
  "ds_read_b64_tr_b16 v[150:151], v28 offset:30528\n" \
  "v_mfma_f32_16x16x32_bf16 v[176:179], v[80:83], v[128:131], 0\n" \
  "v_cvt_pk_bf16_f32 v200, v152, v153\n" \
  "v_cvt_pk_bf16_f32 v201, v154, v155\n" \
  "s_waitcnt vmcnt(12)\n" \
  "s_barrier\n" \
  "s_mov_b32 s78, s88\n" \
  "buffer_atomic_add v0, v25, s[36:39], s78 offen sc0 sc1\n" \
  "s_add_u32 s88, s88, 1536\n" \
  "s_and_b32 s88, s88, 0x3fffffff\n" \
  "v_mfma_f32_16x16x32_bf16 v[180:183], v[104:107], v[128:131], 0\n" \
  "s_add_u32 m0, s76, 8192\n" \
  "s_nop 0\n" \
  "buffer_load_dwordx4 v16, s[40:43], s75 offen offset:0 lds\n" \
  "s_waitcnt lgkmcnt(8)\n" \
  "v_mfma_f32_16x16x32_bf16 v[168:171], v[36:39], v[132:135], v[168:171]\n" \
  "ds_read_b64_tr_b16 v[128:129], v7 offset:128\n" \
  "v_mfma_f32_16x16x32_bf16 v[172:175], v[60:63], v[132:135], v[172:175]\n" \
  "ds_read_b64_tr_b16 v[130:131], v7 offset:1920\n" \
  "v_mfma_f32_16x16x32_bf16 v[176:179], v[84:87], v[132:135], v[176:179]\n" \
  "v_cvt_pk_bf16_f32 v202, v156, v157\n" \
  "v_cvt_pk_bf16_f32 v203, v158, v159\n" \
  "v_mfma_f32_16x16x32_bf16 v[180:183], v[108:111], v[132:135], v[180:183]\n" \
  "s_add_u32 s73, s69, 131072\n" \
  "s_waitcnt lgkmcnt(8)\n" \
  "v_mfma_f32_16x16x32_bf16 v[168:171], v[40:43], v[136:139], v[168:171]\n" \
  "ds_read_b64_tr_b16 v[132:133], v7 offset:14464\n" \
  "v_mfma_f32_16x16x32_bf16 v[172:175], v[64:67], v[136:139], v[172:175]\n" \
  "ds_read_b64_tr_b16 v[134:135], v7 offset:16256\n" \
  "v_mfma_f32_16x16x32_bf16 v[176:179], v[88:91], v[136:139], v[176:179]\n" \
  "buffer_load_dwordx4 v17, s[40:43], s75 offen offset:1024 lds\n" \
  "v_mfma_f32_16x16x32_bf16 v[180:183], v[112:115], v[136:139], v[180:183]\n" \
  "buffer_store_dwordx4 v[200:203], v22, s[36:39], s73 offen sc1\n" \
  "s_waitcnt lgkmcnt(8)\n" \
  "v_mfma_f32_16x16x32_bf16 v[168:171], v[44:47], v[140:143], v[168:171]\n" \
  "ds_read_b64_tr_b16 v[136:137], v7 offset:28800\n" \
  "v_mfma_f32_16x16x32_bf16 v[172:175], v[68:71], v[140:143], v[172:175]\n" \
  "ds_read_b64_tr_b16 v[138:139], v7 offset:30592\n" \
  "v_mfma_f32_16x16x32_bf16 v[176:179], v[92:95], v[140:143], v[176:179]\n" \
  "v_cvt_pk_bf16_f32 v204, v160, v161\n" \
  "v_cvt_pk_bf16_f32 v205, v162, v163\n" \
  "v_mfma_f32_16x16x32_bf16 v[180:183], v[116:119], v[140:143], v[180:183]\n" \
  "buffer_load_dwordx4 v18, s[40:43], s75 offen offset:2048 lds\n" \
  "s_waitcnt lgkmcnt(8)\n" \
  "v_mfma_f32_16x16x32_bf16 v[168:171], v[48:51], v[144:147], v[168:171]\n" \
  "ds_read_b64_tr_b16 v[140:141], v27 offset:128\n" \
  "v_mfma_f32_16x16x32_bf16 v[172:175], v[72:75], v[144:147], v[172:175]\n" \
  "ds_read_b64_tr_b16 v[142:143], v27 offset:1920\n" \
  "v_mfma_f32_16x16x32_bf16 v[176:179], v[96:99], v[144:147], v[176:179]\n" \
  "v_cvt_pk_bf16_f32 v206, v164, v165\n" \
  "v_cvt_pk_bf16_f32 v207, v166, v167\n" \
  "v_mfma_f32_16x16x32_bf16 v[180:183], v[120:123], v[144:147], v[180:183]\n" \
  "s_add_u32 s73, s70, 131072\n" \
  "s_waitcnt lgkmcnt(8)\n" \
  "v_mfma_f32_16x16x32_bf16 v[168:171], v[52:55], v[148:151], v[168:171]\n" \
  "ds_read_b64_tr_b16 v[144:145], v27 offset:14464\n" \
  "v_mfma_f32_16x16x32_bf16 v[172:175], v[76:79], v[148:151], v[172:175]\n" \
  "ds_read_b64_tr_b16 v[146:147], v27 offset:16256\n" \
  "v_mfma_f32_16x16x32_bf16 v[176:179], v[100:103], v[148:151], v[176:179]\n" \
  "buffer_load_dwordx4 v19, s[40:43], s75 offen offset:3072 lds\n" \
  "s_add_u32 s75, s75, 786432\n" \
  "v_mfma_f32_16x16x32_bf16 v[180:183], v[124:127], v[148:151], v[180:183]\n" \
  "buffer_store_dwordx4 v[204:207], v22, s[36:39], s73 offen sc1\n" \
  "s_waitcnt lgkmcnt(8)\n" \
  "v_mfma_f32_16x16x32_bf16 v[152:155], v[32:35], v[128:131], 0\n" \
  "ds_read_b64_tr_b16 v[148:149], v27 offset:28800\n" \
  "v_mfma_f32_16x16x32_bf16 v[156:159], v[56:59], v[128:131], 0\n" \
  "ds_read_b64_tr_b16 v[150:151], v27 offset:30592\n" \
  "v_mfma_f32_16x16x32_bf16 v[160:163], v[80:83], v[128:131], 0\n" \
  "v_cvt_pk_bf16_f32 v200, v168, v169\n" \
  "v_cvt_pk_bf16_f32 v201, v170, v171\n" \
  "v_mfma_f32_16x16x32_bf16 v[164:167], v[104:107], v[128:131], 0\n" \
  "v_cvt_pk_bf16_f32 v202, v172, v173\n" \
  "v_cvt_pk_bf16_f32 v203, v174, v175\n" \
  "s_waitcnt lgkmcnt(8)\n" \
  "v_mfma_f32_16x16x32_bf16 v[152:155], v[36:39], v[132:135], v[152:155]\n" \
  "ds_read_b64_tr_b16 v[128:129], v26 offset:128\n" \
  "v_mfma_f32_16x16x32_bf16 v[156:159], v[60:63], v[132:135], v[156:159]\n" \
  "ds_read_b64_tr_b16 v[130:131], v26 offset:1920\n" \
  "v_mfma_f32_16x16x32_bf16 v[160:163], v[84:87], v[132:135], v[160:163]\n" \
  "s_add_u32 s73, s69, 196608\n" \
  "v_mfma_f32_16x16x32_bf16 v[164:167], v[108:111], v[132:135], v[164:167]\n" \
  "buffer_store_dwordx4 v[200:203], v22, s[36:39], s73 offen sc1\n" \
  "s_waitcnt lgkmcnt(8)\n" \
  "v_mfma_f32_16x16x32_bf16 v[152:155], v[40:43], v[136:139], v[152:155]\n" \
  "ds_read_b64_tr_b16 v[132:133], v26 offset:14464\n" \
  "v_mfma_f32_16x16x32_bf16 v[156:159], v[64:67], v[136:139], v[156:159]\n" \
  "ds_read_b64_tr_b16 v[134:135], v26 offset:16256\n" \
  "v_mfma_f32_16x16x32_bf16 v[160:163], v[88:91], v[136:139], v[160:163]\n" \
  "v_cvt_pk_bf16_f32 v204, v176, v177\n" \
  "v_cvt_pk_bf16_f32 v205, v178, v179\n" \
  "v_mfma_f32_16x16x32_bf16 v[164:167], v[112:115], v[136:139], v[164:167]\n" \
  "v_cvt_pk_bf16_f32 v206, v180, v181\n" \
  "v_cvt_pk_bf16_f32 v207, v182, v183\n" \
  "s_waitcnt lgkmcnt(8)\n" \
  "v_mfma_f32_16x16x32_bf16 v[152:155], v[44:47], v[140:143], v[152:155]\n" \
  "ds_read_b64_tr_b16 v[136:137], v26 offset:28800\n" \
  "v_mfma_f32_16x16x32_bf16 v[156:159], v[68:71], v[140:143], v[156:159]\n" \
  "ds_read_b64_tr_b16 v[138:139], v26 offset:30592\n" \
  "v_mfma_f32_16x16x32_bf16 v[160:163], v[92:95], v[140:143], v[160:163]\n" \
  "s_add_u32 s73, s70, 196608\n" \
  "v_mfma_f32_16x16x32_bf16 v[164:167], v[116:119], v[140:143], v[164:167]\n" \
  "buffer_store_dwordx4 v[204:207], v22, s[36:39], s73 offen sc1\n" \
  "s_waitcnt lgkmcnt(8)\n" \
  "v_mfma_f32_16x16x32_bf16 v[152:155], v[48:51], v[144:147], v[152:155]\n" \
  "ds_read_b64_tr_b16 v[140:141], v28 offset:128\n" \
  "v_mfma_f32_16x16x32_bf16 v[156:159], v[72:75], v[144:147], v[156:159]\n" \
  "ds_read_b64_tr_b16 v[142:143], v28 offset:1920\n" \
  "v_mfma_f32_16x16x32_bf16 v[160:163], v[96:99], v[144:147], v[160:163]\n" \
  "s_getpc_b64 s[82:83]\n" \
  "LCF6_%=:\n" \
  "s_add_u32 s82, s82, LUN2_%=-LCF6_%=\n" \
  "s_addc_u32 s83, s83, 0\n" \
  "v_mbcnt_lo_u32_b32 v240, -1, 0\n" \
  "v_mbcnt_hi_u32_b32 v240, -1, v240\n" \
  "v_lshlrev_b32 v240, 6, v240\n" \
  "global_load_dword v241, v240, s[82:83]\n" \
  "v_mfma_f32_16x16x32_bf16 v[164:167], v[120:123], v[144:147], v[164:167]\n" \
  "s_waitcnt lgkmcnt(8)\n" \
  "v_mfma_f32_16x16x32_bf16 v[152:155], v[52:55], v[148:151], v[152:155]\n" \
  "ds_read_b64_tr_b16 v[144:145], v28 offset:14464\n" \
  "v_mfma_f32_16x16x32_bf16 v[156:159], v[76:79], v[148:151], v[156:159]\n" \
  "ds_read_b64_tr_b16 v[146:147], v28 offset:16256\n" \
  "v_mfma_f32_16x16x32_bf16 v[160:163], v[100:103], v[148:151], v[160:163]\n" \
  "v_mfma_f32_16x16x32_bf16 v[164:167], v[124:127], v[148:151], v[164:167]\n" \
  "s_waitcnt lgkmcnt(8)\n" \
  "v_mfma_f32_16x16x32_bf16 v[168:171], v[32:35], v[128:131], 0\n" \
  "ds_read_b64_tr_b16 v[148:149], v28 offset:28800\n" \
  "v_mfma_f32_16x16x32_bf16 v[172:175], v[56:59], v[128:131], 0\n" \
  "ds_read_b64_tr_b16 v[150:151], v28 offset:30592\n" \
  "v_mfma_f32_16x16x32_bf16 v[176:179], v[80:83], v[128:131], 0\n" \
  "v_cvt_pk_bf16_f32 v200, v152, v153\n" \
  "v_cvt_pk_bf16_f32 v201, v154, v155\n" \
  "v_mfma_f32_16x16x32_bf16 v[180:183], v[104:107], v[128:131], 0\n" \
  "v_cvt_pk_bf16_f32 v202, v156, v157\n" \
  "v_cvt_pk_bf16_f32 v203, v158, v159\n" \
  "s_waitcnt lgkmcnt(8)\n" \
  "v_mfma_f32_16x16x32_bf16 v[168:171], v[36:39], v[132:135], v[168:171]\n" \
  "ds_read_b64_tr_b16 v[128:129], v7 offset:192\n" \
  "v_mfma_f32_16x16x32_bf16 v[172:175], v[60:63], v[132:135], v[172:175]\n" \
  "ds_read_b64_tr_b16 v[130:131], v7 offset:1984\n" \
  "v_mfma_f32_16x16x32_bf16 v[176:179], v[84:87], v[132:135], v[176:179]\n" \
  "s_add_u32 s73, s69, 262144\n" \
  "v_mfma_f32_16x16x32_bf16 v[180:183], v[108:111], v[132:135], v[180:183]\n" \
  "buffer_store_dwordx4 v[200:203], v22, s[36:39], s73 offen sc1\n" \
  "s_waitcnt lgkmcnt(8)\n" \
  "v_mfma_f32_16x16x32_bf16 v[168:171], v[40:43], v[136:139], v[168:171]\n" \
  "ds_read_b64_tr_b16 v[132:133], v7 offset:14528\n" \
  "v_mfma_f32_16x16x32_bf16 v[172:175], v[64:67], v[136:139], v[172:175]\n" \
  "ds_read_b64_tr_b16 v[134:135], v7 offset:16320\n" \
  "v_mfma_f32_16x16x32_bf16 v[176:179], v[88:91], v[136:139], v[176:179]\n" \
  "v_cvt_pk_bf16_f32 v204, v160, v161\n" \
  "v_cvt_pk_bf16_f32 v205, v162, v163\n" \
  "v_mfma_f32_16x16x32_bf16 v[180:183], v[112:115], v[136:139], v[180:183]\n" \
  "v_cvt_pk_bf16_f32 v206, v164, v165\n" \
  "v_cvt_pk_bf16_f32 v207, v166, v167\n" \
  "s_waitcnt lgkmcnt(8)\n" \
  "v_mfma_f32_16x16x32_bf16 v[168:171], v[44:47], v[140:143], v[168:171]\n" \
  "ds_read_b64_tr_b16 v[136:137], v7 offset:28864\n" \
  "v_mfma_f32_16x16x32_bf16 v[172:175], v[68:71], v[140:143], v[172:175]\n" \
  "ds_read_b64_tr_b16 v[138:139], v7 offset:30656\n" \
  "v_mfma_f32_16x16x32_bf16 v[176:179], v[92:95], v[140:143], v[176:179]\n" \
  "s_add_u32 s73, s70, 262144\n" \
  "v_mfma_f32_16x16x32_bf16 v[180:183], v[116:119], v[140:143], v[180:183]\n" \
  "buffer_store_dwordx4 v[204:207], v22, s[36:39], s73 offen sc1\n" \
  "s_waitcnt lgkmcnt(8)\n" \
  "v_mfma_f32_16x16x32_bf16 v[168:171], v[48:51], v[144:147], v[168:171]\n" \
  "ds_read_b64_tr_b16 v[140:141], v27 offset:192\n" \
  "v_mfma_f32_16x16x32_bf16 v[172:175], v[72:75], v[144:147], v[172:175]\n" \
  "ds_read_b64_tr_b16 v[142:143], v27 offset:1984\n" \
  "v_mfma_f32_16x16x32_bf16 v[176:179], v[96:99], v[144:147], v[176:179]\n" \
  "v_mfma_f32_16x16x32_bf16 v[180:183], v[120:123], v[144:147], v[180:183]\n" \
  "s_waitcnt lgkmcnt(8)\n" \
  "v_mfma_f32_16x16x32_bf16 v[168:171], v[52:55], v[148:151], v[168:171]\n" \
  "ds_read_b64_tr_b16 v[144:145], v27 offset:14528\n" \
  "v_mfma_f32_16x16x32_bf16 v[172:175], v[76:79], v[148:151], v[172:175]\n" \
  "ds_read_b64_tr_b16 v[146:147], v27 offset:16320\n" \
  "v_mfma_f32_16x16x32_bf16 v[176:179], v[100:103], v[148:151], v[176:179]\n" \
  "v_mfma_f32_16x16x32_bf16 v[180:183], v[124:127], v[148:151], v[180:183]\n" \
  "s_waitcnt vmcnt(6)\n" \
  "s_barrier\n" \
  "v_readfirstlane_b32 s74, v0\n" \
  "v_mov_b32 v0, 1\n" \
  "s_cmp_eq_u32 s74, 11\n" \
  "s_cselect_b32 s73, s78, 0x40000000\n" \
  "buffer_store_dword v0, v31, s[36:39], s73 offen sc1\n" \
  "s_waitcnt lgkmcnt(8)\n" \
  "v_mfma_f32_16x16x32_bf16 v[184:187], v[32:35], v[128:131], 0\n" \
  "ds_read_b128 v[32:35], v20 offset:0\n" \
  "ds_read_b64_tr_b16 v[148:149], v27 offset:28864\n" \
  "v_mfma_f32_16x16x32_bf16 v[188:191], v[56:59], v[128:131], 0\n" \
  "ds_read_b128 v[56:59], v20 offset:768\n" \
  "ds_read_b64_tr_b16 v[150:151], v27 offset:30656\n" \
  "v_mfma_f32_16x16x32_bf16 v[192:195], v[80:83], v[128:131], 0\n" \
  "ds_read_b128 v[80:83], v20 offset:12288\n" \
  "v_cvt_pk_bf16_f32 v200, v168, v169\n" \
  "v_cvt_pk_bf16_f32 v201, v170, v171\n" \
  "v_mfma_f32_16x16x32_bf16 v[196:199], v[104:107], v[128:131], 0\n" \
  "ds_read_b128 v[104:107], v20 offset:13056\n" \
  "v_cvt_pk_bf16_f32 v202, v172, v173\n" \
  "v_cvt_pk_bf16_f32 v203, v174, v175\n" \
  "s_waitcnt lgkmcnt(12)\n" \
  "v_mfma_f32_16x16x32_bf16 v[184:187], v[36:39], v[132:135], v[184:187]\n" \
  "ds_read_b128 v[36:39], v21 offset:0\n" \
  "ds_read_b64_tr_b16 v[128:129], v7 offset:0\n" \
  "v_mfma_f32_16x16x32_bf16 v[188:191], v[60:63], v[132:135], v[188:191]\n" \
  "ds_read_b128 v[60:63], v21 offset:768\n" \
  "s_waitcnt lgkmcnt(8)\n" \
  "ds_read_b64_tr_b16 v[130:131], v7 offset:1792\n" \
  "v_mfma_f32_16x16x32_bf16 v[192:195], v[84:87], v[132:135], v[192:195]\n" \
  "ds_read_b128 v[84:87], v21 offset:12288\n" \
  "s_add_u32 s73, s69, 327680\n" \
  "v_mfma_f32_16x16x32_bf16 v[196:199], v[108:111], v[132:135], v[196:199]\n" \
  "ds_read_b128 v[108:111], v21 offset:13056\n" \
  "buffer_store_dwordx4 v[200:203], v22, s[36:39], s73 offen sc1\n" \
  "v_mfma_f32_16x16x32_bf16 v[184:187], v[40:43], v[136:139], v[184:187]\n" \
  "ds_read_b128 v[40:43], v20 offset:128\n" \
  "ds_read_b64_tr_b16 v[132:133], v7 offset:14336\n" \
  "v_mfma_f32_16x16x32_bf16 v[188:191], v[64:67], v[136:139], v[188:191]\n" \
  "ds_read_b128 v[64:67], v20 offset:896\n" \
  "ds_read_b64_tr_b16 v[134:135], v7 offset:16128\n" \
  "v_mfma_f32_16x16x32_bf16 v[192:195], v[88:91], v[136:139], v[192:195]\n" \
  "s_waitcnt lgkmcnt(8)\n" \
  "ds_read_b128 v[88:91], v20 offset:12416\n" \
  "v_cvt_pk_bf16_f32 v204, v176, v177\n" \
  "v_cvt_pk_bf16_f32 v205, v178, v179\n" \
  "v_mfma_f32_16x16x32_bf16 v[196:199], v[112:115], v[136:139], v[196:199]\n" \
  "ds_read_b128 v[112:115], v20 offset:13184\n" \
  "v_cvt_pk_bf16_f32 v206, v180, v181\n" \
  "v_cvt_pk_bf16_f32 v207, v182, v183\n" \
  "v_mfma_f32_16x16x32_bf16 v[184:187], v[44:47], v[140:143], v[184:187]\n" \
  "ds_read_b128 v[44:47], v21 offset:128\n" \
  "ds_read_b64_tr_b16 v[136:137], v7 offset:28672\n" \
  "v_mfma_f32_16x16x32_bf16 v[188:191], v[68:71], v[140:143], v[188:191]\n" \
  "ds_read_b128 v[68:71], v21 offset:896\n" \
  "ds_read_b64_tr_b16 v[138:139], v7 offset:30464\n" \
  "v_mfma_f32_16x16x32_bf16 v[192:195], v[92:95], v[140:143], v[192:195]\n" \
  "ds_read_b128 v[92:95], v21 offset:12416\n" \
  "s_add_u32 s73, s70, 327680\n" \
  "v_mfma_f32_16x16x32_bf16 v[196:199], v[116:119], v[140:143], v[196:199]\n" \
  "s_waitcnt lgkmcnt(8)\n" \
  "ds_read_b128 v[116:119], v21 offset:13184\n" \
  "buffer_store_dwordx4 v[204:207], v22, s[36:39], s73 offen sc1\n" \
  "v_mfma_f32_16x16x32_bf16 v[184:187], v[48:51], v[144:147], v[184:187]\n" \
  "ds_read_b128 v[48:51], v20 offset:256\n" \
  "ds_read_b64_tr_b16 v[140:141], v27 offset:0\n" \
  "v_mfma_f32_16x16x32_bf16 v[188:191], v[72:75], v[144:147], v[188:191]\n" \
  "ds_read_b128 v[72:75], v20 offset:1024\n" \
  "ds_read_b64_tr_b16 v[142:143], v27 offset:1792\n" \
  "v_mfma_f32_16x16x32_bf16 v[192:195], v[96:99], v[144:147], v[192:195]\n" \
  "ds_read_b128 v[96:99], v20 offset:12544\n" \
  "s_cmp_lg_u32 s78, 0\n" \
  "v_mfma_f32_16x16x32_bf16 v[196:199], v[120:123], v[144:147], v[196:199]\n" \
  "ds_read_b128 v[120:123], v20 offset:13312\n" \
  "v_mfma_f32_16x16x32_bf16 v[184:187], v[52:55], v[148:151], v[184:187]\n" \
  "s_waitcnt lgkmcnt(8)\n" \
  "ds_read_b128 v[52:55], v21 offset:256\n" \
  "ds_read_b64_tr_b16 v[144:145], v27 offset:14336\n" \
  "v_mfma_f32_16x16x32_bf16 v[188:191], v[76:79], v[148:151], v[188:191]\n" \
  "ds_read_b128 v[76:79], v21 offset:1024\n" \
  "ds_read_b64_tr_b16 v[146:147], v27 offset:16128\n" \
  "v_mfma_f32_16x16x32_bf16 v[192:195], v[100:103], v[148:151], v[192:195]\n" \
  "ds_read_b128 v[100:103], v21 offset:12544\n" \
  "v_mfma_f32_16x16x32_bf16 v[196:199], v[124:127], v[148:151], v[196:199]\n" \
  "ds_read_b128 v[124:127], v21 offset:13312\n" \
  "s_cbranch_scc0 LUN2_%=\n" \
  "s_mov_b32 s85, 0\n" \
  "LSDu_%=:\n" \
  "buffer_load_dwordx2 v[244:245], v6, s[36:39], 0 offen sc1\n" \
  "s_waitcnt vmcnt(0)\n" \
  "v_readlane_b32 s82, v244, 3\n" \
  "s_nop 0\n" \
  "s_add_u32 s82, s82, 1\n" \
  "v_cmp_ne_u32 vcc, s82, v244\n" \
  "s_nop 3\n" \
  "s_and_b32 s82, vcc_lo, 7\n" \
  "s_cbranch_scc0 LSFu_%=\n" \
  "s_add_u32 s85, s85, 1\n" \
  "s_cmp_gt_u32 s85, 200000\n" \
  "s_cbranch_scc1 LSFu_%=\n" \
  "s_sleep 4\n" \
  "s_branch LSDu_%=\n" \
  "LSFu_%=:\n" \
  "v_readlane_b32 s82, v245, 0\n" \
  "v_readlane_b32 s83, v245, 1\n" \
  "v_readlane_b32 s84, v245, 2\n" \
  "s_nop 1\n" \
  "v_lshrrev_b32 v244, 21, v212\n" \
  "v_lshrrev_b64 v[246:247], v244, s[82:83]\n" \
  "v_lshrrev_b32_e64 v248, v244, s84\n" \
  "v_cmp_gt_u32 vcc, 64, v244\n" \
  "s_nop 1\n" \
  "v_cndmask_b32 v246, v248, v246, vcc\n" \
  "v_and_b32 v246, 1, v246\n" \
  "v_lshlrev_b32 v246, 13, v246\n" \
  "v_xor_b32 v212, v212, v246\n" \
  "v_lshrrev_b32 v244, 21, v215\n" \
  "v_lshrrev_b64 v[246:247], v244, s[82:83]\n" \
  "v_lshrrev_b32_e64 v248, v244, s84\n" \
  "v_cmp_gt_u32 vcc, 64, v244\n" \
  "s_nop 1\n" \
  "v_cndmask_b32 v246, v248, v246, vcc\n" \
  "v_and_b32 v246, 1, v246\n" \
  "v_lshlrev_b32 v246, 13, v246\n" \
  "v_xor_b32 v215, v215, v246\n" \
  "v_lshrrev_b32 v244, 21, v216\n" \
  "v_lshrrev_b64 v[246:247], v244, s[82:83]\n" \
  "v_lshrrev_b32_e64 v248, v244, s84\n" \
  "v_cmp_gt_u32 vcc, 64, v244\n" \
  "s_nop 1\n" \
  "v_cndmask_b32 v246, v248, v246, vcc\n" \
  "v_and_b32 v246, 1, v246\n" \
  "v_lshlrev_b32 v246, 13, v246\n" \
  "v_xor_b32 v216, v216, v246\n" \
  "v_lshrrev_b32 v244, 21, v217\n" \
  "v_lshrrev_b64 v[246:247], v244, s[82:83]\n" \
  "v_lshrrev_b32_e64 v248, v244, s84\n" \
  "v_cmp_gt_u32 vcc, 64, v244\n" \
  "s_nop 1\n" \
  "v_cndmask_b32 v246, v248, v246, vcc\n" \
  "v_and_b32 v246, 1, v246\n" \
  "v_lshlrev_b32 v246, 13, v246\n" \
  "v_xor_b32 v217, v217, v246\n" \
  "v_lshrrev_b32 v244, 21, v219\n" \
  "v_lshrrev_b64 v[246:247], v244, s[82:83]\n" \
  "v_lshrrev_b32_e64 v248, v244, s84\n" \
  "v_cmp_gt_u32 vcc, 64, v244\n" \
  "s_nop 1\n" \
  "v_cndmask_b32 v246, v248, v246, vcc\n" \
  "v_and_b32 v246, 1, v246\n" \
  "v_lshlrev_b32 v246, 13, v246\n" \
  "v_xor_b32 v219, v219, v246\n" \
  "s_mov_b32 exec_lo, 0xfffff3fc\n" \
  "v_lshrrev_b32 v244, 21, v23\n" \
  "v_lshrrev_b64 v[246:247], v244, s[82:83]\n" \
  "v_lshrrev_b32_e64 v248, v244, s84\n" \
  "v_cmp_gt_u32 vcc, 64, v244\n" \
  "s_nop 1\n" \
  "v_cndmask_b32 v246, v248, v246, vcc\n" \
  "v_and_b32 v246, 1, v246\n" \
  "v_lshlrev_b32 v246, 13, v246\n" \
  "v_xor_b32 v23, v23, v246\n" \
  "v_lshrrev_b32 v244, 21, v24\n" \
  "v_lshrrev_b64 v[246:247], v244, s[82:83]\n" \
  "v_lshrrev_b32_e64 v248, v244, s84\n" \
  "v_cmp_gt_u32 vcc, 64, v244\n" \
  "s_nop 1\n" \
  "v_cndmask_b32 v246, v248, v246, vcc\n" \
  "v_and_b32 v246, 1, v246\n" \
  "v_lshlrev_b32 v246, 13, v246\n" \
  "v_xor_b32 v24, v24, v246\n" \
  "v_lshrrev_b32 v244, 21, v224\n" \
  "v_lshrrev_b64 v[246:247], v244, s[82:83]\n" \
  "v_lshrrev_b32_e64 v248, v244, s84\n" \
  "v_cmp_gt_u32 vcc, 64, v244\n" \
  "s_nop 1\n" \
  "v_cndmask_b32 v246, v248, v246, vcc\n" \
  "v_and_b32 v246, 1, v246\n" \
  "v_lshlrev_b32 v246, 13, v246\n" \
  "v_xor_b32 v224, v224, v246\n" \
  "s_mov_b32 exec_lo, -1\n" \
  "s_branch LUR_%=\n" \
  "LUN2_%=:\n" \
  "v_mfma_f32_16x16x32_bf16 v[152:155], v[32:35], v[128:131], 0\n" \
  "ds_read_b64_tr_b16 v[148:149], v27 offset:28672\n" \
  "v_mfma_f32_16x16x32_bf16 v[156:159], v[56:59], v[128:131], 0\n" \
  "s_waitcnt lgkmcnt(8)\n" \
  "ds_read_b64_tr_b16 v[150:151], v27 offset:30464\n" \
  "v_mfma_f32_16x16x32_bf16 v[160:163], v[80:83], v[128:131], 0\n" \
  "v_cvt_pk_bf16_f32 v200, v184, v185\n" \
  "v_cvt_pk_bf16_f32 v201, v186, v187\n" \
  "v_mfma_f32_16x16x32_bf16 v[164:167], v[104:107], v[128:131], 0\n" \
  "v_cvt_pk_bf16_f32 v202, v188, v189\n" \
  "v_cvt_pk_bf16_f32 v203, v190, v191\n" \
  "v_mfma_f32_16x16x32_bf16 v[152:155], v[36:39], v[132:135], v[152:155]\n" \
  "ds_read_b64_tr_b16 v[128:129], v26 offset:0\n" \
  "v_mfma_f32_16x16x32_bf16 v[156:159], v[60:63], v[132:135], v[156:159]\n" \
  "ds_read_b64_tr_b16 v[130:131], v26 offset:1792\n" \
  "v_mfma_f32_16x16x32_bf16 v[160:163], v[84:87], v[132:135], v[160:163]\n" \
  "s_add_u32 s73, s71, 393216\n" \
  "v_mfma_f32_16x16x32_bf16 v[164:167], v[108:111], v[132:135], v[164:167]\n" \
  "buffer_store_dwordx4 v[200:203], v22, s[36:39], s73 offen sc1\n" \
  "v_mfma_f32_16x16x32_bf16 v[152:155], v[40:43], v[136:139], v[152:155]\n" \
  "ds_read_b64_tr_b16 v[132:133], v26 offset:14336\n" \
  "v_mfma_f32_16x16x32_bf16 v[156:159], v[64:67], v[136:139], v[156:159]\n" \
  "ds_read_b64_tr_b16 v[134:135], v26 offset:16128\n" \
  "v_mfma_f32_16x16x32_bf16 v[160:163], v[88:91], v[136:139], v[160:163]\n" \
  "v_cvt_pk_bf16_f32 v204, v192, v193\n" \
  "v_cvt_pk_bf16_f32 v205, v194, v195\n" \
  "v_mfma_f32_16x16x32_bf16 v[164:167], v[112:115], v[136:139], v[164:167]\n" \
  "v_cvt_pk_bf16_f32 v206, v196, v197\n" \
  "v_cvt_pk_bf16_f32 v207, v198, v199\n" \
  "v_mfma_f32_16x16x32_bf16 v[152:155], v[44:47], v[140:143], v[152:155]\n" \
  "ds_read_b64_tr_b16 v[136:137], v26 offset:28672\n" \
  "v_mfma_f32_16x16x32_bf16 v[156:159], v[68:71], v[140:143], v[156:159]\n" \
  "ds_read_b64_tr_b16 v[138:139], v26 offset:30464\n" \
  "v_mfma_f32_16x16x32_bf16 v[160:163], v[92:95], v[140:143], v[160:163]\n" \
  "s_add_u32 s73, s72, 393216\n" \
  "v_mfma_f32_16x16x32_bf16 v[164:167], v[116:119], v[140:143], v[164:167]\n" \
  "buffer_store_dwordx4 v[204:207], v22, s[36:39], s73 offen sc1\n" \
  "s_waitcnt lgkmcnt(10)\n" \
  "v_mfma_f32_16x16x32_bf16 v[152:155], v[48:51], v[144:147], v[152:155]\n" \
  "ds_read_b64_tr_b16 v[140:141], v28 offset:0\n" \
  "v_mfma_f32_16x16x32_bf16 v[156:159], v[72:75], v[144:147], v[156:159]\n" \
  "ds_read_b64_tr_b16 v[142:143], v28 offset:1792\n" \
  "v_mfma_f32_16x16x32_bf16 v[160:163], v[96:99], v[144:147], v[160:163]\n" \
  "v_readlane_b32 s69, v23, s68\n" \
  "v_readlane_b32 s71, v24, s68\n" \
  "s_sub_u32 s69, s69, s62\n" \
  "s_sub_u32 s71, s71, s63\n" \
  "s_add_u32 s68, s68, 1\n" \
  "v_mfma_f32_16x16x32_bf16 v[164:167], v[120:123], v[144:147], v[164:167]\n" \
  "v_readlane_b32 s70, v23, s68\n" \
  "v_readlane_b32 s72, v24, s68\n" \
  "s_sub_u32 s70, s70, s62\n" \
  "s_sub_u32 s72, s72, s63\n" \
  "s_add_u32 s68, s68, 1\n" \
  "s_waitcnt lgkmcnt(8)\n" \
  "v_mfma_f32_16x16x32_bf16 v[152:155], v[52:55], v[148:151], v[152:155]\n" \
  "ds_read_b64_tr_b16 v[144:145], v28 offset:14336\n" \
  "v_mfma_f32_16x16x32_bf16 v[156:159], v[76:79], v[148:151], v[156:159]\n" \
  "ds_read_b64_tr_b16 v[146:147], v28 offset:16128\n" \
  "v_mfma_f32_16x16x32_bf16 v[160:163], v[100:103], v[148:151], v[160:163]\n" \
  "s_cmp_eq_u32 s68, s77\n" \
  "s_cselect_b32 s75, 0x40000000, s75\n" \
  "v_mfma_f32_16x16x32_bf16 v[164:167], v[124:127], v[148:151], v[164:167]\n" \
  "s_barrier\n" \
  "s_waitcnt lgkmcnt(8)\n" \
  "v_mfma_f32_16x16x32_bf16 v[168:171], v[32:35], v[128:131], 0\n" \
  "ds_read_b64_tr_b16 v[148:149], v28 offset:28672\n" \
  "v_mfma_f32_16x16x32_bf16 v[172:175], v[56:59], v[128:131], 0\n" \
  "ds_read_b64_tr_b16 v[150:151], v28 offset:30464\n" \
  "v_mfma_f32_16x16x32_bf16 v[176:179], v[80:83], v[128:131], 0\n" \
  "v_cvt_pk_bf16_f32 v200, v152, v153\n" \
  "v_cvt_pk_bf16_f32 v201, v154, v155\n" \
  "v_mfma_f32_16x16x32_bf16 v[180:183], v[104:107], v[128:131], 0\n" \
  "s_add_u32 m0, s76, 0\n" \
  "s_nop 0\n" \
  "buffer_load_dwordx4 v8, s[40:43], s75 offen offset:0 lds\n" \
  "s_waitcnt lgkmcnt(8)\n" \
  "v_mfma_f32_16x16x32_bf16 v[168:171], v[36:39], v[132:135], v[168:171]\n" \
  "ds_read_b64_tr_b16 v[128:129], v7 offset:64\n" \
  "v_mfma_f32_16x16x32_bf16 v[172:175], v[60:63], v[132:135], v[172:175]\n" \
  "ds_read_b64_tr_b16 v[130:131], v7 offset:1856\n" \
  "v_mfma_f32_16x16x32_bf16 v[176:179], v[84:87], v[132:135], v[176:179]\n" \
  "v_cvt_pk_bf16_f32 v202, v156, v157\n" \
  "v_cvt_pk_bf16_f32 v203, v158, v159\n" \
  "v_mfma_f32_16x16x32_bf16 v[180:183], v[108:111], v[132:135], v[180:183]\n" \
  "s_add_u32 s73, s69, 0\n" \
  "buffer_load_dword v242, v30, s[36:39], s78 offen sc1\n" \
  "s_waitcnt lgkmcnt(8)\n" \
  "v_mfma_f32_16x16x32_bf16 v[168:171], v[40:43], v[136:139], v[168:171]\n" \
  "ds_read_b64_tr_b16 v[132:133], v7 offset:14400\n" \
  "v_mfma_f32_16x16x32_bf16 v[172:175], v[64:67], v[136:139], v[172:175]\n" \
  "ds_read_b64_tr_b16 v[134:135], v7 offset:16192\n" \
  "v_mfma_f32_16x16x32_bf16 v[176:179], v[88:91], v[136:139], v[176:179]\n" \
  "buffer_load_dwordx4 v9, s[40:43], s75 offen offset:1024 lds\n" \
  "v_mfma_f32_16x16x32_bf16 v[180:183], v[112:115], v[136:139], v[180:183]\n" \
  "buffer_store_dwordx4 v[200:203], v22, s[36:39], s73 offen sc1\n" \
  "s_waitcnt lgkmcnt(8)\n" \
  "v_mfma_f32_16x16x32_bf16 v[168:171], v[44:47], v[140:143], v[168:171]\n" \
  "ds_read_b64_tr_b16 v[136:137], v7 offset:28736\n" \
  "v_mfma_f32_16x16x32_bf16 v[172:175], v[68:71], v[140:143], v[172:175]\n" \
  "ds_read_b64_tr_b16 v[138:139], v7 offset:30528\n" \
  "v_mfma_f32_16x16x32_bf16 v[176:179], v[92:95], v[140:143], v[176:179]\n" \
  "v_cvt_pk_bf16_f32 v204, v160, v161\n" \
  "v_cvt_pk_bf16_f32 v205, v162, v163\n" \
  "v_mfma_f32_16x16x32_bf16 v[180:183], v[116:119], v[140:143], v[180:183]\n" \
  "buffer_load_dwordx4 v10, s[40:43], s75 offen offset:2048 lds\n" \
  "s_waitcnt lgkmcnt(8)\n" \
  "v_mfma_f32_16x16x32_bf16 v[168:171], v[48:51], v[144:147], v[168:171]\n" \
  "ds_read_b64_tr_b16 v[140:141], v27 offset:64\n" \
  "v_mfma_f32_16x16x32_bf16 v[172:175], v[72:75], v[144:147], v[172:175]\n" \
  "ds_read_b64_tr_b16 v[142:143], v27 offset:1856\n" \
  "v_mfma_f32_16x16x32_bf16 v[176:179], v[96:99], v[144:147], v[176:179]\n" \
  "v_cvt_pk_bf16_f32 v206, v164, v165\n" \
  "v_cvt_pk_bf16_f32 v207, v166, v167\n" \
  "v_mfma_f32_16x16x32_bf16 v[180:183], v[120:123], v[144:147], v[180:183]\n" \
  "s_add_u32 s73, s70, 0\n" \
  "s_waitcnt lgkmcnt(8)\n" \
  "v_mfma_f32_16x16x32_bf16 v[168:171], v[52:55], v[148:151], v[168:171]\n" \
  "ds_read_b64_tr_b16 v[144:145], v27 offset:14400\n" \
  "v_mfma_f32_16x16x32_bf16 v[172:175], v[76:79], v[148:151], v[172:175]\n" \
  "ds_read_b64_tr_b16 v[146:147], v27 offset:16192\n" \
  "v_mfma_f32_16x16x32_bf16 v[176:179], v[100:103], v[148:151], v[176:179]\n" \
  "buffer_load_dwordx4 v11, s[40:43], s75 offen offset:3072 lds\n" \
  "v_mfma_f32_16x16x32_bf16 v[180:183], v[124:127], v[148:151], v[180:183]\n" \
  "buffer_store_dwordx4 v[204:207], v22, s[36:39], s73 offen sc1\n" \
  "s_waitcnt lgkmcnt(8)\n" \
  "v_mfma_f32_16x16x32_bf16 v[152:155], v[32:35], v[128:131], 0\n" \
  "ds_read_b64_tr_b16 v[148:149], v27 offset:28736\n" \
  "v_mfma_f32_16x16x32_bf16 v[156:159], v[56:59], v[128:131], 0\n" \
  "ds_read_b64_tr_b16 v[150:151], v27 offset:30528\n" \
  "v_mfma_f32_16x16x32_bf16 v[160:163], v[80:83], v[128:131], 0\n" \
  "v_cvt_pk_bf16_f32 v200, v168, v169\n" \
  "v_cvt_pk_bf16_f32 v201, v170, v171\n" \
  "v_mfma_f32_16x16x32_bf16 v[164:167], v[104:107], v[128:131], 0\n" \
  "s_add_u32 m0, s76, 4096\n" \
  "s_nop 0\n" \
  "buffer_load_dwordx4 v12, s[40:43], s75 offen offset:0 lds\n" \
  "s_waitcnt lgkmcnt(8)\n" \
  "v_mfma_f32_16x16x32_bf16 v[152:155], v[36:39], v[132:135], v[152:155]\n" \
  "ds_read_b64_tr_b16 v[128:129], v26 offset:64\n" \
  "v_mfma_f32_16x16x32_bf16 v[156:159], v[60:63], v[132:135], v[156:159]\n" \
  "ds_read_b64_tr_b16 v[130:131], v26 offset:1856\n" \
  "v_mfma_f32_16x16x32_bf16 v[160:163], v[84:87], v[132:135], v[160:163]\n" \
  "v_cvt_pk_bf16_f32 v202, v172, v173\n" \
  "v_cvt_pk_bf16_f32 v203, v174, v175\n" \
  "v_mfma_f32_16x16x32_bf16 v[164:167], v[108:111], v[132:135], v[164:167]\n" \
  "s_add_u32 s73, s69, 65536\n" \
  "s_waitcnt lgkmcnt(8)\n" \
  "v_mfma_f32_16x16x32_bf16 v[152:155], v[40:43], v[136:139], v[152:155]\n" \
  "ds_read_b64_tr_b16 v[132:133], v26 offset:14400\n" \
  "v_mfma_f32_16x16x32_bf16 v[156:159], v[64:67], v[136:139], v[156:159]\n" \
  "ds_read_b64_tr_b16 v[134:135], v26 offset:16192\n" \
  "v_mfma_f32_16x16x32_bf16 v[160:163], v[88:91], v[136:139], v[160:163]\n" \
  "buffer_load_dwordx4 v13, s[40:43], s75 offen offset:1024 lds\n" \
  "v_mfma_f32_16x16x32_bf16 v[164:167], v[112:115], v[136:139], v[164:167]\n" \
  "buffer_store_dwordx4 v[200:203], v22, s[36:39], s73 offen sc1\n" \
  "s_waitcnt lgkmcnt(8)\n" \
  "v_mfma_f32_16x16x32_bf16 v[152:155], v[44:47], v[140:143], v[152:155]\n" \
  "ds_read_b64_tr_b16 v[136:137], v26 offset:28736\n" \
  "v_mfma_f32_16x16x32_bf16 v[156:159], v[68:71], v[140:143], v[156:159]\n" \
  "ds_read_b64_tr_b16 v[138:139], v26 offset:30528\n" \
  "v_mfma_f32_16x16x32_bf16 v[160:163], v[92:95], v[140:143], v[160:163]\n" \
  "v_cvt_pk_bf16_f32 v204, v176, v177\n" \
  "v_cvt_pk_bf16_f32 v205, v178, v179\n" \
  "v_mfma_f32_16x16x32_bf16 v[164:167], v[116:119], v[140:143], v[164:167]\n" \
  "buffer_load_dwordx4 v14, s[40:43], s75 offen offset:2048 lds\n" \
  "s_waitcnt lgkmcnt(8)\n" \
  "v_mfma_f32_16x16x32_bf16 v[152:155], v[48:51], v[144:147], v[152:155]\n" \
  "ds_read_b64_tr_b16 v[140:141], v28 offset:64\n" \
  "v_mfma_f32_16x16x32_bf16 v[156:159], v[72:75], v[144:147], v[156:159]\n" \
  "ds_read_b64_tr_b16 v[142:143], v28 offset:1856\n" \
  "v_mfma_f32_16x16x32_bf16 v[160:163], v[96:99], v[144:147], v[160:163]\n" \
  "v_cvt_pk_bf16_f32 v206, v180, v181\n" \
  "v_cvt_pk_bf16_f32 v207, v182, v183\n" \
  "v_mfma_f32_16x16x32_bf16 v[164:167], v[120:123], v[144:147], v[164:167]\n" \
  "s_add_u32 s73, s70, 65536\n" \
  "s_waitcnt vmcnt(9)\n" \
  "v_cmp_ne_u32 vcc, 1, v242\n" \
  "v_readlane_b32 s79, v224, 0\n" \
  "s_waitcnt lgkmcnt(8)\n" \
  "v_mfma_f32_16x16x32_bf16 v[152:155], v[52:55], v[148:151], v[152:155]\n" \
  "ds_read_b64_tr_b16 v[144:145], v28 offset:14400\n" \
  "v_mfma_f32_16x16x32_bf16 v[156:159], v[76:79], v[148:151], v[156:159]\n" \
  "ds_read_b64_tr_b16 v[146:147], v28 offset:16192\n" \
  "v_mfma_f32_16x16x32_bf16 v[160:163], v[100:103], v[148:151], v[160:163]\n" \
  "buffer_load_dwordx4 v15, s[40:43], s75 offen offset:3072 lds\n" \
  "v_mfma_f32_16x16x32_bf16 v[164:167], v[124:127], v[148:151], v[164:167]\n" \
  "buffer_store_dwordx4 v[204:207], v22, s[36:39], s73 offen sc1\n" \
  "s_cbranch_vccnz LUP2_%=\n" \
  "LUK2_%=:\n" \
  "s_waitcnt lgkmcnt(8)\n" \
  "v_mfma_f32_16x16x32_bf16 v[168:171], v[32:35], v[128:131], 0\n" \
  "ds_read_b64_tr_b16 v[148:149], v28 offset:28736\n" \
  "v_mfma_f32_16x16x32_bf16 v[172:175], v[56:59], v[128:131], 0\n" \
  "ds_read_b64_tr_b16 v[150:151], v28 offset:30528\n" \
  "buffer_load_dwordx4 a[0:3], v1, s[36:39], s79 offen sc1\n" \
  "v_readlane_b32 s80, v224, 1\n" \
  "v_mfma_f32_16x16x32_bf16 v[176:179], v[80:83], v[128:131], 0\n" \
  "v_cvt_pk_bf16_f32 v200, v152, v153\n" \
  "v_cvt_pk_bf16_f32 v201, v154, v155\n" \
  "s_waitcnt vmcnt(14)\n" \
  "s_barrier\n" \
  "s_mov_b32 s78, s88\n" \
  "buffer_atomic_add v0, v25, s[36:39], s78 offen sc0 sc1\n" \
  "s_add_u32 s88, s88, 1536\n" \
  "s_and_b32 s88, s88, 0x3fffffff\n" \
  "v_mfma_f32_16x16x32_bf16 v[180:183], v[104:107], v[128:131], 0\n" \
  "s_add_u32 m0, s76, 8192\n" \
  "s_nop 0\n" \
  "buffer_load_dwordx4 v16, s[40:43], s75 offen offset:0 lds\n" \
  "s_waitcnt lgkmcnt(8)\n" \
  "v_mfma_f32_16x16x32_bf16 v[168:171], v[36:39], v[132:135], v[168:171]\n" \
  "ds_read_b64_tr_b16 v[128:129], v7 offset:128\n" \
  "v_mfma_f32_16x16x32_bf16 v[172:175], v[60:63], v[132:135], v[172:175]\n" \
  "ds_read_b64_tr_b16 v[130:131], v7 offset:1920\n" \
  "buffer_load_dwordx4 a[8:11], v2, s[36:39], s79 offen sc1\n" \
  "v_mfma_f32_16x16x32_bf16 v[176:179], v[84:87], v[132:135], v[176:179]\n" \
  "v_cvt_pk_bf16_f32 v202, v156, v157\n" \
  "v_cvt_pk_bf16_f32 v203, v158, v159\n" \
  "v_mfma_f32_16x16x32_bf16 v[180:183], v[108:111], v[132:135], v[180:183]\n" \
  "s_add_u32 s73, s69, 131072\n" \
  "s_waitcnt lgkmcnt(8)\n" \
  "v_mfma_f32_16x16x32_bf16 v[168:171], v[40:43], v[136:139], v[168:171]\n" \
  "ds_read_b64_tr_b16 v[132:133], v7 offset:14464\n" \
  "v_mfma_f32_16x16x32_bf16 v[172:175], v[64:67], v[136:139], v[172:175]\n" \
  "ds_read_b64_tr_b16 v[134:135], v7 offset:16256\n" \
  "buffer_load_dwordx4 a[16:19], v3, s[36:39], s79 offen sc1\n" \
  "v_mfma_f32_16x16x32_bf16 v[176:179], v[88:91], v[136:139], v[176:179]\n" \
  "buffer_load_dwordx4 v17, s[40:43], s75 offen offset:1024 lds\n" \
  "v_mfma_f32_16x16x32_bf16 v[180:183], v[112:115], v[136:139], v[180:183]\n" \
  "buffer_store_dwordx4 v[200:203], v22, s[36:39], s73 offen sc1\n" \
  "s_waitcnt lgkmcnt(8)\n" \
  "v_mfma_f32_16x16x32_bf16 v[168:171], v[44:47], v[140:143], v[168:171]\n" \
  "ds_read_b64_tr_b16 v[136:137], v7 offset:28800\n" \
  "v_mfma_f32_16x16x32_bf16 v[172:175], v[68:71], v[140:143], v[172:175]\n" \
  "ds_read_b64_tr_b16 v[138:139], v7 offset:30592\n" \
  "buffer_load_dwordx4 a[24:27], v4, s[36:39], s79 offen sc1\n" \
  "v_mfma_f32_16x16x32_bf16 v[176:179], v[92:95], v[140:143], v[176:179]\n" \
  "v_cvt_pk_bf16_f32 v204, v160, v161\n" \
  "v_cvt_pk_bf16_f32 v205, v162, v163\n" \
  "v_mfma_f32_16x16x32_bf16 v[180:183], v[116:119], v[140:143], v[180:183]\n" \
  "buffer_load_dwordx4 v18, s[40:43], s75 offen offset:2048 lds\n" \
  "s_waitcnt lgkmcnt(8)\n" \
  "v_mfma_f32_16x16x32_bf16 v[168:171], v[48:51], v[144:147], v[168:171]\n" \
  "ds_read_b64_tr_b16 v[140:141], v27 offset:128\n" \
  "v_mfma_f32_16x16x32_bf16 v[172:175], v[72:75], v[144:147], v[172:175]\n" \
  "ds_read_b64_tr_b16 v[142:143], v27 offset:1920\n" \
  "buffer_load_dwordx4 a[32:35], v5, s[36:39], s79 offen sc1\n" \
  "v_mfma_f32_16x16x32_bf16 v[176:179], v[96:99], v[144:147], v[176:179]\n" \
  "v_cvt_pk_bf16_f32 v206, v164, v165\n" \
  "v_cvt_pk_bf16_f32 v207, v166, v167\n" \
  "v_mfma_f32_16x16x32_bf16 v[180:183], v[120:123], v[144:147], v[180:183]\n" \
  "s_add_u32 s73, s70, 131072\n" \
  "s_waitcnt lgkmcnt(8)\n" \
  "v_mfma_f32_16x16x32_bf16 v[168:171], v[52:55], v[148:151], v[168:171]\n" \
  "ds_read_b64_tr_b16 v[144:145], v27 offset:14464\n" \
  "v_mfma_f32_16x16x32_bf16 v[172:175], v[76:79], v[148:151], v[172:175]\n" \
  "ds_read_b64_tr_b16 v[146:147], v27 offset:16256\n" \
  "buffer_load_dwordx4 a[4:7], v1, s[36:39], s80 offen sc1\n" \
  "v_readlane_b32 s79, v224, 10\n" \
  "v_mfma_f32_16x16x32_bf16 v[176:179], v[100:103], v[148:151], v[176:179]\n" \
  "buffer_load_dwordx4 v19, s[40:43], s75 offen offset:3072 lds\n" \
  "s_add_u32 s75, s75, 786432\n" \
  "v_mfma_f32_16x16x32_bf16 v[180:183], v[124:127], v[148:151], v[180:183]\n" \
  "buffer_store_dwordx4 v[204:207], v22, s[36:39], s73 offen sc1\n" \
  "s_waitcnt lgkmcnt(8)\n" \
  "v_mfma_f32_16x16x32_bf16 v[152:155], v[32:35], v[128:131], 0\n" \
  "ds_read_b64_tr_b16 v[148:149], v27 offset:28800\n" \
  "v_mfma_f32_16x16x32_bf16 v[156:159], v[56:59], v[128:131], 0\n" \
  "ds_read_b64_tr_b16 v[150:151], v27 offset:30592\n" \
  "buffer_load_dwordx4 a[12:15], v2, s[36:39], s80 offen sc1\n" \
  "v_mfma_f32_16x16x32_bf16 v[160:163], v[80:83], v[128:131], 0\n" \
  "v_cvt_pk_bf16_f32 v200, v168, v169\n" \
  "v_cvt_pk_bf16_f32 v201, v170, v171\n" \
  "v_mfma_f32_16x16x32_bf16 v[164:167], v[104:107], v[128:131], 0\n" \
  "v_cvt_pk_bf16_f32 v202, v172, v173\n" \
  "v_cvt_pk_bf16_f32 v203, v174, v175\n" \
  "s_waitcnt lgkmcnt(8)\n" \
  "v_mfma_f32_16x16x32_bf16 v[152:155], v[36:39], v[132:135], v[152:155]\n" \
  "ds_read_b64_tr_b16 v[128:129], v26 offset:128\n" \
  "v_mfma_f32_16x16x32_bf16 v[156:159], v[60:63], v[132:135], v[156:159]\n" \
  "ds_read_b64_tr_b16 v[130:131], v26 offset:1920\n" \
  "buffer_load_dwordx4 a[20:23], v3, s[36:39], s80 offen sc1\n" \
  "v_mfma_f32_16x16x32_bf16 v[160:163], v[84:87], v[132:135], v[160:163]\n" \
  "s_add_u32 s73, s69, 196608\n" \
  "v_mfma_f32_16x16x32_bf16 v[164:167], v[108:111], v[132:135], v[164:167]\n" \
  "buffer_store_dwordx4 v[200:203], v22, s[36:39], s73 offen sc1\n" \
  "s_waitcnt lgkmcnt(8)\n" \
  "v_mfma_f32_16x16x32_bf16 v[152:155], v[40:43], v[136:139], v[152:155]\n" \
  "ds_read_b64_tr_b16 v[132:133], v26 offset:14464\n" \
  "v_mfma_f32_16x16x32_bf16 v[156:159], v[64:67], v[136:139], v[156:159]\n" \
  "ds_read_b64_tr_b16 v[134:135], v26 offset:16256\n" \
  "buffer_load_dwordx4 a[28:31], v4, s[36:39], s80 offen sc1\n" \
  "v_mfma_f32_16x16x32_bf16 v[160:163], v[88:91], v[136:139], v[160:163]\n" \
  "v_cvt_pk_bf16_f32 v204, v176, v177\n" \
  "v_cvt_pk_bf16_f32 v205, v178, v179\n" \
  "v_mfma_f32_16x16x32_bf16 v[164:167], v[112:115], v[136:139], v[164:167]\n" \
  "v_cvt_pk_bf16_f32 v206, v180, v181\n" \
  "v_cvt_pk_bf16_f32 v207, v182, v183\n" \
  "s_waitcnt lgkmcnt(8)\n" \
  "v_mfma_f32_16x16x32_bf16 v[152:155], v[44:47], v[140:143], v[152:155]\n" \
  "ds_read_b64_tr_b16 v[136:137], v26 offset:28800\n" \
  "v_mfma_f32_16x16x32_bf16 v[156:159], v[68:71], v[140:143], v[156:159]\n" \
  "ds_read_b64_tr_b16 v[138:139], v26 offset:30592\n" \
  "buffer_load_dwordx4 a[36:39], v5, s[36:39], s80 offen sc1\n" \
  "v_mfma_f32_16x16x32_bf16 v[160:163], v[92:95], v[140:143], v[160:163]\n" \
  "s_add_u32 s73, s70, 196608\n" \
  "v_mfma_f32_16x16x32_bf16 v[164:167], v[116:119], v[140:143], v[164:167]\n" \
  "buffer_store_dwordx4 v[204:207], v22, s[36:39], s73 offen sc1\n" \
  "s_waitcnt lgkmcnt(8)\n" \
  "v_mfma_f32_16x16x32_bf16 v[152:155], v[48:51], v[144:147], v[152:155]\n" \
  "ds_read_b64_tr_b16 v[140:141], v28 offset:128\n" \
  "v_mfma_f32_16x16x32_bf16 v[156:159], v[72:75], v[144:147], v[156:159]\n" \
  "ds_read_b64_tr_b16 v[142:143], v28 offset:1920\n" \
  "buffer_load_dwordx4 a[40:43], v1, s[36:39], s79 offen sc1\n" \
  "v_readlane_b32 s80, v224, 11\n" \
  "v_mfma_f32_16x16x32_bf16 v[160:163], v[96:99], v[144:147], v[160:163]\n" \
  "s_getpc_b64 s[82:83]\n" \
  "LCF7_%=:\n" \
  "s_add_u32 s82, s82, LUN3_%=-LCF7_%=\n" \
  "s_addc_u32 s83, s83, 0\n" \
  "v_mbcnt_lo_u32_b32 v240, -1, 0\n" \
  "v_mbcnt_hi_u32_b32 v240, -1, v240\n" \
  "v_lshlrev_b32 v240, 6, v240\n" \
  "global_load_dword v241, v240, s[82:83]\n" \
  "v_mfma_f32_16x16x32_bf16 v[164:167], v[120:123], v[144:147], v[164:167]\n" \
  "s_waitcnt lgkmcnt(8)\n" \
  "v_mfma_f32_16x16x32_bf16 v[152:155], v[52:55], v[148:151], v[152:155]\n" \
  "ds_read_b64_tr_b16 v[144:145], v28 offset:14464\n" \
  "v_mfma_f32_16x16x32_bf16 v[156:159], v[76:79], v[148:151], v[156:159]\n" \
  "ds_read_b64_tr_b16 v[146:147], v28 offset:16256\n" \
  "buffer_load_dwordx4 a[48:51], v2, s[36:39], s79 offen sc1\n" \
  "v_mfma_f32_16x16x32_bf16 v[160:163], v[100:103], v[148:151], v[160:163]\n" \
  "v_mfma_f32_16x16x32_bf16 v[164:167], v[124:127], v[148:151], v[164:167]\n" \
  "s_waitcnt lgkmcnt(8)\n" \
  "v_mfma_f32_16x16x32_bf16 v[168:171], v[32:35], v[128:131], 0\n" \
  "ds_read_b64_tr_b16 v[148:149], v28 offset:28800\n" \
  "v_mfma_f32_16x16x32_bf16 v[172:175], v[56:59], v[128:131], 0\n" \
  "ds_read_b64_tr_b16 v[150:151], v28 offset:30592\n" \
  "buffer_load_dwordx4 a[56:59], v3, s[36:39], s79 offen sc1\n" \
  "v_mfma_f32_16x16x32_bf16 v[176:179], v[80:83], v[128:131], 0\n" \
  "v_cvt_pk_bf16_f32 v200, v152, v153\n" \
  "v_cvt_pk_bf16_f32 v201, v154, v155\n" \
  "v_mfma_f32_16x16x32_bf16 v[180:183], v[104:107], v[128:131], 0\n" \
  "v_cvt_pk_bf16_f32 v202, v156, v157\n" \
  "v_cvt_pk_bf16_f32 v203, v158, v159\n" \
  "s_waitcnt lgkmcnt(8)\n" \
  "v_mfma_f32_16x16x32_bf16 v[168:171], v[36:39], v[132:135], v[168:171]\n" \
  "ds_read_b64_tr_b16 v[128:129], v7 offset:192\n" \
  "v_mfma_f32_16x16x32_bf16 v[172:175], v[60:63], v[132:135], v[172:175]\n" \
  "ds_read_b64_tr_b16 v[130:131], v7 offset:1984\n" \
  "buffer_load_dwordx4 a[64:67], v4, s[36:39], s79 offen sc1\n" \
  "v_mfma_f32_16x16x32_bf16 v[176:179], v[84:87], v[132:135], v[176:179]\n" \
  "s_add_u32 s73, s69, 262144\n" \
  "v_mfma_f32_16x16x32_bf16 v[180:183], v[108:111], v[132:135], v[180:183]\n" \
  "buffer_store_dwordx4 v[200:203], v22, s[36:39], s73 offen sc1\n" \
  "s_waitcnt lgkmcnt(8)\n" \
  "v_mfma_f32_16x16x32_bf16 v[168:171], v[40:43], v[136:139], v[168:171]\n" \
  "ds_read_b64_tr_b16 v[132:133], v7 offset:14528\n" \
  "v_mfma_f32_16x16x32_bf16 v[172:175], v[64:67], v[136:139], v[172:175]\n" \
  "ds_read_b64_tr_b16 v[134:135], v7 offset:16320\n" \
  "buffer_load_dwordx4 a[72:75], v5, s[36:39], s79 offen sc1\n" \
  "v_mfma_f32_16x16x32_bf16 v[176:179], v[88:91], v[136:139], v[176:179]\n" \
  "v_cvt_pk_bf16_f32 v204, v160, v161\n" \
  "v_cvt_pk_bf16_f32 v205, v162, v163\n" \
  "v_mfma_f32_16x16x32_bf16 v[180:183], v[112:115], v[136:139], v[180:183]\n" \
  "v_cvt_pk_bf16_f32 v206, v164, v165\n" \
  "v_cvt_pk_bf16_f32 v207, v166, v167\n" \
  "s_waitcnt lgkmcnt(8)\n" \
  "v_mfma_f32_16x16x32_bf16 v[168:171], v[44:47], v[140:143], v[168:171]\n" \
  "ds_read_b64_tr_b16 v[136:137], v7 offset:28864\n" \
  "v_mfma_f32_16x16x32_bf16 v[172:175], v[68:71], v[140:143], v[172:175]\n" \
  "ds_read_b64_tr_b16 v[138:139], v7 offset:30656\n" \
  "buffer_load_dwordx4 a[44:47], v1, s[36:39], s80 offen sc1\n" \
  "v_mfma_f32_16x16x32_bf16 v[176:179], v[92:95], v[140:143], v[176:179]\n" \
  "s_add_u32 s73, s70, 262144\n" \
  "v_mfma_f32_16x16x32_bf16 v[180:183], v[116:119], v[140:143], v[180:183]\n" \
  "buffer_store_dwordx4 v[204:207], v22, s[36:39], s73 offen sc1\n" \
  "s_waitcnt lgkmcnt(8)\n" \
  "v_mfma_f32_16x16x32_bf16 v[168:171], v[48:51], v[144:147], v[168:171]\n" \
  "ds_read_b64_tr_b16 v[140:141], v27 offset:192\n" \
  "v_mfma_f32_16x16x32_bf16 v[172:175], v[72:75], v[144:147], v[172:175]\n" \
  "ds_read_b64_tr_b16 v[142:143], v27 offset:1984\n" \
  "buffer_load_dwordx4 a[52:55], v2, s[36:39], s80 offen sc1\n" \
  "v_mfma_f32_16x16x32_bf16 v[176:179], v[96:99], v[144:147], v[176:179]\n" \
  "v_mfma_f32_16x16x32_bf16 v[180:183], v[120:123], v[144:147], v[180:183]\n" \
  "s_waitcnt lgkmcnt(8)\n" \
  "v_mfma_f32_16x16x32_bf16 v[168:171], v[52:55], v[148:151], v[168:171]\n" \
  "ds_read_b64_tr_b16 v[144:145], v27 offset:14528\n" \
  "v_mfma_f32_16x16x32_bf16 v[172:175], v[76:79], v[148:151], v[172:175]\n" \
  "ds_read_b64_tr_b16 v[146:147], v27 offset:16320\n" \
  "buffer_load_dwordx4 a[60:63], v3, s[36:39], s80 offen sc1\n" \
  "v_mfma_f32_16x16x32_bf16 v[176:179], v[100:103], v[148:151], v[176:179]\n" \
  "v_mfma_f32_16x16x32_bf16 v[180:183], v[124:127], v[148:151], v[180:183]\n" \
  "s_waitcnt vmcnt(18)\n" \
  "s_barrier\n" \
  "v_readfirstlane_b32 s74, v0\n" \
  "v_mov_b32 v0, 1\n" \
  "s_cmp_eq_u32 s74, 11\n" \
  "s_cselect_b32 s73, s78, 0x40000000\n" \
  "buffer_store_dword v0, v31, s[36:39], s73 offen sc1\n" \
  "s_waitcnt lgkmcnt(8)\n" \
  "v_mfma_f32_16x16x32_bf16 v[184:187], v[32:35], v[128:131], 0\n" \
  "ds_read_b128 v[32:35], v20 offset:0\n" \
  "ds_read_b64_tr_b16 v[148:149], v27 offset:28864\n" \
  "v_mfma_f32_16x16x32_bf16 v[188:191], v[56:59], v[128:131], 0\n" \
  "ds_read_b128 v[56:59], v20 offset:768\n" \
  "ds_read_b64_tr_b16 v[150:151], v27 offset:30656\n" \
  "buffer_load_dwordx4 a[68:71], v4, s[36:39], s80 offen sc1\n" \
  "v_mfma_f32_16x16x32_bf16 v[192:195], v[80:83], v[128:131], 0\n" \
  "ds_read_b128 v[80:83], v20 offset:12288\n" \
  "v_cvt_pk_bf16_f32 v200, v168, v169\n" \
  "v_cvt_pk_bf16_f32 v201, v170, v171\n" \
  "v_mfma_f32_16x16x32_bf16 v[196:199], v[104:107], v[128:131], 0\n" \
  "ds_read_b128 v[104:107], v20 offset:13056\n" \
  "v_cvt_pk_bf16_f32 v202, v172, v173\n" \
  "v_cvt_pk_bf16_f32 v203, v174, v175\n" \
  "s_waitcnt lgkmcnt(12)\n" \
  "v_mfma_f32_16x16x32_bf16 v[184:187], v[36:39], v[132:135], v[184:187]\n" \
  "ds_read_b128 v[36:39], v21 offset:0\n" \
  "ds_read_b64_tr_b16 v[128:129], v7 offset:0\n" \
  "v_mfma_f32_16x16x32_bf16 v[188:191], v[60:63], v[132:135], v[188:191]\n" \
  "ds_read_b128 v[60:63], v21 offset:768\n" \
  "s_waitcnt lgkmcnt(8)\n" \
  "ds_read_b64_tr_b16 v[130:131], v7 offset:1792\n" \
  "buffer_load_dwordx4 a[76:79], v5, s[36:39], s80 offen sc1\n" \
  "v_mfma_f32_16x16x32_bf16 v[192:195], v[84:87], v[132:135], v[192:195]\n" \
  "ds_read_b128 v[84:87], v21 offset:12288\n" \
  "s_add_u32 s73, s69, 327680\n" \
  "v_mfma_f32_16x16x32_bf16 v[196:199], v[108:111], v[132:135], v[196:199]\n" \
  "ds_read_b128 v[108:111], v21 offset:13056\n" \
  "buffer_store_dwordx4 v[200:203], v22, s[36:39], s73 offen sc1\n" \
  "v_mfma_f32_16x16x32_bf16 v[184:187], v[40:43], v[136:139], v[184:187]\n" \
  "ds_read_b128 v[40:43], v20 offset:128\n" \
  "ds_read_b64_tr_b16 v[132:133], v7 offset:14336\n" \
  "v_mfma_f32_16x16x32_bf16 v[188:191], v[64:67], v[136:139], v[188:191]\n" \
  "ds_read_b128 v[64:67], v20 offset:896\n" \
  "ds_read_b64_tr_b16 v[134:135], v7 offset:16128\n" \
  "v_mfma_f32_16x16x32_bf16 v[192:195], v[88:91], v[136:139], v[192:195]\n" \
  "s_waitcnt lgkmcnt(8)\n" \
  "ds_read_b128 v[88:91], v20 offset:12416\n" \
  "v_cvt_pk_bf16_f32 v204, v176, v177\n" \
  "v_cvt_pk_bf16_f32 v205, v178, v179\n" \
  "v_mfma_f32_16x16x32_bf16 v[196:199], v[112:115], v[136:139], v[196:199]\n" \
  "ds_read_b128 v[112:115], v20 offset:13184\n" \
  "v_cvt_pk_bf16_f32 v206, v180, v181\n" \
  "v_cvt_pk_bf16_f32 v207, v182, v183\n" \
  "v_mfma_f32_16x16x32_bf16 v[184:187], v[44:47], v[140:143], v[184:187]\n" \
  "ds_read_b128 v[44:47], v21 offset:128\n" \
  "ds_read_b64_tr_b16 v[136:137], v7 offset:28672\n" \
  "v_mfma_f32_16x16x32_bf16 v[188:191], v[68:71], v[140:143], v[188:191]\n" \
  "ds_read_b128 v[68:71], v21 offset:896\n" \
  "ds_read_b64_tr_b16 v[138:139], v7 offset:30464\n" \
  "v_mfma_f32_16x16x32_bf16 v[192:195], v[92:95], v[140:143], v[192:195]\n" \
  "ds_read_b128 v[92:95], v21 offset:12416\n" \
  "s_add_u32 s73, s70, 327680\n" \
  "v_mfma_f32_16x16x32_bf16 v[196:199], v[116:119], v[140:143], v[196:199]\n" \
  "s_waitcnt lgkmcnt(8)\n" \
  "ds_read_b128 v[116:119], v21 offset:13184\n" \
  "buffer_store_dwordx4 v[204:207], v22, s[36:39], s73 offen sc1\n" \
  "v_mfma_f32_16x16x32_bf16 v[184:187], v[48:51], v[144:147], v[184:187]\n" \
  "ds_read_b128 v[48:51], v20 offset:256\n" \
  "ds_read_b64_tr_b16 v[140:141], v27 offset:0\n" \
  "v_mfma_f32_16x16x32_bf16 v[188:191], v[72:75], v[144:147], v[188:191]\n" \
  "ds_read_b128 v[72:75], v20 offset:1024\n" \
  "ds_read_b64_tr_b16 v[142:143], v27 offset:1792\n" \
  "v_mfma_f32_16x16x32_bf16 v[192:195], v[96:99], v[144:147], v[192:195]\n" \
  "ds_read_b128 v[96:99], v20 offset:12544\n" \
  "s_cmp_lg_u32 s78, 0\n" \
  "v_mfma_f32_16x16x32_bf16 v[196:199], v[120:123], v[144:147], v[196:199]\n" \
  "ds_read_b128 v[120:123], v20 offset:13312\n" \
  "v_mfma_f32_16x16x32_bf16 v[184:187], v[52:55], v[148:151], v[184:187]\n" \
  "s_waitcnt lgkmcnt(8)\n" \
  "ds_read_b128 v[52:55], v21 offset:256\n" \
  "ds_read_b64_tr_b16 v[144:145], v27 offset:14336\n" \
  "v_mfma_f32_16x16x32_bf16 v[188:191], v[76:79], v[148:151], v[188:191]\n" \
  "ds_read_b128 v[76:79], v21 offset:1024\n" \
  "ds_read_b64_tr_b16 v[146:147], v27 offset:16128\n" \
  "v_mfma_f32_16x16x32_bf16 v[192:195], v[100:103], v[148:151], v[192:195]\n" \
  "ds_read_b128 v[100:103], v21 offset:12544\n" \
  "v_mfma_f32_16x16x32_bf16 v[196:199], v[124:127], v[148:151], v[196:199]\n" \
  "ds_read_b128 v[124:127], v21 offset:13312\n" \
  "LUN3_%=:\n" \
  "v_mfma_f32_16x16x32_bf16 v[152:155], v[32:35], v[128:131], 0\n" \
  "ds_read_b64_tr_b16 v[148:149], v27 offset:28672\n" \
  "v_mfma_f32_16x16x32_bf16 v[156:159], v[56:59], v[128:131], 0\n" \
  "s_waitcnt lgkmcnt(8)\n" \
  "ds_read_b64_tr_b16 v[150:151], v27 offset:30464\n" \
  "v_mfma_f32_16x16x32_bf16 v[160:163], v[80:83], v[128:131], 0\n" \
  "v_cvt_pk_bf16_f32 v200, v184, v185\n" \
  "v_cvt_pk_bf16_f32 v201, v186, v187\n" \
  "v_mfma_f32_16x16x32_bf16 v[164:167], v[104:107], v[128:131], 0\n" \
  "v_cvt_pk_bf16_f32 v202, v188, v189\n" \
  "v_cvt_pk_bf16_f32 v203, v190, v191\n" \
  "v_mfma_f32_16x16x32_bf16 v[152:155], v[36:39], v[132:135], v[152:155]\n" \
  "ds_read_b64_tr_b16 v[128:129], v26 offset:0\n" \
  "v_mfma_f32_16x16x32_bf16 v[156:159], v[60:63], v[132:135], v[156:159]\n" \
  "ds_read_b64_tr_b16 v[130:131], v26 offset:1792\n" \
  "v_mfma_f32_16x16x32_bf16 v[160:163], v[84:87], v[132:135], v[160:163]\n" \
  "s_add_u32 s73, s71, 393216\n" \
  "v_mfma_f32_16x16x32_bf16 v[164:167], v[108:111], v[132:135], v[164:167]\n" \
  "buffer_store_dwordx4 v[200:203], v22, s[36:39], s73 offen sc1\n" \
  "v_mfma_f32_16x16x32_bf16 v[152:155], v[40:43], v[136:139], v[152:155]\n" \
  "ds_read_b64_tr_b16 v[132:133], v26 offset:14336\n" \
  "v_mfma_f32_16x16x32_bf16 v[156:159], v[64:67], v[136:139], v[156:159]\n" \
  "ds_read_b64_tr_b16 v[134:135], v26 offset:16128\n" \
  "v_mfma_f32_16x16x32_bf16 v[160:163], v[88:91], v[136:139], v[160:163]\n" \
  "v_cvt_pk_bf16_f32 v204, v192, v193\n" \
  "v_cvt_pk_bf16_f32 v205, v194, v195\n" \
  "v_mfma_f32_16x16x32_bf16 v[164:167], v[112:115], v[136:139], v[164:167]\n" \
  "v_cvt_pk_bf16_f32 v206, v196, v197\n" \
  "v_cvt_pk_bf16_f32 v207, v198, v199\n" \
  "v_mfma_f32_16x16x32_bf16 v[152:155], v[44:47], v[140:143], v[152:155]\n" \
  "ds_read_b64_tr_b16 v[136:137], v26 offset:28672\n" \
  "v_mfma_f32_16x16x32_bf16 v[156:159], v[68:71], v[140:143], v[156:159]\n" \
  "ds_read_b64_tr_b16 v[138:139], v26 offset:30464\n" \
  "v_mfma_f32_16x16x32_bf16 v[160:163], v[92:95], v[140:143], v[160:163]\n" \
  "s_add_u32 s73, s72, 393216\n" \
  "v_mfma_f32_16x16x32_bf16 v[164:167], v[116:119], v[140:143], v[164:167]\n" \
  "buffer_store_dwordx4 v[204:207], v22, s[36:39], s73 offen sc1\n" \
  "s_waitcnt lgkmcnt(10)\n" \
  "v_mfma_f32_16x16x32_bf16 v[152:155], v[48:51], v[144:147], v[152:155]\n" \
  "ds_read_b64_tr_b16 v[140:141], v28 offset:0\n" \
  "v_mfma_f32_16x16x32_bf16 v[156:159], v[72:75], v[144:147], v[156:159]\n" \
  "ds_read_b64_tr_b16 v[142:143], v28 offset:1792\n" \
  "v_mfma_f32_16x16x32_bf16 v[160:163], v[96:99], v[144:147], v[160:163]\n" \
  "v_readlane_b32 s69, v23, s68\n" \
  "v_readlane_b32 s71, v24, s68\n" \
  "s_sub_u32 s69, s69, s62\n" \
  "s_sub_u32 s71, s71, s63\n" \
  "s_add_u32 s68, s68, 1\n" \
  "v_mfma_f32_16x16x32_bf16 v[164:167], v[120:123], v[144:147], v[164:167]\n" \
  "v_readlane_b32 s70, v23, s68\n" \
  "v_readlane_b32 s72, v24, s68\n" \
  "s_sub_u32 s70, s70, s62\n" \
  "s_sub_u32 s72, s72, s63\n" \
  "s_add_u32 s68, s68, 1\n" \
  "s_waitcnt lgkmcnt(8)\n" \
  "v_mfma_f32_16x16x32_bf16 v[152:155], v[52:55], v[148:151], v[152:155]\n" \
  "ds_read_b64_tr_b16 v[144:145], v28 offset:14336\n" \
  "v_mfma_f32_16x16x32_bf16 v[156:159], v[76:79], v[148:151], v[156:159]\n" \
  "ds_read_b64_tr_b16 v[146:147], v28 offset:16128\n" \
  "v_mfma_f32_16x16x32_bf16 v[160:163], v[100:103], v[148:151], v[160:163]\n" \
  "s_cmp_eq_u32 s68, s77\n" \
  "s_cselect_b32 s75, 0x40000000, s75\n" \
  "v_mfma_f32_16x16x32_bf16 v[164:167], v[124:127], v[148:151], v[164:167]\n" \
  "s_barrier\n" \
  "s_waitcnt lgkmcnt(8)\n" \
  "v_mfma_f32_16x16x32_bf16 v[168:171], v[32:35], v[128:131], 0\n" \
  "ds_read_b64_tr_b16 v[148:149], v28 offset:28672\n" \
  "v_mfma_f32_16x16x32_bf16 v[172:175], v[56:59], v[128:131], 0\n" \
  "ds_read_b64_tr_b16 v[150:151], v28 offset:30464\n" \
  "v_mfma_f32_16x16x32_bf16 v[176:179], v[80:83], v[128:131], 0\n" \
  "v_cvt_pk_bf16_f32 v200, v152, v153\n" \
  "v_cvt_pk_bf16_f32 v201, v154, v155\n" \
  "v_mfma_f32_16x16x32_bf16 v[180:183], v[104:107], v[128:131], 0\n" \
  "s_add_u32 m0, s76, 0\n" \
  "s_nop 0\n" \
  "buffer_load_dwordx4 v8, s[40:43], s75 offen offset:0 lds\n" \
  "s_waitcnt lgkmcnt(8)\n" \
  "v_mfma_f32_16x16x32_bf16 v[168:171], v[36:39], v[132:135], v[168:171]\n" \
  "ds_read_b64_tr_b16 v[128:129], v7 offset:64\n" \
  "v_mfma_f32_16x16x32_bf16 v[172:175], v[60:63], v[132:135], v[172:175]\n" \
  "ds_read_b64_tr_b16 v[130:131], v7 offset:1856\n" \
  "v_mfma_f32_16x16x32_bf16 v[176:179], v[84:87], v[132:135], v[176:179]\n" \
  "v_cvt_pk_bf16_f32 v202, v156, v157\n" \
  "v_cvt_pk_bf16_f32 v203, v158, v159\n" \
  "v_mfma_f32_16x16x32_bf16 v[180:183], v[108:111], v[132:135], v[180:183]\n" \
  "s_add_u32 s73, s69, 0\n" \
  "buffer_load_dword v242, v30, s[36:39], s78 offen sc1\n" \
  "s_waitcnt lgkmcnt(8)\n" \
  "v_mfma_f32_16x16x32_bf16 v[168:171], v[40:43], v[136:139], v[168:171]\n" \
  "ds_read_b64_tr_b16 v[132:133], v7 offset:14400\n" \
  "v_mfma_f32_16x16x32_bf16 v[172:175], v[64:67], v[136:139], v[172:175]\n" \
  "ds_read_b64_tr_b16 v[134:135], v7 offset:16192\n" \
  "v_mfma_f32_16x16x32_bf16 v[176:179], v[88:91], v[136:139], v[176:179]\n" \
  "buffer_load_dwordx4 v9, s[40:43], s75 offen offset:1024 lds\n" \
  "v_mfma_f32_16x16x32_bf16 v[180:183], v[112:115], v[136:139], v[180:183]\n" \
  "buffer_store_dwordx4 v[200:203], v22, s[36:39], s73 offen sc1\n" \
  "s_waitcnt lgkmcnt(8)\n" \
  "v_mfma_f32_16x16x32_bf16 v[168:171], v[44:47], v[140:143], v[168:171]\n" \
  "ds_read_b64_tr_b16 v[136:137], v7 offset:28736\n" \
  "v_mfma_f32_16x16x32_bf16 v[172:175], v[68:71], v[140:143], v[172:175]\n" \
  "ds_read_b64_tr_b16 v[138:139], v7 offset:30528\n" \
  "v_mfma_f32_16x16x32_bf16 v[176:179], v[92:95], v[140:143], v[176:179]\n" \
  "v_cvt_pk_bf16_f32 v204, v160, v161\n" \
  "v_cvt_pk_bf16_f32 v205, v162, v163\n" \
  "v_mfma_f32_16x16x32_bf16 v[180:183], v[116:119], v[140:143], v[180:183]\n" \
  "buffer_load_dwordx4 v10, s[40:43], s75 offen offset:2048 lds\n" \
  "s_waitcnt lgkmcnt(8)\n" \
  "v_mfma_f32_16x16x32_bf16 v[168:171], v[48:51], v[144:147], v[168:171]\n" \
  "ds_read_b64_tr_b16 v[140:141], v27 offset:64\n" \
  "v_mfma_f32_16x16x32_bf16 v[172:175], v[72:75], v[144:147], v[172:175]\n" \
  "ds_read_b64_tr_b16 v[142:143], v27 offset:1856\n" \
  "v_mfma_f32_16x16x32_bf16 v[176:179], v[96:99], v[144:147], v[176:179]\n" \
  "v_cvt_pk_bf16_f32 v206, v164, v165\n" \
  "v_cvt_pk_bf16_f32 v207, v166, v167\n" \
  "v_mfma_f32_16x16x32_bf16 v[180:183], v[120:123], v[144:147], v[180:183]\n" \
  "s_add_u32 s73, s70, 0\n" \
  "s_waitcnt lgkmcnt(8)\n" \
  "v_mfma_f32_16x16x32_bf16 v[168:171], v[52:55], v[148:151], v[168:171]\n" \
  "ds_read_b64_tr_b16 v[144:145], v27 offset:14400\n" \
  "v_mfma_f32_16x16x32_bf16 v[172:175], v[76:79], v[148:151], v[172:175]\n" \
  "ds_read_b64_tr_b16 v[146:147], v27 offset:16192\n" \
  "v_mfma_f32_16x16x32_bf16 v[176:179], v[100:103], v[148:151], v[176:179]\n" \
  "buffer_load_dwordx4 v11, s[40:43], s75 offen offset:3072 lds\n" \
  "v_mfma_f32_16x16x32_bf16 v[180:183], v[124:127], v[148:151], v[180:183]\n" \
  "buffer_store_dwordx4 v[204:207], v22, s[36:39], s73 offen sc1\n" \
  "s_waitcnt lgkmcnt(8)\n" \
  "v_mfma_f32_16x16x32_bf16 v[152:155], v[32:35], v[128:131], 0\n" \
  "ds_read_b64_tr_b16 v[148:149], v27 offset:28736\n" \
  "v_mfma_f32_16x16x32_bf16 v[156:159], v[56:59], v[128:131], 0\n" \
  "ds_read_b64_tr_b16 v[150:151], v27 offset:30528\n" \
  "v_mfma_f32_16x16x32_bf16 v[160:163], v[80:83], v[128:131], 0\n" \
  "v_cvt_pk_bf16_f32 v200, v168, v169\n" \
  "v_cvt_pk_bf16_f32 v201, v170, v171\n" \
  "v_mfma_f32_16x16x32_bf16 v[164:167], v[104:107], v[128:131], 0\n" \
  "s_add_u32 m0, s76, 4096\n" \
  "s_nop 0\n" \
  "buffer_load_dwordx4 v12, s[40:43], s75 offen offset:0 lds\n" \
  "s_waitcnt lgkmcnt(8)\n" \
  "v_mfma_f32_16x16x32_bf16 v[152:155], v[36:39], v[132:135], v[152:155]\n" \
  "ds_read_b64_tr_b16 v[128:129], v26 offset:64\n" \
  "v_mfma_f32_16x16x32_bf16 v[156:159], v[60:63], v[132:135], v[156:159]\n" \
  "ds_read_b64_tr_b16 v[130:131], v26 offset:1856\n" \
  "v_mfma_f32_16x16x32_bf16 v[160:163], v[84:87], v[132:135], v[160:163]\n" \
  "v_cvt_pk_bf16_f32 v202, v172, v173\n" \
  "v_cvt_pk_bf16_f32 v203, v174, v175\n" \
  "v_mfma_f32_16x16x32_bf16 v[164:167], v[108:111], v[132:135], v[164:167]\n" \
  "s_add_u32 s73, s69, 65536\n" \
  "s_waitcnt lgkmcnt(8)\n" \
  "v_mfma_f32_16x16x32_bf16 v[152:155], v[40:43], v[136:139], v[152:155]\n" \
  "ds_read_b64_tr_b16 v[132:133], v26 offset:14400\n" \
  "v_mfma_f32_16x16x32_bf16 v[156:159], v[64:67], v[136:139], v[156:159]\n" \
  "ds_read_b64_tr_b16 v[134:135], v26 offset:16192\n" \
  "v_mfma_f32_16x16x32_bf16 v[160:163], v[88:91], v[136:139], v[160:163]\n" \
  "buffer_load_dwordx4 v13, s[40:43], s75 offen offset:1024 lds\n" \
  "v_mfma_f32_16x16x32_bf16 v[164:167], v[112:115], v[136:139], v[164:167]\n" \
  "buffer_store_dwordx4 v[200:203], v22, s[36:39], s73 offen sc1\n" \
  "s_waitcnt lgkmcnt(8)\n" \
  "v_mfma_f32_16x16x32_bf16 v[152:155], v[44:47], v[140:143], v[152:155]\n" \
  "ds_read_b64_tr_b16 v[136:137], v26 offset:28736\n" \
  "v_mfma_f32_16x16x32_bf16 v[156:159], v[68:71], v[140:143], v[156:159]\n" \
  "ds_read_b64_tr_b16 v[138:139], v26 offset:30528\n" \
  "v_mfma_f32_16x16x32_bf16 v[160:163], v[92:95], v[140:143], v[160:163]\n" \
  "v_cvt_pk_bf16_f32 v204, v176, v177\n" \
  "v_cvt_pk_bf16_f32 v205, v178, v179\n" \
  "v_mfma_f32_16x16x32_bf16 v[164:167], v[116:119], v[140:143], v[164:167]\n" \
  "buffer_load_dwordx4 v14, s[40:43], s75 offen offset:2048 lds\n" \
  "s_waitcnt lgkmcnt(8)\n" \
  "v_mfma_f32_16x16x32_bf16 v[152:155], v[48:51], v[144:147], v[152:155]\n" \
  "ds_read_b64_tr_b16 v[140:141], v28 offset:64\n" \
  "v_mfma_f32_16x16x32_bf16 v[156:159], v[72:75], v[144:147], v[156:159]\n" \
  "ds_read_b64_tr_b16 v[142:143], v28 offset:1856\n" \
  "v_mfma_f32_16x16x32_bf16 v[160:163], v[96:99], v[144:147], v[160:163]\n" \
  "v_cvt_pk_bf16_f32 v206, v180, v181\n" \
  "v_cvt_pk_bf16_f32 v207, v182, v183\n" \
  "v_mfma_f32_16x16x32_bf16 v[164:167], v[120:123], v[144:147], v[164:167]\n" \
  "s_add_u32 s73, s70, 65536\n" \
  "s_waitcnt vmcnt(9)\n" \
  "v_cmp_ne_u32 vcc, 1, v242\n" \
  "v_readlane_b32 s79, v224, 2\n" \
  "s_waitcnt lgkmcnt(8)\n" \
  "v_mfma_f32_16x16x32_bf16 v[152:155], v[52:55], v[148:151], v[152:155]\n" \
  "ds_read_b64_tr_b16 v[144:145], v28 offset:14400\n" \
  "v_mfma_f32_16x16x32_bf16 v[156:159], v[76:79], v[148:151], v[156:159]\n" \
  "ds_read_b64_tr_b16 v[146:147], v28 offset:16192\n" \
  "v_mfma_f32_16x16x32_bf16 v[160:163], v[100:103], v[148:151], v[160:163]\n" \
  "buffer_load_dwordx4 v15, s[40:43], s75 offen offset:3072 lds\n" \
  "v_mfma_f32_16x16x32_bf16 v[164:167], v[124:127], v[148:151], v[164:167]\n" \
  "buffer_store_dwordx4 v[204:207], v22, s[36:39], s73 offen sc1\n" \
  "s_cbranch_vccnz LUP3_%=\n" \
  "LUK3_%=:\n" \
  "s_waitcnt lgkmcnt(8)\n" \
  "v_mfma_f32_16x16x32_bf16 v[168:171], v[32:35], v[128:131], 0\n" \
  "ds_read_b64_tr_b16 v[148:149], v28 offset:28736\n" \
  "v_mfma_f32_16x16x32_bf16 v[172:175], v[56:59], v[128:131], 0\n" \
  "ds_read_b64_tr_b16 v[150:151], v28 offset:30528\n" \
  "buffer_load_dwordx4 a[80:83], v1, s[36:39], s79 offen sc1\n" \
  "v_readlane_b32 s80, v224, 3\n" \
  "v_mfma_f32_16x16x32_bf16 v[176:179], v[80:83], v[128:131], 0\n" \
  "v_cvt_pk_bf16_f32 v200, v152, v153\n" \
  "v_cvt_pk_bf16_f32 v201, v154, v155\n" \
  "s_waitcnt vmcnt(14)\n" \
  "s_barrier\n" \
  "s_mov_b32 s78, s88\n" \
  "buffer_atomic_add v0, v25, s[36:39], s78 offen sc0 sc1\n" \
  "s_add_u32 s88, s88, 1536\n" \
  "s_and_b32 s88, s88, 0x3fffffff\n" \
  "v_mfma_f32_16x16x32_bf16 v[180:183], v[104:107], v[128:131], 0\n" \
  "s_add_u32 m0, s76, 8192\n" \
  "s_nop 0\n" \
  "buffer_load_dwordx4 v16, s[40:43], s75 offen offset:0 lds\n" \
  "s_waitcnt lgkmcnt(8)\n" \
  "v_mfma_f32_16x16x32_bf16 v[168:171], v[36:39], v[132:135], v[168:171]\n" \
  "ds_read_b64_tr_b16 v[128:129], v7 offset:128\n" \
  "v_mfma_f32_16x16x32_bf16 v[172:175], v[60:63], v[132:135], v[172:175]\n" \
  "ds_read_b64_tr_b16 v[130:131], v7 offset:1920\n" \
  "buffer_load_dwordx4 a[88:91], v2, s[36:39], s79 offen sc1\n" \
  "v_mfma_f32_16x16x32_bf16 v[176:179], v[84:87], v[132:135], v[176:179]\n" \
  "v_cvt_pk_bf16_f32 v202, v156, v157\n" \
  "v_cvt_pk_bf16_f32 v203, v158, v159\n" \
  "v_mfma_f32_16x16x32_bf16 v[180:183], v[108:111], v[132:135], v[180:183]\n" \
  "s_add_u32 s73, s69, 131072\n" \
  "s_waitcnt lgkmcnt(8)\n" \
  "v_mfma_f32_16x16x32_bf16 v[168:171], v[40:43], v[136:139], v[168:171]\n" \
  "ds_read_b64_tr_b16 v[132:133], v7 offset:14464\n" \
  "v_mfma_f32_16x16x32_bf16 v[172:175], v[64:67], v[136:139], v[172:175]\n" \
  "ds_read_b64_tr_b16 v[134:135], v7 offset:16256\n" \
  "buffer_load_dwordx4 a[96:99], v3, s[36:39], s79 offen sc1\n" \
  "v_mfma_f32_16x16x32_bf16 v[176:179], v[88:91], v[136:139], v[176:179]\n" \
  "buffer_load_dwordx4 v17, s[40:43], s75 offen offset:1024 lds\n" \
  "v_mfma_f32_16x16x32_bf16 v[180:183], v[112:115], v[136:139], v[180:183]\n" \
  "buffer_store_dwordx4 v[200:203], v22, s[36:39], s73 offen sc1\n" \
  "s_waitcnt lgkmcnt(8)\n" \
  "v_mfma_f32_16x16x32_bf16 v[168:171], v[44:47], v[140:143], v[168:171]\n" \
  "ds_read_b64_tr_b16 v[136:137], v7 offset:28800\n" \
  "v_mfma_f32_16x16x32_bf16 v[172:175], v[68:71], v[140:143], v[172:175]\n" \
  "ds_read_b64_tr_b16 v[138:139], v7 offset:30592\n" \
  "buffer_load_dwordx4 a[104:107], v4, s[36:39], s79 offen sc1\n" \
  "v_mfma_f32_16x16x32_bf16 v[176:179], v[92:95], v[140:143], v[176:179]\n" \
  "v_cvt_pk_bf16_f32 v204, v160, v161\n" \
  "v_cvt_pk_bf16_f32 v205, v162, v163\n" \
  "v_mfma_f32_16x16x32_bf16 v[180:183], v[116:119], v[140:143], v[180:183]\n" \
  "buffer_load_dwordx4 v18, s[40:43], s75 offen offset:2048 lds\n" \
  "s_waitcnt lgkmcnt(8)\n" \
  "v_mfma_f32_16x16x32_bf16 v[168:171], v[48:51], v[144:147], v[168:171]\n" \
  "ds_read_b64_tr_b16 v[140:141], v27 offset:128\n" \
  "v_mfma_f32_16x16x32_bf16 v[172:175], v[72:75], v[144:147], v[172:175]\n" \
  "ds_read_b64_tr_b16 v[142:143], v27 offset:1920\n" \
  "buffer_load_dwordx4 a[112:115], v5, s[36:39], s79 offen sc1\n" \
  "v_mfma_f32_16x16x32_bf16 v[176:179], v[96:99], v[144:147], v[176:179]\n" \
  "v_cvt_pk_bf16_f32 v206, v164, v165\n" \
  "v_cvt_pk_bf16_f32 v207, v166, v167\n" \
  "v_mfma_f32_16x16x32_bf16 v[180:183], v[120:123], v[144:147], v[180:183]\n" \
  "s_add_u32 s73, s70, 131072\n" \
  "s_waitcnt lgkmcnt(8)\n" \
  "v_mfma_f32_16x16x32_bf16 v[168:171], v[52:55], v[148:151], v[168:171]\n" \
  "ds_read_b64_tr_b16 v[144:145], v27 offset:14464\n" \
  "v_mfma_f32_16x16x32_bf16 v[172:175], v[76:79], v[148:151], v[172:175]\n" \
  "ds_read_b64_tr_b16 v[146:147], v27 offset:16256\n" \
  "buffer_load_dwordx4 a[84:87], v1, s[36:39], s80 offen sc1\n" \
  "v_readlane_b32 s79, v224, 12\n" \
  "v_mfma_f32_16x16x32_bf16 v[176:179], v[100:103], v[148:151], v[176:179]\n" \
  "buffer_load_dwordx4 v19, s[40:43], s75 offen offset:3072 lds\n" \
  "s_add_u32 s75, s75, 786432\n" \
  "v_mfma_f32_16x16x32_bf16 v[180:183], v[124:127], v[148:151], v[180:183]\n" \
  "buffer_store_dwordx4 v[204:207], v22, s[36:39], s73 offen sc1\n" \
  "s_waitcnt lgkmcnt(8)\n" \
  "v_mfma_f32_16x16x32_bf16 v[152:155], v[32:35], v[128:131], 0\n" \
  "ds_read_b64_tr_b16 v[148:149], v27 offset:28800\n" \
  "v_mfma_f32_16x16x32_bf16 v[156:159], v[56:59], v[128:131], 0\n" \
  "ds_read_b64_tr_b16 v[150:151], v27 offset:30592\n" \
  "buffer_load_dwordx4 a[92:95], v2, s[36:39], s80 offen sc1\n" \
  "v_mfma_f32_16x16x32_bf16 v[160:163], v[80:83], v[128:131], 0\n" \
  "v_cvt_pk_bf16_f32 v200, v168, v169\n" \
  "v_cvt_pk_bf16_f32 v201, v170, v171\n" \
  "v_mfma_f32_16x16x32_bf16 v[164:167], v[104:107], v[128:131], 0\n" \
  "v_cvt_pk_bf16_f32 v202, v172, v173\n" \
  "v_cvt_pk_bf16_f32 v203, v174, v175\n" \
  "s_waitcnt lgkmcnt(8)\n" \
  "v_mfma_f32_16x16x32_bf16 v[152:155], v[36:39], v[132:135], v[152:155]\n" \
  "ds_read_b64_tr_b16 v[128:129], v26 offset:128\n" \
  "v_mfma_f32_16x16x32_bf16 v[156:159], v[60:63], v[132:135], v[156:159]\n" \
  "ds_read_b64_tr_b16 v[130:131], v26 offset:1920\n" \
  "buffer_load_dwordx4 a[100:103], v3, s[36:39], s80 offen sc1\n" \
  "v_mfma_f32_16x16x32_bf16 v[160:163], v[84:87], v[132:135], v[160:163]\n" \
  "s_add_u32 s73, s69, 196608\n" \
  "v_mfma_f32_16x16x32_bf16 v[164:167], v[108:111], v[132:135], v[164:167]\n" \
  "buffer_store_dwordx4 v[200:203], v22, s[36:39], s73 offen sc1\n" \
  "s_waitcnt lgkmcnt(8)\n" \
  "v_mfma_f32_16x16x32_bf16 v[152:155], v[40:43], v[136:139], v[152:155]\n" \
  "ds_read_b64_tr_b16 v[132:133], v26 offset:14464\n" \
  "v_mfma_f32_16x16x32_bf16 v[156:159], v[64:67], v[136:139], v[156:159]\n" \
  "ds_read_b64_tr_b16 v[134:135], v26 offset:16256\n" \
  "buffer_load_dwordx4 a[108:111], v4, s[36:39], s80 offen sc1\n" \
  "v_mfma_f32_16x16x32_bf16 v[160:163], v[88:91], v[136:139], v[160:163]\n" \
  "v_cvt_pk_bf16_f32 v204, v176, v177\n" \
  "v_cvt_pk_bf16_f32 v205, v178, v179\n" \
  "v_mfma_f32_16x16x32_bf16 v[164:167], v[112:115], v[136:139], v[164:167]\n" \
  "v_cvt_pk_bf16_f32 v206, v180, v181\n" \
  "v_cvt_pk_bf16_f32 v207, v182, v183\n" \
  "s_waitcnt lgkmcnt(8)\n" \
  "v_mfma_f32_16x16x32_bf16 v[152:155], v[44:47], v[140:143], v[152:155]\n" \
  "ds_read_b64_tr_b16 v[136:137], v26 offset:28800\n" \
  "v_mfma_f32_16x16x32_bf16 v[156:159], v[68:71], v[140:143], v[156:159]\n" \
  "ds_read_b64_tr_b16 v[138:139], v26 offset:30592\n" \
  "buffer_load_dwordx4 a[116:119], v5, s[36:39], s80 offen sc1\n" \
  "v_mfma_f32_16x16x32_bf16 v[160:163], v[92:95], v[140:143], v[160:163]\n" \
  "s_add_u32 s73, s70, 196608\n" \
  "v_mfma_f32_16x16x32_bf16 v[164:167], v[116:119], v[140:143], v[164:167]\n" \
  "buffer_store_dwordx4 v[204:207], v22, s[36:39], s73 offen sc1\n" \
  "s_waitcnt lgkmcnt(8)\n" \
  "v_mfma_f32_16x16x32_bf16 v[152:155], v[48:51], v[144:147], v[152:155]\n" \
  "ds_read_b64_tr_b16 v[140:141], v28 offset:128\n" \
  "v_mfma_f32_16x16x32_bf16 v[156:159], v[72:75], v[144:147], v[156:159]\n" \
  "ds_read_b64_tr_b16 v[142:143], v28 offset:1920\n" \
  "buffer_load_dwordx4 a[120:123], v1, s[36:39], s79 offen sc1\n" \
  "v_readlane_b32 s80, v224, 13\n" \
  "v_mfma_f32_16x16x32_bf16 v[160:163], v[96:99], v[144:147], v[160:163]\n" \
  "s_getpc_b64 s[82:83]\n" \
  "LCF8_%=:\n" \
  "s_add_u32 s82, s82, LUN4_%=-LCF8_%=\n" \
  "s_addc_u32 s83, s83, 0\n" \
  "v_mbcnt_lo_u32_b32 v240, -1, 0\n" \
  "v_mbcnt_hi_u32_b32 v240, -1, v240\n" \
  "v_lshlrev_b32 v240, 6, v240\n" \
  "global_load_dword v241, v240, s[82:83]\n" \
  "v_mfma_f32_16x16x32_bf16 v[164:167], v[120:123], v[144:147], v[164:167]\n" \
  "s_waitcnt lgkmcnt(8)\n" \
  "v_mfma_f32_16x16x32_bf16 v[152:155], v[52:55], v[148:151], v[152:155]\n" \
  "ds_read_b64_tr_b16 v[144:145], v28 offset:14464\n" \
  "v_mfma_f32_16x16x32_bf16 v[156:159], v[76:79], v[148:151], v[156:159]\n" \
  "ds_read_b64_tr_b16 v[146:147], v28 offset:16256\n" \
  "buffer_load_dwordx4 a[128:131], v2, s[36:39], s79 offen sc1\n" \
  "v_mfma_f32_16x16x32_bf16 v[160:163], v[100:103], v[148:151], v[160:163]\n" \
  "v_mfma_f32_16x16x32_bf16 v[164:167], v[124:127], v[148:151], v[164:167]\n" \
  "s_waitcnt lgkmcnt(8)\n" \
  "v_mfma_f32_16x16x32_bf16 v[168:171], v[32:35], v[128:131], 0\n" \
  "ds_read_b64_tr_b16 v[148:149], v28 offset:28800\n" \
  "v_mfma_f32_16x16x32_bf16 v[172:175], v[56:59], v[128:131], 0\n" \
  "ds_read_b64_tr_b16 v[150:151], v28 offset:30592\n" \
  "buffer_load_dwordx4 a[136:139], v3, s[36:39], s79 offen sc1\n" \
  "v_mfma_f32_16x16x32_bf16 v[176:179], v[80:83], v[128:131], 0\n" \
  "v_cvt_pk_bf16_f32 v200, v152, v153\n" \
  "v_cvt_pk_bf16_f32 v201, v154, v155\n" \
  "v_mfma_f32_16x16x32_bf16 v[180:183], v[104:107], v[128:131], 0\n" \
  "v_cvt_pk_bf16_f32 v202, v156, v157\n" \
  "v_cvt_pk_bf16_f32 v203, v158, v159\n" \
  "s_waitcnt lgkmcnt(8)\n" \
  "v_mfma_f32_16x16x32_bf16 v[168:171], v[36:39], v[132:135], v[168:171]\n" \
  "ds_read_b64_tr_b16 v[128:129], v7 offset:192\n" \
  "v_mfma_f32_16x16x32_bf16 v[172:175], v[60:63], v[132:135], v[172:175]\n" \
  "ds_read_b64_tr_b16 v[130:131], v7 offset:1984\n" \
  "buffer_load_dwordx4 a[144:147], v4, s[36:39], s79 offen sc1\n" \
  "v_mfma_f32_16x16x32_bf16 v[176:179], v[84:87], v[132:135], v[176:179]\n" \
  "s_add_u32 s73, s69, 262144\n" \
  "v_mfma_f32_16x16x32_bf16 v[180:183], v[108:111], v[132:135], v[180:183]\n" \
  "buffer_store_dwordx4 v[200:203], v22, s[36:39], s73 offen sc1\n" \
  "s_waitcnt lgkmcnt(8)\n" \
  "v_mfma_f32_16x16x32_bf16 v[168:171], v[40:43], v[136:139], v[168:171]\n" \
  "ds_read_b64_tr_b16 v[132:133], v7 offset:14528\n" \
  "v_mfma_f32_16x16x32_bf16 v[172:175], v[64:67], v[136:139], v[172:175]\n" \
  "ds_read_b64_tr_b16 v[134:135], v7 offset:16320\n" \
  "buffer_load_dwordx4 a[152:155], v5, s[36:39], s79 offen sc1\n" \
  "v_mfma_f32_16x16x32_bf16 v[176:179], v[88:91], v[136:139], v[176:179]\n" \
  "v_cvt_pk_bf16_f32 v204, v160, v161\n" \
  "v_cvt_pk_bf16_f32 v205, v162, v163\n" \
  "v_mfma_f32_16x16x32_bf16 v[180:183], v[112:115], v[136:139], v[180:183]\n" \
  "v_cvt_pk_bf16_f32 v206, v164, v165\n" \
  "v_cvt_pk_bf16_f32 v207, v166, v167\n" \
  "s_waitcnt lgkmcnt(8)\n" \
  "v_mfma_f32_16x16x32_bf16 v[168:171], v[44:47], v[140:143], v[168:171]\n" \
  "ds_read_b64_tr_b16 v[136:137], v7 offset:28864\n" \
  "v_mfma_f32_16x16x32_bf16 v[172:175], v[68:71], v[140:143], v[172:175]\n" \
  "ds_read_b64_tr_b16 v[138:139], v7 offset:30656\n" \
  "buffer_load_dwordx4 a[124:127], v1, s[36:39], s80 offen sc1\n" \
  "v_mfma_f32_16x16x32_bf16 v[176:179], v[92:95], v[140:143], v[176:179]\n" \
  "s_add_u32 s73, s70, 262144\n" \
  "v_mfma_f32_16x16x32_bf16 v[180:183], v[116:119], v[140:143], v[180:183]\n" \
  "buffer_store_dwordx4 v[204:207], v22, s[36:39], s73 offen sc1\n" \
  "s_waitcnt lgkmcnt(8)\n" \
  "v_mfma_f32_16x16x32_bf16 v[168:171], v[48:51], v[144:147], v[168:171]\n" \
  "ds_read_b64_tr_b16 v[140:141], v27 offset:192\n" \
  "v_mfma_f32_16x16x32_bf16 v[172:175], v[72:75], v[144:147], v[172:175]\n" \
  "ds_read_b64_tr_b16 v[142:143], v27 offset:1984\n" \
  "buffer_load_dwordx4 a[132:135], v2, s[36:39], s80 offen sc1\n" \
  "v_mfma_f32_16x16x32_bf16 v[176:179], v[96:99], v[144:147], v[176:179]\n" \
  "v_mfma_f32_16x16x32_bf16 v[180:183], v[120:123], v[144:147], v[180:183]\n" \
  "s_waitcnt lgkmcnt(8)\n" \
  "v_mfma_f32_16x16x32_bf16 v[168:171], v[52:55], v[148:151], v[168:171]\n" \
  "ds_read_b64_tr_b16 v[144:145], v27 offset:14528\n" \
  "v_mfma_f32_16x16x32_bf16 v[172:175], v[76:79], v[148:151], v[172:175]\n" \
  "ds_read_b64_tr_b16 v[146:147], v27 offset:16320\n" \
  "buffer_load_dwordx4 a[140:143], v3, s[36:39], s80 offen sc1\n" \
  "v_mfma_f32_16x16x32_bf16 v[176:179], v[100:103], v[148:151], v[176:179]\n" \
  "v_mfma_f32_16x16x32_bf16 v[180:183], v[124:127], v[148:151], v[180:183]\n" \
  "s_waitcnt vmcnt(18)\n" \
  "s_barrier\n" \
  "v_readfirstlane_b32 s74, v0\n" \
  "v_mov_b32 v0, 1\n" \
  "s_cmp_eq_u32 s74, 11\n" \
  "s_cselect_b32 s73, s78, 0x40000000\n" \
  "buffer_store_dword v0, v31, s[36:39], s73 offen sc1\n" \
  "s_waitcnt lgkmcnt(8)\n" \
  "v_mfma_f32_16x16x32_bf16 v[184:187], v[32:35], v[128:131], 0\n" \
  "ds_read_b128 v[32:35], v20 offset:0\n" \
  "ds_read_b64_tr_b16 v[148:149], v27 offset:28864\n" \
  "v_mfma_f32_16x16x32_bf16 v[188:191], v[56:59], v[128:131], 0\n" \
  "ds_read_b128 v[56:59], v20 offset:768\n" \
  "ds_read_b64_tr_b16 v[150:151], v27 offset:30656\n" \
  "buffer_load_dwordx4 a[148:151], v4, s[36:39], s80 offen sc1\n" \
  "v_mfma_f32_16x16x32_bf16 v[192:195], v[80:83], v[128:131], 0\n" \
  "ds_read_b128 v[80:83], v20 offset:12288\n" \
  "v_cvt_pk_bf16_f32 v200, v168, v169\n" \
  "v_cvt_pk_bf16_f32 v201, v170, v171\n" \
  "v_mfma_f32_16x16x32_bf16 v[196:199], v[104:107], v[128:131], 0\n" \
  "ds_read_b128 v[104:107], v20 offset:13056\n" \
  "v_cvt_pk_bf16_f32 v202, v172, v173\n" \
  "v_cvt_pk_bf16_f32 v203, v174, v175\n" \
  "s_waitcnt lgkmcnt(12)\n" \
  "v_mfma_f32_16x16x32_bf16 v[184:187], v[36:39], v[132:135], v[184:187]\n" \
  "ds_read_b128 v[36:39], v21 offset:0\n" \
  "ds_read_b64_tr_b16 v[128:129], v7 offset:0\n" \
  "v_mfma_f32_16x16x32_bf16 v[188:191], v[60:63], v[132:135], v[188:191]\n" \
  "ds_read_b128 v[60:63], v21 offset:768\n" \
  "s_waitcnt lgkmcnt(8)\n" \
  "ds_read_b64_tr_b16 v[130:131], v7 offset:1792\n" \
  "buffer_load_dwordx4 a[156:159], v5, s[36:39], s80 offen sc1\n" \
  "v_mfma_f32_16x16x32_bf16 v[192:195], v[84:87], v[132:135], v[192:195]\n" \
  "ds_read_b128 v[84:87], v21 offset:12288\n" \
  "s_add_u32 s73, s69, 327680\n" \
  "v_mfma_f32_16x16x32_bf16 v[196:199], v[108:111], v[132:135], v[196:199]\n" \
  "ds_read_b128 v[108:111], v21 offset:13056\n" \
  "buffer_store_dwordx4 v[200:203], v22, s[36:39], s73 offen sc1\n" \
  "v_mfma_f32_16x16x32_bf16 v[184:187], v[40:43], v[136:139], v[184:187]\n" \
  "ds_read_b128 v[40:43], v20 offset:128\n" \
  "ds_read_b64_tr_b16 v[132:133], v7 offset:14336\n" \
  "v_mfma_f32_16x16x32_bf16 v[188:191], v[64:67], v[136:139], v[188:191]\n" \
  "ds_read_b128 v[64:67], v20 offset:896\n" \
  "ds_read_b64_tr_b16 v[134:135], v7 offset:16128\n" \
  "v_mfma_f32_16x16x32_bf16 v[192:195], v[88:91], v[136:139], v[192:195]\n" \
  "s_waitcnt lgkmcnt(8)\n" \
  "ds_read_b128 v[88:91], v20 offset:12416\n" \
  "v_cvt_pk_bf16_f32 v204, v176, v177\n" \
  "v_cvt_pk_bf16_f32 v205, v178, v179\n" \
  "v_mfma_f32_16x16x32_bf16 v[196:199], v[112:115], v[136:139], v[196:199]\n" \
  "ds_read_b128 v[112:115], v20 offset:13184\n" \
  "v_cvt_pk_bf16_f32 v206, v180, v181\n" \
  "v_cvt_pk_bf16_f32 v207, v182, v183\n" \
  "v_mfma_f32_16x16x32_bf16 v[184:187], v[44:47], v[140:143], v[184:187]\n" \
  "ds_read_b128 v[44:47], v21 offset:128\n" \
  "ds_read_b64_tr_b16 v[136:137], v7 offset:28672\n" \
  "v_mfma_f32_16x16x32_bf16 v[188:191], v[68:71], v[140:143], v[188:191]\n" \
  "ds_read_b128 v[68:71], v21 offset:896\n" \
  "ds_read_b64_tr_b16 v[138:139], v7 offset:30464\n" \
  "v_mfma_f32_16x16x32_bf16 v[192:195], v[92:95], v[140:143], v[192:195]\n" \
  "ds_read_b128 v[92:95], v21 offset:12416\n" \
  "s_add_u32 s73, s70, 327680\n" \
  "v_mfma_f32_16x16x32_bf16 v[196:199], v[116:119], v[140:143], v[196:199]\n" \
  "s_waitcnt lgkmcnt(8)\n" \
  "ds_read_b128 v[116:119], v21 offset:13184\n" \
  "buffer_store_dwordx4 v[204:207], v22, s[36:39], s73 offen sc1\n" \
  "v_mfma_f32_16x16x32_bf16 v[184:187], v[48:51], v[144:147], v[184:187]\n" \
  "ds_read_b128 v[48:51], v20 offset:256\n" \
  "ds_read_b64_tr_b16 v[140:141], v27 offset:0\n" \
  "v_mfma_f32_16x16x32_bf16 v[188:191], v[72:75], v[144:147], v[188:191]\n" \
  "ds_read_b128 v[72:75], v20 offset:1024\n" \
  "ds_read_b64_tr_b16 v[142:143], v27 offset:1792\n" \
  "v_mfma_f32_16x16x32_bf16 v[192:195], v[96:99], v[144:147], v[192:195]\n" \
  "ds_read_b128 v[96:99], v20 offset:12544\n" \
  "s_cmp_lg_u32 s78, 0\n" \
  "v_mfma_f32_16x16x32_bf16 v[196:199], v[120:123], v[144:147], v[196:199]\n" \
  "ds_read_b128 v[120:123], v20 offset:13312\n" \
  "v_mfma_f32_16x16x32_bf16 v[184:187], v[52:55], v[148:151], v[184:187]\n" \
  "s_waitcnt lgkmcnt(8)\n" \
  "ds_read_b128 v[52:55], v21 offset:256\n" \
  "ds_read_b64_tr_b16 v[144:145], v27 offset:14336\n" \
  "v_mfma_f32_16x16x32_bf16 v[188:191], v[76:79], v[148:151], v[188:191]\n" \
  "ds_read_b128 v[76:79], v21 offset:1024\n" \
  "ds_read_b64_tr_b16 v[146:147], v27 offset:16128\n" \
  "v_mfma_f32_16x16x32_bf16 v[192:195], v[100:103], v[148:151], v[192:195]\n" \
  "ds_read_b128 v[100:103], v21 offset:12544\n" \
  "v_mfma_f32_16x16x32_bf16 v[196:199], v[124:127], v[148:151], v[196:199]\n" \
  "ds_read_b128 v[124:127], v21 offset:13312\n" \
  "LUN4_%=:\n" \
  "v_mfma_f32_16x16x32_bf16 v[152:155], v[32:35], v[128:131], 0\n" \
  "ds_read_b64_tr_b16 v[148:149], v27 offset:28672\n" \
  "v_mfma_f32_16x16x32_bf16 v[156:159], v[56:59], v[128:131], 0\n" \
  "s_waitcnt lgkmcnt(8)\n" \
  "ds_read_b64_tr_b16 v[150:151], v27 offset:30464\n" \
  "v_mfma_f32_16x16x32_bf16 v[160:163], v[80:83], v[128:131], 0\n" \
  "v_cvt_pk_bf16_f32 v200, v184, v185\n" \
  "v_cvt_pk_bf16_f32 v201, v186, v187\n" \
  "v_mfma_f32_16x16x32_bf16 v[164:167], v[104:107], v[128:131], 0\n" \
  "v_cvt_pk_bf16_f32 v202, v188, v189\n" \
  "v_cvt_pk_bf16_f32 v203, v190, v191\n" \
  "v_mfma_f32_16x16x32_bf16 v[152:155], v[36:39], v[132:135], v[152:155]\n" \
  "ds_read_b64_tr_b16 v[128:129], v26 offset:0\n" \
  "v_mfma_f32_16x16x32_bf16 v[156:159], v[60:63], v[132:135], v[156:159]\n" \
  "ds_read_b64_tr_b16 v[130:131], v26 offset:1792\n" \
  "v_mfma_f32_16x16x32_bf16 v[160:163], v[84:87], v[132:135], v[160:163]\n" \
  "s_add_u32 s73, s71, 393216\n" \
  "v_mfma_f32_16x16x32_bf16 v[164:167], v[108:111], v[132:135], v[164:167]\n" \
  "buffer_store_dwordx4 v[200:203], v22, s[36:39], s73 offen sc1\n" \
  "v_mfma_f32_16x16x32_bf16 v[152:155], v[40:43], v[136:139], v[152:155]\n" \
  "ds_read_b64_tr_b16 v[132:133], v26 offset:14336\n" \
  "v_mfma_f32_16x16x32_bf16 v[156:159], v[64:67], v[136:139], v[156:159]\n" \
  "ds_read_b64_tr_b16 v[134:135], v26 offset:16128\n" \
  "v_mfma_f32_16x16x32_bf16 v[160:163], v[88:91], v[136:139], v[160:163]\n" \
  "v_cvt_pk_bf16_f32 v204, v192, v193\n" \
  "v_cvt_pk_bf16_f32 v205, v194, v195\n" \
  "v_mfma_f32_16x16x32_bf16 v[164:167], v[112:115], v[136:139], v[164:167]\n" \
  "v_cvt_pk_bf16_f32 v206, v196, v197\n" \
  "v_cvt_pk_bf16_f32 v207, v198, v199\n" \
  "v_mfma_f32_16x16x32_bf16 v[152:155], v[44:47], v[140:143], v[152:155]\n" \
  "ds_read_b64_tr_b16 v[136:137], v26 offset:28672\n" \
  "v_mfma_f32_16x16x32_bf16 v[156:159], v[68:71], v[140:143], v[156:159]\n" \
  "ds_read_b64_tr_b16 v[138:139], v26 offset:30464\n" \
  "v_mfma_f32_16x16x32_bf16 v[160:163], v[92:95], v[140:143], v[160:163]\n" \
  "s_add_u32 s73, s72, 393216\n" \
  "v_mfma_f32_16x16x32_bf16 v[164:167], v[116:119], v[140:143], v[164:167]\n" \
  "buffer_store_dwordx4 v[204:207], v22, s[36:39], s73 offen sc1\n" \
  "s_waitcnt lgkmcnt(10)\n" \
  "v_mfma_f32_16x16x32_bf16 v[152:155], v[48:51], v[144:147], v[152:155]\n" \
  "ds_read_b64_tr_b16 v[140:141], v28 offset:0\n" \
  "v_mfma_f32_16x16x32_bf16 v[156:159], v[72:75], v[144:147], v[156:159]\n" \
  "ds_read_b64_tr_b16 v[142:143], v28 offset:1792\n" \
  "v_mfma_f32_16x16x32_bf16 v[160:163], v[96:99], v[144:147], v[160:163]\n" \
  "v_readlane_b32 s69, v23, s68\n" \
  "v_readlane_b32 s71, v24, s68\n" \
  "s_sub_u32 s69, s69, s62\n" \
  "s_sub_u32 s71, s71, s63\n" \
  "s_add_u32 s68, s68, 1\n" \
  "v_mfma_f32_16x16x32_bf16 v[164:167], v[120:123], v[144:147], v[164:167]\n" \
  "v_readlane_b32 s70, v23, s68\n" \
  "v_readlane_b32 s72, v24, s68\n" \
  "s_sub_u32 s70, s70, s62\n" \
  "s_sub_u32 s72, s72, s63\n" \
  "s_add_u32 s68, s68, 1\n" \
  "s_waitcnt lgkmcnt(8)\n" \
  "v_mfma_f32_16x16x32_bf16 v[152:155], v[52:55], v[148:151], v[152:155]\n" \
  "ds_read_b64_tr_b16 v[144:145], v28 offset:14336\n" \
  "v_mfma_f32_16x16x32_bf16 v[156:159], v[76:79], v[148:151], v[156:159]\n" \
  "ds_read_b64_tr_b16 v[146:147], v28 offset:16128\n" \
  "v_mfma_f32_16x16x32_bf16 v[160:163], v[100:103], v[148:151], v[160:163]\n" \
  "s_cmp_eq_u32 s68, s77\n" \
  "s_cselect_b32 s75, 0x40000000, s75\n" \
  "v_mfma_f32_16x16x32_bf16 v[164:167], v[124:127], v[148:151], v[164:167]\n" \
  "s_barrier\n" \
  "s_waitcnt lgkmcnt(8)\n" \
  "v_mfma_f32_16x16x32_bf16 v[168:171], v[32:35], v[128:131], 0\n" \
  "ds_read_b64_tr_b16 v[148:149], v28 offset:28672\n" \
  "v_mfma_f32_16x16x32_bf16 v[172:175], v[56:59], v[128:131], 0\n" \
  "ds_read_b64_tr_b16 v[150:151], v28 offset:30464\n" \
  "v_mfma_f32_16x16x32_bf16 v[176:179], v[80:83], v[128:131], 0\n" \
  "v_cvt_pk_bf16_f32 v200, v152, v153\n" \
  "v_cvt_pk_bf16_f32 v201, v154, v155\n" \
  "v_mfma_f32_16x16x32_bf16 v[180:183], v[104:107], v[128:131], 0\n" \
  "s_add_u32 m0, s76, 0\n" \
  "s_nop 0\n" \
  "buffer_load_dwordx4 v8, s[40:43], s75 offen offset:0 lds\n" \
  "s_waitcnt lgkmcnt(8)\n" \
  "v_mfma_f32_16x16x32_bf16 v[168:171], v[36:39], v[132:135], v[168:171]\n" \
  "ds_read_b64_tr_b16 v[128:129], v7 offset:64\n" \
  "v_mfma_f32_16x16x32_bf16 v[172:175], v[60:63], v[132:135], v[172:175]\n" \
  "ds_read_b64_tr_b16 v[130:131], v7 offset:1856\n" \
  "v_mfma_f32_16x16x32_bf16 v[176:179], v[84:87], v[132:135], v[176:179]\n" \
  "v_cvt_pk_bf16_f32 v202, v156, v157\n" \
  "v_cvt_pk_bf16_f32 v203, v158, v159\n" \
  "v_mfma_f32_16x16x32_bf16 v[180:183], v[108:111], v[132:135], v[180:183]\n" \
  "s_add_u32 s73, s69, 0\n" \
  "buffer_load_dword v242, v30, s[36:39], s78 offen sc1\n" \
  "s_waitcnt lgkmcnt(8)\n" \
  "v_mfma_f32_16x16x32_bf16 v[168:171], v[40:43], v[136:139], v[168:171]\n" \
  "ds_read_b64_tr_b16 v[132:133], v7 offset:14400\n" \
  "v_mfma_f32_16x16x32_bf16 v[172:175], v[64:67], v[136:139], v[172:175]\n" \
  "ds_read_b64_tr_b16 v[134:135], v7 offset:16192\n" \
  "v_mfma_f32_16x16x32_bf16 v[176:179], v[88:91], v[136:139], v[176:179]\n" \
  "buffer_load_dwordx4 v9, s[40:43], s75 offen offset:1024 lds\n" \
  "v_mfma_f32_16x16x32_bf16 v[180:183], v[112:115], v[136:139], v[180:183]\n" \
  "buffer_store_dwordx4 v[200:203], v22, s[36:39], s73 offen sc1\n" \
  "s_waitcnt lgkmcnt(8)\n" \
  "v_mfma_f32_16x16x32_bf16 v[168:171], v[44:47], v[140:143], v[168:171]\n" \
  "ds_read_b64_tr_b16 v[136:137], v7 offset:28736\n" \
  "v_mfma_f32_16x16x32_bf16 v[172:175], v[68:71], v[140:143], v[172:175]\n" \
  "ds_read_b64_tr_b16 v[138:139], v7 offset:30528\n" \
  "v_mfma_f32_16x16x32_bf16 v[176:179], v[92:95], v[140:143], v[176:179]\n" \
  "v_cvt_pk_bf16_f32 v204, v160, v161\n" \
  "v_cvt_pk_bf16_f32 v205, v162, v163\n" \
  "v_mfma_f32_16x16x32_bf16 v[180:183], v[116:119], v[140:143], v[180:183]\n" \
  "buffer_load_dwordx4 v10, s[40:43], s75 offen offset:2048 lds\n" \
  "s_waitcnt lgkmcnt(8)\n" \
  "v_mfma_f32_16x16x32_bf16 v[168:171], v[48:51], v[144:147], v[168:171]\n" \
  "ds_read_b64_tr_b16 v[140:141], v27 offset:64\n" \
  "v_mfma_f32_16x16x32_bf16 v[172:175], v[72:75], v[144:147], v[172:175]\n" \
  "ds_read_b64_tr_b16 v[142:143], v27 offset:1856\n" \
  "v_mfma_f32_16x16x32_bf16 v[176:179], v[96:99], v[144:147], v[176:179]\n" \
  "v_cvt_pk_bf16_f32 v206, v164, v165\n" \
  "v_cvt_pk_bf16_f32 v207, v166, v167\n" \
  "v_mfma_f32_16x16x32_bf16 v[180:183], v[120:123], v[144:147], v[180:183]\n" \
  "s_add_u32 s73, s70, 0\n" \
  "s_waitcnt lgkmcnt(8)\n" \
  "v_mfma_f32_16x16x32_bf16 v[168:171], v[52:55], v[148:151], v[168:171]\n" \
  "ds_read_b64_tr_b16 v[144:145], v27 offset:14400\n" \
  "v_mfma_f32_16x16x32_bf16 v[172:175], v[76:79], v[148:151], v[172:175]\n" \
  "ds_read_b64_tr_b16 v[146:147], v27 offset:16192\n" \
  "v_mfma_f32_16x16x32_bf16 v[176:179], v[100:103], v[148:151], v[176:179]\n" \
  "buffer_load_dwordx4 v11, s[40:43], s75 offen offset:3072 lds\n" \
  "v_mfma_f32_16x16x32_bf16 v[180:183], v[124:127], v[148:151], v[180:183]\n" \
  "buffer_store_dwordx4 v[204:207], v22, s[36:39], s73 offen sc1\n" \
  "s_waitcnt lgkmcnt(8)\n" \
  "v_mfma_f32_16x16x32_bf16 v[152:155], v[32:35], v[128:131], 0\n" \
  "ds_read_b64_tr_b16 v[148:149], v27 offset:28736\n" \
  "v_mfma_f32_16x16x32_bf16 v[156:159], v[56:59], v[128:131], 0\n" \
  "ds_read_b64_tr_b16 v[150:151], v27 offset:30528\n" \
  "v_mfma_f32_16x16x32_bf16 v[160:163], v[80:83], v[128:131], 0\n" \
  "v_cvt_pk_bf16_f32 v200, v168, v169\n" \
  "v_cvt_pk_bf16_f32 v201, v170, v171\n" \
  "v_mfma_f32_16x16x32_bf16 v[164:167], v[104:107], v[128:131], 0\n" \
  "s_add_u32 m0, s76, 4096\n" \
  "s_nop 0\n" \
  "buffer_load_dwordx4 v12, s[40:43], s75 offen offset:0 lds\n" \
  "s_waitcnt lgkmcnt(8)\n" \
  "v_mfma_f32_16x16x32_bf16 v[152:155], v[36:39], v[132:135], v[152:155]\n" \
  "ds_read_b64_tr_b16 v[128:129], v26 offset:64\n" \
  "v_mfma_f32_16x16x32_bf16 v[156:159], v[60:63], v[132:135], v[156:159]\n" \
  "ds_read_b64_tr_b16 v[130:131], v26 offset:1856\n" \
  "v_mfma_f32_16x16x32_bf16 v[160:163], v[84:87], v[132:135], v[160:163]\n" \
  "v_cvt_pk_bf16_f32 v202, v172, v173\n" \
  "v_cvt_pk_bf16_f32 v203, v174, v175\n" \
  "v_mfma_f32_16x16x32_bf16 v[164:167], v[108:111], v[132:135], v[164:167]\n" \
  "s_add_u32 s73, s69, 65536\n" \
  "s_waitcnt lgkmcnt(8)\n" \
  "v_mfma_f32_16x16x32_bf16 v[152:155], v[40:43], v[136:139], v[152:155]\n" \
  "ds_read_b64_tr_b16 v[132:133], v26 offset:14400\n" \
  "v_mfma_f32_16x16x32_bf16 v[156:159], v[64:67], v[136:139], v[156:159]\n" \
  "ds_read_b64_tr_b16 v[134:135], v26 offset:16192\n" \
  "v_mfma_f32_16x16x32_bf16 v[160:163], v[88:91], v[136:139], v[160:163]\n" \
  "buffer_load_dwordx4 v13, s[40:43], s75 offen offset:1024 lds\n" \
  "v_mfma_f32_16x16x32_bf16 v[164:167], v[112:115], v[136:139], v[164:167]\n" \
  "buffer_store_dwordx4 v[200:203], v22, s[36:39], s73 offen sc1\n" \
  "s_waitcnt lgkmcnt(8)\n" \
  "v_mfma_f32_16x16x32_bf16 v[152:155], v[44:47], v[140:143], v[152:155]\n" \
  "ds_read_b64_tr_b16 v[136:137], v26 offset:28736\n" \
  "v_mfma_f32_16x16x32_bf16 v[156:159], v[68:71], v[140:143], v[156:159]\n" \
  "ds_read_b64_tr_b16 v[138:139], v26 offset:30528\n" \
  "v_mfma_f32_16x16x32_bf16 v[160:163], v[92:95], v[140:143], v[160:163]\n" \
  "v_cvt_pk_bf16_f32 v204, v176, v177\n" \
  "v_cvt_pk_bf16_f32 v205, v178, v179\n" \
  "v_mfma_f32_16x16x32_bf16 v[164:167], v[116:119], v[140:143], v[164:167]\n" \
  "buffer_load_dwordx4 v14, s[40:43], s75 offen offset:2048 lds\n" \
  "s_waitcnt lgkmcnt(8)\n" \
  "v_mfma_f32_16x16x32_bf16 v[152:155], v[48:51], v[144:147], v[152:155]\n" \
  "ds_read_b64_tr_b16 v[140:141], v28 offset:64\n" \
  "v_mfma_f32_16x16x32_bf16 v[156:159], v[72:75], v[144:147], v[156:159]\n" \
  "ds_read_b64_tr_b16 v[142:143], v28 offset:1856\n" \
  "v_mfma_f32_16x16x32_bf16 v[160:163], v[96:99], v[144:147], v[160:163]\n" \
  "v_cvt_pk_bf16_f32 v206, v180, v181\n" \
  "v_cvt_pk_bf16_f32 v207, v182, v183\n" \
  "v_mfma_f32_16x16x32_bf16 v[164:167], v[120:123], v[144:147], v[164:167]\n" \
  "s_add_u32 s73, s70, 65536\n" \
  "s_waitcnt vmcnt(9)\n" \
  "v_cmp_ne_u32 vcc, 1, v242\n" \
  "v_readlane_b32 s79, v224, 4\n" \
  "s_waitcnt lgkmcnt(8)\n" \
  "v_mfma_f32_16x16x32_bf16 v[152:155], v[52:55], v[148:151], v[152:155]\n" \
  "ds_read_b64_tr_b16 v[144:145], v28 offset:14400\n" \
  "v_mfma_f32_16x16x32_bf16 v[156:159], v[76:79], v[148:151], v[156:159]\n" \
  "ds_read_b64_tr_b16 v[146:147], v28 offset:16192\n" \
  "v_mfma_f32_16x16x32_bf16 v[160:163], v[100:103], v[148:151], v[160:163]\n" \
  "buffer_load_dwordx4 v15, s[40:43], s75 offen offset:3072 lds\n" \
  "v_mfma_f32_16x16x32_bf16 v[164:167], v[124:127], v[148:151], v[164:167]\n" \
  "buffer_store_dwordx4 v[204:207], v22, s[36:39], s73 offen sc1\n" \
  "s_cbranch_vccnz LUP4_%=\n" \
  "LUK4_%=:\n" \
  "s_waitcnt lgkmcnt(8)\n" \
  "v_mfma_f32_16x16x32_bf16 v[168:171], v[32:35], v[128:131], 0\n" \
  "ds_read_b64_tr_b16 v[148:149], v28 offset:28736\n" \
  "v_mfma_f32_16x16x32_bf16 v[172:175], v[56:59], v[128:131], 0\n" \
  "ds_read_b64_tr_b16 v[150:151], v28 offset:30528\n" \
  "buffer_load_dwordx4 a[160:163], v1, s[36:39], s79 offen sc1\n" \
  "v_readlane_b32 s80, v224, 5\n" \
  "v_mfma_f32_16x16x32_bf16 v[176:179], v[80:83], v[128:131], 0\n" \
  "v_cvt_pk_bf16_f32 v200, v152, v153\n" \
  "v_cvt_pk_bf16_f32 v201, v154, v155\n" \
  "s_waitcnt vmcnt(14)\n" \
  "s_barrier\n" \
  "s_mov_b32 s78, s88\n" \
  "buffer_atomic_add v0, v25, s[36:39], s78 offen sc0 sc1\n" \
  "s_add_u32 s88, s88, 1536\n" \
  "s_and_b32 s88, s88, 0x3fffffff\n" \
  "v_mfma_f32_16x16x32_bf16 v[180:183], v[104:107], v[128:131], 0\n" \
  "s_add_u32 m0, s76, 8192\n" \
  "s_nop 0\n" \
  "buffer_load_dwordx4 v16, s[40:43], s75 offen offset:0 lds\n" \
  "s_waitcnt lgkmcnt(8)\n" \
  "v_mfma_f32_16x16x32_bf16 v[168:171], v[36:39], v[132:135], v[168:171]\n" \
  "ds_read_b64_tr_b16 v[128:129], v7 offset:128\n" \
  "v_mfma_f32_16x16x32_bf16 v[172:175], v[60:63], v[132:135], v[172:175]\n" \
  "ds_read_b64_tr_b16 v[130:131], v7 offset:1920\n" \
  "buffer_load_dwordx4 a[168:171], v2, s[36:39], s79 offen sc1\n" \
  "v_mfma_f32_16x16x32_bf16 v[176:179], v[84:87], v[132:135], v[176:179]\n" \
  "v_cvt_pk_bf16_f32 v202, v156, v157\n" \
  "v_cvt_pk_bf16_f32 v203, v158, v159\n" \
  "v_mfma_f32_16x16x32_bf16 v[180:183], v[108:111], v[132:135], v[180:183]\n" \
  "s_add_u32 s73, s69, 131072\n" \
  "s_waitcnt lgkmcnt(8)\n" \
  "v_mfma_f32_16x16x32_bf16 v[168:171], v[40:43], v[136:139], v[168:171]\n" \
  "ds_read_b64_tr_b16 v[132:133], v7 offset:14464\n" \
  "v_mfma_f32_16x16x32_bf16 v[172:175], v[64:67], v[136:139], v[172:175]\n" \
  "ds_read_b64_tr_b16 v[134:135], v7 offset:16256\n" \
  "buffer_load_dwordx4 a[176:179], v3, s[36:39], s79 offen sc1\n" \
  "v_mfma_f32_16x16x32_bf16 v[176:179], v[88:91], v[136:139], v[176:179]\n" \
  "buffer_load_dwordx4 v17, s[40:43], s75 offen offset:1024 lds\n" \
  "v_mfma_f32_16x16x32_bf16 v[180:183], v[112:115], v[136:139], v[180:183]\n" \
  "buffer_store_dwordx4 v[200:203], v22, s[36:39], s73 offen sc1\n" \
  "s_waitcnt lgkmcnt(8)\n" \
  "v_mfma_f32_16x16x32_bf16 v[168:171], v[44:47], v[140:143], v[168:171]\n" \
  "ds_read_b64_tr_b16 v[136:137], v7 offset:28800\n" \
  "v_mfma_f32_16x16x32_bf16 v[172:175], v[68:71], v[140:143], v[172:175]\n" \
  "ds_read_b64_tr_b16 v[138:139], v7 offset:30592\n" \
  "buffer_load_dwordx4 a[184:187], v4, s[36:39], s79 offen sc1\n" \
  "v_mfma_f32_16x16x32_bf16 v[176:179], v[92:95], v[140:143], v[176:179]\n" \
  "v_cvt_pk_bf16_f32 v204, v160, v161\n" \
  "v_cvt_pk_bf16_f32 v205, v162, v163\n" \
  "v_mfma_f32_16x16x32_bf16 v[180:183], v[116:119], v[140:143], v[180:183]\n" \
  "buffer_load_dwordx4 v18, s[40:43], s75 offen offset:2048 lds\n" \
  "s_waitcnt lgkmcnt(8)\n" \
  "v_mfma_f32_16x16x32_bf16 v[168:171], v[48:51], v[144:147], v[168:171]\n" \
  "ds_read_b64_tr_b16 v[140:141], v27 offset:128\n" \
  "v_mfma_f32_16x16x32_bf16 v[172:175], v[72:75], v[144:147], v[172:175]\n" \
  "ds_read_b64_tr_b16 v[142:143], v27 offset:1920\n" \
  "buffer_load_dwordx4 a[192:195], v5, s[36:39], s79 offen sc1\n" \
  "v_mfma_f32_16x16x32_bf16 v[176:179], v[96:99], v[144:147], v[176:179]\n" \
  "v_cvt_pk_bf16_f32 v206, v164, v165\n" \
  "v_cvt_pk_bf16_f32 v207, v166, v167\n" \
  "v_mfma_f32_16x16x32_bf16 v[180:183], v[120:123], v[144:147], v[180:183]\n" \
  "s_add_u32 s73, s70, 131072\n" \
  "s_waitcnt lgkmcnt(8)\n" \
  "v_mfma_f32_16x16x32_bf16 v[168:171], v[52:55], v[148:151], v[168:171]\n" \
  "ds_read_b64_tr_b16 v[144:145], v27 offset:14464\n" \
  "v_mfma_f32_16x16x32_bf16 v[172:175], v[76:79], v[148:151], v[172:175]\n" \
  "ds_read_b64_tr_b16 v[146:147], v27 offset:16256\n" \
  "buffer_load_dwordx4 a[164:167], v1, s[36:39], s80 offen sc1\n" \
  "v_readlane_b32 s79, v224, 14\n" \
  "v_mfma_f32_16x16x32_bf16 v[176:179], v[100:103], v[148:151], v[176:179]\n" \
  "buffer_load_dwordx4 v19, s[40:43], s75 offen offset:3072 lds\n" \
  "s_add_u32 s75, s75, 786432\n" \
  "v_mfma_f32_16x16x32_bf16 v[180:183], v[124:127], v[148:151], v[180:183]\n" \
  "buffer_store_dwordx4 v[204:207], v22, s[36:39], s73 offen sc1\n" \
  "s_waitcnt lgkmcnt(8)\n" \
  "v_mfma_f32_16x16x32_bf16 v[152:155], v[32:35], v[128:131], 0\n" \
  "ds_read_b64_tr_b16 v[148:149], v27 offset:28800\n" \
  "v_mfma_f32_16x16x32_bf16 v[156:159], v[56:59], v[128:131], 0\n" \
  "ds_read_b64_tr_b16 v[150:151], v27 offset:30592\n" \
  "buffer_load_dwordx4 a[172:175], v2, s[36:39], s80 offen sc1\n" \
  "v_mfma_f32_16x16x32_bf16 v[160:163], v[80:83], v[128:131], 0\n" \
  "v_cvt_pk_bf16_f32 v200, v168, v169\n" \
  "v_cvt_pk_bf16_f32 v201, v170, v171\n" \
  "v_mfma_f32_16x16x32_bf16 v[164:167], v[104:107], v[128:131], 0\n" \
  "v_cvt_pk_bf16_f32 v202, v172, v173\n" \
  "v_cvt_pk_bf16_f32 v203, v174, v175\n" \
  "s_waitcnt lgkmcnt(8)\n" \
  "v_mfma_f32_16x16x32_bf16 v[152:155], v[36:39], v[132:135], v[152:155]\n" \
  "ds_read_b64_tr_b16 v[128:129], v26 offset:128\n" \
  "v_mfma_f32_16x16x32_bf16 v[156:159], v[60:63], v[132:135], v[156:159]\n" \
  "ds_read_b64_tr_b16 v[130:131], v26 offset:1920\n" \
  "buffer_load_dwordx4 a[180:183], v3, s[36:39], s80 offen sc1\n" \
  "v_mfma_f32_16x16x32_bf16 v[160:163], v[84:87], v[132:135], v[160:163]\n" \
  "s_add_u32 s73, s69, 196608\n" \
  "v_mfma_f32_16x16x32_bf16 v[164:167], v[108:111], v[132:135], v[164:167]\n" \
  "buffer_store_dwordx4 v[200:203], v22, s[36:39], s73 offen sc1\n" \
  "s_waitcnt lgkmcnt(8)\n" \
  "v_mfma_f32_16x16x32_bf16 v[152:155], v[40:43], v[136:139], v[152:155]\n" \
  "ds_read_b64_tr_b16 v[132:133], v26 offset:14464\n" \
  "v_mfma_f32_16x16x32_bf16 v[156:159], v[64:67], v[136:139], v[156:159]\n" \
  "ds_read_b64_tr_b16 v[134:135], v26 offset:16256\n" \
  "buffer_load_dwordx4 a[188:191], v4, s[36:39], s80 offen sc1\n" \
  "v_mfma_f32_16x16x32_bf16 v[160:163], v[88:91], v[136:139], v[160:163]\n" \
  "v_cvt_pk_bf16_f32 v204, v176, v177\n" \
  "v_cvt_pk_bf16_f32 v205, v178, v179\n" \
  "v_mfma_f32_16x16x32_bf16 v[164:167], v[112:115], v[136:139], v[164:167]\n" \
  "v_cvt_pk_bf16_f32 v206, v180, v181\n" \
  "v_cvt_pk_bf16_f32 v207, v182, v183\n" \
  "s_waitcnt lgkmcnt(8)\n" \
  "v_mfma_f32_16x16x32_bf16 v[152:155], v[44:47], v[140:143], v[152:155]\n" \
  "ds_read_b64_tr_b16 v[136:137], v26 offset:28800\n" \
  "v_mfma_f32_16x16x32_bf16 v[156:159], v[68:71], v[140:143], v[156:159]\n" \
  "ds_read_b64_tr_b16 v[138:139], v26 offset:30592\n" \
  "buffer_load_dwordx4 a[196:199], v5, s[36:39], s80 offen sc1\n" \
  "v_mfma_f32_16x16x32_bf16 v[160:163], v[92:95], v[140:143], v[160:163]\n" \
  "s_add_u32 s73, s70, 196608\n" \
  "v_mfma_f32_16x16x32_bf16 v[164:167], v[116:119], v[140:143], v[164:167]\n" \
  "buffer_store_dwordx4 v[204:207], v22, s[36:39], s73 offen sc1\n" \
  "s_waitcnt lgkmcnt(8)\n" \
  "v_mfma_f32_16x16x32_bf16 v[152:155], v[48:51], v[144:147], v[152:155]\n" \
  "ds_read_b64_tr_b16 v[140:141], v28 offset:128\n" \
  "v_mfma_f32_16x16x32_bf16 v[156:159], v[72:75], v[144:147], v[156:159]\n" \
  "ds_read_b64_tr_b16 v[142:143], v28 offset:1920\n" \
  "buffer_load_dwordx4 a[200:203], v1, s[36:39], s79 offen sc1\n" \
  "v_readlane_b32 s80, v224, 15\n" \
  "v_mfma_f32_16x16x32_bf16 v[160:163], v[96:99], v[144:147], v[160:163]\n" \
  "s_getpc_b64 s[82:83]\n" \
  "LCF9_%=:\n" \
  "s_add_u32 s82, s82, LUN5_%=-LCF9_%=\n" \
  "s_addc_u32 s83, s83, 0\n" \
  "v_mbcnt_lo_u32_b32 v240, -1, 0\n" \
  "v_mbcnt_hi_u32_b32 v240, -1, v240\n" \
  "v_lshlrev_b32 v240, 6, v240\n" \
  "global_load_dword v241, v240, s[82:83]\n" \
  "v_add_u32 v240, 4096, v240\n" \
  "global_load_dword v241, v240, s[82:83]\n" \
  "v_mfma_f32_16x16x32_bf16 v[164:167], v[120:123], v[144:147], v[164:167]\n" \
  "s_getpc_b64 s[82:83]\n" \
  "LCF10_%=:\n" \
  "s_add_u32 s82, s82, LXIT_%=-4096-LCF10_%=\n" \
  "s_addc_u32 s83, s83, 0\n" \
  "v_mbcnt_lo_u32_b32 v240, -1, 0\n" \
  "v_mbcnt_hi_u32_b32 v240, -1, v240\n" \
  "v_lshlrev_b32 v240, 6, v240\n" \
  "global_load_dword v241, v240, s[82:83]\n" \
  "s_waitcnt lgkmcnt(8)\n" \
  "v_mfma_f32_16x16x32_bf16 v[152:155], v[52:55], v[148:151], v[152:155]\n" \
  "ds_read_b64_tr_b16 v[144:145], v28 offset:14464\n" \
  "v_mfma_f32_16x16x32_bf16 v[156:159], v[76:79], v[148:151], v[156:159]\n" \
  "ds_read_b64_tr_b16 v[146:147], v28 offset:16256\n" \
  "buffer_load_dwordx4 a[208:211], v2, s[36:39], s79 offen sc1\n" \
  "v_mfma_f32_16x16x32_bf16 v[160:163], v[100:103], v[148:151], v[160:163]\n" \
  "v_mfma_f32_16x16x32_bf16 v[164:167], v[124:127], v[148:151], v[164:167]\n" \
  "s_waitcnt lgkmcnt(8)\n" \
  "v_mfma_f32_16x16x32_bf16 v[168:171], v[32:35], v[128:131], 0\n" \
  "ds_read_b64_tr_b16 v[148:149], v28 offset:28800\n" \
  "v_mfma_f32_16x16x32_bf16 v[172:175], v[56:59], v[128:131], 0\n" \
  "ds_read_b64_tr_b16 v[150:151], v28 offset:30592\n" \
  "buffer_load_dwordx4 a[216:219], v3, s[36:39], s79 offen sc1\n" \
  "v_mfma_f32_16x16x32_bf16 v[176:179], v[80:83], v[128:131], 0\n" \
  "v_cvt_pk_bf16_f32 v200, v152, v153\n" \
  "v_cvt_pk_bf16_f32 v201, v154, v155\n" \
  "v_mfma_f32_16x16x32_bf16 v[180:183], v[104:107], v[128:131], 0\n" \
  "v_cvt_pk_bf16_f32 v202, v156, v157\n" \
  "v_cvt_pk_bf16_f32 v203, v158, v159\n" \
  "s_waitcnt lgkmcnt(8)\n" \
  "v_mfma_f32_16x16x32_bf16 v[168:171], v[36:39], v[132:135], v[168:171]\n" \
  "ds_read_b64_tr_b16 v[128:129], v7 offset:192\n" \
  "v_mfma_f32_16x16x32_bf16 v[172:175], v[60:63], v[132:135], v[172:175]\n" \
  "ds_read_b64_tr_b16 v[130:131], v7 offset:1984\n" \
  "buffer_load_dwordx4 a[224:227], v4, s[36:39], s79 offen sc1\n" \
  "v_mfma_f32_16x16x32_bf16 v[176:179], v[84:87], v[132:135], v[176:179]\n" \
  "s_add_u32 s73, s69, 262144\n" \
  "v_mfma_f32_16x16x32_bf16 v[180:183], v[108:111], v[132:135], v[180:183]\n" \
  "buffer_store_dwordx4 v[200:203], v22, s[36:39], s73 offen sc1\n" \
  "s_waitcnt lgkmcnt(8)\n" \
  "v_mfma_f32_16x16x32_bf16 v[168:171], v[40:43], v[136:139], v[168:171]\n" \
  "ds_read_b64_tr_b16 v[132:133], v7 offset:14528\n" \
  "v_mfma_f32_16x16x32_bf16 v[172:175], v[64:67], v[136:139], v[172:175]\n" \
  "ds_read_b64_tr_b16 v[134:135], v7 offset:16320\n" \
  "buffer_load_dwordx4 a[232:235], v5, s[36:39], s79 offen sc1\n" \
  "v_mfma_f32_16x16x32_bf16 v[176:179], v[88:91], v[136:139], v[176:179]\n" \
  "v_cvt_pk_bf16_f32 v204, v160, v161\n" \
  "v_cvt_pk_bf16_f32 v205, v162, v163\n" \
  "v_mfma_f32_16x16x32_bf16 v[180:183], v[112:115], v[136:139], v[180:183]\n" \
  "v_cvt_pk_bf16_f32 v206, v164, v165\n" \
  "v_cvt_pk_bf16_f32 v207, v166, v167\n" \
  "s_waitcnt lgkmcnt(8)\n" \
  "v_mfma_f32_16x16x32_bf16 v[168:171], v[44:47], v[140:143], v[168:171]\n" \
  "ds_read_b64_tr_b16 v[136:137], v7 offset:28864\n" \
  "v_mfma_f32_16x16x32_bf16 v[172:175], v[68:71], v[140:143], v[172:175]\n" \
  "ds_read_b64_tr_b16 v[138:139], v7 offset:30656\n" \
  "buffer_load_dwordx4 a[204:207], v1, s[36:39], s80 offen sc1\n" \
  "v_mfma_f32_16x16x32_bf16 v[176:179], v[92:95], v[140:143], v[176:179]\n" \
  "s_add_u32 s73, s70, 262144\n" \
  "v_mfma_f32_16x16x32_bf16 v[180:183], v[116:119], v[140:143], v[180:183]\n" \
  "buffer_store_dwordx4 v[204:207], v22, s[36:39], s73 offen sc1\n" \
  "s_waitcnt lgkmcnt(8)\n" \
  "v_mfma_f32_16x16x32_bf16 v[168:171], v[48:51], v[144:147], v[168:171]\n" \
  "ds_read_b64_tr_b16 v[140:141], v27 offset:192\n" \
  "v_mfma_f32_16x16x32_bf16 v[172:175], v[72:75], v[144:147], v[172:175]\n" \
  "ds_read_b64_tr_b16 v[142:143], v27 offset:1984\n" \
  "buffer_load_dwordx4 a[212:215], v2, s[36:39], s80 offen sc1\n" \
  "v_mfma_f32_16x16x32_bf16 v[176:179], v[96:99], v[144:147], v[176:179]\n" \
  "v_mfma_f32_16x16x32_bf16 v[180:183], v[120:123], v[144:147], v[180:183]\n" \
  "s_waitcnt lgkmcnt(8)\n" \
  "v_mfma_f32_16x16x32_bf16 v[168:171], v[52:55], v[148:151], v[168:171]\n" \
  "ds_read_b64_tr_b16 v[144:145], v27 offset:14528\n" \
  "v_mfma_f32_16x16x32_bf16 v[172:175], v[76:79], v[148:151], v[172:175]\n" \
  "ds_read_b64_tr_b16 v[146:147], v27 offset:16320\n" \
  "buffer_load_dwordx4 a[220:223], v3, s[36:39], s80 offen sc1\n" \
  "v_mfma_f32_16x16x32_bf16 v[176:179], v[100:103], v[148:151], v[176:179]\n" \
  "v_mfma_f32_16x16x32_bf16 v[180:183], v[124:127], v[148:151], v[180:183]\n" \
  "s_waitcnt vmcnt(20)\n" \
  "s_barrier\n" \
  "v_readfirstlane_b32 s74, v0\n" \
  "v_mov_b32 v0, 1\n" \
  "s_cmp_eq_u32 s74, 11\n" \
  "s_cselect_b32 s73, s78, 0x40000000\n" \
  "buffer_store_dword v0, v31, s[36:39], s73 offen sc1\n" \
  "s_waitcnt lgkmcnt(8)\n" \
  "v_mfma_f32_16x16x32_bf16 v[184:187], v[32:35], v[128:131], 0\n" \
  "ds_read_b128 v[32:35], v20 offset:0\n" \
  "ds_read_b64_tr_b16 v[148:149], v27 offset:28864\n" \
  "v_mfma_f32_16x16x32_bf16 v[188:191], v[56:59], v[128:131], 0\n" \
  "ds_read_b128 v[56:59], v20 offset:768\n" \
  "ds_read_b64_tr_b16 v[150:151], v27 offset:30656\n" \
  "buffer_load_dwordx4 a[228:231], v4, s[36:39], s80 offen sc1\n" \
  "v_mfma_f32_16x16x32_bf16 v[192:195], v[80:83], v[128:131], 0\n" \
  "ds_read_b128 v[80:83], v20 offset:12288\n" \
  "v_cvt_pk_bf16_f32 v200, v168, v169\n" \
  "v_cvt_pk_bf16_f32 v201, v170, v171\n" \
  "v_mfma_f32_16x16x32_bf16 v[196:199], v[104:107], v[128:131], 0\n" \
  "ds_read_b128 v[104:107], v20 offset:13056\n" \
  "v_cvt_pk_bf16_f32 v202, v172, v173\n" \
  "v_cvt_pk_bf16_f32 v203, v174, v175\n" \
  "s_waitcnt lgkmcnt(12)\n" \
  "v_mfma_f32_16x16x32_bf16 v[184:187], v[36:39], v[132:135], v[184:187]\n" \
  "ds_read_b128 v[36:39], v21 offset:0\n" \
  "ds_read_b64_tr_b16 v[128:129], v7 offset:0\n" \
  "v_mfma_f32_16x16x32_bf16 v[188:191], v[60:63], v[132:135], v[188:191]\n" \
  "ds_read_b128 v[60:63], v21 offset:768\n" \
  "s_waitcnt lgkmcnt(8)\n" \
  "ds_read_b64_tr_b16 v[130:131], v7 offset:1792\n" \
  "buffer_load_dwordx4 a[236:239], v5, s[36:39], s80 offen sc1\n" \
  "v_mfma_f32_16x16x32_bf16 v[192:195], v[84:87], v[132:135], v[192:195]\n" \
  "ds_read_b128 v[84:87], v21 offset:12288\n" \
  "s_add_u32 s73, s69, 327680\n" \
  "v_mfma_f32_16x16x32_bf16 v[196:199], v[108:111], v[132:135], v[196:199]\n" \
  "ds_read_b128 v[108:111], v21 offset:13056\n" \
  "buffer_store_dwordx4 v[200:203], v22, s[36:39], s73 offen sc1\n" \
  "v_mfma_f32_16x16x32_bf16 v[184:187], v[40:43], v[136:139], v[184:187]\n" \
  "ds_read_b128 v[40:43], v20 offset:128\n" \
  "ds_read_b64_tr_b16 v[132:133], v7 offset:14336\n" \
  "v_mfma_f32_16x16x32_bf16 v[188:191], v[64:67], v[136:139], v[188:191]\n" \
  "ds_read_b128 v[64:67], v20 offset:896\n" \
  "ds_read_b64_tr_b16 v[134:135], v7 offset:16128\n" \
  "v_mfma_f32_16x16x32_bf16 v[192:195], v[88:91], v[136:139], v[192:195]\n" \
  "s_waitcnt lgkmcnt(8)\n" \
  "ds_read_b128 v[88:91], v20 offset:12416\n" \
  "v_cvt_pk_bf16_f32 v204, v176, v177\n" \
  "v_cvt_pk_bf16_f32 v205, v178, v179\n" \
  "v_mfma_f32_16x16x32_bf16 v[196:199], v[112:115], v[136:139], v[196:199]\n" \
  "ds_read_b128 v[112:115], v20 offset:13184\n" \
  "v_cvt_pk_bf16_f32 v206, v180, v181\n" \
  "v_cvt_pk_bf16_f32 v207, v182, v183\n" \
  "v_mfma_f32_16x16x32_bf16 v[184:187], v[44:47], v[140:143], v[184:187]\n" \
  "ds_read_b128 v[44:47], v21 offset:128\n" \
  "ds_read_b64_tr_b16 v[136:137], v7 offset:28672\n" \
  "v_mfma_f32_16x16x32_bf16 v[188:191], v[68:71], v[140:143], v[188:191]\n" \
  "ds_read_b128 v[68:71], v21 offset:896\n" \
  "ds_read_b64_tr_b16 v[138:139], v7 offset:30464\n" \
  "v_mfma_f32_16x16x32_bf16 v[192:195], v[92:95], v[140:143], v[192:195]\n" \
  "ds_read_b128 v[92:95], v21 offset:12416\n" \
  "s_add_u32 s73, s70, 327680\n" \
  "v_mfma_f32_16x16x32_bf16 v[196:199], v[116:119], v[140:143], v[196:199]\n" \
  "s_waitcnt lgkmcnt(8)\n" \
  "ds_read_b128 v[116:119], v21 offset:13184\n" \
  "buffer_store_dwordx4 v[204:207], v22, s[36:39], s73 offen sc1\n" \
  "v_mfma_f32_16x16x32_bf16 v[184:187], v[48:51], v[144:147], v[184:187]\n" \
  "ds_read_b128 v[48:51], v20 offset:256\n" \
  "ds_read_b64_tr_b16 v[140:141], v27 offset:0\n" \
  "v_mfma_f32_16x16x32_bf16 v[188:191], v[72:75], v[144:147], v[188:191]\n" \
  "ds_read_b128 v[72:75], v20 offset:1024\n" \
  "ds_read_b64_tr_b16 v[142:143], v27 offset:1792\n" \
  "v_mfma_f32_16x16x32_bf16 v[192:195], v[96:99], v[144:147], v[192:195]\n" \
  "ds_read_b128 v[96:99], v20 offset:12544\n" \
  "s_cmp_lg_u32 s78, 0\n" \
  "v_mfma_f32_16x16x32_bf16 v[196:199], v[120:123], v[144:147], v[196:199]\n" \
  "ds_read_b128 v[120:123], v20 offset:13312\n" \
  "v_mfma_f32_16x16x32_bf16 v[184:187], v[52:55], v[148:151], v[184:187]\n" \
  "s_waitcnt lgkmcnt(8)\n" \
  "ds_read_b128 v[52:55], v21 offset:256\n" \
  "ds_read_b64_tr_b16 v[144:145], v27 offset:14336\n" \
  "v_mfma_f32_16x16x32_bf16 v[188:191], v[76:79], v[148:151], v[188:191]\n" \
  "ds_read_b128 v[76:79], v21 offset:1024\n" \
  "ds_read_b64_tr_b16 v[146:147], v27 offset:16128\n" \
  "v_mfma_f32_16x16x32_bf16 v[192:195], v[100:103], v[148:151], v[192:195]\n" \
  "ds_read_b128 v[100:103], v21 offset:12544\n" \
  "v_mfma_f32_16x16x32_bf16 v[196:199], v[124:127], v[148:151], v[196:199]\n" \
  "ds_read_b128 v[124:127], v21 offset:13312\n" \
  "LUN5_%=:\n" \
  "s_nop 7\n" \
  "v_cvt_pk_bf16_f32 v200, v184, v185\n" \
  "v_cvt_pk_bf16_f32 v201, v186, v187\n" \
  "v_cvt_pk_bf16_f32 v202, v188, v189\n" \
  "v_cvt_pk_bf16_f32 v203, v190, v191\n" \
  "s_add_u32 s73, s71, 393216\n" \
  "buffer_store_dwordx4 v[200:203], v22, s[36:39], s73 offen sc1\n" \
  "v_cvt_pk_bf16_f32 v204, v192, v193\n" \
  "v_cvt_pk_bf16_f32 v205, v194, v195\n" \
  "v_cvt_pk_bf16_f32 v206, v196, v197\n" \
  "v_cvt_pk_bf16_f32 v207, v198, v199\n" \
  "s_add_u32 s73, s72, 393216\n" \
  "buffer_store_dwordx4 v[204:207], v22, s[36:39], s73 offen sc1\n" \
  "s_waitcnt vmcnt(0) lgkmcnt(0)\n" \
  "s_barrier\n" \
  "s_add_u32 s78, s78, 1536\n" \
  "buffer_atomic_add v0, v25, s[36:39], s78 offen sc0 sc1\n" \
  "s_waitcnt vmcnt(0)\n" \
  "v_readfirstlane_b32 s74, v0\n" \
  "v_mov_b32 v0, 1\n" \
  "s_cmp_eq_u32 s74, 11\n" \
  "s_cselect_b32 s73, s78, 0x40000000\n" \
  "buffer_store_dword v0, v31, s[36:39], s73 offen sc1\n" \
  "s_branch LUE_%=\n" \
  "LUP2_%=:\n" \
  "buffer_load_dword v242, v30, s[36:39], s78 offen sc1\n" \
  "s_waitcnt vmcnt(0)\n" \
  "v_cmp_ne_u32 vcc, 1, v242\n" \
  "s_nop 3\n" \
  "s_cbranch_vccz LUK2_%=\n" \
  "s_add_u32 s81, s81, 1\n" \
  "s_cmp_gt_u32 s81, 200000\n" \
  "s_cbranch_scc1 LUK2_%=\n" \
  "s_sleep 2\n" \
  "s_branch LUP2_%=\n" \
  "LUP3_%=:\n" \
  "buffer_load_dword v242, v30, s[36:39], s78 offen sc1\n" \
  "s_waitcnt vmcnt(0)\n" \
  "v_cmp_ne_u32 vcc, 1, v242\n" \
  "s_nop 3\n" \
  "s_cbranch_vccz LUK3_%=\n" \
  "s_add_u32 s81, s81, 1\n" \
  "s_cmp_gt_u32 s81, 200000\n" \
  "s_cbranch_scc1 LUK3_%=\n" \
  "s_sleep 2\n" \
  "s_branch LUP3_%=\n" \
  "LUP4_%=:\n" \
  "buffer_load_dword v242, v30, s[36:39], s78 offen sc1\n" \
  "s_waitcnt vmcnt(0)\n" \
  "v_cmp_ne_u32 vcc, 1, v242\n" \
  "s_nop 3\n" \
  "s_cbranch_vccz LUK4_%=\n" \
  "s_add_u32 s81, s81, 1\n" \
  "s_cmp_gt_u32 s81, 200000\n" \
  "s_cbranch_scc1 LUK4_%=\n" \
  "s_sleep 2\n" \
  "s_branch LUP4_%=\n" \
  "LUE_%=:\n"
#define TAKE0_CLOBBERS \
  "v2", "v3"
#define TAKE0_ASM \
  "v_add_u32 v2, 61440, v0\n" \
  "v_add_u32 v3, 61440, v1\n" \
  "s_cmp_eq_u32 s52, 3\n" \
  "s_cbranch_scc1 LTV_%=\n" \
  "ds_read_b64_tr_b16 a[0:1], v0 offset:0\n" \
  "ds_read_b64_tr_b16 a[2:3], v0 offset:1536\n" \
  "ds_read_b64_tr_b16 a[80:81], v0 offset:8\n" \
  "ds_read_b64_tr_b16 a[82:83], v0 offset:1544\n" \
  "ds_read_b64_tr_b16 a[160:161], v1 offset:0\n" \
  "ds_read_b64_tr_b16 a[162:163], v1 offset:1536\n" \
  "ds_read_b64_tr_b16 a[4:5], v0 offset:12288\n" \
  "ds_read_b64_tr_b16 a[6:7], v0 offset:13824\n" \
  "ds_read_b64_tr_b16 a[84:85], v0 offset:12296\n" \
  "ds_read_b64_tr_b16 a[86:87], v0 offset:13832\n" \
  "ds_read_b64_tr_b16 a[164:165], v1 offset:12288\n" \
  "ds_read_b64_tr_b16 a[166:167], v1 offset:13824\n" \
  "ds_read_b64_tr_b16 a[8:9], v0 offset:24576\n" \
  "ds_read_b64_tr_b16 a[10:11], v0 offset:26112\n" \
  "ds_read_b64_tr_b16 a[88:89], v0 offset:24584\n" \
  "s_waitcnt lgkmcnt(8)\n" \
  "ds_read_b64_tr_b16 a[90:91], v0 offset:26120\n" \
  "ds_read_b64_tr_b16 a[168:169], v1 offset:24576\n" \
  "ds_read_b64_tr_b16 a[170:171], v1 offset:26112\n" \
  "ds_read_b64_tr_b16 a[12:13], v0 offset:36864\n" \
  "ds_read_b64_tr_b16 a[14:15], v0 offset:38400\n" \
  "ds_read_b64_tr_b16 a[92:93], v0 offset:36872\n" \
  "ds_read_b64_tr_b16 a[94:95], v0 offset:38408\n" \
  "s_waitcnt lgkmcnt(8)\n" \
  "ds_read_b64_tr_b16 a[172:173], v1 offset:36864\n" \
  "ds_read_b64_tr_b16 a[174:175], v1 offset:38400\n" \
  "ds_read_b64_tr_b16 a[16:17], v0 offset:49152\n" \
  "ds_read_b64_tr_b16 a[18:19], v0 offset:50688\n" \
  "ds_read_b64_tr_b16 a[96:97], v0 offset:49160\n" \
  "ds_read_b64_tr_b16 a[98:99], v0 offset:50696\n" \
  "ds_read_b64_tr_b16 a[176:177], v1 offset:49152\n" \
  "s_waitcnt lgkmcnt(8)\n" \
  "ds_read_b64_tr_b16 a[178:179], v1 offset:50688\n" \
  "ds_read_b64_tr_b16 a[20:21], v2 offset:0\n" \
  "ds_read_b64_tr_b16 a[22:23], v2 offset:1536\n" \
  "ds_read_b64_tr_b16 a[100:101], v2 offset:8\n" \
  "ds_read_b64_tr_b16 a[102:103], v2 offset:1544\n" \
  "ds_read_b64_tr_b16 a[180:181], v3 offset:0\n" \
  "ds_read_b64_tr_b16 a[182:183], v3 offset:1536\n" \
  "s_waitcnt lgkmcnt(8)\n" \
  "ds_read_b64_tr_b16 a[24:25], v2 offset:12288\n" \
  "ds_read_b64_tr_b16 a[26:27], v2 offset:13824\n" \
  "ds_read_b64_tr_b16 a[104:105], v2 offset:12296\n" \
  "ds_read_b64_tr_b16 a[106:107], v2 offset:13832\n" \
  "ds_read_b64_tr_b16 a[184:185], v3 offset:12288\n" \
  "ds_read_b64_tr_b16 a[186:187], v3 offset:13824\n" \
  "ds_read_b64_tr_b16 a[28:29], v2 offset:24576\n" \
  "s_waitcnt lgkmcnt(8)\n" \
  "ds_read_b64_tr_b16 a[30:31], v2 offset:26112\n" \
  "ds_read_b64_tr_b16 a[108:109], v2 offset:24584\n" \
  "ds_read_b64_tr_b16 a[110:111], v2 offset:26120\n" \
  "ds_read_b64_tr_b16 a[188:189], v3 offset:24576\n" \
  "ds_read_b64_tr_b16 a[190:191], v3 offset:26112\n" \
  "ds_read_b64_tr_b16 a[32:33], v2 offset:36864\n" \
  "ds_read_b64_tr_b16 a[34:35], v2 offset:38400\n" \
  "s_waitcnt lgkmcnt(8)\n" \
  "ds_read_b64_tr_b16 a[112:113], v2 offset:36872\n" \
  "ds_read_b64_tr_b16 a[114:115], v2 offset:38408\n" \
  "ds_read_b64_tr_b16 a[192:193], v3 offset:36864\n" \
  "ds_read_b64_tr_b16 a[194:195], v3 offset:38400\n" \
  "ds_read_b64_tr_b16 a[36:37], v2 offset:49152\n" \
  "ds_read_b64_tr_b16 a[38:39], v2 offset:50688\n" \
  "ds_read_b64_tr_b16 a[116:117], v2 offset:49160\n" \
  "s_waitcnt lgkmcnt(8)\n" \
  "ds_read_b64_tr_b16 a[118:119], v2 offset:50696\n" \
  "ds_read_b64_tr_b16 a[196:197], v3 offset:49152\n" \
  "ds_read_b64_tr_b16 a[198:199], v3 offset:50688\n" \
  "s_branch LTE_%=\n" \
  "LTV_%=:\n" \
  "ds_read_b64_tr_b16 a[0:1], v0 offset:0\n" \
  "ds_read_b64_tr_b16 a[2:3], v0 offset:5120\n" \
  "ds_read_b64_tr_b16 a[16:17], v1 offset:0\n" \
  "ds_read_b64_tr_b16 a[24:25], v0 offset:32\n" \
  "ds_read_b64_tr_b16 a[26:27], v0 offset:5152\n" \
  "ds_read_b64_tr_b16 a[40:41], v1 offset:32\n" \
  "ds_read_b64_tr_b16 a[48:49], v0 offset:64\n" \
  "ds_read_b64_tr_b16 a[50:51], v0 offset:5184\n" \
  "ds_read_b64_tr_b16 a[64:65], v1 offset:64\n" \
  "ds_read_b64_tr_b16 a[72:73], v0 offset:96\n" \
  "ds_read_b64_tr_b16 a[74:75], v0 offset:5216\n" \
  "ds_read_b64_tr_b16 a[88:89], v1 offset:96\n" \
  "ds_read_b64_tr_b16 a[96:97], v0 offset:128\n" \
  "ds_read_b64_tr_b16 a[98:99], v0 offset:5248\n" \
  "ds_read_b64_tr_b16 a[112:113], v1 offset:128\n" \
  "s_waitcnt lgkmcnt(8)\n" \
  "ds_read_b64_tr_b16 a[120:121], v0 offset:160\n" \
  "ds_read_b64_tr_b16 a[122:123], v0 offset:5280\n" \
  "ds_read_b64_tr_b16 a[136:137], v1 offset:160\n" \
  "ds_read_b64_tr_b16 a[144:145], v0 offset:192\n" \
  "ds_read_b64_tr_b16 a[146:147], v0 offset:5312\n" \
  "ds_read_b64_tr_b16 a[160:161], v1 offset:192\n" \
  "ds_read_b64_tr_b16 a[168:169], v0 offset:224\n" \
  "s_waitcnt lgkmcnt(8)\n" \
  "ds_read_b64_tr_b16 a[170:171], v0 offset:5344\n" \
  "ds_read_b64_tr_b16 a[184:185], v1 offset:224\n" \
  "ds_read_b64_tr_b16 a[192:193], v0 offset:256\n" \
  "ds_read_b64_tr_b16 a[194:195], v0 offset:5376\n" \
  "ds_read_b64_tr_b16 a[208:209], v1 offset:256\n" \
  "ds_read_b64_tr_b16 a[216:217], v0 offset:288\n" \
  "ds_read_b64_tr_b16 a[218:219], v0 offset:5408\n" \
  "s_waitcnt lgkmcnt(8)\n" \
  "ds_read_b64_tr_b16 a[232:233], v1 offset:288\n" \
  "ds_read_b64_tr_b16 a[4:5], v2 offset:0\n" \
  "ds_read_b64_tr_b16 a[6:7], v2 offset:5120\n" \
  "ds_read_b64_tr_b16 a[18:19], v3 offset:0\n" \
  "ds_read_b64_tr_b16 a[28:29], v2 offset:32\n" \
  "ds_read_b64_tr_b16 a[30:31], v2 offset:5152\n" \
  "ds_read_b64_tr_b16 a[42:43], v3 offset:32\n" \
  "s_waitcnt lgkmcnt(8)\n" \
  "ds_read_b64_tr_b16 a[52:53], v2 offset:64\n" \
  "ds_read_b64_tr_b16 a[54:55], v2 offset:5184\n" \
  "ds_read_b64_tr_b16 a[66:67], v3 offset:64\n" \
  "ds_read_b64_tr_b16 a[76:77], v2 offset:96\n" \
  "ds_read_b64_tr_b16 a[78:79], v2 offset:5216\n" \
  "ds_read_b64_tr_b16 a[90:91], v3 offset:96\n" \
  "ds_read_b64_tr_b16 a[100:101], v2 offset:128\n" \
  "s_waitcnt lgkmcnt(8)\n" \
  "ds_read_b64_tr_b16 a[102:103], v2 offset:5248\n" \
  "ds_read_b64_tr_b16 a[114:115], v3 offset:128\n" \
  "ds_read_b64_tr_b16 a[124:125], v2 offset:160\n" \
  "ds_read_b64_tr_b16 a[126:127], v2 offset:5280\n" \
  "ds_read_b64_tr_b16 a[138:139], v3 offset:160\n" \
  "ds_read_b64_tr_b16 a[148:149], v2 offset:192\n" \
  "ds_read_b64_tr_b16 a[150:151], v2 offset:5312\n" \
  "s_waitcnt lgkmcnt(8)\n" \
  "ds_read_b64_tr_b16 a[162:163], v3 offset:192\n" \
  "ds_read_b64_tr_b16 a[172:173], v2 offset:224\n" \
  "ds_read_b64_tr_b16 a[174:175], v2 offset:5344\n" \
  "ds_read_b64_tr_b16 a[186:187], v3 offset:224\n" \
  "ds_read_b64_tr_b16 a[196:197], v2 offset:256\n" \
  "ds_read_b64_tr_b16 a[198:199], v2 offset:5376\n" \
  "ds_read_b64_tr_b16 a[210:211], v3 offset:256\n" \
  "s_waitcnt lgkmcnt(8)\n" \
  "ds_read_b64_tr_b16 a[220:221], v2 offset:288\n" \
  "ds_read_b64_tr_b16 a[222:223], v2 offset:5408\n" \
  "ds_read_b64_tr_b16 a[234:235], v3 offset:288\n" \
  "LTE_%=:\n" \
  "s_waitcnt lgkmcnt(0)\n"
#define TAKE1_CLOBBERS \
  "v2", "v3"
#define TAKE1_ASM \
  "v_add_u32 v2, 61440, v0\n" \
  "v_add_u32 v3, 61440, v1\n" \
  "s_cmp_eq_u32 s52, 3\n" \
  "s_cbranch_scc1 LTV_%=\n" \
  "ds_read_b64_tr_b16 a[40:41], v0 offset:0\n" \
  "ds_read_b64_tr_b16 a[42:43], v0 offset:1536\n" \
  "ds_read_b64_tr_b16 a[120:121], v0 offset:8\n" \
  "ds_read_b64_tr_b16 a[122:123], v0 offset:1544\n" \
  "ds_read_b64_tr_b16 a[200:201], v1 offset:0\n" \
  "ds_read_b64_tr_b16 a[202:203], v1 offset:1536\n" \
  "ds_read_b64_tr_b16 a[44:45], v0 offset:12288\n" \
  "ds_read_b64_tr_b16 a[46:47], v0 offset:13824\n" \
  "ds_read_b64_tr_b16 a[124:125], v0 offset:12296\n" \
  "ds_read_b64_tr_b16 a[126:127], v0 offset:13832\n" \
  "ds_read_b64_tr_b16 a[204:205], v1 offset:12288\n" \
  "ds_read_b64_tr_b16 a[206:207], v1 offset:13824\n" \
  "ds_read_b64_tr_b16 a[48:49], v0 offset:24576\n" \
  "ds_read_b64_tr_b16 a[50:51], v0 offset:26112\n" \
  "ds_read_b64_tr_b16 a[128:129], v0 offset:24584\n" \
  "s_waitcnt lgkmcnt(8)\n" \
  "ds_read_b64_tr_b16 a[130:131], v0 offset:26120\n" \
  "ds_read_b64_tr_b16 a[208:209], v1 offset:24576\n" \
  "ds_read_b64_tr_b16 a[210:211], v1 offset:26112\n" \
  "ds_read_b64_tr_b16 a[52:53], v0 offset:36864\n" \
  "ds_read_b64_tr_b16 a[54:55], v0 offset:38400\n" \
  "ds_read_b64_tr_b16 a[132:133], v0 offset:36872\n" \
  "ds_read_b64_tr_b16 a[134:135], v0 offset:38408\n" \
  "s_waitcnt lgkmcnt(8)\n" \
  "ds_read_b64_tr_b16 a[212:213], v1 offset:36864\n" \
  "ds_read_b64_tr_b16 a[214:215], v1 offset:38400\n" \
  "ds_read_b64_tr_b16 a[56:57], v0 offset:49152\n" \
  "ds_read_b64_tr_b16 a[58:59], v0 offset:50688\n" \
  "ds_read_b64_tr_b16 a[136:137], v0 offset:49160\n" \
  "ds_read_b64_tr_b16 a[138:139], v0 offset:50696\n" \
  "ds_read_b64_tr_b16 a[216:217], v1 offset:49152\n" \
  "s_waitcnt lgkmcnt(8)\n" \
  "ds_read_b64_tr_b16 a[218:219], v1 offset:50688\n" \
  "ds_read_b64_tr_b16 a[60:61], v2 offset:0\n" \
  "ds_read_b64_tr_b16 a[62:63], v2 offset:1536\n" \
  "ds_read_b64_tr_b16 a[140:141], v2 offset:8\n" \
  "ds_read_b64_tr_b16 a[142:143], v2 offset:1544\n" \
  "ds_read_b64_tr_b16 a[220:221], v3 offset:0\n" \
  "ds_read_b64_tr_b16 a[222:223], v3 offset:1536\n" \
  "s_waitcnt lgkmcnt(8)\n" \
  "ds_read_b64_tr_b16 a[64:65], v2 offset:12288\n" \
  "ds_read_b64_tr_b16 a[66:67], v2 offset:13824\n" \
  "ds_read_b64_tr_b16 a[144:145], v2 offset:12296\n" \
  "ds_read_b64_tr_b16 a[146:147], v2 offset:13832\n" \
  "ds_read_b64_tr_b16 a[224:225], v3 offset:12288\n" \
  "ds_read_b64_tr_b16 a[226:227], v3 offset:13824\n" \
  "ds_read_b64_tr_b16 a[68:69], v2 offset:24576\n" \
  "s_waitcnt lgkmcnt(8)\n" \
  "ds_read_b64_tr_b16 a[70:71], v2 offset:26112\n" \
  "ds_read_b64_tr_b16 a[148:149], v2 offset:24584\n" \
  "ds_read_b64_tr_b16 a[150:151], v2 offset:26120\n" \
  "ds_read_b64_tr_b16 a[228:229], v3 offset:24576\n" \
  "ds_read_b64_tr_b16 a[230:231], v3 offset:26112\n" \
  "ds_read_b64_tr_b16 a[72:73], v2 offset:36864\n" \
  "ds_read_b64_tr_b16 a[74:75], v2 offset:38400\n" \
  "s_waitcnt lgkmcnt(8)\n" \
  "ds_read_b64_tr_b16 a[152:153], v2 offset:36872\n" \
  "ds_read_b64_tr_b16 a[154:155], v2 offset:38408\n" \
  "ds_read_b64_tr_b16 a[232:233], v3 offset:36864\n" \
  "ds_read_b64_tr_b16 a[234:235], v3 offset:38400\n" \
  "ds_read_b64_tr_b16 a[76:77], v2 offset:49152\n" \
  "ds_read_b64_tr_b16 a[78:79], v2 offset:50688\n" \
  "ds_read_b64_tr_b16 a[156:157], v2 offset:49160\n" \
  "s_waitcnt lgkmcnt(8)\n" \
  "ds_read_b64_tr_b16 a[158:159], v2 offset:50696\n" \
  "ds_read_b64_tr_b16 a[236:237], v3 offset:49152\n" \
  "ds_read_b64_tr_b16 a[238:239], v3 offset:50688\n" \
  "s_branch LTE_%=\n" \
  "LTV_%=:\n" \
  "ds_read_b64_tr_b16 a[8:9], v0 offset:0\n" \
  "ds_read_b64_tr_b16 a[10:11], v0 offset:5120\n" \
  "ds_read_b64_tr_b16 a[20:21], v1 offset:0\n" \
  "ds_read_b64_tr_b16 a[32:33], v0 offset:32\n" \
  "ds_read_b64_tr_b16 a[34:35], v0 offset:5152\n" \
  "ds_read_b64_tr_b16 a[44:45], v1 offset:32\n" \
  "ds_read_b64_tr_b16 a[56:57], v0 offset:64\n" \
  "ds_read_b64_tr_b16 a[58:59], v0 offset:5184\n" \
  "ds_read_b64_tr_b16 a[68:69], v1 offset:64\n" \
  "ds_read_b64_tr_b16 a[80:81], v0 offset:96\n" \
  "ds_read_b64_tr_b16 a[82:83], v0 offset:5216\n" \
  "ds_read_b64_tr_b16 a[92:93], v1 offset:96\n" \
  "ds_read_b64_tr_b16 a[104:105], v0 offset:128\n" \
  "ds_read_b64_tr_b16 a[106:107], v0 offset:5248\n" \
  "ds_read_b64_tr_b16 a[116:117], v1 offset:128\n" \
  "s_waitcnt lgkmcnt(8)\n" \
  "ds_read_b64_tr_b16 a[128:129], v0 offset:160\n" \
  "ds_read_b64_tr_b16 a[130:131], v0 offset:5280\n" \
  "ds_read_b64_tr_b16 a[140:141], v1 offset:160\n" \
  "ds_read_b64_tr_b16 a[152:153], v0 offset:192\n" \
  "ds_read_b64_tr_b16 a[154:155], v0 offset:5312\n" \
  "ds_read_b64_tr_b16 a[164:165], v1 offset:192\n" \
  "ds_read_b64_tr_b16 a[176:177], v0 offset:224\n" \
  "s_waitcnt lgkmcnt(8)\n" \
  "ds_read_b64_tr_b16 a[178:179], v0 offset:5344\n" \
  "ds_read_b64_tr_b16 a[188:189], v1 offset:224\n" \
  "ds_read_b64_tr_b16 a[200:201], v0 offset:256\n" \
  "ds_read_b64_tr_b16 a[202:203], v0 offset:5376\n" \
  "ds_read_b64_tr_b16 a[212:213], v1 offset:256\n" \
  "ds_read_b64_tr_b16 a[224:225], v0 offset:288\n" \
  "ds_read_b64_tr_b16 a[226:227], v0 offset:5408\n" \
  "s_waitcnt lgkmcnt(8)\n" \
  "ds_read_b64_tr_b16 a[236:237], v1 offset:288\n" \
  "ds_read_b64_tr_b16 a[12:13], v2 offset:0\n" \
  "ds_read_b64_tr_b16 a[14:15], v2 offset:5120\n" \
  "ds_read_b64_tr_b16 a[22:23], v3 offset:0\n" \
  "ds_read_b64_tr_b16 a[36:37], v2 offset:32\n" \
  "ds_read_b64_tr_b16 a[38:39], v2 offset:5152\n" \
  "ds_read_b64_tr_b16 a[46:47], v3 offset:32\n" \
  "s_waitcnt lgkmcnt(8)\n" \
  "ds_read_b64_tr_b16 a[60:61], v2 offset:64\n" \
  "ds_read_b64_tr_b16 a[62:63], v2 offset:5184\n" \
  "ds_read_b64_tr_b16 a[70:71], v3 offset:64\n" \
  "ds_read_b64_tr_b16 a[84:85], v2 offset:96\n" \
  "ds_read_b64_tr_b16 a[86:87], v2 offset:5216\n" \
  "ds_read_b64_tr_b16 a[94:95], v3 offset:96\n" \
  "ds_read_b64_tr_b16 a[108:109], v2 offset:128\n" \
  "s_waitcnt lgkmcnt(8)\n" \
  "ds_read_b64_tr_b16 a[110:111], v2 offset:5248\n" \
  "ds_read_b64_tr_b16 a[118:119], v3 offset:128\n" \
  "ds_read_b64_tr_b16 a[132:133], v2 offset:160\n" \
  "ds_read_b64_tr_b16 a[134:135], v2 offset:5280\n" \
  "ds_read_b64_tr_b16 a[142:143], v3 offset:160\n" \
  "ds_read_b64_tr_b16 a[156:157], v2 offset:192\n" \
  "ds_read_b64_tr_b16 a[158:159], v2 offset:5312\n" \
  "s_waitcnt lgkmcnt(8)\n" \
  "ds_read_b64_tr_b16 a[166:167], v3 offset:192\n" \
  "ds_read_b64_tr_b16 a[180:181], v2 offset:224\n" \
  "ds_read_b64_tr_b16 a[182:183], v2 offset:5344\n" \
  "ds_read_b64_tr_b16 a[190:191], v3 offset:224\n" \
  "ds_read_b64_tr_b16 a[204:205], v2 offset:256\n" \
  "ds_read_b64_tr_b16 a[206:207], v2 offset:5376\n" \
  "ds_read_b64_tr_b16 a[214:215], v3 offset:256\n" \
  "s_waitcnt lgkmcnt(8)\n" \
  "ds_read_b64_tr_b16 a[228:229], v2 offset:288\n" \
  "ds_read_b64_tr_b16 a[230:231], v2 offset:5408\n" \
  "ds_read_b64_tr_b16 a[238:239], v3 offset:288\n" \
  "LTE_%=:\n" \
  "s_waitcnt lgkmcnt(0)\n"

__device__ __forceinline__ int fresh_lane() {
  int ln;
  asm volatile("v_mbcnt_lo_u32_b32 %0, -1, 0\n\tv_mbcnt_hi_u32_b32 %0, -1, %0" : "=v"(ln));
  __builtin_assume(ln >= 0 && ln < 64);
  return ln;
}
__device__ __forceinline__ unsigned now_ticks() { return (unsigned)__builtin_amdgcn_s_memrealtime(); }
template <int SLEEP>
__device__ __forceinline__ void wait_ge(gu32* ctrl, long word, int need) {
#pragma unroll 1
  for (int spin = 0;; ++spin) {
    const int x = (int)__hip_atomic_load(ctrl + word, RLX_AGENT);
    if (__builtin_amdgcn_ballot_w64(x < need) == 0) return;
    if (spin > SPIN_CAP || ((spin & 1023) == 1023 && rfl(__hip_atomic_load(ctrl + C_ERR, RLX_AGENT)))) { __hip_atomic_store(ctrl + C_ERR, 1u, RLX_AGENT); return; }
    __builtin_amdgcn_s_sleep(SLEEP);
  }
}
template <typename VT>
__device__ __forceinline__ void set_rsrc(VT& sr, int i, const void* ptr, unsigned bytes) {
  const unsigned long long a = uni64((unsigned long long)ptr);
  sr[4 * i] = (unsigned)a; sr[4 * i + 1] = (unsigned)(a >> 32) & 0xffffu; sr[4 * i + 2] = bytes; sr[4 * i + 3] = 0x00020000u;
}
__device__ __forceinline__ int side_a(const Params& p, int blk) {
  const unsigned w = (p.m0 & -(unsigned)(blk < 32)) | (p.m1 & -(unsigned)(blk >> 5 == 1)) | (p.m2 & -(unsigned)(blk >= 64));
  return (blk << 21) | (int)((w >> (blk & 31) & 1) << 13);
}
__device__ __forceinline__ int hand_off(const Params& p, int s) { return side_a(p, HB + s % HRING / 6) + (s % 6) * 2 * H_STEP; }
__device__ __forceinline__ int pt_off(const Params& p, int s) { return side_a(p, PB + s / 12) + (s % 12) * 2 * PT_SEG; }
__device__ __forceinline__ int z_off(const Params& p, int s) { return (side_a(p, slot_blk(s) + 1) ^ 8192) + 524288 + s / RING * 655360; }
template <typename VT> __device__ __forceinline__ void pin(VT& v) {
#pragma unroll
  for (unsigned i = 0; i < __builtin_vectorelements(VT); ++i) { unsigned r = rfl(v[i]); asm volatile("" : "+s"(r)); v[i] = r; }
}
__device__ __forceinline__ void pause(unsigned ticks) {
  const unsigned t0 = now_ticks();
#pragma unroll 1
  while (now_ticks() - t0 < ticks) __builtin_amdgcn_s_sleep(8);
}
#define OPAQUE(v) asm volatile("" : "=v"(v))
__device__ __forceinline__ const __attribute__((address_space(4))) Params* fresh_params() {
  const __attribute__((address_space(4))) Params* pk = (const __attribute__((address_space(4))) Params*)__builtin_amdgcn_kernarg_segment_ptr();
  asm volatile("" : "+s"(pk) :: "memory");
  return pk;
}

__device__ __forceinline__ v4u ld16s(rsrc_t r, int vo, int so) { return __builtin_amdgcn_raw_buffer_load_b128(r, vo, so, 16); }
__device__ __forceinline__ v2u ld8s(rsrc_t r, int vo, int so) { return __builtin_amdgcn_raw_buffer_load_b64(r, vo, so, 16); }
#define TS(cond, i)
#define W240_IN(w) "{a[0:31]}"(w[0]), "{a[32:63]}"(w[1]), "{a[64:95]}"(w[2]), "{a[96:127]}"(w[3]), "{a[128:159]}"(w[4]), "{a[160:191]}"(w[5]), \
                   "{a[192:223]}"(w[6]), "{a[224:239]}"(w##7)
#define W240_IO(m, w) m "{a[0:31]}"(w[0]), m "{a[32:63]}"(w[1]), m "{a[64:95]}"(w[2]), m "{a[96:127]}"(w[3]), m "{a[128:159]}"(w[4]), m "{a[160:191]}"(w[5]), \
                      m "{a[192:223]}"(w[6]), m "{a[224:239]}"(w##7)
constexpr int L_MISC = 122880;
#define FFN_WAIT 100
typedef unsigned v3u __attribute__((ext_vector_type(3)));
__device__ __forceinline__ Params load_params(v3u par) {
  const __attribute__((address_space(4))) Params* k = fresh_params();
  Params p;
  p.x = k->x; p.mem0 = k->mem0; p.wq = k->wq; p.wk = k->wk; p.wv = k->wv; p.wo = k->wo; p.wr = k->wr; p.wg = k->wg; p.wu = k->wu; p.wd = k->wd; p.c = k->c; p.wmod = k->wmod;
  p.out = k->out; p.ws = k->ws; p.dE = k->dE; p.dL = k->dL; p.dM = k->dM; p.dZ = k->dZ; p.dS = k->dS; p.lead = k->lead;
  p.m0 = par[0]; p.m1 = par[1]; p.m2 = par[2]; p.co = side_a(p, LB) ^ 8192;
  return p;
}
__device__ __forceinline__ int look_off(int lane) { return lmap(H_PLAN) + (lane & 3) * 4; }
__device__ __forceinline__ int sum_off(int lane) { return (lane & 3) == 3 ? EPOCH_OFF : SUM_OFF + (blockIdx.x & 7) * 128 + (lane & 3) * 8; }
__device__ __forceinline__ int u_off(int lane, int hi) { return (2 * (USLOT + lane % 20) + hi) << 21 | (lane % 10 == 1) << 13; }
__device__ __forceinline__ int flag_off(const Params& p, int h) { return side_a(p, HB) + lmap(H_FLAG + h * 128); }

#define FPACE 12
#define WARMQ 1
#define SEL_AT 61
__device__ __forceinline__ unsigned wave_max_u32(unsigned m) {
  m = max(m, (unsigned)__builtin_amdgcn_update_dpp(0, (int)m, 0xb1, 0xf, 0xf, true)); m = max(m, (unsigned)__builtin_amdgcn_update_dpp(0, (int)m, 0x4e, 0xf, 0xf, true));
  m = max(m, (unsigned)__builtin_amdgcn_update_dpp(0, (int)m, 0x124, 0xf, 0xf, true)); m = max(m, ror8u(m));
  auto a = __builtin_amdgcn_permlane16_swap(m, m, false, false); m = max(a[0], a[1]);
  auto b = __builtin_amdgcn_permlane32_swap(m, m, false, false); return max(b[0], b[1]);
}
__device__ __forceinline__ unsigned score_key(unsigned r, int row) {
  unsigned u = f2u(u2f(r) + 0.f);
  u = (u & 0x80000000u) ? ~u : (u | 0x80000000u);
  return (u & 0xffff0000u) | (unsigned)(S - 1 - row);
}
template <int N>
__device__ __forceinline__ unsigned top8(const unsigned* key, int lane) {
  unsigned mine = 0, bound = 0;
#pragma unroll 1
  for (int it = 0; it < TOPK; ++it) {
    unsigned m = 0;
#pragma unroll
    for (int i = 0; i < N; ++i) m = max(m, key[i] - bound);
    bound += wave_max_u32(m);
    if ((lane & 7) == it) mine = bound;
  }
  return mine;
}
__device__ __forceinline__ void select_rows(v3u par, int lane) {
  const Params p = load_params(par);
  gu32* ctrl = (gu32*)(p.ws + p.co);
  const rsrc_t rR = mkrsrc(p.ws + p.co + C_R * 4, S * 4);
  unsigned key[28], mine;
  wait_ge<32>(ctrl, C_G, SEL_AT);
  if (lane == 0) __hip_atomic_fetch_add((gu32*)(p.ws + EPOCH_OFF), 1u, RLX_AGENT);
#pragma unroll 1
  for (int spin = 0;; ++spin) {
    asm volatile("" ::: "memory");
    v4u rr[7];
#pragma unroll
    for (int i = 0; i < 7; ++i) rr[i] = ld16s(rR, lane * 16 + i * 1024, 0);
    __builtin_amdgcn_sched_barrier(0);
    unsigned m = 0;
#pragma unroll
    for (int i = 0; i < 28; ++i) { m = max(m, rr[i >> 2][i & 3]); key[i] = score_key(rr[i >> 2][i & 3], (i >> 2) * 256 + lane * 4 + (i & 3)); }
    if (__builtin_amdgcn_ballot_w64(m == ~0u) == 0) break;
    if (spin > SPIN_CAP / 4) { __hip_atomic_store(ctrl + C_ERR, 1u, RLX_AGENT); break; }
    __builtin_amdgcn_s_sleep(16);
  }
  mine = top8<28>(key, lane);
  __hip_atomic_store(ctrl + C_IDX + (lane >> 3) * 32 + (lane & 7), S - (mine & 0xffffu), RLX_AGENT);
  TS(lane == 0, 16)
  key[4] = lane < 8 ? mine : 0u;
#pragma unroll 1
  for (int spin = -1;; ++spin) {
    asm volatile("" ::: "memory");
    const v4u r = ld16s(rR, lane * 16 + 7 * 1024, 0);
    const bool all = __builtin_amdgcn_ballot_w64(max(max(r[0], r[1]), max(r[2], r[3])) == ~0u) == 0 || spin > SPIN_CAP;
    if (!all && spin >= 0) { __builtin_amdgcn_s_sleep(1); continue; }
    TS(lane == 0, 17)
#pragma unroll
    for (int i = 0; i < 4; ++i) key[i] = score_key(r[i], S - 256 + lane * 4 + i);
    unsigned fin = mine;
    if (spin < 0 || __builtin_amdgcn_ballot_w64(max(max(key[0], key[1]), max(key[2], key[3])) > (unsigned)__builtin_amdgcn_readlane(mine, 7))) fin = top8<5>(key, lane);
    if (!all) continue;
    __hip_atomic_store(ctrl + C_IDX + 256 + (lane >> 3) * 32 + (lane & 7), S - (fin & 0xffffu), RLX_AGENT);
    break;
  }
  TS(lane == 0, 18)
}

__device__ __forceinline__ void ffn_warm(int m, int i, int lane) {
  const auto* k = fresh_params();
  asm volatile("s_mov_b32 m0, %0\n\ts_nop 3\n\tbuffer_load_dword %1, %2, %3 offen lds" ::"s"(MAIN_LDS - 256), "v"(lane * 128), "s"(mkrsrc(m == 0 ? k->wg : m == 1 ? k->wu : k->wd, DM * FF * 2)), "s"(i * 8192) : "memory");
}
__device__ __forceinline__ void ffn_role(const Params& p, int role, int lane, int wave) {
  gu32* ctrl = (gu32*)(p.ws + p.co);
  const int g = lane >> 4, n = lane & 15, q = (lane >> 2) & 3, pp = lane & 3;
  const bool down = role >= 8;
  const int c = role - 8;
  v4u wa[20], wd[12];
  {
    const rsrc_t rg = mkrsrc(p.wg, DM * FF * 2), ru = mkrsrc(p.wu, DM * FF * 2), rd = mkrsrc(p.wd, FF * DM * 2);
    BAR();
#pragma unroll 1
    for (int i = 0; i < 10; ++i) {
      const int vo = ((wave * 160 + i * 16 + (lane >> 2)) * FF + role * 32) * 2 + (lane & 3) * 16;
      dma16(rg, vo, 0, wave * 32768 + i * 1024); dma16(ru, vo, 0, wave * 32768 + 10240 + i * 1024);
      __builtin_amdgcn_s_sleep(FPACE);
    }
#pragma unroll 1
    for (int i = 0; i < 12; ++i) { dma16(rd, down ? ((wave * 384 + i * 32 + (lane >> 1)) * DM + c * 16) * 2 + (lane & 1) * 16 : OOB, 0, wave * 32768 + 20480 + i * 1024); __builtin_amdgcn_s_sleep(FPACE); }
    VMCNT(0);
#pragma unroll
    for (int i = 0; i < 32; ++i) {
      const unsigned a = wave * 32768 + (i < 20 ? ((i / 5) & 1) * 10240 + (i % 5) * 2048 + (8 * g + q) * 64 + (i / 10) * 32 : i * 1024 + (8 * g + q) * 32) + pp * 8;
      const v2u lo = trread(a), hi = trread(a + (i < 20 ? 256 : 128));
      const v4u x = {lo[0], lo[1], hi[0], hi[1]};
      if (i < 20) wa[i] = x; else wd[i - 20] = x;
    }
  }
  const rsrc_t rT = mkrsrc(p.ws + X1_OFF, (unsigned)(WS_END - X1_OFF));
  constexpr int SO_H2 = DM * 2, SO_ACT = (int)(ACT_OFF - X1_OFF), RED = 131072, LW = RED + 24576;
  int row = -1;
  v2u res = {};
#pragma unroll 1
  for (int rnd = 0; rnd < 2; ++rnd) {
    if (wave == 0) {
      const int w = C_IDX + rnd * 256 + (role & 7) * 32 + (lane & 7);
      if (rnd) wait_ge<1>(ctrl, w, 1); else wait_ge<16>(ctrl, w, 1);
      if (lane < 8) *(LAS unsigned*)(LW + lane * 4) = __hip_atomic_load(ctrl + w, RLX_AGENT);
    }
    __syncthreads();
    const unsigned t0 = now_ticks();
    const int nr = (*(LAS int*)(LW + (n & 7) * 4) - 1) & (S - 1);
    if (__builtin_amdgcn_ballot_w64(nr != row) == 0) break;
    row = nr;
    v2u x1, h2, gb; unsigned rv;
    if (down && wave == 0) { gb = ld8s(mkrsrc(p.ws + MODB_OFF, 7680), (5 * DM + c * 16 + 4 * g) * 2, 0); x1 = ld8s(rT, (row * 2 * DM + c * 16 + 4 * g) * 2, 0); h2 = ld8s(rT, (row * 2 * DM + c * 16 + 4 * g) * 2, SO_H2); rv = __hip_atomic_load(ctrl + C_R + row, RLX_AGENT); }
    {
      v4u xb[5];
#pragma unroll
      for (int i = 0; i < 5; ++i) xb[i] = ld16s(rT, (row * 2 * DM + wave * 160 + i * 32 + 8 * g) * 2, SO_H2);
      __builtin_amdgcn_sched_barrier(0);
      f4 ac[4] = {};
#pragma unroll
      for (int i = 0; i < 20; ++i) ac[i & 3] = mfma32(wa[(i & 3) * 5 + (i >> 2)], xb[i >> 2], ac[i & 3]);
#pragma unroll
      for (int k = 0; k < 4; ++k) *(LAS f4*)(RED + wave * 4096 + k * 1024 + lane * 16) = ac[k];
      __syncthreads();
      if (wave >= 2) {
        f4 ag = {}, au = {};
#pragma unroll
        for (int w = 0; w < 4; ++w) { ag += *(LAS f4*)(RED + w * 4096 + (wave - 2) * 2048 + lane * 16); au += *(LAS f4*)(RED + w * 4096 + (wave - 2) * 2048 + 1024 + lane * 16); }
        float a[4];
#pragma unroll
        for (int r = 0; r < 4; ++r) { const float gs = rbf(ag[r]); a[r] = rbf(gs / (1.f + __expf(-gs))) * rbf(au[r]); }
        __builtin_amdgcn_raw_buffer_store_b64(v2u{unsent(pack2(a[0], a[1])), unsent(pack2(a[2], a[3]))}, rT,
                                              n < 8 ? rnd * 24576 + (role * 2 + wave - 2) * 256 + (g >> 1) * 128 + n * 16 + (g & 1) * 8 : OOB, SO_ACT, 16);
      }
    }
    if (!down) continue;
    while ((int)(now_ticks() - t0) < FFN_WAIT) __builtin_amdgcn_s_sleep(1);
    v4u ab[12];
#pragma unroll 1
    for (int spin = 0;; ++spin) {
      asm volatile("" ::: "memory");
      unsigned m = 0;
#pragma unroll
      for (int i = 0; i < 12; ++i) ab[i] = ld16s(rT, rnd * 24576 + (wave * 12 + i) * 512 + g * 128 + (n & 7) * 16, SO_ACT);
      __builtin_amdgcn_sched_barrier(0);
#pragma unroll
      for (int i = 0; i < 12; ++i) m = max(max(m, max(ab[i][0], ab[i][1])), max(ab[i][2], ab[i][3]));
      if (__builtin_amdgcn_ballot_w64(m == ~0u) == 0) break;
      if (spin > SPIN_CAP) { __hip_atomic_store(ctrl + C_ERR, 1u, RLX_AGENT); break; }
      __builtin_amdgcn_s_sleep(2);
    }
    f4 y = {};
#pragma unroll
    for (int i = 0; i < 12; ++i) y = mfma32(wd[i], ab[i], y);
    *(LAS f4*)(RED + 16384 + wave * 1024 + lane * 16) = y;
    __syncthreads();
    if (wave == 0) {
#pragma unroll
      for (int w = 1; w < 4; ++w) y += *(LAS f4*)(RED + 16384 + w * 1024 + lane * 16);
      const float rs = rbf(1.f / (1.f + __expf(-u2f(rv))));
      float o[4];
#pragma unroll
      for (int r = 0; r < 4; ++r) {
        const float hv = (r & 1) ? hi2f(h2[r >> 1]) : lo2f(h2[r >> 1]), xv = (r & 1) ? hi2f(x1[r >> 1]) : lo2f(x1[r >> 1]);
        o[r] = xv + rbf(((r & 1) ? hi2f(gb[r >> 1]) : lo2f(gb[r >> 1])) * rbf(hv + rbf(rbf(y[r]) * rs)));
      }
      res = v2u{pack2(o[0], o[1]), pack2(o[2], o[3])};
    }
    TS(role == 8 && wave == 0 && lane == 0, 14 + rnd)
  }
  if (down && wave == 0) __builtin_amdgcn_raw_buffer_store_b64(res, mkrsrc(p.out, S * DM * 2), n < 8 ? (row * DM + c * 16 + 4 * g) * 2 : OOB, 0, 16);
}

constexpr int L_LN = 123904, L_XR = 139264;
__device__ __forceinline__ void ln1_fetch(const u16* x, int tile, int lane, int wave) {
  const rsrc_t rx = mkrsrc(x, S * DM * 2);
#pragma unroll
  for (int k = 0; k < 4; ++k) dma16(rx, lane * 16, (tile * 16 + wave * 4 + k) * 1280, L_LN + (wave * 4 + k) * 1040);
  dma16(rx, (lane >> 4) * 1280 + 1024 + (lane & 15) * 16, (tile * 16 + wave * 4) * 1280, L_LN + 16640 + wave * 1024);
}
__device__ __forceinline__ void ln1_rows(unsigned char* ws, unsigned epoch, int tile, int lane, int wave) {
  const rsrc_t rh = mkrsrc(ws + HF_OFF, (unsigned)HF_BYTES), rm = mkrsrc(ws + MODG_OFF, 5120);
  const int r = wave * 8 + (lane & 7);
  v4u a[10];
#pragma unroll
  for (int i = 0; i < 10; ++i) a[i] = lds128(L_LN + (i < 8 ? r * 1040 : 16640 - 1024 + r * 256) + (lane >> 3) * 16 + i * 128);
  float s = 0.f, q = 0.f;
  const bf2 one = {(__bf16)1.f, (__bf16)1.f};
#pragma unroll
  for (int i = 0; i < 10; ++i)
#pragma unroll
    for (int k = 0; k < 4; ++k) { const unsigned w = a[i][k]; s = __builtin_amdgcn_fdot2_f32_bf16(__builtin_bit_cast(bf2, w), one, s, false); }
  s += ror8f(s); s = swapsum16(s, s); s = swapsum32(s, s);
  const float mean = s * (1.f / DM);
#pragma unroll
  for (int i = 0; i < 10; ++i)
#pragma unroll
    for (int k = 0; k < 4; ++k) { const float d0 = lo2f(a[i][k]) - mean, d1 = hi2f(a[i][k]) - mean; q = fmaf(d0, d0, fmaf(d1, d1, q)); }
  q += ror8f(q); q = swapsum16(q, q); q = swapsum32(q, q);
  const float mb = rbf(mean), isd = __builtin_amdgcn_rcpf(rbf(__builtin_amdgcn_sqrtf(rbf(rbf(q * (1.f / DM)) + 1e-5f))));
#pragma unroll
  for (int i = 0; i < 10; ++i) {
#pragma unroll
    for (int k = 0; k < 4; ++k) { const unsigned d = pack2(lo2f(a[i][k]) - mb, hi2f(a[i][k]) - mb); a[i][k] = pack2(lo2f(d) * isd, hi2f(d) * isd); }
    asm volatile("" : "+v"(a[i]));
  }
  const unsigned lm = L_LN + 21504 + wave * 2560;
#pragma unroll 1
  for (int spin = 0;; ++spin) {
    asm volatile("" ::: "memory");
    v4u m[5];
    bool ok = true;
#pragma unroll
    for (int i = 0; i < 5; ++i) m[i] = ld16s(rm, lane * 16 + i * 1024, 0);
#pragma unroll
    for (int i = 0; i < 5; ++i) { ok &= m[i][0] == epoch + 1 && m[i][2] == epoch + 1; *(LAS v2u*)(lm + i * 512 + lane * 8) = v2u{m[i][1], m[i][3]}; }
    if (__builtin_amdgcn_ballot_w64(!ok) == 0 || spin > SPIN_CAP) break;
    __builtin_amdgcn_s_sleep(8);
  }
  LGKM0();
  { const auto& p = *fresh_params(); TS(wave == 0 && lane == 0 && tile == 15, 61) }
#pragma unroll
  for (int i = 0; i < 10; ++i) {
    const v4u sh = lds128(lm + (lane >> 3) * 16 + i * 128), sc = lds128(lm + 1280 + (lane >> 3) * 16 + i * 128);
    v4u o;
#pragma unroll
    for (int k = 0; k < 4; ++k) {
      const unsigned m = pack2(lo2f(a[i][k]) * lo2f(sc[k]), hi2f(a[i][k]) * hi2f(sc[k]));
      o[k] = pack2(lo2f(m) + lo2f(sh[k]), hi2f(m) + hi2f(sh[k]));
    }
    __builtin_amdgcn_raw_buffer_store_b128(o, rh, tile * 20480 + i * 2048 + (lane >> 3) * 256 + r * 16, 0, 16);
  }
}
__device__ __forceinline__ void w_issue(rsrc_t rw, int type, int h, int m, int slot, int i0, int cnt, int lane) {
#pragma unroll 1
  for (int i = i0; i < i0 + cnt; ++i) {
    const int c = i * 64 + lane;
    dma16(rw, type < 3 ? ((m * 160 + c / 24) * HD + h * DH) * 2 + (c % 24) * 16 : (h * DH + (i < 40 ? 32 * m : 96 + 16 * m)) * DM * 2 + c * 16, 0, slot * 61440 + i * 1024);
  }
}
__device__ __forceinline__ rsrc_t w_rsrc(int type) { const auto* k = fresh_params(); return mkrsrc(type == 0 ? k->wk : type == 1 ? k->wq : type == 2 ? k->wv : k->wo, DM * HD * 2); }
__device__ __forceinline__ void put_line(unsigned char* ws, int off, v4u v, int lanes, int lane) {
  __builtin_amdgcn_raw_buffer_store_b128(v, mkrsrc(ws, WS_BYTES), lane < lanes ? off + lane * 16 : OOB, 0, 16);
}
__device__ __forceinline__ void sum_table(unsigned char* ws, unsigned old, int lane) {
  const unsigned epoch = old + 1;
  typedef GAS unsigned long long gu64;
#pragma unroll 1
  for (int spin = 0;; ++spin) {
    const unsigned long long a = __hip_atomic_load((gu64*)(ws + TAB_OFF) + lane * 16, RLX_AGENT), b = __hip_atomic_load((gu64*)(ws + TAB_OFF) + min(64 + lane, NTAB - 1) * 16, RLX_AGENT);
    if (__builtin_amdgcn_ballot_w64((unsigned)a != epoch || (unsigned)b != epoch) == 0 || spin > SPIN_CAP) {
      LAS int* v = (LAS int*)L_MISC;
      v[lane] = 0; v[128 + lane] = 0;
      LGKM0();
      v[16 + lane] = (int)(a >> 32); v[80 + lane] = (int)(b >> 32);
      LGKM0();
      int sa = 0, sb = 0;
#pragma unroll
      for (int k = -CALWIN; k < CALWIN; ++k) { sa += v[16 + lane + k]; sb += v[80 + lane + k]; }
      const unsigned long long lo = __builtin_amdgcn_ballot_w64((sa > 0) ^ (lane & 1)), hi = __builtin_amdgcn_ballot_w64((sb > 0) ^ (lane & 1));
      put_line(ws, SUM_OFF, lane & 1 ? v4u{epoch, (unsigned)hi, epoch, 0u} : v4u{epoch, (unsigned)lo, epoch, (unsigned)(lo >> 32)}, 64, lane);
      return;
    }
    __builtin_amdgcn_s_sleep(2);
  }
}
__device__ __forceinline__ v3u wait_table(unsigned char* ws, unsigned old, int lane) {
  const unsigned epoch = old + 1;
#pragma unroll 1
  for (int spin = 0;; ++spin) {
    const unsigned long long a = __hip_atomic_load((GAS unsigned long long*)(ws + SUM_OFF) + (blockIdx.x & 7) * 16 + (lane & 3), RLX_AGENT);
    if (__builtin_amdgcn_ballot_w64((unsigned)a != epoch) == 0 || spin > SPIN_CAP)
      return v3u{(unsigned)__builtin_amdgcn_readlane(a >> 32, 0), (unsigned)__builtin_amdgcn_readlane(a >> 32, 1), (unsigned)__builtin_amdgcn_readlane(a >> 32, 2)};
    __builtin_amdgcn_s_sleep(4);
  }
}
__device__ __forceinline__ void calibrate(unsigned char* ws, unsigned epoch, int blk, int lane) {
  const rsrc_t r = mkrsrc(ws, WS_BYTES);
  unsigned best[2] = {1u << 30, 1u << 30};
  const bool busy = (unsigned)(blk - 2 * USLOT) % 20u < 4u && blk < 2 * USLOT + 40;
#pragma unroll 1
  for (int rep = -1; rep < (busy ? -1 : CALREP); ++rep) {
    const int off = lane < (rep < 0 ? 16 : 8) ? (blk << 21) + 0x1FC000 + (rep < 0 ? lane >> 3 : rep & 1) * 8192 + (lane & 7) * 16 : OOB;
    const unsigned long long t1 = __builtin_amdgcn_s_memtime();
    LGKM0();
    __builtin_amdgcn_raw_buffer_store_b128(v4u{1u, 2u, 3u, 4u}, r, off, 0, 16);
    VMCNT(0);
    const unsigned d = rep < 0 ? 1u << 30 : (unsigned)(__builtin_amdgcn_s_memtime() - t1);
    if (rep & 1) best[1] = min(best[1], d); else best[0] = min(best[0], d);
  }
  const int d = min(max((int)(best[1] - best[0]), -250), 250);
  put_line(ws, TAB_OFF + blk * 128, v4u{epoch + 1, (unsigned)(blk & 1 ? -d : d), 0u, 0u}, 8, lane);
}
__device__ __forceinline__ v3u token_start(v32u (&wa)[7], v16u& wa7, int type, int h, int lane, int wave) {
  const auto* k = fresh_params();
  const unsigned epoch = __hip_atomic_load((gu32*)(k->ws + EPOCH_OFF), RLX_AGENT);
  const int duty = (0x2130 >> 4 * type) & 3;
  const int tile = duty < 3 ? h * 3 + duty : 96 + h, cb = duty * 32 + h, g = lane >> 4, q = (lane >> 2) & 3, pp = lane & 3;
  const rsrc_t rw = w_rsrc(type);
  const int cal = wave * 32 + (h & 7) * 4 + (h >> 3);
  if (duty == 3 && cal < NCAL) calibrate(k->ws, epoch, cal, lane);
  if (duty == 3 && wave == 3) {
#pragma unroll
    for (int j = 0; j < 3; ++j) {
      const int i = (j * 32 + h) * 64 + lane;
      __builtin_amdgcn_raw_buffer_store_b128(v4u{0u, 0u, 0u, 0u}, mkrsrc(k->ws, WS_BYTES),
                                             i < ZERO_BYTES / 16 ? CTRL_OFF + i * 16 : i < ZERO_BYTES / 16 + 512 ? (HB << 21) + lmap(H_FLAG) + (i & 255) * 16 + (i & 256) * 32 : OOB, 0, 16);
    }
    VMCNT(0);
    put_line(k->ws, TAB_OFF + (NCAL + h) * 128, v4u{epoch + 1, 0u, 0u, 0u}, 8, lane);
  }
  if (duty == 3) { BAR(); if (wave < 3 && WARMAT == 0) ffn_warm(wave, blockIdx.x, lane); }
  if (cb == 127 && wave == 0) sum_table(k->ws, epoch, lane);
  LAS float* red = (LAS float*)(L_LN + 20736);
  if (wave < 2) {
    const rsrc_t rm = mkrsrc(k->wmod, DM * 6 * DM * 2), rc = mkrsrc(k->c, DM * 2);
    const int rg = wave * 320 + (lane >> 2);
    v4u w[20]; unsigned cv[5];
#pragma unroll
    for (int i = 0; i < 5; ++i) cv[i] = __builtin_amdgcn_raw_buffer_load_b16(rc, (wave * 320 + i * 64 + lane) * 2, 0, 0);
#pragma unroll
    for (int i = 0; i < 20; ++i) w[i] = __builtin_amdgcn_raw_buffer_load_b128(rm, cb < 120 ? ((rg + 16 * i) * 3840 + cb * 32 + pp * 8) * 2 : OOB, 0, MODAUX);
    ln1_fetch(k->x, tile, lane, 2 * wave); ln1_fetch(k->x, tile, lane, 2 * wave + 1);
    __builtin_amdgcn_sched_barrier(0);
    LAS float* sl = (LAS float*)(L_LN + 21504 + wave * 2560);
#pragma unroll
    for (int i = 0; i < 5; ++i) { const float c = bf2f((u16)cv[i]); sl[i * 64 + lane] = rbf(c * __builtin_amdgcn_rcpf(1.f + __expf(-c))); }
    LGKM0();
    float acc[8] = {};
#pragma unroll
    for (int i = 0; i < 20; ++i) {
      const float sv = sl[16 * i + (lane >> 2)];
#pragma unroll
      for (int j = 0; j < 4; ++j) { acc[2 * j] += sv * lo2f(w[i][j]); acc[2 * j + 1] += sv * hi2f(w[i][j]); }
    }
#pragma unroll
    for (int j = 0; j < 8; ++j) {
      float v = acc[j];
      v += u2f(__builtin_amdgcn_update_dpp(0, (int)f2u(v), 0x124, 0xf, 0xf, true)); v += ror8f(v); v = swapsum16(v, v); v = swapsum32(v, v);
      if (lane < 4) red[wave * 32 + lane * 8 + j] = v;
    }
    VMCNT(0); LGKM0();
    { const auto& p = *k; TS(wave == 0 && lane == 0 && cb == 5, 60) }
  } else if (cb >= 40) { __builtin_amdgcn_s_sleep(8); w_issue(rw, type, h, wave - 2, wave - 2, 0, 30, lane); }
  BAR();
  if (wave >= 2) w_issue(rw, type, h, wave - 2, wave - 2, cb >= 40 ? 30 : 0, cb >= 40 ? 30 : 60, lane);
  else {
    if (wave == 1) {
      const int c = 2 * (lane & 15);
      float s0 = red[c] + red[32 + c], s1 = red[c + 1] + red[33 + c];
      if ((cb / 20) % 3 == 1) { s0 = 1.f + rbf(s0); s1 = 1.f + rbf(s1); }
      if (cb < 40) __builtin_amdgcn_raw_buffer_store_b64(v2u{epoch + 1, pack2(s0, s1)}, mkrsrc(k->ws + MODG_OFF, 5120), lane < 16 ? cb * 128 + lane * 8 : OOB, 0, 16);
      else __builtin_amdgcn_raw_buffer_store_b32(pack2(s0, s1), mkrsrc(k->ws + MODB_OFF, 7680), lane < 16 ? cb * 64 + lane * 4 : OOB, 0, 16);
    }
    ln1_rows(k->ws, epoch, tile, lane, wave);
  }
  const v3u par = wait_table(k->ws, epoch, lane);
  const Params p = load_params(par);
  gu32* ctrl = (gu32*)(p.ws + p.co);
  VMCNT(0);
  if (wave < 2 && lane == 0) {
    __hip_atomic_fetch_add(ctrl + C_LN + (duty < 3 ? tile % 6 : 6 + (h & 1)) * 32, 1u, RLX_AGENT);
    if (wave == 1 && cb >= 40 && cb < 120) __hip_atomic_fetch_add(ctrl + C_MOD + (cb & 1) * 32, 1u, RLX_AGENT);
  }
  TS(wave == 0 && lane == 0 && type == 0, 640 + h)
  const v2u tb = type < 3 ? v2u{(unsigned)((8 * g + q) * 384 + (32 * wave + 8 * pp) * 2), (unsigned)((8 * g + q) * 384 + (128 + 16 * wave + 4 * pp) * 2)}
                          : v2u{(unsigned)((8 * g + q) * 1280 + (160 * wave + 4 * pp) * 2), (unsigned)((32 + 4 * g + q) * 1280 + (160 * wave + 4 * pp) * 2)};
  BAR();
  asm volatile(TAKE0_ASM : W240_IO("=", wa) : "{v[0:1]}"(tb), "{s52}"(type) : "memory", "scc", TAKE0_CLOBBERS);
  BAR();
  TS(wave == 0 && lane == 0 && cb == 5, 62)
  w_issue(rw, type, h, 2 + (wave >> 1), wave >> 1, (wave & 1) * 30, 30, lane);
  VMCNT(0); BAR();
  asm volatile(TAKE1_ASM : W240_IO("+", wa) : "{v[0:1]}"(tb), "{s52}"(type) : "memory", "scc", TAKE1_CLOBBERS);
  if (wave == 2 && type < 3) wait_ge<2>(ctrl, C_LN + (lane % 6) * 32, 32);
  BAR();
  TS(wave == 0 && lane == 0, 512 + type * 32 + h)
  return par;
}
__device__ __forceinline__ void c_args(v32u& vc, v16u& sc, const Params& p, int c8, int rep, int lane, int wave) {
  constexpr int L_Z = 10240;
  const int g = lane >> 4, n = lane & 15, j0 = 4 * (g & 1) + 2 * (g >> 1), hi = c8 >> 6;
  OPAQUE(vc);
  vc[0] = n < 8 ? 0x4444 : 0xEEEE;
  vc[1] = lane * 16;
  vc[2] = L_Z + (g >> 1) * 256 + n * 16 + (g & 1) * 8;
  vc[3] = L_Z + n * 16 + j0 * 2;
  vc[4] = hand_off(p, lane) + rep * 2 * H_COPY;
  vc[5] = (c8 & 63) * 32768 + (n >> 3) * 512 + g * 128 + (n & 7) * 16;
  vc[7] = lane == 0 && (wave == 0 || wave == 3) ? p.co + (C_ANCH + 128 + rep * 32) * 4 : OOB;
  vc[8] = p.co + (C_ANCH + rep * 32 + lane % 3) * 4;
  if (wave == 3) {
    vc[9] = side_a(p, slot_blk(lane) + hi);
    vc[10] = (n >> 3) * 512 + g * 128 + (n & 7) * 16;
    vc[11] = 0;
    vc[6] = vc[10] ^ 512;
  } else {
    vc[9] = z_off(p, lane);
    vc[10] = lmap(c8 * 4096) + lane * 16;
    vc[11] = lane == 0 ? p.co + (C_Z + (c8 & 7) * 32) * 4 : OOB;
  }
  vc[12] = ((n & 7) >> 1) - 2 * (g & 1);
  vc[13] = lmap(c8 * 128) + (n & 7) * 16 + j0 * 2 + ((rep + (n >> 3)) & 3) * 2 * H_COPY;
  if (wave == 3) vc[2] = lmap(c8 * 128) + (n & 7) * 16 + j0 * 2 + ((rep + 2 + (n >> 3)) & 3) * 2 * H_COPY;
  vc[14] = CTRL_OFF + (C_U + 3 * 384 + 256 + rep * 32 + (lane & 7)) * 4;
  vc[15] = 1;
#pragma unroll
  for (int i = 0; i < 5; ++i) {
    vc[18 + i] = vc[5] + lmap((wave * 5 + i) * 1024);
    vc[23 + i] = c8 >= 0 && wave * 5 + i < 16 ? vc[18 + i] : OOB;
  }
  vc[28] = sum_off(lane);
  vc[16] = u_off(lane, c8 >= 64); vc[17] = look_off(lane);
#pragma unroll
  for (int i = 4; i < 16; ++i) sc[i] = 0;
  sc[0] = wave; sc[1] = p.dL + p.dM; sc[2] = p.dS; sc[3] = p.dZ; sc[8] = rfl(rep * 2 * H_COPY);
}

__device__ __forceinline__ void u_role(const Params& p, int h, int th, int c8, int rep, int lane, int wave) {
  const int g = lane >> 4, n = lane & 15, q = (lane >> 2) & 3, pp = lane & 3;
  v32u vc; v12u sr; v16u sc;
  OPAQUE(vc);
#pragma unroll
  for (int k = 0; k < 7; ++k) { const int c = k * 64 + lane; vc[k] = ((h * DH + c / 28) * DM + 208 * th) * 2 + ((c % 28) ^ (c / 28 >> 2 & 2)) * 16 - (k & 3) * 1024; }
  const int ch = wave >> 1;
  vc[7] = (8 * g + q) * 448 + pp * 8 + ((7 * ch) ^ (g & 1)) * 32;
  vc[26] = (8 * g + q) * 448 + pp * 8 + ((7 * ch + 1) ^ (g & 1)) * 32;
#pragma unroll
  for (int k = 0; k < 12; ++k) { const int c = k * 64 + lane, r = c / 24; vc[8 + k] = (r * HD + h * DH) * 2 + (((c % 24) ^ (r >> 2)) * 16) - (k & 3) * 1024; }
  const int row = 16 * (n >> 3) + 8 * ((n >> 1) & 1) + 4 * ((n >> 2) & 1) + (n & 1);
  vc[20] = 86016 + (wave & 1) * 24576 + row * 384 + (g ^ (row >> 2)) * 16;
  vc[21] = vc[20] ^ 64;
  vc[22] = (26 * th + 14 * ch + (n >> 3)) * 32768 + lmap(h * 512) + g * 128 + (n & 7) * 16;
  vc[23] = u_off(lane, th == 2 && ch); vc[24] = u_off(lane, th == 2);
#pragma unroll
  for (int k = 0; k < 3; ++k) { const int c = (k * 12 + th * 4 + wave) * 64 + lane; vc[27 + k] = (c / 3 * HD + h * DH) * 2 + c % 3 * 128; }
  set_rsrc(sr, 0, p.ws, WS_BYTES); set_rsrc(sr, 1, p.wv, DM * HD * 2); set_rsrc(sr, 2, p.wo, DM * HD * 2);
  vc[25] = lane == 0 && wave == 0 ? CTRL_OFF + (C_U + (h & 7) * 32) * 4 : OOB;
  vc[30] = CTRL_OFF + (C_U + 256 + rep * 32 + (lane & 7)) * 4;
  vc[31] = lane < 4 ? CTRL_OFF + (C_U + 256 + lane * 32 + (h & 7)) * 4 : OOB;
  v32u vd;
  c_args(vd, sc, p, c8, rep, lane, wave);
  sc[9] = c8; sc[10] = (th == 2 && ch) << 21; sc[11] = (th == 2) << 21;
  pin(sc); pin(sr);
  asm volatile(UG_ASM CROLE_ASM : "+{v[0:31]}"(vc), "+{v[208:239]}"(vd), "+{s[52:67]}"(sc), "+{s[36:47]}"(sr) :: "memory", "vcc", "scc", CROLE_CLOBBERS);
  gu32* ctrl = (gu32*)(fresh_params()->ws + CTRL_OFF);
  if (c8 >= 0 && fresh_lane() == 0) {
    if (sc[1] > 20000u || sc[5] > 20000u) { __hip_atomic_store(ctrl + C_ERR, 1u, RLX_AGENT); __hip_atomic_store(ctrl + C_ERR + 2048, 1u, RLX_AGENT); }
  }
}

__device__ __forceinline__ void proj_role(const v32u (&wa)[7], const v16u& wa7, const Params& p, int type, int head, int lane, int wave) {
  gu32* ctrl = (gu32*)(p.ws + p.co);
  {
    const rsrc_t rh = mkrsrc(p.ws + HF_OFF, (unsigned)HF_BYTES);
#pragma unroll
    for (int k = 0; k < 20; ++k) dma16_sc1(rh, wave * 10240 + lane * 16, (k / 10) * 40960 + (k % 10) * 1024, (k / 10) * 40960 + wave * 10240 + (k % 10) * 1024);
  }
  if (type < 2)
#pragma unroll 1
    for (int j = 0; j < 4; ++j)
      dma16(mkrsrc(p.x, S * DM * 2), lane < 40 ? lane * 16 : OOB, ((OSPLIT + (type * 32 + head) / 10 + (j >> 1) * 6) * 32 + (type * 32 + head) % 10 * 4 + wave) * 1280 + (j & 1) * 640, L_XR + (wave * 4 + j) * 1024);
  v12u vc; v8u sr;
  vc[0] = lane * 16; vc[1] = wave * 10240 + lane * 16;
  if (type < 2) {
    vc[2] = lmap(KQ_OFF + head * 24576 + type * 8192) + wave * 2048 + lane * 16;
    vc[3] = lmap(KQ_OFF + head * 24576 + 16384) + type * 4096 + wave * 1024 + lane * 16;
    vc[4] = side_a(p, slot_blk(lane) + 1);
    vc[5] = lane == 0 ? flag_off(p, head) + (type * 4 + wave) * 4 : OOB;
  } else {
    vc[2] = head * 32768 + wave * 2048 + lane * 16;
    vc[3] = head * 32768 + 16384 + wave * 1024 + lane * 16;
    vc[4] = side_a(p, slot_blk(lane)) ^ 8192;
    vc[5] = lane == 0 ? p.co + (C_VF + head * 32 + wave) * 4 : OOB;
  }
  vc[6] = 0;
  {
    const int pc = wave * 64 + type * 32 + head, k = pc % 34, o = k < 10 ? k : k + 6;
    vc[7] = type < 2 && pc < 136 ? (pc / 34) * 2 * H_COPY + lmap(o * 1024) + lane * 16 : OOB;
    vc[8] = lane < 63 ? hand_off(p, lane + 1) : OOB;
  }
  vc[9] = 0; vc[10] = 0; vc[11] = 0;
  set_rsrc(sr, 0, p.ws + HF_OFF, (unsigned)HF_BYTES); set_rsrc(sr, 1, p.ws, WS_BYTES);
#pragma unroll 1
  for (int k = 0; k < 11; ++k) {
    if (wave == 0 && k > 1) {
      if (k == 2) wait_ge<8>(ctrl, C_LN + (6 + (lane & 1)) * 32, 32);
      wait_ge<8>(ctrl, C_G + (head & 15) * 32, max(6 * k - AHEAD, 1));
      if (6 * k + 5 >= RING) wait_ge<8>(ctrl, C_AF + min(max(6 * k + lane % 6 - RING, 0), NSEG - 1 - RING) * 32, 2);
    }
    v4u sc = {6u * k, min(6u * k + 6u, 64u), (unsigned)wave * 10240u, 0u};
    v12u vt = vc;
    pin(sc);
    asm volatile(PROJ_ASM : "+{s[52:55]}"(sc), "+{v[0:11]}"(vt) : "{s[36:43]}"(sr), W240_IN(wa) : "memory", "vcc", "scc", PROJ_CLOBBERS);
    TS(wave == 0 && lane == 0 && head == 0 && (k == 4 || k == 7 || k == 10), 20 + type * 4 + (k - 4) / 3)
  }
}

__device__ __forceinline__ void first_markers(const Params& p, int i) {
  const rsrc_t r = mkrsrc(p.ws, WS_BYTES);
  const v4u m = {~0u, ~0u, ~0u, ~0u};
  const int h0 = side_a(p, HB);
#pragma unroll 1
  for (int j = i; j < 4 * 2560; j += 8192) {
    const int o = j % 2560;
    v4u val = m;
    if (o < 640) { const v4u x = *(const GAS v4u*)(p.mem0 + (o & 7) * DM + (o >> 3) * 8); val = v4u{unsent(x[0]), unsent(x[1]), unsent(x[2]), unsent(x[3])}; }
    __builtin_amdgcn_raw_buffer_store_b128(val, r, o < 640 || o >= 1024 ? h0 + lmap(j * 16) : OOB, 0, 16);
  }
  __builtin_amdgcn_raw_buffer_store_b128(m, r, i < 512 ? p.co + C_R * 4 + i * 16 : i < 512 + TOPK * FF / 4 ? ACT_OFF + (i - 512) * 16 : OOB, 0, 16);
#pragma unroll 1
  for (int j = i; j < (NSEG - OSPLIT) * 32 * 80; j += 8192) __builtin_amdgcn_raw_buffer_store_b128(m, r, X1_OFF + (OSPLIT * 32 + j / 80) * 2560 + (j % 80) * 16, 0, 16);
}
__device__ __forceinline__ void vo_role(const v32u (&wa)[7], const v16u& wa7, const Params& p, int h, int lane, int wave) {
  const int g = lane >> 4, n = lane & 15, q = (lane >> 2) & 3, pp = lane & 3;
  first_markers(p, h * 256 + wave * 64 + lane);
  v16u vc; v4u sr;
  vc[0] = lane * 16;
  vc[1] = h * 32768 + wave * 2048 + lane * 16;
  vc[2] = h * 32768 + 16384 + wave * 1024 + lane * 16;
  vc[3] = (20 * wave + (n >> 3)) * 32768 + lmap(h * 512) + g * 128 + (n & 7) * 16;
  vc[4] = side_a(p, slot_blk(lane));
  vc[5] = wave == 3 ? side_a(p, slot_blk(lane) + 1) - (1 << 21) : vc[4];
  vc[6] = vc[4] ^ 8192;
  vc[7] = lane == 0 ? flag_off(p, h) + (8 + wave) * 4 : OOB;
  vc[8] = 0;
  vc[9] = p.co + (C_VF + h * 32 + pp) * 4;
#pragma unroll
  for (int k = 10; k < 16; ++k) vc[k] = 0;
  set_rsrc(sr, 0, p.ws, WS_BYTES);
  v4u sc = {0u, 64u, (unsigned)wave, 0u};
  pin(sc); pin(sr);
  asm volatile(VO_ASM : "+{s[52:55]}"(sc), "+{v[0:15]}"(vc) : "{s[36:39]}"(sr), W240_IN(wa) : "memory", "vcc", "scc", VO_CLOBBERS);
}

typedef float f2 __attribute__((ext_vector_type(2)));
__device__ __forceinline__ f2 up2(unsigned w) { return f2{lo2f(w), hi2f(w)}; }
constexpr int L_PAR = 123904;
__device__ __forceinline__ void row_params(v3u par, int wave) {
  const Params p = load_params(par);
  const int tid = wave * 64 + fresh_lane();
  const rsrc_t rm = mkrsrc(p.ws + MODB_OFF + 2560, 5120), rr = mkrsrc(p.wr, 1280);
  wait_ge<8>((gu32*)(p.ws + p.co), C_MOD + (tid & 1) * 32, 40);
  unsigned w[5];
#pragma unroll
  for (int k = 0; k < 5; ++k) w[k] = __builtin_amdgcn_raw_buffer_load_b32(rm, (k * 256 + tid) * 4, 0, 16);
  const v2u r = __builtin_amdgcn_raw_buffer_load_b64(rr, tid * 8, 0, 0);
  BAR();
#pragma unroll
  for (int k = 0; k < 5; ++k) *(LAS f2*)(L_PAR + (k * 256 + tid) * 8) = up2(w[k]);
  *(LAS v2u*)(L_PAR + 10240 + tid * 8) = r;
  LGKM0(); BAR();
}
__device__ __forceinline__ v3u o_start(int id, int lane, int wave) {
  const auto* k = fresh_params();
  const v3u par = wait_table(k->ws, __hip_atomic_load((gu32*)(k->ws + EPOCH_OFF), RLX_AGENT), lane);
  if (id == 15) return par;
  if (wave < 2) {
    const unsigned x = __builtin_amdgcn_raw_buffer_load_b32(mkrsrc(wave ? k->out : k->x, S * DM * 2), lane * 65536 + id * 128, 0, 0);
    asm volatile("" ::"v"(x));
  }
  row_params(par, wave);
  return par;
}
__device__ __forceinline__ float sum16(float v) {
  v += u2f(__builtin_amdgcn_update_dpp(0, (int)f2u(v), 0xb1, 0xf, 0xf, true)); v += u2f(__builtin_amdgcn_update_dpp(0, (int)f2u(v), 0x4e, 0xf, 0xf, true));
  v += u2f(__builtin_amdgcn_update_dpp(0, (int)f2u(v), 0x124, 0xf, 0xf, true));
  return v + ror8f(v);
}
struct RowIn { v4u x[5], y[5]; };
__device__ __forceinline__ void rows_load(RowIn& r, int row0) {
  const auto* k = fresh_params();
  const int lane = fresh_lane(), row = row0 + (lane >> 4), co = (lane & 15) * 16;
  const rsrc_t rx = mkrsrc(k->x, S * DM * 2), rT = mkrsrc(k->ws + X1_OFF, 2 * HF_BYTES);
#pragma unroll
  for (int i = 0; i < 5; ++i) { r.y[i] = ld16s(rT, row * 2560 + co + i * 256, 0); r.x[i] = __builtin_amdgcn_raw_buffer_load_b128(rx, row * 1280 + co + i * 256, 0, 0); }
}
__device__ __forceinline__ unsigned rows_do(const RowIn& r, int row0) {
  const auto* kp = fresh_params();
  const int lane = fresh_lane(), row = row0 + (lane >> 4), co = (lane & 15) * 16;
  const rsrc_t rT = mkrsrc(kp->ws + X1_OFF, (unsigned)(WS_END - X1_OFF)), rO = mkrsrc(kp->out, S * DM * 2);
  auto par = [&](int a, int i, int k) { const f4 v = *(LAS f4*)(L_PAR + a * 2560 + co * 2 + i * 512 + (k >> 1) * 16); return k & 1 ? f2{v[2], v[3]} : f2{v[0], v[1]}; };
  f2 x1[20], s = {}, q = {};
  float rs = 0.f;
#pragma unroll
  for (int i = 0; i < 5; ++i) {
    v4u w;
#pragma unroll
    for (int k = 0; k < 4; ++k) {
      const f2 t = __builtin_elementwise_fma(par(0, i, k), up2(r.y[i][k]), up2(r.x[i][k]));
      w[k] = pack2(t[0], t[1]); x1[4 * i + k] = up2(w[k]); s += x1[4 * i + k];
    }
    __builtin_amdgcn_raw_buffer_store_b128(w, rT, row * 2560 + co + i * 256, 0, 16);
  }
  const float mean = sum16(s[0] + s[1]) * (1.f / DM);
#pragma unroll
  for (int i = 0; i < 20; ++i) { const f2 d = x1[i] - mean; q = __builtin_elementwise_fma(d, d, q); }
  const float isd = __builtin_amdgcn_rcpf(rbf(__builtin_amdgcn_sqrtf(rbf(rbf(sum16(q[0] + q[1]) * (1.f / DM)) + 1e-5f)))), mi = -rbf(mean) * isd;
#pragma unroll
  for (int i = 0; i < 5; ++i) {
    const v4u wr = lds128(L_PAR + 10240 + co + i * 256);
    v4u h, o;
#pragma unroll
    for (int k = 0; k < 4; ++k) {
      const f2 hh = __builtin_elementwise_fma(__builtin_elementwise_fma(x1[4 * i + k], f2{isd, isd}, f2{mi, mi}), par(2, i, k), par(1, i, k));
      const f2 oo = __builtin_elementwise_fma(par(3, i, k), hh, x1[4 * i + k]);
      const unsigned hb = pack2(hh[0], hh[1]), wk = wr[k];
      h[k] = hb; o[k] = pack2(oo[0], oo[1]);
      rs = __builtin_amdgcn_fdot2_f32_bf16(__builtin_bit_cast(bf2, hb), __builtin_bit_cast(bf2, wk), rs, false);
    }
    __builtin_amdgcn_raw_buffer_store_b128(h, rT, row * 2560 + 1280 + co + i * 256, 0, 16);
    __builtin_amdgcn_raw_buffer_store_b128(o, rO, row * 1280 + co + i * 256, 0, 16);
  }
  return f2u(rbf(sum16(rs)));
}
__device__ __forceinline__ void row_one(v3u par, int row, const unsigned* x) {
  const Params p = load_params(par);
  const int lane = fresh_lane();
  const rsrc_t rT = mkrsrc(p.ws + X1_OFF, (unsigned)(WS_END - X1_OFF)), rO = mkrsrc(p.out, S * DM * 2);
  f2 pr[4][5]; unsigned wr[5], y[5];
#pragma unroll
  for (int i = 0; i < 5; ++i) {
#pragma unroll
    for (int a = 0; a < 4; ++a) pr[a][i] = *(LAS f2*)(L_PAR + a * 2560 + i * 512 + lane * 8);
    wr[i] = *(LAS unsigned*)(L_PAR + 10240 + i * 256 + lane * 4);
  }
#pragma unroll 1
  for (int spin = 0;; ++spin) {
    asm volatile("" ::: "memory");
#pragma unroll
    for (int i = 0; i < 5; ++i) y[i] = __builtin_amdgcn_raw_buffer_load_b32(rT, row * 2560 + i * 256 + lane * 4, 0, 16);
    if (__builtin_amdgcn_ballot_w64(max(max(max(y[0], y[1]), max(y[2], y[3])), y[4]) == ~0u) == 0) break;
    if (spin > SPIN_CAP) { __hip_atomic_store((gu32*)(p.ws + p.co) + C_ERR, 1u, RLX_AGENT); break; }
    __builtin_amdgcn_s_sleep(1);
  }
  f2 x1[5], s = {}, q = {};
#pragma unroll
  for (int i = 0; i < 5; ++i) {
    const f2 t = __builtin_elementwise_fma(pr[0][i], up2(y[i]), up2(x[i]));
    const unsigned w = pack2(t[0], t[1]);
    x1[i] = up2(w); s += x1[i];
    __builtin_amdgcn_raw_buffer_store_b32(w, rT, row * 2560 + i * 256 + lane * 4, 0, 16);
  }
  const float mean = wave_sum(s[0] + s[1]) * (1.f / DM);
#pragma unroll
  for (int i = 0; i < 5; ++i) { const f2 d = x1[i] - mean; q = __builtin_elementwise_fma(d, d, q); }
  const float isd = __builtin_amdgcn_rcpf(rbf(__builtin_amdgcn_sqrtf(rbf(rbf(wave_sum(q[0] + q[1]) * (1.f / DM)) + 1e-5f)))), mi = -rbf(mean) * isd;
  float rs = 0.f;
#pragma unroll
  for (int i = 0; i < 5; ++i) {
    const f2 hh = __builtin_elementwise_fma(__builtin_elementwise_fma(x1[i], f2{isd, isd}, f2{mi, mi}), pr[2][i], pr[1][i]);
    const f2 oo = __builtin_elementwise_fma(pr[3][i], hh, x1[i]);
    const unsigned hb = pack2(hh[0], hh[1]), wk = wr[i];
    __builtin_amdgcn_raw_buffer_store_b32(hb, rT, row * 2560 + 1280 + i * 256 + lane * 4, 0, 16);
    __builtin_amdgcn_raw_buffer_store_b32(pack2(oo[0], oo[1]), rO, row * 1280 + i * 256 + lane * 4, 0, 16);
    rs = __builtin_amdgcn_fdot2_f32_bf16(__builtin_bit_cast(bf2, hb), __builtin_bit_cast(bf2, wk), rs, false);
  }
  const unsigned sc = f2u(rbf(wave_sum(rs)));
  VMCNT(0);
  if (lane == 0) __hip_atomic_store((gu32*)(p.ws + p.co) + C_R + row, sc, RLX_AGENT);
}
#define OGATE (seg < 58 ? seg + 2 : 60 + (seg - 58) / 2)
template <int NT>
__device__ __forceinline__ void o_unit(const Params& p, int seg, int t0, int rep, int lane, int wave) {
  gu32* ctrl = (gu32*)(p.ws + p.co);
  constexpr int NB = NT < 4 ? NT : 4;
  const rsrc_t rH = mkrsrc(p.ws, WS_BYTES);
  const int g = lane >> 4, n = lane & 15;
  const int sa = rfl(side_a(p, slot_blk(seg))), sb = rfl(side_a(p, slot_blk(seg) + 1)), sp = rfl(pt_off(p, seg));
  if (wave == 0) {
    const long w = ((long)flag_off(p, lane & 31) - p.co) / 4 + 8 + (lane >> 5) * 2;
    wait_ge<16>(ctrl, C_G + rep * 32, OGATE);
    if (OGATE < seg + 1) { wait_ge<32>(ctrl, w, seg + 1); wait_ge<32>(ctrl, w + 1, seg + 1); }
  }
  BAR();
  const int vy = (n >> 3) * 32768 + lmap(wave * 4096) + g * 128 + (n & 7) * 16, vz = (n >> 3) * 4096 + wave * 1024 + g * 128 + (n & 7) * 16;
  v4u y[NB][8], z[NB][2], pt[2][8], pm[2][2];
  auto ldy = [&](int t) {
    const int T = t0 + t, so = T < 32 ? sa + T * 65536 : sb + (T - 32) * 65536;
#pragma unroll
    for (int j = 0; j < 8; ++j) y[t % NB][j] = ld16s(rH, vy + j * 512, so);
  };
  auto ldz = [&](int t) {
#pragma unroll
    for (int j = 0; j < 2; ++j) z[t % NB][j] = ld16s(rH, vz + j * 512, (sb ^ 8192) + 524288 + seg / RING * 655360 + (t0 + t) * 16384);
  };
#pragma unroll
  for (int t = 0; t < NB; ++t) ldy(t);
  __builtin_amdgcn_sched_barrier(0);
  if (wave == 0) {
    if (seg < NSEG - 3) wait_ge<8>(ctrl, C_G + rep * 32, seg + 4);
    else wait_ge<2>(ctrl, C_PT + (lane & 15) * 32, lane & 8 ? 10 : 8);
  }
  BAR();
#pragma unroll
  for (int j = 0; j < 16; ++j) pt[j & 1][j >> 1] = ld16s(rH, lane * 16 + (j & 3) * 1024, sp + wave * 32768 + (j >> 3) * 16384 + ((j >> 2) & 1) * 4096);
#pragma unroll
  for (int j = 0; j < 4; ++j) pm[j & 1][j >> 1] = ld16s(rH, g * 512 + n * 16 + (j & 1) * 256 + (j >> 1) * 2048, sp + lmap(65536 + wave * 4096));
#pragma unroll
  for (int t = 0; t < NB; ++t) ldz(t);
  __builtin_amdgcn_sched_barrier(0);
#pragma unroll
  for (int t = 0; t < NT; ++t) {
    f4 acc[2] = {};
#pragma unroll
    for (int j = 0; j < 8; ++j) { acc[0] = mfma32(y[t % NB][j], pt[0][j], acc[0]); acc[1] = mfma32(y[t % NB][j], pt[1][j], acc[1]); }
#pragma unroll
    for (int j = 0; j < 2; ++j) { acc[0] = mfma32(z[t % NB][j], pm[0][j], acc[0]); acc[1] = mfma32(z[t % NB][j], pm[1][j], acc[1]); }
    __builtin_amdgcn_sched_barrier(0);
    if (t + NB < NT) { ldy(t + NB); ldz(t + NB); }
#pragma unroll
    for (int mt = 0; mt < 2; ++mt) *(LAS f4*)(wave * (32 * (NT * 64 + 16)) + (mt * 16 + n) * (NT * 64 + 16) + t * 64 + g * 16) = acc[mt];
    __builtin_amdgcn_sched_barrier(0);
  }
  LGKM0(); BAR();
  const rsrc_t rT = mkrsrc(p.ws + X1_OFF, (unsigned)(WS_END - X1_OFF));
#pragma unroll
  for (int i = 0; i < (NT + 3) / 4; ++i) {
    const int it = i * 256 + wave * 64 + lane, tok = (it / (2 * NT)) & 31, ch = it % (2 * NT);
    f4 a = {}, b = {};
#pragma unroll
    for (int w = 0; w < 4; ++w) { const unsigned ad = w * (32 * (NT * 64 + 16)) + tok * (NT * 64 + 16) + ch * 32; a += *(LAS f4*)ad; b += *(LAS f4*)(ad + 16); }
    __builtin_amdgcn_raw_buffer_store_b128(v4u{unsent(pack2(a[0], a[1])), unsent(pack2(a[2], a[3])), unsent(pack2(b[0], b[1])), unsent(pack2(b[2], b[3]))}, rT,
                                           it < 64 * NT ? (seg * 32 + tok) * 2560 + t0 * 32 + ch * 16 : OOB, 0, 16);
  }
}
static_assert(OSPLIT <= 2 * RING && OSPLIT >= NSEG - RING, "Z offset, arrival counts");
__device__ __forceinline__ void o_stream(v3u par, int id, int nb, int rep, int wave) {
  const Params p = load_params(par);
  gu32* ctrl = (gu32*)(p.ws + p.co);
  const int lane = fresh_lane(), g = lane >> 4, n = lane & 15;
  const rsrc_t rH = mkrsrc(p.ws, WS_BYTES);
  const int ta = side_a(p, slot_blk(lane)), tb = side_a(p, slot_blk(lane) + 1), tp = pt_off(p, lane);
  const int vy = (n >> 3) * 32768 + g * 128 + (n & 7) * 16, vz = (n >> 3) * 4096 + g * 128 + (n & 7) * 16;
  v4u y[5][8];
  auto ld = [&](int u, int it, int t) {
    const int sa = __builtin_amdgcn_readlane(ta, u >> 1), sb = __builtin_amdgcn_readlane(tb, u >> 1), T = (u & 1) * 20 + wave * 5 + t;
    const int so = it < 4 ? (T < 32 ? sa : sb - (1 << 21)) + T * 65536 : (sb ^ 8192) + (u >= 2 * RING ? 1179648 : 524288) + T * 16384;
    const int vo = it < 4 ? vy + lmap(((it + wave) & 3) * 4096) : vz;
#pragma unroll
    for (int j = 0; j < 8; ++j) y[t][j] = ld16s(rH, vo + j * 512, so);
  };
  if (wave == 0) wait_ge<8>(ctrl, C_G + rep * 32, (id >> 1) + 1);
  BAR();
#pragma unroll
  for (int t = 0; t < 5; ++t) ld(id, 0, t);
  int up = -1, srow = OOB;
  unsigned score = 0;
  RowIn ri;
  auto put_score = [&]() { __builtin_amdgcn_raw_buffer_store_b32(score, rH, n == 0 ? srow : OOB, 0, 16); };
#pragma unroll 1
  for (int u = id; u < 2 * OSPLIT; u += nb) {
    const int seg = u >> 1, un = min(u + nb, 2 * OSPLIT - 1);
    TS(id == 3 && wave == 0 && lane == 0 && u == id + 2 * nb, 40)
    if (wave == 0) wait_ge<8>(ctrl, C_G + rep * 32, seg + 4);
    BAR();
    TS(id == 3 && wave == 0 && lane == 0 && u == id + 2 * nb, 41)
    const int sp = __builtin_amdgcn_readlane(tp, seg);
#pragma unroll 1
    for (int k = 0; k < 20; ++k) dma16_sc1(rH, lane * 16, sp + lmap((wave * 20 + k) * 1024), (wave * 20 + k) * 1024);
    VMCNT(0); BAR();
    put_score();
    if (up >= 0 && wave == 1 && lane == 0) __hip_atomic_fetch_add(ctrl + C_AF + (up >> 1) * 32, 1u, RLX_AGENT);
    TS(id == 3 && wave == 0 && lane == 0 && u == id + 2 * nb, 42)
    f4 acc[2][5] = {};
#pragma unroll
    for (int it = 0; it < 5; ++it) {
      if (it == 4) {
        if (wave == 0) wait_ge<8>(ctrl, lane & 1 ? C_G + rep * 32 : C_AF + (max(up, 0) >> 1) * 32, lane & 1 ? (un >> 1) + 1 : up >= 0 ? 2 : 0);
        BAR();
        rows_load(ri, up * 16 + wave * 4);
      }
      v4u pf[2][8];
      const unsigned lp = it < 4 ? ((it + wave) & 3) * 16384 + lane * 16 : 65536 + g * 512 + n * 16;
#pragma unroll
      for (int j = 0; j < 16; ++j) pf[j & 1][j >> 1] = lds128(lp + (j >> 1) * 2048 + (j & 1) * (it < 4 ? 1024 : 256));
#pragma unroll
      for (int t = 0; t < 5; ++t) {
#pragma unroll
        for (int j = 0; j < 8; ++j) { acc[0][t] = mfma32(y[t][j], pf[0][j], acc[0][t]); acc[1][t] = mfma32(y[t][j], pf[1][j], acc[1][t]); }
        __builtin_amdgcn_sched_barrier(0);
        if (it < 4) ld(u, it + 1, t); else ld(un, 0, t);
        __builtin_amdgcn_sched_barrier(0);
      }
    }
#pragma unroll
    for (int k = 0; k < 10; ++k) {
      const f4 a = acc[k & 1][k >> 1];
      *(LAS v2u*)(81920 + ((k & 1) * 16 + n) * 656 + (wave * 5 + (k >> 1)) * 32 + g * 8) = v2u{pack2(a[0], a[1]), pack2(a[2], a[3])};
    }
    LGKM0(); BAR();
#pragma unroll
    for (int k = 0; k < 5; ++k) {
      const int c = k * 64 + lane, r = wave * 8 + c / 40, o = (c % 40) * 16;
      __builtin_amdgcn_raw_buffer_store_b128(lds128(81920 + r * 656 + o), rH, X1_OFF + (seg * 32 + r) * 2560 + (u & 1) * 640 + o, 0, 16);
    }
    __builtin_amdgcn_sched_barrier(0);
    TS(id == 3 && wave == 0 && lane == 0 && u == id + 2 * nb, 43)
    if (up >= 0) { score = rows_do(ri, up * 16 + wave * 4); srow = p.co + (C_R + up * 16 + wave * 4 + g) * 4; }
    TS(id == 3 && wave == 0 && lane == 0 && u == id + 2 * nb, 44)
    TS(up >= 0 && wave == 0 && lane == 0, 704 + (up >> 1))
    up = u;
  }
  VMCNT(0); BAR();
  put_score();
  if (wave == 0) {
    if (lane == 0) __hip_atomic_fetch_add(ctrl + C_AF + (up >> 1) * 32, 1u, RLX_AGENT);
    wait_ge<4>(ctrl, C_AF + (up >> 1) * 32, 2);
  }
  BAR();
  rows_load(ri, up * 16 + wave * 4);
  score = rows_do(ri, up * 16 + wave * 4); srow = p.co + (C_R + up * 16 + wave * 4 + g) * 4;
  VMCNT(0);
  put_score();
  TS(wave == 0 && lane == 0, 704 + (up >> 1))
}
__device__ __forceinline__ void o_role(v3u par, int u, int rep, int wave) {
#pragma unroll 1
  for (int seg = OSPLIT + u / 10; seg < NSEG; seg += 6) {
    const Params p = load_params(par);
    const int lane = fresh_lane(), row = seg * 32 + (u % 10) * 4 + wave;
    unsigned x[5];
#pragma unroll
    for (int i = 0; i < 5; ++i) { const int o = i * 256 + lane * 4; x[i] = *(LAS unsigned*)(L_XR + (wave * 4 + (seg >= OSPLIT + 6) * 2) * 1024 + o + (o >= 640) * 384); }
    o_unit<4>(p, seg, (u % 10) * 4, rep, lane, wave);
    TS(u % 10 == 0 && wave == 0 && lane == 0, 1500 + seg)
    if (seg < NSEG - 4) pause(150);
    if (u % 10 < 8) row_one(par, row, x);
    TS(u % 10 == 0 && wave == 0 && lane == 0, 704 + seg)
  }
}

__device__ __forceinline__ void s_role(const Params& p, int h, int rep, int lane, int wave) {
  constexpr int L_K = 24576, L_D = 30720, L_Q = 57344, L_MEM = 63488, L_RES = 73728;
  const int g = lane >> 4, n = lane & 15, q = (lane >> 2) & 3, pp = lane & 3, j0 = 4 * (g & 1) + 2 * (g >> 1);
  v32u vc; v12u sr; v16u sc;
  vc[0] = n < 8 ? 0x4444 : 0xEEEE;
  vc[1] = lane * 16;
  vc[2] = L_MEM + lane * 16;
  vc[3] = L_RES + (wave ^ wave >> 1) * 22528 + lane * 16;
  vc[4] = 0;
  vc[5] = L_Q + g * 1024 + q * 128 + (4 * g + q) * 8 + pp * 2;
  vc[6] = L_K + g * 128 + (n ^ (8 * (g >> 1))) * 8;
  vc[7] = lane < 63 ? side_a(p, slot_blk(lane + 1) + 1) : OOB;
  vc[8] = lmap(KQ_OFF + h * 24576) + lane * 16;
  vc[9] = L_K + pp * 128 + ((4 * g + q) ^ (8 * (pp >> 1))) * 8;
  vc[10] = 0;
  vc[11] = hand_off(p, lane) + rep * 2 * H_COPY;
  vc[12] = L_D + lane * 4;
  const int kt = wave == 2, tok = (g * 16 + 8 * kt + 4 * (q >> 1) + pp) * 16;
  if (wave == 1 || wave == 2) {
    vc[13] = n - 4 * g;
    vc[14] = lane == 0 ? p.co + (C_PT + (h & 7) * 32) * 4 : OOB;
    vc[15] = pt_off(p, lane);
    vc[16] = lmap(h * 2048 + kt * 1024 + lane * 16);
    vc[17] = lmap(65536 + h * 512 + (16 * kt + n) * 16 + j0 * 2);
    vc[18] = lane * 16;
  } else {
    vc[13] = lmap(H_P) + (n < 8 ? h * 128 + n * 16 + j0 * 2 : 4096 + (h >> 3) * 1024 + ((2 * (((h >> 1) ^ h) & 1) + (g & 1)) * 16 + (h & 1) * 8 + n - 8) * 16 + ((h >> 2) & 1) * 8 + (g >> 1) * 4);
    vc[14] = j0 == (n & 7) ? 0x3f800000u : 0u;
    vc[15] = j0 + 1 == (n & 7) ? 0x3f800000u : 0u;
    vc[16] = (h & 7) == 0 && lane < 8 ? p.co + (C_G0 + rep * 32) * 4 + lane * 16 : OOB; vc[17] = 0;
    vc[18] = flag_off(p, h) + (lane % 12) * 4;
  }
  if (wave == 3) { vc[13] = lane < 4 ? lane * 2 * H_COPY + lmap(H_NOTE + rep * 128) : OOB; vc[14] = look_off(lane); }
  vc[19] = sum_off(lane);
  vc[20] = (q & 1) * 1024 + tok;
  vc[21] = 16384 + (q & 1) * 8 + tok;
  vc[22] = lmap(H_E + h * 512 + (2 * kt + (g >> 1)) * 128 + (n & 7) * 16 + (g & 1) * 8 + (n >> 3) * 4);
  vc[23] = 0;
#pragma unroll
  for (int k = 0; k < 3; ++k) { const int c = k * 64 + lane; vc[24 + k] = ((16 * wave + c / 24) * HD + h * DH) * 2 + ((c % 24) ^ (c / 24 & 4)) * 16 - k * 1024; }
  vc[27] = (16 * (g >> 1) + 8 * (q >> 1) + 4 * (g & 1) + (q & 1)) * 384 + (((32 * wave + 8 * pp) * 2) ^ (64 * (g & 1)));
  vc[28] = lane == 0 ? p.co + (C_ANCH + 128 + rep * 32) * 4 : OOB;
  vc[29] = p.co + (C_ANCH + rep * 32 + lane % 3) * 4;
  vc[30] = (16 * (g >> 1) + 8 * (q >> 1) + 4 * (g & 1) + (q & 1)) * 384 + (((128 + 16 * wave + 4 * pp) * 2) ^ (64 * (g & 1))); vc[31] = 0;
  set_rsrc(sr, 0, p.ws, WS_BYTES); set_rsrc(sr, 1, p.wq, DM * HD * 2); set_rsrc(sr, 2, p.wk, DM * HD * 2);
#pragma unroll
  for (int i = 4; i < 16; ++i) sc[i] = 0;
  sc[0] = wave; sc[1] = p.dL + p.dM; sc[2] = p.dS; sc[3] = rfl(side_a(p, 1)); sc[9] = p.dL * 23 / 4 + KQ_DELAY - 250; sc[10] = rfl(rep * 2 * H_COPY);
#pragma unroll
  for (int k = 1; k < 4; ++k) sc[5 + k] = rfl((((rep + k) & 3) - rep) * 2 * H_COPY);
  pin(sc); pin(sr);
  asm volatile(SROLE_ASM : "+{s[52:67]}"(sc), "+{v[0:31]}"(vc), "+{s[36:47]}"(sr) :: "memory", "vcc", "scc", SROLE_CLOBBERS);
  gu32* ctrl = (gu32*)(fresh_params()->ws + CTRL_OFF);
  if (fresh_lane() == 0) {
    if (sc[1] > 20000u || sc[4] > 20000u) { __hip_atomic_store(ctrl + C_ERR, 1u, RLX_AGENT); __hip_atomic_store(ctrl + C_ERR + 2048, 1u, RLX_AGENT); }
  }
}

__device__ __forceinline__ void start_chain(const Params& p, int lane
) {
  gu32* ctrl = (gu32*)(p.ws + p.co);
  const int ho = hand_off(p, lane);
  const rsrc_t rH = mkrsrc(p.ws, WS_BYTES);
  wait_ge<1>(ctrl, C_ANCH + 128 + (lane & 3) * 32, 28);
  unsigned A = now_ticks() * 4 + p.lead;
  int x = XSTART, lev = 0;
  unsigned due = 0;
  float inv = 1.f / FSTART;
  auto plan = [&](int s) {
    const float sl = inv - 1.f / 23.f;
    const int dE = p.dE + lev + (int)(4.f * SLOPE_E * sl), dL = p.dL + lev + (int)(4.f * SLOPE_L * sl), dM = p.dM + lev / 2 + (int)(4.f * SLOPE_M * sl);
    const v4u v = {A | (s == 0), (A + x + dE) | (s == 0), (A + x + dL) | (s == 0), 0u};
    const int hs = __builtin_amdgcn_readlane(ho, (s - 1) & 63);
    __builtin_amdgcn_raw_buffer_store_b128(v, rH, lane >= 32 ? OOB : (s ? hs + (lane >> 3) * 2 * H_COPY + lmap(H_PLAN) : p.co + (C_ANCH + (lane >> 3) * 32) * 4) + (lane & 7) * 16, 0, 16);
    if (lane == s) due = A + x + dL;
    A += dL + dM + 2 * x; x >>= 1;
  };
#pragma unroll 1
  for (int s = 0; s <= HORIZON; ++s) plan(s);
  unsigned e1 = 0, a1 = 0, e2 = 0, a2 = 0;
#pragma unroll 1
  for (int g = 1, n1 = 0, n2 = 0, n3 = 0, calm = 0; g <= NSEG; ++g) {
    v4u w;
    {
      const unsigned t = __builtin_amdgcn_readlane(due, g - 1) + n1 * p.dS + GLOOK;
#pragma unroll 1
      while ((unsigned)(t - now_ticks() * 4) - 1u < 4000u) __builtin_amdgcn_s_sleep(1);
    }
#pragma unroll 1
    for (int spin = 0;; ++spin) {
      asm volatile("" ::: "memory");
      w = ld16s(rH, p.co + (C_G0 + (lane & 3) * 32) * 4, 0);
      if (__builtin_amdgcn_ballot_w64((int)w[0] < g << 16) == 0) break;
      if (spin > SPIN_CAP) { __hip_atomic_store(ctrl + C_ERR, 1u, RLX_AGENT); break; }
      __builtin_amdgcn_s_sleep(GPOLL);
    }
    if (lane < 16) __hip_atomic_store(ctrl + C_G + lane * 32, (unsigned)g, RLX_AGENT);
    const int n = rfl(w[0]) & 0xffff;
    if (n > n1 && n1 > n2 && n2 > n3 && g >= calm && g < NSEG) {
      const int hs = __builtin_amdgcn_readlane(ho, g);
      if (lane < 4) __hip_atomic_store(ctrl + ((long)hs + lane * 2 * H_COPY + lmap(H_PLAN) - p.co) / 4 + 3, (unsigned)(n + MEET), RLX_AGENT);
      calm = g + 4;
    }
    if (n > n1 && n1 == n2) lev += LEVUP;
    if (g > 2 && n == n2) {
      float r = 0.25f * (float)(w[2] - a2) / (float)(w[1] - e2);
      r = fmaxf(r, u2f(__builtin_amdgcn_update_dpp(0, (int)f2u(r), 0xb1, 0xf, 0xf, true))); r = fmaxf(r, u2f(__builtin_amdgcn_update_dpp(0, (int)f2u(r), 0x4e, 0xf, 0xf, true)));
      r = fminf(fmaxf(r, 1.f / 25.f), 1.f / 15.f);
      inv = fmaxf(r, inv + (r - inv) * (g < 6 ? 0.5f : FDECAY));
    }
    n3 = n2; n2 = n1; n1 = n; e2 = e1; a2 = a1; e1 = w[1]; a1 = w[2];
    if (g + HORIZON < NSEG) plan(g + HORIZON);
  }
}

__global__ void __launch_bounds__(256, 1) k_main(Params p0) {
  extern __shared__ __attribute__((aligned(16))) unsigned char smem[];
  int job = (blockIdx.x & 7) * 32 + (blockIdx.x >> 3) | rfl(threadIdx.x >> 6) << 8;
  {
    unsigned long long pc;
    asm volatile("s_getpc_b64 s[40:41]\n\ts_add_u32 s40, s40, _Z6k_main6Params@rel32@lo+4\n\ts_addc_u32 s41, s41, _Z6k_main6Params@rel32@hi+12" : "={s[40:41]}"(pc) :: "scc");
    const int lane = fresh_lane(), wv = job >> 8;
    const rsrc_t r = wv == 3 ? mkrsrc((const void*)pc, CODE_BYTES) : mkrsrc(p0.ws, WS_BYTES);
    const int vo = wv == 3 ? (lane * 32 + (job & 31)) * 64 : wv < 2 ? ((wv * 64 + lane) << 21) + 0x1FF000 + (job & 31) * 128 : OOB;
    asm volatile("s_mov_b32 m0, %0\n\ts_nop 3\n\tbuffer_load_dword %1, %2, 0 offen lds" ::"s"(MAIN_LDS - 256), "v"(vo), "s"(r) : "memory");
    if (wv < 3 && (job & 0x83) != 0x81) ffn_warm(wv, blockIdx.x, lane);
  }
  v3u par;
#define JOB() job = rfl(job); asm volatile("" : "+s"(job)); const int wv = job >> 8, jb = job & 255, jx = jb >> 5, jt = jb & 31, type = jb & 3, head = (jb - 128) >> 2; (void)jx; (void)jt; (void)type; (void)head;
#define TSJ(i) { JOB() const Params p = load_params(par); TS(wv == 0 && fresh_lane() == 0, (i) + jb) }
  {
    JOB()
    if (jb >= 128) {
      v32u wa[7]; v16u wa7;
      par = token_start(wa, wa7, type, head, fresh_lane(), wv);
      pin(par);
      if (type < 3) proj_role(wa, wa7, load_params(par), type, head, fresh_lane(), wv);
      else { vo_role(wa, wa7, load_params(par), head, fresh_lane(), wv); }
    } else {
      par = v3u{0u, 0u, 0u};
      if (jt < 8) s_role(load_params(par), jx * 8 + jt, jx, fresh_lane(), wv);
      else {
        u_role(load_params(par), ((jx + (jt - 8) / 3) & 3) * 8 + (jt - 8) / 3, (jt - 8) % 3, jt < 28 ? jx * 20 + jt - 8 : -1, jx, fresh_lane(), wv);
        JOB()
        if (jt >= 28) par = o_start(jx * 4 + jt - 28, fresh_lane(), wv);
      }
    }
    pin(par);
    TSJ(256)
  }
  JOB()
  if (jb >= 128 || jt >= 28) {
    if (jb == 127) { if (wv == 0) start_chain(load_params(par), fresh_lane()
); else if (wv == 1) select_rows(par, fresh_lane()); }
    JOB()
    const int u = type * 32 + head;
    if (jb < 127 || (jb > 127 && u < 60)) {
      if (jb > 127) row_params(par, wv);
      if (jb < 127) o_stream(par, jx * 4 + jt - 28, 15, jb & 15, wv); else o_role(par, u, jb & 15, wv);
    } else if (jb > 127 && u < 108) ffn_role(load_params(par), u - 60, fresh_lane(), wv);
  }
  TSJ(1024)
}

#define SCHED_DE 378
#define SCHED_DL 436
#define SCHED_DM 178
#define SCHED_DZ 96
#define SCHED_DS 400
#define SCHED_LEAD 600

extern "C" void kernel_launch(void* const* d_in, const int* in_sizes, int n_in, void* d_out, int out_size, void* d_ws, size_t ws_size,
                              hipStream_t stream) {
  Params p;
  p.x = (const u16*)d_in[0]; p.mem0 = (const u16*)d_in[1]; p.wq = (const u16*)d_in[2]; p.wk = (const u16*)d_in[3];
  p.wv = (const u16*)d_in[4]; p.wo = (const u16*)d_in[5]; p.wr = (const u16*)d_in[8]; p.wg = (const u16*)d_in[9];
  p.wu = (const u16*)d_in[10]; p.wd = (const u16*)d_in[11]; p.c = (const u16*)d_in[12]; p.wmod = (const u16*)d_in[13];
  p.out = (u16*)d_out;
  p.ws = (unsigned char*)(((uintptr_t)d_ws + ws_size - WS_BYTES) & ~(uintptr_t)(2 * MB - 1));
  (void)hipFuncSetAttribute((const void*)k_main, hipFuncAttributeMaxDynamicSharedMemorySize, MAIN_LDS);
  p.dE = SCHED_DE; p.dL = SCHED_DL; p.dM = SCHED_DM; p.dZ = SCHED_DZ; p.dS = SCHED_DS; p.lead = SCHED_LEAD;
  p.m0 = 0; p.m1 = 0; p.m2 = 0; p.co = 0;
  k_main<<<256, 256, MAIN_LDS, stream>>>(p);
}
```

```cpp
#include <hip/hip_runtime.h>
#include <hip/hip_bf16.h>
#include <stdint.h>
#include <type_traits>

#define GAS __attribute__((address_space(1)))
#define LAS __attribute__((address_space(3)))
#define RLX_AGENT __ATOMIC_RELAXED, __HIP_MEMORY_SCOPE_AGENT
typedef unsigned short u16;
typedef unsigned v4u __attribute__((ext_vector_type(4)));
typedef unsigned v2u __attribute__((ext_vector_type(2)));
typedef unsigned v8u __attribute__((ext_vector_type(8)));
typedef float f4 __attribute__((ext_vector_type(4)));
typedef __bf16 bf2 __attribute__((ext_vector_type(2)));
typedef __bf16 bf8 __attribute__((ext_vector_type(8)));
typedef __bf16 bf16v __attribute__((ext_vector_type(16)));
typedef short s4 __attribute__((ext_vector_type(4)));
typedef GAS unsigned gu32;
typedef __amdgpu_buffer_rsrc_t rsrc_t;

constexpr int S = 2048, DM = 640, NH = 32, DH = 192, HD = NH * DH, NSEG = 64, FF = 1536, TOPK = 8;

constexpr size_t MB = 1u << 20;
constexpr int RING = 32, HRING = 24, USLOT = 12, HB = 64, PB = 68, LB = 74;
__host__ __device__ constexpr int slot_blk(int s) { return 2 * (s % RING); }
constexpr int NCAL = LB + 1, NBLK = LB + 6;
constexpr unsigned WS_BYTES = (unsigned)NBLK << 21;
constexpr int CTRL_OFF = LB << 21;
constexpr int CTRL_BYTES = 131072;
constexpr int MODB_OFF = CTRL_OFF + CTRL_BYTES;
constexpr int MODG_OFF = MODB_OFF + 8192;
constexpr int HF_OFF = CTRL_OFF + (1 << 21);
constexpr int HF_BYTES = S * DM * 2;
constexpr int X1_OFF = HF_OFF + HF_BYTES;
constexpr int ACT_OFF = X1_OFF + 2 * HF_BYTES;
constexpr int WS_END = ACT_OFF + 2 * TOPK * FF * 2;
constexpr int TRACE_OFF = (LB + 5) << 21;
static_assert(WS_END <= TRACE_OFF, "workspace");
constexpr int H_COPY = 40960, H_STEP = 4 * H_COPY, H_FLAG = 10240, H_NOTE = 14336, H_PLAN = 15360, H_E = 16384, H_P = 32768, KQ_OFF = 262144, PT_SEG = 81920;

constexpr int C_ANCH = 0;
constexpr int C_IDX = 256;
constexpr int C_LN = 1152;
constexpr int C_MOD = 1408;
constexpr int C_PT = 1536;
constexpr int C_Z = 1792;
constexpr int C_G = 4096;
constexpr int C_G0 = 4608;
constexpr int C_ERR = 4736;
constexpr int C_VF = 5120;
constexpr int C_AF = 8192;
constexpr int C_U = 12288;
constexpr int C_R = 16384;
constexpr int ZERO_BYTES = 65536;
constexpr int TAB_OFF = CTRL_OFF + 0x1C000, SUM_OFF = TAB_OFF + 0x3800, EPOCH_OFF = TAB_OFF + 0x3F00, NTAB = NCAL + 32;
static_assert(NTAB <= 112 && NCAL <= 96, "table");
constexpr int DBG_OFF = CTRL_OFF + 262144, C_STAT = 0, C_DBG = 2048;
__host__ __device__ constexpr int lmap(int off) { return ((off >> 13) << 14) | (off & 8191); }
constexpr int OOB = 0x40000000;

__device__ __forceinline__ float u2f(unsigned v) { return __builtin_bit_cast(float, v); }
__device__ __forceinline__ unsigned f2u(float v) { return __builtin_bit_cast(unsigned, v); }
__device__ __forceinline__ float bf2f(u16 v) { return __builtin_bit_cast(float, (unsigned)v << 16); }
__device__ __forceinline__ float lo2f(unsigned v) { return __builtin_bit_cast(float, v << 16); }
__device__ __forceinline__ float hi2f(unsigned v) { return __builtin_bit_cast(float, v & 0xffff0000u); }
__device__ __forceinline__ unsigned pack2(float a, float b) { bf2 v = {(__bf16)a, (__bf16)b}; return __builtin_bit_cast(unsigned, v); }
__device__ __forceinline__ float rbf(float a) { return (float)(__bf16)a; }
__device__ __forceinline__ unsigned long long uni64(unsigned long long a) {
  return (unsigned)__builtin_amdgcn_readfirstlane((unsigned)a) | (unsigned long long)(unsigned)__builtin_amdgcn_readfirstlane((unsigned)(a >> 32)) << 32;
}
__device__ __forceinline__ rsrc_t mkrsrc(const void* p, unsigned bytes) { return __builtin_amdgcn_make_buffer_rsrc((void*)uni64((unsigned long long)p), 0, bytes, 0x00020000); }
__device__ __forceinline__ unsigned xcc_id() { return (unsigned)__builtin_amdgcn_s_getreg(20 | (3 << 11)) & 0xfu; }
__device__ __forceinline__ int rfl(int v) { return __builtin_amdgcn_readfirstlane(v); }
__device__ __forceinline__ f4 mfma32(v4u a, v4u b, f4 c) { return __builtin_amdgcn_mfma_f32_16x16x32_bf16(__builtin_bit_cast(bf8, a), __builtin_bit_cast(bf8, b), c, 0, 0, 0); }
__device__ __forceinline__ f4 mfma16(v2u a, v2u b, f4 c) { return mfma32(v4u{a[0], a[1], 0u, 0u}, v4u{b[0], b[1], 0u, 0u}, c); }
__device__ __forceinline__ f4 smfma(v4u a, v8u b, f4 c, int idx) { return __builtin_amdgcn_smfmac_f32_16x16x64_bf16(__builtin_bit_cast(bf8, a), __builtin_bit_cast(bf16v, b), c, idx, 0, 0); }
__device__ __forceinline__ v2u trread(unsigned addr) { return __builtin_bit_cast(v2u, __builtin_amdgcn_ds_read_tr16_b64_v4i16((LAS s4*)addr)); }
__device__ __forceinline__ v4u lds128(unsigned addr) { return *(LAS v4u*)addr; }
__device__ __forceinline__ v2u lds64(unsigned addr) { return *(LAS v2u*)addr; }
__device__ __forceinline__ void dma16(rsrc_t r, int voff, int soff, unsigned ldsaddr) {
  asm volatile("s_mov_b32 m0, %0\n\ts_nop 3\n\tbuffer_load_dwordx4 %1, %2, %3 offen lds" ::"s"(ldsaddr), "v"(voff), "s"(r), "s"(soff) : "memory");
}
__device__ __forceinline__ void dma16_sc1(rsrc_t r, int voff, int soff, unsigned ldsaddr) {
  asm volatile("s_mov_b32 m0, %0\n\ts_nop 3\n\tbuffer_load_dwordx4 %1, %2, %3 offen sc1 lds" ::"s"(ldsaddr), "v"(voff), "s"(r), "s"(soff) : "memory");
}
#define VMCNT_(n) asm volatile("s_waitcnt vmcnt(" #n ")" ::: "memory")
#define VMCNT(n) VMCNT_(n)
#define LGKM0() asm volatile("s_waitcnt lgkmcnt(0)" ::: "memory")
#define BAR() asm volatile("s_barrier" ::: "memory")
__device__ __forceinline__ float swapsum32(float a, float b) {
  auto r = __builtin_amdgcn_permlane32_swap(__builtin_bit_cast(unsigned, a), __builtin_bit_cast(unsigned, b), false, false);
  return u2f(r[0]) + u2f(r[1]);
}
__device__ __forceinline__ float swapsum16(float a, float b) {
  auto r = __builtin_amdgcn_permlane16_swap(__builtin_bit_cast(unsigned, a), __builtin_bit_cast(unsigned, b), false, false);
  return u2f(r[0]) + u2f(r[1]);
}
__device__ __forceinline__ float max_x32(float a) {
  auto r = __builtin_amdgcn_permlane32_swap(__builtin_bit_cast(unsigned, a), __builtin_bit_cast(unsigned, a), false, false);
  return fmaxf(u2f(r[0]), u2f(r[1]));
}
__device__ __forceinline__ float max_x16(float a) {
  auto r = __builtin_amdgcn_permlane16_swap(__builtin_bit_cast(unsigned, a), __builtin_bit_cast(unsigned, a), false, false);
  return fmaxf(u2f(r[0]), u2f(r[1]));
}
__device__ __forceinline__ unsigned ror8u(unsigned v) { return (unsigned)__builtin_amdgcn_update_dpp(0, (int)v, 0x128, 0xf, 0xf, true); }
__device__ __forceinline__ float ror8f(float v) { return __builtin_bit_cast(float, ror8u(__builtin_bit_cast(unsigned, v))); }
__device__ __forceinline__ float wave_sum(float v) {
  v += u2f(__builtin_amdgcn_update_dpp(0, (int)f2u(v), 0xb1, 0xf, 0xf, true)); v += u2f(__builtin_amdgcn_update_dpp(0, (int)f2u(v), 0x4e, 0xf, 0xf, true));
  v += u2f(__builtin_amdgcn_update_dpp(0, (int)f2u(v), 0x124, 0xf, 0xf, true)); v += ror8f(v);
  v = swapsum16(v, v);
  return swapsum32(v, v);
}
typedef unsigned short us2 __attribute__((ext_vector_type(2)));
__device__ __forceinline__ unsigned unsent(unsigned w) { return __builtin_bit_cast(unsigned, __builtin_elementwise_min(__builtin_bit_cast(us2, w), us2{0xfffe, 0xfffe})); }

constexpr int OSPLIT = 48;
constexpr unsigned CODE_BYTES = 131072;
struct Params {
  const u16 *x, *mem0, *wq, *wk, *wv, *wo, *wr, *wg, *wu, *wd, *c, *wmod;
  u16* out;
  unsigned char* ws;
  int dE, dL, dM, dZ, dS, lead;
  unsigned m0, m1, m2;
  int co;
};

#define XSTART 32
#define MEET 3
#define AHEAD 11
#define AHEAD2 15
static_assert(AHEAD + 9 <= HRING && AHEAD2 + 9 <= HRING, "hand-off ring");
#define SDELAY 0
#define KDELAY 0
#define FSTART 21.f
#define FDECAY 0.2f
#define LEVUP 4
#define SLOPE_E 2200.f
#define SLOPE_L 2600.f
#define SLOPE_M 800.f
#define HORIZON 2
#define GLOOK 60
#define CALREP 4
#define SUMWAIT 60
#define CALWIN 14
#define MODAUX 2
#define WARMAT 0
#define BUSY 12
#define GPOLL 12
#define KQ_DELAY -100
constexpr int MAIN_LDS = 163840;
constexpr int SPIN_CAP = 1 << 18;
typedef unsigned v12u __attribute__((ext_vector_type(12)));
typedef unsigned v16u __attribute__((ext_vector_type(16)));
typedef unsigned v32u __attribute__((ext_vector_type(32)));

#define SROLE_CLOBBERS \
  "v32", "v33", "v34", "v35", "v36", "v37", "v38", "v39", "v40", "v41", "v42", "v43", "v44", "v45", "v46", "v47", "v48", "v49", "v50", "v51", "v52", \
  "v53", "v54", "v55", "v56", "v57", "v58", "v59", "v60", "v61", "v62", "v63", "v64", "v65", "v66", "v67", "v68", "v69", "v70", "v71", "v72", "v73", \
  "v74", "v75", "v76", "v77", "v78", "v79", "v80", "v81", "v82", "v83", "v84", "v85", "v86", "v87", "v88", "v89", "v90", "v91", "v92", "v93", "v94", \
  "v95", "v96", "v97", "v98", "v99", "v100", "v101", "v102", "v103", "v104", "v105", "v106", "v107", "v108", "v109", "v110", "v111", "v112", "v113", \
  "v114", "v115", "v116", "v117", "v118", "v119", "v120", "v121", "v122", "v123", "v124", "v125", "v126", "v127", "v128", "v129", "v130", "v131", \
  "v132", "v133", "v134", "v135", "v136", "v137", "v138", "v139", "v140", "v141", "v142", "v143", "v144", "v145", "v146", "v147", "v148", "v149", \
  "v150", "v151", "v152", "v153", "v154", "v155", "v156", "v157", "v158", "v159", "v160", "v161", "v162", "v163", "v164", "v165", "v166", "v167", \
  "v168", "v169", "v170", "v171", "v172", "v173", "v174", "v175", "v176", "v177", "v178", "v179", "v180", "v181", "v182", "v183", "v184", "v185", \
  "v186", "v187", "v188", "v189", "v190", "v191", "v192", "v193", "v194", "v195", "v196", "v197", "v198", "v199", "v200", "v201", "v202", "v203", \
  "v204", "v205", "v206", "v207", "v208", "v209", "v210", "v211", "v212", "v213", "v214", "v215", "v216", "v217", "v218", "v219", "v220", "v221", \
  "v222", "v223", "v224", "v225", "v226", "v227", "v228", "v229", "v230", "v231", "v232", "v233", "v234", "v235", "v236", "v237", "v238", "v239", \
  "v240", "v241", "v242", "v243", "v244", "v245", "v246", "v247", "v248", "v249", "v250", "v251", "v252", "v253", "v254", "v255", "s34", "s35", "s48", \
  "s49", "s50", "s51", "s68", "s69", "s70", "s71", "s72", "s73", "s74", "s75", "s76", "s77", "s78", "s79", "s80", "s81", "s82", "s83", "s84", "s85", \
  "s86", "s87", "s88", "s89", "s90", "s91", "s93", "s94", "s95", "s96", "s97", "s98", "a0", "a1", "a2", "a3", "a4", "a5", "a6", "a7", "a8", "a9", "a10", \
  "a11", "a12", "a13", "a14", "a15", "a16", "a17", "a18", "a19", "a20", "a21", "a22", "a23", "a24", "a25", "a26", "a27", "a28", "a29", "a30", "a31", \
  "a32", "a33", "a34", "a35", "a36", "a37", "a38", "a39", "a40", "a41", "a42", "a43", "a44", "a45", "a46", "a47", "a48", "a49", "a50", "a51", "a52", \
  "a53", "a54", "a55", "a56", "a57", "a58", "a59", "a60", "a61", "a62", "a63", "a64", "a65", "a66", "a67", "a68", "a69", "a70", "a71", "a72", "a73", \
  "a74", "a75", "a76", "a77", "a78", "a79", "a80", "a81", "a82", "a83", "a84", "a85", "a86", "a87", "a88", "a89", "a90", "a91", "a92", "a93", "a94", \
  "a95", "a96", "a97", "a98", "a99", "a100", "a101", "a102", "a103", "a104", "a105", "a106", "a107", "a108", "a109", "a110", "a111", "a112", "a113", \
  "a114", "a115", "a116", "a117", "a118", "a119", "a120", "a121", "a122", "a123", "a124", "a125", "a126", "a127", "a128", "a129", "a130", "a131", \
  "a132", "a133", "a134", "a135", "a136", "a137", "a138", "a139", "a140", "a141", "a142", "a143", "a144", "a145", "a146", "a147", "a148", "a149", \
  "a150", "a151", "a152", "a153", "a154", "a155", "a156", "a157", "a158", "a159", "a160", "a161", "a162", "a163", "a164", "a165", "a166", "a167", \
  "a168", "a169", "a170", "a171", "a172", "a173", "a174", "a175", "a176", "a177", "a178", "a179", "a180", "a181", "a182", "a183", "a184", "a185", \
  "a186", "a187", "a188", "a189", "a190", "a191", "a192", "a193", "a194", "a195", "a196", "a197", "a198", "a199", "a200", "a201", "a202", "a203", \
  "a204", "a205", "a206", "a207", "a208", "a209", "a210", "a211", "a212", "a213", "a214", "a215", "a216", "a217", "a218", "a219", "a220", "a221", \
  "a222", "a223", "a224", "a225", "a226", "a227", "a228", "a229", "a230", "a231", "a232", "a233", "a234", "a235", "a236", "a237", "a238", "a239", \
  "a240", "a241", "a242", "a243", "a244", "a245", "a246", "a247", "a248", "a249", "a250", "a251", "a252", "a253", "a254", "a255"
#define SROLE_ASM \
  "s_mov_b32 s34, 0x00ff00ff\n" \
  "s_mov_b32 s35, 0x00ff00ff\n" \
  "s_mov_b32 s68, 0\n" \
  "s_mov_b32 s73, 0\n" \
  "s_mov_b32 s56, 0\n" \
  "s_mov_b32 s57, 0\n" \
  "s_mov_b32 s51, 0\n" \
  "s_mov_b32 s71, 0\n" \
  "s_mov_b32 s91, 0\n" \
  "s_mov_b32 s50, 5900\n" \
  "v_mov_b32 v4, 0x3dd53b94\n" \
  "s_mov_b32 s93, s58\n" \
  "s_mov_b32 s94, s59\n" \
  "s_mov_b32 s95, s60\n" \
  "s_mov_b32 s96, s61\n" \
  "s_mov_b32 s58, 0\n" \
  "s_mov_b32 s59, 0\n" \
  "s_mov_b32 s60, 0\n" \
  "s_mov_b32 s61, 0\n" \
  "s_mov_b32 s62, 0\n" \
  "s_mov_b32 s63, 0\n" \
  "s_mov_b32 s64, 0\n" \
  "s_mov_b32 s65, 0\n" \
  "s_mov_b32 s66, 0\n" \
  "s_mov_b32 s67, 0\n" \
  "s_memrealtime s[76:77]\n" \
  "s_memtime s[78:79]\n" \
  "s_waitcnt lgkmcnt(0)\n" \
  "s_mov_b32 s88, s76\n" \
  "s_mov_b32 s89, s78\n" \
  "s_mul_i32 s72, s52, 6144\n" \
  "s_add_u32 m0, s72, 0\n" \
  "s_mov_b32 s74, 0\n" \
  "buffer_load_dwordx4 v24, s[40:43], s74 offen offset:0 lds\n" \
  "buffer_load_dwordx4 v25, s[40:43], s74 offen offset:1024 lds\n" \
  "buffer_load_dwordx4 v26, s[40:43], s74 offen offset:2048 lds\n" \
  "s_add_u32 m0, s72, 3072\n" \
  "s_mov_b32 s74, 98304\n" \
  "buffer_load_dwordx4 v24, s[40:43], s74 offen offset:0 lds\n" \
  "buffer_load_dwordx4 v25, s[40:43], s74 offen offset:1024 lds\n" \
  "buffer_load_dwordx4 v26, s[40:43], s74 offen offset:2048 lds\n" \
  "s_add_u32 m0, s72, 24576\n" \
  "s_mov_b32 s74, 0\n" \
  "buffer_load_dwordx4 v24, s[44:47], s74 offen offset:0 lds\n" \
  "buffer_load_dwordx4 v25, s[44:47], s74 offen offset:1024 lds\n" \
  "buffer_load_dwordx4 v26, s[44:47], s74 offen offset:2048 lds\n" \
  "s_add_u32 m0, s72, 27648\n" \
  "s_mov_b32 s74, 98304\n" \
  "buffer_load_dwordx4 v24, s[44:47], s74 offen offset:0 lds\n" \
  "buffer_load_dwordx4 v25, s[44:47], s74 offen offset:1024 lds\n" \
  "buffer_load_dwordx4 v26, s[44:47], s74 offen offset:2048 lds\n" \
  "s_add_u32 m0, s72, 49152\n" \
  "s_mov_b32 s74, 786432\n" \
  "buffer_load_dwordx4 v24, s[40:43], s74 offen offset:0 lds\n" \
  "buffer_load_dwordx4 v25, s[40:43], s74 offen offset:1024 lds\n" \
  "buffer_load_dwordx4 v26, s[40:43], s74 offen offset:2048 lds\n" \
  "s_add_u32 m0, s72, 52224\n" \
  "s_mov_b32 s74, 884736\n" \
  "buffer_load_dwordx4 v24, s[40:43], s74 offen offset:0 lds\n" \
  "buffer_load_dwordx4 v25, s[40:43], s74 offen offset:1024 lds\n" \
  "buffer_load_dwordx4 v26, s[40:43], s74 offen offset:2048 lds\n" \
  "s_add_u32 m0, s72, 73728\n" \
  "s_mov_b32 s74, 786432\n" \
  "buffer_load_dwordx4 v24, s[44:47], s74 offen offset:0 lds\n" \
  "buffer_load_dwordx4 v25, s[44:47], s74 offen offset:1024 lds\n" \
  "buffer_load_dwordx4 v26, s[44:47], s74 offen offset:2048 lds\n" \
  "s_add_u32 m0, s72, 76800\n" \
  "s_mov_b32 s74, 884736\n" \
  "buffer_load_dwordx4 v24, s[44:47], s74 offen offset:0 lds\n" \
  "buffer_load_dwordx4 v25, s[44:47], s74 offen offset:1024 lds\n" \
  "buffer_load_dwordx4 v26, s[44:47], s74 offen offset:2048 lds\n" \
  "s_add_u32 m0, s72, 98304\n" \
  "s_mov_b32 s74, 1572864\n" \
  "buffer_load_dwordx4 v24, s[40:43], s74 offen offset:0 lds\n" \
  "buffer_load_dwordx4 v25, s[40:43], s74 offen offset:1024 lds\n" \
  "buffer_load_dwordx4 v26, s[40:43], s74 offen offset:2048 lds\n" \
  "s_add_u32 m0, s72, 101376\n" \
  "s_mov_b32 s74, 1671168\n" \
  "buffer_load_dwordx4 v24, s[40:43], s74 offen offset:0 lds\n" \
  "buffer_load_dwordx4 v25, s[40:43], s74 offen offset:1024 lds\n" \
  "buffer_load_dwordx4 v26, s[40:43], s74 offen offset:2048 lds\n" \
  "v_add_u32 v31, 73728, v27\n" \
  "v_add_u32 v23, 73728, v30\n" \
  "s_waitcnt vmcnt(24) lgkmcnt(0)\n" \
  "s_barrier\n" \
  "s_add_u32 m0, s72, 122880\n" \
  "s_mov_b32 s74, 1572864\n" \
  "buffer_load_dwordx4 v24, s[44:47], s74 offen offset:0 lds\n" \
  "buffer_load_dwordx4 v25, s[44:47], s74 offen offset:1024 lds\n" \
  "buffer_load_dwordx4 v26, s[44:47], s74 offen offset:2048 lds\n" \
  "s_add_u32 m0, s72, 125952\n" \
  "s_mov_b32 s74, 1671168\n" \
  "buffer_load_dwordx4 v24, s[44:47], s74 offen offset:0 lds\n" \
  "buffer_load_dwordx4 v25, s[44:47], s74 offen offset:1024 lds\n" \
  "buffer_load_dwordx4 v26, s[44:47], s74 offen offset:2048 lds\n" \
  "ds_read_b64_tr_b16 v[64:65], v27 offset:0\n" \
  "ds_read_b64_tr_b16 v[66:67], v27 offset:768\n" \
  "ds_read_b64_tr_b16 v[68:69], v27 offset:12288\n" \
  "ds_read_b64_tr_b16 v[70:71], v27 offset:13056\n" \
  "ds_read_b64_tr_b16 v[72:73], v27 offset:8\n" \
  "ds_read_b64_tr_b16 v[74:75], v27 offset:776\n" \
  "ds_read_b64_tr_b16 v[76:77], v27 offset:12296\n" \
  "ds_read_b64_tr_b16 v[78:79], v27 offset:13064\n" \
  "ds_read_b64_tr_b16 a[0:1], v30 offset:0\n" \
  "ds_read_b64_tr_b16 a[2:3], v30 offset:768\n" \
  "ds_read_b64_tr_b16 a[4:5], v30 offset:12288\n" \
  "ds_read_b64_tr_b16 a[6:7], v30 offset:13056\n" \
  "s_waitcnt vmcnt(24) lgkmcnt(0)\n" \
  "s_barrier\n" \
  "s_add_u32 m0, s72, 0\n" \
  "s_mov_b32 s74, 2359296\n" \
  "buffer_load_dwordx4 v24, s[40:43], s74 offen offset:0 lds\n" \
  "buffer_load_dwordx4 v25, s[40:43], s74 offen offset:1024 lds\n" \
  "buffer_load_dwordx4 v26, s[40:43], s74 offen offset:2048 lds\n" \
  "s_add_u32 m0, s72, 3072\n" \
  "s_mov_b32 s74, 2457600\n" \
  "buffer_load_dwordx4 v24, s[40:43], s74 offen offset:0 lds\n" \
  "buffer_load_dwordx4 v25, s[40:43], s74 offen offset:1024 lds\n" \
  "buffer_load_dwordx4 v26, s[40:43], s74 offen offset:2048 lds\n" \
  "ds_read_b64_tr_b16 a[8:9], v27 offset:24576\n" \
  "ds_read_b64_tr_b16 a[10:11], v27 offset:25344\n" \
  "ds_read_b64_tr_b16 a[12:13], v27 offset:36864\n" \
  "ds_read_b64_tr_b16 a[14:15], v27 offset:37632\n" \
  "ds_read_b64_tr_b16 a[16:17], v27 offset:24584\n" \
  "ds_read_b64_tr_b16 a[18:19], v27 offset:25352\n" \
  "ds_read_b64_tr_b16 a[20:21], v27 offset:36872\n" \
  "ds_read_b64_tr_b16 a[22:23], v27 offset:37640\n" \
  "ds_read_b64_tr_b16 v[32:33], v30 offset:24576\n" \
  "ds_read_b64_tr_b16 v[34:35], v30 offset:25344\n" \
  "ds_read_b64_tr_b16 v[36:37], v30 offset:36864\n" \
  "ds_read_b64_tr_b16 v[38:39], v30 offset:37632\n" \
  "s_waitcnt vmcnt(24) lgkmcnt(0)\n" \
  "s_barrier\n" \
  "s_add_u32 m0, s72, 24576\n" \
  "s_mov_b32 s74, 2359296\n" \
  "buffer_load_dwordx4 v24, s[44:47], s74 offen offset:0 lds\n" \
  "buffer_load_dwordx4 v25, s[44:47], s74 offen offset:1024 lds\n" \
  "buffer_load_dwordx4 v26, s[44:47], s74 offen offset:2048 lds\n" \
  "s_add_u32 m0, s72, 27648\n" \
  "s_mov_b32 s74, 2457600\n" \
  "buffer_load_dwordx4 v24, s[44:47], s74 offen offset:0 lds\n" \
  "buffer_load_dwordx4 v25, s[44:47], s74 offen offset:1024 lds\n" \
  "buffer_load_dwordx4 v26, s[44:47], s74 offen offset:2048 lds\n" \
  "ds_read_b64_tr_b16 a[24:25], v27 offset:49152\n" \
  "ds_read_b64_tr_b16 a[26:27], v27 offset:49920\n" \
  "ds_read_b64_tr_b16 a[28:29], v27 offset:61440\n" \
  "ds_read_b64_tr_b16 a[30:31], v27 offset:62208\n" \
  "ds_read_b64_tr_b16 a[32:33], v27 offset:49160\n" \
  "ds_read_b64_tr_b16 a[34:35], v27 offset:49928\n" \
  "ds_read_b64_tr_b16 a[36:37], v27 offset:61448\n" \
  "ds_read_b64_tr_b16 a[38:39], v27 offset:62216\n" \
  "ds_read_b64_tr_b16 a[40:41], v30 offset:49152\n" \
  "ds_read_b64_tr_b16 a[42:43], v30 offset:49920\n" \
  "ds_read_b64_tr_b16 a[44:45], v30 offset:61440\n" \
  "ds_read_b64_tr_b16 a[46:47], v30 offset:62208\n" \
  "s_waitcnt vmcnt(24) lgkmcnt(0)\n" \
  "s_barrier\n" \
  "s_add_u32 m0, s72, 49152\n" \
  "s_mov_b32 s74, 3145728\n" \
  "buffer_load_dwordx4 v24, s[40:43], s74 offen offset:0 lds\n" \
  "buffer_load_dwordx4 v25, s[40:43], s74 offen offset:1024 lds\n" \
  "buffer_load_dwordx4 v26, s[40:43], s74 offen offset:2048 lds\n" \
  "s_add_u32 m0, s72, 52224\n" \
  "s_mov_b32 s74, 3244032\n" \
  "buffer_load_dwordx4 v24, s[40:43], s74 offen offset:0 lds\n" \
  "buffer_load_dwordx4 v25, s[40:43], s74 offen offset:1024 lds\n" \
  "buffer_load_dwordx4 v26, s[40:43], s74 offen offset:2048 lds\n" \
  "ds_read_b64_tr_b16 a[48:49], v31 offset:0\n" \
  "ds_read_b64_tr_b16 a[50:51], v31 offset:768\n" \
  "ds_read_b64_tr_b16 a[52:53], v31 offset:12288\n" \
  "ds_read_b64_tr_b16 a[54:55], v31 offset:13056\n" \
  "ds_read_b64_tr_b16 a[56:57], v31 offset:8\n" \
  "ds_read_b64_tr_b16 a[58:59], v31 offset:776\n" \
  "ds_read_b64_tr_b16 a[60:61], v31 offset:12296\n" \
  "ds_read_b64_tr_b16 a[62:63], v31 offset:13064\n" \
  "ds_read_b64_tr_b16 v[40:41], v23 offset:0\n" \
  "ds_read_b64_tr_b16 v[42:43], v23 offset:768\n" \
  "ds_read_b64_tr_b16 v[44:45], v23 offset:12288\n" \
  "ds_read_b64_tr_b16 v[46:47], v23 offset:13056\n" \
  "s_waitcnt vmcnt(24) lgkmcnt(0)\n" \
  "s_barrier\n" \
  "s_add_u32 m0, s72, 73728\n" \
  "s_mov_b32 s74, 3145728\n" \
  "buffer_load_dwordx4 v24, s[44:47], s74 offen offset:0 lds\n" \
  "buffer_load_dwordx4 v25, s[44:47], s74 offen offset:1024 lds\n" \
  "buffer_load_dwordx4 v26, s[44:47], s74 offen offset:2048 lds\n" \
  "s_add_u32 m0, s72, 76800\n" \
  "s_mov_b32 s74, 3244032\n" \
  "buffer_load_dwordx4 v24, s[44:47], s74 offen offset:0 lds\n" \
  "buffer_load_dwordx4 v25, s[44:47], s74 offen offset:1024 lds\n" \
  "buffer_load_dwordx4 v26, s[44:47], s74 offen offset:2048 lds\n" \
  "ds_read_b64_tr_b16 a[64:65], v31 offset:24576\n" \
  "ds_read_b64_tr_b16 a[66:67], v31 offset:25344\n" \
  "ds_read_b64_tr_b16 a[68:69], v31 offset:36864\n" \
  "ds_read_b64_tr_b16 a[70:71], v31 offset:37632\n" \
  "ds_read_b64_tr_b16 a[72:73], v31 offset:24584\n" \
  "ds_read_b64_tr_b16 a[74:75], v31 offset:25352\n" \
  "ds_read_b64_tr_b16 a[76:77], v31 offset:36872\n" \
  "ds_read_b64_tr_b16 a[78:79], v31 offset:37640\n" \
  "ds_read_b64_tr_b16 a[80:81], v23 offset:24576\n" \
  "ds_read_b64_tr_b16 a[82:83], v23 offset:25344\n" \
  "ds_read_b64_tr_b16 a[84:85], v23 offset:36864\n" \
  "ds_read_b64_tr_b16 a[86:87], v23 offset:37632\n" \
  "s_waitcnt vmcnt(24) lgkmcnt(0)\n" \
  "s_barrier\n" \
  "s_add_u32 m0, s72, 98304\n" \
  "s_mov_b32 s74, 3932160\n" \
  "buffer_load_dwordx4 v24, s[40:43], s74 offen offset:0 lds\n" \
  "buffer_load_dwordx4 v25, s[40:43], s74 offen offset:1024 lds\n" \
  "buffer_load_dwordx4 v26, s[40:43], s74 offen offset:2048 lds\n" \
  "s_add_u32 m0, s72, 101376\n" \
  "s_mov_b32 s74, 4030464\n" \
  "buffer_load_dwordx4 v24, s[40:43], s74 offen offset:0 lds\n" \
  "buffer_load_dwordx4 v25, s[40:43], s74 offen offset:1024 lds\n" \
  "buffer_load_dwordx4 v26, s[40:43], s74 offen offset:2048 lds\n" \
  "ds_read_b64_tr_b16 a[88:89], v31 offset:49152\n" \
  "ds_read_b64_tr_b16 a[90:91], v31 offset:49920\n" \
  "ds_read_b64_tr_b16 a[92:93], v31 offset:61440\n" \
  "ds_read_b64_tr_b16 a[94:95], v31 offset:62208\n" \
  "ds_read_b64_tr_b16 a[96:97], v31 offset:49160\n" \
  "ds_read_b64_tr_b16 a[98:99], v31 offset:49928\n" \
  "ds_read_b64_tr_b16 a[100:101], v31 offset:61448\n" \
  "ds_read_b64_tr_b16 a[102:103], v31 offset:62216\n" \
  "ds_read_b64_tr_b16 v[48:49], v23 offset:49152\n" \
  "ds_read_b64_tr_b16 v[50:51], v23 offset:49920\n" \
  "ds_read_b64_tr_b16 v[52:53], v23 offset:61440\n" \
  "ds_read_b64_tr_b16 v[54:55], v23 offset:62208\n" \
  "s_waitcnt vmcnt(24) lgkmcnt(0)\n" \
  "s_barrier\n" \
  "s_add_u32 m0, s72, 122880\n" \
  "s_mov_b32 s74, 3932160\n" \
  "buffer_load_dwordx4 v24, s[44:47], s74 offen offset:0 lds\n" \
  "buffer_load_dwordx4 v25, s[44:47], s74 offen offset:1024 lds\n" \
  "buffer_load_dwordx4 v26, s[44:47], s74 offen offset:2048 lds\n" \
  "s_add_u32 m0, s72, 125952\n" \
  "s_mov_b32 s74, 4030464\n" \
  "buffer_load_dwordx4 v24, s[44:47], s74 offen offset:0 lds\n" \
  "buffer_load_dwordx4 v25, s[44:47], s74 offen offset:1024 lds\n" \
  "buffer_load_dwordx4 v26, s[44:47], s74 offen offset:2048 lds\n" \
  "ds_read_b64_tr_b16 a[104:105], v27 offset:0\n" \
  "ds_read_b64_tr_b16 a[106:107], v27 offset:768\n" \
  "ds_read_b64_tr_b16 a[108:109], v27 offset:12288\n" \
  "ds_read_b64_tr_b16 a[110:111], v27 offset:13056\n" \
  "ds_read_b64_tr_b16 v[80:81], v27 offset:8\n" \
  "ds_read_b64_tr_b16 v[82:83], v27 offset:776\n" \
  "ds_read_b64_tr_b16 v[84:85], v27 offset:12296\n" \
  "ds_read_b64_tr_b16 v[86:87], v27 offset:13064\n" \
  "ds_read_b64_tr_b16 a[112:113], v30 offset:0\n" \
  "ds_read_b64_tr_b16 a[114:115], v30 offset:768\n" \
  "ds_read_b64_tr_b16 a[116:117], v30 offset:12288\n" \
  "ds_read_b64_tr_b16 a[118:119], v30 offset:13056\n" \
  "s_waitcnt vmcnt(24) lgkmcnt(0)\n" \
  "s_barrier\n" \
  "s_add_u32 m0, s72, 0\n" \
  "s_mov_b32 s74, 4718592\n" \
  "buffer_load_dwordx4 v24, s[40:43], s74 offen offset:0 lds\n" \
  "buffer_load_dwordx4 v25, s[40:43], s74 offen offset:1024 lds\n" \
  "buffer_load_dwordx4 v26, s[40:43], s74 offen offset:2048 lds\n" \
  "s_add_u32 m0, s72, 3072\n" \
  "s_mov_b32 s74, 4816896\n" \
  "buffer_load_dwordx4 v24, s[40:43], s74 offen offset:0 lds\n" \
  "buffer_load_dwordx4 v25, s[40:43], s74 offen offset:1024 lds\n" \
  "buffer_load_dwordx4 v26, s[40:43], s74 offen offset:2048 lds\n" \
  "ds_read_b64_tr_b16 a[120:121], v27 offset:24576\n" \
  "ds_read_b64_tr_b16 a[122:123], v27 offset:25344\n" \
  "ds_read_b64_tr_b16 a[124:125], v27 offset:36864\n" \
  "ds_read_b64_tr_b16 a[126:127], v27 offset:37632\n" \
  "ds_read_b64_tr_b16 a[128:129], v27 offset:24584\n" \
  "ds_read_b64_tr_b16 a[130:131], v27 offset:25352\n" \
  "ds_read_b64_tr_b16 a[132:133], v27 offset:36872\n" \
  "ds_read_b64_tr_b16 a[134:135], v27 offset:37640\n" \
  "ds_read_b64_tr_b16 v[56:57], v30 offset:24576\n" \
  "ds_read_b64_tr_b16 v[58:59], v30 offset:25344\n" \
  "ds_read_b64_tr_b16 v[60:61], v30 offset:36864\n" \
  "ds_read_b64_tr_b16 v[62:63], v30 offset:37632\n" \
  "s_waitcnt vmcnt(24) lgkmcnt(0)\n" \
  "s_barrier\n" \
  "s_add_u32 m0, s72, 24576\n" \
  "s_mov_b32 s74, 4718592\n" \
  "buffer_load_dwordx4 v24, s[44:47], s74 offen offset:0 lds\n" \
  "buffer_load_dwordx4 v25, s[44:47], s74 offen offset:1024 lds\n" \
  "buffer_load_dwordx4 v26, s[44:47], s74 offen offset:2048 lds\n" \
  "s_add_u32 m0, s72, 27648\n" \
  "s_mov_b32 s74, 4816896\n" \
  "buffer_load_dwordx4 v24, s[44:47], s74 offen offset:0 lds\n" \
  "buffer_load_dwordx4 v25, s[44:47], s74 offen offset:1024 lds\n" \
  "buffer_load_dwordx4 v26, s[44:47], s74 offen offset:2048 lds\n" \
  "ds_read_b64_tr_b16 v[88:89], v27 offset:49152\n" \
  "ds_read_b64_tr_b16 v[90:91], v27 offset:49920\n" \
  "ds_read_b64_tr_b16 v[92:93], v27 offset:61440\n" \
  "ds_read_b64_tr_b16 v[94:95], v27 offset:62208\n" \
  "ds_read_b64_tr_b16 a[136:137], v27 offset:49160\n" \
  "ds_read_b64_tr_b16 a[138:139], v27 offset:49928\n" \
  "ds_read_b64_tr_b16 a[140:141], v27 offset:61448\n" \
  "ds_read_b64_tr_b16 a[142:143], v27 offset:62216\n" \
  "ds_read_b64_tr_b16 a[144:145], v30 offset:49152\n" \
  "ds_read_b64_tr_b16 a[146:147], v30 offset:49920\n" \
  "ds_read_b64_tr_b16 a[148:149], v30 offset:61440\n" \
  "ds_read_b64_tr_b16 a[150:151], v30 offset:62208\n" \
  "s_waitcnt vmcnt(24) lgkmcnt(0)\n" \
  "s_barrier\n" \
  "s_add_u32 m0, s72, 49152\n" \
  "s_mov_b32 s74, 5505024\n" \
  "buffer_load_dwordx4 v24, s[40:43], s74 offen offset:0 lds\n" \
  "buffer_load_dwordx4 v25, s[40:43], s74 offen offset:1024 lds\n" \
  "buffer_load_dwordx4 v26, s[40:43], s74 offen offset:2048 lds\n" \
  "s_add_u32 m0, s72, 52224\n" \
  "s_mov_b32 s74, 5603328\n" \
  "buffer_load_dwordx4 v24, s[40:43], s74 offen offset:0 lds\n" \
  "buffer_load_dwordx4 v25, s[40:43], s74 offen offset:1024 lds\n" \
  "buffer_load_dwordx4 v26, s[40:43], s74 offen offset:2048 lds\n" \
  "ds_read_b64_tr_b16 a[152:153], v31 offset:0\n" \
  "ds_read_b64_tr_b16 a[154:155], v31 offset:768\n" \
  "ds_read_b64_tr_b16 a[156:157], v31 offset:12288\n" \
  "ds_read_b64_tr_b16 a[158:159], v31 offset:13056\n" \
  "ds_read_b64_tr_b16 a[160:161], v31 offset:8\n" \
  "ds_read_b64_tr_b16 a[162:163], v31 offset:776\n" \
  "ds_read_b64_tr_b16 a[164:165], v31 offset:12296\n" \
  "ds_read_b64_tr_b16 a[166:167], v31 offset:13064\n" \
  "ds_read_b64_tr_b16 a[168:169], v23 offset:0\n" \
  "ds_read_b64_tr_b16 a[170:171], v23 offset:768\n" \
  "ds_read_b64_tr_b16 a[172:173], v23 offset:12288\n" \
  "ds_read_b64_tr_b16 a[174:175], v23 offset:13056\n" \
  "s_waitcnt vmcnt(24) lgkmcnt(0)\n" \
  "s_barrier\n" \
  "s_add_u32 m0, s72, 73728\n" \
  "s_mov_b32 s74, 5505024\n" \
  "buffer_load_dwordx4 v24, s[44:47], s74 offen offset:0 lds\n" \
  "buffer_load_dwordx4 v25, s[44:47], s74 offen offset:1024 lds\n" \
  "buffer_load_dwordx4 v26, s[44:47], s74 offen offset:2048 lds\n" \
  "s_add_u32 m0, s72, 76800\n" \
  "s_mov_b32 s74, 5603328\n" \
  "buffer_load_dwordx4 v24, s[44:47], s74 offen offset:0 lds\n" \
  "buffer_load_dwordx4 v25, s[44:47], s74 offen offset:1024 lds\n" \
  "buffer_load_dwordx4 v26, s[44:47], s74 offen offset:2048 lds\n" \
  "ds_read_b64_tr_b16 v[96:97], v31 offset:24576\n" \
  "ds_read_b64_tr_b16 v[98:99], v31 offset:25344\n" \
  "ds_read_b64_tr_b16 v[100:101], v31 offset:36864\n" \
  "ds_read_b64_tr_b16 v[102:103], v31 offset:37632\n" \
  "ds_read_b64_tr_b16 a[176:177], v31 offset:24584\n" \
  "ds_read_b64_tr_b16 a[178:179], v31 offset:25352\n" \
  "ds_read_b64_tr_b16 a[180:181], v31 offset:36872\n" \
  "ds_read_b64_tr_b16 a[182:183], v31 offset:37640\n" \
  "ds_read_b64_tr_b16 a[184:185], v23 offset:24576\n" \
  "ds_read_b64_tr_b16 a[186:187], v23 offset:25344\n" \
  "ds_read_b64_tr_b16 a[188:189], v23 offset:36864\n" \
  "ds_read_b64_tr_b16 a[190:191], v23 offset:37632\n" \
  "s_waitcnt vmcnt(24) lgkmcnt(0)\n" \
  "s_barrier\n" \
  "s_add_u32 m0, s72, 98304\n" \
  "s_mov_b32 s74, 6291456\n" \
  "buffer_load_dwordx4 v24, s[40:43], s74 offen offset:0 lds\n" \
  "buffer_load_dwordx4 v25, s[40:43], s74 offen offset:1024 lds\n" \
  "buffer_load_dwordx4 v26, s[40:43], s74 offen offset:2048 lds\n" \
  "s_add_u32 m0, s72, 101376\n" \
  "s_mov_b32 s74, 6389760\n" \
  "buffer_load_dwordx4 v24, s[40:43], s74 offen offset:0 lds\n" \
  "buffer_load_dwordx4 v25, s[40:43], s74 offen offset:1024 lds\n" \
  "buffer_load_dwordx4 v26, s[40:43], s74 offen offset:2048 lds\n" \
  "ds_read_b64_tr_b16 a[192:193], v31 offset:49152\n" \
  "ds_read_b64_tr_b16 a[194:195], v31 offset:49920\n" \
  "ds_read_b64_tr_b16 a[196:197], v31 offset:61440\n" \
  "ds_read_b64_tr_b16 a[198:199], v31 offset:62208\n" \
  "ds_read_b64_tr_b16 a[200:201], v31 offset:49160\n" \
  "ds_read_b64_tr_b16 a[202:203], v31 offset:49928\n" \
  "ds_read_b64_tr_b16 a[204:205], v31 offset:61448\n" \
  "ds_read_b64_tr_b16 a[206:207], v31 offset:62216\n" \
  "ds_read_b64_tr_b16 a[208:209], v23 offset:49152\n" \
  "ds_read_b64_tr_b16 a[210:211], v23 offset:49920\n" \
  "ds_read_b64_tr_b16 a[212:213], v23 offset:61440\n" \
  "ds_read_b64_tr_b16 a[214:215], v23 offset:62208\n" \
  "s_waitcnt vmcnt(24) lgkmcnt(0)\n" \
  "s_barrier\n" \
  "s_add_u32 m0, s72, 122880\n" \
  "s_mov_b32 s74, 6291456\n" \
  "buffer_load_dwordx4 v24, s[44:47], s74 offen offset:0 lds\n" \
  "buffer_load_dwordx4 v25, s[44:47], s74 offen offset:1024 lds\n" \
  "buffer_load_dwordx4 v26, s[44:47], s74 offen offset:2048 lds\n" \
  "s_add_u32 m0, s72, 125952\n" \
  "s_mov_b32 s74, 6389760\n" \
  "buffer_load_dwordx4 v24, s[44:47], s74 offen offset:0 lds\n" \
  "buffer_load_dwordx4 v25, s[44:47], s74 offen offset:1024 lds\n" \
  "buffer_load_dwordx4 v26, s[44:47], s74 offen offset:2048 lds\n" \
  "ds_read_b64_tr_b16 a[216:217], v27 offset:0\n" \
  "ds_read_b64_tr_b16 a[218:219], v27 offset:768\n" \
  "ds_read_b64_tr_b16 a[220:221], v27 offset:12288\n" \
  "ds_read_b64_tr_b16 a[222:223], v27 offset:13056\n" \
  "ds_read_b64_tr_b16 a[224:225], v27 offset:8\n" \
  "ds_read_b64_tr_b16 a[226:227], v27 offset:776\n" \
  "ds_read_b64_tr_b16 a[228:229], v27 offset:12296\n" \
  "ds_read_b64_tr_b16 a[230:231], v27 offset:13064\n" \
  "ds_read_b64_tr_b16 a[232:233], v30 offset:0\n" \
  "ds_read_b64_tr_b16 a[234:235], v30 offset:768\n" \
  "ds_read_b64_tr_b16 a[236:237], v30 offset:12288\n" \
  "ds_read_b64_tr_b16 a[238:239], v30 offset:13056\n" \
  "s_waitcnt vmcnt(24) lgkmcnt(0)\n" \
  "s_barrier\n" \
  "s_add_u32 m0, s72, 0\n" \
  "s_mov_b32 s74, 7077888\n" \
  "buffer_load_dwordx4 v24, s[40:43], s74 offen offset:0 lds\n" \
  "buffer_load_dwordx4 v25, s[40:43], s74 offen offset:1024 lds\n" \
  "buffer_load_dwordx4 v26, s[40:43], s74 offen offset:2048 lds\n" \
  "s_add_u32 m0, s72, 3072\n" \
  "s_mov_b32 s74, 7176192\n" \
  "buffer_load_dwordx4 v24, s[40:43], s74 offen offset:0 lds\n" \
  "buffer_load_dwordx4 v25, s[40:43], s74 offen offset:1024 lds\n" \
  "buffer_load_dwordx4 v26, s[40:43], s74 offen offset:2048 lds\n" \
  "ds_read_b64_tr_b16 a[240:241], v27 offset:24576\n" \
  "ds_read_b64_tr_b16 a[242:243], v27 offset:25344\n" \
  "ds_read_b64_tr_b16 a[244:245], v27 offset:36864\n" \
  "ds_read_b64_tr_b16 a[246:247], v27 offset:37632\n" \
  "ds_read_b64_tr_b16 a[248:249], v27 offset:24584\n" \
  "ds_read_b64_tr_b16 a[250:251], v27 offset:25352\n" \
  "ds_read_b64_tr_b16 a[252:253], v27 offset:36872\n" \
  "ds_read_b64_tr_b16 a[254:255], v27 offset:37640\n" \
  "ds_read_b64_tr_b16 v[120:121], v30 offset:24576\n" \
  "ds_read_b64_tr_b16 v[122:123], v30 offset:25344\n" \
  "ds_read_b64_tr_b16 v[124:125], v30 offset:36864\n" \
  "ds_read_b64_tr_b16 v[126:127], v30 offset:37632\n" \
  "s_waitcnt vmcnt(24) lgkmcnt(0)\n" \
  "s_barrier\n" \
  "s_add_u32 m0, s72, 24576\n" \
  "s_mov_b32 s74, 7077888\n" \
  "buffer_load_dwordx4 v24, s[44:47], s74 offen offset:0 lds\n" \
  "buffer_load_dwordx4 v25, s[44:47], s74 offen offset:1024 lds\n" \
  "buffer_load_dwordx4 v26, s[44:47], s74 offen offset:2048 lds\n" \
  "s_add_u32 m0, s72, 27648\n" \
  "s_mov_b32 s74, 7176192\n" \
  "buffer_load_dwordx4 v24, s[44:47], s74 offen offset:0 lds\n" \
  "buffer_load_dwordx4 v25, s[44:47], s74 offen offset:1024 lds\n" \
  "buffer_load_dwordx4 v26, s[44:47], s74 offen offset:2048 lds\n" \
  "ds_read_b64_tr_b16 v[128:129], v27 offset:49152\n" \
  "ds_read_b64_tr_b16 v[130:131], v27 offset:49920\n" \
  "ds_read_b64_tr_b16 v[132:133], v27 offset:61440\n" \
  "ds_read_b64_tr_b16 v[134:135], v27 offset:62208\n" \
  "ds_read_b64_tr_b16 v[112:113], v27 offset:49160\n" \
  "ds_read_b64_tr_b16 v[114:115], v27 offset:49928\n" \
  "ds_read_b64_tr_b16 v[116:117], v27 offset:61448\n" \
  "ds_read_b64_tr_b16 v[118:119], v27 offset:62216\n" \
  "ds_read_b64_tr_b16 v[136:137], v30 offset:49152\n" \
  "ds_read_b64_tr_b16 v[138:139], v30 offset:49920\n" \
  "ds_read_b64_tr_b16 v[140:141], v30 offset:61440\n" \
  "ds_read_b64_tr_b16 v[142:143], v30 offset:62208\n" \
  "s_waitcnt vmcnt(24) lgkmcnt(0)\n" \
  "s_barrier\n" \
  "ds_read_b64_tr_b16 v[144:145], v31 offset:0\n" \
  "ds_read_b64_tr_b16 v[146:147], v31 offset:768\n" \
  "ds_read_b64_tr_b16 v[148:149], v31 offset:12288\n" \
  "ds_read_b64_tr_b16 v[150:151], v31 offset:13056\n" \
  "ds_read_b64_tr_b16 v[152:153], v31 offset:8\n" \
  "ds_read_b64_tr_b16 v[154:155], v31 offset:776\n" \
  "ds_read_b64_tr_b16 v[156:157], v31 offset:12296\n" \
  "ds_read_b64_tr_b16 v[158:159], v31 offset:13064\n" \
  "ds_read_b64_tr_b16 v[160:161], v23 offset:0\n" \
  "ds_read_b64_tr_b16 v[162:163], v23 offset:768\n" \
  "ds_read_b64_tr_b16 v[164:165], v23 offset:12288\n" \
  "ds_read_b64_tr_b16 v[166:167], v23 offset:13056\n" \
  "s_waitcnt vmcnt(18) lgkmcnt(0)\n" \
  "s_barrier\n" \
  "s_getpc_b64 s[82:83]\n" \
  "LCF1_%=:\n" \
  "s_add_u32 s82, s82, LSTARTs0_%=-LCF1_%=\n" \
  "s_addc_u32 s83, s83, 0\n" \
  "v_mbcnt_lo_u32_b32 v5, -1, 0\n" \
  "v_mbcnt_hi_u32_b32 v5, -1, v5\n" \
  "v_lshlrev_b32 v5, 6, v5\n" \
  "global_load_dword v10, v5, s[82:83]\n" \
  "v_add_u32 v5, 4096, v5\n" \
  "global_load_dword v10, v5, s[82:83]\n" \
  "v_add_u32 v5, 4096, v5\n" \
  "global_load_dword v10, v5, s[82:83]\n" \
  "s_getpc_b64 s[82:83]\n" \
  "LCF2_%=:\n" \
  "s_add_u32 s82, s82, LHOT_%=-4096-LCF2_%=\n" \
  "s_addc_u32 s83, s83, 0\n" \
  "v_mbcnt_lo_u32_b32 v5, -1, 0\n" \
  "v_mbcnt_hi_u32_b32 v5, -1, v5\n" \
  "v_lshlrev_b32 v5, 6, v5\n" \
  "global_load_dword v10, v5, s[82:83]\n" \
  "ds_read_b64_tr_b16 v[168:169], v31 offset:24576\n" \
  "ds_read_b64_tr_b16 v[170:171], v31 offset:25344\n" \
  "ds_read_b64_tr_b16 v[172:173], v31 offset:36864\n" \
  "ds_read_b64_tr_b16 v[174:175], v31 offset:37632\n" \
  "ds_read_b64_tr_b16 v[176:177], v31 offset:24584\n" \
  "ds_read_b64_tr_b16 v[178:179], v31 offset:25352\n" \
  "ds_read_b64_tr_b16 v[180:181], v31 offset:36872\n" \
  "ds_read_b64_tr_b16 v[182:183], v31 offset:37640\n" \
  "ds_read_b64_tr_b16 v[184:185], v23 offset:24576\n" \
  "ds_read_b64_tr_b16 v[186:187], v23 offset:25344\n" \
  "ds_read_b64_tr_b16 v[188:189], v23 offset:36864\n" \
  "ds_read_b64_tr_b16 v[190:191], v23 offset:37632\n" \
  "s_waitcnt vmcnt(16) lgkmcnt(0)\n" \
  "s_barrier\n" \
  "ds_read_b64_tr_b16 v[192:193], v31 offset:49152\n" \
  "ds_read_b64_tr_b16 v[194:195], v31 offset:49920\n" \
  "ds_read_b64_tr_b16 v[196:197], v31 offset:61440\n" \
  "ds_read_b64_tr_b16 v[198:199], v31 offset:62208\n" \
  "ds_read_b64_tr_b16 v[200:201], v31 offset:49160\n" \
  "ds_read_b64_tr_b16 v[202:203], v31 offset:49928\n" \
  "ds_read_b64_tr_b16 v[204:205], v31 offset:61448\n" \
  "ds_read_b64_tr_b16 v[206:207], v31 offset:62216\n" \
  "ds_read_b64_tr_b16 v[208:209], v23 offset:49152\n" \
  "ds_read_b64_tr_b16 v[210:211], v23 offset:49920\n" \
  "ds_read_b64_tr_b16 v[212:213], v23 offset:61440\n" \
  "ds_read_b64_tr_b16 v[214:215], v23 offset:62208\n" \
  "s_waitcnt vmcnt(10) lgkmcnt(0)\n" \
  "s_barrier\n" \
  "ds_read_b64_tr_b16 v[104:105], v27 offset:0\n" \
  "ds_read_b64_tr_b16 v[106:107], v27 offset:768\n" \
  "ds_read_b64_tr_b16 v[108:109], v27 offset:12288\n" \
  "ds_read_b64_tr_b16 v[110:111], v27 offset:13056\n" \
  "ds_read_b64_tr_b16 v[216:217], v27 offset:8\n" \
  "ds_read_b64_tr_b16 v[218:219], v27 offset:776\n" \
  "ds_read_b64_tr_b16 v[220:221], v27 offset:12296\n" \
  "ds_read_b64_tr_b16 v[222:223], v27 offset:13064\n" \
  "ds_read_b64_tr_b16 v[224:225], v30 offset:0\n" \
  "ds_read_b64_tr_b16 v[226:227], v30 offset:768\n" \
  "ds_read_b64_tr_b16 v[228:229], v30 offset:12288\n" \
  "ds_read_b64_tr_b16 v[230:231], v30 offset:13056\n" \
  "s_waitcnt vmcnt(0) lgkmcnt(0)\n" \
  "s_barrier\n" \
  "ds_read_b64_tr_b16 v[232:233], v27 offset:24576\n" \
  "ds_read_b64_tr_b16 v[234:235], v27 offset:25344\n" \
  "ds_read_b64_tr_b16 v[236:237], v27 offset:36864\n" \
  "ds_read_b64_tr_b16 v[238:239], v27 offset:37632\n" \
  "ds_read_b64_tr_b16 v[240:241], v27 offset:24584\n" \
  "ds_read_b64_tr_b16 v[242:243], v27 offset:25352\n" \
  "ds_read_b64_tr_b16 v[244:245], v27 offset:36872\n" \
  "ds_read_b64_tr_b16 v[246:247], v27 offset:37640\n" \
  "ds_read_b64_tr_b16 v[248:249], v30 offset:24576\n" \
  "ds_read_b64_tr_b16 v[250:251], v30 offset:25344\n" \
  "ds_read_b64_tr_b16 v[252:253], v30 offset:36864\n" \
  "ds_read_b64_tr_b16 v[254:255], v30 offset:37632\n" \
  "s_waitcnt lgkmcnt(0)\n" \
  "s_barrier\n" \
  "ds_write_b128 v3, v[32:35] offset:0\n" \
  "ds_write_b128 v3, v[36:39] offset:1024\n" \
  "ds_write_b128 v3, v[40:43] offset:2048\n" \
  "ds_write_b128 v3, v[44:47] offset:3072\n" \
  "ds_write_b128 v3, v[48:51] offset:4096\n" \
  "ds_write_b128 v3, v[52:55] offset:5120\n" \
  "ds_write_b128 v3, v[56:59] offset:6144\n" \
  "ds_write_b128 v3, v[60:63] offset:7168\n" \
  "ds_write_b128 v3, v[64:67] offset:8192\n" \
  "ds_write_b128 v3, v[68:71] offset:9216\n" \
  "ds_write_b128 v3, v[72:75] offset:10240\n" \
  "ds_write_b128 v3, v[76:79] offset:11264\n" \
  "ds_write_b128 v3, v[80:83] offset:12288\n" \
  "ds_write_b128 v3, v[84:87] offset:13312\n" \
  "ds_write_b128 v3, v[88:91] offset:14336\n" \
  "s_waitcnt lgkmcnt(8)\n" \
  "ds_write_b128 v3, v[92:95] offset:15360\n" \
  "ds_write_b128 v3, v[96:99] offset:16384\n" \
  "ds_write_b128 v3, v[100:103] offset:17408\n" \
  "ds_write_b128 v3, v[104:107] offset:18432\n" \
  "ds_write_b128 v3, v[108:111] offset:19456\n" \
  "ds_write_b128 v3, v[112:115] offset:20480\n" \
  "ds_write_b128 v3, v[116:119] offset:21504\n" \
  "s_waitcnt lgkmcnt(0)\n" \
  "s_cmp_eq_u32 s52, 1\n" \
  "s_cbranch_scc1 LSTARTs1_%=\n" \
  "s_cmp_eq_u32 s52, 2\n" \
  "s_cbranch_scc1 LSTARTs2_%=\n" \
  "s_cmp_eq_u32 s52, 3\n" \
  "s_cbranch_scc1 LSTARTs3_%=\n" \
  "LSTARTs0_%=:\n" \
  "v_mov_b32 v5, v20\n" \
  "v_mov_b32 v10, v21\n" \
  "v_mov_b32 v16, v22\n" \
  "s_mov_b32 s79, 0\n" \
  "LSDs0_%=:\n" \
  "buffer_load_dwordx2 v[64:65], v19, s[36:39], 0 offen sc1\n" \
  "s_waitcnt vmcnt(0)\n" \
  "v_readlane_b32 s76, v64, 3\n" \
  "s_nop 0\n" \
  "s_add_u32 s76, s76, 1\n" \
  "v_cmp_ne_u32 vcc, s76, v64\n" \
  "s_nop 3\n" \
  "s_and_b32 s76, vcc_lo, 7\n" \
  "s_cbranch_scc0 LSFs0_%=\n" \
  "s_add_u32 s79, s79, 1\n" \
  "s_cmp_gt_u32 s79, 200000\n" \
  "s_cbranch_scc1 LSFs0_%=\n" \
  "s_sleep 4\n" \
  "s_branch LSDs0_%=\n" \
  "LSFs0_%=:\n" \
  "v_readlane_b32 s76, v65, 0\n" \
  "v_readlane_b32 s77, v65, 1\n" \
  "v_readlane_b32 s78, v65, 2\n" \
  "s_nop 1\n" \
  "v_lshrrev_b32 v64, 21, v7\n" \
  "v_lshrrev_b64 v[66:67], v64, s[76:77]\n" \
  "v_lshrrev_b32_e64 v68, v64, s78\n" \
  "v_cmp_gt_u32 vcc, 64, v64\n" \
  "s_nop 1\n" \
  "v_cndmask_b32 v66, v68, v66, vcc\n" \
  "v_and_b32 v66, 1, v66\n" \
  "v_lshlrev_b32 v66, 13, v66\n" \
  "v_xor_b32 v7, v7, v66\n" \
  "v_lshrrev_b32 v64, 21, v11\n" \
  "v_lshrrev_b64 v[66:67], v64, s[76:77]\n" \
  "v_lshrrev_b32_e64 v68, v64, s78\n" \
  "v_cmp_gt_u32 vcc, 64, v64\n" \
  "s_nop 1\n" \
  "v_cndmask_b32 v66, v68, v66, vcc\n" \
  "v_and_b32 v66, 1, v66\n" \
  "v_lshlrev_b32 v66, 13, v66\n" \
  "v_xor_b32 v11, v11, v66\n" \
  "v_lshrrev_b32 v64, 21, v28\n" \
  "v_lshrrev_b64 v[66:67], v64, s[76:77]\n" \
  "v_lshrrev_b32_e64 v68, v64, s78\n" \
  "v_cmp_gt_u32 vcc, 64, v64\n" \
  "s_nop 1\n" \
  "v_cndmask_b32 v66, v68, v66, vcc\n" \
  "v_and_b32 v66, 1, v66\n" \
  "v_lshlrev_b32 v66, 13, v66\n" \
  "v_xor_b32 v28, v28, v66\n" \
  "v_lshrrev_b32 v64, 21, v18\n" \
  "v_lshrrev_b64 v[66:67], v64, s[76:77]\n" \
  "v_lshrrev_b32_e64 v68, v64, s78\n" \
  "v_cmp_gt_u32 vcc, 64, v64\n" \
  "s_nop 1\n" \
  "v_cndmask_b32 v66, v68, v66, vcc\n" \
  "v_and_b32 v66, 1, v66\n" \
  "v_lshlrev_b32 v66, 13, v66\n" \
  "v_xor_b32 v18, v18, v66\n" \
  "s_lshl_b32 s74, s76, 12\n" \
  "s_and_b32 s74, s74, 0x2000\n" \
  "s_or_b32 s55, s55, s74\n" \
  "v_mov_b32 v17, v28\n" \
  "s_mov_b32 s47, s38\n" \
  "s_mov_b32 s38, 0\n" \
  "s_mov_b32 s68, 63\n" \
  "s_mov_b32 s73, 20001\n" \
  "s_mov_b32 s56, 19999\n" \
  "s_mov_b32 s97, s54\n" \
  "s_mov_b32 s54, 0\n" \
  "s_mov_b32 s98, s96\n" \
  "s_mov_b32 s96, 0\n" \
  "LPASSs0_%=:\n" \
  "s_mov_b32 s40, 0\n" \
  "s_mov_b32 s42, 0x40000000\n" \
  "v_and_b32 v5, 0x7fff, v5\n" \
  "v_and_b32 v10, 0x7fff, v10\n" \
  "v_mov_b32 v64, 0\n" \
  "ds_write_b32 v12, v64 offset:512\n" \
  "s_waitcnt lgkmcnt(0)\n" \
  "LQGs0_%=:\n" \
  "buffer_load_dword v19, v18, s[36:39], 0 offen sc1\n" \
  "s_waitcnt vmcnt(0)\n" \
  "v_cmp_ge_u32 vcc, 0, v19\n" \
  "s_nop 3\n" \
  "s_cbranch_vccz LRGs0_%=\n" \
  "s_add_u32 s73, s73, 1\n" \
  "s_cmp_gt_u32 s73, 20000\n" \
  "s_cbranch_scc1 LRGs0_%=\n" \
  "s_sleep 4\n" \
  "s_branch LQGs0_%=\n" \
  "LRGs0_%=:\n" \
  "v_mov_b32 v64, 1\n" \
  "buffer_atomic_add v64, v17, s[36:39], 0 offen sc1\n" \
  "s_barrier\n" \
  "s_add_u32 m0, s40, 0\n" \
  "s_add_u32 s75, s55, 0\n" \
  "buffer_load_dwordx4 v8, s[36:39], s75 offen offset:0 sc1 lds\n" \
  "buffer_load_dwordx4 v8, s[36:39], s75 offen offset:1024 sc1 lds\n" \
  "buffer_load_dwordx4 v8, s[36:39], s75 offen offset:2048 sc1 lds\n" \
  "buffer_load_dwordx4 v8, s[36:39], s75 offen offset:3072 sc1 lds\n" \
  "s_add_u32 m0, s40, 4096\n" \
  "s_add_u32 s75, s55, 4096\n" \
  "buffer_load_dwordx4 v8, s[36:39], s75 offen offset:0 sc1 lds\n" \
  "buffer_load_dwordx4 v8, s[36:39], s75 offen offset:1024 sc1 lds\n" \
  "s_xor_b32 s40, s40, 0x8000\n" \
  "LQHs0_%=:\n" \
  "buffer_load_dword v19, v18, s[36:39], 0 offen sc1\n" \
  "s_waitcnt vmcnt(0)\n" \
  "v_cmp_ge_u32 vcc, 1, v19\n" \
  "s_nop 3\n" \
  "s_cbranch_vccz LRHs0_%=\n" \
  "s_add_u32 s73, s73, 1\n" \
  "s_cmp_gt_u32 s73, 20000\n" \
  "s_cbranch_scc1 LRHs0_%=\n" \
  "s_sleep 2\n" \
  "s_branch LQHs0_%=\n" \
  "LRHs0_%=:\n" \
  "v_readlane_b32 s72, v11, 0\n" \
  "s_nop 0\n" \
  "s_add_u32 s69, s72, 2048\n" \
  "s_add_u32 s70, s72, 6144\n" \
  "s_add_u32 s44, s72, s93\n" \
  "s_add_u32 s45, s72, s94\n" \
  "s_add_u32 s46, s72, s95\n" \
  "v_readlane_b32 s41, v7, 0\n" \
  "s_nop 1\n" \
  "ds_read_b128 v[88:91], v3 offset:8192\n" \
  "ds_read_b128 v[92:95], v3 offset:9216\n" \
  "ds_read_b128 v[96:99], v3 offset:10240\n" \
  "ds_read_b128 v[100:103], v3 offset:11264\n" \
  "ds_read_b128 v[104:107], v3 offset:0\n" \
  "ds_read_b128 v[108:111], v3 offset:1024\n" \
  "ds_read_b128 v[112:115], v3 offset:22528\n" \
  "ds_read_b128 v[116:119], v3 offset:23552\n" \
  "v_mov_b32 v60, -1\n" \
  "v_mov_b32 v61, -1\n" \
  "ds_write_b64 v6, v[60:61] offset:32768\n" \
  "ds_write_b64 v6, v[60:61] offset:33280\n" \
  "ds_write_b64 v6, v[60:61] offset:36864\n" \
  "ds_write_b64 v2, v[60:61] offset:0\n" \
  "v_mov_b32 v20, 0\n" \
  "v_mov_b32 v21, 0\n" \
  "v_mov_b32 v22, 0\n" \
  "v_mov_b32 v23, 0\n" \
  "v_mov_b32 v64, 0\n" \
  "v_mov_b32 v65, 0\n" \
  "v_mov_b32 v66, 0\n" \
  "v_mov_b32 v67, 0\n" \
  "v_mov_b32 v68, 0\n" \
  "v_mov_b32 v69, 0\n" \
  "v_mov_b32 v70, 0\n" \
  "v_mov_b32 v71, 0\n" \
  "v_mov_b32 v72, 0\n" \
  "v_mov_b32 v73, 0\n" \
  "v_mov_b32 v74, 0\n" \
  "v_mov_b32 v75, 0\n" \
  "v_mov_b32 v76, 0\n" \
  "v_mov_b32 v77, 0\n" \
  "v_mov_b32 v78, 0\n" \
  "v_mov_b32 v79, 0\n" \
  "v_mov_b32 v80, 0\n" \
  "v_mov_b32 v81, 0\n" \
  "v_mov_b32 v82, 0\n" \
  "v_mov_b32 v83, 0\n" \
  "v_mov_b32 v84, 0\n" \
  "v_mov_b32 v85, 0\n" \
  "v_mov_b32 v86, 0\n" \
  "v_mov_b32 v87, 0\n" \
  "s_waitcnt lgkmcnt(0)\n" \
  "s_cmp_eq_u32 s38, 0\n" \
  "s_cselect_b32 s74, -1, 0\n" \
  "v_mov_b32 v64, s74\n" \
  "LOOPs0_%=:\n" \
  "s_barrier\n" \
  "buffer_load_dwordx4 v[24:27], v1, s[36:39], s72 offen offset:0 sc1\n" \
  "s_mov_b32 m0, 65536\n" \
  "s_nop 0\n" \
  "buffer_load_dwordx4 v1, s[36:39], s69 offen sc1 lds\n" \
  "buffer_load_dwordx4 v[28:31], v1, s[36:39], s72 offen offset:1024 sc1\n" \
  "s_mov_b32 m0, 69632\n" \
  "s_nop 0\n" \
  "buffer_load_dwordx4 v1, s[36:39], s70 offen sc1 lds\n" \
  "ds_write_b64 v6, v[60:61] offset:0\n" \
  "ds_write_b64 v6, v[60:61] offset:512\n" \
  "ds_write_b64 v6, v[60:61] offset:4096\n" \
  "s_add_u32 s74, s68, 2\n" \
  "s_min_u32 s43, s74, 63\n" \
  "s_add_u32 s68, s68, 1\n" \
  "LMs0_%=:\n" \
  "s_memtime s[76:77]\n" \
  "s_waitcnt vmcnt(3)\n" \
  "v_smfmac_f32_16x16x64_bf16 v[64:67], v[24:27], v[88:95], v0\n" \
  "ds_read_b128 v[88:91], v3 offset:2048\n" \
  "v_smfmac_f32_16x16x64_bf16 v[68:71], v[24:27], v[96:103], v0\n" \
  "ds_read_b128 v[92:95], v3 offset:3072\n" \
  "v_smfmac_f32_16x16x64_bf16 v[72:75], v[24:27], a[0:7], v0\n" \
  "ds_read_b128 v[96:99], v3 offset:24576\n" \
  "v_smfmac_f32_16x16x64_bf16 v[76:79], v[24:27], a[8:15], v0\n" \
  "ds_read_b128 v[100:103], v3 offset:25600\n" \
  "v_smfmac_f32_16x16x64_bf16 v[80:83], v[24:27], a[16:23], v0\n" \
  "v_smfmac_f32_16x16x64_bf16 v[84:87], v[24:27], v[104:111], v0\n" \
  "ds_read_b128 v[104:107], v3 offset:12288\n" \
  "v_smfmac_f32_16x16x64_bf16 v[20:23], v[24:27], v[112:119], v0\n" \
  "s_waitcnt vmcnt(2)\n" \
  "s_waitcnt lgkmcnt(5)\n" \
  "s_barrier\n" \
  "ds_read_b128 v[32:35], v2 offset:2048\n" \
  "s_waitcnt vmcnt(1)\n" \
  "v_smfmac_f32_16x16x64_bf16 v[64:67], v[28:31], a[24:31], v0\n" \
  "ds_read_b128 v[36:39], v2 offset:3072\n" \
  "v_smfmac_f32_16x16x64_bf16 v[68:71], v[28:31], a[32:39], v0\n" \
  "ds_read_b128 v[40:43], v2 offset:4096\n" \
  "v_smfmac_f32_16x16x64_bf16 v[72:75], v[28:31], a[40:47], v0\n" \
  "ds_read_b128 v[44:47], v2 offset:5120\n" \
  "s_waitcnt lgkmcnt(7)\n" \
  "v_smfmac_f32_16x16x64_bf16 v[84:87], v[28:31], v[88:95], v0\n" \
  "ds_read_b128 v[108:111], v3 offset:13312\n" \
  "s_waitcnt lgkmcnt(6)\n" \
  "v_smfmac_f32_16x16x64_bf16 v[20:23], v[28:31], v[96:103], v0\n" \
  "ds_read_b128 v[112:115], v3 offset:14336\n" \
  "s_waitcnt lgkmcnt(5)\n" \
  "v_smfmac_f32_16x16x64_bf16 v[64:67], v[32:35], a[64:71], v0\n" \
  "ds_read_b128 v[116:119], v3 offset:15360\n" \
  "v_smfmac_f32_16x16x64_bf16 v[68:71], v[32:35], a[72:79], v0\n" \
  "ds_read_b128 v[88:91], v3 offset:16384\n" \
  "v_smfmac_f32_16x16x64_bf16 v[72:75], v[32:35], a[80:87], v0\n" \
  "ds_read_b128 v[92:95], v3 offset:17408\n" \
  "s_waitcnt lgkmcnt(7)\n" \
  "v_smfmac_f32_16x16x64_bf16 v[64:67], v[36:39], a[104:111], v0\n" \
  "ds_read_b128 v[96:99], v3 offset:18432\n" \
  "s_waitcnt lgkmcnt(5)\n" \
  "v_smfmac_f32_16x16x64_bf16 v[68:71], v[36:39], v[104:111], v0\n" \
  "s_waitcnt vmcnt(0)\n" \
  "s_barrier\n" \
  "v_smfmac_f32_16x16x64_bf16 v[72:75], v[36:39], a[112:119], v0\n" \
  "ds_read_b128 v[48:51], v2 offset:6144\n" \
  "s_waitcnt lgkmcnt(4)\n" \
  "v_smfmac_f32_16x16x64_bf16 v[64:67], v[40:43], v[112:119], v0\n" \
  "ds_read_b128 v[52:55], v2 offset:7168\n" \
  "v_smfmac_f32_16x16x64_bf16 v[68:71], v[40:43], a[136:143], v0\n" \
  "ds_read_b128 v[56:59], v2 offset:8192\n" \
  "v_smfmac_f32_16x16x64_bf16 v[72:75], v[40:43], a[144:151], v0\n" \
  "ds_read_b128 v[60:63], v2 offset:9216\n" \
  "s_waitcnt lgkmcnt(5)\n" \
  "v_smfmac_f32_16x16x64_bf16 v[64:67], v[44:47], v[88:95], v0\n" \
  "ds_read_b128 v[100:103], v3 offset:19456\n" \
  "v_smfmac_f32_16x16x64_bf16 v[68:71], v[44:47], a[176:183], v0\n" \
  "ds_read_b128 v[104:107], v3 offset:20480\n" \
  "v_smfmac_f32_16x16x64_bf16 v[72:75], v[44:47], a[184:191], v0\n" \
  "ds_read_b128 v[108:111], v3 offset:21504\n" \
  "s_waitcnt lgkmcnt(6)\n" \
  "v_smfmac_f32_16x16x64_bf16 v[64:67], v[48:51], a[216:223], v0\n" \
  "ds_read_b128 v[112:115], v3 offset:26624\n" \
  "s_waitcnt lgkmcnt(6)\n" \
  "v_smfmac_f32_16x16x64_bf16 v[64:67], v[52:55], v[128:135], v0\n" \
  "ds_read_b128 v[116:119], v3 offset:27648\n" \
  "s_waitcnt lgkmcnt(6)\n" \
  "v_smfmac_f32_16x16x64_bf16 v[64:67], v[56:59], v[168:175], v0\n" \
  "ds_read_b128 v[88:91], v3 offset:28672\n" \
  "s_waitcnt lgkmcnt(5)\n" \
  "v_smfmac_f32_16x16x64_bf16 v[64:67], v[60:63], v[96:103], v0\n" \
  "ds_read_b128 v[92:95], v3 offset:29696\n" \
  "v_smfmac_f32_16x16x64_bf16 v[68:71], v[48:51], a[224:231], v0\n" \
  "ds_read_b128 v[96:99], v3 offset:4096\n" \
  "s_waitcnt lgkmcnt(5)\n" \
  "v_smfmac_f32_16x16x64_bf16 v[68:71], v[52:55], v[104:111], v0\n" \
  "buffer_load_dword v19, v18, s[36:39], 0 offen sc1\n" \
  "v_cvt_pk_bf16_f32 v64, v64, v65\n" \
  "v_smfmac_f32_16x16x64_bf16 v[68:71], v[56:59], v[176:183], v0\n" \
  "ds_read_b128 v[100:103], v3 offset:5120\n" \
  "v_cvt_pk_bf16_f32 v65, v66, v67\n" \
  "v_smfmac_f32_16x16x64_bf16 v[68:71], v[60:63], v[216:223], v0\n" \
  "ds_write_b64 v6, v[64:65] offset:32768\n" \
  "v_cmp_u_f32 vcc, v64, v65\n" \
  "v_smfmac_f32_16x16x64_bf16 v[72:75], v[48:51], a[232:239], v0\n" \
  "s_cbranch_vccnz LMQs0_%=\n" \
  "v_smfmac_f32_16x16x64_bf16 v[72:75], v[52:55], v[136:143], v0\n" \
  "ds_read_b128 v[104:107], v3 offset:6144\n" \
  "v_cvt_pk_bf16_f32 v68, v68, v69\n" \
  "v_smfmac_f32_16x16x64_bf16 v[72:75], v[56:59], v[184:191], v0\n" \
  "ds_read_b128 v[108:111], v3 offset:7168\n" \
  "v_cvt_pk_bf16_f32 v69, v70, v71\n" \
  "v_smfmac_f32_16x16x64_bf16 v[72:75], v[60:63], v[224:231], v0\n" \
  "ds_write_b64 v6, v[68:69] offset:33280\n" \
  "v_smfmac_f32_16x16x64_bf16 v[76:79], v[28:31], a[48:55], v0\n" \
  "v_smfmac_f32_16x16x64_bf16 v[80:83], v[28:31], a[56:63], v0\n" \
  "s_waitcnt lgkmcnt(8)\n" \
  "v_cvt_pk_bf16_f32 v72, v72, v73\n" \
  "v_smfmac_f32_16x16x64_bf16 v[20:23], v[32:35], v[112:119], v0\n" \
  "ds_read_b64_tr_b16 v[24:25], v9 offset:32768\n" \
  "s_waitcnt lgkmcnt(7)\n" \
  "v_cvt_pk_bf16_f32 v73, v74, v75\n" \
  "v_smfmac_f32_16x16x64_bf16 v[20:23], v[36:39], v[88:95], v0\n" \
  "ds_write_b64 v6, v[72:73] offset:36864\n" \
  "s_waitcnt lgkmcnt(6)\n" \
  "v_smfmac_f32_16x16x64_bf16 v[84:87], v[32:35], v[96:103], v0\n" \
  "ds_read_b64_tr_b16 v[28:29], v9 offset:33792\n" \
  "s_waitcnt lgkmcnt(4)\n" \
  "v_smfmac_f32_16x16x64_bf16 v[84:87], v[36:39], v[104:111], v0\n" \
  "ds_write_b128 v2, v[20:23] offset:0\n" \
  "v_smfmac_f32_16x16x64_bf16 v[76:79], v[32:35], a[88:95], v0\n" \
  "ds_read_b64_tr_b16 v[112:113], v9 offset:34816\n" \
  "v_smfmac_f32_16x16x64_bf16 v[76:79], v[36:39], a[120:127], v0\n" \
  "ds_read_b64_tr_b16 v[64:65], v9 offset:35840\n" \
  "v_smfmac_f32_16x16x64_bf16 v[76:79], v[40:43], a[152:159], v0\n" \
  "ds_read_b64_tr_b16 v[26:27], v9 offset:33280\n" \
  "v_smfmac_f32_16x16x64_bf16 v[76:79], v[44:47], a[192:199], v0\n" \
  "ds_read_b64_tr_b16 v[30:31], v9 offset:34304\n" \
  "v_smfmac_f32_16x16x64_bf16 v[76:79], v[48:51], a[240:247], v0\n" \
  "ds_read_b64_tr_b16 v[114:115], v9 offset:35328\n" \
  "v_smfmac_f32_16x16x64_bf16 v[76:79], v[52:55], v[144:151], v0\n" \
  "ds_read_b64_tr_b16 v[66:67], v9 offset:36352\n" \
  "v_smfmac_f32_16x16x64_bf16 v[76:79], v[56:59], v[192:199], v0\n" \
  "ds_read_b64_tr_b16 v[68:69], v9 offset:36864\n" \
  "v_smfmac_f32_16x16x64_bf16 v[76:79], v[60:63], v[232:239], v0\n" \
  "ds_read_b64_tr_b16 v[70:71], v9 offset:37376\n" \
  "v_smfmac_f32_16x16x64_bf16 v[80:83], v[32:35], a[96:103], v0\n" \
  "ds_read_b64_tr_b16 v[72:73], v9 offset:37888\n" \
  "v_smfmac_f32_16x16x64_bf16 v[80:83], v[36:39], a[128:135], v0\n" \
  "ds_read_b64_tr_b16 v[74:75], v9 offset:38400\n" \
  "v_cvt_pk_bf16_f32 v76, v76, v77\n" \
  "v_smfmac_f32_16x16x64_bf16 v[80:83], v[40:43], a[160:167], v0\n" \
  "v_cvt_pk_bf16_f32 v77, v78, v79\n" \
  "v_smfmac_f32_16x16x64_bf16 v[80:83], v[44:47], a[200:207], v0\n" \
  "s_waitcnt lgkmcnt(8)\n" \
  "ds_write_b64 v6, v[76:77] offset:0\n" \
  "v_smfmac_f32_16x16x64_bf16 v[80:83], v[48:51], a[248:255], v0\n" \
  "v_smfmac_f32_16x16x64_bf16 v[80:83], v[52:55], v[152:159], v0\n" \
  "v_smfmac_f32_16x16x64_bf16 v[80:83], v[56:59], v[200:207], v0\n" \
  "ds_read_b64_tr_b16 v[88:89], v9 offset:0\n" \
  "v_smfmac_f32_16x16x64_bf16 v[80:83], v[60:63], v[240:247], v0\n" \
  "ds_read_b64_tr_b16 v[92:93], v9 offset:1024\n" \
  "v_smfmac_f32_16x16x64_bf16 v[84:87], v[40:43], a[168:175], v0\n" \
  "ds_read_b64_tr_b16 v[96:97], v9 offset:2048\n" \
  "v_smfmac_f32_16x16x64_bf16 v[84:87], v[44:47], a[208:215], v0\n" \
  "ds_read_b64_tr_b16 v[100:101], v9 offset:3072\n" \
  "v_cvt_pk_bf16_f32 v80, v80, v81\n" \
  "v_smfmac_f32_16x16x64_bf16 v[84:87], v[48:51], v[120:127], v0\n" \
  "v_cvt_pk_bf16_f32 v81, v82, v83\n" \
  "v_smfmac_f32_16x16x64_bf16 v[84:87], v[52:55], v[160:167], v0\n" \
  "ds_write_b64 v6, v[80:81] offset:512\n" \
  "v_smfmac_f32_16x16x64_bf16 v[84:87], v[56:59], v[208:215], v0\n" \
  "v_smfmac_f32_16x16x64_bf16 v[84:87], v[60:63], v[248:255], v0\n" \
  "s_waitcnt lgkmcnt(6)\n" \
  "ds_read_b64_tr_b16 v[90:91], v9 offset:512\n" \
  "ds_read_b64_tr_b16 v[102:103], v9 offset:3584\n" \
  "s_nop 4\n" \
  "v_cvt_pk_bf16_f32 v84, v84, v85\n" \
  "v_cvt_pk_bf16_f32 v85, v86, v87\n" \
  "ds_write_b64 v6, v[84:85] offset:4096\n" \
  "s_waitcnt lgkmcnt(2)\n" \
  "v_mfma_f32_16x16x32_bf16 v[116:119], v[88:91], v[24:27], 0\n" \
  "s_waitcnt lgkmcnt(1)\n" \
  "v_mfma_f32_16x16x32_bf16 v[116:119], v[100:103], v[64:67], v[116:119]\n" \
  "LSRs0_%=:\n" \
  "s_barrier ; timing only\n" \
  "ds_read_b64_tr_b16 v[94:95], v9 offset:1536\n" \
  "ds_read_b64_tr_b16 v[98:99], v9 offset:2560\n" \
  "ds_read_b64_tr_b16 v[104:105], v9 offset:4096\n" \
  "ds_read_b64_tr_b16 v[106:107], v9 offset:4608\n" \
  "ds_read_b64_tr_b16 v[108:109], v9 offset:5120\n" \
  "ds_read_b64_tr_b16 v[110:111], v9 offset:5632\n" \
  "ds_read2_b32 v[34:35], v12 offset0:0 offset1:64\n" \
  "s_waitcnt lgkmcnt(6)\n" \
  "v_mfma_f32_16x16x32_bf16 v[116:119], v[92:95], v[28:31], v[116:119]\n" \
  "s_waitcnt lgkmcnt(5)\n" \
  "v_mfma_f32_16x16x32_bf16 v[116:119], v[96:99], v[112:115], v[116:119]\n" \
  "s_waitcnt lgkmcnt(3)\n" \
  "v_mfma_f32_16x16x32_bf16 v[116:119], v[104:107], v[68:71], v[116:119]\n" \
  "s_waitcnt lgkmcnt(1)\n" \
  "v_mfma_f32_16x16x32_bf16 v[116:119], v[108:111], v[72:75], v[116:119]\n" \
  "s_waitcnt lgkmcnt(0)\n" \
  "v_add_f32 v34, v34, v35\n" \
  "s_nop 1\n" \
  "v_add_f32_dpp v34, v34, v34 row_ror:8 row_mask:0xf bank_mask:0xf\n" \
  "s_nop 2\n" \
  "v_permlane32_swap_b32 v116, v118\n" \
  "v_permlane32_swap_b32 v117, v119\n" \
  "v_add_f32 v118, v116, v118\n" \
  "v_add_f32 v119, v117, v119\n" \
  "v_mul_f32 v116, v4, v118\n" \
  "v_mul_f32 v117, v4, v119\n" \
  "v_fmac_f32_dpp v116, v118, v4 row_ror:8 row_mask:0xf bank_mask:0xf\n" \
  "v_fmac_f32_dpp v117, v119, v4 row_ror:8 row_mask:0xf bank_mask:0xf\n" \
  "v_exp_f32 v32, v116\n" \
  "v_exp_f32 v33, v117\n" \
  "v_add_f32 v34, v34, v32\n" \
  "v_add_f32 v36, v34, v33\n" \
  "v_add_f32 v37, v34, v33\n" \
  "v_cndmask_b32 v32, v14, v32, s[34:35]\n" \
  "v_cndmask_b32 v33, v15, v33, s[34:35]\n" \
  "v_permlane16_swap_b32 v36, v37\n" \
  "v_add_f32 v34, v36, v37\n" \
  "v_add_f32 v35, v36, v37\n" \
  "s_nop 1\n" \
  "v_permlane32_swap_b32 v34, v35\n" \
  "v_add_f32 v34, v34, v35\n" \
  "v_rcp_f32 v38, v34\n" \
  "v_subrev_u32 v34, 0x1f800000, v34\n" \
  "v_cmp_lt_u32 vcc, 0x40000000, v34\n" \
  "v_mul_f32 v32, v32, v38\n" \
  "v_mul_f32 v33, v33, v38\n" \
  "v_cvt_pk_bf16_f32 v32, v32, v33\n" \
  "s_cbranch_vccnz LMXs0_%=\n" \
  "buffer_store_dword v32, v13, s[36:39], s72 offen offset:0 sc1\n" \
  "buffer_store_dword v32, v13, s[36:39], s44 offen offset:0 sc1\n" \
  "buffer_store_dword v32, v13, s[36:39], s45 offen offset:0 sc1\n" \
  "buffer_store_dword v32, v13, s[36:39], s46 offen offset:0 sc1\n" \
  "s_barrier\n" \
  "LMYs0_%=:\n" \
  "s_waitcnt vmcnt(4)\n" \
  "v_cmp_ge_u32 vcc, s43, v19\n" \
  "s_nop 3\n" \
  "s_cbranch_vccnz LFFs0_%=\n" \
  "LFOKs0_%=:\n" \
  "v_xor_b32 v5, 0x8000, v5\n" \
  "v_xor_b32 v10, 0x8000, v10\n" \
  "ds_read_b128 v[88:91], v3 offset:8192\n" \
  "ds_read_b128 v[92:95], v3 offset:9216\n" \
  "ds_read_b128 v[96:99], v3 offset:10240\n" \
  "ds_read_b128 v[100:103], v3 offset:11264\n" \
  "ds_read_b128 v[104:107], v3 offset:0\n" \
  "ds_read_b128 v[108:111], v3 offset:1024\n" \
  "ds_read_b128 v[112:115], v3 offset:22528\n" \
  "ds_read_b128 v[116:119], v3 offset:23552\n" \
  "v_mov_b32 v60, -1\n" \
  "v_mov_b32 v61, -1\n" \
  "ds_write_b64 v6, v[60:61] offset:32768\n" \
  "ds_write_b64 v6, v[60:61] offset:33280\n" \
  "ds_write_b64 v6, v[60:61] offset:36864\n" \
  "ds_write_b64 v2, v[60:61] offset:0\n" \
  "v_mov_b32 v20, 0\n" \
  "v_mov_b32 v21, 0\n" \
  "v_mov_b32 v22, 0\n" \
  "v_mov_b32 v23, 0\n" \
  "v_mov_b32 v64, 0\n" \
  "v_mov_b32 v65, 0\n" \
  "v_mov_b32 v66, 0\n" \
  "v_mov_b32 v67, 0\n" \
  "v_mov_b32 v68, 0\n" \
  "v_mov_b32 v69, 0\n" \
  "v_mov_b32 v70, 0\n" \
  "v_mov_b32 v71, 0\n" \
  "v_mov_b32 v72, 0\n" \
  "v_mov_b32 v73, 0\n" \
  "v_mov_b32 v74, 0\n" \
  "v_mov_b32 v75, 0\n" \
  "v_mov_b32 v76, 0\n" \
  "v_mov_b32 v77, 0\n" \
  "v_mov_b32 v78, 0\n" \
  "v_mov_b32 v79, 0\n" \
  "v_mov_b32 v80, 0\n" \
  "v_mov_b32 v81, 0\n" \
  "v_mov_b32 v82, 0\n" \
  "v_mov_b32 v83, 0\n" \
  "v_mov_b32 v84, 0\n" \
  "v_mov_b32 v85, 0\n" \
  "v_mov_b32 v86, 0\n" \
  "v_mov_b32 v87, 0\n" \
  "s_waitcnt lgkmcnt(0)\n" \
  "s_add_u32 s49, s76, s96\n" \
  "LUKs0_%=:\n" \
  "s_memtime s[78:79]\n" \
  "s_waitcnt lgkmcnt(0)\n" \
  "s_sub_i32 s74, s49, s78\n" \
  "s_cmp_gt_i32 s74, 0\n" \
  "s_cbranch_scc1 LUKs0_%=\n" \
  "s_add_u32 m0, s40, 0\n" \
  "s_add_u32 s75, s41, 0\n" \
  "buffer_load_dwordx4 v8, s[36:39], s75 offen offset:0 sc1 lds\n" \
  "buffer_load_dwordx4 v8, s[36:39], s75 offen offset:1024 sc1 lds\n" \
  "buffer_load_dwordx4 v8, s[36:39], s75 offen offset:2048 sc1 lds\n" \
  "buffer_load_dwordx4 v8, s[36:39], s75 offen offset:3072 sc1 lds\n" \
  "s_add_u32 m0, s40, 4096\n" \
  "s_add_u32 s75, s41, 4096\n" \
  "buffer_load_dwordx4 v8, s[36:39], s75 offen offset:0 sc1 lds\n" \
  "buffer_load_dwordx4 v8, s[36:39], s75 offen offset:1024 sc1 lds\n" \
  "s_add_u32 m0, s40, 4096\n" \
  "s_add_u32 s75, s41, 4096\n" \
  "buffer_load_dwordx4 v8, s[36:39], s75 offen offset:2048 sc1 lds\n" \
  "buffer_load_dwordx4 v8, s[36:39], s75 offen offset:3072 sc1 lds\n" \
  "s_add_u32 m0, s40, 8192\n" \
  "s_add_u32 s75, s41, 16384\n" \
  "buffer_load_dwordx4 v8, s[36:39], s75 offen offset:0 sc1 lds\n" \
  "buffer_load_dwordx4 v8, s[36:39], s75 offen offset:1024 sc1 lds\n" \
  "buffer_load_dwordx4 v8, s[36:39], s75 offen offset:2048 sc1 lds\n" \
  "buffer_load_dwordx4 v8, s[36:39], s75 offen offset:3072 sc1 lds\n" \
  "s_add_u32 m0, s40, 12288\n" \
  "s_add_u32 s75, s41, 20480\n" \
  "buffer_load_dwordx4 v8, s[36:39], s75 offen offset:0 sc1 lds\n" \
  "buffer_load_dwordx4 v8, s[36:39], s75 offen offset:1024 sc1 lds\n" \
  "buffer_load_dwordx4 v8, s[36:39], s75 offen offset:2048 sc1 lds\n" \
  "buffer_load_dwordx4 v8, s[36:39], s75 offen offset:3072 sc1 lds\n" \
  "s_add_u32 m0, s40, 16384\n" \
  "s_add_u32 s75, s41, 32768\n" \
  "buffer_load_dwordx4 v8, s[36:39], s75 offen offset:0 sc1 lds\n" \
  "buffer_load_dwordx4 v8, s[36:39], s75 offen offset:1024 sc1 lds\n" \
  "s_add_u32 m0, s40, 16384\n" \
  "s_add_u32 s75, s41, 32768\n" \
  "buffer_load_dwordx4 v8, s[36:39], s75 offen offset:2048 sc1 lds\n" \
  "buffer_load_dwordx4 v8, s[36:39], s75 offen offset:3072 sc1 lds\n" \
  "s_add_u32 m0, s40, 20480\n" \
  "s_add_u32 s75, s41, 36864\n" \
  "buffer_load_dwordx4 v8, s[36:39], s75 offen offset:0 sc1 lds\n" \
  "buffer_load_dwordx4 v8, s[36:39], s75 offen offset:1024 sc1 lds\n" \
  "buffer_load_dwordx4 v8, s[36:39], s75 offen offset:2048 sc1 lds\n" \
  "buffer_load_dwordx4 v8, s[36:39], s75 offen offset:3072 sc1 lds\n" \
  "s_xor_b32 s40, s40, 0x8000\n" \
  "s_cmp_eq_u32 s68, 64\n" \
  "s_cbranch_scc1 LENDs0_%=\n" \
  "v_readlane_b32 s72, v11, s68\n" \
  "s_nop 0\n" \
  "s_add_u32 s69, s72, 2048\n" \
  "s_add_u32 s70, s72, 6144\n" \
  "s_add_u32 s44, s72, s93\n" \
  "s_add_u32 s45, s72, s94\n" \
  "s_add_u32 s46, s72, s95\n" \
  "v_readlane_b32 s41, v7, s68\n" \
  "s_nop 1\n" \
  "s_branch LOOPs0_%=\n" \
  "LFFs0_%=:\n" \
  "LQFs0_%=:\n" \
  "buffer_load_dword v19, v18, s[36:39], 0 offen sc1\n" \
  "s_waitcnt vmcnt(0)\n" \
  "v_cmp_ge_u32 vcc, s43, v19\n" \
  "s_nop 3\n" \
  "s_cbranch_vccz LRFs0_%=\n" \
  "s_add_u32 s73, s73, 1\n" \
  "s_cmp_gt_u32 s73, 20000\n" \
  "s_cbranch_scc1 LRFs0_%=\n" \
  "s_sleep 16\n" \
  "s_branch LQFs0_%=\n" \
  "LRFs0_%=:\n" \
  "s_branch LFOKs0_%=\n" \
  "LMFs0_%=:\n" \
  "ds_read_b128 v[88:91], v3 offset:8192\n" \
  "ds_read_b128 v[92:95], v3 offset:9216\n" \
  "ds_read_b128 v[96:99], v3 offset:10240\n" \
  "ds_read_b128 v[100:103], v3 offset:11264\n" \
  "ds_read_b128 v[104:107], v3 offset:0\n" \
  "ds_read_b128 v[108:111], v3 offset:1024\n" \
  "ds_read_b128 v[112:115], v3 offset:22528\n" \
  "ds_read_b128 v[116:119], v3 offset:23552\n" \
  "v_mov_b32 v60, -1\n" \
  "v_mov_b32 v61, -1\n" \
  "ds_write_b64 v6, v[60:61] offset:32768\n" \
  "ds_write_b64 v6, v[60:61] offset:33280\n" \
  "ds_write_b64 v6, v[60:61] offset:36864\n" \
  "ds_write_b64 v2, v[60:61] offset:0\n" \
  "v_mov_b32 v20, 0\n" \
  "v_mov_b32 v21, 0\n" \
  "v_mov_b32 v22, 0\n" \
  "v_mov_b32 v23, 0\n" \
  "v_mov_b32 v64, 0\n" \
  "v_mov_b32 v65, 0\n" \
  "v_mov_b32 v66, 0\n" \
  "v_mov_b32 v67, 0\n" \
  "v_mov_b32 v68, 0\n" \
  "v_mov_b32 v69, 0\n" \
  "v_mov_b32 v70, 0\n" \
  "v_mov_b32 v71, 0\n" \
  "v_mov_b32 v72, 0\n" \
  "v_mov_b32 v73, 0\n" \
  "v_mov_b32 v74, 0\n" \
  "v_mov_b32 v75, 0\n" \
  "v_mov_b32 v76, 0\n" \
  "v_mov_b32 v77, 0\n" \
  "v_mov_b32 v78, 0\n" \
  "v_mov_b32 v79, 0\n" \
  "v_mov_b32 v80, 0\n" \
  "v_mov_b32 v81, 0\n" \
  "v_mov_b32 v82, 0\n" \
  "v_mov_b32 v83, 0\n" \
  "v_mov_b32 v84, 0\n" \
  "v_mov_b32 v85, 0\n" \
  "v_mov_b32 v86, 0\n" \
  "v_mov_b32 v87, 0\n" \
  "s_waitcnt lgkmcnt(0)\n" \
  "s_barrier\n" \
  "buffer_load_dwordx4 v[24:27], v1, s[36:39], s72 offen offset:0 sc1\n" \
  "s_mov_b32 m0, 65536\n" \
  "s_nop 0\n" \
  "buffer_load_dwordx4 v1, s[36:39], s69 offen sc1 lds\n" \
  "buffer_load_dwordx4 v[28:31], v1, s[36:39], s72 offen offset:1024 sc1\n" \
  "s_mov_b32 m0, 69632\n" \
  "s_nop 0\n" \
  "buffer_load_dwordx4 v1, s[36:39], s70 offen sc1 lds\n" \
  "ds_write_b64 v6, v[60:61] offset:0\n" \
  "ds_write_b64 v6, v[60:61] offset:512\n" \
  "ds_write_b64 v6, v[60:61] offset:4096\n" \
  "s_cmp_eq_u32 s91, 0\n" \
  "s_cbranch_scc1 LMs0_%=\n" \
  "s_waitcnt lgkmcnt(0)\n" \
  "s_branch LSAFEs0_%=\n" \
  "LMQs0_%=:\n" \
  "s_waitcnt lgkmcnt(0)\n" \
  "s_barrier\n" \
  "s_mov_b32 s74, 2\n" \
  "s_branch LMGs0_%=\n" \
  "LMXs0_%=:\n" \
  "v_cmp_u_f32 vcc, v38, v38\n" \
  "s_nop 3\n" \
  "s_cmp_lg_u64 vcc, 0\n" \
  "s_cselect_b32 s74, 3, 1\n" \
  "LMGs0_%=:\n" \
  "s_cmp_gt_u32 s56, 20000\n" \
  "s_cselect_b32 s74, 1, s74\n" \
  "v_mov_b32 v34, s74\n" \
  "ds_write_b32 v12, v34 offset:512\n" \
  "s_waitcnt lgkmcnt(0)\n" \
  "s_barrier\n" \
  "s_barrier\n" \
  "v_mov_b32 v34, 0\n" \
  "ds_write_b32 v12, v34 offset:512\n" \
  "s_and_b32 s91, s74, 1\n" \
  "s_lshr_b32 s74, s74, 1\n" \
  "s_add_u32 s56, s56, s74\n" \
  "s_cmp_lg_u32 s74, 0\n" \
  "s_cbranch_scc1 LMFs0_%=\n" \
  "s_add_u32 s57, s57, 1\n" \
  "v_mov_b32 v46, v116\n" \
  "v_mov_b32 v47, v117\n" \
  "ds_read_b128 v[88:91], v5 offset:0\n" \
  "ds_read_b128 v[92:95], v5 offset:2048\n" \
  "ds_read_b128 v[96:99], v5 offset:4096\n" \
  "ds_read_b128 v[100:103], v5 offset:6144\n" \
  "ds_read_b64 v[104:105], v10 offset:0\n" \
  "ds_read_b64 v[106:107], v10 offset:1024\n" \
  "ds_read_b64 v[108:109], v10 offset:2048\n" \
  "ds_read_b64 v[110:111], v10 offset:3072\n" \
  "s_waitcnt lgkmcnt(7)\n" \
  "v_mfma_f32_16x16x32_bf16 v[48:51], v[88:91], v[24:27], 0\n" \
  "s_waitcnt lgkmcnt(6)\n" \
  "v_mfma_f32_16x16x32_bf16 v[48:51], v[92:95], v[28:31], v[48:51]\n" \
  "s_waitcnt lgkmcnt(5)\n" \
  "v_mfma_f32_16x16x32_bf16 v[48:51], v[96:99], v[112:115], v[48:51]\n" \
  "s_waitcnt lgkmcnt(4)\n" \
  "v_mfma_f32_16x16x32_bf16 v[48:51], v[100:103], v[64:67], v[48:51]\n" \
  "s_waitcnt lgkmcnt(2)\n" \
  "v_mfma_f32_16x16x32_bf16 v[48:51], v[104:107], v[68:71], v[48:51]\n" \
  "s_waitcnt lgkmcnt(0)\n" \
  "v_mfma_f32_16x16x32_bf16 v[48:51], v[108:111], v[72:75], v[48:51]\n" \
  "s_nop 7\n" \
  "ds_read_b128 v[88:91], v5 offset:128\n" \
  "ds_read_b128 v[92:95], v5 offset:2176\n" \
  "ds_read_b128 v[96:99], v5 offset:4224\n" \
  "ds_read_b128 v[100:103], v5 offset:6272\n" \
  "ds_read_b64 v[104:105], v10 offset:128\n" \
  "ds_read_b64 v[106:107], v10 offset:1152\n" \
  "ds_read_b64 v[108:109], v10 offset:2176\n" \
  "ds_read_b64 v[110:111], v10 offset:3200\n" \
  "s_waitcnt lgkmcnt(7)\n" \
  "v_mfma_f32_16x16x32_bf16 v[52:55], v[88:91], v[24:27], 0\n" \
  "s_waitcnt lgkmcnt(6)\n" \
  "v_mfma_f32_16x16x32_bf16 v[52:55], v[92:95], v[28:31], v[52:55]\n" \
  "s_waitcnt lgkmcnt(5)\n" \
  "v_mfma_f32_16x16x32_bf16 v[52:55], v[96:99], v[112:115], v[52:55]\n" \
  "s_waitcnt lgkmcnt(4)\n" \
  "v_mfma_f32_16x16x32_bf16 v[52:55], v[100:103], v[64:67], v[52:55]\n" \
  "s_waitcnt lgkmcnt(2)\n" \
  "v_mfma_f32_16x16x32_bf16 v[52:55], v[104:107], v[68:71], v[52:55]\n" \
  "s_waitcnt lgkmcnt(0)\n" \
  "v_mfma_f32_16x16x32_bf16 v[52:55], v[108:111], v[72:75], v[52:55]\n" \
  "s_nop 7\n" \
  "v_add_f32_dpp v48, v48, v48 row_ror:8 row_mask:0xf bank_mask:0xf\n" \
  "v_mul_f32 v48, v4, v48\n" \
  "v_add_f32_dpp v49, v49, v49 row_ror:8 row_mask:0xf bank_mask:0xf\n" \
  "v_mul_f32 v49, v4, v49\n" \
  "v_add_f32_dpp v50, v50, v50 row_ror:8 row_mask:0xf bank_mask:0xf\n" \
  "v_mul_f32 v50, v4, v50\n" \
  "v_add_f32_dpp v51, v51, v51 row_ror:8 row_mask:0xf bank_mask:0xf\n" \
  "v_mul_f32 v51, v4, v51\n" \
  "v_add_f32_dpp v52, v52, v52 row_ror:8 row_mask:0xf bank_mask:0xf\n" \
  "v_mul_f32 v52, v4, v52\n" \
  "v_add_f32_dpp v53, v53, v53 row_ror:8 row_mask:0xf bank_mask:0xf\n" \
  "v_mul_f32 v53, v4, v53\n" \
  "v_add_f32_dpp v54, v54, v54 row_ror:8 row_mask:0xf bank_mask:0xf\n" \
  "v_mul_f32 v54, v4, v54\n" \
  "v_add_f32_dpp v55, v55, v55 row_ror:8 row_mask:0xf bank_mask:0xf\n" \
  "v_mul_f32 v55, v4, v55\n" \
  "v_max_f32 v32, v46, v47\n" \
  "v_max3_f32 v32, v32, v48, v49\n" \
  "v_max3_f32 v32, v32, v50, v51\n" \
  "v_max3_f32 v32, v32, v52, v53\n" \
  "v_max3_f32 v32, v32, v54, v55\n" \
  "v_mov_b32 v33, v32\n" \
  "s_nop 1\n" \
  "v_permlane16_swap_b32 v32, v33\n" \
  "v_max_f32 v32, v32, v33\n" \
  "v_mov_b32 v33, v32\n" \
  "s_nop 1\n" \
  "v_permlane32_swap_b32 v32, v33\n" \
  "v_max_f32 v32, v32, v33\n" \
  "v_sub_f32 v46, v46, v32\n" \
  "v_sub_f32 v47, v47, v32\n" \
  "v_sub_f32 v48, v48, v32\n" \
  "v_sub_f32 v49, v49, v32\n" \
  "v_sub_f32 v50, v50, v32\n" \
  "v_sub_f32 v51, v51, v32\n" \
  "v_sub_f32 v52, v52, v32\n" \
  "v_sub_f32 v53, v53, v32\n" \
  "v_sub_f32 v54, v54, v32\n" \
  "v_sub_f32 v55, v55, v32\n" \
  "v_exp_f32 v46, v46\n" \
  "v_exp_f32 v47, v47\n" \
  "v_exp_f32 v48, v48\n" \
  "v_exp_f32 v49, v49\n" \
  "v_exp_f32 v50, v50\n" \
  "v_exp_f32 v51, v51\n" \
  "v_exp_f32 v52, v52\n" \
  "v_exp_f32 v53, v53\n" \
  "v_exp_f32 v54, v54\n" \
  "v_exp_f32 v55, v55\n" \
  "v_add_f32 v32, v46, v47\n" \
  "v_add_f32 v32, v32, v48\n" \
  "v_add_f32 v32, v32, v49\n" \
  "v_add_f32 v32, v32, v50\n" \
  "v_add_f32 v32, v32, v51\n" \
  "v_add_f32 v32, v32, v52\n" \
  "v_add_f32 v32, v32, v53\n" \
  "v_add_f32 v32, v32, v54\n" \
  "v_add_f32 v32, v32, v55\n" \
  "v_mov_b32 v33, v32\n" \
  "s_nop 1\n" \
  "v_permlane16_swap_b32 v32, v33\n" \
  "v_add_f32 v32, v32, v33\n" \
  "v_mov_b32 v33, v32\n" \
  "s_nop 1\n" \
  "v_permlane32_swap_b32 v32, v33\n" \
  "v_add_f32 v32, v32, v33\n" \
  "v_rcp_f32 v32, v32\n" \
  "v_cvt_pk_bf16_f32 v34, v48, v49\n" \
  "v_pk_min_u16 v34, v34, -2 op_sel_hi:[1,0]\n" \
  "v_cvt_pk_bf16_f32 v35, v50, v51\n" \
  "v_pk_min_u16 v35, v35, -2 op_sel_hi:[1,0]\n" \
  "v_cvt_pk_bf16_f32 v36, v52, v53\n" \
  "v_pk_min_u16 v36, v36, -2 op_sel_hi:[1,0]\n" \
  "v_cvt_pk_bf16_f32 v37, v54, v55\n" \
  "v_pk_min_u16 v37, v37, -2 op_sel_hi:[1,0]\n" \
  "v_cndmask_b32 v34, v35, v34, s[34:35]\n" \
  "buffer_store_dword v34, v16, s[36:39], s72 offen offset:0 sc1\n" \
  "buffer_store_dword v34, v16, s[36:39], s44 offen offset:0 sc1\n" \
  "buffer_store_dword v34, v16, s[36:39], s45 offen offset:0 sc1\n" \
  "buffer_store_dword v34, v16, s[36:39], s46 offen offset:0 sc1\n" \
  "v_cndmask_b32 v36, v37, v36, s[34:35]\n" \
  "buffer_store_dword v36, v16, s[36:39], s72 offen offset:256 sc1\n" \
  "buffer_store_dword v36, v16, s[36:39], s44 offen offset:256 sc1\n" \
  "buffer_store_dword v36, v16, s[36:39], s45 offen offset:256 sc1\n" \
  "buffer_store_dword v36, v16, s[36:39], s46 offen offset:256 sc1\n" \
  "v_cndmask_b32 v46, v14, v46, s[34:35]\n" \
  "v_cndmask_b32 v47, v15, v47, s[34:35]\n" \
  "v_mul_f32 v46, v46, v32\n" \
  "v_mul_f32 v47, v47, v32\n" \
  "v_cvt_pk_bf16_f32 v46, v46, v47\n" \
  "v_pk_min_u16 v46, v46, -2 op_sel_hi:[1,0]\n" \
  "v_or_b32 v33, v14, v15\n" \
  "v_cmp_eq_u32 vcc, 0, v33\n" \
  "v_mov_b32 v33, 0xfffe\n" \
  "s_nop 3\n" \
  "s_andn2_b64 vcc, vcc, s[34:35]\n" \
  "s_nop 3\n" \
  "v_cndmask_b32 v46, v46, v33, vcc\n" \
  "s_waitcnt vmcnt(0) lgkmcnt(0)\n" \
  "buffer_store_dword v46, v13, s[36:39], s72 offen offset:0 sc1\n" \
  "buffer_store_dword v46, v13, s[36:39], s44 offen offset:0 sc1\n" \
  "buffer_store_dword v46, v13, s[36:39], s45 offen offset:0 sc1\n" \
  "buffer_store_dword v46, v13, s[36:39], s46 offen offset:0 sc1\n" \
  "s_branch LMYs0_%=\n" \
  "LENDs0_%=:\n" \
  "s_cmp_lg_u32 s38, 0\n" \
  "s_cbranch_scc1 LFINs0_%=\n" \
  "s_waitcnt vmcnt(0) lgkmcnt(0)\n" \
  "s_mov_b32 s38, s47\n" \
  "s_mov_b32 s54, s97\n" \
  "s_mov_b32 s96, s98\n" \
  "s_mov_b32 s68, 0\n" \
  "s_mov_b32 s73, 0\n" \
  "s_mov_b32 s56, 0\n" \
  "s_mov_b32 s57, 0\n" \
  "s_branch LPASSs0_%=\n" \
  "LFINs0_%=:\n" \
  "s_branch LDONE_%=\n" \
  "LSTARTs1_%=:\n" \
  "s_mov_b32 s79, 0\n" \
  "LSDs1_%=:\n" \
  "buffer_load_dwordx2 v[64:65], v19, s[36:39], 0 offen sc1\n" \
  "s_waitcnt vmcnt(0)\n" \
  "v_readlane_b32 s76, v64, 3\n" \
  "s_nop 0\n" \
  "s_add_u32 s76, s76, 1\n" \
  "v_cmp_ne_u32 vcc, s76, v64\n" \
  "s_nop 3\n" \
  "s_and_b32 s76, vcc_lo, 7\n" \
  "s_cbranch_scc0 LSFs1_%=\n" \
  "s_add_u32 s79, s79, 1\n" \
  "s_cmp_gt_u32 s79, 200000\n" \
  "s_cbranch_scc1 LSFs1_%=\n" \
  "s_sleep 4\n" \
  "s_branch LSDs1_%=\n" \
  "LSFs1_%=:\n" \
  "v_readlane_b32 s76, v65, 0\n" \
  "v_readlane_b32 s77, v65, 1\n" \
  "v_readlane_b32 s78, v65, 2\n" \
  "s_nop 1\n" \
  "v_lshrrev_b32 v64, 21, v7\n" \
  "v_lshrrev_b64 v[66:67], v64, s[76:77]\n" \
  "v_lshrrev_b32_e64 v68, v64, s78\n" \
  "v_cmp_gt_u32 vcc, 64, v64\n" \
  "s_nop 1\n" \
  "v_cndmask_b32 v66, v68, v66, vcc\n" \
  "v_and_b32 v66, 1, v66\n" \
  "v_lshlrev_b32 v66, 13, v66\n" \
  "v_xor_b32 v7, v7, v66\n" \
  "v_lshrrev_b32 v64, 21, v11\n" \
  "v_lshrrev_b64 v[66:67], v64, s[76:77]\n" \
  "v_lshrrev_b32_e64 v68, v64, s78\n" \
  "v_cmp_gt_u32 vcc, 64, v64\n" \
  "s_nop 1\n" \
  "v_cndmask_b32 v66, v68, v66, vcc\n" \
  "v_and_b32 v66, 1, v66\n" \
  "v_lshlrev_b32 v66, 13, v66\n" \
  "v_xor_b32 v11, v11, v66\n" \
  "v_lshrrev_b32 v64, 21, v28\n" \
  "v_lshrrev_b64 v[66:67], v64, s[76:77]\n" \
  "v_lshrrev_b32_e64 v68, v64, s78\n" \
  "v_cmp_gt_u32 vcc, 64, v64\n" \
  "s_nop 1\n" \
  "v_cndmask_b32 v66, v68, v66, vcc\n" \
  "v_and_b32 v66, 1, v66\n" \
  "v_lshlrev_b32 v66, 13, v66\n" \
  "v_xor_b32 v28, v28, v66\n" \
  "v_lshrrev_b32 v64, 21, v14\n" \
  "v_lshrrev_b64 v[66:67], v64, s[76:77]\n" \
  "v_lshrrev_b32_e64 v68, v64, s78\n" \
  "v_cmp_gt_u32 vcc, 64, v64\n" \
  "s_nop 1\n" \
  "v_cndmask_b32 v66, v68, v66, vcc\n" \
  "v_and_b32 v66, 1, v66\n" \
  "v_lshlrev_b32 v66, 13, v66\n" \
  "v_xor_b32 v14, v14, v66\n" \
  "v_lshrrev_b32 v64, 21, v15\n" \
  "v_lshrrev_b64 v[66:67], v64, s[76:77]\n" \
  "v_lshrrev_b32_e64 v68, v64, s78\n" \
  "v_cmp_gt_u32 vcc, 64, v64\n" \
  "s_nop 1\n" \
  "v_cndmask_b32 v66, v68, v66, vcc\n" \
  "v_and_b32 v66, 1, v66\n" \
  "v_lshlrev_b32 v66, 13, v66\n" \
  "v_xor_b32 v15, v15, v66\n" \
  "s_lshl_b32 s74, s76, 12\n" \
  "s_and_b32 s74, s74, 0x2000\n" \
  "s_or_b32 s55, s55, s74\n" \
  "s_mov_b32 s47, s38\n" \
  "s_mov_b32 s38, 0\n" \
  "s_mov_b32 s68, 63\n" \
  "s_mov_b32 s73, 20001\n" \
  "s_mov_b32 s56, 19999\n" \
  "s_mov_b32 s97, s54\n" \
  "s_mov_b32 s54, 0\n" \
  "s_mov_b32 s98, s96\n" \
  "s_mov_b32 s96, 0\n" \
  "LPASSs1_%=:\n" \
  "s_mov_b32 s40, 0\n" \
  "s_mov_b32 s42, 0x40000000\n" \
  "v_and_b32 v20, 0x7fff, v20\n" \
  "v_and_b32 v21, 0x7fff, v21\n" \
  "v_and_b32 v18, 0x7fff, v18\n" \
  "s_barrier\n" \
  "s_add_u32 m0, s40, 4096\n" \
  "s_add_u32 s75, s55, 4096\n" \
  "buffer_load_dwordx4 v8, s[36:39], s75 offen offset:2048 sc1 lds\n" \
  "buffer_load_dwordx4 v8, s[36:39], s75 offen offset:3072 sc1 lds\n" \
  "s_add_u32 m0, s40, 8192\n" \
  "s_add_u32 s75, s55, 16384\n" \
  "buffer_load_dwordx4 v8, s[36:39], s75 offen offset:0 sc1 lds\n" \
  "buffer_load_dwordx4 v8, s[36:39], s75 offen offset:1024 sc1 lds\n" \
  "buffer_load_dwordx4 v8, s[36:39], s75 offen offset:2048 sc1 lds\n" \
  "buffer_load_dwordx4 v8, s[36:39], s75 offen offset:3072 sc1 lds\n" \
  "s_xor_b32 s40, s40, 0x8000\n" \
  "v_readlane_b32 s72, v11, 0\n" \
  "s_nop 0\n" \
  "s_add_u32 s69, s72, 3072\n" \
  "s_add_u32 s70, s72, 7168\n" \
  "s_add_u32 s44, s72, s93\n" \
  "s_add_u32 s45, s72, s94\n" \
  "s_add_u32 s46, s72, s95\n" \
  "v_readlane_b32 s41, v7, 0\n" \
  "s_nop 1\n" \
  "LOOPs1_%=:\n" \
  "v_mov_b32 v60, -1\n" \
  "v_mov_b32 v61, -1\n" \
  "v_mov_b32 v64, 0\n" \
  "v_mov_b32 v65, 0\n" \
  "v_mov_b32 v66, 0\n" \
  "v_mov_b32 v67, 0\n" \
  "v_mov_b32 v68, 0\n" \
  "v_mov_b32 v69, 0\n" \
  "v_mov_b32 v70, 0\n" \
  "v_mov_b32 v71, 0\n" \
  "v_mov_b32 v72, 0\n" \
  "v_mov_b32 v73, 0\n" \
  "v_mov_b32 v74, 0\n" \
  "v_mov_b32 v75, 0\n" \
  "v_mov_b32 v76, 0\n" \
  "v_mov_b32 v77, 0\n" \
  "v_mov_b32 v78, 0\n" \
  "v_mov_b32 v79, 0\n" \
  "v_mov_b32 v80, 0\n" \
  "v_mov_b32 v81, 0\n" \
  "v_mov_b32 v82, 0\n" \
  "v_mov_b32 v83, 0\n" \
  "s_barrier\n" \
  "buffer_load_dwordx4 v[24:27], v1, s[36:39], s72 offen offset:0 sc1\n" \
  "s_mov_b32 m0, 66560\n" \
  "s_nop 0\n" \
  "buffer_load_dwordx4 v1, s[36:39], s69 offen sc1 lds\n" \
  "buffer_load_dwordx4 v[28:31], v1, s[36:39], s72 offen offset:1024 sc1\n" \
  "s_mov_b32 m0, 70656\n" \
  "s_nop 0\n" \
  "buffer_load_dwordx4 v1, s[36:39], s70 offen sc1 lds\n" \
  "ds_read_b128 v[88:91], v3 offset:8192\n" \
  "ds_read_b128 v[92:95], v3 offset:9216\n" \
  "ds_read_b128 v[96:99], v3 offset:10240\n" \
  "ds_read_b128 v[100:103], v3 offset:11264\n" \
  "ds_read_b128 v[104:107], v3 offset:12288\n" \
  "ds_read_b128 v[108:111], v3 offset:13312\n" \
  "ds_read_b128 v[112:115], v3 offset:14336\n" \
  "ds_read_b128 v[116:119], v3 offset:15360\n" \
  "ds_write_b64 v6, v[60:61] offset:33792\n" \
  "ds_write_b64 v6, v[60:61] offset:34304\n" \
  "ds_write_b64 v6, v[60:61] offset:37376\n" \
  "ds_write_b64 v6, v[60:61] offset:1024\n" \
  "ds_write_b64 v6, v[60:61] offset:1536\n" \
  "ds_write_b64 v6, v[60:61] offset:4608\n" \
  "ds_write_b32 v12, v60 offset:0\n" \
  "s_add_u32 s68, s68, 1\n" \
  "LMs1_%=:\n" \
  "buffer_store_dwordx4 v[52:55], v16, s[36:39], s42 offen sc1\n" \
  "buffer_store_dword v56, v17, s[36:39], s42 offen sc1\n" \
  "s_mov_b32 s42, 0x40000000\n" \
  "s_waitcnt vmcnt(5)\n" \
  "s_waitcnt lgkmcnt(13)\n" \
  "v_smfmac_f32_16x16x64_bf16 v[64:67], v[24:27], v[88:95], v0\n" \
  "ds_read_b128 v[88:91], v3 offset:16384\n" \
  "s_waitcnt lgkmcnt(12)\n" \
  "v_smfmac_f32_16x16x64_bf16 v[68:71], v[24:27], v[96:103], v0\n" \
  "ds_read_b128 v[92:95], v3 offset:17408\n" \
  "v_smfmac_f32_16x16x64_bf16 v[72:75], v[24:27], a[0:7], v0\n" \
  "ds_read_b128 v[96:99], v3 offset:18432\n" \
  "v_smfmac_f32_16x16x64_bf16 v[76:79], v[24:27], a[8:15], v0\n" \
  "ds_read_b128 v[100:103], v3 offset:19456\n" \
  "v_smfmac_f32_16x16x64_bf16 v[80:83], v[24:27], a[16:23], v0\n" \
  "s_waitcnt vmcnt(4)\n" \
  "s_waitcnt lgkmcnt(4)\n" \
  "s_barrier\n" \
  "ds_read_b128 v[32:35], v2 offset:2048\n" \
  "s_waitcnt vmcnt(3)\n" \
  "v_smfmac_f32_16x16x64_bf16 v[64:67], v[28:31], a[24:31], v0\n" \
  "ds_read_b128 v[36:39], v2 offset:3072\n" \
  "v_smfmac_f32_16x16x64_bf16 v[68:71], v[28:31], a[32:39], v0\n" \
  "ds_read_b128 v[40:43], v2 offset:4096\n" \
  "v_smfmac_f32_16x16x64_bf16 v[72:75], v[28:31], a[40:47], v0\n" \
  "ds_read_b128 v[44:47], v2 offset:5120\n" \
  "v_smfmac_f32_16x16x64_bf16 v[76:79], v[28:31], a[48:55], v0\n" \
  "v_smfmac_f32_16x16x64_bf16 v[80:83], v[28:31], a[56:63], v0\n" \
  "s_waitcnt lgkmcnt(3)\n" \
  "v_smfmac_f32_16x16x64_bf16 v[64:67], v[32:35], a[64:71], v0\n" \
  "v_smfmac_f32_16x16x64_bf16 v[68:71], v[32:35], a[72:79], v0\n" \
  "v_smfmac_f32_16x16x64_bf16 v[72:75], v[32:35], a[80:87], v0\n" \
  "s_waitcnt lgkmcnt(2)\n" \
  "v_smfmac_f32_16x16x64_bf16 v[64:67], v[36:39], a[104:111], v0\n" \
  "v_smfmac_f32_16x16x64_bf16 v[68:71], v[36:39], v[104:111], v0\n" \
  "s_waitcnt vmcnt(2)\n" \
  "s_barrier\n" \
  "v_smfmac_f32_16x16x64_bf16 v[72:75], v[36:39], a[112:119], v0\n" \
  "ds_read_b128 v[48:51], v2 offset:6144\n" \
  "s_waitcnt lgkmcnt(2)\n" \
  "v_smfmac_f32_16x16x64_bf16 v[64:67], v[40:43], v[112:119], v0\n" \
  "ds_read_b128 v[52:55], v2 offset:7168\n" \
  "v_smfmac_f32_16x16x64_bf16 v[68:71], v[40:43], a[136:143], v0\n" \
  "ds_read_b128 v[56:59], v2 offset:8192\n" \
  "v_smfmac_f32_16x16x64_bf16 v[72:75], v[40:43], a[144:151], v0\n" \
  "ds_read_b128 v[60:63], v2 offset:9216\n" \
  "s_waitcnt lgkmcnt(4)\n" \
  "v_smfmac_f32_16x16x64_bf16 v[64:67], v[44:47], v[88:95], v0\n" \
  "ds_read_b128 v[104:107], v3 offset:20480\n" \
  "v_smfmac_f32_16x16x64_bf16 v[68:71], v[44:47], a[176:183], v0\n" \
  "ds_read_b128 v[108:111], v3 offset:21504\n" \
  "v_smfmac_f32_16x16x64_bf16 v[72:75], v[44:47], a[184:191], v0\n" \
  "ds_read_b128 v[88:91], v20 offset:0\n" \
  "s_waitcnt lgkmcnt(6)\n" \
  "v_smfmac_f32_16x16x64_bf16 v[64:67], v[48:51], a[216:223], v0\n" \
  "ds_read_b128 v[92:95], v20 offset:2048\n" \
  "s_waitcnt lgkmcnt(6)\n" \
  "v_smfmac_f32_16x16x64_bf16 v[64:67], v[52:55], v[128:135], v0\n" \
  "s_waitcnt lgkmcnt(5)\n" \
  "v_smfmac_f32_16x16x64_bf16 v[64:67], v[56:59], v[168:175], v0\n" \
  "s_waitcnt lgkmcnt(4)\n" \
  "v_smfmac_f32_16x16x64_bf16 v[64:67], v[60:63], v[96:103], v0\n" \
  "ds_read_b128 v[96:99], v20 offset:4096\n" \
  "v_smfmac_f32_16x16x64_bf16 v[68:71], v[48:51], a[224:231], v0\n" \
  "ds_read_b128 v[100:103], v20 offset:6144\n" \
  "s_waitcnt lgkmcnt(4)\n" \
  "v_smfmac_f32_16x16x64_bf16 v[68:71], v[52:55], v[104:111], v0\n" \
  "ds_read_b64 v[104:105], v21 offset:0\n" \
  "v_cvt_pk_bf16_f32 v64, v64, v65\n" \
  "v_smfmac_f32_16x16x64_bf16 v[68:71], v[56:59], v[176:183], v0\n" \
  "ds_read_b64 v[106:107], v21 offset:1024\n" \
  "v_cvt_pk_bf16_f32 v65, v66, v67\n" \
  "v_smfmac_f32_16x16x64_bf16 v[68:71], v[60:63], v[216:223], v0\n" \
  "ds_write_b64 v6, v[64:65] offset:33792\n" \
  "v_cmp_u_f32 vcc, v64, v65\n" \
  "v_smfmac_f32_16x16x64_bf16 v[72:75], v[48:51], a[232:239], v0\n" \
  "s_cbranch_vccnz LMQs1_%=\n" \
  "v_smfmac_f32_16x16x64_bf16 v[72:75], v[52:55], v[136:143], v0\n" \
  "ds_read_b64 v[108:109], v21 offset:2048\n" \
  "v_cvt_pk_bf16_f32 v68, v68, v69\n" \
  "v_smfmac_f32_16x16x64_bf16 v[72:75], v[56:59], v[184:191], v0\n" \
  "ds_read_b64 v[110:111], v21 offset:3072\n" \
  "v_cvt_pk_bf16_f32 v69, v70, v71\n" \
  "v_smfmac_f32_16x16x64_bf16 v[72:75], v[60:63], v[224:231], v0\n" \
  "ds_write_b64 v6, v[68:69] offset:34304\n" \
  "v_smfmac_f32_16x16x64_bf16 v[76:79], v[32:35], a[88:95], v0\n" \
  "ds_read_b64_tr_b16 v[24:25], v9 offset:32768\n" \
  "v_smfmac_f32_16x16x64_bf16 v[76:79], v[36:39], a[120:127], v0\n" \
  "ds_read_b64_tr_b16 v[28:29], v9 offset:33792\n" \
  "v_cvt_pk_bf16_f32 v72, v72, v73\n" \
  "v_smfmac_f32_16x16x64_bf16 v[76:79], v[40:43], a[152:159], v0\n" \
  "ds_read_b64_tr_b16 v[112:113], v9 offset:34816\n" \
  "ds_read_b64_tr_b16 v[64:65], v9 offset:35840\n" \
  "v_cvt_pk_bf16_f32 v73, v74, v75\n" \
  "v_smfmac_f32_16x16x64_bf16 v[76:79], v[44:47], a[192:199], v0\n" \
  "ds_write_b64 v6, v[72:73] offset:37376\n" \
  "v_smfmac_f32_16x16x64_bf16 v[76:79], v[48:51], a[240:247], v0\n" \
  "s_waitcnt lgkmcnt(8)\n" \
  "ds_read_b64_tr_b16 v[26:27], v9 offset:33280\n" \
  "v_smfmac_f32_16x16x64_bf16 v[76:79], v[52:55], v[144:151], v0\n" \
  "ds_read_b64_tr_b16 v[30:31], v9 offset:34304\n" \
  "v_smfmac_f32_16x16x64_bf16 v[76:79], v[56:59], v[192:199], v0\n" \
  "ds_read_b64_tr_b16 v[114:115], v9 offset:35328\n" \
  "ds_read_b64_tr_b16 v[66:67], v9 offset:36352\n" \
  "v_smfmac_f32_16x16x64_bf16 v[76:79], v[60:63], v[232:239], v0\n" \
  "ds_read_b64_tr_b16 v[68:69], v9 offset:36864\n" \
  "v_smfmac_f32_16x16x64_bf16 v[80:83], v[32:35], a[96:103], v0\n" \
  "ds_read_b64_tr_b16 v[70:71], v9 offset:37376\n" \
  "v_smfmac_f32_16x16x64_bf16 v[80:83], v[36:39], a[128:135], v0\n" \
  "ds_read_b64_tr_b16 v[72:73], v9 offset:37888\n" \
  "s_waitcnt lgkmcnt(8)\n" \
  "ds_read_b64_tr_b16 v[74:75], v9 offset:38400\n" \
  "v_cvt_pk_bf16_f32 v76, v76, v77\n" \
  "s_waitcnt lgkmcnt(7)\n" \
  "v_mfma_f32_16x16x32_bf16 v[116:119], v[88:91], v[24:27], 0\n" \
  "s_waitcnt lgkmcnt(6)\n" \
  "v_cvt_pk_bf16_f32 v77, v78, v79\n" \
  "v_mfma_f32_16x16x32_bf16 v[116:119], v[92:95], v[28:31], v[116:119]\n" \
  "ds_write_b64 v6, v[76:77] offset:1024\n" \
  "s_waitcnt lgkmcnt(6)\n" \
  "v_mfma_f32_16x16x32_bf16 v[116:119], v[96:99], v[112:115], v[116:119]\n" \
  "s_waitcnt lgkmcnt(5)\n" \
  "v_mfma_f32_16x16x32_bf16 v[116:119], v[100:103], v[64:67], v[116:119]\n" \
  "v_smfmac_f32_16x16x64_bf16 v[80:83], v[40:43], a[160:167], v0\n" \
  "s_waitcnt lgkmcnt(3)\n" \
  "v_mfma_f32_16x16x32_bf16 v[116:119], v[104:107], v[68:71], v[116:119]\n" \
  "ds_read_b128 v[84:87], v2 offset:0\n" \
  "s_waitcnt lgkmcnt(2)\n" \
  "v_mfma_f32_16x16x32_bf16 v[116:119], v[108:111], v[72:75], v[116:119]\n" \
  "v_smfmac_f32_16x16x64_bf16 v[80:83], v[44:47], a[200:207], v0\n" \
  "v_smfmac_f32_16x16x64_bf16 v[80:83], v[48:51], a[248:255], v0\n" \
  "v_add_f32_dpp v88, v116, v116 row_ror:8 row_mask:0xf bank_mask:0x3\n" \
  "v_add_f32_dpp v89, v117, v117 row_ror:8 row_mask:0xf bank_mask:0x3\n" \
  "v_smfmac_f32_16x16x64_bf16 v[80:83], v[52:55], v[152:159], v0\n" \
  "v_add_f32_dpp v88, v118, v118 row_ror:8 row_mask:0xf bank_mask:0xc\n" \
  "v_add_f32_dpp v89, v119, v119 row_ror:8 row_mask:0xf bank_mask:0xc\n" \
  "v_smfmac_f32_16x16x64_bf16 v[80:83], v[56:59], v[200:207], v0\n" \
  "v_mul_f32 v88, v4, v88\n" \
  "v_mul_f32 v89, v4, v89\n" \
  "v_smfmac_f32_16x16x64_bf16 v[80:83], v[60:63], v[240:247], v0\n" \
  "s_waitcnt lgkmcnt(0)\n" \
  "v_exp_f32 v88, v88\n" \
  "v_exp_f32 v89, v89\n" \
  "v_smfmac_f32_16x16x64_bf16 v[84:87], v[40:43], a[168:175], v0\n" \
  "v_cmp_o_f32 vcc, v88, v89\n" \
  "v_cvt_pk_bf16_f32 v91, v88, v89\n" \
  "v_smfmac_f32_16x16x64_bf16 v[84:87], v[44:47], a[208:215], v0\n" \
  "s_mov_b64 exec, vcc\n" \
  "s_waitcnt vmcnt(0)\n" \
  "buffer_store_dword v91, v22, s[36:39], s72 offen offset:0 sc1\n" \
  "v_cvt_pk_bf16_f32 v80, v80, v81\n" \
  "v_smfmac_f32_16x16x64_bf16 v[84:87], v[48:51], v[120:127], v0\n" \
  "buffer_store_dword v91, v22, s[36:39], s44 offen offset:0 sc1\n" \
  "v_cvt_pk_bf16_f32 v81, v82, v83\n" \
  "v_smfmac_f32_16x16x64_bf16 v[84:87], v[52:55], v[160:167], v0\n" \
  "ds_write_b64 v6, v[80:81] offset:1536\n" \
  "v_smfmac_f32_16x16x64_bf16 v[84:87], v[56:59], v[208:215], v0\n" \
  "buffer_store_dword v91, v22, s[36:39], s45 offen offset:0 sc1\n" \
  "v_smfmac_f32_16x16x64_bf16 v[84:87], v[60:63], v[248:255], v0\n" \
  "buffer_store_dword v91, v22, s[36:39], s46 offen offset:0 sc1\n" \
  "s_mov_b64 exec, -1\n" \
  "v_add_f32 v90, v88, v89\n" \
  "ds_write_b32 v12, v90 offset:0\n" \
  "s_nop 3\n" \
  "v_cvt_pk_bf16_f32 v84, v84, v85\n" \
  "v_cvt_pk_bf16_f32 v85, v86, v87\n" \
  "ds_write_b64 v6, v[84:85] offset:4608\n" \
  "LSRs1_%=:\n" \
  "s_barrier ; timing only\n" \
  "v_mov_b32 v61, 0xf149f2ca\n" \
  "v_cmp_gt_i32 vcc, 0, v13\n" \
  "s_nop 1\n" \
  "v_cndmask_b32 v112, 0, v61, vcc\n" \
  "v_cmp_gt_i32 vcc, 1, v13\n" \
  "s_nop 1\n" \
  "v_cndmask_b32 v113, 0, v61, vcc\n" \
  "v_cmp_gt_i32 vcc, 2, v13\n" \
  "s_nop 1\n" \
  "v_cndmask_b32 v114, 0, v61, vcc\n" \
  "v_cmp_gt_i32 vcc, 3, v13\n" \
  "s_nop 1\n" \
  "v_cndmask_b32 v115, 0, v61, vcc\n" \
  "ds_read_b128 v[24:27], v18 offset:8192\n" \
  "ds_read_b128 v[64:67], v18 offset:0\n" \
  "ds_read_b64_tr_b16 v[48:49], v9 offset:0\n" \
  "ds_read_b64_tr_b16 v[50:51], v9 offset:512\n" \
  "ds_read_b128 v[28:31], v18 offset:10240\n" \
  "ds_read_b128 v[68:71], v18 offset:2048\n" \
  "ds_read_b64_tr_b16 v[52:53], v9 offset:1024\n" \
  "ds_read_b64_tr_b16 v[54:55], v9 offset:1536\n" \
  "ds_read_b128 v[32:35], v18 offset:12288\n" \
  "ds_read_b128 v[72:75], v18 offset:4096\n" \
  "ds_read_b64_tr_b16 v[56:57], v9 offset:2048\n" \
  "ds_read_b64_tr_b16 v[58:59], v9 offset:2560\n" \
  "s_waitcnt lgkmcnt(10)\n" \
  "v_mfma_f32_16x16x32_bf16 v[112:115], v[64:67], v[24:27], v[112:115]\n" \
  "s_waitcnt lgkmcnt(8)\n" \
  "v_mfma_f32_16x16x32_bf16 v[108:111], v[48:51], v[24:27], 0\n" \
  "ds_read_b128 v[36:39], v18 offset:14336\n" \
  "ds_read_b128 v[76:79], v18 offset:6144\n" \
  "ds_read_b64_tr_b16 v[48:49], v9 offset:3072\n" \
  "ds_read_b64_tr_b16 v[50:51], v9 offset:3584\n" \
  "s_waitcnt lgkmcnt(10)\n" \
  "v_mfma_f32_16x16x32_bf16 v[112:115], v[68:71], v[28:31], v[112:115]\n" \
  "s_waitcnt lgkmcnt(8)\n" \
  "v_mfma_f32_16x16x32_bf16 v[108:111], v[52:55], v[28:31], v[108:111]\n" \
  "ds_read_b64 v[40:41], v18 offset:20480\n" \
  "ds_read_b64 v[42:43], v18 offset:21504\n" \
  "ds_read_b64 v[80:81], v18 offset:16384\n" \
  "ds_read_b64 v[82:83], v18 offset:17408\n" \
  "ds_read_b64_tr_b16 v[52:53], v9 offset:4096\n" \
  "ds_read_b64_tr_b16 v[54:55], v9 offset:4608\n" \
  "s_waitcnt lgkmcnt(12)\n" \
  "v_mfma_f32_16x16x32_bf16 v[112:115], v[72:75], v[32:35], v[112:115]\n" \
  "s_waitcnt lgkmcnt(10)\n" \
  "v_mfma_f32_16x16x32_bf16 v[108:111], v[56:59], v[32:35], v[108:111]\n" \
  "ds_read_b64 v[44:45], v18 offset:22528\n" \
  "ds_read_b64 v[46:47], v18 offset:23552\n" \
  "ds_read_b64 v[84:85], v18 offset:18432\n" \
  "ds_read_b64 v[86:87], v18 offset:19456\n" \
  "ds_read_b64_tr_b16 v[56:57], v9 offset:5120\n" \
  "s_waitcnt lgkmcnt(8)\n" \
  "ds_read_b64_tr_b16 v[58:59], v9 offset:5632\n" \
  "v_mfma_f32_16x16x32_bf16 v[112:115], v[76:79], v[36:39], v[112:115]\n" \
  "v_mfma_f32_16x16x32_bf16 v[108:111], v[48:51], v[36:39], v[108:111]\n" \
  "s_waitcnt lgkmcnt(8)\n" \
  "v_mfma_f32_16x16x32_bf16 v[112:115], v[80:83], v[40:43], v[112:115]\n" \
  "s_waitcnt lgkmcnt(6)\n" \
  "v_mfma_f32_16x16x32_bf16 v[108:111], v[52:55], v[40:43], v[108:111]\n" \
  "s_barrier\n" \
  "ds_read_b32 v60, v12 offset:512\n" \
  "s_waitcnt lgkmcnt(3)\n" \
  "v_mfma_f32_16x16x32_bf16 v[112:115], v[84:87], v[44:47], v[112:115]\n" \
  "s_waitcnt lgkmcnt(1)\n" \
  "v_mfma_f32_16x16x32_bf16 v[108:111], v[56:59], v[44:47], v[108:111]\n" \
  "s_waitcnt lgkmcnt(0)\n" \
  "v_cmp_ne_u32 vcc, 0, v60\n" \
  "s_nop 3\n" \
  "s_cbranch_vccnz LMXs1_%=\n" \
  "LMYs1_%=:\n" \
  "s_nop 0\n" \
  "v_cmp_u_f32 vcc, v108, v109\n" \
  "v_cmp_u_f32 s[82:83], v110, v111\n" \
  "s_nop 1\n" \
  "s_or_b64 vcc, vcc, s[82:83]\n" \
  "s_cbranch_scc1 LAR1_%=\n" \
  "LAK1_%=:\n" \
  "v_permlane32_swap_b32 v108, v110\n" \
  "v_permlane32_swap_b32 v109, v111\n" \
  "v_pk_add_f32 v[110:111], v[108:109], v[110:111]\n" \
  "v_mov_b32 v32, 0x3d93cd3a\n" \
  "v_mov_b32 v34, 0x3fb8aa3b\n" \
  "v_cvt_pk_bf16_f32 v24, v110, v111\n" \
  "v_cvt_pk_bf16_f32 v25, v112, v113\n" \
  "v_cvt_pk_bf16_f32 v26, v114, v115\n" \
  "v_lshlrev_b32 v110, 16, v24\n" \
  "v_and_b32 v111, 0xffff0000, v24\n" \
  "v_pk_mul_f32 v[110:111], v[110:111], v[32:33] op_sel_hi:[1,0]\n" \
  "v_cvt_pk_bf16_f32 v24, v110, v111\n" \
  "v_lshlrev_b32 v110, 16, v24\n" \
  "v_and_b32 v111, 0xffff0000, v24\n" \
  "v_lshlrev_b32 v112, 16, v25\n" \
  "v_and_b32 v113, 0xffff0000, v25\n" \
  "v_pk_mul_f32 v[112:113], v[112:113], v[32:33] op_sel_hi:[1,0]\n" \
  "v_cvt_pk_bf16_f32 v25, v112, v113\n" \
  "v_lshlrev_b32 v112, 16, v25\n" \
  "v_and_b32 v113, 0xffff0000, v25\n" \
  "v_lshlrev_b32 v114, 16, v26\n" \
  "v_and_b32 v115, 0xffff0000, v26\n" \
  "v_pk_mul_f32 v[114:115], v[114:115], v[32:33] op_sel_hi:[1,0]\n" \
  "v_cvt_pk_bf16_f32 v26, v114, v115\n" \
  "v_lshlrev_b32 v114, 16, v26\n" \
  "v_and_b32 v115, 0xffff0000, v26\n" \
  "v_max_f32 v24, v110, v111\n" \
  "v_max3_f32 v24, v24, v112, v113\n" \
  "v_max3_f32 v24, v24, v114, v115\n" \
  "v_mov_b32 v25, v24\n" \
  "s_nop 1\n" \
  "v_permlane16_swap_b32 v24, v25\n" \
  "v_max_f32 v24, v24, v25\n" \
  "v_mov_b32 v25, v24\n" \
  "s_nop 1\n" \
  "v_permlane32_swap_b32 v24, v25\n" \
  "v_max_f32 v24, v24, v25\n" \
  "v_mul_f32 v24, v24, v34\n" \
  "v_pk_fma_f32 v[110:111], v[110:111], v[34:35], v[24:25] op_sel_hi:[1,0,0] neg_lo:[0,0,1] neg_hi:[0,0,1]\n" \
  "v_pk_fma_f32 v[112:113], v[112:113], v[34:35], v[24:25] op_sel_hi:[1,0,0] neg_lo:[0,0,1] neg_hi:[0,0,1]\n" \
  "v_pk_fma_f32 v[114:115], v[114:115], v[34:35], v[24:25] op_sel_hi:[1,0,0] neg_lo:[0,0,1] neg_hi:[0,0,1]\n" \
  "v_exp_f32 v110, v110\n" \
  "v_exp_f32 v111, v111\n" \
  "v_exp_f32 v112, v112\n" \
  "v_exp_f32 v113, v113\n" \
  "v_exp_f32 v114, v114\n" \
  "v_exp_f32 v115, v115\n" \
  "v_pk_add_f32 v[24:25], v[110:111], v[112:113]\n" \
  "v_pk_add_f32 v[24:25], v[24:25], v[114:115]\n" \
  "v_add_f32 v24, v24, v25\n" \
  "v_mov_b32 v25, v24\n" \
  "s_nop 1\n" \
  "v_permlane16_swap_b32 v24, v25\n" \
  "v_add_f32 v24, v24, v25\n" \
  "v_mov_b32 v25, v24\n" \
  "s_nop 1\n" \
  "v_permlane32_swap_b32 v24, v25\n" \
  "v_add_f32 v24, v24, v25\n" \
  "v_rcp_f32 v24, v24\n" \
  "v_mov_b32 v54, 0\n" \
  "v_mov_b32 v55, 0\n" \
  "v_pk_mul_f32 v[110:111], v[110:111], v[24:25] op_sel_hi:[1,0]\n" \
  "v_pk_mul_f32 v[112:113], v[112:113], v[24:25] op_sel_hi:[1,0]\n" \
  "v_pk_mul_f32 v[114:115], v[114:115], v[24:25] op_sel_hi:[1,0]\n" \
  "v_cvt_pk_bf16_f32 v52, v112, v113\n" \
  "v_cvt_pk_bf16_f32 v53, v114, v115\n" \
  "v_cvt_pk_bf16_f32 v56, v110, v111\n" \
  "s_sub_u32 s74, s68, 1\n" \
  "v_readlane_b32 s42, v15, s74\n" \
  "v_xor_b32 v20, 0x8000, v20\n" \
  "v_xor_b32 v21, 0x8000, v21\n" \
  "v_xor_b32 v18, 0x8000, v18\n" \
  "s_cmp_eq_u32 s68, 64\n" \
  "s_cbranch_scc1 LENDs1_%=\n" \
  "v_readlane_b32 s72, v11, s68\n" \
  "s_nop 0\n" \
  "s_add_u32 s69, s72, 3072\n" \
  "s_add_u32 s70, s72, 7168\n" \
  "s_add_u32 s44, s72, s93\n" \
  "s_add_u32 s45, s72, s94\n" \
  "s_add_u32 s46, s72, s95\n" \
  "v_readlane_b32 s41, v7, s68\n" \
  "s_nop 1\n" \
  "s_branch LOOPs1_%=\n" \
  "LAR1_%=:\n" \
  "ds_read_b64_tr_b16 v[64:65], v9 offset:0\n" \
  "ds_read_b64_tr_b16 v[66:67], v9 offset:512\n" \
  "ds_read_b64_tr_b16 v[68:69], v9 offset:1024\n" \
  "ds_read_b64_tr_b16 v[70:71], v9 offset:1536\n" \
  "ds_read_b64_tr_b16 v[72:73], v9 offset:2048\n" \
  "ds_read_b64_tr_b16 v[74:75], v9 offset:2560\n" \
  "ds_read_b64_tr_b16 v[76:77], v9 offset:3072\n" \
  "ds_read_b64_tr_b16 v[78:79], v9 offset:3584\n" \
  "ds_read_b64_tr_b16 v[80:81], v9 offset:4096\n" \
  "ds_read_b64_tr_b16 v[82:83], v9 offset:4608\n" \
  "ds_read_b64_tr_b16 v[84:85], v9 offset:5120\n" \
  "ds_read_b64_tr_b16 v[86:87], v9 offset:5632\n" \
  "s_waitcnt lgkmcnt(10)\n" \
  "v_mfma_f32_16x16x32_bf16 v[108:111], v[64:67], v[24:27], 0\n" \
  "s_waitcnt lgkmcnt(8)\n" \
  "v_mfma_f32_16x16x32_bf16 v[108:111], v[68:71], v[28:31], v[108:111]\n" \
  "s_waitcnt lgkmcnt(6)\n" \
  "v_mfma_f32_16x16x32_bf16 v[108:111], v[72:75], v[32:35], v[108:111]\n" \
  "s_waitcnt lgkmcnt(4)\n" \
  "v_mfma_f32_16x16x32_bf16 v[108:111], v[76:79], v[36:39], v[108:111]\n" \
  "s_waitcnt lgkmcnt(2)\n" \
  "v_mfma_f32_16x16x32_bf16 v[108:111], v[80:83], v[40:43], v[108:111]\n" \
  "s_waitcnt lgkmcnt(0)\n" \
  "v_mfma_f32_16x16x32_bf16 v[108:111], v[84:87], v[44:47], v[108:111]\n" \
  "s_nop 7\n" \
  "s_branch LAK1_%=\n" \
  "LMFs1_%=:\n" \
  "ds_read_b128 v[88:91], v3 offset:8192\n" \
  "ds_read_b128 v[92:95], v3 offset:9216\n" \
  "ds_read_b128 v[96:99], v3 offset:10240\n" \
  "ds_read_b128 v[100:103], v3 offset:11264\n" \
  "ds_read_b128 v[104:107], v3 offset:12288\n" \
  "ds_read_b128 v[108:111], v3 offset:13312\n" \
  "ds_read_b128 v[112:115], v3 offset:14336\n" \
  "ds_read_b128 v[116:119], v3 offset:15360\n" \
  "v_mov_b32 v60, -1\n" \
  "v_mov_b32 v61, -1\n" \
  "ds_write_b64 v6, v[60:61] offset:33792\n" \
  "ds_write_b64 v6, v[60:61] offset:34304\n" \
  "ds_write_b64 v6, v[60:61] offset:37376\n" \
  "v_mov_b32 v64, 0\n" \
  "v_mov_b32 v65, 0\n" \
  "v_mov_b32 v66, 0\n" \
  "v_mov_b32 v67, 0\n" \
  "v_mov_b32 v68, 0\n" \
  "v_mov_b32 v69, 0\n" \
  "v_mov_b32 v70, 0\n" \
  "v_mov_b32 v71, 0\n" \
  "v_mov_b32 v72, 0\n" \
  "v_mov_b32 v73, 0\n" \
  "v_mov_b32 v74, 0\n" \
  "v_mov_b32 v75, 0\n" \
  "v_mov_b32 v76, 0\n" \
  "v_mov_b32 v77, 0\n" \
  "v_mov_b32 v78, 0\n" \
  "v_mov_b32 v79, 0\n" \
  "v_mov_b32 v80, 0\n" \
  "v_mov_b32 v81, 0\n" \
  "v_mov_b32 v82, 0\n" \
  "v_mov_b32 v83, 0\n" \
  "s_waitcnt lgkmcnt(0)\n" \
  "s_barrier\n" \
  "buffer_load_dwordx4 v[24:27], v1, s[36:39], s72 offen offset:0 sc1\n" \
  "s_mov_b32 m0, 66560\n" \
  "s_nop 0\n" \
  "buffer_load_dwordx4 v1, s[36:39], s69 offen sc1 lds\n" \
  "buffer_load_dwordx4 v[28:31], v1, s[36:39], s72 offen offset:1024 sc1\n" \
  "s_mov_b32 m0, 70656\n" \
  "s_nop 0\n" \
  "buffer_load_dwordx4 v1, s[36:39], s70 offen sc1 lds\n" \
  "ds_write_b64 v6, v[60:61] offset:1024\n" \
  "ds_write_b64 v6, v[60:61] offset:1536\n" \
  "ds_write_b64 v6, v[60:61] offset:4608\n" \
  "ds_write_b32 v12, v60 offset:0\n" \
  "s_cmp_eq_u32 s91, 0\n" \
  "s_cbranch_scc1 LMs1_%=\n" \
  "s_waitcnt lgkmcnt(0)\n" \
  "s_branch LSAFEs1_%=\n" \
  "LMQs1_%=:\n" \
  "s_waitcnt lgkmcnt(0)\n" \
  "s_barrier\n" \
  "s_barrier\n" \
  "ds_read_b32 v60, v12 offset:512\n" \
  "s_waitcnt lgkmcnt(0)\n" \
  "LMXs1_%=:\n" \
  "v_cmp_lt_u32 vcc, 1, v60\n" \
  "v_readfirstlane_b32 s91, v60\n" \
  "s_waitcnt lgkmcnt(0)\n" \
  "s_nop 2\n" \
  "s_and_b32 s91, s91, 1\n" \
  "s_cbranch_vccnz LMVs1_%=\n" \
  "s_waitcnt vmcnt(0)\n" \
  "LMVs1_%=:\n" \
  "s_barrier\n" \
  "s_cbranch_vccnz LMFs1_%=\n" \
  "s_branch LMYs1_%=\n" \
  "LENDs1_%=:\n" \
  "s_cmp_lg_u32 s38, 0\n" \
  "s_cbranch_scc1 LFINs1_%=\n" \
  "s_waitcnt vmcnt(0) lgkmcnt(0)\n" \
  "s_mov_b32 s38, s47\n" \
  "s_mov_b32 s54, s97\n" \
  "s_mov_b32 s96, s98\n" \
  "s_mov_b32 s68, 0\n" \
  "s_mov_b32 s73, 0\n" \
  "s_mov_b32 s56, 0\n" \
  "s_mov_b32 s57, 0\n" \
  "s_branch LPASSs1_%=\n" \
  "LFINs1_%=:\n" \
  "s_nop 1\n" \
  "buffer_store_dwordx4 v[52:55], v16, s[36:39], s42 offen sc1\n" \
  "buffer_store_dword v56, v17, s[36:39], s42 offen sc1\n" \
  "s_waitcnt vmcnt(0)\n" \
  "v_mov_b32 v19, 1\n" \
  "buffer_atomic_add v19, v14, s[36:39], 0 offen sc1\n" \
  "s_branch LDONE_%=\n" \
  "LSTARTs2_%=:\n" \
  "s_mov_b32 s79, 0\n" \
  "LSDs2_%=:\n" \
  "buffer_load_dwordx2 v[64:65], v19, s[36:39], 0 offen sc1\n" \
  "s_waitcnt vmcnt(0)\n" \
  "v_readlane_b32 s76, v64, 3\n" \
  "s_nop 0\n" \
  "s_add_u32 s76, s76, 1\n" \
  "v_cmp_ne_u32 vcc, s76, v64\n" \
  "s_nop 3\n" \
  "s_and_b32 s76, vcc_lo, 7\n" \
  "s_cbranch_scc0 LSFs2_%=\n" \
  "s_add_u32 s79, s79, 1\n" \
  "s_cmp_gt_u32 s79, 200000\n" \
  "s_cbranch_scc1 LSFs2_%=\n" \
  "s_sleep 4\n" \
  "s_branch LSDs2_%=\n" \
  "LSFs2_%=:\n" \
  "v_readlane_b32 s76, v65, 0\n" \
  "v_readlane_b32 s77, v65, 1\n" \
  "v_readlane_b32 s78, v65, 2\n" \
  "s_nop 1\n" \
  "v_lshrrev_b32 v64, 21, v7\n" \
  "v_lshrrev_b64 v[66:67], v64, s[76:77]\n" \
  "v_lshrrev_b32_e64 v68, v64, s78\n" \
  "v_cmp_gt_u32 vcc, 64, v64\n" \
  "s_nop 1\n" \
  "v_cndmask_b32 v66, v68, v66, vcc\n" \
  "v_and_b32 v66, 1, v66\n" \
  "v_lshlrev_b32 v66, 13, v66\n" \
  "v_xor_b32 v7, v7, v66\n" \
  "v_lshrrev_b32 v64, 21, v11\n" \
  "v_lshrrev_b64 v[66:67], v64, s[76:77]\n" \
  "v_lshrrev_b32_e64 v68, v64, s78\n" \
  "v_cmp_gt_u32 vcc, 64, v64\n" \
  "s_nop 1\n" \
  "v_cndmask_b32 v66, v68, v66, vcc\n" \
  "v_and_b32 v66, 1, v66\n" \
  "v_lshlrev_b32 v66, 13, v66\n" \
  "v_xor_b32 v11, v11, v66\n" \
  "v_lshrrev_b32 v64, 21, v28\n" \
  "v_lshrrev_b64 v[66:67], v64, s[76:77]\n" \
  "v_lshrrev_b32_e64 v68, v64, s78\n" \
  "v_cmp_gt_u32 vcc, 64, v64\n" \
  "s_nop 1\n" \
  "v_cndmask_b32 v66, v68, v66, vcc\n" \
  "v_and_b32 v66, 1, v66\n" \
  "v_lshlrev_b32 v66, 13, v66\n" \
  "v_xor_b32 v28, v28, v66\n" \
  "v_lshrrev_b32 v64, 21, v14\n" \
  "v_lshrrev_b64 v[66:67], v64, s[76:77]\n" \
  "v_lshrrev_b32_e64 v68, v64, s78\n" \
  "v_cmp_gt_u32 vcc, 64, v64\n" \
  "s_nop 1\n" \
  "v_cndmask_b32 v66, v68, v66, vcc\n" \
  "v_and_b32 v66, 1, v66\n" \
  "v_lshlrev_b32 v66, 13, v66\n" \
  "v_xor_b32 v14, v14, v66\n" \
  "v_lshrrev_b32 v64, 21, v15\n" \
  "v_lshrrev_b64 v[66:67], v64, s[76:77]\n" \
  "v_lshrrev_b32_e64 v68, v64, s78\n" \
  "v_cmp_gt_u32 vcc, 64, v64\n" \
  "s_nop 1\n" \
  "v_cndmask_b32 v66, v68, v66, vcc\n" \
  "v_and_b32 v66, 1, v66\n" \
  "v_lshlrev_b32 v66, 13, v66\n" \
  "v_xor_b32 v15, v15, v66\n" \
  "s_lshl_b32 s74, s76, 12\n" \
  "s_and_b32 s74, s74, 0x2000\n" \
  "s_or_b32 s55, s55, s74\n" \
  "s_mov_b32 s47, s38\n" \
  "s_mov_b32 s38, 0\n" \
  "s_mov_b32 s68, 63\n" \
  "s_mov_b32 s73, 20001\n" \
  "s_mov_b32 s56, 19999\n" \
  "s_mov_b32 s97, s54\n" \
  "s_mov_b32 s54, 0\n" \
  "s_mov_b32 s98, s96\n" \
  "s_mov_b32 s96, 0\n" \
  "LPASSs2_%=:\n" \
  "s_mov_b32 s40, 0\n" \
  "s_mov_b32 s42, 0x40000000\n" \
  "v_and_b32 v20, 0x7fff, v20\n" \
  "v_and_b32 v21, 0x7fff, v21\n" \
  "v_and_b32 v18, 0x7fff, v18\n" \
  "s_barrier\n" \
  "s_add_u32 m0, s40, 12288\n" \
  "s_add_u32 s75, s55, 20480\n" \
  "buffer_load_dwordx4 v8, s[36:39], s75 offen offset:0 sc1 lds\n" \
  "buffer_load_dwordx4 v8, s[36:39], s75 offen offset:1024 sc1 lds\n" \
  "buffer_load_dwordx4 v8, s[36:39], s75 offen offset:2048 sc1 lds\n" \
  "buffer_load_dwordx4 v8, s[36:39], s75 offen offset:3072 sc1 lds\n" \
  "s_add_u32 m0, s40, 16384\n" \
  "s_add_u32 s75, s55, 32768\n" \
  "buffer_load_dwordx4 v8, s[36:39], s75 offen offset:0 sc1 lds\n" \
  "buffer_load_dwordx4 v8, s[36:39], s75 offen offset:1024 sc1 lds\n" \
  "s_xor_b32 s40, s40, 0x8000\n" \
  "v_readlane_b32 s72, v11, 0\n" \
  "s_nop 0\n" \
  "s_add_u32 s69, s72, 4096\n" \
  "s_add_u32 s70, s72, 16384\n" \
  "s_add_u32 s44, s72, s93\n" \
  "s_add_u32 s45, s72, s94\n" \
  "s_add_u32 s46, s72, s95\n" \
  "v_readlane_b32 s41, v7, 0\n" \
  "s_nop 1\n" \
  "LOOPs2_%=:\n" \
  "v_mov_b32 v60, -1\n" \
  "v_mov_b32 v61, -1\n" \
  "v_mov_b32 v64, 0\n" \
  "v_mov_b32 v65, 0\n" \
  "v_mov_b32 v66, 0\n" \
  "v_mov_b32 v67, 0\n" \
  "v_mov_b32 v68, 0\n" \
  "v_mov_b32 v69, 0\n" \
  "v_mov_b32 v70, 0\n" \
  "v_mov_b32 v71, 0\n" \
  "v_mov_b32 v72, 0\n" \
  "v_mov_b32 v73, 0\n" \
  "v_mov_b32 v74, 0\n" \
  "v_mov_b32 v75, 0\n" \
  "v_mov_b32 v76, 0\n" \
  "v_mov_b32 v77, 0\n" \
  "v_mov_b32 v78, 0\n" \
  "v_mov_b32 v79, 0\n" \
  "v_mov_b32 v80, 0\n" \
  "v_mov_b32 v81, 0\n" \
  "v_mov_b32 v82, 0\n" \
  "v_mov_b32 v83, 0\n" \
  "s_barrier\n" \
  "buffer_load_dwordx4 v[24:27], v1, s[36:39], s72 offen offset:0 sc1\n" \
  "s_mov_b32 m0, 67584\n" \
  "s_nop 0\n" \
  "buffer_load_dwordx4 v1, s[36:39], s69 offen sc1 lds\n" \
  "buffer_load_dwordx4 v[28:31], v1, s[36:39], s72 offen offset:1024 sc1\n" \
  "s_mov_b32 m0, 71680\n" \
  "s_nop 0\n" \
  "buffer_load_dwordx4 v1, s[36:39], s70 offen sc1 lds\n" \
  "ds_read_b128 v[88:91], v3 offset:8192\n" \
  "ds_read_b128 v[92:95], v3 offset:9216\n" \
  "ds_read_b128 v[96:99], v3 offset:10240\n" \
  "ds_read_b128 v[100:103], v3 offset:11264\n" \
  "ds_read_b128 v[104:107], v3 offset:12288\n" \
  "ds_read_b128 v[108:111], v3 offset:13312\n" \
  "ds_read_b128 v[112:115], v3 offset:14336\n" \
  "ds_read_b128 v[116:119], v3 offset:15360\n" \
  "ds_write_b64 v6, v[60:61] offset:34816\n" \
  "ds_write_b64 v6, v[60:61] offset:35328\n" \
  "ds_write_b64 v6, v[60:61] offset:37888\n" \
  "ds_write_b64 v6, v[60:61] offset:2048\n" \
  "ds_write_b64 v6, v[60:61] offset:2560\n" \
  "ds_write_b64 v6, v[60:61] offset:5120\n" \
  "ds_write_b32 v12, v60 offset:256\n" \
  "s_add_u32 s68, s68, 1\n" \
  "LMs2_%=:\n" \
  "buffer_store_dwordx4 v[52:55], v16, s[36:39], s42 offen sc1\n" \
  "buffer_store_dword v56, v17, s[36:39], s42 offen sc1\n" \
  "s_mov_b32 s42, 0x40000000\n" \
  "s_waitcnt vmcnt(5)\n" \
  "s_waitcnt lgkmcnt(13)\n" \
  "v_smfmac_f32_16x16x64_bf16 v[64:67], v[24:27], v[88:95], v0\n" \
  "ds_read_b128 v[88:91], v3 offset:16384\n" \
  "s_waitcnt lgkmcnt(12)\n" \
  "v_smfmac_f32_16x16x64_bf16 v[68:71], v[24:27], v[96:103], v0\n" \
  "ds_read_b128 v[92:95], v3 offset:17408\n" \
  "v_smfmac_f32_16x16x64_bf16 v[72:75], v[24:27], a[0:7], v0\n" \
  "ds_read_b128 v[96:99], v3 offset:18432\n" \
  "v_smfmac_f32_16x16x64_bf16 v[76:79], v[24:27], a[8:15], v0\n" \
  "ds_read_b128 v[100:103], v3 offset:19456\n" \
  "v_smfmac_f32_16x16x64_bf16 v[80:83], v[24:27], a[16:23], v0\n" \
  "s_waitcnt vmcnt(4)\n" \
  "s_waitcnt lgkmcnt(4)\n" \
  "s_barrier\n" \
  "ds_read_b128 v[32:35], v2 offset:2048\n" \
  "s_waitcnt vmcnt(3)\n" \
  "v_smfmac_f32_16x16x64_bf16 v[64:67], v[28:31], a[24:31], v0\n" \
  "ds_read_b128 v[36:39], v2 offset:3072\n" \
  "v_smfmac_f32_16x16x64_bf16 v[68:71], v[28:31], a[32:39], v0\n" \
  "ds_read_b128 v[40:43], v2 offset:4096\n" \
  "v_smfmac_f32_16x16x64_bf16 v[72:75], v[28:31], a[40:47], v0\n" \
  "ds_read_b128 v[44:47], v2 offset:5120\n" \
  "v_smfmac_f32_16x16x64_bf16 v[76:79], v[28:31], a[48:55], v0\n" \
  "v_smfmac_f32_16x16x64_bf16 v[80:83], v[28:31], a[56:63], v0\n" \
  "s_waitcnt lgkmcnt(3)\n" \
  "v_smfmac_f32_16x16x64_bf16 v[64:67], v[32:35], a[64:71], v0\n" \
  "v_smfmac_f32_16x16x64_bf16 v[68:71], v[32:35], a[72:79], v0\n" \
  "v_smfmac_f32_16x16x64_bf16 v[72:75], v[32:35], a[80:87], v0\n" \
  "s_waitcnt lgkmcnt(2)\n" \
  "v_smfmac_f32_16x16x64_bf16 v[64:67], v[36:39], a[104:111], v0\n" \
  "v_smfmac_f32_16x16x64_bf16 v[68:71], v[36:39], v[104:111], v0\n" \
  "s_waitcnt vmcnt(2)\n" \
  "s_barrier\n" \
  "v_smfmac_f32_16x16x64_bf16 v[72:75], v[36:39], a[112:119], v0\n" \
  "ds_read_b128 v[48:51], v2 offset:6144\n" \
  "s_waitcnt lgkmcnt(2)\n" \
  "v_smfmac_f32_16x16x64_bf16 v[64:67], v[40:43], v[112:119], v0\n" \
  "ds_read_b128 v[52:55], v2 offset:7168\n" \
  "v_smfmac_f32_16x16x64_bf16 v[68:71], v[40:43], a[136:143], v0\n" \
  "ds_read_b128 v[56:59], v2 offset:8192\n" \
  "v_smfmac_f32_16x16x64_bf16 v[72:75], v[40:43], a[144:151], v0\n" \
  "ds_read_b128 v[60:63], v2 offset:9216\n" \
  "s_waitcnt lgkmcnt(4)\n" \
  "v_smfmac_f32_16x16x64_bf16 v[64:67], v[44:47], v[88:95], v0\n" \
  "ds_read_b128 v[104:107], v3 offset:20480\n" \
  "v_smfmac_f32_16x16x64_bf16 v[68:71], v[44:47], a[176:183], v0\n" \
  "ds_read_b128 v[108:111], v3 offset:21504\n" \
  "v_smfmac_f32_16x16x64_bf16 v[72:75], v[44:47], a[184:191], v0\n" \
  "ds_read_b128 v[88:91], v20 offset:0\n" \
  "s_waitcnt lgkmcnt(6)\n" \
  "v_smfmac_f32_16x16x64_bf16 v[64:67], v[48:51], a[216:223], v0\n" \
  "ds_read_b128 v[92:95], v20 offset:2048\n" \
  "s_waitcnt lgkmcnt(6)\n" \
  "v_smfmac_f32_16x16x64_bf16 v[64:67], v[52:55], v[128:135], v0\n" \
  "s_waitcnt lgkmcnt(5)\n" \
  "v_smfmac_f32_16x16x64_bf16 v[64:67], v[56:59], v[168:175], v0\n" \
  "s_waitcnt lgkmcnt(4)\n" \
  "v_smfmac_f32_16x16x64_bf16 v[64:67], v[60:63], v[96:103], v0\n" \
  "ds_read_b128 v[96:99], v20 offset:4096\n" \
  "v_smfmac_f32_16x16x64_bf16 v[68:71], v[48:51], a[224:231], v0\n" \
  "ds_read_b128 v[100:103], v20 offset:6144\n" \
  "s_waitcnt lgkmcnt(4)\n" \
  "v_smfmac_f32_16x16x64_bf16 v[68:71], v[52:55], v[104:111], v0\n" \
  "ds_read_b64 v[104:105], v21 offset:0\n" \
  "v_cvt_pk_bf16_f32 v64, v64, v65\n" \
  "v_smfmac_f32_16x16x64_bf16 v[68:71], v[56:59], v[176:183], v0\n" \
  "ds_read_b64 v[106:107], v21 offset:1024\n" \
  "v_cvt_pk_bf16_f32 v65, v66, v67\n" \
  "v_smfmac_f32_16x16x64_bf16 v[68:71], v[60:63], v[216:223], v0\n" \
  "ds_write_b64 v6, v[64:65] offset:34816\n" \
  "v_cmp_u_f32 vcc, v64, v65\n" \
  "v_smfmac_f32_16x16x64_bf16 v[72:75], v[48:51], a[232:239], v0\n" \
  "s_cbranch_vccnz LMQs2_%=\n" \
  "v_smfmac_f32_16x16x64_bf16 v[72:75], v[52:55], v[136:143], v0\n" \
  "ds_read_b64 v[108:109], v21 offset:2048\n" \
  "v_cvt_pk_bf16_f32 v68, v68, v69\n" \
  "v_smfmac_f32_16x16x64_bf16 v[72:75], v[56:59], v[184:191], v0\n" \
  "ds_read_b64 v[110:111], v21 offset:3072\n" \
  "v_cvt_pk_bf16_f32 v69, v70, v71\n" \
  "v_smfmac_f32_16x16x64_bf16 v[72:75], v[60:63], v[224:231], v0\n" \
  "ds_write_b64 v6, v[68:69] offset:35328\n" \
  "v_smfmac_f32_16x16x64_bf16 v[76:79], v[32:35], a[88:95], v0\n" \
  "ds_read_b64_tr_b16 v[24:25], v9 offset:32768\n" \
  "v_smfmac_f32_16x16x64_bf16 v[76:79], v[36:39], a[120:127], v0\n" \
  "ds_read_b64_tr_b16 v[28:29], v9 offset:33792\n" \
  "v_cvt_pk_bf16_f32 v72, v72, v73\n" \
  "v_smfmac_f32_16x16x64_bf16 v[76:79], v[40:43], a[152:159], v0\n" \
  "ds_read_b64_tr_b16 v[112:113], v9 offset:34816\n" \
  "ds_read_b64_tr_b16 v[64:65], v9 offset:35840\n" \
  "v_cvt_pk_bf16_f32 v73, v74, v75\n" \
  "v_smfmac_f32_16x16x64_bf16 v[76:79], v[44:47], a[192:199], v0\n" \
  "ds_write_b64 v6, v[72:73] offset:37888\n" \
  "v_smfmac_f32_16x16x64_bf16 v[76:79], v[48:51], a[240:247], v0\n" \
  "s_waitcnt lgkmcnt(8)\n" \
  "ds_read_b64_tr_b16 v[26:27], v9 offset:33280\n" \
  "v_smfmac_f32_16x16x64_bf16 v[76:79], v[52:55], v[144:151], v0\n" \
  "ds_read_b64_tr_b16 v[30:31], v9 offset:34304\n" \
  "v_smfmac_f32_16x16x64_bf16 v[76:79], v[56:59], v[192:199], v0\n" \
  "ds_read_b64_tr_b16 v[114:115], v9 offset:35328\n" \
  "ds_read_b64_tr_b16 v[66:67], v9 offset:36352\n" \
  "v_smfmac_f32_16x16x64_bf16 v[76:79], v[60:63], v[232:239], v0\n" \
  "ds_read_b64_tr_b16 v[68:69], v9 offset:36864\n" \
  "v_smfmac_f32_16x16x64_bf16 v[80:83], v[32:35], a[96:103], v0\n" \
  "ds_read_b64_tr_b16 v[70:71], v9 offset:37376\n" \
  "v_smfmac_f32_16x16x64_bf16 v[80:83], v[36:39], a[128:135], v0\n" \
  "ds_read_b64_tr_b16 v[72:73], v9 offset:37888\n" \
  "s_waitcnt lgkmcnt(8)\n" \
  "ds_read_b64_tr_b16 v[74:75], v9 offset:38400\n" \
  "v_cvt_pk_bf16_f32 v76, v76, v77\n" \
  "s_waitcnt lgkmcnt(7)\n" \
  "v_mfma_f32_16x16x32_bf16 v[116:119], v[88:91], v[24:27], 0\n" \
  "s_waitcnt lgkmcnt(6)\n" \
  "v_cvt_pk_bf16_f32 v77, v78, v79\n" \
  "v_mfma_f32_16x16x32_bf16 v[116:119], v[92:95], v[28:31], v[116:119]\n" \
  "ds_write_b64 v6, v[76:77] offset:2048\n" \
  "s_waitcnt lgkmcnt(6)\n" \
  "v_mfma_f32_16x16x32_bf16 v[116:119], v[96:99], v[112:115], v[116:119]\n" \
  "s_waitcnt lgkmcnt(5)\n" \
  "v_mfma_f32_16x16x32_bf16 v[116:119], v[100:103], v[64:67], v[116:119]\n" \
  "v_smfmac_f32_16x16x64_bf16 v[80:83], v[40:43], a[160:167], v0\n" \
  "s_waitcnt lgkmcnt(3)\n" \
  "v_mfma_f32_16x16x32_bf16 v[116:119], v[104:107], v[68:71], v[116:119]\n" \
  "ds_read_b128 v[84:87], v2 offset:1024\n" \
  "s_waitcnt lgkmcnt(2)\n" \
  "v_mfma_f32_16x16x32_bf16 v[116:119], v[108:111], v[72:75], v[116:119]\n" \
  "v_smfmac_f32_16x16x64_bf16 v[80:83], v[44:47], a[200:207], v0\n" \
  "v_smfmac_f32_16x16x64_bf16 v[80:83], v[48:51], a[248:255], v0\n" \
  "v_add_f32_dpp v88, v116, v116 row_ror:8 row_mask:0xf bank_mask:0x3\n" \
  "v_add_f32_dpp v89, v117, v117 row_ror:8 row_mask:0xf bank_mask:0x3\n" \
  "v_smfmac_f32_16x16x64_bf16 v[80:83], v[52:55], v[152:159], v0\n" \
  "v_add_f32_dpp v88, v118, v118 row_ror:8 row_mask:0xf bank_mask:0xc\n" \
  "v_add_f32_dpp v89, v119, v119 row_ror:8 row_mask:0xf bank_mask:0xc\n" \
  "v_smfmac_f32_16x16x64_bf16 v[80:83], v[56:59], v[200:207], v0\n" \
  "v_mul_f32 v88, v4, v88\n" \
  "v_mul_f32 v89, v4, v89\n" \
  "v_smfmac_f32_16x16x64_bf16 v[80:83], v[60:63], v[240:247], v0\n" \
  "s_waitcnt lgkmcnt(0)\n" \
  "v_exp_f32 v88, v88\n" \
  "v_exp_f32 v89, v89\n" \
  "v_smfmac_f32_16x16x64_bf16 v[84:87], v[40:43], a[168:175], v0\n" \
  "v_cmp_o_f32 vcc, v88, v89\n" \
  "v_cvt_pk_bf16_f32 v91, v88, v89\n" \
  "v_smfmac_f32_16x16x64_bf16 v[84:87], v[44:47], a[208:215], v0\n" \
  "s_mov_b64 exec, vcc\n" \
  "s_waitcnt vmcnt(0)\n" \
  "buffer_store_dword v91, v22, s[36:39], s72 offen offset:0 sc1\n" \
  "v_cvt_pk_bf16_f32 v80, v80, v81\n" \
  "v_smfmac_f32_16x16x64_bf16 v[84:87], v[48:51], v[120:127], v0\n" \
  "buffer_store_dword v91, v22, s[36:39], s44 offen offset:0 sc1\n" \
  "v_cvt_pk_bf16_f32 v81, v82, v83\n" \
  "v_smfmac_f32_16x16x64_bf16 v[84:87], v[52:55], v[160:167], v0\n" \
  "ds_write_b64 v6, v[80:81] offset:2560\n" \
  "v_smfmac_f32_16x16x64_bf16 v[84:87], v[56:59], v[208:215], v0\n" \
  "buffer_store_dword v91, v22, s[36:39], s45 offen offset:0 sc1\n" \
  "v_smfmac_f32_16x16x64_bf16 v[84:87], v[60:63], v[248:255], v0\n" \
  "buffer_store_dword v91, v22, s[36:39], s46 offen offset:0 sc1\n" \
  "s_mov_b64 exec, -1\n" \
  "v_add_f32 v90, v88, v89\n" \
  "ds_write_b32 v12, v90 offset:256\n" \
  "s_nop 3\n" \
  "v_cvt_pk_bf16_f32 v84, v84, v85\n" \
  "v_cvt_pk_bf16_f32 v85, v86, v87\n" \
  "ds_write_b64 v6, v[84:85] offset:5120\n" \
  "LSRs2_%=:\n" \
  "s_barrier ; timing only\n" \
  "v_mov_b32 v61, 0xf149f2ca\n" \
  "v_cmp_gt_i32 vcc, 0, v13\n" \
  "s_nop 1\n" \
  "v_cndmask_b32 v116, 0, v61, vcc\n" \
  "v_cmp_gt_i32 vcc, 1, v13\n" \
  "s_nop 1\n" \
  "v_cndmask_b32 v117, 0, v61, vcc\n" \
  "v_cmp_gt_i32 vcc, 2, v13\n" \
  "s_nop 1\n" \
  "v_cndmask_b32 v118, 0, v61, vcc\n" \
  "v_cmp_gt_i32 vcc, 3, v13\n" \
  "s_nop 1\n" \
  "v_cndmask_b32 v119, 0, v61, vcc\n" \
  "ds_read_b128 v[24:27], v18 offset:9216\n" \
  "ds_read_b128 v[64:67], v18 offset:0\n" \
  "ds_read_b128 v[88:91], v18 offset:1024\n" \
  "ds_read_b64_tr_b16 v[48:49], v9 offset:0\n" \
  "ds_read_b64_tr_b16 v[50:51], v9 offset:512\n" \
  "ds_read_b128 v[28:31], v18 offset:11264\n" \
  "ds_read_b128 v[68:71], v18 offset:2048\n" \
  "ds_read_b128 v[92:95], v18 offset:3072\n" \
  "ds_read_b64_tr_b16 v[52:53], v9 offset:1024\n" \
  "ds_read_b64_tr_b16 v[54:55], v9 offset:1536\n" \
  "ds_read_b128 v[32:35], v18 offset:13312\n" \
  "ds_read_b128 v[72:75], v18 offset:4096\n" \
  "s_waitcnt lgkmcnt(8)\n" \
  "ds_read_b128 v[96:99], v18 offset:5120\n" \
  "ds_read_b64_tr_b16 v[56:57], v9 offset:2048\n" \
  "ds_read_b64_tr_b16 v[58:59], v9 offset:2560\n" \
  "v_mfma_f32_16x16x32_bf16 v[112:115], v[64:67], v[24:27], 0\n" \
  "v_mfma_f32_16x16x32_bf16 v[116:119], v[88:91], v[24:27], v[116:119]\n" \
  "s_waitcnt lgkmcnt(10)\n" \
  "v_mfma_f32_16x16x32_bf16 v[108:111], v[48:51], v[24:27], 0\n" \
  "ds_read_b128 v[36:39], v18 offset:15360\n" \
  "ds_read_b128 v[76:79], v18 offset:6144\n" \
  "ds_read_b128 v[100:103], v18 offset:7168\n" \
  "ds_read_b64_tr_b16 v[48:49], v9 offset:3072\n" \
  "ds_read_b64_tr_b16 v[50:51], v9 offset:3584\n" \
  "s_waitcnt lgkmcnt(13)\n" \
  "v_mfma_f32_16x16x32_bf16 v[112:115], v[68:71], v[28:31], v[112:115]\n" \
  "s_waitcnt lgkmcnt(12)\n" \
  "v_mfma_f32_16x16x32_bf16 v[116:119], v[92:95], v[28:31], v[116:119]\n" \
  "s_waitcnt lgkmcnt(10)\n" \
  "v_mfma_f32_16x16x32_bf16 v[108:111], v[52:55], v[28:31], v[108:111]\n" \
  "ds_read_b64 v[40:41], v18 offset:20488\n" \
  "ds_read_b64 v[42:43], v18 offset:21512\n" \
  "ds_read_b64 v[80:81], v18 offset:16384\n" \
  "ds_read_b64 v[82:83], v18 offset:17408\n" \
  "ds_read_b64 v[104:105], v18 offset:16392\n" \
  "s_waitcnt lgkmcnt(8)\n" \
  "ds_read_b64 v[106:107], v18 offset:17416\n" \
  "ds_read_b64_tr_b16 v[52:53], v9 offset:4096\n" \
  "ds_read_b64_tr_b16 v[54:55], v9 offset:4608\n" \
  "v_mfma_f32_16x16x32_bf16 v[112:115], v[72:75], v[32:35], v[112:115]\n" \
  "v_mfma_f32_16x16x32_bf16 v[116:119], v[96:99], v[32:35], v[116:119]\n" \
  "v_mfma_f32_16x16x32_bf16 v[108:111], v[56:59], v[32:35], v[108:111]\n" \
  "ds_read_b64 v[44:45], v18 offset:22536\n" \
  "ds_read_b64 v[46:47], v18 offset:23560\n" \
  "ds_read_b64 v[84:85], v18 offset:18432\n" \
  "ds_read_b64 v[86:87], v18 offset:19456\n" \
  "s_waitcnt lgkmcnt(8)\n" \
  "ds_read_b64 v[88:89], v18 offset:18440\n" \
  "ds_read_b64 v[90:91], v18 offset:19464\n" \
  "ds_read_b64_tr_b16 v[56:57], v9 offset:5120\n" \
  "ds_read_b64_tr_b16 v[58:59], v9 offset:5632\n" \
  "s_barrier\n" \
  "ds_read_b32 v60, v12 offset:512\n" \
  "v_mfma_f32_16x16x32_bf16 v[112:115], v[76:79], v[36:39], v[112:115]\n" \
  "v_mfma_f32_16x16x32_bf16 v[116:119], v[100:103], v[36:39], v[116:119]\n" \
  "v_mfma_f32_16x16x32_bf16 v[108:111], v[48:51], v[36:39], v[108:111]\n" \
  "v_mfma_f32_16x16x32_bf16 v[112:115], v[80:83], v[40:43], v[112:115]\n" \
  "s_waitcnt lgkmcnt(11)\n" \
  "v_mfma_f32_16x16x32_bf16 v[116:119], v[104:107], v[40:43], v[116:119]\n" \
  "s_waitcnt lgkmcnt(9)\n" \
  "v_mfma_f32_16x16x32_bf16 v[108:111], v[52:55], v[40:43], v[108:111]\n" \
  "s_waitcnt lgkmcnt(5)\n" \
  "v_mfma_f32_16x16x32_bf16 v[112:115], v[84:87], v[44:47], v[112:115]\n" \
  "s_waitcnt lgkmcnt(3)\n" \
  "v_mfma_f32_16x16x32_bf16 v[116:119], v[88:91], v[44:47], v[116:119]\n" \
  "s_waitcnt lgkmcnt(1)\n" \
  "v_mfma_f32_16x16x32_bf16 v[108:111], v[56:59], v[44:47], v[108:111]\n" \
  "s_waitcnt lgkmcnt(0)\n" \
  "v_cmp_ne_u32 vcc, 0, v60\n" \
  "s_nop 3\n" \
  "s_cbranch_vccnz LMXs2_%=\n" \
  "LMYs2_%=:\n" \
  "s_nop 0\n" \
  "v_cmp_u_f32 vcc, v108, v109\n" \
  "v_cmp_u_f32 s[82:83], v110, v111\n" \
  "s_nop 1\n" \
  "s_or_b64 vcc, vcc, s[82:83]\n" \
  "s_cbranch_scc1 LAR2_%=\n" \
  "LAK2_%=:\n" \
  "v_permlane32_swap_b32 v108, v110\n" \
  "v_permlane32_swap_b32 v109, v111\n" \
  "v_pk_add_f32 v[110:111], v[108:109], v[110:111]\n" \
  "v_mov_b32 v32, 0x3d93cd3a\n" \
  "v_mov_b32 v34, 0x3fb8aa3b\n" \
  "v_cvt_pk_bf16_f32 v24, v110, v111\n" \
  "v_cvt_pk_bf16_f32 v25, v112, v113\n" \
  "v_cvt_pk_bf16_f32 v26, v114, v115\n" \
  "v_cvt_pk_bf16_f32 v27, v116, v117\n" \
  "v_cvt_pk_bf16_f32 v28, v118, v119\n" \
  "v_lshlrev_b32 v110, 16, v24\n" \
  "v_and_b32 v111, 0xffff0000, v24\n" \
  "v_pk_mul_f32 v[110:111], v[110:111], v[32:33] op_sel_hi:[1,0]\n" \
  "v_cvt_pk_bf16_f32 v24, v110, v111\n" \
  "v_lshlrev_b32 v110, 16, v24\n" \
  "v_and_b32 v111, 0xffff0000, v24\n" \
  "v_lshlrev_b32 v112, 16, v25\n" \
  "v_and_b32 v113, 0xffff0000, v25\n" \
  "v_pk_mul_f32 v[112:113], v[112:113], v[32:33] op_sel_hi:[1,0]\n" \
  "v_cvt_pk_bf16_f32 v25, v112, v113\n" \
  "v_lshlrev_b32 v112, 16, v25\n" \
  "v_and_b32 v113, 0xffff0000, v25\n" \
  "v_lshlrev_b32 v114, 16, v26\n" \
  "v_and_b32 v115, 0xffff0000, v26\n" \
  "v_pk_mul_f32 v[114:115], v[114:115], v[32:33] op_sel_hi:[1,0]\n" \
  "v_cvt_pk_bf16_f32 v26, v114, v115\n" \
  "v_lshlrev_b32 v114, 16, v26\n" \
  "v_and_b32 v115, 0xffff0000, v26\n" \
  "v_lshlrev_b32 v116, 16, v27\n" \
  "v_and_b32 v117, 0xffff0000, v27\n" \
  "v_pk_mul_f32 v[116:117], v[116:117], v[32:33] op_sel_hi:[1,0]\n" \
  "v_cvt_pk_bf16_f32 v27, v116, v117\n" \
  "v_lshlrev_b32 v116, 16, v27\n" \
  "v_and_b32 v117, 0xffff0000, v27\n" \
  "v_lshlrev_b32 v118, 16, v28\n" \
  "v_and_b32 v119, 0xffff0000, v28\n" \
  "v_pk_mul_f32 v[118:119], v[118:119], v[32:33] op_sel_hi:[1,0]\n" \
  "v_cvt_pk_bf16_f32 v28, v118, v119\n" \
  "v_lshlrev_b32 v118, 16, v28\n" \
  "v_and_b32 v119, 0xffff0000, v28\n" \
  "v_max_f32 v24, v110, v111\n" \
  "v_max3_f32 v24, v24, v112, v113\n" \
  "v_max3_f32 v24, v24, v114, v115\n" \
  "v_max3_f32 v24, v24, v116, v117\n" \
  "v_max3_f32 v24, v24, v118, v119\n" \
  "v_mov_b32 v25, v24\n" \
  "s_nop 1\n" \
  "v_permlane16_swap_b32 v24, v25\n" \
  "v_max_f32 v24, v24, v25\n" \
  "v_mov_b32 v25, v24\n" \
  "s_nop 1\n" \
  "v_permlane32_swap_b32 v24, v25\n" \
  "v_max_f32 v24, v24, v25\n" \
  "v_mul_f32 v24, v24, v34\n" \
  "v_pk_fma_f32 v[110:111], v[110:111], v[34:35], v[24:25] op_sel_hi:[1,0,0] neg_lo:[0,0,1] neg_hi:[0,0,1]\n" \
  "v_pk_fma_f32 v[112:113], v[112:113], v[34:35], v[24:25] op_sel_hi:[1,0,0] neg_lo:[0,0,1] neg_hi:[0,0,1]\n" \
  "v_pk_fma_f32 v[114:115], v[114:115], v[34:35], v[24:25] op_sel_hi:[1,0,0] neg_lo:[0,0,1] neg_hi:[0,0,1]\n" \
  "v_pk_fma_f32 v[116:117], v[116:117], v[34:35], v[24:25] op_sel_hi:[1,0,0] neg_lo:[0,0,1] neg_hi:[0,0,1]\n" \
  "v_pk_fma_f32 v[118:119], v[118:119], v[34:35], v[24:25] op_sel_hi:[1,0,0] neg_lo:[0,0,1] neg_hi:[0,0,1]\n" \
  "v_exp_f32 v110, v110\n" \
  "v_exp_f32 v111, v111\n" \
  "v_exp_f32 v112, v112\n" \
  "v_exp_f32 v113, v113\n" \
  "v_exp_f32 v114, v114\n" \
  "v_exp_f32 v115, v115\n" \
  "v_exp_f32 v116, v116\n" \
  "v_exp_f32 v117, v117\n" \
  "v_exp_f32 v118, v118\n" \
  "v_exp_f32 v119, v119\n" \
  "v_pk_add_f32 v[24:25], v[110:111], v[112:113]\n" \
  "v_pk_add_f32 v[24:25], v[24:25], v[114:115]\n" \
  "v_pk_add_f32 v[24:25], v[24:25], v[116:117]\n" \
  "v_pk_add_f32 v[24:25], v[24:25], v[118:119]\n" \
  "v_add_f32 v24, v24, v25\n" \
  "v_mov_b32 v25, v24\n" \
  "s_nop 1\n" \
  "v_permlane16_swap_b32 v24, v25\n" \
  "v_add_f32 v24, v24, v25\n" \
  "v_mov_b32 v25, v24\n" \
  "s_nop 1\n" \
  "v_permlane32_swap_b32 v24, v25\n" \
  "v_add_f32 v24, v24, v25\n" \
  "v_rcp_f32 v24, v24\n" \
  "s_nop 0\n" \
  "v_pk_mul_f32 v[110:111], v[110:111], v[24:25] op_sel_hi:[1,0]\n" \
  "v_pk_mul_f32 v[112:113], v[112:113], v[24:25] op_sel_hi:[1,0]\n" \
  "v_pk_mul_f32 v[114:115], v[114:115], v[24:25] op_sel_hi:[1,0]\n" \
  "v_pk_mul_f32 v[116:117], v[116:117], v[24:25] op_sel_hi:[1,0]\n" \
  "v_pk_mul_f32 v[118:119], v[118:119], v[24:25] op_sel_hi:[1,0]\n" \
  "v_cvt_pk_bf16_f32 v52, v112, v113\n" \
  "v_cvt_pk_bf16_f32 v53, v114, v115\n" \
  "v_cvt_pk_bf16_f32 v54, v116, v117\n" \
  "v_cvt_pk_bf16_f32 v55, v118, v119\n" \
  "v_cvt_pk_bf16_f32 v56, v110, v111\n" \
  "s_sub_u32 s74, s68, 1\n" \
  "v_readlane_b32 s42, v15, s74\n" \
  "v_xor_b32 v20, 0x8000, v20\n" \
  "v_xor_b32 v21, 0x8000, v21\n" \
  "v_xor_b32 v18, 0x8000, v18\n" \
  "s_cmp_eq_u32 s68, 64\n" \
  "s_cbranch_scc1 LENDs2_%=\n" \
  "v_readlane_b32 s72, v11, s68\n" \
  "s_nop 0\n" \
  "s_add_u32 s69, s72, 4096\n" \
  "s_add_u32 s70, s72, 16384\n" \
  "s_add_u32 s44, s72, s93\n" \
  "s_add_u32 s45, s72, s94\n" \
  "s_add_u32 s46, s72, s95\n" \
  "v_readlane_b32 s41, v7, s68\n" \
  "s_nop 1\n" \
  "s_branch LOOPs2_%=\n" \
  "LAR2_%=:\n" \
  "ds_read_b64_tr_b16 v[64:65], v9 offset:0\n" \
  "ds_read_b64_tr_b16 v[66:67], v9 offset:512\n" \
  "ds_read_b64_tr_b16 v[68:69], v9 offset:1024\n" \
  "ds_read_b64_tr_b16 v[70:71], v9 offset:1536\n" \
  "ds_read_b64_tr_b16 v[72:73], v9 offset:2048\n" \
  "ds_read_b64_tr_b16 v[74:75], v9 offset:2560\n" \
  "ds_read_b64_tr_b16 v[76:77], v9 offset:3072\n" \
  "ds_read_b64_tr_b16 v[78:79], v9 offset:3584\n" \
  "ds_read_b64_tr_b16 v[80:81], v9 offset:4096\n" \
  "ds_read_b64_tr_b16 v[82:83], v9 offset:4608\n" \
  "ds_read_b64_tr_b16 v[84:85], v9 offset:5120\n" \
  "ds_read_b64_tr_b16 v[86:87], v9 offset:5632\n" \
  "s_waitcnt lgkmcnt(10)\n" \
  "v_mfma_f32_16x16x32_bf16 v[108:111], v[64:67], v[24:27], 0\n" \
  "s_waitcnt lgkmcnt(8)\n" \
  "v_mfma_f32_16x16x32_bf16 v[108:111], v[68:71], v[28:31], v[108:111]\n" \
  "s_waitcnt lgkmcnt(6)\n" \
  "v_mfma_f32_16x16x32_bf16 v[108:111], v[72:75], v[32:35], v[108:111]\n" \
  "s_waitcnt lgkmcnt(4)\n" \
  "v_mfma_f32_16x16x32_bf16 v[108:111], v[76:79], v[36:39], v[108:111]\n" \
  "s_waitcnt lgkmcnt(2)\n" \
  "v_mfma_f32_16x16x32_bf16 v[108:111], v[80:83], v[40:43], v[108:111]\n" \
  "s_waitcnt lgkmcnt(0)\n" \
  "v_mfma_f32_16x16x32_bf16 v[108:111], v[84:87], v[44:47], v[108:111]\n" \
  "s_nop 7\n" \
  "s_branch LAK2_%=\n" \
  "LMFs2_%=:\n" \
  "ds_read_b128 v[88:91], v3 offset:8192\n" \
  "ds_read_b128 v[92:95], v3 offset:9216\n" \
  "ds_read_b128 v[96:99], v3 offset:10240\n" \
  "ds_read_b128 v[100:103], v3 offset:11264\n" \
  "ds_read_b128 v[104:107], v3 offset:12288\n" \
  "ds_read_b128 v[108:111], v3 offset:13312\n" \
  "ds_read_b128 v[112:115], v3 offset:14336\n" \
  "ds_read_b128 v[116:119], v3 offset:15360\n" \
  "v_mov_b32 v60, -1\n" \
  "v_mov_b32 v61, -1\n" \
  "ds_write_b64 v6, v[60:61] offset:34816\n" \
  "ds_write_b64 v6, v[60:61] offset:35328\n" \
  "ds_write_b64 v6, v[60:61] offset:37888\n" \
  "v_mov_b32 v64, 0\n" \
  "v_mov_b32 v65, 0\n" \
  "v_mov_b32 v66, 0\n" \
  "v_mov_b32 v67, 0\n" \
  "v_mov_b32 v68, 0\n" \
  "v_mov_b32 v69, 0\n" \
  "v_mov_b32 v70, 0\n" \
  "v_mov_b32 v71, 0\n" \
  "v_mov_b32 v72, 0\n" \
  "v_mov_b32 v73, 0\n" \
  "v_mov_b32 v74, 0\n" \
  "v_mov_b32 v75, 0\n" \
  "v_mov_b32 v76, 0\n" \
  "v_mov_b32 v77, 0\n" \
  "v_mov_b32 v78, 0\n" \
  "v_mov_b32 v79, 0\n" \
  "v_mov_b32 v80, 0\n" \
  "v_mov_b32 v81, 0\n" \
  "v_mov_b32 v82, 0\n" \
  "v_mov_b32 v83, 0\n" \
  "s_waitcnt lgkmcnt(0)\n" \
  "s_barrier\n" \
  "buffer_load_dwordx4 v[24:27], v1, s[36:39], s72 offen offset:0 sc1\n" \
  "s_mov_b32 m0, 67584\n" \
  "s_nop 0\n" \
  "buffer_load_dwordx4 v1, s[36:39], s69 offen sc1 lds\n" \
  "buffer_load_dwordx4 v[28:31], v1, s[36:39], s72 offen offset:1024 sc1\n" \
  "s_mov_b32 m0, 71680\n" \
  "s_nop 0\n" \
  "buffer_load_dwordx4 v1, s[36:39], s70 offen sc1 lds\n" \
  "ds_write_b64 v6, v[60:61] offset:2048\n" \
  "ds_write_b64 v6, v[60:61] offset:2560\n" \
  "ds_write_b64 v6, v[60:61] offset:5120\n" \
  "ds_write_b32 v12, v60 offset:256\n" \
  "s_cmp_eq_u32 s91, 0\n" \
  "s_cbranch_scc1 LMs2_%=\n" \
  "s_waitcnt lgkmcnt(0)\n" \
  "s_branch LSAFEs2_%=\n" \
  "LMQs2_%=:\n" \
  "s_waitcnt lgkmcnt(0)\n" \
  "s_barrier\n" \
  "s_barrier\n" \
  "ds_read_b32 v60, v12 offset:512\n" \
  "s_waitcnt lgkmcnt(0)\n" \
  "LMXs2_%=:\n" \
  "v_cmp_lt_u32 vcc, 1, v60\n" \
  "v_readfirstlane_b32 s91, v60\n" \
  "s_waitcnt lgkmcnt(0)\n" \
  "s_nop 2\n" \
  "s_and_b32 s91, s91, 1\n" \
  "s_cbranch_vccnz LMVs2_%=\n" \
  "s_waitcnt vmcnt(0)\n" \
  "LMVs2_%=:\n" \
  "s_barrier\n" \
  "s_cbranch_vccnz LMFs2_%=\n" \
  "s_branch LMYs2_%=\n" \
  "LENDs2_%=:\n" \
  "s_cmp_lg_u32 s38, 0\n" \
  "s_cbranch_scc1 LFINs2_%=\n" \
  "s_waitcnt vmcnt(0) lgkmcnt(0)\n" \
  "s_mov_b32 s38, s47\n" \
  "s_mov_b32 s54, s97\n" \
  "s_mov_b32 s96, s98\n" \
  "s_mov_b32 s68, 0\n" \
  "s_mov_b32 s73, 0\n" \
  "s_mov_b32 s56, 0\n" \
  "s_mov_b32 s57, 0\n" \
  "s_branch LPASSs2_%=\n" \
  "LFINs2_%=:\n" \
  "s_nop 1\n" \
  "buffer_store_dwordx4 v[52:55], v16, s[36:39], s42 offen sc1\n" \
  "buffer_store_dword v56, v17, s[36:39], s42 offen sc1\n" \
  "s_waitcnt vmcnt(0)\n" \
  "v_mov_b32 v19, 1\n" \
  "buffer_atomic_add v19, v14, s[36:39], 0 offen sc1\n" \
  "s_branch LDONE_%=\n" \
  "LSTARTs3_%=:\n" \
  "s_mov_b32 s79, 0\n" \
  "LSDs3_%=:\n" \
  "buffer_load_dwordx2 v[64:65], v19, s[36:39], 0 offen sc1\n" \
  "s_waitcnt vmcnt(0)\n" \
  "v_readlane_b32 s76, v64, 3\n" \
  "s_nop 0\n" \
  "s_add_u32 s76, s76, 1\n" \
  "v_cmp_ne_u32 vcc, s76, v64\n" \
  "s_nop 3\n" \
  "s_and_b32 s76, vcc_lo, 7\n" \
  "s_cbranch_scc0 LSFs3_%=\n" \
  "s_add_u32 s79, s79, 1\n" \
  "s_cmp_gt_u32 s79, 200000\n" \
  "s_cbranch_scc1 LSFs3_%=\n" \
  "s_sleep 4\n" \
  "s_branch LSDs3_%=\n" \
  "LSFs3_%=:\n" \
  "v_readlane_b32 s76, v65, 0\n" \
  "v_readlane_b32 s77, v65, 1\n" \
  "v_readlane_b32 s78, v65, 2\n" \
  "s_nop 1\n" \
  "v_lshrrev_b32 v64, 21, v7\n" \
  "v_lshrrev_b64 v[66:67], v64, s[76:77]\n" \
  "v_lshrrev_b32_e64 v68, v64, s78\n" \
  "v_cmp_gt_u32 vcc, 64, v64\n" \
  "s_nop 1\n" \
  "v_cndmask_b32 v66, v68, v66, vcc\n" \
  "v_and_b32 v66, 1, v66\n" \
  "v_lshlrev_b32 v66, 13, v66\n" \
  "v_xor_b32 v7, v7, v66\n" \
  "v_lshrrev_b32 v64, 21, v11\n" \
  "v_lshrrev_b64 v[66:67], v64, s[76:77]\n" \
  "v_lshrrev_b32_e64 v68, v64, s78\n" \
  "v_cmp_gt_u32 vcc, 64, v64\n" \
  "s_nop 1\n" \
  "v_cndmask_b32 v66, v68, v66, vcc\n" \
  "v_and_b32 v66, 1, v66\n" \
  "v_lshlrev_b32 v66, 13, v66\n" \
  "v_xor_b32 v11, v11, v66\n" \
  "v_lshrrev_b32 v64, 21, v28\n" \
  "v_lshrrev_b64 v[66:67], v64, s[76:77]\n" \
  "v_lshrrev_b32_e64 v68, v64, s78\n" \
  "v_cmp_gt_u32 vcc, 64, v64\n" \
  "s_nop 1\n" \
  "v_cndmask_b32 v66, v68, v66, vcc\n" \
  "v_and_b32 v66, 1, v66\n" \
  "v_lshlrev_b32 v66, 13, v66\n" \
  "v_xor_b32 v28, v28, v66\n" \
  "v_lshrrev_b32 v64, 21, v16\n" \
  "v_lshrrev_b64 v[66:67], v64, s[76:77]\n" \
  "v_lshrrev_b32_e64 v68, v64, s78\n" \
  "v_cmp_gt_u32 vcc, 64, v64\n" \
  "s_nop 1\n" \
  "v_cndmask_b32 v66, v68, v66, vcc\n" \
  "v_and_b32 v66, 1, v66\n" \
  "v_lshlrev_b32 v66, 13, v66\n" \
  "v_xor_b32 v16, v16, v66\n" \
  "v_lshrrev_b32 v64, 21, v29\n" \
  "v_lshrrev_b64 v[66:67], v64, s[76:77]\n" \
  "v_lshrrev_b32_e64 v68, v64, s78\n" \
  "v_cmp_gt_u32 vcc, 64, v64\n" \
  "s_nop 1\n" \
  "v_cndmask_b32 v66, v68, v66, vcc\n" \
  "v_and_b32 v66, 1, v66\n" \
  "v_lshlrev_b32 v66, 13, v66\n" \
  "v_xor_b32 v29, v29, v66\n" \
  "s_lshl_b32 s74, s76, 12\n" \
  "s_and_b32 s74, s74, 0x2000\n" \
  "s_or_b32 s55, s55, s74\n" \
  "v_mov_b32 v17, v29\n" \
  "s_mov_b32 s47, s38\n" \
  "s_mov_b32 s38, 0\n" \
  "s_mov_b32 s68, 63\n" \
  "s_mov_b32 s73, 20001\n" \
  "s_mov_b32 s56, 19999\n" \
  "s_mov_b32 s97, s54\n" \
  "s_mov_b32 s54, 0\n" \
  "s_mov_b32 s98, s96\n" \
  "s_mov_b32 s96, 0\n" \
  "s_getpc_b64 s[82:83]\n" \
  "LPCAs3_%=:\n" \
  "s_add_u32 s84, s82, LEAs3_%=-LPCAs3_%=+24\n" \
  "s_addc_u32 s85, s83, 0\n" \
  "LPASSs3_%=:\n" \
  "s_mov_b32 s40, 0\n" \
  "s_mov_b32 s42, 0x40000000\n" \
  "s_barrier\n" \
  "s_add_u32 m0, s40, 16384\n" \
  "s_add_u32 s75, s55, 32768\n" \
  "buffer_load_dwordx4 v8, s[36:39], s75 offen offset:2048 sc1 lds\n" \
  "buffer_load_dwordx4 v8, s[36:39], s75 offen offset:3072 sc1 lds\n" \
  "s_add_u32 m0, s40, 20480\n" \
  "s_add_u32 s75, s55, 36864\n" \
  "buffer_load_dwordx4 v8, s[36:39], s75 offen offset:0 sc1 lds\n" \
  "buffer_load_dwordx4 v8, s[36:39], s75 offen offset:1024 sc1 lds\n" \
  "buffer_load_dwordx4 v8, s[36:39], s75 offen offset:2048 sc1 lds\n" \
  "buffer_load_dwordx4 v8, s[36:39], s75 offen offset:3072 sc1 lds\n" \
  "s_xor_b32 s40, s40, 0x8000\n" \
  "s_memrealtime s[76:77]\n" \
  "s_memtime s[78:79]\n" \
  "s_waitcnt lgkmcnt(0)\n" \
  "s_lshl_b32 s75, s76, 2\n" \
  "s_sub_i32 s75, s48, s75\n" \
  "s_mul_i32 s75, s75, s50\n" \
  "s_ashr_i32 s75, s75, 10\n" \
  "s_sub_i32 s74, s49, s78\n" \
  "s_min_i32 s74, s74, s75\n" \
  "s_sub_i32 s75, s75, 92\n" \
  "s_max_i32 s74, s74, s75\n" \
  "s_add_u32 s49, s74, s78\n" \
  "s_sub_u32 s74, s78, s89\n" \
  "s_sub_u32 s75, s76, s88\n" \
  "v_cvt_f32_u32 v24, s74\n" \
  "v_cvt_f32_u32 v25, s75\n" \
  "v_rcp_f32 v25, v25\n" \
  "v_mul_f32 v24, 0x43820000, v24\n" \
  "v_mul_f32 v24, v24, v25\n" \
  "v_cvt_u32_f32 v24, v24\n" \
  "s_nop 0\n" \
  "v_readfirstlane_b32 s74, v24\n" \
  "s_min_u32 s74, s74, 6400\n" \
  "s_cmp_gt_u32 s75, 150\n" \
  "s_cselect_b32 s50, s74, s50\n" \
  "s_cselect_b32 s88, s76, s88\n" \
  "s_cselect_b32 s89, s78, s89\n" \
  "v_mov_b32 v24, 0\n" \
  "v_mov_b32 v25, 0\n" \
  "v_mov_b32 v26, 0\n" \
  "v_mov_b32 v27, s50\n" \
  "buffer_store_dwordx4 v[24:27], v16, s[36:39], 0 offen sc1\n" \
  "LQAs3_%=:\n" \
  "buffer_load_dword v64, v17, s[36:39], 0 offen sc1\n" \
  "s_waitcnt vmcnt(0)\n" \
  "v_cmp_eq_u32 vcc, 0, v64\n" \
  "s_nop 3\n" \
  "s_cbranch_vccz LRAs3_%=\n" \
  "s_add_u32 s73, s73, 1\n" \
  "s_cmp_gt_u32 s73, 20000\n" \
  "s_cbranch_scc1 LRAs3_%=\n" \
  "s_sleep 2\n" \
  "s_branch LQAs3_%=\n" \
  "LRAs3_%=:\n" \
  "s_memrealtime s[76:77]\n" \
  "s_memtime s[78:79]\n" \
  "s_waitcnt lgkmcnt(0)\n" \
  "s_lshl_b32 s48, s76, 2\n" \
  "v_readlane_b32 s90, v64, 0\n" \
  "s_nop 1\n" \
  "s_sub_u32 s74, s90, s48\n" \
  "s_add_u32 s74, s74, 40000\n" \
  "s_cmp_gt_u32 s74, 80000\n" \
  "s_cselect_b32 s90, s48, s90\n" \
  "s_sub_u32 s74, s78, s89\n" \
  "s_sub_u32 s75, s76, s88\n" \
  "v_cvt_f32_u32 v24, s74\n" \
  "v_cvt_f32_u32 v25, s75\n" \
  "v_rcp_f32 v25, v25\n" \
  "v_mul_f32 v24, 0x43820000, v24\n" \
  "v_mul_f32 v24, v24, v25\n" \
  "v_cvt_u32_f32 v24, v24\n" \
  "s_nop 0\n" \
  "v_readfirstlane_b32 s74, v24\n" \
  "s_min_u32 s74, s74, 6400\n" \
  "s_cmp_gt_u32 s75, 150\n" \
  "s_cselect_b32 s50, s74, s50\n" \
  "s_cselect_b32 s88, s76, s88\n" \
  "s_cselect_b32 s89, s78, s89\n" \
  "s_add_u32 s49, s78, 0x1000000\n" \
  "s_mul_i32 s86, s54, 6\n" \
  "s_mov_b32 s87, -200\n" \
  "s_mov_b32 s51, 0\n" \
  "s_mov_b32 s71, 0\n" \
  "s_mul_i32 s74, s51, s54\n" \
  "s_add_u32 s74, s74, s90\n" \
  "s_sub_i32 s75, s74, s48\n" \
  "s_mov_b32 s48, s74\n" \
  "s_mul_i32 s75, s75, s50\n" \
  "s_ashr_i32 s75, s75, 10\n" \
  "s_add_u32 s49, s49, s75\n" \
  "s_cmp_eq_u32 s38, 0\n" \
  "s_cselect_b32 s87, 0x80000000, s87\n" \
  "v_readlane_b32 s72, v11, 0\n" \
  "s_nop 0\n" \
  "s_add_u32 s69, s72, 5120\n" \
  "s_add_u32 s70, s72, 17408\n" \
  "s_add_u32 s44, s72, s93\n" \
  "s_add_u32 s45, s72, s94\n" \
  "s_add_u32 s46, s72, s95\n" \
  "v_readlane_b32 s41, v7, 0\n" \
  "s_nop 1\n" \
  "ds_read_b128 v[88:91], v3 offset:8192\n" \
  "ds_read_b128 v[92:95], v3 offset:9216\n" \
  "ds_read_b128 v[96:99], v3 offset:10240\n" \
  "ds_read_b128 v[100:103], v3 offset:11264\n" \
  "ds_read_b128 v[104:107], v3 offset:0\n" \
  "ds_read_b128 v[108:111], v3 offset:1024\n" \
  "ds_read_b128 v[112:115], v3 offset:22528\n" \
  "ds_read_b128 v[116:119], v3 offset:23552\n" \
  "v_mov_b32 v60, -1\n" \
  "v_mov_b32 v61, -1\n" \
  "ds_write_b64 v6, v[60:61] offset:35840\n" \
  "ds_write_b64 v6, v[60:61] offset:36352\n" \
  "ds_write_b64 v6, v[60:61] offset:38400\n" \
  "ds_write_b64 v2, v[60:61] offset:1024\n" \
  "v_mov_b32 v20, 0\n" \
  "v_mov_b32 v21, 0\n" \
  "v_mov_b32 v22, 0\n" \
  "v_mov_b32 v23, 0\n" \
  "v_mov_b32 v64, 0\n" \
  "v_mov_b32 v65, 0\n" \
  "v_mov_b32 v66, 0\n" \
  "v_mov_b32 v67, 0\n" \
  "v_mov_b32 v68, 0\n" \
  "v_mov_b32 v69, 0\n" \
  "v_mov_b32 v70, 0\n" \
  "v_mov_b32 v71, 0\n" \
  "v_mov_b32 v72, 0\n" \
  "v_mov_b32 v73, 0\n" \
  "v_mov_b32 v74, 0\n" \
  "v_mov_b32 v75, 0\n" \
  "v_mov_b32 v76, 0\n" \
  "v_mov_b32 v77, 0\n" \
  "v_mov_b32 v78, 0\n" \
  "v_mov_b32 v79, 0\n" \
  "v_mov_b32 v80, 0\n" \
  "v_mov_b32 v81, 0\n" \
  "v_mov_b32 v82, 0\n" \
  "v_mov_b32 v83, 0\n" \
  "v_mov_b32 v84, 0\n" \
  "v_mov_b32 v85, 0\n" \
  "v_mov_b32 v86, 0\n" \
  "v_mov_b32 v87, 0\n" \
  "s_waitcnt lgkmcnt(0)\n" \
  "s_cmp_eq_u32 s38, 0\n" \
  "s_cselect_b32 s74, -1, 0\n" \
  "v_mov_b32 v64, s74\n" \
  "LOOPs3_%=:\n" \
  "LWAs3_%=:\n" \
  "s_memrealtime s[76:77]\n" \
  "s_memtime s[78:79]\n" \
  "s_waitcnt lgkmcnt(0)\n" \
  "s_lshl_b32 s75, s76, 2\n" \
  "s_sub_i32 s75, s48, s75\n" \
  "s_mul_i32 s75, s75, s50\n" \
  "s_ashr_i32 s75, s75, 10\n" \
  "s_sub_i32 s74, s49, s78\n" \
  "s_min_i32 s74, s74, s75\n" \
  "s_sub_i32 s75, s75, 92\n" \
  "s_max_i32 s74, s74, s75\n" \
  "s_add_u32 s49, s74, s78\n" \
  "s_cmp_gt_i32 s74, 168\n" \
  "s_cbranch_scc1 LWAs3_%=\n" \
  "s_cmp_lt_i32 s74, s87\n" \
  "s_cbranch_scc1 LSAs3_%=\n" \
  "s_max_i32 s74, s74, 48\n" \
  "s_lshr_b32 s74, s74, 3\n" \
  "s_lshl_b32 s74, s74, 2\n" \
  "s_sub_u32 s80, s84, s74\n" \
  "s_subb_u32 s81, s85, 0\n" \
  "s_setpc_b64 s[80:81]\n" \
  "s_nop 1\n" \
  "s_nop 1\n" \
  "s_nop 1\n" \
  "s_nop 1\n" \
  "s_nop 1\n" \
  "s_nop 1\n" \
  "s_nop 1\n" \
  "s_nop 1\n" \
  "s_nop 1\n" \
  "s_nop 1\n" \
  "s_nop 1\n" \
  "s_nop 1\n" \
  "s_nop 1\n" \
  "s_nop 1\n" \
  "s_nop 1\n" \
  "s_nop 1\n" \
  "LEAs3_%=:\n" \
  "s_barrier\n" \
  "buffer_load_dwordx4 v[24:27], v1, s[36:39], s72 offen offset:0 sc1\n" \
  "s_mov_b32 m0, 68608\n" \
  "s_nop 0\n" \
  "buffer_load_dwordx4 v1, s[36:39], s69 offen sc1 lds\n" \
  "buffer_load_dwordx4 v[28:31], v1, s[36:39], s72 offen offset:1024 sc1\n" \
  "s_mov_b32 m0, 72704\n" \
  "s_nop 0\n" \
  "buffer_load_dwordx4 v1, s[36:39], s70 offen sc1 lds\n" \
  "ds_write_b64 v6, v[60:61] offset:3072\n" \
  "ds_write_b64 v6, v[60:61] offset:3584\n" \
  "ds_write_b64 v6, v[60:61] offset:5632\n" \
  "s_add_u32 s68, s68, 1\n" \
  "LMs3_%=:\n" \
  "s_waitcnt vmcnt(3)\n" \
  "v_smfmac_f32_16x16x64_bf16 v[64:67], v[24:27], v[88:95], v0\n" \
  "ds_read_b128 v[88:91], v3 offset:2048\n" \
  "v_smfmac_f32_16x16x64_bf16 v[68:71], v[24:27], v[96:103], v0\n" \
  "ds_read_b128 v[92:95], v3 offset:3072\n" \
  "v_smfmac_f32_16x16x64_bf16 v[72:75], v[24:27], a[0:7], v0\n" \
  "ds_read_b128 v[96:99], v3 offset:24576\n" \
  "v_smfmac_f32_16x16x64_bf16 v[76:79], v[24:27], a[8:15], v0\n" \
  "ds_read_b128 v[100:103], v3 offset:25600\n" \
  "v_smfmac_f32_16x16x64_bf16 v[80:83], v[24:27], a[16:23], v0\n" \
  "v_smfmac_f32_16x16x64_bf16 v[84:87], v[24:27], v[104:111], v0\n" \
  "ds_read_b128 v[104:107], v3 offset:12288\n" \
  "v_smfmac_f32_16x16x64_bf16 v[20:23], v[24:27], v[112:119], v0\n" \
  "s_waitcnt vmcnt(2)\n" \
  "s_waitcnt lgkmcnt(5)\n" \
  "s_barrier\n" \
  "ds_read_b128 v[32:35], v2 offset:2048\n" \
  "s_waitcnt vmcnt(1)\n" \
  "v_smfmac_f32_16x16x64_bf16 v[64:67], v[28:31], a[24:31], v0\n" \
  "ds_read_b128 v[36:39], v2 offset:3072\n" \
  "v_smfmac_f32_16x16x64_bf16 v[68:71], v[28:31], a[32:39], v0\n" \
  "ds_read_b128 v[40:43], v2 offset:4096\n" \
  "v_smfmac_f32_16x16x64_bf16 v[72:75], v[28:31], a[40:47], v0\n" \
  "ds_read_b128 v[44:47], v2 offset:5120\n" \
  "s_waitcnt lgkmcnt(7)\n" \
  "v_smfmac_f32_16x16x64_bf16 v[84:87], v[28:31], v[88:95], v0\n" \
  "ds_read_b128 v[108:111], v3 offset:13312\n" \
  "s_waitcnt lgkmcnt(6)\n" \
  "v_smfmac_f32_16x16x64_bf16 v[20:23], v[28:31], v[96:103], v0\n" \
  "ds_read_b128 v[112:115], v3 offset:14336\n" \
  "s_waitcnt lgkmcnt(5)\n" \
  "v_smfmac_f32_16x16x64_bf16 v[64:67], v[32:35], a[64:71], v0\n" \
  "ds_read_b128 v[116:119], v3 offset:15360\n" \
  "v_smfmac_f32_16x16x64_bf16 v[68:71], v[32:35], a[72:79], v0\n" \
  "ds_read_b128 v[88:91], v3 offset:16384\n" \
  "v_smfmac_f32_16x16x64_bf16 v[72:75], v[32:35], a[80:87], v0\n" \
  "ds_read_b128 v[92:95], v3 offset:17408\n" \
  "s_waitcnt lgkmcnt(7)\n" \
  "v_smfmac_f32_16x16x64_bf16 v[64:67], v[36:39], a[104:111], v0\n" \
  "ds_read_b128 v[96:99], v3 offset:18432\n" \
  "s_waitcnt lgkmcnt(5)\n" \
  "v_smfmac_f32_16x16x64_bf16 v[68:71], v[36:39], v[104:111], v0\n" \
  "s_waitcnt vmcnt(0)\n" \
  "s_barrier\n" \
  "v_smfmac_f32_16x16x64_bf16 v[72:75], v[36:39], a[112:119], v0\n" \
  "ds_read_b128 v[48:51], v2 offset:6144\n" \
  "s_waitcnt lgkmcnt(4)\n" \
  "v_smfmac_f32_16x16x64_bf16 v[64:67], v[40:43], v[112:119], v0\n" \
  "ds_read_b128 v[52:55], v2 offset:7168\n" \
  "v_smfmac_f32_16x16x64_bf16 v[68:71], v[40:43], a[136:143], v0\n" \
  "ds_read_b128 v[56:59], v2 offset:8192\n" \
  "v_smfmac_f32_16x16x64_bf16 v[72:75], v[40:43], a[144:151], v0\n" \
  "ds_read_b128 v[60:63], v2 offset:9216\n" \
  "s_waitcnt lgkmcnt(5)\n" \
  "v_smfmac_f32_16x16x64_bf16 v[64:67], v[44:47], v[88:95], v0\n" \
  "ds_read_b128 v[100:103], v3 offset:19456\n" \
  "v_smfmac_f32_16x16x64_bf16 v[68:71], v[44:47], a[176:183], v0\n" \
  "ds_read_b128 v[104:107], v3 offset:20480\n" \
  "v_smfmac_f32_16x16x64_bf16 v[72:75], v[44:47], a[184:191], v0\n" \
  "ds_read_b128 v[108:111], v3 offset:21504\n" \
  "s_waitcnt lgkmcnt(6)\n" \
  "v_smfmac_f32_16x16x64_bf16 v[64:67], v[48:51], a[216:223], v0\n" \
  "ds_read_b128 v[112:115], v3 offset:26624\n" \
  "s_waitcnt lgkmcnt(6)\n" \
  "v_smfmac_f32_16x16x64_bf16 v[64:67], v[52:55], v[128:135], v0\n" \
  "ds_read_b128 v[116:119], v3 offset:27648\n" \
  "s_waitcnt lgkmcnt(6)\n" \
  "v_smfmac_f32_16x16x64_bf16 v[64:67], v[56:59], v[168:175], v0\n" \
  "ds_read_b128 v[88:91], v3 offset:28672\n" \
  "s_waitcnt lgkmcnt(5)\n" \
  "v_smfmac_f32_16x16x64_bf16 v[64:67], v[60:63], v[96:103], v0\n" \
  "ds_read_b128 v[92:95], v3 offset:29696\n" \
  "v_smfmac_f32_16x16x64_bf16 v[68:71], v[48:51], a[224:231], v0\n" \
  "ds_read_b128 v[96:99], v3 offset:4096\n" \
  "s_waitcnt lgkmcnt(5)\n" \
  "v_smfmac_f32_16x16x64_bf16 v[68:71], v[52:55], v[104:111], v0\n" \
  "ds_read_b128 v[100:103], v3 offset:5120\n" \
  "v_cvt_pk_bf16_f32 v64, v64, v65\n" \
  "v_smfmac_f32_16x16x64_bf16 v[68:71], v[56:59], v[176:183], v0\n" \
  "ds_read_b128 v[104:107], v3 offset:6144\n" \
  "v_cvt_pk_bf16_f32 v65, v66, v67\n" \
  "v_smfmac_f32_16x16x64_bf16 v[68:71], v[60:63], v[216:223], v0\n" \
  "ds_write_b64 v6, v[64:65] offset:35840\n" \
  "v_cmp_u_f32 vcc, v64, v65\n" \
  "v_smfmac_f32_16x16x64_bf16 v[72:75], v[48:51], a[232:239], v0\n" \
  "s_cbranch_vccnz LMQs3_%=\n" \
  "v_smfmac_f32_16x16x64_bf16 v[72:75], v[52:55], v[136:143], v0\n" \
  "ds_read_b128 v[108:111], v3 offset:7168\n" \
  "v_cvt_pk_bf16_f32 v68, v68, v69\n" \
  "v_smfmac_f32_16x16x64_bf16 v[72:75], v[56:59], v[184:191], v0\n" \
  "v_cvt_pk_bf16_f32 v69, v70, v71\n" \
  "v_smfmac_f32_16x16x64_bf16 v[72:75], v[60:63], v[224:231], v0\n" \
  "ds_write_b64 v6, v[68:69] offset:36352\n" \
  "v_smfmac_f32_16x16x64_bf16 v[76:79], v[28:31], a[48:55], v0\n" \
  "v_smfmac_f32_16x16x64_bf16 v[80:83], v[28:31], a[56:63], v0\n" \
  "s_waitcnt lgkmcnt(8)\n" \
  "v_cvt_pk_bf16_f32 v72, v72, v73\n" \
  "v_smfmac_f32_16x16x64_bf16 v[20:23], v[32:35], v[112:119], v0\n" \
  "s_waitcnt lgkmcnt(6)\n" \
  "v_cvt_pk_bf16_f32 v73, v74, v75\n" \
  "v_smfmac_f32_16x16x64_bf16 v[20:23], v[36:39], v[88:95], v0\n" \
  "ds_write_b64 v6, v[72:73] offset:38400\n" \
  "s_waitcnt lgkmcnt(5)\n" \
  "v_smfmac_f32_16x16x64_bf16 v[84:87], v[32:35], v[96:103], v0\n" \
  "s_waitcnt lgkmcnt(2)\n" \
  "v_smfmac_f32_16x16x64_bf16 v[84:87], v[36:39], v[104:111], v0\n" \
  "ds_write_b128 v2, v[20:23] offset:1024\n" \
  "v_smfmac_f32_16x16x64_bf16 v[76:79], v[32:35], a[88:95], v0\n" \
  "v_smfmac_f32_16x16x64_bf16 v[76:79], v[36:39], a[120:127], v0\n" \
  "v_smfmac_f32_16x16x64_bf16 v[76:79], v[40:43], a[152:159], v0\n" \
  "v_smfmac_f32_16x16x64_bf16 v[76:79], v[44:47], a[192:199], v0\n" \
  "v_smfmac_f32_16x16x64_bf16 v[76:79], v[48:51], a[240:247], v0\n" \
  "v_smfmac_f32_16x16x64_bf16 v[76:79], v[52:55], v[144:151], v0\n" \
  "v_smfmac_f32_16x16x64_bf16 v[76:79], v[56:59], v[192:199], v0\n" \
  "v_smfmac_f32_16x16x64_bf16 v[76:79], v[60:63], v[232:239], v0\n" \
  "v_smfmac_f32_16x16x64_bf16 v[80:83], v[32:35], a[96:103], v0\n" \
  "v_smfmac_f32_16x16x64_bf16 v[80:83], v[36:39], a[128:135], v0\n" \
  "v_cvt_pk_bf16_f32 v76, v76, v77\n" \
  "v_smfmac_f32_16x16x64_bf16 v[80:83], v[40:43], a[160:167], v0\n" \
  "v_cvt_pk_bf16_f32 v77, v78, v79\n" \
  "v_smfmac_f32_16x16x64_bf16 v[80:83], v[44:47], a[200:207], v0\n" \
  "ds_write_b64 v6, v[76:77] offset:3072\n" \
  "v_smfmac_f32_16x16x64_bf16 v[80:83], v[48:51], a[248:255], v0\n" \
  "v_smfmac_f32_16x16x64_bf16 v[80:83], v[52:55], v[152:159], v0\n" \
  "v_smfmac_f32_16x16x64_bf16 v[80:83], v[56:59], v[200:207], v0\n" \
  "v_smfmac_f32_16x16x64_bf16 v[80:83], v[60:63], v[240:247], v0\n" \
  "v_smfmac_f32_16x16x64_bf16 v[84:87], v[40:43], a[168:175], v0\n" \
  "v_smfmac_f32_16x16x64_bf16 v[84:87], v[44:47], a[208:215], v0\n" \
  "v_cvt_pk_bf16_f32 v80, v80, v81\n" \
  "v_smfmac_f32_16x16x64_bf16 v[84:87], v[48:51], v[120:127], v0\n" \
  "v_cvt_pk_bf16_f32 v81, v82, v83\n" \
  "v_smfmac_f32_16x16x64_bf16 v[84:87], v[52:55], v[160:167], v0\n" \
  "ds_write_b64 v6, v[80:81] offset:3584\n" \
  "v_smfmac_f32_16x16x64_bf16 v[84:87], v[56:59], v[208:215], v0\n" \
  "v_smfmac_f32_16x16x64_bf16 v[84:87], v[60:63], v[248:255], v0\n" \
  "s_nop 7\n" \
  "v_cvt_pk_bf16_f32 v84, v84, v85\n" \
  "v_cvt_pk_bf16_f32 v85, v86, v87\n" \
  "ds_write_b64 v6, v[84:85] offset:5632\n" \
  "LSRs3_%=:\n" \
  "s_barrier ; timing only\n" \
  "s_waitcnt lgkmcnt(0)\n" \
  "s_barrier\n" \
  "ds_read_b32 v60, v12 offset:512\n" \
  "s_waitcnt lgkmcnt(0)\n" \
  "v_cmp_ne_u32 vcc, 0, v60\n" \
  "s_nop 3\n" \
  "s_cbranch_vccnz LMXs3_%=\n" \
  "LMYs3_%=:\n" \
  "buffer_load_dword v15, v14, s[36:39], s72 offen sc1\n" \
  "s_lshl_b32 s74, s68, 16\n" \
  "s_or_b32 s74, s74, s51\n" \
  "v_mov_b32 v24, s74\n" \
  "v_mov_b32 v25, s49\n" \
  "v_mov_b32 v26, s48\n" \
  "v_mov_b32 v27, s50\n" \
  "buffer_store_dwordx4 v[24:27], v16, s[36:39], 0 offen sc1\n" \
  "ds_read_b128 v[88:91], v3 offset:8192\n" \
  "ds_read_b128 v[92:95], v3 offset:9216\n" \
  "ds_read_b128 v[96:99], v3 offset:10240\n" \
  "ds_read_b128 v[100:103], v3 offset:11264\n" \
  "ds_read_b128 v[104:107], v3 offset:0\n" \
  "ds_read_b128 v[108:111], v3 offset:1024\n" \
  "ds_read_b128 v[112:115], v3 offset:22528\n" \
  "ds_read_b128 v[116:119], v3 offset:23552\n" \
  "v_mov_b32 v60, -1\n" \
  "v_mov_b32 v61, -1\n" \
  "ds_write_b64 v6, v[60:61] offset:35840\n" \
  "ds_write_b64 v6, v[60:61] offset:36352\n" \
  "ds_write_b64 v6, v[60:61] offset:38400\n" \
  "ds_write_b64 v2, v[60:61] offset:1024\n" \
  "v_mov_b32 v20, 0\n" \
  "v_mov_b32 v21, 0\n" \
  "v_mov_b32 v22, 0\n" \
  "v_mov_b32 v23, 0\n" \
  "v_mov_b32 v64, 0\n" \
  "v_mov_b32 v65, 0\n" \
  "v_mov_b32 v66, 0\n" \
  "v_mov_b32 v67, 0\n" \
  "v_mov_b32 v68, 0\n" \
  "v_mov_b32 v69, 0\n" \
  "v_mov_b32 v70, 0\n" \
  "v_mov_b32 v71, 0\n" \
  "v_mov_b32 v72, 0\n" \
  "v_mov_b32 v73, 0\n" \
  "v_mov_b32 v74, 0\n" \
  "v_mov_b32 v75, 0\n" \
  "v_mov_b32 v76, 0\n" \
  "v_mov_b32 v77, 0\n" \
  "v_mov_b32 v78, 0\n" \
  "v_mov_b32 v79, 0\n" \
  "v_mov_b32 v80, 0\n" \
  "v_mov_b32 v81, 0\n" \
  "v_mov_b32 v82, 0\n" \
  "v_mov_b32 v83, 0\n" \
  "v_mov_b32 v84, 0\n" \
  "v_mov_b32 v85, 0\n" \
  "v_mov_b32 v86, 0\n" \
  "v_mov_b32 v87, 0\n" \
  "s_waitcnt lgkmcnt(0)\n" \
  "s_memrealtime s[76:77]\n" \
  "s_memtime s[78:79]\n" \
  "s_waitcnt lgkmcnt(0)\n" \
  "s_lshl_b32 s75, s76, 2\n" \
  "s_sub_i32 s75, s48, s75\n" \
  "s_mul_i32 s75, s75, s50\n" \
  "s_ashr_i32 s75, s75, 10\n" \
  "s_sub_i32 s74, s49, s78\n" \
  "s_min_i32 s74, s74, s75\n" \
  "s_sub_i32 s75, s75, 92\n" \
  "s_max_i32 s74, s74, s75\n" \
  "s_add_u32 s49, s74, s78\n" \
  "s_sub_u32 s74, s78, s89\n" \
  "s_sub_u32 s75, s76, s88\n" \
  "v_cvt_f32_u32 v24, s74\n" \
  "v_cvt_f32_u32 v25, s75\n" \
  "v_rcp_f32 v25, v25\n" \
  "v_mul_f32 v24, 0x43820000, v24\n" \
  "v_mul_f32 v24, v24, v25\n" \
  "v_cvt_u32_f32 v24, v24\n" \
  "s_nop 0\n" \
  "v_readfirstlane_b32 s74, v24\n" \
  "s_min_u32 s74, s74, 6400\n" \
  "s_cmp_gt_u32 s75, 900\n" \
  "s_cselect_b32 s50, s74, s50\n" \
  "s_cselect_b32 s88, s76, s88\n" \
  "s_cselect_b32 s89, s78, s89\n" \
  "s_waitcnt vmcnt(1)\n" \
  "v_readlane_b32 s74, v15, 3\n" \
  "s_nop 0\n" \
  "s_sub_u32 s75, s74, s51\n" \
  "s_cmp_lt_u32 s75, 16\n" \
  "s_cselect_b32 s51, s74, s51\n" \
  "v_readlane_b32 s74, v15, 0\n" \
  "s_add_u32 s90, s90, s53\n" \
  "s_sub_u32 s75, s74, s90\n" \
  "s_add_u32 s75, s75, 2000\n" \
  "s_cmp_lt_u32 s75, 4000\n" \
  "s_cselect_b32 s90, s74, s90\n" \
  "s_mul_i32 s74, s51, s54\n" \
  "s_add_u32 s74, s74, s90\n" \
  "s_sub_i32 s75, s74, s48\n" \
  "s_mov_b32 s48, s74\n" \
  "s_mul_i32 s75, s75, s50\n" \
  "s_ashr_i32 s75, s75, 10\n" \
  "s_add_u32 s49, s49, s75\n" \
  "s_mov_b32 s71, 0\n" \
  "s_cmp_eq_u32 s68, 64\n" \
  "s_cbranch_scc1 LENDs3_%=\n" \
  "v_readlane_b32 s72, v11, s68\n" \
  "s_nop 0\n" \
  "s_add_u32 s69, s72, 5120\n" \
  "s_add_u32 s70, s72, 17408\n" \
  "s_add_u32 s44, s72, s93\n" \
  "s_add_u32 s45, s72, s94\n" \
  "s_add_u32 s46, s72, s95\n" \
  "v_readlane_b32 s41, v7, s68\n" \
  "s_nop 1\n" \
  "s_branch LOOPs3_%=\n" \
  "LSAs3_%=:\n" \
  "s_add_i32 s71, s71, 48\n" \
  "s_cmp_gt_i32 s71, 0\n" \
  "s_cselect_b32 s74, s54, 0\n" \
  "s_addc_u32 s51, s51, 0\n" \
  "s_sub_i32 s71, s71, s74\n" \
  "s_mul_i32 s74, s51, s54\n" \
  "s_add_u32 s74, s74, s90\n" \
  "s_add_u32 s74, s74, s71\n" \
  "s_sub_i32 s75, s74, s48\n" \
  "s_mov_b32 s48, s74\n" \
  "s_mul_i32 s75, s75, s50\n" \
  "s_ashr_i32 s75, s75, 10\n" \
  "s_add_u32 s49, s49, s75\n" \
  "s_branch LWAs3_%=\n" \
  "LMFs3_%=:\n" \
  "ds_read_b128 v[88:91], v3 offset:8192\n" \
  "ds_read_b128 v[92:95], v3 offset:9216\n" \
  "ds_read_b128 v[96:99], v3 offset:10240\n" \
  "ds_read_b128 v[100:103], v3 offset:11264\n" \
  "ds_read_b128 v[104:107], v3 offset:0\n" \
  "ds_read_b128 v[108:111], v3 offset:1024\n" \
  "ds_read_b128 v[112:115], v3 offset:22528\n" \
  "ds_read_b128 v[116:119], v3 offset:23552\n" \
  "v_mov_b32 v60, -1\n" \
  "v_mov_b32 v61, -1\n" \
  "ds_write_b64 v6, v[60:61] offset:35840\n" \
  "ds_write_b64 v6, v[60:61] offset:36352\n" \
  "ds_write_b64 v6, v[60:61] offset:38400\n" \
  "ds_write_b64 v2, v[60:61] offset:1024\n" \
  "v_mov_b32 v20, 0\n" \
  "v_mov_b32 v21, 0\n" \
  "v_mov_b32 v22, 0\n" \
  "v_mov_b32 v23, 0\n" \
  "v_mov_b32 v64, 0\n" \
  "v_mov_b32 v65, 0\n" \
  "v_mov_b32 v66, 0\n" \
  "v_mov_b32 v67, 0\n" \
  "v_mov_b32 v68, 0\n" \
  "v_mov_b32 v69, 0\n" \
  "v_mov_b32 v70, 0\n" \
  "v_mov_b32 v71, 0\n" \
  "v_mov_b32 v72, 0\n" \
  "v_mov_b32 v73, 0\n" \
  "v_mov_b32 v74, 0\n" \
  "v_mov_b32 v75, 0\n" \
  "v_mov_b32 v76, 0\n" \
  "v_mov_b32 v77, 0\n" \
  "v_mov_b32 v78, 0\n" \
  "v_mov_b32 v79, 0\n" \
  "v_mov_b32 v80, 0\n" \
  "v_mov_b32 v81, 0\n" \
  "v_mov_b32 v82, 0\n" \
  "v_mov_b32 v83, 0\n" \
  "v_mov_b32 v84, 0\n" \
  "v_mov_b32 v85, 0\n" \
  "v_mov_b32 v86, 0\n" \
  "v_mov_b32 v87, 0\n" \
  "s_waitcnt lgkmcnt(0)\n" \
  "s_mov_b32 s71, 0\n" \
  "s_mul_i32 s74, s51, s54\n" \
  "s_add_u32 s74, s74, s90\n" \
  "s_sub_i32 s75, s74, s48\n" \
  "s_mov_b32 s48, s74\n" \
  "s_mul_i32 s75, s75, s50\n" \
  "s_ashr_i32 s75, s75, 10\n" \
  "s_add_u32 s49, s49, s75\n" \
  "LHMs3_%=:\n" \
  "s_add_u32 s48, s48, s54\n" \
  "s_add_u32 s49, s49, s86\n" \
  "s_add_u32 s51, s51, 1\n" \
  "LGMs3_%=:\n" \
  "s_memrealtime s[76:77]\n" \
  "s_memtime s[78:79]\n" \
  "s_waitcnt lgkmcnt(0)\n" \
  "s_lshl_b32 s75, s76, 2\n" \
  "s_sub_i32 s75, s48, s75\n" \
  "s_mul_i32 s75, s75, s50\n" \
  "s_ashr_i32 s75, s75, 10\n" \
  "s_sub_i32 s74, s49, s78\n" \
  "s_min_i32 s74, s74, s75\n" \
  "s_sub_i32 s75, s75, 92\n" \
  "s_max_i32 s74, s74, s75\n" \
  "s_add_u32 s49, s74, s78\n" \
  "s_cmp_gt_i32 s74, 20\n" \
  "s_cbranch_scc1 LGMs3_%=\n" \
  "s_cmp_lt_i32 s74, s87\n" \
  "s_cbranch_scc1 LHMs3_%=\n" \
  "s_barrier\n" \
  "buffer_load_dwordx4 v[24:27], v1, s[36:39], s72 offen offset:0 sc1\n" \
  "s_mov_b32 m0, 68608\n" \
  "s_nop 0\n" \
  "buffer_load_dwordx4 v1, s[36:39], s69 offen sc1 lds\n" \
  "buffer_load_dwordx4 v[28:31], v1, s[36:39], s72 offen offset:1024 sc1\n" \
  "s_mov_b32 m0, 72704\n" \
  "s_nop 0\n" \
  "buffer_load_dwordx4 v1, s[36:39], s70 offen sc1 lds\n" \
  "ds_write_b64 v6, v[60:61] offset:3072\n" \
  "ds_write_b64 v6, v[60:61] offset:3584\n" \
  "ds_write_b64 v6, v[60:61] offset:5632\n" \
  "s_cmp_eq_u32 s91, 0\n" \
  "s_cbranch_scc1 LMs3_%=\n" \
  "s_waitcnt lgkmcnt(0)\n" \
  "s_branch LSAFEs3_%=\n" \
  "LMQs3_%=:\n" \
  "s_waitcnt lgkmcnt(0)\n" \
  "s_barrier\n" \
  "s_barrier\n" \
  "ds_read_b32 v60, v12 offset:512\n" \
  "s_waitcnt lgkmcnt(0)\n" \
  "LMXs3_%=:\n" \
  "v_cmp_lt_u32 vcc, 1, v60\n" \
  "v_readfirstlane_b32 s91, v60\n" \
  "s_waitcnt lgkmcnt(0)\n" \
  "s_nop 2\n" \
  "s_and_b32 s91, s91, 1\n" \
  "s_barrier\n" \
  "s_cbranch_vccnz LMFs3_%=\n" \
  "s_branch LMYs3_%=\n" \
  "LENDs3_%=:\n" \
  "s_cmp_lg_u32 s38, 0\n" \
  "s_cbranch_scc1 LFINs3_%=\n" \
  "s_waitcnt vmcnt(0) lgkmcnt(0)\n" \
  "s_mov_b32 s38, s47\n" \
  "s_mov_b32 s54, s97\n" \
  "s_mov_b32 s96, s98\n" \
  "s_mov_b32 s68, 0\n" \
  "s_mov_b32 s73, 0\n" \
  "s_mov_b32 s56, 0\n" \
  "s_mov_b32 s57, 0\n" \
  "s_branch LPASSs3_%=\n" \
  "LFINs3_%=:\n" \
  "s_branch LDONE_%=\n" \
  "LHOT_%=:\n" \
  "LSAFEs0_%=:\n" \
  "s_memtime s[76:77]\n" \
  "s_waitcnt vmcnt(3)\n" \
  "v_smfmac_f32_16x16x64_bf16 v[64:67], v[24:27], v[88:95], v0\n" \
  "ds_read_b128 v[88:91], v3 offset:2048\n" \
  "v_smfmac_f32_16x16x64_bf16 v[68:71], v[24:27], v[96:103], v0\n" \
  "ds_read_b128 v[92:95], v3 offset:3072\n" \
  "v_smfmac_f32_16x16x64_bf16 v[72:75], v[24:27], a[0:7], v0\n" \
  "ds_read_b128 v[96:99], v3 offset:24576\n" \
  "v_smfmac_f32_16x16x64_bf16 v[76:79], v[24:27], a[8:15], v0\n" \
  "ds_read_b128 v[100:103], v3 offset:25600\n" \
  "v_smfmac_f32_16x16x64_bf16 v[80:83], v[24:27], a[16:23], v0\n" \
  "v_smfmac_f32_16x16x64_bf16 v[84:87], v[24:27], v[104:111], v0\n" \
  "ds_read_b128 v[104:107], v3 offset:12288\n" \
  "v_smfmac_f32_16x16x64_bf16 v[20:23], v[24:27], v[112:119], v0\n" \
  "s_waitcnt vmcnt(2)\n" \
  "s_barrier\n" \
  "ds_read_b128 v[32:35], v2 offset:2048\n" \
  "s_waitcnt vmcnt(1)\n" \
  "v_smfmac_f32_16x16x64_bf16 v[64:67], v[28:31], a[24:31], v0\n" \
  "ds_read_b128 v[36:39], v2 offset:3072\n" \
  "v_smfmac_f32_16x16x64_bf16 v[68:71], v[28:31], a[32:39], v0\n" \
  "ds_read_b128 v[40:43], v2 offset:4096\n" \
  "v_smfmac_f32_16x16x64_bf16 v[72:75], v[28:31], a[40:47], v0\n" \
  "ds_read_b128 v[44:47], v2 offset:5120\n" \
  "s_waitcnt lgkmcnt(7)\n" \
  "v_smfmac_f32_16x16x64_bf16 v[84:87], v[28:31], v[88:95], v0\n" \
  "ds_read_b128 v[108:111], v3 offset:13312\n" \
  "s_waitcnt lgkmcnt(6)\n" \
  "v_smfmac_f32_16x16x64_bf16 v[20:23], v[28:31], v[96:103], v0\n" \
  "ds_read_b128 v[112:115], v3 offset:14336\n" \
  "s_waitcnt lgkmcnt(5)\n" \
  "v_smfmac_f32_16x16x64_bf16 v[64:67], v[32:35], a[64:71], v0\n" \
  "ds_read_b128 v[116:119], v3 offset:15360\n" \
  "v_smfmac_f32_16x16x64_bf16 v[68:71], v[32:35], a[72:79], v0\n" \
  "ds_read_b128 v[88:91], v3 offset:16384\n" \
  "v_smfmac_f32_16x16x64_bf16 v[72:75], v[32:35], a[80:87], v0\n" \
  "ds_read_b128 v[92:95], v3 offset:17408\n" \
  "s_waitcnt lgkmcnt(7)\n" \
  "v_smfmac_f32_16x16x64_bf16 v[64:67], v[36:39], a[104:111], v0\n" \
  "ds_read_b128 v[96:99], v3 offset:18432\n" \
  "s_waitcnt lgkmcnt(5)\n" \
  "v_smfmac_f32_16x16x64_bf16 v[68:71], v[36:39], v[104:111], v0\n" \
  "s_waitcnt vmcnt(0)\n" \
  "s_barrier\n" \
  "v_smfmac_f32_16x16x64_bf16 v[72:75], v[36:39], a[112:119], v0\n" \
  "ds_read_b128 v[48:51], v2 offset:6144\n" \
  "s_waitcnt lgkmcnt(4)\n" \
  "v_smfmac_f32_16x16x64_bf16 v[64:67], v[40:43], v[112:119], v0\n" \
  "ds_read_b128 v[52:55], v2 offset:7168\n" \
  "v_smfmac_f32_16x16x64_bf16 v[68:71], v[40:43], a[136:143], v0\n" \
  "ds_read_b128 v[56:59], v2 offset:8192\n" \
  "v_smfmac_f32_16x16x64_bf16 v[72:75], v[40:43], a[144:151], v0\n" \
  "ds_read_b128 v[60:63], v2 offset:9216\n" \
  "s_waitcnt lgkmcnt(5)\n" \
  "v_smfmac_f32_16x16x64_bf16 v[64:67], v[44:47], v[88:95], v0\n" \
  "ds_read_b128 v[100:103], v3 offset:19456\n" \
  "v_smfmac_f32_16x16x64_bf16 v[68:71], v[44:47], a[176:183], v0\n" \
  "ds_read_b128 v[104:107], v3 offset:20480\n" \
  "v_smfmac_f32_16x16x64_bf16 v[72:75], v[44:47], a[184:191], v0\n" \
  "ds_read_b128 v[108:111], v3 offset:21504\n" \
  "s_waitcnt lgkmcnt(6)\n" \
  "v_smfmac_f32_16x16x64_bf16 v[64:67], v[48:51], a[216:223], v0\n" \
  "ds_read_b128 v[112:115], v3 offset:26624\n" \
  "s_waitcnt lgkmcnt(6)\n" \
  "v_smfmac_f32_16x16x64_bf16 v[64:67], v[52:55], v[128:135], v0\n" \
  "ds_read_b128 v[116:119], v3 offset:27648\n" \
  "s_waitcnt lgkmcnt(6)\n" \
  "v_smfmac_f32_16x16x64_bf16 v[64:67], v[56:59], v[168:175], v0\n" \
  "ds_read_b128 v[88:91], v3 offset:28672\n" \
  "s_waitcnt lgkmcnt(5)\n" \
  "v_smfmac_f32_16x16x64_bf16 v[64:67], v[60:63], v[96:103], v0\n" \
  "ds_read_b128 v[92:95], v3 offset:29696\n" \
  "v_smfmac_f32_16x16x64_bf16 v[68:71], v[48:51], a[224:231], v0\n" \
  "ds_read_b128 v[96:99], v3 offset:4096\n" \
  "s_waitcnt lgkmcnt(5)\n" \
  "v_smfmac_f32_16x16x64_bf16 v[68:71], v[52:55], v[104:111], v0\n" \
  "buffer_load_dword v19, v18, s[36:39], 0 offen sc1\n" \
  "v_cvt_pk_bf16_f32 v64, v64, v65\n" \
  "v_smfmac_f32_16x16x64_bf16 v[68:71], v[56:59], v[176:183], v0\n" \
  "ds_read_b128 v[100:103], v3 offset:5120\n" \
  "v_cvt_pk_bf16_f32 v65, v66, v67\n" \
  "v_smfmac_f32_16x16x64_bf16 v[68:71], v[60:63], v[216:223], v0\n" \
  "ds_write_b64 v6, v[64:65] offset:32768\n" \
  "v_cmp_u_f32 vcc, v64, v65\n" \
  "v_smfmac_f32_16x16x64_bf16 v[72:75], v[48:51], a[232:239], v0\n" \
  "s_waitcnt lgkmcnt(0)\n" \
  "s_barrier\n" \
  "v_smfmac_f32_16x16x64_bf16 v[72:75], v[52:55], v[136:143], v0\n" \
  "ds_read_b128 v[104:107], v3 offset:6144\n" \
  "v_cvt_pk_bf16_f32 v68, v68, v69\n" \
  "v_smfmac_f32_16x16x64_bf16 v[72:75], v[56:59], v[184:191], v0\n" \
  "ds_read_b128 v[108:111], v3 offset:7168\n" \
  "v_cvt_pk_bf16_f32 v69, v70, v71\n" \
  "v_smfmac_f32_16x16x64_bf16 v[72:75], v[60:63], v[224:231], v0\n" \
  "ds_write_b64 v6, v[68:69] offset:33280\n" \
  "v_smfmac_f32_16x16x64_bf16 v[76:79], v[28:31], a[48:55], v0\n" \
  "s_waitcnt lgkmcnt(0)\n" \
  "s_barrier\n" \
  "v_smfmac_f32_16x16x64_bf16 v[80:83], v[28:31], a[56:63], v0\n" \
  "v_cvt_pk_bf16_f32 v72, v72, v73\n" \
  "v_smfmac_f32_16x16x64_bf16 v[20:23], v[32:35], v[112:119], v0\n" \
  "ds_read_b64_tr_b16 v[24:25], v9 offset:32768\n" \
  "v_cvt_pk_bf16_f32 v73, v74, v75\n" \
  "v_smfmac_f32_16x16x64_bf16 v[20:23], v[36:39], v[88:95], v0\n" \
  "ds_write_b64 v6, v[72:73] offset:36864\n" \
  "v_smfmac_f32_16x16x64_bf16 v[84:87], v[32:35], v[96:103], v0\n" \
  "s_waitcnt lgkmcnt(0)\n" \
  "s_barrier\n" \
  "ds_read_b64_tr_b16 v[28:29], v9 offset:33792\n" \
  "v_smfmac_f32_16x16x64_bf16 v[84:87], v[36:39], v[104:111], v0\n" \
  "ds_write_b128 v2, v[20:23] offset:0\n" \
  "v_smfmac_f32_16x16x64_bf16 v[76:79], v[32:35], a[88:95], v0\n" \
  "s_waitcnt lgkmcnt(0)\n" \
  "s_barrier\n" \
  "ds_read_b64_tr_b16 v[112:113], v9 offset:34816\n" \
  "v_smfmac_f32_16x16x64_bf16 v[76:79], v[36:39], a[120:127], v0\n" \
  "ds_read_b64_tr_b16 v[64:65], v9 offset:35840\n" \
  "v_smfmac_f32_16x16x64_bf16 v[76:79], v[40:43], a[152:159], v0\n" \
  "ds_read_b64_tr_b16 v[26:27], v9 offset:33280\n" \
  "v_smfmac_f32_16x16x64_bf16 v[76:79], v[44:47], a[192:199], v0\n" \
  "ds_read_b64_tr_b16 v[30:31], v9 offset:34304\n" \
  "v_smfmac_f32_16x16x64_bf16 v[76:79], v[48:51], a[240:247], v0\n" \
  "ds_read_b64_tr_b16 v[114:115], v9 offset:35328\n" \
  "v_smfmac_f32_16x16x64_bf16 v[76:79], v[52:55], v[144:151], v0\n" \
  "ds_read_b64_tr_b16 v[66:67], v9 offset:36352\n" \
  "v_smfmac_f32_16x16x64_bf16 v[76:79], v[56:59], v[192:199], v0\n" \
  "ds_read_b64_tr_b16 v[68:69], v9 offset:36864\n" \
  "v_smfmac_f32_16x16x64_bf16 v[76:79], v[60:63], v[232:239], v0\n" \
  "ds_read_b64_tr_b16 v[70:71], v9 offset:37376\n" \
  "v_smfmac_f32_16x16x64_bf16 v[80:83], v[32:35], a[96:103], v0\n" \
  "ds_read_b64_tr_b16 v[72:73], v9 offset:37888\n" \
  "v_smfmac_f32_16x16x64_bf16 v[80:83], v[36:39], a[128:135], v0\n" \
  "ds_read_b64_tr_b16 v[74:75], v9 offset:38400\n" \
  "v_cvt_pk_bf16_f32 v76, v76, v77\n" \
  "v_smfmac_f32_16x16x64_bf16 v[80:83], v[40:43], a[160:167], v0\n" \
  "v_cvt_pk_bf16_f32 v77, v78, v79\n" \
  "v_smfmac_f32_16x16x64_bf16 v[80:83], v[44:47], a[200:207], v0\n" \
  "ds_write_b64 v6, v[76:77] offset:0\n" \
  "v_smfmac_f32_16x16x64_bf16 v[80:83], v[48:51], a[248:255], v0\n" \
  "s_waitcnt lgkmcnt(0)\n" \
  "s_barrier\n" \
  "v_smfmac_f32_16x16x64_bf16 v[80:83], v[52:55], v[152:159], v0\n" \
  "v_smfmac_f32_16x16x64_bf16 v[80:83], v[56:59], v[200:207], v0\n" \
  "ds_read_b64_tr_b16 v[88:89], v9 offset:0\n" \
  "v_smfmac_f32_16x16x64_bf16 v[80:83], v[60:63], v[240:247], v0\n" \
  "ds_read_b64_tr_b16 v[92:93], v9 offset:1024\n" \
  "v_smfmac_f32_16x16x64_bf16 v[84:87], v[40:43], a[168:175], v0\n" \
  "ds_read_b64_tr_b16 v[96:97], v9 offset:2048\n" \
  "v_smfmac_f32_16x16x64_bf16 v[84:87], v[44:47], a[208:215], v0\n" \
  "ds_read_b64_tr_b16 v[100:101], v9 offset:3072\n" \
  "v_cvt_pk_bf16_f32 v80, v80, v81\n" \
  "v_smfmac_f32_16x16x64_bf16 v[84:87], v[48:51], v[120:127], v0\n" \
  "v_cvt_pk_bf16_f32 v81, v82, v83\n" \
  "v_smfmac_f32_16x16x64_bf16 v[84:87], v[52:55], v[160:167], v0\n" \
  "ds_write_b64 v6, v[80:81] offset:512\n" \
  "v_smfmac_f32_16x16x64_bf16 v[84:87], v[56:59], v[208:215], v0\n" \
  "s_waitcnt lgkmcnt(0)\n" \
  "s_barrier\n" \
  "v_smfmac_f32_16x16x64_bf16 v[84:87], v[60:63], v[248:255], v0\n" \
  "s_waitcnt lgkmcnt(6)\n" \
  "ds_read_b64_tr_b16 v[90:91], v9 offset:512\n" \
  "ds_read_b64_tr_b16 v[102:103], v9 offset:3584\n" \
  "s_nop 4\n" \
  "v_cvt_pk_bf16_f32 v84, v84, v85\n" \
  "v_cvt_pk_bf16_f32 v85, v86, v87\n" \
  "ds_write_b64 v6, v[84:85] offset:4096\n" \
  "s_waitcnt lgkmcnt(2)\n" \
  "v_mfma_f32_16x16x32_bf16 v[116:119], v[88:91], v[24:27], 0\n" \
  "s_waitcnt lgkmcnt(1)\n" \
  "v_mfma_f32_16x16x32_bf16 v[116:119], v[100:103], v[64:67], v[116:119]\n" \
  "s_waitcnt lgkmcnt(0)\n" \
  "s_branch LSRs0_%=\n" \
  "LSAFEs1_%=:\n" \
  "buffer_store_dwordx4 v[52:55], v16, s[36:39], s42 offen sc1\n" \
  "buffer_store_dword v56, v17, s[36:39], s42 offen sc1\n" \
  "s_mov_b32 s42, 0x40000000\n" \
  "s_waitcnt vmcnt(5)\n" \
  "v_smfmac_f32_16x16x64_bf16 v[64:67], v[24:27], v[88:95], v0\n" \
  "ds_read_b128 v[88:91], v3 offset:16384\n" \
  "v_smfmac_f32_16x16x64_bf16 v[68:71], v[24:27], v[96:103], v0\n" \
  "ds_read_b128 v[92:95], v3 offset:17408\n" \
  "v_smfmac_f32_16x16x64_bf16 v[72:75], v[24:27], a[0:7], v0\n" \
  "ds_read_b128 v[96:99], v3 offset:18432\n" \
  "v_smfmac_f32_16x16x64_bf16 v[76:79], v[24:27], a[8:15], v0\n" \
  "ds_read_b128 v[100:103], v3 offset:19456\n" \
  "v_smfmac_f32_16x16x64_bf16 v[80:83], v[24:27], a[16:23], v0\n" \
  "s_waitcnt vmcnt(4)\n" \
  "s_barrier\n" \
  "ds_read_b128 v[32:35], v2 offset:2048\n" \
  "s_waitcnt vmcnt(3)\n" \
  "v_smfmac_f32_16x16x64_bf16 v[64:67], v[28:31], a[24:31], v0\n" \
  "ds_read_b128 v[36:39], v2 offset:3072\n" \
  "v_smfmac_f32_16x16x64_bf16 v[68:71], v[28:31], a[32:39], v0\n" \
  "ds_read_b128 v[40:43], v2 offset:4096\n" \
  "v_smfmac_f32_16x16x64_bf16 v[72:75], v[28:31], a[40:47], v0\n" \
  "ds_read_b128 v[44:47], v2 offset:5120\n" \
  "v_smfmac_f32_16x16x64_bf16 v[76:79], v[28:31], a[48:55], v0\n" \
  "v_smfmac_f32_16x16x64_bf16 v[80:83], v[28:31], a[56:63], v0\n" \
  "s_waitcnt lgkmcnt(3)\n" \
  "v_smfmac_f32_16x16x64_bf16 v[64:67], v[32:35], a[64:71], v0\n" \
  "v_smfmac_f32_16x16x64_bf16 v[68:71], v[32:35], a[72:79], v0\n" \
  "v_smfmac_f32_16x16x64_bf16 v[72:75], v[32:35], a[80:87], v0\n" \
  "s_waitcnt lgkmcnt(2)\n" \
  "v_smfmac_f32_16x16x64_bf16 v[64:67], v[36:39], a[104:111], v0\n" \
  "v_smfmac_f32_16x16x64_bf16 v[68:71], v[36:39], v[104:111], v0\n" \
  "s_waitcnt vmcnt(2)\n" \
  "s_barrier\n" \
  "v_smfmac_f32_16x16x64_bf16 v[72:75], v[36:39], a[112:119], v0\n" \
  "ds_read_b128 v[48:51], v2 offset:6144\n" \
  "s_waitcnt lgkmcnt(2)\n" \
  "v_smfmac_f32_16x16x64_bf16 v[64:67], v[40:43], v[112:119], v0\n" \
  "ds_read_b128 v[52:55], v2 offset:7168\n" \
  "v_smfmac_f32_16x16x64_bf16 v[68:71], v[40:43], a[136:143], v0\n" \
  "ds_read_b128 v[56:59], v2 offset:8192\n" \
  "v_smfmac_f32_16x16x64_bf16 v[72:75], v[40:43], a[144:151], v0\n" \
  "ds_read_b128 v[60:63], v2 offset:9216\n" \
  "s_waitcnt lgkmcnt(4)\n" \
  "v_smfmac_f32_16x16x64_bf16 v[64:67], v[44:47], v[88:95], v0\n" \
  "ds_read_b128 v[104:107], v3 offset:20480\n" \
  "v_smfmac_f32_16x16x64_bf16 v[68:71], v[44:47], a[176:183], v0\n" \
  "ds_read_b128 v[108:111], v3 offset:21504\n" \
  "v_smfmac_f32_16x16x64_bf16 v[72:75], v[44:47], a[184:191], v0\n" \
  "ds_read_b128 v[88:91], v20 offset:0\n" \
  "s_waitcnt lgkmcnt(6)\n" \
  "v_smfmac_f32_16x16x64_bf16 v[64:67], v[48:51], a[216:223], v0\n" \
  "ds_read_b128 v[92:95], v20 offset:2048\n" \
  "s_waitcnt lgkmcnt(6)\n" \
  "v_smfmac_f32_16x16x64_bf16 v[64:67], v[52:55], v[128:135], v0\n" \
  "s_waitcnt lgkmcnt(5)\n" \
  "v_smfmac_f32_16x16x64_bf16 v[64:67], v[56:59], v[168:175], v0\n" \
  "s_waitcnt lgkmcnt(4)\n" \
  "v_smfmac_f32_16x16x64_bf16 v[64:67], v[60:63], v[96:103], v0\n" \
  "ds_read_b128 v[96:99], v20 offset:4096\n" \
  "v_smfmac_f32_16x16x64_bf16 v[68:71], v[48:51], a[224:231], v0\n" \
  "ds_read_b128 v[100:103], v20 offset:6144\n" \
  "s_waitcnt lgkmcnt(4)\n" \
  "v_smfmac_f32_16x16x64_bf16 v[68:71], v[52:55], v[104:111], v0\n" \
  "ds_read_b64 v[104:105], v21 offset:0\n" \
  "v_cvt_pk_bf16_f32 v64, v64, v65\n" \
  "v_smfmac_f32_16x16x64_bf16 v[68:71], v[56:59], v[176:183], v0\n" \
  "ds_read_b64 v[106:107], v21 offset:1024\n" \
  "v_cvt_pk_bf16_f32 v65, v66, v67\n" \
  "v_smfmac_f32_16x16x64_bf16 v[68:71], v[60:63], v[216:223], v0\n" \
  "ds_write_b64 v6, v[64:65] offset:33792\n" \
  "v_cmp_u_f32 vcc, v64, v65\n" \
  "v_smfmac_f32_16x16x64_bf16 v[72:75], v[48:51], a[232:239], v0\n" \
  "s_waitcnt lgkmcnt(0)\n" \
  "s_barrier\n" \
  "v_smfmac_f32_16x16x64_bf16 v[72:75], v[52:55], v[136:143], v0\n" \
  "ds_read_b64 v[108:109], v21 offset:2048\n" \
  "v_cvt_pk_bf16_f32 v68, v68, v69\n" \
  "v_smfmac_f32_16x16x64_bf16 v[72:75], v[56:59], v[184:191], v0\n" \
  "ds_read_b64 v[110:111], v21 offset:3072\n" \
  "v_cvt_pk_bf16_f32 v69, v70, v71\n" \
  "v_smfmac_f32_16x16x64_bf16 v[72:75], v[60:63], v[224:231], v0\n" \
  "ds_write_b64 v6, v[68:69] offset:34304\n" \
  "v_smfmac_f32_16x16x64_bf16 v[76:79], v[32:35], a[88:95], v0\n" \
  "s_waitcnt lgkmcnt(0)\n" \
  "s_barrier\n" \
  "ds_read_b64_tr_b16 v[24:25], v9 offset:32768\n" \
  "v_smfmac_f32_16x16x64_bf16 v[76:79], v[36:39], a[120:127], v0\n" \
  "ds_read_b64_tr_b16 v[28:29], v9 offset:33792\n" \
  "v_cvt_pk_bf16_f32 v72, v72, v73\n" \
  "v_smfmac_f32_16x16x64_bf16 v[76:79], v[40:43], a[152:159], v0\n" \
  "ds_read_b64_tr_b16 v[112:113], v9 offset:34816\n" \
  "ds_read_b64_tr_b16 v[64:65], v9 offset:35840\n" \
  "v_cvt_pk_bf16_f32 v73, v74, v75\n" \
  "v_smfmac_f32_16x16x64_bf16 v[76:79], v[44:47], a[192:199], v0\n" \
  "ds_write_b64 v6, v[72:73] offset:37376\n" \
  "v_smfmac_f32_16x16x64_bf16 v[76:79], v[48:51], a[240:247], v0\n" \
  "s_waitcnt lgkmcnt(0)\n" \
  "s_barrier\n" \
  "s_barrier\n" \
  "ds_read_b64_tr_b16 v[26:27], v9 offset:33280\n" \
  "v_smfmac_f32_16x16x64_bf16 v[76:79], v[52:55], v[144:151], v0\n" \
  "ds_read_b64_tr_b16 v[30:31], v9 offset:34304\n" \
  "v_smfmac_f32_16x16x64_bf16 v[76:79], v[56:59], v[192:199], v0\n" \
  "ds_read_b64_tr_b16 v[114:115], v9 offset:35328\n" \
  "ds_read_b64_tr_b16 v[66:67], v9 offset:36352\n" \
  "v_smfmac_f32_16x16x64_bf16 v[76:79], v[60:63], v[232:239], v0\n" \
  "ds_read_b64_tr_b16 v[68:69], v9 offset:36864\n" \
  "v_smfmac_f32_16x16x64_bf16 v[80:83], v[32:35], a[96:103], v0\n" \
  "ds_read_b64_tr_b16 v[70:71], v9 offset:37376\n" \
  "v_smfmac_f32_16x16x64_bf16 v[80:83], v[36:39], a[128:135], v0\n" \
  "ds_read_b64_tr_b16 v[72:73], v9 offset:37888\n" \
  "ds_read_b64_tr_b16 v[74:75], v9 offset:38400\n" \
  "v_cvt_pk_bf16_f32 v76, v76, v77\n" \
  "s_waitcnt lgkmcnt(7)\n" \
  "v_mfma_f32_16x16x32_bf16 v[116:119], v[88:91], v[24:27], 0\n" \
  "v_cvt_pk_bf16_f32 v77, v78, v79\n" \
  "s_waitcnt lgkmcnt(6)\n" \
  "v_mfma_f32_16x16x32_bf16 v[116:119], v[92:95], v[28:31], v[116:119]\n" \
  "ds_write_b64 v6, v[76:77] offset:1024\n" \
  "s_waitcnt lgkmcnt(6)\n" \
  "v_mfma_f32_16x16x32_bf16 v[116:119], v[96:99], v[112:115], v[116:119]\n" \
  "s_waitcnt lgkmcnt(0)\n" \
  "s_barrier\n" \
  "v_mfma_f32_16x16x32_bf16 v[116:119], v[100:103], v[64:67], v[116:119]\n" \
  "v_smfmac_f32_16x16x64_bf16 v[80:83], v[40:43], a[160:167], v0\n" \
  "v_mfma_f32_16x16x32_bf16 v[116:119], v[104:107], v[68:71], v[116:119]\n" \
  "ds_read_b128 v[84:87], v2 offset:0\n" \
  "v_mfma_f32_16x16x32_bf16 v[116:119], v[108:111], v[72:75], v[116:119]\n" \
  "v_smfmac_f32_16x16x64_bf16 v[80:83], v[44:47], a[200:207], v0\n" \
  "v_smfmac_f32_16x16x64_bf16 v[80:83], v[48:51], a[248:255], v0\n" \
  "v_add_f32_dpp v88, v116, v116 row_ror:8 row_mask:0xf bank_mask:0x3\n" \
  "v_add_f32_dpp v89, v117, v117 row_ror:8 row_mask:0xf bank_mask:0x3\n" \
  "v_smfmac_f32_16x16x64_bf16 v[80:83], v[52:55], v[152:159], v0\n" \
  "v_add_f32_dpp v88, v118, v118 row_ror:8 row_mask:0xf bank_mask:0xc\n" \
  "v_add_f32_dpp v89, v119, v119 row_ror:8 row_mask:0xf bank_mask:0xc\n" \
  "v_smfmac_f32_16x16x64_bf16 v[80:83], v[56:59], v[200:207], v0\n" \
  "v_mul_f32 v88, v4, v88\n" \
  "v_mul_f32 v89, v4, v89\n" \
  "v_smfmac_f32_16x16x64_bf16 v[80:83], v[60:63], v[240:247], v0\n" \
  "v_exp_f32 v88, v88\n" \
  "v_exp_f32 v89, v89\n" \
  "s_waitcnt lgkmcnt(0)\n" \
  "v_smfmac_f32_16x16x64_bf16 v[84:87], v[40:43], a[168:175], v0\n" \
  "v_cmp_o_f32 vcc, v88, v89\n" \
  "v_cvt_pk_bf16_f32 v91, v88, v89\n" \
  "v_smfmac_f32_16x16x64_bf16 v[84:87], v[44:47], a[208:215], v0\n" \
  "s_mov_b64 exec, vcc\n" \
  "s_waitcnt vmcnt(0)\n" \
  "buffer_store_dword v91, v22, s[36:39], s72 offen offset:0 sc1\n" \
  "v_cvt_pk_bf16_f32 v80, v80, v81\n" \
  "v_smfmac_f32_16x16x64_bf16 v[84:87], v[48:51], v[120:127], v0\n" \
  "buffer_store_dword v91, v22, s[36:39], s44 offen offset:0 sc1\n" \
  "v_cvt_pk_bf16_f32 v81, v82, v83\n" \
  "v_smfmac_f32_16x16x64_bf16 v[84:87], v[52:55], v[160:167], v0\n" \
  "ds_write_b64 v6, v[80:81] offset:1536\n" \
  "v_smfmac_f32_16x16x64_bf16 v[84:87], v[56:59], v[208:215], v0\n" \
  "s_waitcnt lgkmcnt(0)\n" \
  "s_barrier\n" \
  "buffer_store_dword v91, v22, s[36:39], s45 offen offset:0 sc1\n" \
  "v_smfmac_f32_16x16x64_bf16 v[84:87], v[60:63], v[248:255], v0\n" \
  "buffer_store_dword v91, v22, s[36:39], s46 offen offset:0 sc1\n" \
  "s_mov_b64 exec, -1\n" \
  "v_add_f32 v90, v88, v89\n" \
  "ds_write_b32 v12, v90 offset:0\n" \
  "s_nop 3\n" \
  "v_cvt_pk_bf16_f32 v84, v84, v85\n" \
  "v_cvt_pk_bf16_f32 v85, v86, v87\n" \
  "ds_write_b64 v6, v[84:85] offset:4608\n" \
  "s_waitcnt lgkmcnt(0)\n" \
  "s_branch LSRs1_%=\n" \
  "LSAFEs2_%=:\n" \
  "buffer_store_dwordx4 v[52:55], v16, s[36:39], s42 offen sc1\n" \
  "buffer_store_dword v56, v17, s[36:39], s42 offen sc1\n" \
  "s_mov_b32 s42, 0x40000000\n" \
  "s_waitcnt vmcnt(5)\n" \
  "v_smfmac_f32_16x16x64_bf16 v[64:67], v[24:27], v[88:95], v0\n" \
  "ds_read_b128 v[88:91], v3 offset:16384\n" \
  "v_smfmac_f32_16x16x64_bf16 v[68:71], v[24:27], v[96:103], v0\n" \
  "ds_read_b128 v[92:95], v3 offset:17408\n" \
  "v_smfmac_f32_16x16x64_bf16 v[72:75], v[24:27], a[0:7], v0\n" \
  "ds_read_b128 v[96:99], v3 offset:18432\n" \
  "v_smfmac_f32_16x16x64_bf16 v[76:79], v[24:27], a[8:15], v0\n" \
  "ds_read_b128 v[100:103], v3 offset:19456\n" \
  "v_smfmac_f32_16x16x64_bf16 v[80:83], v[24:27], a[16:23], v0\n" \
  "s_waitcnt vmcnt(4)\n" \
  "s_barrier\n" \
  "ds_read_b128 v[32:35], v2 offset:2048\n" \
  "s_waitcnt vmcnt(3)\n" \
  "v_smfmac_f32_16x16x64_bf16 v[64:67], v[28:31], a[24:31], v0\n" \
  "ds_read_b128 v[36:39], v2 offset:3072\n" \
  "v_smfmac_f32_16x16x64_bf16 v[68:71], v[28:31], a[32:39], v0\n" \
  "ds_read_b128 v[40:43], v2 offset:4096\n" \
  "v_smfmac_f32_16x16x64_bf16 v[72:75], v[28:31], a[40:47], v0\n" \
  "ds_read_b128 v[44:47], v2 offset:5120\n" \
  "v_smfmac_f32_16x16x64_bf16 v[76:79], v[28:31], a[48:55], v0\n" \
  "v_smfmac_f32_16x16x64_bf16 v[80:83], v[28:31], a[56:63], v0\n" \
  "s_waitcnt lgkmcnt(3)\n" \
  "v_smfmac_f32_16x16x64_bf16 v[64:67], v[32:35], a[64:71], v0\n" \
  "v_smfmac_f32_16x16x64_bf16 v[68:71], v[32:35], a[72:79], v0\n" \
  "v_smfmac_f32_16x16x64_bf16 v[72:75], v[32:35], a[80:87], v0\n" \
  "s_waitcnt lgkmcnt(2)\n" \
  "v_smfmac_f32_16x16x64_bf16 v[64:67], v[36:39], a[104:111], v0\n" \
  "v_smfmac_f32_16x16x64_bf16 v[68:71], v[36:39], v[104:111], v0\n" \
  "s_waitcnt vmcnt(2)\n" \
  "s_barrier\n" \
  "v_smfmac_f32_16x16x64_bf16 v[72:75], v[36:39], a[112:119], v0\n" \
  "ds_read_b128 v[48:51], v2 offset:6144\n" \
  "s_waitcnt lgkmcnt(2)\n" \
  "v_smfmac_f32_16x16x64_bf16 v[64:67], v[40:43], v[112:119], v0\n" \
  "ds_read_b128 v[52:55], v2 offset:7168\n" \
  "v_smfmac_f32_16x16x64_bf16 v[68:71], v[40:43], a[136:143], v0\n" \
  "ds_read_b128 v[56:59], v2 offset:8192\n" \
  "v_smfmac_f32_16x16x64_bf16 v[72:75], v[40:43], a[144:151], v0\n" \
  "ds_read_b128 v[60:63], v2 offset:9216\n" \
  "s_waitcnt lgkmcnt(4)\n" \
  "v_smfmac_f32_16x16x64_bf16 v[64:67], v[44:47], v[88:95], v0\n" \
  "ds_read_b128 v[104:107], v3 offset:20480\n" \
  "v_smfmac_f32_16x16x64_bf16 v[68:71], v[44:47], a[176:183], v0\n" \
  "ds_read_b128 v[108:111], v3 offset:21504\n" \
  "v_smfmac_f32_16x16x64_bf16 v[72:75], v[44:47], a[184:191], v0\n" \
  "ds_read_b128 v[88:91], v20 offset:0\n" \
  "s_waitcnt lgkmcnt(6)\n" \
  "v_smfmac_f32_16x16x64_bf16 v[64:67], v[48:51], a[216:223], v0\n" \
  "ds_read_b128 v[92:95], v20 offset:2048\n" \
  "s_waitcnt lgkmcnt(6)\n" \
  "v_smfmac_f32_16x16x64_bf16 v[64:67], v[52:55], v[128:135], v0\n" \
  "s_waitcnt lgkmcnt(5)\n" \
  "v_smfmac_f32_16x16x64_bf16 v[64:67], v[56:59], v[168:175], v0\n" \
  "s_waitcnt lgkmcnt(4)\n" \
  "v_smfmac_f32_16x16x64_bf16 v[64:67], v[60:63], v[96:103], v0\n" \
  "ds_read_b128 v[96:99], v20 offset:4096\n" \
  "v_smfmac_f32_16x16x64_bf16 v[68:71], v[48:51], a[224:231], v0\n" \
  "ds_read_b128 v[100:103], v20 offset:6144\n" \
  "s_waitcnt lgkmcnt(4)\n" \
  "v_smfmac_f32_16x16x64_bf16 v[68:71], v[52:55], v[104:111], v0\n" \
  "ds_read_b64 v[104:105], v21 offset:0\n" \
  "v_cvt_pk_bf16_f32 v64, v64, v65\n" \
  "v_smfmac_f32_16x16x64_bf16 v[68:71], v[56:59], v[176:183], v0\n" \
  "ds_read_b64 v[106:107], v21 offset:1024\n" \
  "v_cvt_pk_bf16_f32 v65, v66, v67\n" \
  "v_smfmac_f32_16x16x64_bf16 v[68:71], v[60:63], v[216:223], v0\n" \
  "ds_write_b64 v6, v[64:65] offset:34816\n" \
  "v_cmp_u_f32 vcc, v64, v65\n" \
  "v_smfmac_f32_16x16x64_bf16 v[72:75], v[48:51], a[232:239], v0\n" \
  "s_waitcnt lgkmcnt(0)\n" \
  "s_barrier\n" \
  "v_smfmac_f32_16x16x64_bf16 v[72:75], v[52:55], v[136:143], v0\n" \
  "ds_read_b64 v[108:109], v21 offset:2048\n" \
  "v_cvt_pk_bf16_f32 v68, v68, v69\n" \
  "v_smfmac_f32_16x16x64_bf16 v[72:75], v[56:59], v[184:191], v0\n" \
  "ds_read_b64 v[110:111], v21 offset:3072\n" \
  "v_cvt_pk_bf16_f32 v69, v70, v71\n" \
  "v_smfmac_f32_16x16x64_bf16 v[72:75], v[60:63], v[224:231], v0\n" \
  "ds_write_b64 v6, v[68:69] offset:35328\n" \
  "v_smfmac_f32_16x16x64_bf16 v[76:79], v[32:35], a[88:95], v0\n" \
  "s_waitcnt lgkmcnt(0)\n" \
  "s_barrier\n" \
  "ds_read_b64_tr_b16 v[24:25], v9 offset:32768\n" \
  "v_smfmac_f32_16x16x64_bf16 v[76:79], v[36:39], a[120:127], v0\n" \
  "ds_read_b64_tr_b16 v[28:29], v9 offset:33792\n" \
  "v_cvt_pk_bf16_f32 v72, v72, v73\n" \
  "v_smfmac_f32_16x16x64_bf16 v[76:79], v[40:43], a[152:159], v0\n" \
  "ds_read_b64_tr_b16 v[112:113], v9 offset:34816\n" \
  "ds_read_b64_tr_b16 v[64:65], v9 offset:35840\n" \
  "v_cvt_pk_bf16_f32 v73, v74, v75\n" \
  "v_smfmac_f32_16x16x64_bf16 v[76:79], v[44:47], a[192:199], v0\n" \
  "ds_write_b64 v6, v[72:73] offset:37888\n" \
  "v_smfmac_f32_16x16x64_bf16 v[76:79], v[48:51], a[240:247], v0\n" \
  "s_waitcnt lgkmcnt(0)\n" \
  "s_barrier\n" \
  "s_barrier\n" \
  "ds_read_b64_tr_b16 v[26:27], v9 offset:33280\n" \
  "v_smfmac_f32_16x16x64_bf16 v[76:79], v[52:55], v[144:151], v0\n" \
  "ds_read_b64_tr_b16 v[30:31], v9 offset:34304\n" \
  "v_smfmac_f32_16x16x64_bf16 v[76:79], v[56:59], v[192:199], v0\n" \
  "ds_read_b64_tr_b16 v[114:115], v9 offset:35328\n" \
  "ds_read_b64_tr_b16 v[66:67], v9 offset:36352\n" \
  "v_smfmac_f32_16x16x64_bf16 v[76:79], v[60:63], v[232:239], v0\n" \
  "ds_read_b64_tr_b16 v[68:69], v9 offset:36864\n" \
  "v_smfmac_f32_16x16x64_bf16 v[80:83], v[32:35], a[96:103], v0\n" \
  "ds_read_b64_tr_b16 v[70:71], v9 offset:37376\n" \
  "v_smfmac_f32_16x16x64_bf16 v[80:83], v[36:39], a[128:135], v0\n" \
  "ds_read_b64_tr_b16 v[72:73], v9 offset:37888\n" \
  "ds_read_b64_tr_b16 v[74:75], v9 offset:38400\n" \
  "v_cvt_pk_bf16_f32 v76, v76, v77\n" \
  "s_waitcnt lgkmcnt(7)\n" \
  "v_mfma_f32_16x16x32_bf16 v[116:119], v[88:91], v[24:27], 0\n" \
  "v_cvt_pk_bf16_f32 v77, v78, v79\n" \
  "s_waitcnt lgkmcnt(6)\n" \
  "v_mfma_f32_16x16x32_bf16 v[116:119], v[92:95], v[28:31], v[116:119]\n" \
  "ds_write_b64 v6, v[76:77] offset:2048\n" \
  "s_waitcnt lgkmcnt(6)\n" \
  "v_mfma_f32_16x16x32_bf16 v[116:119], v[96:99], v[112:115], v[116:119]\n" \
  "s_waitcnt lgkmcnt(0)\n" \
  "s_barrier\n" \
  "v_mfma_f32_16x16x32_bf16 v[116:119], v[100:103], v[64:67], v[116:119]\n" \
  "v_smfmac_f32_16x16x64_bf16 v[80:83], v[40:43], a[160:167], v0\n" \
  "v_mfma_f32_16x16x32_bf16 v[116:119], v[104:107], v[68:71], v[116:119]\n" \
  "ds_read_b128 v[84:87], v2 offset:1024\n" \
  "v_mfma_f32_16x16x32_bf16 v[116:119], v[108:111], v[72:75], v[116:119]\n" \
  "v_smfmac_f32_16x16x64_bf16 v[80:83], v[44:47], a[200:207], v0\n" \
  "v_smfmac_f32_16x16x64_bf16 v[80:83], v[48:51], a[248:255], v0\n" \
  "v_add_f32_dpp v88, v116, v116 row_ror:8 row_mask:0xf bank_mask:0x3\n" \
  "v_add_f32_dpp v89, v117, v117 row_ror:8 row_mask:0xf bank_mask:0x3\n" \
  "v_smfmac_f32_16x16x64_bf16 v[80:83], v[52:55], v[152:159], v0\n" \
  "v_add_f32_dpp v88, v118, v118 row_ror:8 row_mask:0xf bank_mask:0xc\n" \
  "v_add_f32_dpp v89, v119, v119 row_ror:8 row_mask:0xf bank_mask:0xc\n" \
  "v_smfmac_f32_16x16x64_bf16 v[80:83], v[56:59], v[200:207], v0\n" \
  "v_mul_f32 v88, v4, v88\n" \
  "v_mul_f32 v89, v4, v89\n" \
  "v_smfmac_f32_16x16x64_bf16 v[80:83], v[60:63], v[240:247], v0\n" \
  "v_exp_f32 v88, v88\n" \
  "v_exp_f32 v89, v89\n" \
  "s_waitcnt lgkmcnt(0)\n" \
  "v_smfmac_f32_16x16x64_bf16 v[84:87], v[40:43], a[168:175], v0\n" \
  "v_cmp_o_f32 vcc, v88, v89\n" \
  "v_cvt_pk_bf16_f32 v91, v88, v89\n" \
  "v_smfmac_f32_16x16x64_bf16 v[84:87], v[44:47], a[208:215], v0\n" \
  "s_mov_b64 exec, vcc\n" \
  "s_waitcnt vmcnt(0)\n" \
  "buffer_store_dword v91, v22, s[36:39], s72 offen offset:0 sc1\n" \
  "v_cvt_pk_bf16_f32 v80, v80, v81\n" \
  "v_smfmac_f32_16x16x64_bf16 v[84:87], v[48:51], v[120:127], v0\n" \
  "buffer_store_dword v91, v22, s[36:39], s44 offen offset:0 sc1\n" \
  "v_cvt_pk_bf16_f32 v81, v82, v83\n" \
  "v_smfmac_f32_16x16x64_bf16 v[84:87], v[52:55], v[160:167], v0\n" \
  "ds_write_b64 v6, v[80:81] offset:2560\n" \
  "v_smfmac_f32_16x16x64_bf16 v[84:87], v[56:59], v[208:215], v0\n" \
  "s_waitcnt lgkmcnt(0)\n" \
  "s_barrier\n" \
  "buffer_store_dword v91, v22, s[36:39], s45 offen offset:0 sc1\n" \
  "v_smfmac_f32_16x16x64_bf16 v[84:87], v[60:63], v[248:255], v0\n" \
  "buffer_store_dword v91, v22, s[36:39], s46 offen offset:0 sc1\n" \
  "s_mov_b64 exec, -1\n" \
  "v_add_f32 v90, v88, v89\n" \
  "ds_write_b32 v12, v90 offset:256\n" \
  "s_nop 3\n" \
  "v_cvt_pk_bf16_f32 v84, v84, v85\n" \
  "v_cvt_pk_bf16_f32 v85, v86, v87\n" \
  "ds_write_b64 v6, v[84:85] offset:5120\n" \
  "s_waitcnt lgkmcnt(0)\n" \
  "s_branch LSRs2_%=\n" \
  "LSAFEs3_%=:\n" \
  "s_waitcnt vmcnt(3)\n" \
  "v_smfmac_f32_16x16x64_bf16 v[64:67], v[24:27], v[88:95], v0\n" \
  "ds_read_b128 v[88:91], v3 offset:2048\n" \
  "v_smfmac_f32_16x16x64_bf16 v[68:71], v[24:27], v[96:103], v0\n" \
  "ds_read_b128 v[92:95], v3 offset:3072\n" \
  "v_smfmac_f32_16x16x64_bf16 v[72:75], v[24:27], a[0:7], v0\n" \
  "ds_read_b128 v[96:99], v3 offset:24576\n" \
  "v_smfmac_f32_16x16x64_bf16 v[76:79], v[24:27], a[8:15], v0\n" \
  "ds_read_b128 v[100:103], v3 offset:25600\n" \
  "v_smfmac_f32_16x16x64_bf16 v[80:83], v[24:27], a[16:23], v0\n" \
  "v_smfmac_f32_16x16x64_bf16 v[84:87], v[24:27], v[104:111], v0\n" \
  "ds_read_b128 v[104:107], v3 offset:12288\n" \
  "v_smfmac_f32_16x16x64_bf16 v[20:23], v[24:27], v[112:119], v0\n" \
  "s_waitcnt vmcnt(2)\n" \
  "s_barrier\n" \
  "ds_read_b128 v[32:35], v2 offset:2048\n" \
  "s_waitcnt vmcnt(1)\n" \
  "v_smfmac_f32_16x16x64_bf16 v[64:67], v[28:31], a[24:31], v0\n" \
  "ds_read_b128 v[36:39], v2 offset:3072\n" \
  "v_smfmac_f32_16x16x64_bf16 v[68:71], v[28:31], a[32:39], v0\n" \
  "ds_read_b128 v[40:43], v2 offset:4096\n" \
  "v_smfmac_f32_16x16x64_bf16 v[72:75], v[28:31], a[40:47], v0\n" \
  "ds_read_b128 v[44:47], v2 offset:5120\n" \
  "s_waitcnt lgkmcnt(7)\n" \
  "v_smfmac_f32_16x16x64_bf16 v[84:87], v[28:31], v[88:95], v0\n" \
  "ds_read_b128 v[108:111], v3 offset:13312\n" \
  "s_waitcnt lgkmcnt(6)\n" \
  "v_smfmac_f32_16x16x64_bf16 v[20:23], v[28:31], v[96:103], v0\n" \
  "ds_read_b128 v[112:115], v3 offset:14336\n" \
  "s_waitcnt lgkmcnt(5)\n" \
  "v_smfmac_f32_16x16x64_bf16 v[64:67], v[32:35], a[64:71], v0\n" \
  "ds_read_b128 v[116:119], v3 offset:15360\n" \
  "v_smfmac_f32_16x16x64_bf16 v[68:71], v[32:35], a[72:79], v0\n" \
  "ds_read_b128 v[88:91], v3 offset:16384\n" \
  "v_smfmac_f32_16x16x64_bf16 v[72:75], v[32:35], a[80:87], v0\n" \
  "ds_read_b128 v[92:95], v3 offset:17408\n" \
  "s_waitcnt lgkmcnt(7)\n" \
  "v_smfmac_f32_16x16x64_bf16 v[64:67], v[36:39], a[104:111], v0\n" \
  "ds_read_b128 v[96:99], v3 offset:18432\n" \
  "s_waitcnt lgkmcnt(5)\n" \
  "v_smfmac_f32_16x16x64_bf16 v[68:71], v[36:39], v[104:111], v0\n" \
  "s_waitcnt vmcnt(0)\n" \
  "s_barrier\n" \
  "v_smfmac_f32_16x16x64_bf16 v[72:75], v[36:39], a[112:119], v0\n" \
  "ds_read_b128 v[48:51], v2 offset:6144\n" \
  "s_waitcnt lgkmcnt(4)\n" \
  "v_smfmac_f32_16x16x64_bf16 v[64:67], v[40:43], v[112:119], v0\n" \
  "ds_read_b128 v[52:55], v2 offset:7168\n" \
  "v_smfmac_f32_16x16x64_bf16 v[68:71], v[40:43], a[136:143], v0\n" \
  "ds_read_b128 v[56:59], v2 offset:8192\n" \
  "v_smfmac_f32_16x16x64_bf16 v[72:75], v[40:43], a[144:151], v0\n" \
  "ds_read_b128 v[60:63], v2 offset:9216\n" \
  "s_waitcnt lgkmcnt(5)\n" \
  "v_smfmac_f32_16x16x64_bf16 v[64:67], v[44:47], v[88:95], v0\n" \
  "ds_read_b128 v[100:103], v3 offset:19456\n" \
  "v_smfmac_f32_16x16x64_bf16 v[68:71], v[44:47], a[176:183], v0\n" \
  "ds_read_b128 v[104:107], v3 offset:20480\n" \
  "v_smfmac_f32_16x16x64_bf16 v[72:75], v[44:47], a[184:191], v0\n" \
  "ds_read_b128 v[108:111], v3 offset:21504\n" \
  "s_waitcnt lgkmcnt(6)\n" \
  "v_smfmac_f32_16x16x64_bf16 v[64:67], v[48:51], a[216:223], v0\n" \
  "ds_read_b128 v[112:115], v3 offset:26624\n" \
  "s_waitcnt lgkmcnt(6)\n" \
  "v_smfmac_f32_16x16x64_bf16 v[64:67], v[52:55], v[128:135], v0\n" \
  "ds_read_b128 v[116:119], v3 offset:27648\n" \
  "s_waitcnt lgkmcnt(6)\n" \
  "v_smfmac_f32_16x16x64_bf16 v[64:67], v[56:59], v[168:175], v0\n" \
  "ds_read_b128 v[88:91], v3 offset:28672\n" \
  "s_waitcnt lgkmcnt(5)\n" \
  "v_smfmac_f32_16x16x64_bf16 v[64:67], v[60:63], v[96:103], v0\n" \
  "ds_read_b128 v[92:95], v3 offset:29696\n" \
  "v_smfmac_f32_16x16x64_bf16 v[68:71], v[48:51], a[224:231], v0\n" \
  "ds_read_b128 v[96:99], v3 offset:4096\n" \
  "s_waitcnt lgkmcnt(5)\n" \
  "v_smfmac_f32_16x16x64_bf16 v[68:71], v[52:55], v[104:111], v0\n" \
  "ds_read_b128 v[100:103], v3 offset:5120\n" \
  "v_cvt_pk_bf16_f32 v64, v64, v65\n" \
  "v_smfmac_f32_16x16x64_bf16 v[68:71], v[56:59], v[176:183], v0\n" \
  "ds_read_b128 v[104:107], v3 offset:6144\n" \
  "v_cvt_pk_bf16_f32 v65, v66, v67\n" \
  "v_smfmac_f32_16x16x64_bf16 v[68:71], v[60:63], v[216:223], v0\n" \
  "ds_write_b64 v6, v[64:65] offset:35840\n" \
  "v_cmp_u_f32 vcc, v64, v65\n" \
  "v_smfmac_f32_16x16x64_bf16 v[72:75], v[48:51], a[232:239], v0\n" \
  "s_waitcnt lgkmcnt(0)\n" \
  "s_barrier\n" \
  "v_smfmac_f32_16x16x64_bf16 v[72:75], v[52:55], v[136:143], v0\n" \
  "ds_read_b128 v[108:111], v3 offset:7168\n" \
  "v_cvt_pk_bf16_f32 v68, v68, v69\n" \
  "v_smfmac_f32_16x16x64_bf16 v[72:75], v[56:59], v[184:191], v0\n" \
  "v_cvt_pk_bf16_f32 v69, v70, v71\n" \
  "v_smfmac_f32_16x16x64_bf16 v[72:75], v[60:63], v[224:231], v0\n" \
  "ds_write_b64 v6, v[68:69] offset:36352\n" \
  "v_smfmac_f32_16x16x64_bf16 v[76:79], v[28:31], a[48:55], v0\n" \
  "s_waitcnt lgkmcnt(0)\n" \
  "s_barrier\n" \
  "v_smfmac_f32_16x16x64_bf16 v[80:83], v[28:31], a[56:63], v0\n" \
  "v_cvt_pk_bf16_f32 v72, v72, v73\n" \
  "v_smfmac_f32_16x16x64_bf16 v[20:23], v[32:35], v[112:119], v0\n" \
  "v_cvt_pk_bf16_f32 v73, v74, v75\n" \
  "v_smfmac_f32_16x16x64_bf16 v[20:23], v[36:39], v[88:95], v0\n" \
  "ds_write_b64 v6, v[72:73] offset:38400\n" \
  "v_smfmac_f32_16x16x64_bf16 v[84:87], v[32:35], v[96:103], v0\n" \
  "s_waitcnt lgkmcnt(0)\n" \
  "s_barrier\n" \
  "v_smfmac_f32_16x16x64_bf16 v[84:87], v[36:39], v[104:111], v0\n" \
  "ds_write_b128 v2, v[20:23] offset:1024\n" \
  "v_smfmac_f32_16x16x64_bf16 v[76:79], v[32:35], a[88:95], v0\n" \
  "s_waitcnt lgkmcnt(0)\n" \
  "s_barrier\n" \
  "v_smfmac_f32_16x16x64_bf16 v[76:79], v[36:39], a[120:127], v0\n" \
  "v_smfmac_f32_16x16x64_bf16 v[76:79], v[40:43], a[152:159], v0\n" \
  "v_smfmac_f32_16x16x64_bf16 v[76:79], v[44:47], a[192:199], v0\n" \
  "v_smfmac_f32_16x16x64_bf16 v[76:79], v[48:51], a[240:247], v0\n" \
  "v_smfmac_f32_16x16x64_bf16 v[76:79], v[52:55], v[144:151], v0\n" \
  "v_smfmac_f32_16x16x64_bf16 v[76:79], v[56:59], v[192:199], v0\n" \
  "v_smfmac_f32_16x16x64_bf16 v[76:79], v[60:63], v[232:239], v0\n" \
  "v_smfmac_f32_16x16x64_bf16 v[80:83], v[32:35], a[96:103], v0\n" \
  "v_smfmac_f32_16x16x64_bf16 v[80:83], v[36:39], a[128:135], v0\n" \
  "v_cvt_pk_bf16_f32 v76, v76, v77\n" \
  "v_smfmac_f32_16x16x64_bf16 v[80:83], v[40:43], a[160:167], v0\n" \
  "v_cvt_pk_bf16_f32 v77, v78, v79\n" \
  "v_smfmac_f32_16x16x64_bf16 v[80:83], v[44:47], a[200:207], v0\n" \
  "ds_write_b64 v6, v[76:77] offset:3072\n" \
  "v_smfmac_f32_16x16x64_bf16 v[80:83], v[48:51], a[248:255], v0\n" \
  "s_waitcnt lgkmcnt(0)\n" \
  "s_barrier\n" \
  "v_smfmac_f32_16x16x64_bf16 v[80:83], v[52:55], v[152:159], v0\n" \
  "v_smfmac_f32_16x16x64_bf16 v[80:83], v[56:59], v[200:207], v0\n" \
  "v_smfmac_f32_16x16x64_bf16 v[80:83], v[60:63], v[240:247], v0\n" \
  "v_smfmac_f32_16x16x64_bf16 v[84:87], v[40:43], a[168:175], v0\n" \
  "v_smfmac_f32_16x16x64_bf16 v[84:87], v[44:47], a[208:215], v0\n" \
  "v_cvt_pk_bf16_f32 v80, v80, v81\n" \
  "v_smfmac_f32_16x16x64_bf16 v[84:87], v[48:51], v[120:127], v0\n" \
  "v_cvt_pk_bf16_f32 v81, v82, v83\n" \
  "v_smfmac_f32_16x16x64_bf16 v[84:87], v[52:55], v[160:167], v0\n" \
  "ds_write_b64 v6, v[80:81] offset:3584\n" \
  "v_smfmac_f32_16x16x64_bf16 v[84:87], v[56:59], v[208:215], v0\n" \
  "s_waitcnt lgkmcnt(0)\n" \
  "s_barrier\n" \
  "v_smfmac_f32_16x16x64_bf16 v[84:87], v[60:63], v[248:255], v0\n" \
  "s_nop 7\n" \
  "v_cvt_pk_bf16_f32 v84, v84, v85\n" \
  "v_cvt_pk_bf16_f32 v85, v86, v87\n" \
  "ds_write_b64 v6, v[84:85] offset:5632\n" \
  "s_waitcnt lgkmcnt(0)\n" \
  "s_branch LSRs3_%=\n" \
  "LDONE_%=:\n" \
  "s_waitcnt vmcnt(0) lgkmcnt(0)\n" \
  "s_mov_b32 s53, s73\n" \
  "s_mov_b32 s54, s51\n" \
  "s_mov_b32 s55, s50\n"
#define CROLE_CLOBBERS \
  "v32", "v33", "v34", "v35", "v36", "v37", "v38", "v39", "v40", "v41", "v42", "v43", "v44", "v45", "v46", "v47", "v48", "v49", "v50", "v51", "v52", \
  "v53", "v54", "v55", "v56", "v57", "v58", "v59", "v60", "v61", "v62", "v63", "v64", "v65", "v66", "v67", "v68", "v69", "v70", "v71", "v72", "v73", \
  "v74", "v75", "v76", "v77", "v78", "v79", "v80", "v81", "v82", "v83", "v84", "v85", "v86", "v87", "v88", "v89", "v90", "v91", "v92", "v93", "v94", \
  "v95", "v96", "v97", "v98", "v99", "v100", "v101", "v102", "v103", "v104", "v105", "v106", "v107", "v108", "v109", "v110", "v111", "v112", "v113", \
  "v114", "v115", "v116", "v117", "v118", "v119", "v120", "v121", "v122", "v123", "v124", "v125", "v126", "v127", "v128", "v129", "v130", "v131", \
  "v132", "v133", "v134", "v135", "v136", "v137", "v138", "v139", "v140", "v141", "v142", "v143", "v144", "v145", "v146", "v147", "v148", "v149", \
  "v150", "v151", "v152", "v153", "v154", "v155", "v156", "v157", "v158", "v159", "v160", "v161", "v162", "v163", "v164", "v165", "v166", "v167", \
  "v168", "v169", "v170", "v171", "v172", "v173", "v174", "v175", "v176", "v177", "v178", "v179", "v180", "v181", "v182", "v183", "v184", "v185", \
  "v186", "v187", "v188", "v189", "v190", "v191", "v192", "v193", "v194", "v195", "v196", "v197", "v198", "v199", "v200", "v201", "v202", "v203", \
  "v204", "v205", "v206", "v207", "v240", "v241", "v242", "v243", "v244", "v245", "v246", "v247", "v248", "v249", "v250", "v251", "v252", "v253", \
  "v254", "v255", "s34", "s35", "s48", "s49", "s50", "s51", "s68", "s69", "s70", "s71", "s72", "s73", "s74", "s75", "s76", "s77", "s78", "s79", "s80", \
  "s81", "s82", "s83", "s84", "s85", "s86", "s87", "s88", "s89", "s90", "s91", "s93", "s94", "s95", "s96", "a0", "a1", "a2", "a3", "a4", "a5", "a6", \
  "a7", "a8", "a9", "a10", "a11", "a12", "a13", "a14", "a15", "a16", "a17", "a18", "a19", "a20", "a21", "a22", "a23", "a24", "a25", "a26", "a27", "a28", \
  "a29", "a30", "a31", "a32", "a33", "a34", "a35", "a36", "a37", "a38", "a39", "a40", "a41", "a42", "a43", "a44", "a45", "a46", "a47", "a48", "a49", \
  "a50", "a51", "a52", "a53", "a54", "a55", "a56", "a57", "a58", "a59", "a60", "a61", "a62", "a63", "a64", "a65", "a66", "a67", "a68", "a69", "a70", \
  "a71", "a72", "a73", "a74", "a75", "a76", "a77", "a78", "a79", "a80", "a81", "a82", "a83", "a84", "a85", "a86", "a87", "a88", "a89", "a90", "a91", \
  "a92", "a93", "a94", "a95", "a96", "a97", "a98", "a99", "a100", "a101", "a102", "a103", "a104", "a105", "a106", "a107", "a108", "a109", "a110", \
  "a111", "a112", "a113", "a114", "a115", "a116", "a117", "a118", "a119", "a120", "a121", "a122", "a123", "a124", "a125", "a126", "a127", "a128", \
  "a129", "a130", "a131", "a132", "a133", "a134", "a135", "a136", "a137", "a138", "a139", "a140", "a141", "a142", "a143", "a144", "a145", "a146", \
  "a147", "a148", "a149", "a150", "a151", "a152", "a153", "a154", "a155", "a156", "a157", "a158", "a159", "a160", "a161", "a162", "a163", "a164", \
  "a165", "a166", "a167", "a168", "a169", "a170", "a171", "a172", "a173", "a174", "a175", "a176", "a177", "a178", "a179", "a180", "a181", "a182", \
  "a183", "a184", "a185", "a186", "a187", "a188", "a189", "a190", "a191", "a192", "a193", "a194", "a195", "a196", "a197", "a198", "a199", "a200", \
  "a201", "a202", "a203", "a204", "a205", "a206", "a207", "a208", "a209", "a210", "a211", "a212", "a213", "a214", "a215", "a216", "a217", "a218", \
  "a219", "a220", "a221", "a222", "a223", "a224", "a225", "a226", "a227", "a228", "a229", "a230", "a231", "a232", "a233", "a234", "a235", "a236", \
  "a237", "a238", "a239", "a240", "a241", "a242", "a243", "a244", "a245", "a246", "a247", "a248", "a249", "a250", "a251", "a252", "a253", "a254", "a255"
#define CROLE_ASM \
  "s_cmp_lt_i32 s61, 0\n" \
  "s_cbranch_scc1 LXIT_%=\n" \
  "v_mov_b32 v0, v208\n" \
  "v_mov_b32 v1, v209\n" \
  "v_mov_b32 v2, v210\n" \
  "v_mov_b32 v3, v211\n" \
  "v_mov_b32 v4, v212\n" \
  "v_mov_b32 v5, v213\n" \
  "v_mov_b32 v6, v214\n" \
  "v_mov_b32 v7, v215\n" \
  "v_mov_b32 v8, v216\n" \
  "v_mov_b32 v9, v217\n" \
  "v_mov_b32 v10, v218\n" \
  "v_mov_b32 v11, v219\n" \
  "v_mov_b32 v12, v220\n" \
  "v_mov_b32 v13, v221\n" \
  "v_mov_b32 v14, v222\n" \
  "v_mov_b32 v15, v223\n" \
  "v_mov_b32 v16, v224\n" \
  "v_mov_b32 v17, v225\n" \
  "v_mov_b32 v18, v226\n" \
  "v_mov_b32 v19, v227\n" \
  "v_mov_b32 v20, v228\n" \
  "v_mov_b32 v21, v229\n" \
  "v_mov_b32 v22, v230\n" \
  "v_mov_b32 v23, v231\n" \
  "v_mov_b32 v24, v232\n" \
  "v_mov_b32 v25, v233\n" \
  "v_mov_b32 v26, v234\n" \
  "v_mov_b32 v27, v235\n" \
  "v_mov_b32 v28, v236\n" \
  "v_mov_b32 v29, v237\n" \
  "v_mov_b32 v30, v238\n" \
  "v_mov_b32 v31, v239\n" \
  "s_mov_b32 s34, 0x00ff00ff\n" \
  "s_mov_b32 s35, 0x00ff00ff\n" \
  "s_mov_b32 s40, 0x00ff00ff\n" \
  "s_mov_b32 s41, 0xff00ff00\n" \
  "s_mov_b32 s68, 0\n" \
  "s_mov_b32 s73, 0\n" \
  "s_mov_b32 s58, 0\n" \
  "s_mov_b32 s59, 0\n" \
  "s_mov_b32 s51, 0\n" \
  "s_mov_b32 s71, 0\n" \
  "s_mov_b32 s44, 0\n" \
  "s_mov_b32 s50, 5900\n" \
  "s_mov_b32 s42, 0x40000000\n" \
  "s_mov_b32 s93, s60\n" \
  "s_mov_b32 s60, 0\n" \
  "s_mov_b32 s61, 0\n" \
  "s_mov_b32 s62, 0\n" \
  "s_mov_b32 s63, 0\n" \
  "s_mov_b32 s64, 0\n" \
  "s_mov_b32 s65, 0\n" \
  "s_mov_b32 s66, 0\n" \
  "s_mov_b32 s67, 0\n" \
  "s_cmp_eq_u32 s52, 3\n" \
  "s_cbranch_scc1 LSTARTc3_%=\n" \
  "LSTARTc0_%=:\n" \
  "LQU0c0_%=:\n" \
  "buffer_load_dword v92, v14, s[36:39], 0 offen offset:0 sc1\n" \
  "s_waitcnt vmcnt(0)\n" \
  "v_cmp_ne_u32 vcc, 1, v92\n" \
  "s_nop 3\n" \
  "s_cbranch_vccz LRU0c0_%=\n" \
  "s_add_u32 s73, s73, 1\n" \
  "s_cmp_gt_u32 s73, 20000\n" \
  "s_cbranch_scc1 LRU0c0_%=\n" \
  "s_sleep 1\n" \
  "s_branch LQU0c0_%=\n" \
  "LRU0c0_%=:\n" \
  "v_readlane_b32 s46, v16, 6\n" \
  "s_nop 4\n" \
  "buffer_load_dwordx4 a[240:243], v18, s[36:39], s46 offen sc1\n" \
  "buffer_load_dwordx4 a[248:251], v19, s[36:39], s46 offen sc1\n" \
  "buffer_load_dwordx4 v[112:115], v20, s[36:39], s46 offen sc1\n" \
  "buffer_load_dwordx4 v[120:123], v21, s[36:39], s46 offen sc1\n" \
  "buffer_load_dwordx4 v[128:131], v22, s[36:39], s46 offen sc1\n" \
  "s_barrier\n" \
  "v_readlane_b32 s46, v16, 7\n" \
  "s_nop 4\n" \
  "buffer_load_dwordx4 a[244:247], v18, s[36:39], s46 offen sc1\n" \
  "buffer_load_dwordx4 a[252:255], v19, s[36:39], s46 offen sc1\n" \
  "buffer_load_dwordx4 v[116:119], v20, s[36:39], s46 offen sc1\n" \
  "buffer_load_dwordx4 v[124:127], v21, s[36:39], s46 offen sc1\n" \
  "buffer_load_dwordx4 v[132:135], v22, s[36:39], s46 offen sc1\n" \
  "s_barrier\n" \
  "v_readlane_b32 s46, v16, 16\n" \
  "s_nop 4\n" \
  "buffer_load_dwordx4 v[136:139], v18, s[36:39], s46 offen sc1\n" \
  "buffer_load_dwordx4 v[144:147], v19, s[36:39], s46 offen sc1\n" \
  "buffer_load_dwordx4 v[152:155], v20, s[36:39], s46 offen sc1\n" \
  "buffer_load_dwordx4 v[160:163], v21, s[36:39], s46 offen sc1\n" \
  "buffer_load_dwordx4 v[168:171], v22, s[36:39], s46 offen sc1\n" \
  "v_readlane_b32 s46, v16, 17\n" \
  "s_nop 4\n" \
  "buffer_load_dwordx4 v[140:143], v18, s[36:39], s46 offen sc1\n" \
  "buffer_load_dwordx4 v[148:151], v19, s[36:39], s46 offen sc1\n" \
  "buffer_load_dwordx4 v[156:159], v20, s[36:39], s46 offen sc1\n" \
  "buffer_load_dwordx4 v[164:167], v21, s[36:39], s46 offen sc1\n" \
  "buffer_load_dwordx4 v[172:175], v22, s[36:39], s46 offen sc1\n" \
  "s_barrier\n" \
  "LQU4c0_%=:\n" \
  "buffer_load_dword v92, v14, s[36:39], 0 offen offset:1536 sc1\n" \
  "s_waitcnt vmcnt(0)\n" \
  "v_cmp_ne_u32 vcc, 1, v92\n" \
  "s_nop 3\n" \
  "s_cbranch_vccz LRU4c0_%=\n" \
  "s_add_u32 s73, s73, 1\n" \
  "s_cmp_gt_u32 s73, 20000\n" \
  "s_cbranch_scc1 LRU4c0_%=\n" \
  "s_sleep 1\n" \
  "s_branch LQU4c0_%=\n" \
  "LRU4c0_%=:\n" \
  "v_readlane_b32 s46, v16, 8\n" \
  "s_nop 4\n" \
  "buffer_load_dwordx4 v[176:179], v18, s[36:39], s46 offen sc1\n" \
  "buffer_load_dwordx4 v[184:187], v19, s[36:39], s46 offen sc1\n" \
  "buffer_load_dwordx4 v[192:195], v20, s[36:39], s46 offen sc1\n" \
  "buffer_load_dwordx4 v[200:203], v21, s[36:39], s46 offen sc1\n" \
  "buffer_load_dwordx4 v[208:211], v22, s[36:39], s46 offen sc1\n" \
  "buffer_atomic_add v15, v7, s[36:39], 0 offen sc1\n" \
  "v_readlane_b32 s46, v16, 9\n" \
  "s_nop 4\n" \
  "buffer_load_dwordx4 v[180:183], v18, s[36:39], s46 offen sc1\n" \
  "buffer_load_dwordx4 v[188:191], v19, s[36:39], s46 offen sc1\n" \
  "buffer_load_dwordx4 v[196:199], v20, s[36:39], s46 offen sc1\n" \
  "buffer_load_dwordx4 v[204:207], v21, s[36:39], s46 offen sc1\n" \
  "buffer_load_dwordx4 v[212:215], v22, s[36:39], s46 offen sc1\n" \
  "s_barrier\n" \
  "v_readlane_b32 s46, v16, 18\n" \
  "s_nop 4\n" \
  "buffer_load_dwordx4 v[216:219], v18, s[36:39], s46 offen sc1\n" \
  "buffer_load_dwordx4 v[224:227], v19, s[36:39], s46 offen sc1\n" \
  "buffer_load_dwordx4 v[232:235], v20, s[36:39], s46 offen sc1\n" \
  "buffer_load_dwordx4 v[240:243], v21, s[36:39], s46 offen sc1\n" \
  "buffer_load_dwordx4 v[248:251], v22, s[36:39], s46 offen sc1\n" \
  "v_readlane_b32 s46, v16, 19\n" \
  "s_nop 4\n" \
  "buffer_load_dwordx4 v[220:223], v18, s[36:39], s46 offen sc1\n" \
  "buffer_load_dwordx4 v[228:231], v19, s[36:39], s46 offen sc1\n" \
  "buffer_load_dwordx4 v[236:239], v20, s[36:39], s46 offen sc1\n" \
  "buffer_load_dwordx4 v[244:247], v21, s[36:39], s46 offen sc1\n" \
  "buffer_load_dwordx4 v[252:255], v22, s[36:39], s46 offen sc1\n" \
  "s_cmp_eq_u32 s52, 1\n" \
  "s_cbranch_scc1 LSTARTc1_%=\n" \
  "s_cmp_eq_u32 s52, 2\n" \
  "s_cbranch_scc1 LSTARTc2_%=\n" \
  "s_mov_b32 s57, 0\n" \
  "v_readlane_b32 s44, v4, 0\n" \
  "s_nop 3\n" \
  "LOOPc0_%=:\n" \
  "s_mov_b32 m0, 0\n" \
  "s_add_u32 s75, s44, 0\n" \
  "s_barrier\n" \
  "buffer_load_dwordx4 v1, s[36:39], s75 offen offset:0 sc1 lds\n" \
  "buffer_load_dwordx4 v1, s[36:39], s75 offen offset:1024 sc1 lds\n" \
  "buffer_load_dwordx4 v1, s[36:39], s75 offen offset:2048 sc1 lds\n" \
  "buffer_load_dwordx4 v1, s[36:39], s75 offen offset:3072 sc1 lds\n" \
  "v_mov_b32 v56, 0\n" \
  "v_mov_b32 v57, 0\n" \
  "v_mov_b32 v58, 0\n" \
  "v_mov_b32 v59, 0\n" \
  "v_mov_b32 v60, 0\n" \
  "v_mov_b32 v61, 0\n" \
  "v_mov_b32 v62, 0\n" \
  "v_mov_b32 v63, 0\n" \
  "v_mov_b32 v64, 0\n" \
  "v_mov_b32 v65, 0\n" \
  "v_mov_b32 v66, 0\n" \
  "v_mov_b32 v67, 0\n" \
  "v_mov_b32 v68, 0\n" \
  "v_mov_b32 v69, 0\n" \
  "v_mov_b32 v70, 0\n" \
  "v_mov_b32 v71, 0\n" \
  "v_mov_b32 v72, 0\n" \
  "v_mov_b32 v73, 0\n" \
  "v_mov_b32 v74, 0\n" \
  "v_mov_b32 v75, 0\n" \
  "s_add_u32 s68, s68, 1\n" \
  "s_min_u32 s74, s68, 63\n" \
  "v_readlane_b32 s45, v4, s74\n" \
  "s_waitcnt vmcnt(0)\n" \
  "s_barrier\n" \
  "buffer_store_dwordx4 v[76:79], v10, s[36:39], s42 offen offset:0 sc1\n" \
  "buffer_store_dwordx4 v[80:83], v10, s[36:39], s42 offen offset:1024 sc1\n" \
  "buffer_store_dwordx4 v[84:87], v10, s[36:39], s42 offen offset:2048 sc1\n" \
  "buffer_store_dwordx4 v[88:91], v10, s[36:39], s42 offen offset:3072 sc1\n" \
  "LMc0_%=:\n" \
  "ds_read_b128 v[16:19], v1 offset:0\n" \
  "ds_read_b128 v[20:23], v1 offset:1024\n" \
  "ds_read_b128 v[24:27], v1 offset:2048\n" \
  "ds_read_b128 v[28:31], v1 offset:3072\n" \
  "ds_read_b128 v[32:35], v1 offset:4096\n" \
  "ds_read_b128 v[36:39], v1 offset:5120\n" \
  "ds_read_b128 v[40:43], v1 offset:6144\n" \
  "ds_read_b128 v[44:47], v1 offset:7168\n" \
  "ds_read_b128 v[48:51], v1 offset:8192\n" \
  "ds_read_b128 v[52:55], v1 offset:9216\n" \
  "s_waitcnt lgkmcnt(9)\n" \
  "v_smfmac_f32_16x16x64_bf16 v[56:59], v[16:19], a[0:7], v0\n" \
  "s_waitcnt lgkmcnt(8)\n" \
  "v_smfmac_f32_16x16x64_bf16 v[56:59], v[20:23], a[80:87], v0\n" \
  "s_waitcnt lgkmcnt(7)\n" \
  "v_smfmac_f32_16x16x64_bf16 v[56:59], v[24:27], a[160:167], v0\n" \
  "s_waitcnt lgkmcnt(6)\n" \
  "v_smfmac_f32_16x16x64_bf16 v[56:59], v[28:31], a[240:247], v0\n" \
  "s_waitcnt lgkmcnt(5)\n" \
  "v_smfmac_f32_16x16x64_bf16 v[56:59], v[32:35], v[176:183], v0\n" \
  "s_waitcnt lgkmcnt(4)\n" \
  "v_smfmac_f32_16x16x64_bf16 v[56:59], v[36:39], a[40:47], v0\n" \
  "s_waitcnt lgkmcnt(3)\n" \
  "v_smfmac_f32_16x16x64_bf16 v[56:59], v[40:43], a[120:127], v0\n" \
  "s_waitcnt lgkmcnt(2)\n" \
  "v_smfmac_f32_16x16x64_bf16 v[56:59], v[44:47], a[200:207], v0\n" \
  "s_waitcnt lgkmcnt(1)\n" \
  "v_smfmac_f32_16x16x64_bf16 v[56:59], v[48:51], v[136:143], v0\n" \
  "s_waitcnt lgkmcnt(0)\n" \
  "v_smfmac_f32_16x16x64_bf16 v[56:59], v[52:55], v[216:223], v0\n" \
  "v_smfmac_f32_16x16x64_bf16 v[60:63], v[16:19], a[8:15], v0\n" \
  "v_smfmac_f32_16x16x64_bf16 v[64:67], v[16:19], a[16:23], v0\n" \
  "v_cmp_u_f32 vcc, v56, v57\n" \
  "v_cmp_u_f32 s[82:83], v58, v59\n" \
  "s_nop 1\n" \
  "s_or_b64 vcc, vcc, s[82:83]\n" \
  "s_cbranch_scc1 LMFc0_%=\n" \
  "LMOKc0_%=:\n" \
  "v_smfmac_f32_16x16x64_bf16 v[68:71], v[16:19], a[24:31], v0\n" \
  "v_smfmac_f32_16x16x64_bf16 v[72:75], v[16:19], a[32:39], v0\n" \
  "v_smfmac_f32_16x16x64_bf16 v[60:63], v[20:23], a[88:95], v0\n" \
  "v_smfmac_f32_16x16x64_bf16 v[64:67], v[20:23], a[96:103], v0\n" \
  "v_smfmac_f32_16x16x64_bf16 v[68:71], v[20:23], a[104:111], v0\n" \
  "v_smfmac_f32_16x16x64_bf16 v[72:75], v[20:23], a[112:119], v0\n" \
  "v_smfmac_f32_16x16x64_bf16 v[60:63], v[24:27], a[168:175], v0\n" \
  "v_smfmac_f32_16x16x64_bf16 v[64:67], v[24:27], a[176:183], v0\n" \
  "v_smfmac_f32_16x16x64_bf16 v[68:71], v[24:27], a[184:191], v0\n" \
  "v_smfmac_f32_16x16x64_bf16 v[72:75], v[24:27], a[192:199], v0\n" \
  "v_smfmac_f32_16x16x64_bf16 v[60:63], v[28:31], a[248:255], v0\n" \
  "v_smfmac_f32_16x16x64_bf16 v[64:67], v[28:31], v[112:119], v0\n" \
  "v_smfmac_f32_16x16x64_bf16 v[68:71], v[28:31], v[120:127], v0\n" \
  "v_smfmac_f32_16x16x64_bf16 v[72:75], v[28:31], v[128:135], v0\n" \
  "v_smfmac_f32_16x16x64_bf16 v[60:63], v[32:35], v[184:191], v0\n" \
  "v_smfmac_f32_16x16x64_bf16 v[64:67], v[32:35], v[192:199], v0\n" \
  "v_smfmac_f32_16x16x64_bf16 v[68:71], v[32:35], v[200:207], v0\n" \
  "v_smfmac_f32_16x16x64_bf16 v[72:75], v[32:35], v[208:215], v0\n" \
  "v_smfmac_f32_16x16x64_bf16 v[60:63], v[36:39], a[48:55], v0\n" \
  "v_smfmac_f32_16x16x64_bf16 v[64:67], v[36:39], a[56:63], v0\n" \
  "v_smfmac_f32_16x16x64_bf16 v[68:71], v[36:39], a[64:71], v0\n" \
  "v_smfmac_f32_16x16x64_bf16 v[72:75], v[36:39], a[72:79], v0\n" \
  "v_smfmac_f32_16x16x64_bf16 v[60:63], v[40:43], a[128:135], v0\n" \
  "v_smfmac_f32_16x16x64_bf16 v[64:67], v[40:43], a[136:143], v0\n" \
  "v_smfmac_f32_16x16x64_bf16 v[68:71], v[40:43], a[144:151], v0\n" \
  "v_smfmac_f32_16x16x64_bf16 v[72:75], v[40:43], a[152:159], v0\n" \
  "v_smfmac_f32_16x16x64_bf16 v[60:63], v[44:47], a[208:215], v0\n" \
  "v_smfmac_f32_16x16x64_bf16 v[64:67], v[44:47], a[216:223], v0\n" \
  "v_smfmac_f32_16x16x64_bf16 v[68:71], v[44:47], a[224:231], v0\n" \
  "v_smfmac_f32_16x16x64_bf16 v[72:75], v[44:47], a[232:239], v0\n" \
  "v_smfmac_f32_16x16x64_bf16 v[60:63], v[48:51], v[144:151], v0\n" \
  "v_smfmac_f32_16x16x64_bf16 v[64:67], v[48:51], v[152:159], v0\n" \
  "v_smfmac_f32_16x16x64_bf16 v[68:71], v[48:51], v[160:167], v0\n" \
  "v_smfmac_f32_16x16x64_bf16 v[72:75], v[48:51], v[168:175], v0\n" \
  "v_smfmac_f32_16x16x64_bf16 v[60:63], v[52:55], v[224:231], v0\n" \
  "v_smfmac_f32_16x16x64_bf16 v[64:67], v[52:55], v[232:239], v0\n" \
  "v_smfmac_f32_16x16x64_bf16 v[68:71], v[52:55], v[240:247], v0\n" \
  "v_smfmac_f32_16x16x64_bf16 v[72:75], v[52:55], v[248:255], v0\n" \
  "v_permlane32_swap_b32 v56, v60\n" \
  "v_permlane32_swap_b32 v57, v61\n" \
  "v_permlane32_swap_b32 v58, v62\n" \
  "v_permlane32_swap_b32 v59, v63\n" \
  "v_pk_add_f32 v[56:57], v[56:57], v[60:61]\n" \
  "v_pk_add_f32 v[58:59], v[58:59], v[62:63]\n" \
  "v_cvt_pk_bf16_f32 v56, v56, v57\n" \
  "v_cvt_pk_bf16_f32 v57, v58, v59\n" \
  "ds_write_b64 v2, v[56:57] offset:0\n" \
  "v_permlane32_swap_b32 v64, v68\n" \
  "v_permlane32_swap_b32 v65, v69\n" \
  "v_permlane32_swap_b32 v66, v70\n" \
  "v_permlane32_swap_b32 v67, v71\n" \
  "v_pk_add_f32 v[64:65], v[64:65], v[68:69]\n" \
  "v_pk_add_f32 v[66:67], v[66:67], v[70:71]\n" \
  "v_cvt_pk_bf16_f32 v64, v64, v65\n" \
  "v_cvt_pk_bf16_f32 v65, v66, v67\n" \
  "ds_write_b64 v2, v[64:65] offset:512\n" \
  "v_permlane32_swap_b32 v72, v74\n" \
  "v_permlane32_swap_b32 v73, v75\n" \
  "v_pk_add_f32 v[72:73], v[72:73], v[74:75]\n" \
  "v_cvt_pk_bf16_f32 v72, v72, v73\n" \
  "ds_write_b32 v3, v72 offset:1024\n" \
  "s_waitcnt lgkmcnt(0)\n" \
  "s_waitcnt vmcnt(0)\n" \
  "s_barrier\n" \
  "ds_read_b128 v[76:79], v1 offset:10240\n" \
  "ds_read_b128 v[80:83], v1 offset:11264\n" \
  "ds_read_b128 v[84:87], v1 offset:12288\n" \
  "ds_read_b128 v[88:91], v1 offset:13312\n" \
  "s_sub_u32 s74, s68, 1\n" \
  "v_readlane_b32 s42, v9, s74\n" \
  "s_waitcnt lgkmcnt(0)\n" \
  "s_mov_b32 s44, s45\n" \
  "s_barrier\n" \
  "s_cmp_eq_u32 s68, 64\n" \
  "s_cbranch_scc0 LOOPc0_%=\n" \
  "buffer_store_dwordx4 v[76:79], v10, s[36:39], s42 offen offset:0 sc1\n" \
  "buffer_store_dwordx4 v[80:83], v10, s[36:39], s42 offen offset:1024 sc1\n" \
  "buffer_store_dwordx4 v[84:87], v10, s[36:39], s42 offen offset:2048 sc1\n" \
  "buffer_store_dwordx4 v[88:91], v10, s[36:39], s42 offen offset:3072 sc1\n" \
  "s_waitcnt vmcnt(0)\n" \
  "buffer_atomic_add v15, v11, s[36:39], 0 offen sc1\n" \
  "s_branch LDONE_%=\n" \
  "LMFc0_%=:\n" \
  "v_max_u32 v92, v16, v17\n" \
  "v_max3_u32 v92, v92, v18, v19\n" \
  "v_max3_u32 v92, v92, v20, v21\n" \
  "v_max3_u32 v92, v92, v22, v23\n" \
  "v_max3_u32 v92, v92, v24, v25\n" \
  "v_max3_u32 v92, v92, v26, v27\n" \
  "v_max3_u32 v92, v92, v28, v29\n" \
  "v_max3_u32 v92, v92, v30, v31\n" \
  "v_max3_u32 v92, v92, v32, v33\n" \
  "v_max3_u32 v92, v92, v34, v35\n" \
  "v_max3_u32 v92, v92, v36, v37\n" \
  "v_max3_u32 v92, v92, v38, v39\n" \
  "v_max3_u32 v92, v92, v40, v41\n" \
  "v_max3_u32 v92, v92, v42, v43\n" \
  "v_max3_u32 v92, v92, v44, v45\n" \
  "v_max3_u32 v92, v92, v46, v47\n" \
  "v_max3_u32 v92, v92, v48, v49\n" \
  "v_max3_u32 v92, v92, v50, v51\n" \
  "v_max3_u32 v92, v92, v52, v53\n" \
  "v_max3_u32 v92, v92, v54, v55\n" \
  "v_cmp_eq_u32 vcc, -1, v92\n" \
  "s_nop 3\n" \
  "s_cbranch_vccz LMOKc0_%=\n" \
  "s_cmp_gt_u32 s57, 20000\n" \
  "s_cbranch_scc1 LMOKc0_%=\n" \
  "s_add_u32 s57, s57, 1\n" \
  "s_barrier\n" \
  "buffer_load_dwordx4 v1, s[36:39], s75 offen offset:0 sc1 lds\n" \
  "buffer_load_dwordx4 v1, s[36:39], s75 offen offset:1024 sc1 lds\n" \
  "buffer_load_dwordx4 v1, s[36:39], s75 offen offset:2048 sc1 lds\n" \
  "buffer_load_dwordx4 v1, s[36:39], s75 offen offset:3072 sc1 lds\n" \
  "v_mov_b32 v56, 0\n" \
  "v_mov_b32 v57, 0\n" \
  "v_mov_b32 v58, 0\n" \
  "v_mov_b32 v59, 0\n" \
  "v_mov_b32 v60, 0\n" \
  "v_mov_b32 v61, 0\n" \
  "v_mov_b32 v62, 0\n" \
  "v_mov_b32 v63, 0\n" \
  "v_mov_b32 v64, 0\n" \
  "v_mov_b32 v65, 0\n" \
  "v_mov_b32 v66, 0\n" \
  "v_mov_b32 v67, 0\n" \
  "v_mov_b32 v68, 0\n" \
  "v_mov_b32 v69, 0\n" \
  "v_mov_b32 v70, 0\n" \
  "v_mov_b32 v71, 0\n" \
  "v_mov_b32 v72, 0\n" \
  "v_mov_b32 v73, 0\n" \
  "v_mov_b32 v74, 0\n" \
  "v_mov_b32 v75, 0\n" \
  "s_waitcnt vmcnt(0)\n" \
  "s_barrier\n" \
  "s_branch LMc0_%=\n" \
  "LSTARTc1_%=:\n" \
  "s_mov_b32 s57, 0\n" \
  "v_readlane_b32 s44, v4, 0\n" \
  "s_nop 3\n" \
  "LOOPc1_%=:\n" \
  "s_mov_b32 m0, 4096\n" \
  "s_add_u32 s75, s44, 4096\n" \
  "s_barrier\n" \
  "buffer_load_dwordx4 v1, s[36:39], s75 offen offset:0 sc1 lds\n" \
  "buffer_load_dwordx4 v1, s[36:39], s75 offen offset:1024 sc1 lds\n" \
  "buffer_load_dwordx4 v1, s[36:39], s75 offen offset:2048 sc1 lds\n" \
  "buffer_load_dwordx4 v1, s[36:39], s75 offen offset:3072 sc1 lds\n" \
  "v_mov_b32 v56, 0\n" \
  "v_mov_b32 v57, 0\n" \
  "v_mov_b32 v58, 0\n" \
  "v_mov_b32 v59, 0\n" \
  "v_mov_b32 v60, 0\n" \
  "v_mov_b32 v61, 0\n" \
  "v_mov_b32 v62, 0\n" \
  "v_mov_b32 v63, 0\n" \
  "v_mov_b32 v64, 0\n" \
  "v_mov_b32 v65, 0\n" \
  "v_mov_b32 v66, 0\n" \
  "v_mov_b32 v67, 0\n" \
  "v_mov_b32 v68, 0\n" \
  "v_mov_b32 v69, 0\n" \
  "v_mov_b32 v70, 0\n" \
  "v_mov_b32 v71, 0\n" \
  "v_mov_b32 v72, 0\n" \
  "v_mov_b32 v73, 0\n" \
  "v_mov_b32 v74, 0\n" \
  "v_mov_b32 v75, 0\n" \
  "s_add_u32 s68, s68, 1\n" \
  "s_min_u32 s74, s68, 63\n" \
  "v_readlane_b32 s45, v4, s74\n" \
  "s_waitcnt vmcnt(0)\n" \
  "s_barrier\n" \
  "LMc1_%=:\n" \
  "ds_read_b128 v[16:19], v1 offset:0\n" \
  "ds_read_b128 v[20:23], v1 offset:1024\n" \
  "ds_read_b128 v[24:27], v1 offset:2048\n" \
  "ds_read_b128 v[28:31], v1 offset:3072\n" \
  "ds_read_b128 v[32:35], v1 offset:4096\n" \
  "ds_read_b128 v[36:39], v1 offset:5120\n" \
  "ds_read_b128 v[40:43], v1 offset:6144\n" \
  "ds_read_b128 v[44:47], v1 offset:7168\n" \
  "ds_read_b128 v[48:51], v1 offset:8192\n" \
  "ds_read_b128 v[52:55], v1 offset:9216\n" \
  "s_waitcnt lgkmcnt(9)\n" \
  "v_smfmac_f32_16x16x64_bf16 v[56:59], v[16:19], a[0:7], v0\n" \
  "s_waitcnt lgkmcnt(8)\n" \
  "v_smfmac_f32_16x16x64_bf16 v[56:59], v[20:23], a[80:87], v0\n" \
  "s_waitcnt lgkmcnt(7)\n" \
  "v_smfmac_f32_16x16x64_bf16 v[56:59], v[24:27], a[160:167], v0\n" \
  "s_waitcnt lgkmcnt(6)\n" \
  "v_smfmac_f32_16x16x64_bf16 v[56:59], v[28:31], a[240:247], v0\n" \
  "s_waitcnt lgkmcnt(5)\n" \
  "v_smfmac_f32_16x16x64_bf16 v[56:59], v[32:35], v[176:183], v0\n" \
  "s_waitcnt lgkmcnt(4)\n" \
  "v_smfmac_f32_16x16x64_bf16 v[56:59], v[36:39], a[40:47], v0\n" \
  "s_waitcnt lgkmcnt(3)\n" \
  "v_smfmac_f32_16x16x64_bf16 v[56:59], v[40:43], a[120:127], v0\n" \
  "s_waitcnt lgkmcnt(2)\n" \
  "v_smfmac_f32_16x16x64_bf16 v[56:59], v[44:47], a[200:207], v0\n" \
  "s_waitcnt lgkmcnt(1)\n" \
  "v_smfmac_f32_16x16x64_bf16 v[56:59], v[48:51], v[136:143], v0\n" \
  "s_waitcnt lgkmcnt(0)\n" \
  "v_smfmac_f32_16x16x64_bf16 v[56:59], v[52:55], v[216:223], v0\n" \
  "v_smfmac_f32_16x16x64_bf16 v[60:63], v[16:19], a[8:15], v0\n" \
  "v_smfmac_f32_16x16x64_bf16 v[64:67], v[16:19], a[16:23], v0\n" \
  "v_cmp_u_f32 vcc, v56, v57\n" \
  "v_cmp_u_f32 s[82:83], v58, v59\n" \
  "s_nop 1\n" \
  "s_or_b64 vcc, vcc, s[82:83]\n" \
  "s_cbranch_scc1 LMFc1_%=\n" \
  "LMOKc1_%=:\n" \
  "v_smfmac_f32_16x16x64_bf16 v[68:71], v[16:19], a[24:31], v0\n" \
  "v_smfmac_f32_16x16x64_bf16 v[72:75], v[16:19], a[32:39], v0\n" \
  "v_smfmac_f32_16x16x64_bf16 v[60:63], v[20:23], a[88:95], v0\n" \
  "v_smfmac_f32_16x16x64_bf16 v[64:67], v[20:23], a[96:103], v0\n" \
  "v_smfmac_f32_16x16x64_bf16 v[68:71], v[20:23], a[104:111], v0\n" \
  "v_smfmac_f32_16x16x64_bf16 v[72:75], v[20:23], a[112:119], v0\n" \
  "v_smfmac_f32_16x16x64_bf16 v[60:63], v[24:27], a[168:175], v0\n" \
  "v_smfmac_f32_16x16x64_bf16 v[64:67], v[24:27], a[176:183], v0\n" \
  "v_smfmac_f32_16x16x64_bf16 v[68:71], v[24:27], a[184:191], v0\n" \
  "v_smfmac_f32_16x16x64_bf16 v[72:75], v[24:27], a[192:199], v0\n" \
  "v_smfmac_f32_16x16x64_bf16 v[60:63], v[28:31], a[248:255], v0\n" \
  "v_smfmac_f32_16x16x64_bf16 v[64:67], v[28:31], v[112:119], v0\n" \
  "v_smfmac_f32_16x16x64_bf16 v[68:71], v[28:31], v[120:127], v0\n" \
  "v_smfmac_f32_16x16x64_bf16 v[72:75], v[28:31], v[128:135], v0\n" \
  "v_smfmac_f32_16x16x64_bf16 v[60:63], v[32:35], v[184:191], v0\n" \
  "v_smfmac_f32_16x16x64_bf16 v[64:67], v[32:35], v[192:199], v0\n" \
  "v_smfmac_f32_16x16x64_bf16 v[68:71], v[32:35], v[200:207], v0\n" \
  "v_smfmac_f32_16x16x64_bf16 v[72:75], v[32:35], v[208:215], v0\n" \
  "v_smfmac_f32_16x16x64_bf16 v[60:63], v[36:39], a[48:55], v0\n" \
  "v_smfmac_f32_16x16x64_bf16 v[64:67], v[36:39], a[56:63], v0\n" \
  "v_smfmac_f32_16x16x64_bf16 v[68:71], v[36:39], a[64:71], v0\n" \
  "v_smfmac_f32_16x16x64_bf16 v[72:75], v[36:39], a[72:79], v0\n" \
  "v_smfmac_f32_16x16x64_bf16 v[60:63], v[40:43], a[128:135], v0\n" \
  "v_smfmac_f32_16x16x64_bf16 v[64:67], v[40:43], a[136:143], v0\n" \
  "v_smfmac_f32_16x16x64_bf16 v[68:71], v[40:43], a[144:151], v0\n" \
  "v_smfmac_f32_16x16x64_bf16 v[72:75], v[40:43], a[152:159], v0\n" \
  "v_smfmac_f32_16x16x64_bf16 v[60:63], v[44:47], a[208:215], v0\n" \
  "v_smfmac_f32_16x16x64_bf16 v[64:67], v[44:47], a[216:223], v0\n" \
  "v_smfmac_f32_16x16x64_bf16 v[68:71], v[44:47], a[224:231], v0\n" \
  "v_smfmac_f32_16x16x64_bf16 v[72:75], v[44:47], a[232:239], v0\n" \
  "v_smfmac_f32_16x16x64_bf16 v[60:63], v[48:51], v[144:151], v0\n" \
  "v_smfmac_f32_16x16x64_bf16 v[64:67], v[48:51], v[152:159], v0\n" \
  "v_smfmac_f32_16x16x64_bf16 v[68:71], v[48:51], v[160:167], v0\n" \
  "v_smfmac_f32_16x16x64_bf16 v[72:75], v[48:51], v[168:175], v0\n" \
  "v_smfmac_f32_16x16x64_bf16 v[60:63], v[52:55], v[224:231], v0\n" \
  "v_smfmac_f32_16x16x64_bf16 v[64:67], v[52:55], v[232:239], v0\n" \
  "v_smfmac_f32_16x16x64_bf16 v[68:71], v[52:55], v[240:247], v0\n" \
  "v_smfmac_f32_16x16x64_bf16 v[72:75], v[52:55], v[248:255], v0\n" \
  "v_permlane32_swap_b32 v56, v60\n" \
  "v_permlane32_swap_b32 v57, v61\n" \
  "v_permlane32_swap_b32 v58, v62\n" \
  "v_permlane32_swap_b32 v59, v63\n" \
  "v_pk_add_f32 v[56:57], v[56:57], v[60:61]\n" \
  "v_pk_add_f32 v[58:59], v[58:59], v[62:63]\n" \
  "v_cvt_pk_bf16_f32 v56, v56, v57\n" \
  "v_cvt_pk_bf16_f32 v57, v58, v59\n" \
  "ds_write_b64 v2, v[56:57] offset:1280\n" \
  "v_permlane32_swap_b32 v64, v68\n" \
  "v_permlane32_swap_b32 v65, v69\n" \
  "v_permlane32_swap_b32 v66, v70\n" \
  "v_permlane32_swap_b32 v67, v71\n" \
  "v_pk_add_f32 v[64:65], v[64:65], v[68:69]\n" \
  "v_pk_add_f32 v[66:67], v[66:67], v[70:71]\n" \
  "v_cvt_pk_bf16_f32 v64, v64, v65\n" \
  "v_cvt_pk_bf16_f32 v65, v66, v67\n" \
  "ds_write_b64 v2, v[64:65] offset:1792\n" \
  "v_permlane32_swap_b32 v72, v74\n" \
  "v_permlane32_swap_b32 v73, v75\n" \
  "v_pk_add_f32 v[72:73], v[72:73], v[74:75]\n" \
  "v_cvt_pk_bf16_f32 v72, v72, v73\n" \
  "ds_write_b32 v3, v72 offset:2304\n" \
  "s_waitcnt lgkmcnt(0)\n" \
  "s_barrier\n" \
  "s_mov_b32 s44, s45\n" \
  "s_barrier\n" \
  "s_cmp_eq_u32 s68, 64\n" \
  "s_cbranch_scc0 LOOPc1_%=\n" \
  "s_branch LDONE_%=\n" \
  "LMFc1_%=:\n" \
  "v_max_u32 v92, v16, v17\n" \
  "v_max3_u32 v92, v92, v18, v19\n" \
  "v_max3_u32 v92, v92, v20, v21\n" \
  "v_max3_u32 v92, v92, v22, v23\n" \
  "v_max3_u32 v92, v92, v24, v25\n" \
  "v_max3_u32 v92, v92, v26, v27\n" \
  "v_max3_u32 v92, v92, v28, v29\n" \
  "v_max3_u32 v92, v92, v30, v31\n" \
  "v_max3_u32 v92, v92, v32, v33\n" \
  "v_max3_u32 v92, v92, v34, v35\n" \
  "v_max3_u32 v92, v92, v36, v37\n" \
  "v_max3_u32 v92, v92, v38, v39\n" \
  "v_max3_u32 v92, v92, v40, v41\n" \
  "v_max3_u32 v92, v92, v42, v43\n" \
  "v_max3_u32 v92, v92, v44, v45\n" \
  "v_max3_u32 v92, v92, v46, v47\n" \
  "v_max3_u32 v92, v92, v48, v49\n" \
  "v_max3_u32 v92, v92, v50, v51\n" \
  "v_max3_u32 v92, v92, v52, v53\n" \
  "v_max3_u32 v92, v92, v54, v55\n" \
  "v_cmp_eq_u32 vcc, -1, v92\n" \
  "s_nop 3\n" \
  "s_cbranch_vccz LMOKc1_%=\n" \
  "s_cmp_gt_u32 s57, 20000\n" \
  "s_cbranch_scc1 LMOKc1_%=\n" \
  "s_add_u32 s57, s57, 1\n" \
  "s_barrier\n" \
  "buffer_load_dwordx4 v1, s[36:39], s75 offen offset:0 sc1 lds\n" \
  "buffer_load_dwordx4 v1, s[36:39], s75 offen offset:1024 sc1 lds\n" \
  "buffer_load_dwordx4 v1, s[36:39], s75 offen offset:2048 sc1 lds\n" \
  "buffer_load_dwordx4 v1, s[36:39], s75 offen offset:3072 sc1 lds\n" \
  "v_mov_b32 v56, 0\n" \
  "v_mov_b32 v57, 0\n" \
  "v_mov_b32 v58, 0\n" \
  "v_mov_b32 v59, 0\n" \
  "v_mov_b32 v60, 0\n" \
  "v_mov_b32 v61, 0\n" \
  "v_mov_b32 v62, 0\n" \
  "v_mov_b32 v63, 0\n" \
  "v_mov_b32 v64, 0\n" \
  "v_mov_b32 v65, 0\n" \
  "v_mov_b32 v66, 0\n" \
  "v_mov_b32 v67, 0\n" \
  "v_mov_b32 v68, 0\n" \
  "v_mov_b32 v69, 0\n" \
  "v_mov_b32 v70, 0\n" \
  "v_mov_b32 v71, 0\n" \
  "v_mov_b32 v72, 0\n" \
  "v_mov_b32 v73, 0\n" \
  "v_mov_b32 v74, 0\n" \
  "v_mov_b32 v75, 0\n" \
  "s_waitcnt vmcnt(0)\n" \
  "s_barrier\n" \
  "s_branch LMc1_%=\n" \
  "LSTARTc2_%=:\n" \
  "s_mov_b32 s57, 0\n" \
  "v_readlane_b32 s44, v4, 0\n" \
  "s_nop 3\n" \
  "LOOPc2_%=:\n" \
  "s_mov_b32 m0, 8192\n" \
  "s_add_u32 s75, s44, 16384\n" \
  "s_barrier\n" \
  "buffer_load_dwordx4 v1, s[36:39], s75 offen offset:0 sc1 lds\n" \
  "buffer_load_dwordx4 v1, s[36:39], s75 offen offset:1024 sc1 lds\n" \
  "v_mov_b32 v56, 0\n" \
  "v_mov_b32 v57, 0\n" \
  "v_mov_b32 v58, 0\n" \
  "v_mov_b32 v59, 0\n" \
  "v_mov_b32 v60, 0\n" \
  "v_mov_b32 v61, 0\n" \
  "v_mov_b32 v62, 0\n" \
  "v_mov_b32 v63, 0\n" \
  "v_mov_b32 v64, 0\n" \
  "v_mov_b32 v65, 0\n" \
  "v_mov_b32 v66, 0\n" \
  "v_mov_b32 v67, 0\n" \
  "v_mov_b32 v68, 0\n" \
  "v_mov_b32 v69, 0\n" \
  "v_mov_b32 v70, 0\n" \
  "v_mov_b32 v71, 0\n" \
  "v_mov_b32 v72, 0\n" \
  "v_mov_b32 v73, 0\n" \
  "v_mov_b32 v74, 0\n" \
  "v_mov_b32 v75, 0\n" \
  "s_add_u32 s68, s68, 1\n" \
  "s_min_u32 s74, s68, 63\n" \
  "v_readlane_b32 s45, v4, s74\n" \
  "s_waitcnt vmcnt(0)\n" \
  "s_barrier\n" \
  "LMc2_%=:\n" \
  "ds_read_b128 v[16:19], v1 offset:0\n" \
  "ds_read_b128 v[20:23], v1 offset:1024\n" \
  "ds_read_b128 v[24:27], v1 offset:2048\n" \
  "ds_read_b128 v[28:31], v1 offset:3072\n" \
  "ds_read_b128 v[32:35], v1 offset:4096\n" \
  "ds_read_b128 v[36:39], v1 offset:5120\n" \
  "ds_read_b128 v[40:43], v1 offset:6144\n" \
  "ds_read_b128 v[44:47], v1 offset:7168\n" \
  "ds_read_b128 v[48:51], v1 offset:8192\n" \
  "ds_read_b128 v[52:55], v1 offset:9216\n" \
  "s_waitcnt lgkmcnt(9)\n" \
  "v_smfmac_f32_16x16x64_bf16 v[56:59], v[16:19], a[0:7], v0\n" \
  "s_waitcnt lgkmcnt(8)\n" \
  "v_smfmac_f32_16x16x64_bf16 v[56:59], v[20:23], a[80:87], v0\n" \
  "s_waitcnt lgkmcnt(7)\n" \
  "v_smfmac_f32_16x16x64_bf16 v[56:59], v[24:27], a[160:167], v0\n" \
  "s_waitcnt lgkmcnt(6)\n" \
  "v_smfmac_f32_16x16x64_bf16 v[56:59], v[28:31], a[240:247], v0\n" \
  "s_waitcnt lgkmcnt(5)\n" \
  "v_smfmac_f32_16x16x64_bf16 v[56:59], v[32:35], v[176:183], v0\n" \
  "s_waitcnt lgkmcnt(4)\n" \
  "v_smfmac_f32_16x16x64_bf16 v[56:59], v[36:39], a[40:47], v0\n" \
  "s_waitcnt lgkmcnt(3)\n" \
  "v_smfmac_f32_16x16x64_bf16 v[56:59], v[40:43], a[120:127], v0\n" \
  "s_waitcnt lgkmcnt(2)\n" \
  "v_smfmac_f32_16x16x64_bf16 v[56:59], v[44:47], a[200:207], v0\n" \
  "s_waitcnt lgkmcnt(1)\n" \
  "v_smfmac_f32_16x16x64_bf16 v[56:59], v[48:51], v[136:143], v0\n" \
  "s_waitcnt lgkmcnt(0)\n" \
  "v_smfmac_f32_16x16x64_bf16 v[56:59], v[52:55], v[216:223], v0\n" \
  "v_smfmac_f32_16x16x64_bf16 v[60:63], v[16:19], a[8:15], v0\n" \
  "v_smfmac_f32_16x16x64_bf16 v[64:67], v[16:19], a[16:23], v0\n" \
  "v_cmp_u_f32 vcc, v56, v57\n" \
  "v_cmp_u_f32 s[82:83], v58, v59\n" \
  "s_nop 1\n" \
  "s_or_b64 vcc, vcc, s[82:83]\n" \
  "s_cbranch_scc1 LMFc2_%=\n" \
  "LMOKc2_%=:\n" \
  "v_smfmac_f32_16x16x64_bf16 v[68:71], v[16:19], a[24:31], v0\n" \
  "v_smfmac_f32_16x16x64_bf16 v[72:75], v[16:19], a[32:39], v0\n" \
  "v_smfmac_f32_16x16x64_bf16 v[60:63], v[20:23], a[88:95], v0\n" \
  "v_smfmac_f32_16x16x64_bf16 v[64:67], v[20:23], a[96:103], v0\n" \
  "v_smfmac_f32_16x16x64_bf16 v[68:71], v[20:23], a[104:111], v0\n" \
  "v_smfmac_f32_16x16x64_bf16 v[72:75], v[20:23], a[112:119], v0\n" \
  "v_smfmac_f32_16x16x64_bf16 v[60:63], v[24:27], a[168:175], v0\n" \
  "v_smfmac_f32_16x16x64_bf16 v[64:67], v[24:27], a[176:183], v0\n" \
  "v_smfmac_f32_16x16x64_bf16 v[68:71], v[24:27], a[184:191], v0\n" \
  "v_smfmac_f32_16x16x64_bf16 v[72:75], v[24:27], a[192:199], v0\n" \
  "v_smfmac_f32_16x16x64_bf16 v[60:63], v[28:31], a[248:255], v0\n" \
  "v_smfmac_f32_16x16x64_bf16 v[64:67], v[28:31], v[112:119], v0\n" \
  "v_smfmac_f32_16x16x64_bf16 v[68:71], v[28:31], v[120:127], v0\n" \
  "v_smfmac_f32_16x16x64_bf16 v[72:75], v[28:31], v[128:135], v0\n" \
  "v_smfmac_f32_16x16x64_bf16 v[60:63], v[32:35], v[184:191], v0\n" \
  "v_smfmac_f32_16x16x64_bf16 v[64:67], v[32:35], v[192:199], v0\n" \
  "v_smfmac_f32_16x16x64_bf16 v[68:71], v[32:35], v[200:207], v0\n" \
  "v_smfmac_f32_16x16x64_bf16 v[72:75], v[32:35], v[208:215], v0\n" \
  "v_smfmac_f32_16x16x64_bf16 v[60:63], v[36:39], a[48:55], v0\n" \
  "v_smfmac_f32_16x16x64_bf16 v[64:67], v[36:39], a[56:63], v0\n" \
  "v_smfmac_f32_16x16x64_bf16 v[68:71], v[36:39], a[64:71], v0\n" \
  "v_smfmac_f32_16x16x64_bf16 v[72:75], v[36:39], a[72:79], v0\n" \
  "v_smfmac_f32_16x16x64_bf16 v[60:63], v[40:43], a[128:135], v0\n" \
  "v_smfmac_f32_16x16x64_bf16 v[64:67], v[40:43], a[136:143], v0\n" \
  "v_smfmac_f32_16x16x64_bf16 v[68:71], v[40:43], a[144:151], v0\n" \
  "v_smfmac_f32_16x16x64_bf16 v[72:75], v[40:43], a[152:159], v0\n" \
  "v_smfmac_f32_16x16x64_bf16 v[60:63], v[44:47], a[208:215], v0\n" \
  "v_smfmac_f32_16x16x64_bf16 v[64:67], v[44:47], a[216:223], v0\n" \
  "v_smfmac_f32_16x16x64_bf16 v[68:71], v[44:47], a[224:231], v0\n" \
  "v_smfmac_f32_16x16x64_bf16 v[72:75], v[44:47], a[232:239], v0\n" \
  "v_smfmac_f32_16x16x64_bf16 v[60:63], v[48:51], v[144:151], v0\n" \
  "v_smfmac_f32_16x16x64_bf16 v[64:67], v[48:51], v[152:159], v0\n" \
  "v_smfmac_f32_16x16x64_bf16 v[68:71], v[48:51], v[160:167], v0\n" \
  "v_smfmac_f32_16x16x64_bf16 v[72:75], v[48:51], v[168:175], v0\n" \
  "v_smfmac_f32_16x16x64_bf16 v[60:63], v[52:55], v[224:231], v0\n" \
  "v_smfmac_f32_16x16x64_bf16 v[64:67], v[52:55], v[232:239], v0\n" \
  "v_smfmac_f32_16x16x64_bf16 v[68:71], v[52:55], v[240:247], v0\n" \
  "v_smfmac_f32_16x16x64_bf16 v[72:75], v[52:55], v[248:255], v0\n" \
  "v_permlane32_swap_b32 v56, v60\n" \
  "v_permlane32_swap_b32 v57, v61\n" \
  "v_permlane32_swap_b32 v58, v62\n" \
  "v_permlane32_swap_b32 v59, v63\n" \
  "v_pk_add_f32 v[56:57], v[56:57], v[60:61]\n" \
  "v_pk_add_f32 v[58:59], v[58:59], v[62:63]\n" \
  "v_cvt_pk_bf16_f32 v56, v56, v57\n" \
  "v_cvt_pk_bf16_f32 v57, v58, v59\n" \
  "ds_write_b64 v2, v[56:57] offset:2560\n" \
  "v_permlane32_swap_b32 v64, v68\n" \
  "v_permlane32_swap_b32 v65, v69\n" \
  "v_permlane32_swap_b32 v66, v70\n" \
  "v_permlane32_swap_b32 v67, v71\n" \
  "v_pk_add_f32 v[64:65], v[64:65], v[68:69]\n" \
  "v_pk_add_f32 v[66:67], v[66:67], v[70:71]\n" \
  "v_cvt_pk_bf16_f32 v64, v64, v65\n" \
  "v_cvt_pk_bf16_f32 v65, v66, v67\n" \
  "ds_write_b64 v2, v[64:65] offset:3072\n" \
  "v_permlane32_swap_b32 v72, v74\n" \
  "v_permlane32_swap_b32 v73, v75\n" \
  "v_pk_add_f32 v[72:73], v[72:73], v[74:75]\n" \
  "v_cvt_pk_bf16_f32 v72, v72, v73\n" \
  "ds_write_b32 v3, v72 offset:3584\n" \
  "s_waitcnt lgkmcnt(0)\n" \
  "s_barrier\n" \
  "s_mov_b32 s44, s45\n" \
  "s_barrier\n" \
  "s_cmp_eq_u32 s68, 64\n" \
  "s_cbranch_scc0 LOOPc2_%=\n" \
  "s_branch LDONE_%=\n" \
  "LMFc2_%=:\n" \
  "v_max_u32 v92, v16, v17\n" \
  "v_max3_u32 v92, v92, v18, v19\n" \
  "v_max3_u32 v92, v92, v20, v21\n" \
  "v_max3_u32 v92, v92, v22, v23\n" \
  "v_max3_u32 v92, v92, v24, v25\n" \
  "v_max3_u32 v92, v92, v26, v27\n" \
  "v_max3_u32 v92, v92, v28, v29\n" \
  "v_max3_u32 v92, v92, v30, v31\n" \
  "v_max3_u32 v92, v92, v32, v33\n" \
  "v_max3_u32 v92, v92, v34, v35\n" \
  "v_max3_u32 v92, v92, v36, v37\n" \
  "v_max3_u32 v92, v92, v38, v39\n" \
  "v_max3_u32 v92, v92, v40, v41\n" \
  "v_max3_u32 v92, v92, v42, v43\n" \
  "v_max3_u32 v92, v92, v44, v45\n" \
  "v_max3_u32 v92, v92, v46, v47\n" \
  "v_max3_u32 v92, v92, v48, v49\n" \
  "v_max3_u32 v92, v92, v50, v51\n" \
  "v_max3_u32 v92, v92, v52, v53\n" \
  "v_max3_u32 v92, v92, v54, v55\n" \
  "v_cmp_eq_u32 vcc, -1, v92\n" \
  "s_nop 3\n" \
  "s_cbranch_vccz LMOKc2_%=\n" \
  "s_cmp_gt_u32 s57, 20000\n" \
  "s_cbranch_scc1 LMOKc2_%=\n" \
  "s_add_u32 s57, s57, 1\n" \
  "s_barrier\n" \
  "buffer_load_dwordx4 v1, s[36:39], s75 offen offset:0 sc1 lds\n" \
  "buffer_load_dwordx4 v1, s[36:39], s75 offen offset:1024 sc1 lds\n" \
  "v_mov_b32 v56, 0\n" \
  "v_mov_b32 v57, 0\n" \
  "v_mov_b32 v58, 0\n" \
  "v_mov_b32 v59, 0\n" \
  "v_mov_b32 v60, 0\n" \
  "v_mov_b32 v61, 0\n" \
  "v_mov_b32 v62, 0\n" \
  "v_mov_b32 v63, 0\n" \
  "v_mov_b32 v64, 0\n" \
  "v_mov_b32 v65, 0\n" \
  "v_mov_b32 v66, 0\n" \
  "v_mov_b32 v67, 0\n" \
  "v_mov_b32 v68, 0\n" \
  "v_mov_b32 v69, 0\n" \
  "v_mov_b32 v70, 0\n" \
  "v_mov_b32 v71, 0\n" \
  "v_mov_b32 v72, 0\n" \
  "v_mov_b32 v73, 0\n" \
  "v_mov_b32 v74, 0\n" \
  "v_mov_b32 v75, 0\n" \
  "s_waitcnt vmcnt(0)\n" \
  "s_barrier\n" \
  "s_branch LMc2_%=\n" \
  "LSTARTc3_%=:\n" \
  "v_mov_b32 v96, v16\n" \
  "v_mov_b32 v97, v17\n" \
  "v_mov_b32 v100, v18\n" \
  "v_mov_b32 v101, v29\n" \
  "s_memrealtime s[76:77]\n" \
  "s_memtime s[78:79]\n" \
  "s_waitcnt lgkmcnt(0)\n" \
  "s_mov_b32 s88, s76\n" \
  "s_mov_b32 s89, s78\n" \
  "s_mov_b32 s56, s38\n" \
  "s_mov_b32 s38, 0\n" \
  "s_mov_b32 s73, 20001\n" \
  "s_mov_b32 s57, 20001\n" \
  "s_getpc_b64 s[82:83]\n" \
  "LPCEc3_%=:\n" \
  "s_add_u32 s84, s82, LEEc3_%=-LPCEc3_%=+24\n" \
  "s_addc_u32 s85, s83, 0\n" \
  "s_getpc_b64 s[82:83]\n" \
  "LPCLc3_%=:\n" \
  "s_add_u32 s90, s82, LELc3_%=-LPCLc3_%=+24\n" \
  "s_addc_u32 s91, s83, 0\n" \
  "v_mov_b32 v92, 0\n" \
  "s_memrealtime s[76:77]\n" \
  "s_memtime s[78:79]\n" \
  "s_waitcnt lgkmcnt(0)\n" \
  "s_lshl_b32 s48, s76, 2\n" \
  "v_readlane_b32 s72, v92, 0\n" \
  "s_nop 1\n" \
  "s_sub_u32 s74, s72, s48\n" \
  "s_add_u32 s74, s74, 40000\n" \
  "s_cmp_gt_u32 s74, 80000\n" \
  "s_cselect_b32 s72, s48, s72\n" \
  "v_readlane_b32 s94, v92, 1\n" \
  "s_nop 1\n" \
  "s_sub_u32 s74, s94, s48\n" \
  "s_add_u32 s74, s74, 40000\n" \
  "s_cmp_gt_u32 s74, 80000\n" \
  "s_cselect_b32 s94, s48, s94\n" \
  "v_readlane_b32 s95, v92, 2\n" \
  "s_nop 1\n" \
  "s_sub_u32 s74, s95, s48\n" \
  "s_add_u32 s74, s74, 40000\n" \
  "s_cmp_gt_u32 s74, 80000\n" \
  "s_cselect_b32 s95, s48, s95\n" \
  "s_sub_u32 s74, s78, s89\n" \
  "s_sub_u32 s75, s76, s88\n" \
  "v_cvt_f32_u32 v92, s74\n" \
  "v_cvt_f32_u32 v93, s75\n" \
  "v_rcp_f32 v93, v93\n" \
  "v_mul_f32 v92, 0x43820000, v92\n" \
  "v_mul_f32 v92, v92, v93\n" \
  "v_cvt_u32_f32 v92, v92\n" \
  "s_nop 0\n" \
  "v_readfirstlane_b32 s74, v92\n" \
  "s_min_u32 s74, s74, 6400\n" \
  "s_cmp_gt_u32 s75, 150\n" \
  "s_cselect_b32 s50, s74, s50\n" \
  "s_cselect_b32 s88, s76, s88\n" \
  "s_cselect_b32 s89, s78, s89\n" \
  "s_add_u32 s49, s78, 0x1000000\n" \
  "s_mul_i32 s86, s54, 6\n" \
  "s_mov_b32 s87, -200\n" \
  "s_mov_b32 s51, 0\n" \
  "s_mov_b32 s96, 0\n" \
  "s_mul_i32 s74, s51, s54\n" \
  "s_add_u32 s74, s74, s72\n" \
  "s_sub_i32 s75, s74, s48\n" \
  "s_mov_b32 s48, s74\n" \
  "s_mul_i32 s75, s75, s50\n" \
  "s_ashr_i32 s75, s75, 10\n" \
  "s_add_u32 s49, s49, s75\n" \
  "s_mov_b32 s87, 0x80000000\n" \
  "v_accvgpr_write_b32 a88, -1\n" \
  "s_branch LOOPc3_%=\n" \
  "LPASSc3_%=:\n" \
  "s_waitcnt vmcnt(0)\n" \
  "s_mov_b32 s38, s56\n" \
  "s_mov_b32 s68, 0\n" \
  "s_mov_b32 s73, 0\n" \
  "s_mov_b32 s58, 0\n" \
  "s_mov_b32 s59, 0\n" \
  "LQU4c3_%=:\n" \
  "buffer_load_dword v92, v14, s[36:39], 0 offen offset:1536 sc1\n" \
  "s_waitcnt vmcnt(0)\n" \
  "v_cmp_ne_u32 vcc, 1, v92\n" \
  "s_nop 3\n" \
  "s_cbranch_vccz LRU4c3_%=\n" \
  "s_add_u32 s73, s73, 1\n" \
  "s_cmp_gt_u32 s73, 20000\n" \
  "s_cbranch_scc1 LRU4c3_%=\n" \
  "s_sleep 1\n" \
  "s_branch LQU4c3_%=\n" \
  "LRU4c3_%=:\n" \
  "v_readlane_b32 s46, v96, 6\n" \
  "s_nop 4\n" \
  "buffer_load_dwordx4 a[88:91], v100, s[36:39], s46 offen sc1\n" \
  "v_readlane_b32 s46, v96, 8\n" \
  "s_nop 4\n" \
  "buffer_load_dwordx4 a[104:107], v100, s[36:39], s46 offen sc1\n" \
  "v_readlane_b32 s46, v96, 7\n" \
  "s_nop 4\n" \
  "buffer_load_dwordx4 a[92:95], v100, s[36:39], s46 offen sc1\n" \
  "v_readlane_b32 s46, v96, 9\n" \
  "s_nop 4\n" \
  "buffer_load_dwordx4 a[108:111], v100, s[36:39], s46 offen sc1\n" \
  "v_readlane_b32 s46, v96, 16\n" \
  "s_nop 4\n" \
  "buffer_load_dwordx4 a[96:99], v100, s[36:39], s46 offen sc1\n" \
  "v_readlane_b32 s46, v96, 18\n" \
  "s_nop 4\n" \
  "buffer_load_dwordx4 a[112:115], v100, s[36:39], s46 offen sc1\n" \
  "v_readlane_b32 s46, v96, 17\n" \
  "s_nop 4\n" \
  "buffer_load_dwordx4 a[100:103], v100, s[36:39], s46 offen sc1\n" \
  "v_readlane_b32 s46, v96, 19\n" \
  "s_nop 4\n" \
  "buffer_load_dwordx4 a[116:119], v100, s[36:39], s46 offen sc1\n" \
  "LQAc3_%=:\n" \
  "buffer_load_dword v92, v8, s[36:39], 0 offen offset:0 sc1\n" \
  "s_waitcnt vmcnt(0)\n" \
  "v_cmp_eq_u32 vcc, 0, v92\n" \
  "s_nop 3\n" \
  "s_cbranch_vccz LRAc3_%=\n" \
  "s_add_u32 s73, s73, 1\n" \
  "s_cmp_gt_u32 s73, 20000\n" \
  "s_cbranch_scc1 LRAc3_%=\n" \
  "s_sleep 2\n" \
  "s_branch LQAc3_%=\n" \
  "LRAc3_%=:\n" \
  "s_memrealtime s[76:77]\n" \
  "s_memtime s[78:79]\n" \
  "s_waitcnt lgkmcnt(0)\n" \
  "s_lshl_b32 s48, s76, 2\n" \
  "v_readlane_b32 s72, v92, 0\n" \
  "s_nop 1\n" \
  "s_sub_u32 s74, s72, s48\n" \
  "s_add_u32 s74, s74, 40000\n" \
  "s_cmp_gt_u32 s74, 80000\n" \
  "s_cselect_b32 s72, s48, s72\n" \
  "v_readlane_b32 s94, v92, 1\n" \
  "s_nop 1\n" \
  "s_sub_u32 s74, s94, s48\n" \
  "s_add_u32 s74, s74, 40000\n" \
  "s_cmp_gt_u32 s74, 80000\n" \
  "s_cselect_b32 s94, s48, s94\n" \
  "v_readlane_b32 s95, v92, 2\n" \
  "s_nop 1\n" \
  "s_sub_u32 s74, s95, s48\n" \
  "s_add_u32 s74, s74, 40000\n" \
  "s_cmp_gt_u32 s74, 80000\n" \
  "s_cselect_b32 s95, s48, s95\n" \
  "s_sub_u32 s74, s78, s89\n" \
  "s_sub_u32 s75, s76, s88\n" \
  "v_cvt_f32_u32 v92, s74\n" \
  "v_cvt_f32_u32 v93, s75\n" \
  "v_rcp_f32 v93, v93\n" \
  "v_mul_f32 v92, 0x43820000, v92\n" \
  "v_mul_f32 v92, v92, v93\n" \
  "v_cvt_u32_f32 v92, v92\n" \
  "s_nop 0\n" \
  "v_readfirstlane_b32 s74, v92\n" \
  "s_min_u32 s74, s74, 6400\n" \
  "s_cmp_gt_u32 s75, 150\n" \
  "s_cselect_b32 s50, s74, s50\n" \
  "s_cselect_b32 s88, s76, s88\n" \
  "s_cselect_b32 s89, s78, s89\n" \
  "s_add_u32 s49, s78, 0x1000000\n" \
  "s_mul_i32 s86, s54, 6\n" \
  "s_mov_b32 s87, -200\n" \
  "s_mov_b32 s51, 0\n" \
  "s_mov_b32 s96, 0\n" \
  "s_mov_b32 s57, 0\n" \
  "v_readlane_b32 s44, v4, 0\n" \
  "s_nop 3\n" \
  "LOOPc3_%=:\n" \
  "LZWc3_%=:\n" \
  "s_memrealtime s[76:77]\n" \
  "s_memtime s[78:79]\n" \
  "s_waitcnt lgkmcnt(0)\n" \
  "s_lshl_b32 s75, s76, 2\n" \
  "s_sub_i32 s75, s48, s75\n" \
  "s_mul_i32 s75, s75, s50\n" \
  "s_ashr_i32 s75, s75, 10\n" \
  "s_sub_i32 s74, s49, s78\n" \
  "s_min_i32 s74, s74, s75\n" \
  "s_sub_i32 s75, s75, 92\n" \
  "s_max_i32 s74, s74, s75\n" \
  "s_add_u32 s49, s74, s78\n" \
  "s_cmp_gt_i32 s74, 60\n" \
  "s_cbranch_scc1 LZWc3_%=\n" \
  "s_barrier\n" \
  "v_mov_b32 v56, 0\n" \
  "v_mov_b32 v57, 0\n" \
  "v_mov_b32 v58, 0\n" \
  "v_mov_b32 v59, 0\n" \
  "v_readlane_b32 s74, v9, s68\n" \
  "s_nop 0\n" \
  "s_add_u32 s46, s74, 0\n" \
  "s_add_u32 s47, s74, 4096\n" \
  "s_add_u32 s69, s74, 16384\n" \
  "s_add_u32 s70, s74, 20480\n" \
  "s_add_u32 s68, s68, 1\n" \
  "s_min_u32 s74, s68, 63\n" \
  "v_readlane_b32 s45, v4, s74\n" \
  "s_nop 15\n" \
  "buffer_load_dwordx4 a[8:11], v5, s[36:39], s46 offen offset:0 sc1\n" \
  "buffer_load_dwordx4 a[12:15], v5, s[36:39], s46 offen offset:1024 sc1\n" \
  "buffer_load_dwordx4 a[16:19], v5, s[36:39], s46 offen offset:2048 sc1\n" \
  "buffer_load_dwordx4 a[20:23], v5, s[36:39], s46 offen offset:3072 sc1\n" \
  "buffer_load_dwordx4 a[24:27], v5, s[36:39], s47 offen offset:0 sc1\n" \
  "buffer_load_dwordx4 a[28:31], v5, s[36:39], s47 offen offset:1024 sc1\n" \
  "buffer_load_dwordx4 a[32:35], v5, s[36:39], s47 offen offset:2048 sc1\n" \
  "buffer_load_dwordx4 a[36:39], v5, s[36:39], s47 offen offset:3072 sc1\n" \
  "buffer_load_dwordx4 a[48:51], v5, s[36:39], s69 offen offset:0 sc1\n" \
  "buffer_load_dwordx4 a[52:55], v5, s[36:39], s69 offen offset:1024 sc1\n" \
  "buffer_load_dwordx4 a[56:59], v5, s[36:39], s69 offen offset:2048 sc1\n" \
  "buffer_load_dwordx4 a[60:63], v5, s[36:39], s69 offen offset:3072 sc1\n" \
  "buffer_load_dwordx4 a[64:67], v5, s[36:39], s70 offen offset:0 sc1\n" \
  "buffer_load_dwordx4 a[68:71], v5, s[36:39], s70 offen offset:1024 sc1\n" \
  "buffer_load_dwordx4 a[72:75], v5, s[36:39], s70 offen offset:2048 sc1\n" \
  "buffer_load_dwordx4 a[76:79], v5, s[36:39], s70 offen offset:3072 sc1\n" \
  "buffer_load_dword v98, v97, s[36:39], s44 offen sc1\n" \
  "s_barrier\n" \
  "LMc3_%=:\n" \
  "ds_read_b128 v[16:19], v1 offset:0\n" \
  "ds_read_b128 v[20:23], v1 offset:1024\n" \
  "ds_read_b128 v[24:27], v1 offset:2048\n" \
  "ds_read_b128 v[28:31], v1 offset:3072\n" \
  "ds_read_b128 v[32:35], v1 offset:4096\n" \
  "ds_read_b128 v[36:39], v1 offset:5120\n" \
  "ds_read_b128 v[40:43], v1 offset:6144\n" \
  "ds_read_b128 v[44:47], v1 offset:7168\n" \
  "ds_read_b128 v[48:51], v1 offset:8192\n" \
  "ds_read_b128 v[52:55], v1 offset:9216\n" \
  "s_waitcnt lgkmcnt(9)\n" \
  "v_smfmac_f32_16x16x64_bf16 v[56:59], v[16:19], a[0:7], v0\n" \
  "s_waitcnt lgkmcnt(8)\n" \
  "v_smfmac_f32_16x16x64_bf16 v[56:59], v[20:23], a[80:87], v0\n" \
  "s_waitcnt lgkmcnt(7)\n" \
  "v_smfmac_f32_16x16x64_bf16 v[56:59], v[24:27], a[160:167], v0\n" \
  "s_waitcnt lgkmcnt(6)\n" \
  "v_smfmac_f32_16x16x64_bf16 v[56:59], v[28:31], a[88:95], v0\n" \
  "s_waitcnt lgkmcnt(5)\n" \
  "v_smfmac_f32_16x16x64_bf16 v[56:59], v[32:35], a[104:111], v0\n" \
  "s_waitcnt lgkmcnt(4)\n" \
  "v_smfmac_f32_16x16x64_bf16 v[56:59], v[36:39], a[40:47], v0\n" \
  "s_waitcnt lgkmcnt(3)\n" \
  "v_smfmac_f32_16x16x64_bf16 v[56:59], v[40:43], a[120:127], v0\n" \
  "s_waitcnt lgkmcnt(2)\n" \
  "v_smfmac_f32_16x16x64_bf16 v[56:59], v[44:47], a[200:207], v0\n" \
  "s_waitcnt lgkmcnt(1)\n" \
  "v_smfmac_f32_16x16x64_bf16 v[56:59], v[48:51], a[96:103], v0\n" \
  "s_waitcnt lgkmcnt(0)\n" \
  "v_smfmac_f32_16x16x64_bf16 v[56:59], v[52:55], a[112:119], v0\n" \
  "s_nop 7\n" \
  "v_cmp_u_f32 vcc, v56, v57\n" \
  "v_cmp_u_f32 s[82:83], v58, v59\n" \
  "s_nop 1\n" \
  "s_or_b64 vcc, vcc, s[82:83]\n" \
  "s_cbranch_scc1 LMFc3_%=\n" \
  "LMOKc3_%=:\n" \
  "v_permlane32_swap_b32 v56, v58\n" \
  "v_permlane32_swap_b32 v57, v59\n" \
  "v_pk_add_f32 v[56:57], v[56:57], v[58:59]\n" \
  "v_cvt_pk_bf16_f32 v56, v56, v57\n" \
  "ds_write_b32 v3, v56 offset:3840\n" \
  "s_waitcnt lgkmcnt(0)\n" \
  "s_cmp_eq_u32 s68, 64\n" \
  "s_cbranch_scc1 LENDc3_%=\n" \
  "s_add_u32 s46, s44, 32768\n" \
  "s_add_u32 s47, s44, 36864\n" \
  "s_add_u32 s69, s44, 49152\n" \
  "s_add_u32 s70, s44, 53248\n" \
  "s_sub_u32 s43, s45, s93\n" \
  "s_mul_i32 s74, s51, s54\n" \
  "s_add_u32 s74, s74, s94\n" \
  "s_sub_i32 s75, s74, s48\n" \
  "s_mov_b32 s48, s74\n" \
  "s_mul_i32 s75, s75, s50\n" \
  "s_ashr_i32 s75, s75, 10\n" \
  "s_add_u32 s49, s49, s75\n" \
  "s_memrealtime s[76:77]\n" \
  "s_memtime s[78:79]\n" \
  "s_waitcnt lgkmcnt(0)\n" \
  "s_lshl_b32 s75, s76, 2\n" \
  "s_sub_i32 s75, s48, s75\n" \
  "s_mul_i32 s75, s75, s50\n" \
  "s_ashr_i32 s75, s75, 10\n" \
  "s_sub_i32 s74, s49, s78\n" \
  "s_min_i32 s74, s74, s75\n" \
  "s_sub_i32 s75, s75, 92\n" \
  "s_max_i32 s74, s74, s75\n" \
  "s_add_u32 s49, s74, s78\n" \
  "s_sub_u32 s74, s78, s89\n" \
  "s_sub_u32 s75, s76, s88\n" \
  "v_cvt_f32_u32 v92, s74\n" \
  "v_cvt_f32_u32 v93, s75\n" \
  "v_rcp_f32 v93, v93\n" \
  "v_mul_f32 v92, 0x43820000, v92\n" \
  "v_mul_f32 v92, v92, v93\n" \
  "v_cvt_u32_f32 v92, v92\n" \
  "s_nop 0\n" \
  "v_readfirstlane_b32 s74, v92\n" \
  "s_min_u32 s74, s74, 6400\n" \
  "s_cmp_gt_u32 s75, 900\n" \
  "s_cselect_b32 s50, s74, s50\n" \
  "s_cselect_b32 s88, s76, s88\n" \
  "s_cselect_b32 s89, s78, s89\n" \
  "LWEc3_%=:\n" \
  "s_memrealtime s[76:77]\n" \
  "s_memtime s[78:79]\n" \
  "s_waitcnt lgkmcnt(0)\n" \
  "s_lshl_b32 s75, s76, 2\n" \
  "s_sub_i32 s75, s48, s75\n" \
  "s_mul_i32 s75, s75, s50\n" \
  "s_ashr_i32 s75, s75, 10\n" \
  "s_sub_i32 s74, s49, s78\n" \
  "s_min_i32 s74, s74, s75\n" \
  "s_sub_i32 s75, s75, 92\n" \
  "s_max_i32 s74, s74, s75\n" \
  "s_add_u32 s49, s74, s78\n" \
  "s_cmp_gt_i32 s74, 168\n" \
  "s_cbranch_scc1 LWEc3_%=\n" \
  "s_cmp_lt_i32 s74, s87\n" \
  "s_cbranch_scc1 LSEc3_%=\n" \
  "s_max_i32 s74, s74, 48\n" \
  "s_lshr_b32 s74, s74, 3\n" \
  "s_lshl_b32 s74, s74, 2\n" \
  "s_sub_u32 s80, s84, s74\n" \
  "s_subb_u32 s81, s85, 0\n" \
  "s_setpc_b64 s[80:81]\n" \
  "s_nop 1\n" \
  "s_nop 1\n" \
  "s_nop 1\n" \
  "s_nop 1\n" \
  "s_nop 1\n" \
  "s_nop 1\n" \
  "s_nop 1\n" \
  "s_nop 1\n" \
  "s_nop 1\n" \
  "s_nop 1\n" \
  "s_nop 1\n" \
  "s_nop 1\n" \
  "s_nop 1\n" \
  "s_nop 1\n" \
  "s_nop 1\n" \
  "s_nop 1\n" \
  "LEEc3_%=:\n" \
  "buffer_load_dwordx4 v[112:115], v10, s[36:39], s46 offen offset:0 sc1\n" \
  "buffer_load_dwordx4 v[120:123], v10, s[36:39], s46 offen offset:2048 sc1\n" \
  "buffer_load_dwordx4 v[116:119], v6, s[36:39], s46 offen offset:1024 sc1\n" \
  "buffer_load_dwordx4 v[124:127], v6, s[36:39], s46 offen offset:3072 sc1\n" \
  "buffer_load_dwordx4 v[132:135], v6, s[36:39], s47 offen offset:1024 sc1\n" \
  "buffer_load_dwordx4 v[140:143], v6, s[36:39], s47 offen offset:3072 sc1\n" \
  "buffer_load_dwordx4 v[128:131], v10, s[36:39], s47 offen offset:0 sc1\n" \
  "buffer_load_dwordx4 v[136:139], v10, s[36:39], s47 offen offset:2048 sc1\n" \
  "buffer_load_dwordx4 v[144:147], v10, s[36:39], s69 offen offset:0 sc1\n" \
  "buffer_load_dwordx4 v[152:155], v10, s[36:39], s69 offen offset:2048 sc1\n" \
  "buffer_load_dwordx4 v[148:151], v6, s[36:39], s69 offen offset:1024 sc1\n" \
  "buffer_load_dwordx4 v[156:159], v6, s[36:39], s69 offen offset:3072 sc1\n" \
  "buffer_load_dwordx4 v[164:167], v6, s[36:39], s70 offen offset:1024 sc1\n" \
  "buffer_load_dwordx4 v[172:175], v6, s[36:39], s70 offen offset:3072 sc1\n" \
  "buffer_load_dwordx4 v[160:163], v10, s[36:39], s70 offen offset:0 sc1\n" \
  "buffer_load_dwordx4 v[168:171], v10, s[36:39], s70 offen offset:2048 sc1\n" \
  "s_add_u32 s46, s44, 65536\n" \
  "s_add_u32 s47, s44, 69632\n" \
  "s_mul_i32 s74, s51, s54\n" \
  "s_add_u32 s74, s74, s95\n" \
  "s_add_u32 s74, s74, s96\n" \
  "s_sub_i32 s75, s74, s48\n" \
  "s_mov_b32 s48, s74\n" \
  "s_mul_i32 s75, s75, s50\n" \
  "s_ashr_i32 s75, s75, 10\n" \
  "s_add_u32 s49, s49, s75\n" \
  "LULc3_%=:\n" \
  "s_memtime s[78:79]\n" \
  "s_waitcnt lgkmcnt(0)\n" \
  "s_sub_i32 s74, s49, s78\n" \
  "s_cmp_gt_i32 s74, 168\n" \
  "s_cbranch_scc1 LULc3_%=\n" \
  "s_max_i32 s74, s74, 48\n" \
  "s_lshr_b32 s74, s74, 3\n" \
  "s_lshl_b32 s74, s74, 2\n" \
  "s_sub_u32 s80, s90, s74\n" \
  "s_subb_u32 s81, s91, 0\n" \
  "s_setpc_b64 s[80:81]\n" \
  "s_nop 1\n" \
  "s_nop 1\n" \
  "s_nop 1\n" \
  "s_nop 1\n" \
  "s_nop 1\n" \
  "s_nop 1\n" \
  "s_nop 1\n" \
  "s_nop 1\n" \
  "s_nop 1\n" \
  "s_nop 1\n" \
  "s_nop 1\n" \
  "s_nop 1\n" \
  "s_nop 1\n" \
  "s_nop 1\n" \
  "s_nop 1\n" \
  "s_nop 1\n" \
  "LELc3_%=:\n" \
  "buffer_load_dwordx4 v[16:19], v1, s[36:39], s46 offen offset:0 sc1\n" \
  "buffer_load_dwordx4 v[20:23], v1, s[36:39], s46 offen offset:1024 sc1\n" \
  "buffer_load_dwordx4 v[24:27], v1, s[36:39], s46 offen offset:2048 sc1\n" \
  "buffer_load_dwordx4 v[28:31], v1, s[36:39], s46 offen offset:3072 sc1\n" \
  "buffer_load_dwordx4 v[32:35], v1, s[36:39], s47 offen offset:0 sc1\n" \
  "buffer_load_dwordx4 v[36:39], v1, s[36:39], s47 offen offset:1024 sc1\n" \
  "buffer_load_dwordx4 v[40:43], v1, s[36:39], s47 offen offset:2048 sc1\n" \
  "buffer_load_dwordx4 v[44:47], v1, s[36:39], s47 offen offset:3072 sc1\n" \
  "s_cmp_lg_u32 s71, 0\n" \
  "s_cbranch_scc1 LNBc3_%=\n" \
  "s_barrier\n" \
  "LNBc3_%=:\n" \
  "s_mov_b32 s71, 0\n" \
  "ds_read_b128 v[224:227], v1 offset:10240\n" \
  "ds_read_b128 v[228:231], v1 offset:11264\n" \
  "ds_read_b128 v[232:235], v1 offset:12288\n" \
  "ds_read_b128 v[236:239], v1 offset:13312\n" \
  "s_mov_b64 exec, s[40:41]\n" \
  "s_waitcnt vmcnt(23)\n" \
  "v_mfma_f32_16x16x32_bf16 v[176:179], v[112:115], a[8:11], 0\n" \
  "s_waitcnt vmcnt(22)\n" \
  "v_mfma_f32_16x16x32_bf16 v[184:187], v[120:123], a[16:19], 0\n" \
  "s_waitcnt vmcnt(20)\n" \
  "v_mfma_f32_16x16x32_bf16 v[180:183], v[116:119], a[12:15], 0\n" \
  "v_cvt_pk_bf16_f32 v208, v176, v177\n" \
  "v_cvt_pk_bf16_f32 v209, v178, v179\n" \
  "v_mfma_f32_16x16x32_bf16 v[188:191], v[124:127], a[20:23], 0\n" \
  "s_waitcnt vmcnt(18)\n" \
  "v_cvt_pk_bf16_f32 v210, v184, v185\n" \
  "v_cvt_pk_bf16_f32 v211, v186, v187\n" \
  "v_mfma_f32_16x16x32_bf16 v[196:199], v[132:135], a[28:31], 0\n" \
  "s_not_b64 exec, exec\n" \
  "v_cvt_pk_bf16_f32 v208, v180, v181\n" \
  "v_cvt_pk_bf16_f32 v209, v182, v183\n" \
  "v_mfma_f32_16x16x32_bf16 v[204:207], v[140:143], a[36:39], 0\n" \
  "s_waitcnt vmcnt(16)\n" \
  "v_cvt_pk_bf16_f32 v210, v188, v189\n" \
  "v_cvt_pk_bf16_f32 v211, v190, v191\n" \
  "v_mfma_f32_16x16x32_bf16 v[192:195], v[128:131], a[24:27], 0\n" \
  "v_cvt_pk_bf16_f32 v212, v196, v197\n" \
  "v_cvt_pk_bf16_f32 v213, v198, v199\n" \
  "v_mfma_f32_16x16x32_bf16 v[200:203], v[136:139], a[32:35], 0\n" \
  "s_waitcnt vmcnt(14)\n" \
  "v_cvt_pk_bf16_f32 v214, v204, v205\n" \
  "v_cvt_pk_bf16_f32 v215, v206, v207\n" \
  "v_mfma_f32_16x16x32_bf16 v[176:179], v[144:147], a[48:51], 0\n" \
  "s_not_b64 exec, exec\n" \
  "v_cvt_pk_bf16_f32 v212, v192, v193\n" \
  "v_cvt_pk_bf16_f32 v213, v194, v195\n" \
  "v_mfma_f32_16x16x32_bf16 v[184:187], v[152:155], a[56:59], 0\n" \
  "s_waitcnt vmcnt(12)\n" \
  "v_cvt_pk_bf16_f32 v214, v200, v201\n" \
  "v_cvt_pk_bf16_f32 v215, v202, v203\n" \
  "v_mfma_f32_16x16x32_bf16 v[180:183], v[148:151], a[52:55], 0\n" \
  "v_cvt_pk_bf16_f32 v216, v176, v177\n" \
  "v_cvt_pk_bf16_f32 v217, v178, v179\n" \
  "v_mfma_f32_16x16x32_bf16 v[188:191], v[156:159], a[60:63], 0\n" \
  "s_waitcnt vmcnt(10)\n" \
  "v_cvt_pk_bf16_f32 v218, v184, v185\n" \
  "v_cvt_pk_bf16_f32 v219, v186, v187\n" \
  "v_mfma_f32_16x16x32_bf16 v[196:199], v[164:167], a[68:71], 0\n" \
  "s_not_b64 exec, exec\n" \
  "v_cvt_pk_bf16_f32 v216, v180, v181\n" \
  "v_cvt_pk_bf16_f32 v217, v182, v183\n" \
  "v_mfma_f32_16x16x32_bf16 v[204:207], v[172:175], a[76:79], 0\n" \
  "s_waitcnt vmcnt(9)\n" \
  "v_cvt_pk_bf16_f32 v218, v188, v189\n" \
  "v_cvt_pk_bf16_f32 v219, v190, v191\n" \
  "v_mfma_f32_16x16x32_bf16 v[192:195], v[160:163], a[64:67], 0\n" \
  "s_waitcnt vmcnt(8)\n" \
  "v_cvt_pk_bf16_f32 v220, v196, v197\n" \
  "v_cvt_pk_bf16_f32 v221, v198, v199\n" \
  "v_mfma_f32_16x16x32_bf16 v[200:203], v[168:171], a[72:75], 0\n" \
  "v_cvt_pk_bf16_f32 v222, v204, v205\n" \
  "v_cvt_pk_bf16_f32 v223, v206, v207\n" \
  "s_waitcnt vmcnt(7)\n" \
  "s_waitcnt lgkmcnt(3)\n" \
  "v_mfma_f32_16x16x32_bf16 v[240:243], v[224:227], v[16:19], 0\n" \
  "s_not_b64 exec, exec\n" \
  "v_cvt_pk_bf16_f32 v220, v192, v193\n" \
  "v_cvt_pk_bf16_f32 v221, v194, v195\n" \
  "s_waitcnt vmcnt(6)\n" \
  "s_waitcnt lgkmcnt(2)\n" \
  "v_mfma_f32_16x16x32_bf16 v[240:243], v[228:231], v[20:23], v[240:243]\n" \
  "v_cvt_pk_bf16_f32 v222, v200, v201\n" \
  "v_cvt_pk_bf16_f32 v223, v202, v203\n" \
  "s_waitcnt vmcnt(5)\n" \
  "s_waitcnt lgkmcnt(1)\n" \
  "v_mfma_f32_16x16x32_bf16 v[240:243], v[232:235], v[24:27], v[240:243]\n" \
  "s_mov_b64 exec, -1\n" \
  "s_waitcnt vmcnt(4)\n" \
  "s_waitcnt lgkmcnt(0)\n" \
  "v_mfma_f32_16x16x32_bf16 v[240:243], v[236:239], v[28:31], v[240:243]\n" \
  "s_waitcnt vmcnt(3)\n" \
  "v_mfma_f32_16x16x32_bf16 v[240:243], v[208:211], v[32:35], v[240:243]\n" \
  "s_waitcnt vmcnt(2)\n" \
  "v_mfma_f32_16x16x32_bf16 v[240:243], v[212:215], v[36:39], v[240:243]\n" \
  "s_waitcnt vmcnt(1)\n" \
  "v_mfma_f32_16x16x32_bf16 v[240:243], v[216:219], v[40:43], v[240:243]\n" \
  "s_waitcnt vmcnt(0)\n" \
  "v_mfma_f32_16x16x32_bf16 v[240:243], v[220:223], v[44:47], v[240:243]\n" \
  "s_nop 7\n" \
  "v_permlane32_swap_b32 v240, v242\n" \
  "v_permlane32_swap_b32 v241, v243\n" \
  "s_nop 0\n" \
  "v_add_f32_dpp v240, v242, v240 row_ror:8 row_mask:0xf bank_mask:0xf\n" \
  "v_add_f32_dpp v241, v243, v241 row_ror:8 row_mask:0xf bank_mask:0xf\n" \
  "v_cmp_u_f32 vcc, v240, v241\n" \
  "v_cvt_pk_bf16_f32 v240, v240, v241\n" \
  "s_and_b64 vcc, vcc, s[34:35]\n" \
  "s_cbranch_scc1 LPFc3_%=\n" \
  "LPOKc3_%=:\n" \
  "v_mov_b32_dpp v240, v240 row_ror:8 row_mask:0xf bank_mask:0xc\n" \
  "buffer_store_dword v240, v13, s[36:39], s43 offen sc1\n" \
  "buffer_store_dword v240, v2, s[36:39], s43 offen sc1\n" \
  "v_readlane_b32 s74, v98, 3\n" \
  "s_nop 0\n" \
  "s_sub_u32 s75, s74, s51\n" \
  "s_cmp_lt_u32 s75, 16\n" \
  "s_cselect_b32 s51, s74, s51\n" \
  "v_readlane_b32 s74, v98, 0\n" \
  "s_add_u32 s72, s72, s53\n" \
  "s_sub_u32 s75, s74, s72\n" \
  "s_add_u32 s75, s75, 2000\n" \
  "s_cmp_lt_u32 s75, 4000\n" \
  "s_cselect_b32 s72, s74, s72\n" \
  "v_readlane_b32 s74, v98, 1\n" \
  "s_add_u32 s94, s94, s53\n" \
  "s_sub_u32 s75, s74, s94\n" \
  "s_add_u32 s75, s75, 2000\n" \
  "s_cmp_lt_u32 s75, 4000\n" \
  "s_cselect_b32 s94, s74, s94\n" \
  "v_readlane_b32 s74, v98, 2\n" \
  "s_add_u32 s95, s95, s53\n" \
  "s_sub_u32 s75, s74, s95\n" \
  "s_add_u32 s75, s75, 2000\n" \
  "s_cmp_lt_u32 s75, 4000\n" \
  "s_cselect_b32 s95, s74, s95\n" \
  "s_mul_i32 s74, s51, s54\n" \
  "s_add_u32 s74, s74, s72\n" \
  "s_add_u32 s74, s74, s55\n" \
  "s_sub_i32 s75, s74, s48\n" \
  "s_mov_b32 s48, s74\n" \
  "s_mul_i32 s75, s75, s50\n" \
  "s_ashr_i32 s75, s75, 10\n" \
  "s_add_u32 s49, s49, s75\n" \
  "s_mov_b32 s96, 0\n" \
  "s_barrier\n" \
  "s_mov_b32 s44, s45\n" \
  "s_cmp_lg_u32 s38, 0\n" \
  "s_cbranch_scc1 LOOPc3_%=\n" \
  "s_branch LPASSc3_%=\n" \
  "LSEc3_%=:\n" \
  "s_add_i32 s96, s96, 48\n" \
  "s_cmp_gt_i32 s96, 0\n" \
  "s_cselect_b32 s74, s54, 0\n" \
  "s_addc_u32 s51, s51, 0\n" \
  "s_sub_i32 s96, s96, s74\n" \
  "s_mul_i32 s74, s51, s54\n" \
  "s_add_u32 s74, s74, s94\n" \
  "s_add_u32 s74, s74, s96\n" \
  "s_sub_i32 s75, s74, s48\n" \
  "s_mov_b32 s48, s74\n" \
  "s_mul_i32 s75, s75, s50\n" \
  "s_ashr_i32 s75, s75, 10\n" \
  "s_add_u32 s49, s49, s75\n" \
  "s_branch LWEc3_%=\n" \
  "LENDc3_%=:\n" \
  "s_barrier\n" \
  "s_barrier\n" \
  "s_branch LDONE_%=\n" \
  "LPFc3_%=:\n" \
  "s_add_u32 s46, s44, 32768\n" \
  "s_add_u32 s47, s44, 36864\n" \
  "s_add_u32 s69, s44, 49152\n" \
  "s_add_u32 s70, s44, 53248\n" \
  "s_sub_i32 s71, s54, s96\n" \
  "s_sub_i32 s71, s71, 40\n" \
  "v_max_u32 v92, v112, v113\n" \
  "v_max3_u32 v92, v92, v114, v115\n" \
  "v_max3_u32 v92, v92, v116, v117\n" \
  "v_max3_u32 v92, v92, v118, v119\n" \
  "v_max3_u32 v92, v92, v120, v121\n" \
  "v_max3_u32 v92, v92, v122, v123\n" \
  "v_max3_u32 v92, v92, v124, v125\n" \
  "v_max3_u32 v92, v92, v126, v127\n" \
  "v_max3_u32 v92, v92, v128, v129\n" \
  "v_max3_u32 v92, v92, v130, v131\n" \
  "v_max3_u32 v92, v92, v132, v133\n" \
  "v_max3_u32 v92, v92, v134, v135\n" \
  "v_max3_u32 v92, v92, v136, v137\n" \
  "v_max3_u32 v92, v92, v138, v139\n" \
  "v_max3_u32 v92, v92, v140, v141\n" \
  "v_max3_u32 v92, v92, v142, v143\n" \
  "v_max3_u32 v92, v92, v144, v145\n" \
  "v_max3_u32 v92, v92, v146, v147\n" \
  "v_max3_u32 v92, v92, v148, v149\n" \
  "v_max3_u32 v92, v92, v150, v151\n" \
  "v_max3_u32 v92, v92, v152, v153\n" \
  "v_max3_u32 v92, v92, v154, v155\n" \
  "v_max3_u32 v92, v92, v156, v157\n" \
  "v_max3_u32 v92, v92, v158, v159\n" \
  "v_max3_u32 v92, v92, v160, v161\n" \
  "v_max3_u32 v92, v92, v162, v163\n" \
  "v_max3_u32 v92, v92, v164, v165\n" \
  "v_max3_u32 v92, v92, v166, v167\n" \
  "v_max3_u32 v92, v92, v168, v169\n" \
  "v_max3_u32 v92, v92, v170, v171\n" \
  "v_max3_u32 v92, v92, v172, v173\n" \
  "v_max3_u32 v92, v92, v174, v175\n" \
  "v_cmp_eq_u32 vcc, -1, v92\n" \
  "s_nop 3\n" \
  "s_cbranch_vccnz LPRc3_%=\n" \
  "s_mov_b32 s71, 256\n" \
  "s_add_u32 s59, s59, 1\n" \
  "v_max_u32 v92, v16, v17\n" \
  "v_max3_u32 v92, v92, v18, v19\n" \
  "v_max3_u32 v92, v92, v20, v21\n" \
  "v_max3_u32 v92, v92, v22, v23\n" \
  "v_max3_u32 v92, v92, v24, v25\n" \
  "v_max3_u32 v92, v92, v26, v27\n" \
  "v_max3_u32 v92, v92, v28, v29\n" \
  "v_max3_u32 v92, v92, v30, v31\n" \
  "v_max3_u32 v92, v92, v32, v33\n" \
  "v_max3_u32 v92, v92, v34, v35\n" \
  "v_max3_u32 v92, v92, v36, v37\n" \
  "v_max3_u32 v92, v92, v38, v39\n" \
  "v_max3_u32 v92, v92, v40, v41\n" \
  "v_max3_u32 v92, v92, v42, v43\n" \
  "v_max3_u32 v92, v92, v44, v45\n" \
  "v_max3_u32 v92, v92, v46, v47\n" \
  "v_cmp_eq_u32 vcc, -1, v92\n" \
  "s_nop 3\n" \
  "s_cbranch_vccz LPEc3_%=\n" \
  "LPRc3_%=:\n" \
  "s_add_u32 s73, s73, 1\n" \
  "s_cmp_gt_u32 s73, 20000\n" \
  "s_cbranch_scc1 LPEc3_%=\n" \
  "s_add_u32 s58, s58, 1\n" \
  "s_add_i32 s96, s96, s71\n" \
  "s_cmp_gt_i32 s96, 0\n" \
  "s_cselect_b32 s74, s54, 0\n" \
  "s_addc_u32 s51, s51, 0\n" \
  "s_sub_i32 s96, s96, s74\n" \
  "s_mul_i32 s74, s51, s54\n" \
  "s_add_u32 s74, s74, s94\n" \
  "s_add_u32 s74, s74, s96\n" \
  "s_sub_i32 s75, s74, s48\n" \
  "s_mov_b32 s48, s74\n" \
  "s_mul_i32 s75, s75, s50\n" \
  "s_ashr_i32 s75, s75, 10\n" \
  "s_add_u32 s49, s49, s75\n" \
  "s_branch LWEc3_%=\n" \
  "LPEc3_%=:\n" \
  "s_mov_b32 s71, 0\n" \
  "buffer_load_dwordx4 v[112:115], v10, s[36:39], s46 offen offset:0 sc1\n" \
  "buffer_load_dwordx4 v[120:123], v10, s[36:39], s46 offen offset:2048 sc1\n" \
  "buffer_load_dwordx4 v[116:119], v6, s[36:39], s46 offen offset:1024 sc1\n" \
  "buffer_load_dwordx4 v[124:127], v6, s[36:39], s46 offen offset:3072 sc1\n" \
  "buffer_load_dwordx4 v[132:135], v6, s[36:39], s47 offen offset:1024 sc1\n" \
  "buffer_load_dwordx4 v[140:143], v6, s[36:39], s47 offen offset:3072 sc1\n" \
  "buffer_load_dwordx4 v[128:131], v10, s[36:39], s47 offen offset:0 sc1\n" \
  "buffer_load_dwordx4 v[136:139], v10, s[36:39], s47 offen offset:2048 sc1\n" \
  "buffer_load_dwordx4 v[144:147], v10, s[36:39], s69 offen offset:0 sc1\n" \
  "buffer_load_dwordx4 v[152:155], v10, s[36:39], s69 offen offset:2048 sc1\n" \
  "buffer_load_dwordx4 v[148:151], v6, s[36:39], s69 offen offset:1024 sc1\n" \
  "buffer_load_dwordx4 v[156:159], v6, s[36:39], s69 offen offset:3072 sc1\n" \
  "buffer_load_dwordx4 v[164:167], v6, s[36:39], s70 offen offset:1024 sc1\n" \
  "buffer_load_dwordx4 v[172:175], v6, s[36:39], s70 offen offset:3072 sc1\n" \
  "buffer_load_dwordx4 v[160:163], v10, s[36:39], s70 offen offset:0 sc1\n" \
  "buffer_load_dwordx4 v[168:171], v10, s[36:39], s70 offen offset:2048 sc1\n" \
  "s_waitcnt vmcnt(0)\n" \
  "LPGc3_%=:\n" \
  "v_cmp_eq_u32 vcc, 0, v12\n" \
  "s_nop 1\n" \
  "v_cndmask_b32 v32, 0, v32, vcc\n" \
  "s_nop 1\n" \
  "v_cndmask_b32 v36, 0, v36, vcc\n" \
  "s_nop 1\n" \
  "v_cndmask_b32 v40, 0, v40, vcc\n" \
  "s_nop 1\n" \
  "v_cndmask_b32 v44, 0, v44, vcc\n" \
  "v_cmp_eq_u32 vcc, 1, v12\n" \
  "s_nop 1\n" \
  "v_cndmask_b32 v33, 0, v33, vcc\n" \
  "s_nop 1\n" \
  "v_cndmask_b32 v37, 0, v37, vcc\n" \
  "s_nop 1\n" \
  "v_cndmask_b32 v41, 0, v41, vcc\n" \
  "s_nop 1\n" \
  "v_cndmask_b32 v45, 0, v45, vcc\n" \
  "v_cmp_eq_u32 vcc, 0, v12\n" \
  "s_nop 1\n" \
  "v_cndmask_b32 v34, 0, v34, vcc\n" \
  "s_nop 1\n" \
  "v_cndmask_b32 v38, 0, v38, vcc\n" \
  "s_nop 1\n" \
  "v_cndmask_b32 v42, 0, v42, vcc\n" \
  "s_nop 1\n" \
  "v_cndmask_b32 v46, 0, v46, vcc\n" \
  "v_cmp_eq_u32 vcc, 1, v12\n" \
  "s_nop 1\n" \
  "v_cndmask_b32 v35, 0, v35, vcc\n" \
  "s_nop 1\n" \
  "v_cndmask_b32 v39, 0, v39, vcc\n" \
  "s_nop 1\n" \
  "v_cndmask_b32 v43, 0, v43, vcc\n" \
  "s_nop 1\n" \
  "v_cndmask_b32 v47, 0, v47, vcc\n" \
  "v_mfma_f32_16x16x32_bf16 v[176:179], v[112:115], a[8:11], 0\n" \
  "v_mfma_f32_16x16x32_bf16 v[180:183], v[116:119], a[12:15], 0\n" \
  "v_mfma_f32_16x16x32_bf16 v[184:187], v[120:123], a[16:19], 0\n" \
  "v_mfma_f32_16x16x32_bf16 v[188:191], v[124:127], a[20:23], 0\n" \
  "v_mfma_f32_16x16x32_bf16 v[192:195], v[128:131], a[24:27], 0\n" \
  "v_mfma_f32_16x16x32_bf16 v[196:199], v[132:135], a[28:31], 0\n" \
  "v_mfma_f32_16x16x32_bf16 v[200:203], v[136:139], a[32:35], 0\n" \
  "v_mfma_f32_16x16x32_bf16 v[204:207], v[140:143], a[36:39], 0\n" \
  "s_mov_b64 exec, s[40:41]\n" \
  "v_cvt_pk_bf16_f32 v208, v176, v177\n" \
  "v_cvt_pk_bf16_f32 v209, v178, v179\n" \
  "v_cvt_pk_bf16_f32 v210, v184, v185\n" \
  "v_cvt_pk_bf16_f32 v211, v186, v187\n" \
  "v_cvt_pk_bf16_f32 v212, v192, v193\n" \
  "v_cvt_pk_bf16_f32 v213, v194, v195\n" \
  "v_cvt_pk_bf16_f32 v214, v200, v201\n" \
  "v_cvt_pk_bf16_f32 v215, v202, v203\n" \
  "s_not_b64 exec, exec\n" \
  "v_cvt_pk_bf16_f32 v208, v180, v181\n" \
  "v_cvt_pk_bf16_f32 v209, v182, v183\n" \
  "v_cvt_pk_bf16_f32 v210, v188, v189\n" \
  "v_cvt_pk_bf16_f32 v211, v190, v191\n" \
  "v_cvt_pk_bf16_f32 v212, v196, v197\n" \
  "v_cvt_pk_bf16_f32 v213, v198, v199\n" \
  "v_cvt_pk_bf16_f32 v214, v204, v205\n" \
  "v_cvt_pk_bf16_f32 v215, v206, v207\n" \
  "s_mov_b64 exec, -1\n" \
  "v_mfma_f32_16x16x32_bf16 v[176:179], v[144:147], a[48:51], 0\n" \
  "v_mfma_f32_16x16x32_bf16 v[180:183], v[148:151], a[52:55], 0\n" \
  "v_mfma_f32_16x16x32_bf16 v[184:187], v[152:155], a[56:59], 0\n" \
  "v_mfma_f32_16x16x32_bf16 v[188:191], v[156:159], a[60:63], 0\n" \
  "v_mfma_f32_16x16x32_bf16 v[192:195], v[160:163], a[64:67], 0\n" \
  "v_mfma_f32_16x16x32_bf16 v[196:199], v[164:167], a[68:71], 0\n" \
  "v_mfma_f32_16x16x32_bf16 v[200:203], v[168:171], a[72:75], 0\n" \
  "v_mfma_f32_16x16x32_bf16 v[204:207], v[172:175], a[76:79], 0\n" \
  "s_mov_b64 exec, s[40:41]\n" \
  "v_cvt_pk_bf16_f32 v216, v176, v177\n" \
  "v_cvt_pk_bf16_f32 v217, v178, v179\n" \
  "v_cvt_pk_bf16_f32 v218, v184, v185\n" \
  "v_cvt_pk_bf16_f32 v219, v186, v187\n" \
  "v_cvt_pk_bf16_f32 v220, v192, v193\n" \
  "v_cvt_pk_bf16_f32 v221, v194, v195\n" \
  "v_cvt_pk_bf16_f32 v222, v200, v201\n" \
  "v_cvt_pk_bf16_f32 v223, v202, v203\n" \
  "s_not_b64 exec, exec\n" \
  "v_cvt_pk_bf16_f32 v216, v180, v181\n" \
  "v_cvt_pk_bf16_f32 v217, v182, v183\n" \
  "v_cvt_pk_bf16_f32 v218, v188, v189\n" \
  "v_cvt_pk_bf16_f32 v219, v190, v191\n" \
  "v_cvt_pk_bf16_f32 v220, v196, v197\n" \
  "v_cvt_pk_bf16_f32 v221, v198, v199\n" \
  "v_cvt_pk_bf16_f32 v222, v204, v205\n" \
  "v_cvt_pk_bf16_f32 v223, v206, v207\n" \
  "s_mov_b64 exec, -1\n" \
  "v_mfma_f32_16x16x32_bf16 v[240:243], v[224:227], v[16:19], 0\n" \
  "v_mfma_f32_16x16x32_bf16 v[240:243], v[228:231], v[20:23], v[240:243]\n" \
  "v_mfma_f32_16x16x32_bf16 v[240:243], v[232:235], v[24:27], v[240:243]\n" \
  "v_mfma_f32_16x16x32_bf16 v[240:243], v[236:239], v[28:31], v[240:243]\n" \
  "v_mfma_f32_16x16x32_bf16 v[240:243], v[208:211], v[32:35], v[240:243]\n" \
  "v_mfma_f32_16x16x32_bf16 v[240:243], v[212:215], v[36:39], v[240:243]\n" \
  "v_mfma_f32_16x16x32_bf16 v[240:243], v[216:219], v[40:43], v[240:243]\n" \
  "v_mfma_f32_16x16x32_bf16 v[240:243], v[220:223], v[44:47], v[240:243]\n" \
  "s_nop 7\n" \
  "v_permlane32_swap_b32 v240, v242\n" \
  "v_permlane32_swap_b32 v241, v243\n" \
  "s_nop 0\n" \
  "v_add_f32_dpp v240, v242, v240 row_ror:8 row_mask:0xf bank_mask:0xf\n" \
  "v_add_f32_dpp v241, v243, v241 row_ror:8 row_mask:0xf bank_mask:0xf\n" \
  "v_cvt_pk_bf16_f32 v240, v240, v241\n" \
  "v_pk_min_u16 v240, v240, -2 op_sel_hi:[1,0]\n" \
  "s_nop 1\n" \
  "s_branch LPOKc3_%=\n" \
  "LMFc3_%=:\n" \
  "v_max_u32 v92, v16, v17\n" \
  "v_max3_u32 v92, v92, v18, v19\n" \
  "v_max3_u32 v92, v92, v20, v21\n" \
  "v_max3_u32 v92, v92, v22, v23\n" \
  "v_max3_u32 v92, v92, v24, v25\n" \
  "v_max3_u32 v92, v92, v26, v27\n" \
  "v_max3_u32 v92, v92, v28, v29\n" \
  "v_max3_u32 v92, v92, v30, v31\n" \
  "v_max3_u32 v92, v92, v32, v33\n" \
  "v_max3_u32 v92, v92, v34, v35\n" \
  "v_max3_u32 v92, v92, v36, v37\n" \
  "v_max3_u32 v92, v92, v38, v39\n" \
  "v_max3_u32 v92, v92, v40, v41\n" \
  "v_max3_u32 v92, v92, v42, v43\n" \
  "v_max3_u32 v92, v92, v44, v45\n" \
  "v_max3_u32 v92, v92, v46, v47\n" \
  "v_max3_u32 v92, v92, v48, v49\n" \
  "v_max3_u32 v92, v92, v50, v51\n" \
  "v_max3_u32 v92, v92, v52, v53\n" \
  "v_max3_u32 v92, v92, v54, v55\n" \
  "v_cmp_eq_u32 vcc, -1, v92\n" \
  "s_nop 3\n" \
  "s_cbranch_vccz LMOKc3_%=\n" \
  "s_cmp_gt_u32 s57, 20000\n" \
  "s_cbranch_scc1 LMOKc3_%=\n" \
  "s_add_u32 s57, s57, 1\n" \
  "LHMc3_%=:\n" \
  "s_add_u32 s48, s48, s54\n" \
  "s_add_u32 s49, s49, s86\n" \
  "s_add_u32 s51, s51, 1\n" \
  "LGMc3_%=:\n" \
  "s_memrealtime s[76:77]\n" \
  "s_memtime s[78:79]\n" \
  "s_waitcnt lgkmcnt(0)\n" \
  "s_lshl_b32 s75, s76, 2\n" \
  "s_sub_i32 s75, s48, s75\n" \
  "s_mul_i32 s75, s75, s50\n" \
  "s_ashr_i32 s75, s75, 10\n" \
  "s_sub_i32 s74, s49, s78\n" \
  "s_min_i32 s74, s74, s75\n" \
  "s_sub_i32 s75, s75, 92\n" \
  "s_max_i32 s74, s74, s75\n" \
  "s_add_u32 s49, s74, s78\n" \
  "s_cmp_gt_i32 s74, 60\n" \
  "s_cbranch_scc1 LGMc3_%=\n" \
  "s_cmp_lt_i32 s74, s87\n" \
  "s_cbranch_scc1 LHMc3_%=\n" \
  "s_barrier\n" \
  "v_mov_b32 v56, 0\n" \
  "v_mov_b32 v57, 0\n" \
  "v_mov_b32 v58, 0\n" \
  "v_mov_b32 v59, 0\n" \
  "s_waitcnt vmcnt(0)\n" \
  "s_barrier\n" \
  "s_branch LMc3_%=\n" \
  "LDONE_%=:\n" \
  "s_waitcnt vmcnt(0) lgkmcnt(0)\n" \
  "s_mov_b32 s53, s73\n" \
  "s_mov_b32 s54, s51\n" \
  "s_mov_b32 s55, s50\n" \
  "LXIT_%=:\n"
#define PROJ_CLOBBERS \
  "v12", "v13", "v14", "v15", "v16", "v17", "v18", "v19", "v20", "v21", "v22", "v23", "v24", "v25", "v26", "v27", "v28", "v29", "v30", "v31", "v32", \
  "v33", "v34", "v35", "v36", "v37", "v38", "v39", "v40", "v41", "v42", "v43", "v44", "v45", "v46", "v47", "v48", "v49", "v50", "v51", "v52", "v53", \
  "v54", "v55", "v56", "v57", "v58", "v59", "v60", "v61", "v62", "v63", "v64", "v65", "v66", "v67", "v68", "v69", "v70", "v71", "v72", "v73", "v74", \
  "v75", "v76", "v77", "v78", "v79", "v100", "v101", "v102", "v103", "v104", "v105", "v106", "v107", "v108", "v109", "v110", "v111", "v112", "v113", \
  "v114", "v115", "v116", "v117", "v118", "v119", "v120", "v121", "v122", "v123", "s68", "s69", "s70", "s71", "s72", "s73", "s74", "s75", "s76", "s77"
#define PROJ_ASM \
  "s_getpc_b64 s[74:75]\n" \
  "LCF3_%=:\n" \
  "s_add_u32 s74, s74, LPL_%=-LCF3_%=\n" \
  "s_addc_u32 s75, s75, 0\n" \
  "v_mbcnt_lo_u32_b32 v12, -1, 0\n" \
  "v_mbcnt_hi_u32_b32 v12, -1, v12\n" \
  "v_lshlrev_b32 v12, 6, v12\n" \
  "global_load_dword v13, v12, s[74:75]\n" \
  "v_add_u32 v12, 4096, v12\n" \
  "global_load_dword v13, v12, s[74:75]\n" \
  "s_getpc_b64 s[74:75]\n" \
  "LCF4_%=:\n" \
  "s_add_u32 s74, s74, LPE_%=-4096-LCF4_%=\n" \
  "s_addc_u32 s75, s75, 0\n" \
  "v_mbcnt_lo_u32_b32 v12, -1, 0\n" \
  "v_mbcnt_hi_u32_b32 v12, -1, v12\n" \
  "v_lshlrev_b32 v12, 6, v12\n" \
  "global_load_dword v13, v12, s[74:75]\n" \
  "s_waitcnt vmcnt(0)\n" \
  "s_barrier\n" \
  "ds_read_b128 v[32:35], v0 offset:0\n" \
  "ds_read_b128 v[36:39], v0 offset:20480\n" \
  "ds_read_b128 v[40:43], v0 offset:1024\n" \
  "ds_read_b128 v[44:47], v0 offset:21504\n" \
  "v_add_u32 v9, 40960, v0\n" \
  "v_add_u32 v10, 81920, v0\n" \
  "s_mul_i32 s68, s52, 40960\n" \
  "s_add_u32 s68, s68, 81920\n" \
  "s_mov_b32 s69, 0x40000000\n" \
  "s_mov_b32 s71, 0x40000000\n" \
  "v_mov_b32 v12, -1\n" \
  "v_mov_b32 v13, -1\n" \
  "v_mov_b32 v14, -1\n" \
  "v_mov_b32 v15, -1\n" \
  "LPL_%=:\n" \
  "s_waitcnt lgkmcnt(2)\n" \
  "v_mfma_f32_16x16x32_bf16 v[56:59], a[0:3], v[32:35], 0\n" \
  "ds_read_b128 v[48:51], v0 offset:2048\n" \
  "v_mfma_f32_16x16x32_bf16 v[68:71], a[0:3], v[36:39], 0\n" \
  "ds_read_b128 v[52:55], v0 offset:22528\n" \
  "v_mfma_f32_16x16x32_bf16 v[60:63], a[80:83], v[32:35], 0\n" \
  "buffer_store_dwordx4 v[12:15], v7, s[40:43], s71 offen sc1\n" \
  "v_mfma_f32_16x16x32_bf16 v[72:75], a[80:83], v[36:39], 0\n" \
  "v_cvt_pk_bf16_f32 v16, v100, v101\n" \
  "v_mfma_f32_16x16x32_bf16 v[64:67], a[160:163], v[32:35], 0\n" \
  "v_cvt_pk_bf16_f32 v17, v102, v103\n" \
  "v_mfma_f32_16x16x32_bf16 v[76:79], a[160:163], v[36:39], 0\n" \
  "v_cvt_pk_bf16_f32 v18, v104, v105\n" \
  "s_waitcnt lgkmcnt(2)\n" \
  "v_mfma_f32_16x16x32_bf16 v[56:59], a[4:7], v[40:43], v[56:59]\n" \
  "ds_read_b128 v[32:35], v0 offset:3072\n" \
  "v_mfma_f32_16x16x32_bf16 v[68:71], a[4:7], v[44:47], v[68:71]\n" \
  "ds_read_b128 v[36:39], v0 offset:23552\n" \
  "v_mfma_f32_16x16x32_bf16 v[60:63], a[84:87], v[40:43], v[60:63]\n" \
  "v_cvt_pk_bf16_f32 v19, v106, v107\n" \
  "v_mfma_f32_16x16x32_bf16 v[72:75], a[84:87], v[44:47], v[72:75]\n" \
  "buffer_store_dwordx4 v[16:19], v2, s[40:43], s69 offen sc1\n" \
  "v_mfma_f32_16x16x32_bf16 v[64:67], a[164:167], v[40:43], v[64:67]\n" \
  "v_cvt_pk_bf16_f32 v20, v112, v113\n" \
  "v_mfma_f32_16x16x32_bf16 v[76:79], a[164:167], v[44:47], v[76:79]\n" \
  "v_cvt_pk_bf16_f32 v21, v114, v115\n" \
  "s_waitcnt lgkmcnt(2)\n" \
  "v_mfma_f32_16x16x32_bf16 v[56:59], a[8:11], v[48:51], v[56:59]\n" \
  "ds_read_b128 v[40:43], v0 offset:4096\n" \
  "v_mfma_f32_16x16x32_bf16 v[68:71], a[8:11], v[52:55], v[68:71]\n" \
  "ds_read_b128 v[44:47], v0 offset:24576\n" \
  "v_mfma_f32_16x16x32_bf16 v[60:63], a[88:91], v[48:51], v[60:63]\n" \
  "v_cvt_pk_bf16_f32 v22, v116, v117\n" \
  "v_mfma_f32_16x16x32_bf16 v[72:75], a[88:91], v[52:55], v[72:75]\n" \
  "v_cvt_pk_bf16_f32 v23, v118, v119\n" \
  "v_mfma_f32_16x16x32_bf16 v[64:67], a[168:171], v[48:51], v[64:67]\n" \
  "buffer_store_dwordx4 v[20:23], v2, s[40:43], s69 offen offset:1024 sc1\n" \
  "v_mfma_f32_16x16x32_bf16 v[76:79], a[168:171], v[52:55], v[76:79]\n" \
  "v_cvt_pk_bf16_f32 v24, v108, v109\n" \
  "s_waitcnt lgkmcnt(2)\n" \
  "v_mfma_f32_16x16x32_bf16 v[56:59], a[12:15], v[32:35], v[56:59]\n" \
  "ds_read_b128 v[48:51], v0 offset:5120\n" \
  "v_mfma_f32_16x16x32_bf16 v[68:71], a[12:15], v[36:39], v[68:71]\n" \
  "ds_read_b128 v[52:55], v0 offset:25600\n" \
  "v_mfma_f32_16x16x32_bf16 v[60:63], a[92:95], v[32:35], v[60:63]\n" \
  "v_cvt_pk_bf16_f32 v25, v110, v111\n" \
  "v_mfma_f32_16x16x32_bf16 v[72:75], a[92:95], v[36:39], v[72:75]\n" \
  "v_cvt_pk_bf16_f32 v26, v120, v121\n" \
  "v_mfma_f32_16x16x32_bf16 v[64:67], a[172:175], v[32:35], v[64:67]\n" \
  "v_cvt_pk_bf16_f32 v27, v122, v123\n" \
  "v_mfma_f32_16x16x32_bf16 v[76:79], a[172:175], v[36:39], v[76:79]\n" \
  "buffer_store_dwordx4 v[24:27], v3, s[40:43], s69 offen sc1\n" \
  "s_waitcnt lgkmcnt(2)\n" \
  "v_mfma_f32_16x16x32_bf16 v[56:59], a[16:19], v[40:43], v[56:59]\n" \
  "ds_read_b128 v[32:35], v0 offset:6144\n" \
  "v_mfma_f32_16x16x32_bf16 v[68:71], a[16:19], v[44:47], v[68:71]\n" \
  "ds_read_b128 v[36:39], v0 offset:26624\n" \
  "v_mfma_f32_16x16x32_bf16 v[60:63], a[96:99], v[40:43], v[60:63]\n" \
  "s_add_u32 s72, s68, 4096\n" \
  "v_mfma_f32_16x16x32_bf16 v[72:75], a[96:99], v[44:47], v[72:75]\n" \
  "s_add_u32 s73, s68, 8192\n" \
  "v_mfma_f32_16x16x32_bf16 v[64:67], a[176:179], v[40:43], v[64:67]\n" \
  "s_add_u32 m0, s54, 81920\n" \
  "v_mfma_f32_16x16x32_bf16 v[76:79], a[176:179], v[44:47], v[76:79]\n" \
  "buffer_load_dwordx4 v1, s[36:39], s68 offen offset:0 sc1 lds\n" \
  "s_waitcnt lgkmcnt(2)\n" \
  "v_mfma_f32_16x16x32_bf16 v[56:59], a[20:23], v[48:51], v[56:59]\n" \
  "ds_read_b128 v[40:43], v0 offset:7168\n" \
  "v_mfma_f32_16x16x32_bf16 v[68:71], a[20:23], v[52:55], v[68:71]\n" \
  "ds_read_b128 v[44:47], v0 offset:27648\n" \
  "v_mfma_f32_16x16x32_bf16 v[60:63], a[100:103], v[48:51], v[60:63]\n" \
  "buffer_load_dwordx4 v1, s[36:39], s68 offen offset:1024 sc1 lds\n" \
  "v_mfma_f32_16x16x32_bf16 v[72:75], a[100:103], v[52:55], v[72:75]\n" \
  "v_mfma_f32_16x16x32_bf16 v[64:67], a[180:183], v[48:51], v[64:67]\n" \
  "v_mfma_f32_16x16x32_bf16 v[76:79], a[180:183], v[52:55], v[76:79]\n" \
  "s_waitcnt lgkmcnt(2)\n" \
  "v_mfma_f32_16x16x32_bf16 v[56:59], a[24:27], v[32:35], v[56:59]\n" \
  "ds_read_b128 v[48:51], v0 offset:8192\n" \
  "v_mfma_f32_16x16x32_bf16 v[68:71], a[24:27], v[36:39], v[68:71]\n" \
  "ds_read_b128 v[52:55], v0 offset:28672\n" \
  "v_mfma_f32_16x16x32_bf16 v[60:63], a[104:107], v[32:35], v[60:63]\n" \
  "buffer_load_dwordx4 v1, s[36:39], s68 offen offset:2048 sc1 lds\n" \
  "v_mfma_f32_16x16x32_bf16 v[72:75], a[104:107], v[36:39], v[72:75]\n" \
  "v_mfma_f32_16x16x32_bf16 v[64:67], a[184:187], v[32:35], v[64:67]\n" \
  "v_mfma_f32_16x16x32_bf16 v[76:79], a[184:187], v[36:39], v[76:79]\n" \
  "s_waitcnt lgkmcnt(2)\n" \
  "v_mfma_f32_16x16x32_bf16 v[56:59], a[28:31], v[40:43], v[56:59]\n" \
  "ds_read_b128 v[32:35], v0 offset:9216\n" \
  "v_mfma_f32_16x16x32_bf16 v[68:71], a[28:31], v[44:47], v[68:71]\n" \
  "ds_read_b128 v[36:39], v0 offset:29696\n" \
  "v_mfma_f32_16x16x32_bf16 v[60:63], a[108:111], v[40:43], v[60:63]\n" \
  "buffer_load_dwordx4 v1, s[36:39], s68 offen offset:3072 sc1 lds\n" \
  "v_mfma_f32_16x16x32_bf16 v[72:75], a[108:111], v[44:47], v[72:75]\n" \
  "v_mfma_f32_16x16x32_bf16 v[64:67], a[188:191], v[40:43], v[64:67]\n" \
  "v_mfma_f32_16x16x32_bf16 v[76:79], a[188:191], v[44:47], v[76:79]\n" \
  "s_waitcnt lgkmcnt(2)\n" \
  "v_mfma_f32_16x16x32_bf16 v[56:59], a[32:35], v[48:51], v[56:59]\n" \
  "ds_read_b128 v[40:43], v0 offset:10240\n" \
  "v_mfma_f32_16x16x32_bf16 v[68:71], a[32:35], v[52:55], v[68:71]\n" \
  "ds_read_b128 v[44:47], v0 offset:30720\n" \
  "v_mfma_f32_16x16x32_bf16 v[60:63], a[112:115], v[48:51], v[60:63]\n" \
  "s_add_u32 m0, s54, 86016\n" \
  "v_mfma_f32_16x16x32_bf16 v[72:75], a[112:115], v[52:55], v[72:75]\n" \
  "buffer_load_dwordx4 v1, s[36:39], s72 offen offset:0 sc1 lds\n" \
  "v_mfma_f32_16x16x32_bf16 v[64:67], a[192:195], v[48:51], v[64:67]\n" \
  "v_mfma_f32_16x16x32_bf16 v[76:79], a[192:195], v[52:55], v[76:79]\n" \
  "s_waitcnt lgkmcnt(2)\n" \
  "v_mfma_f32_16x16x32_bf16 v[56:59], a[36:39], v[32:35], v[56:59]\n" \
  "ds_read_b128 v[48:51], v0 offset:11264\n" \
  "v_mfma_f32_16x16x32_bf16 v[68:71], a[36:39], v[36:39], v[68:71]\n" \
  "ds_read_b128 v[52:55], v0 offset:31744\n" \
  "v_mfma_f32_16x16x32_bf16 v[60:63], a[116:119], v[32:35], v[60:63]\n" \
  "buffer_load_dwordx4 v1, s[36:39], s72 offen offset:1024 sc1 lds\n" \
  "v_mfma_f32_16x16x32_bf16 v[72:75], a[116:119], v[36:39], v[72:75]\n" \
  "v_mfma_f32_16x16x32_bf16 v[64:67], a[196:199], v[32:35], v[64:67]\n" \
  "v_mfma_f32_16x16x32_bf16 v[76:79], a[196:199], v[36:39], v[76:79]\n" \
  "s_waitcnt lgkmcnt(2)\n" \
  "v_mfma_f32_16x16x32_bf16 v[56:59], a[40:43], v[40:43], v[56:59]\n" \
  "ds_read_b128 v[32:35], v0 offset:12288\n" \
  "v_mfma_f32_16x16x32_bf16 v[68:71], a[40:43], v[44:47], v[68:71]\n" \
  "ds_read_b128 v[36:39], v0 offset:32768\n" \
  "v_mfma_f32_16x16x32_bf16 v[60:63], a[120:123], v[40:43], v[60:63]\n" \
  "buffer_load_dwordx4 v1, s[36:39], s72 offen offset:2048 sc1 lds\n" \
  "v_mfma_f32_16x16x32_bf16 v[72:75], a[120:123], v[44:47], v[72:75]\n" \
  "v_mfma_f32_16x16x32_bf16 v[64:67], a[200:203], v[40:43], v[64:67]\n" \
  "v_mfma_f32_16x16x32_bf16 v[76:79], a[200:203], v[44:47], v[76:79]\n" \
  "s_waitcnt lgkmcnt(2)\n" \
  "v_mfma_f32_16x16x32_bf16 v[56:59], a[44:47], v[48:51], v[56:59]\n" \
  "ds_read_b128 v[40:43], v0 offset:13312\n" \
  "v_mfma_f32_16x16x32_bf16 v[68:71], a[44:47], v[52:55], v[68:71]\n" \
  "ds_read_b128 v[44:47], v0 offset:33792\n" \
  "v_mfma_f32_16x16x32_bf16 v[60:63], a[124:127], v[48:51], v[60:63]\n" \
  "buffer_load_dwordx4 v1, s[36:39], s72 offen offset:3072 sc1 lds\n" \
  "v_mfma_f32_16x16x32_bf16 v[72:75], a[124:127], v[52:55], v[72:75]\n" \
  "v_mfma_f32_16x16x32_bf16 v[64:67], a[204:207], v[48:51], v[64:67]\n" \
  "v_mfma_f32_16x16x32_bf16 v[76:79], a[204:207], v[52:55], v[76:79]\n" \
  "s_waitcnt lgkmcnt(2)\n" \
  "v_mfma_f32_16x16x32_bf16 v[56:59], a[48:51], v[32:35], v[56:59]\n" \
  "ds_read_b128 v[48:51], v0 offset:14336\n" \
  "v_mfma_f32_16x16x32_bf16 v[68:71], a[48:51], v[36:39], v[68:71]\n" \
  "ds_read_b128 v[52:55], v0 offset:34816\n" \
  "v_mfma_f32_16x16x32_bf16 v[60:63], a[128:131], v[32:35], v[60:63]\n" \
  "s_add_u32 m0, s54, 90112\n" \
  "v_mfma_f32_16x16x32_bf16 v[72:75], a[128:131], v[36:39], v[72:75]\n" \
  "buffer_load_dwordx4 v1, s[36:39], s73 offen offset:0 sc1 lds\n" \
  "v_mfma_f32_16x16x32_bf16 v[64:67], a[208:211], v[32:35], v[64:67]\n" \
  "v_mfma_f32_16x16x32_bf16 v[76:79], a[208:211], v[36:39], v[76:79]\n" \
  "s_waitcnt lgkmcnt(2)\n" \
  "v_mfma_f32_16x16x32_bf16 v[56:59], a[52:55], v[40:43], v[56:59]\n" \
  "ds_read_b128 v[32:35], v0 offset:15360\n" \
  "v_mfma_f32_16x16x32_bf16 v[68:71], a[52:55], v[44:47], v[68:71]\n" \
  "ds_read_b128 v[36:39], v0 offset:35840\n" \
  "v_mfma_f32_16x16x32_bf16 v[60:63], a[132:135], v[40:43], v[60:63]\n" \
  "buffer_load_dwordx4 v1, s[36:39], s73 offen offset:1024 sc1 lds\n" \
  "v_mfma_f32_16x16x32_bf16 v[72:75], a[132:135], v[44:47], v[72:75]\n" \
  "v_mfma_f32_16x16x32_bf16 v[64:67], a[212:215], v[40:43], v[64:67]\n" \
  "v_mfma_f32_16x16x32_bf16 v[76:79], a[212:215], v[44:47], v[76:79]\n" \
  "s_waitcnt lgkmcnt(2)\n" \
  "v_mfma_f32_16x16x32_bf16 v[56:59], a[56:59], v[48:51], v[56:59]\n" \
  "ds_read_b128 v[40:43], v0 offset:16384\n" \
  "v_mfma_f32_16x16x32_bf16 v[68:71], a[56:59], v[52:55], v[68:71]\n" \
  "ds_read_b128 v[44:47], v0 offset:36864\n" \
  "v_mfma_f32_16x16x32_bf16 v[60:63], a[136:139], v[48:51], v[60:63]\n" \
  "s_add_u32 s68, s68, 40960\n" \
  "v_mfma_f32_16x16x32_bf16 v[72:75], a[136:139], v[52:55], v[72:75]\n" \
  "v_mfma_f32_16x16x32_bf16 v[64:67], a[216:219], v[48:51], v[64:67]\n" \
  "v_mfma_f32_16x16x32_bf16 v[76:79], a[216:219], v[52:55], v[76:79]\n" \
  "s_waitcnt lgkmcnt(2)\n" \
  "v_mfma_f32_16x16x32_bf16 v[56:59], a[60:63], v[32:35], v[56:59]\n" \
  "ds_read_b128 v[48:51], v0 offset:17408\n" \
  "v_mfma_f32_16x16x32_bf16 v[68:71], a[60:63], v[36:39], v[68:71]\n" \
  "ds_read_b128 v[52:55], v0 offset:37888\n" \
  "v_mfma_f32_16x16x32_bf16 v[60:63], a[140:143], v[32:35], v[60:63]\n" \
  "v_mov_b32 v11, s52\n" \
  "v_mfma_f32_16x16x32_bf16 v[72:75], a[140:143], v[36:39], v[72:75]\n" \
  "s_waitcnt vmcnt(10)\n" \
  "v_mfma_f32_16x16x32_bf16 v[64:67], a[220:223], v[32:35], v[64:67]\n" \
  "buffer_store_dword v11, v5, s[40:43], 0 offen sc1\n" \
  "v_mfma_f32_16x16x32_bf16 v[76:79], a[220:223], v[36:39], v[76:79]\n" \
  "s_waitcnt lgkmcnt(2)\n" \
  "v_mfma_f32_16x16x32_bf16 v[56:59], a[64:67], v[40:43], v[56:59]\n" \
  "ds_read_b128 v[32:35], v0 offset:18432\n" \
  "v_mfma_f32_16x16x32_bf16 v[68:71], a[64:67], v[44:47], v[68:71]\n" \
  "ds_read_b128 v[36:39], v0 offset:38912\n" \
  "v_mfma_f32_16x16x32_bf16 v[60:63], a[144:147], v[40:43], v[60:63]\n" \
  "v_mfma_f32_16x16x32_bf16 v[72:75], a[144:147], v[44:47], v[72:75]\n" \
  "v_mfma_f32_16x16x32_bf16 v[64:67], a[224:227], v[40:43], v[64:67]\n" \
  "v_mfma_f32_16x16x32_bf16 v[76:79], a[224:227], v[44:47], v[76:79]\n" \
  "s_waitcnt lgkmcnt(2)\n" \
  "v_mfma_f32_16x16x32_bf16 v[56:59], a[68:71], v[48:51], v[56:59]\n" \
  "ds_read_b128 v[40:43], v0 offset:19456\n" \
  "v_mfma_f32_16x16x32_bf16 v[68:71], a[68:71], v[52:55], v[68:71]\n" \
  "ds_read_b128 v[44:47], v0 offset:39936\n" \
  "v_mfma_f32_16x16x32_bf16 v[60:63], a[148:151], v[48:51], v[60:63]\n" \
  "v_readlane_b32 s69, v4, s52\n" \
  "v_mfma_f32_16x16x32_bf16 v[72:75], a[148:151], v[52:55], v[72:75]\n" \
  "v_readlane_b32 s71, v8, s52\n" \
  "v_mfma_f32_16x16x32_bf16 v[64:67], a[228:231], v[48:51], v[64:67]\n" \
  "s_add_u32 s52, s52, 1\n" \
  "v_mfma_f32_16x16x32_bf16 v[76:79], a[228:231], v[52:55], v[76:79]\n" \
  "s_waitcnt lgkmcnt(0)\n" \
  "s_barrier\n" \
  "v_mfma_f32_16x16x32_bf16 v[56:59], a[72:75], v[32:35], v[56:59]\n" \
  "ds_read_b128 v[48:51], v9 offset:0\n" \
  "v_mfma_f32_16x16x32_bf16 v[68:71], a[72:75], v[36:39], v[68:71]\n" \
  "ds_read_b128 v[52:55], v9 offset:20480\n" \
  "v_mfma_f32_16x16x32_bf16 v[60:63], a[152:155], v[32:35], v[60:63]\n" \
  "v_mfma_f32_16x16x32_bf16 v[72:75], a[152:155], v[36:39], v[72:75]\n" \
  "v_mfma_f32_16x16x32_bf16 v[64:67], a[232:235], v[32:35], v[64:67]\n" \
  "v_mfma_f32_16x16x32_bf16 v[76:79], a[232:235], v[36:39], v[76:79]\n" \
  "v_mfma_f32_16x16x32_bf16 v[56:59], a[76:79], v[40:43], v[56:59]\n" \
  "ds_read_b128 v[32:35], v9 offset:1024\n" \
  "v_mfma_f32_16x16x32_bf16 v[68:71], a[76:79], v[44:47], v[68:71]\n" \
  "ds_read_b128 v[36:39], v9 offset:21504\n" \
  "v_mfma_f32_16x16x32_bf16 v[60:63], a[156:159], v[40:43], v[60:63]\n" \
  "s_cmp_eq_u32 s52, s53\n" \
  "v_mfma_f32_16x16x32_bf16 v[72:75], a[156:159], v[44:47], v[72:75]\n" \
  "v_mfma_f32_16x16x32_bf16 v[64:67], a[236:239], v[40:43], v[64:67]\n" \
  "v_mfma_f32_16x16x32_bf16 v[76:79], a[236:239], v[44:47], v[76:79]\n" \
  "s_waitcnt lgkmcnt(2)\n" \
  "v_mfma_f32_16x16x32_bf16 v[100:103], a[0:3], v[48:51], 0\n" \
  "ds_read_b128 v[40:43], v9 offset:2048\n" \
  "v_mfma_f32_16x16x32_bf16 v[112:115], a[0:3], v[52:55], 0\n" \
  "ds_read_b128 v[44:47], v9 offset:22528\n" \
  "v_mfma_f32_16x16x32_bf16 v[104:107], a[80:83], v[48:51], 0\n" \
  "buffer_store_dwordx4 v[12:15], v7, s[40:43], s71 offen sc1\n" \
  "v_mfma_f32_16x16x32_bf16 v[116:119], a[80:83], v[52:55], 0\n" \
  "v_cvt_pk_bf16_f32 v16, v56, v57\n" \
  "v_mfma_f32_16x16x32_bf16 v[108:111], a[160:163], v[48:51], 0\n" \
  "v_cvt_pk_bf16_f32 v17, v58, v59\n" \
  "v_mfma_f32_16x16x32_bf16 v[120:123], a[160:163], v[52:55], 0\n" \
  "v_cvt_pk_bf16_f32 v18, v60, v61\n" \
  "s_waitcnt lgkmcnt(2)\n" \
  "v_mfma_f32_16x16x32_bf16 v[100:103], a[4:7], v[32:35], v[100:103]\n" \
  "ds_read_b128 v[48:51], v9 offset:3072\n" \
  "v_mfma_f32_16x16x32_bf16 v[112:115], a[4:7], v[36:39], v[112:115]\n" \
  "ds_read_b128 v[52:55], v9 offset:23552\n" \
  "v_mfma_f32_16x16x32_bf16 v[104:107], a[84:87], v[32:35], v[104:107]\n" \
  "v_cvt_pk_bf16_f32 v19, v62, v63\n" \
  "v_mfma_f32_16x16x32_bf16 v[116:119], a[84:87], v[36:39], v[116:119]\n" \
  "buffer_store_dwordx4 v[16:19], v2, s[40:43], s69 offen sc1\n" \
  "v_mfma_f32_16x16x32_bf16 v[108:111], a[164:167], v[32:35], v[108:111]\n" \
  "v_cvt_pk_bf16_f32 v20, v68, v69\n" \
  "v_mfma_f32_16x16x32_bf16 v[120:123], a[164:167], v[36:39], v[120:123]\n" \
  "v_cvt_pk_bf16_f32 v21, v70, v71\n" \
  "s_waitcnt lgkmcnt(2)\n" \
  "v_mfma_f32_16x16x32_bf16 v[100:103], a[8:11], v[40:43], v[100:103]\n" \
  "ds_read_b128 v[32:35], v9 offset:4096\n" \
  "v_mfma_f32_16x16x32_bf16 v[112:115], a[8:11], v[44:47], v[112:115]\n" \
  "ds_read_b128 v[36:39], v9 offset:24576\n" \
  "v_mfma_f32_16x16x32_bf16 v[104:107], a[88:91], v[40:43], v[104:107]\n" \
  "v_cvt_pk_bf16_f32 v22, v72, v73\n" \
  "v_mfma_f32_16x16x32_bf16 v[116:119], a[88:91], v[44:47], v[116:119]\n" \
  "v_cvt_pk_bf16_f32 v23, v74, v75\n" \
  "v_mfma_f32_16x16x32_bf16 v[108:111], a[168:171], v[40:43], v[108:111]\n" \
  "buffer_store_dwordx4 v[20:23], v2, s[40:43], s69 offen offset:1024 sc1\n" \
  "v_mfma_f32_16x16x32_bf16 v[120:123], a[168:171], v[44:47], v[120:123]\n" \
  "v_cvt_pk_bf16_f32 v24, v64, v65\n" \
  "s_waitcnt lgkmcnt(2)\n" \
  "v_mfma_f32_16x16x32_bf16 v[100:103], a[12:15], v[48:51], v[100:103]\n" \
  "ds_read_b128 v[40:43], v9 offset:5120\n" \
  "v_mfma_f32_16x16x32_bf16 v[112:115], a[12:15], v[52:55], v[112:115]\n" \
  "ds_read_b128 v[44:47], v9 offset:25600\n" \
  "v_mfma_f32_16x16x32_bf16 v[104:107], a[92:95], v[48:51], v[104:107]\n" \
  "v_cvt_pk_bf16_f32 v25, v66, v67\n" \
  "v_mfma_f32_16x16x32_bf16 v[116:119], a[92:95], v[52:55], v[116:119]\n" \
  "v_cvt_pk_bf16_f32 v26, v76, v77\n" \
  "v_mfma_f32_16x16x32_bf16 v[108:111], a[172:175], v[48:51], v[108:111]\n" \
  "v_cvt_pk_bf16_f32 v27, v78, v79\n" \
  "v_mfma_f32_16x16x32_bf16 v[120:123], a[172:175], v[52:55], v[120:123]\n" \
  "buffer_store_dwordx4 v[24:27], v3, s[40:43], s69 offen sc1\n" \
  "s_waitcnt lgkmcnt(2)\n" \
  "v_mfma_f32_16x16x32_bf16 v[100:103], a[16:19], v[32:35], v[100:103]\n" \
  "ds_read_b128 v[48:51], v9 offset:6144\n" \
  "v_mfma_f32_16x16x32_bf16 v[112:115], a[16:19], v[36:39], v[112:115]\n" \
  "ds_read_b128 v[52:55], v9 offset:26624\n" \
  "v_mfma_f32_16x16x32_bf16 v[104:107], a[96:99], v[32:35], v[104:107]\n" \
  "s_add_u32 s72, s68, 4096\n" \
  "v_mfma_f32_16x16x32_bf16 v[116:119], a[96:99], v[36:39], v[116:119]\n" \
  "s_add_u32 s73, s68, 8192\n" \
  "v_mfma_f32_16x16x32_bf16 v[108:111], a[176:179], v[32:35], v[108:111]\n" \
  "s_add_u32 m0, s54, 0\n" \
  "v_mfma_f32_16x16x32_bf16 v[120:123], a[176:179], v[36:39], v[120:123]\n" \
  "buffer_load_dwordx4 v1, s[36:39], s68 offen offset:0 sc1 lds\n" \
  "s_waitcnt lgkmcnt(2)\n" \
  "v_mfma_f32_16x16x32_bf16 v[100:103], a[20:23], v[40:43], v[100:103]\n" \
  "ds_read_b128 v[32:35], v9 offset:7168\n" \
  "v_mfma_f32_16x16x32_bf16 v[112:115], a[20:23], v[44:47], v[112:115]\n" \
  "ds_read_b128 v[36:39], v9 offset:27648\n" \
  "v_mfma_f32_16x16x32_bf16 v[104:107], a[100:103], v[40:43], v[104:107]\n" \
  "buffer_load_dwordx4 v1, s[36:39], s68 offen offset:1024 sc1 lds\n" \
  "v_mfma_f32_16x16x32_bf16 v[116:119], a[100:103], v[44:47], v[116:119]\n" \
  "v_mfma_f32_16x16x32_bf16 v[108:111], a[180:183], v[40:43], v[108:111]\n" \
  "v_mfma_f32_16x16x32_bf16 v[120:123], a[180:183], v[44:47], v[120:123]\n" \
  "s_waitcnt lgkmcnt(2)\n" \
  "v_mfma_f32_16x16x32_bf16 v[100:103], a[24:27], v[48:51], v[100:103]\n" \
  "ds_read_b128 v[40:43], v9 offset:8192\n" \
  "v_mfma_f32_16x16x32_bf16 v[112:115], a[24:27], v[52:55], v[112:115]\n" \
  "ds_read_b128 v[44:47], v9 offset:28672\n" \
  "v_mfma_f32_16x16x32_bf16 v[104:107], a[104:107], v[48:51], v[104:107]\n" \
  "buffer_load_dwordx4 v1, s[36:39], s68 offen offset:2048 sc1 lds\n" \
  "v_mfma_f32_16x16x32_bf16 v[116:119], a[104:107], v[52:55], v[116:119]\n" \
  "v_mfma_f32_16x16x32_bf16 v[108:111], a[184:187], v[48:51], v[108:111]\n" \
  "v_mfma_f32_16x16x32_bf16 v[120:123], a[184:187], v[52:55], v[120:123]\n" \
  "s_waitcnt lgkmcnt(2)\n" \
  "v_mfma_f32_16x16x32_bf16 v[100:103], a[28:31], v[32:35], v[100:103]\n" \
  "ds_read_b128 v[48:51], v9 offset:9216\n" \
  "v_mfma_f32_16x16x32_bf16 v[112:115], a[28:31], v[36:39], v[112:115]\n" \
  "ds_read_b128 v[52:55], v9 offset:29696\n" \
  "v_mfma_f32_16x16x32_bf16 v[104:107], a[108:111], v[32:35], v[104:107]\n" \
  "buffer_load_dwordx4 v1, s[36:39], s68 offen offset:3072 sc1 lds\n" \
  "v_mfma_f32_16x16x32_bf16 v[116:119], a[108:111], v[36:39], v[116:119]\n" \
  "v_mfma_f32_16x16x32_bf16 v[108:111], a[188:191], v[32:35], v[108:111]\n" \
  "v_mfma_f32_16x16x32_bf16 v[120:123], a[188:191], v[36:39], v[120:123]\n" \
  "s_waitcnt lgkmcnt(2)\n" \
  "v_mfma_f32_16x16x32_bf16 v[100:103], a[32:35], v[40:43], v[100:103]\n" \
  "ds_read_b128 v[32:35], v9 offset:10240\n" \
  "v_mfma_f32_16x16x32_bf16 v[112:115], a[32:35], v[44:47], v[112:115]\n" \
  "ds_read_b128 v[36:39], v9 offset:30720\n" \
  "v_mfma_f32_16x16x32_bf16 v[104:107], a[112:115], v[40:43], v[104:107]\n" \
  "s_add_u32 m0, s54, 4096\n" \
  "v_mfma_f32_16x16x32_bf16 v[116:119], a[112:115], v[44:47], v[116:119]\n" \
  "buffer_load_dwordx4 v1, s[36:39], s72 offen offset:0 sc1 lds\n" \
  "v_mfma_f32_16x16x32_bf16 v[108:111], a[192:195], v[40:43], v[108:111]\n" \
  "v_mfma_f32_16x16x32_bf16 v[120:123], a[192:195], v[44:47], v[120:123]\n" \
  "s_waitcnt lgkmcnt(2)\n" \
  "v_mfma_f32_16x16x32_bf16 v[100:103], a[36:39], v[48:51], v[100:103]\n" \
  "ds_read_b128 v[40:43], v9 offset:11264\n" \
  "v_mfma_f32_16x16x32_bf16 v[112:115], a[36:39], v[52:55], v[112:115]\n" \
  "ds_read_b128 v[44:47], v9 offset:31744\n" \
  "v_mfma_f32_16x16x32_bf16 v[104:107], a[116:119], v[48:51], v[104:107]\n" \
  "buffer_load_dwordx4 v1, s[36:39], s72 offen offset:1024 sc1 lds\n" \
  "v_mfma_f32_16x16x32_bf16 v[116:119], a[116:119], v[52:55], v[116:119]\n" \
  "v_mfma_f32_16x16x32_bf16 v[108:111], a[196:199], v[48:51], v[108:111]\n" \
  "v_mfma_f32_16x16x32_bf16 v[120:123], a[196:199], v[52:55], v[120:123]\n" \
  "s_waitcnt lgkmcnt(2)\n" \
  "v_mfma_f32_16x16x32_bf16 v[100:103], a[40:43], v[32:35], v[100:103]\n" \
  "ds_read_b128 v[48:51], v9 offset:12288\n" \
  "v_mfma_f32_16x16x32_bf16 v[112:115], a[40:43], v[36:39], v[112:115]\n" \
  "ds_read_b128 v[52:55], v9 offset:32768\n" \
  "v_mfma_f32_16x16x32_bf16 v[104:107], a[120:123], v[32:35], v[104:107]\n" \
  "buffer_load_dwordx4 v1, s[36:39], s72 offen offset:2048 sc1 lds\n" \
  "v_mfma_f32_16x16x32_bf16 v[116:119], a[120:123], v[36:39], v[116:119]\n" \
  "v_mfma_f32_16x16x32_bf16 v[108:111], a[200:203], v[32:35], v[108:111]\n" \
  "v_mfma_f32_16x16x32_bf16 v[120:123], a[200:203], v[36:39], v[120:123]\n" \
  "s_waitcnt lgkmcnt(2)\n" \
  "v_mfma_f32_16x16x32_bf16 v[100:103], a[44:47], v[40:43], v[100:103]\n" \
  "ds_read_b128 v[32:35], v9 offset:13312\n" \
  "v_mfma_f32_16x16x32_bf16 v[112:115], a[44:47], v[44:47], v[112:115]\n" \
  "ds_read_b128 v[36:39], v9 offset:33792\n" \
  "v_mfma_f32_16x16x32_bf16 v[104:107], a[124:127], v[40:43], v[104:107]\n" \
  "buffer_load_dwordx4 v1, s[36:39], s72 offen offset:3072 sc1 lds\n" \
  "v_mfma_f32_16x16x32_bf16 v[116:119], a[124:127], v[44:47], v[116:119]\n" \
  "v_mfma_f32_16x16x32_bf16 v[108:111], a[204:207], v[40:43], v[108:111]\n" \
  "v_mfma_f32_16x16x32_bf16 v[120:123], a[204:207], v[44:47], v[120:123]\n" \
  "s_waitcnt lgkmcnt(2)\n" \
  "v_mfma_f32_16x16x32_bf16 v[100:103], a[48:51], v[48:51], v[100:103]\n" \
  "ds_read_b128 v[40:43], v9 offset:14336\n" \
  "v_mfma_f32_16x16x32_bf16 v[112:115], a[48:51], v[52:55], v[112:115]\n" \
  "ds_read_b128 v[44:47], v9 offset:34816\n" \
  "v_mfma_f32_16x16x32_bf16 v[104:107], a[128:131], v[48:51], v[104:107]\n" \
  "s_add_u32 m0, s54, 8192\n" \
  "v_mfma_f32_16x16x32_bf16 v[116:119], a[128:131], v[52:55], v[116:119]\n" \
  "buffer_load_dwordx4 v1, s[36:39], s73 offen offset:0 sc1 lds\n" \
  "v_mfma_f32_16x16x32_bf16 v[108:111], a[208:211], v[48:51], v[108:111]\n" \
  "v_mfma_f32_16x16x32_bf16 v[120:123], a[208:211], v[52:55], v[120:123]\n" \
  "s_waitcnt lgkmcnt(2)\n" \
  "v_mfma_f32_16x16x32_bf16 v[100:103], a[52:55], v[32:35], v[100:103]\n" \
  "ds_read_b128 v[48:51], v9 offset:15360\n" \
  "v_mfma_f32_16x16x32_bf16 v[112:115], a[52:55], v[36:39], v[112:115]\n" \
  "ds_read_b128 v[52:55], v9 offset:35840\n" \
  "v_mfma_f32_16x16x32_bf16 v[104:107], a[132:135], v[32:35], v[104:107]\n" \
  "buffer_load_dwordx4 v1, s[36:39], s73 offen offset:1024 sc1 lds\n" \
  "v_mfma_f32_16x16x32_bf16 v[116:119], a[132:135], v[36:39], v[116:119]\n" \
  "v_mfma_f32_16x16x32_bf16 v[108:111], a[212:215], v[32:35], v[108:111]\n" \
  "v_mfma_f32_16x16x32_bf16 v[120:123], a[212:215], v[36:39], v[120:123]\n" \
  "s_waitcnt lgkmcnt(2)\n" \
  "v_mfma_f32_16x16x32_bf16 v[100:103], a[56:59], v[40:43], v[100:103]\n" \
  "ds_read_b128 v[32:35], v9 offset:16384\n" \
  "v_mfma_f32_16x16x32_bf16 v[112:115], a[56:59], v[44:47], v[112:115]\n" \
  "ds_read_b128 v[36:39], v9 offset:36864\n" \
  "v_mfma_f32_16x16x32_bf16 v[104:107], a[136:139], v[40:43], v[104:107]\n" \
  "s_add_u32 s68, s68, 40960\n" \
  "v_mfma_f32_16x16x32_bf16 v[116:119], a[136:139], v[44:47], v[116:119]\n" \
  "v_mfma_f32_16x16x32_bf16 v[108:111], a[216:219], v[40:43], v[108:111]\n" \
  "v_mfma_f32_16x16x32_bf16 v[120:123], a[216:219], v[44:47], v[120:123]\n" \
  "s_waitcnt lgkmcnt(2)\n" \
  "v_mfma_f32_16x16x32_bf16 v[100:103], a[60:63], v[48:51], v[100:103]\n" \
  "ds_read_b128 v[40:43], v9 offset:17408\n" \
  "v_mfma_f32_16x16x32_bf16 v[112:115], a[60:63], v[52:55], v[112:115]\n" \
  "ds_read_b128 v[44:47], v9 offset:37888\n" \
  "v_mfma_f32_16x16x32_bf16 v[104:107], a[140:143], v[48:51], v[104:107]\n" \
  "v_mov_b32 v11, s52\n" \
  "v_mfma_f32_16x16x32_bf16 v[116:119], a[140:143], v[52:55], v[116:119]\n" \
  "s_waitcnt vmcnt(10)\n" \
  "v_mfma_f32_16x16x32_bf16 v[108:111], a[220:223], v[48:51], v[108:111]\n" \
  "buffer_store_dword v11, v5, s[40:43], 0 offen sc1\n" \
  "v_mfma_f32_16x16x32_bf16 v[120:123], a[220:223], v[52:55], v[120:123]\n" \
  "s_waitcnt lgkmcnt(2)\n" \
  "v_mfma_f32_16x16x32_bf16 v[100:103], a[64:67], v[32:35], v[100:103]\n" \
  "ds_read_b128 v[48:51], v9 offset:18432\n" \
  "v_mfma_f32_16x16x32_bf16 v[112:115], a[64:67], v[36:39], v[112:115]\n" \
  "ds_read_b128 v[52:55], v9 offset:38912\n" \
  "v_mfma_f32_16x16x32_bf16 v[104:107], a[144:147], v[32:35], v[104:107]\n" \
  "v_mfma_f32_16x16x32_bf16 v[116:119], a[144:147], v[36:39], v[116:119]\n" \
  "v_mfma_f32_16x16x32_bf16 v[108:111], a[224:227], v[32:35], v[108:111]\n" \
  "v_mfma_f32_16x16x32_bf16 v[120:123], a[224:227], v[36:39], v[120:123]\n" \
  "s_waitcnt lgkmcnt(2)\n" \
  "v_mfma_f32_16x16x32_bf16 v[100:103], a[68:71], v[40:43], v[100:103]\n" \
  "ds_read_b128 v[32:35], v9 offset:19456\n" \
  "v_mfma_f32_16x16x32_bf16 v[112:115], a[68:71], v[44:47], v[112:115]\n" \
  "ds_read_b128 v[36:39], v9 offset:39936\n" \
  "v_mfma_f32_16x16x32_bf16 v[104:107], a[148:151], v[40:43], v[104:107]\n" \
  "v_readlane_b32 s69, v4, s52\n" \
  "v_mfma_f32_16x16x32_bf16 v[116:119], a[148:151], v[44:47], v[116:119]\n" \
  "v_readlane_b32 s71, v8, s52\n" \
  "v_mfma_f32_16x16x32_bf16 v[108:111], a[228:231], v[40:43], v[108:111]\n" \
  "s_add_u32 s52, s52, 1\n" \
  "v_mfma_f32_16x16x32_bf16 v[120:123], a[228:231], v[44:47], v[120:123]\n" \
  "s_waitcnt lgkmcnt(0)\n" \
  "s_barrier\n" \
  "v_mfma_f32_16x16x32_bf16 v[100:103], a[72:75], v[48:51], v[100:103]\n" \
  "ds_read_b128 v[40:43], v10 offset:0\n" \
  "v_mfma_f32_16x16x32_bf16 v[112:115], a[72:75], v[52:55], v[112:115]\n" \
  "ds_read_b128 v[44:47], v10 offset:20480\n" \
  "v_mfma_f32_16x16x32_bf16 v[104:107], a[152:155], v[48:51], v[104:107]\n" \
  "v_mfma_f32_16x16x32_bf16 v[116:119], a[152:155], v[52:55], v[116:119]\n" \
  "v_mfma_f32_16x16x32_bf16 v[108:111], a[232:235], v[48:51], v[108:111]\n" \
  "v_mfma_f32_16x16x32_bf16 v[120:123], a[232:235], v[52:55], v[120:123]\n" \
  "v_mfma_f32_16x16x32_bf16 v[100:103], a[76:79], v[32:35], v[100:103]\n" \
  "ds_read_b128 v[48:51], v10 offset:1024\n" \
  "v_mfma_f32_16x16x32_bf16 v[112:115], a[76:79], v[36:39], v[112:115]\n" \
  "ds_read_b128 v[52:55], v10 offset:21504\n" \
  "v_mfma_f32_16x16x32_bf16 v[104:107], a[156:159], v[32:35], v[104:107]\n" \
  "s_cmp_eq_u32 s52, s53\n" \
  "v_mfma_f32_16x16x32_bf16 v[116:119], a[156:159], v[36:39], v[116:119]\n" \
  "v_mfma_f32_16x16x32_bf16 v[108:111], a[236:239], v[32:35], v[108:111]\n" \
  "v_mfma_f32_16x16x32_bf16 v[120:123], a[236:239], v[36:39], v[120:123]\n" \
  "s_waitcnt lgkmcnt(2)\n" \
  "v_mfma_f32_16x16x32_bf16 v[56:59], a[0:3], v[40:43], 0\n" \
  "ds_read_b128 v[32:35], v10 offset:2048\n" \
  "v_mfma_f32_16x16x32_bf16 v[68:71], a[0:3], v[44:47], 0\n" \
  "ds_read_b128 v[36:39], v10 offset:22528\n" \
  "v_mfma_f32_16x16x32_bf16 v[60:63], a[80:83], v[40:43], 0\n" \
  "buffer_store_dwordx4 v[12:15], v7, s[40:43], s71 offen sc1\n" \
  "v_mfma_f32_16x16x32_bf16 v[72:75], a[80:83], v[44:47], 0\n" \
  "v_cvt_pk_bf16_f32 v16, v100, v101\n" \
  "v_mfma_f32_16x16x32_bf16 v[64:67], a[160:163], v[40:43], 0\n" \
  "v_cvt_pk_bf16_f32 v17, v102, v103\n" \
  "v_mfma_f32_16x16x32_bf16 v[76:79], a[160:163], v[44:47], 0\n" \
  "v_cvt_pk_bf16_f32 v18, v104, v105\n" \
  "s_waitcnt lgkmcnt(2)\n" \
  "v_mfma_f32_16x16x32_bf16 v[56:59], a[4:7], v[48:51], v[56:59]\n" \
  "ds_read_b128 v[40:43], v10 offset:3072\n" \
  "v_mfma_f32_16x16x32_bf16 v[68:71], a[4:7], v[52:55], v[68:71]\n" \
  "ds_read_b128 v[44:47], v10 offset:23552\n" \
  "v_mfma_f32_16x16x32_bf16 v[60:63], a[84:87], v[48:51], v[60:63]\n" \
  "v_cvt_pk_bf16_f32 v19, v106, v107\n" \
  "v_mfma_f32_16x16x32_bf16 v[72:75], a[84:87], v[52:55], v[72:75]\n" \
  "buffer_store_dwordx4 v[16:19], v2, s[40:43], s69 offen sc1\n" \
  "v_mfma_f32_16x16x32_bf16 v[64:67], a[164:167], v[48:51], v[64:67]\n" \
  "v_cvt_pk_bf16_f32 v20, v112, v113\n" \
  "v_mfma_f32_16x16x32_bf16 v[76:79], a[164:167], v[52:55], v[76:79]\n" \
  "v_cvt_pk_bf16_f32 v21, v114, v115\n" \
  "s_waitcnt lgkmcnt(2)\n" \
  "v_mfma_f32_16x16x32_bf16 v[56:59], a[8:11], v[32:35], v[56:59]\n" \
  "ds_read_b128 v[48:51], v10 offset:4096\n" \
  "v_mfma_f32_16x16x32_bf16 v[68:71], a[8:11], v[36:39], v[68:71]\n" \
  "ds_read_b128 v[52:55], v10 offset:24576\n" \
  "v_mfma_f32_16x16x32_bf16 v[60:63], a[88:91], v[32:35], v[60:63]\n" \
  "v_cvt_pk_bf16_f32 v22, v116, v117\n" \
  "v_mfma_f32_16x16x32_bf16 v[72:75], a[88:91], v[36:39], v[72:75]\n" \
  "v_cvt_pk_bf16_f32 v23, v118, v119\n" \
  "v_mfma_f32_16x16x32_bf16 v[64:67], a[168:171], v[32:35], v[64:67]\n" \
  "buffer_store_dwordx4 v[20:23], v2, s[40:43], s69 offen offset:1024 sc1\n" \
  "v_mfma_f32_16x16x32_bf16 v[76:79], a[168:171], v[36:39], v[76:79]\n" \
  "v_cvt_pk_bf16_f32 v24, v108, v109\n" \
  "s_waitcnt lgkmcnt(2)\n" \
  "v_mfma_f32_16x16x32_bf16 v[56:59], a[12:15], v[40:43], v[56:59]\n" \
  "ds_read_b128 v[32:35], v10 offset:5120\n" \
  "v_mfma_f32_16x16x32_bf16 v[68:71], a[12:15], v[44:47], v[68:71]\n" \
  "ds_read_b128 v[36:39], v10 offset:25600\n" \
  "v_mfma_f32_16x16x32_bf16 v[60:63], a[92:95], v[40:43], v[60:63]\n" \
  "v_cvt_pk_bf16_f32 v25, v110, v111\n" \
  "v_mfma_f32_16x16x32_bf16 v[72:75], a[92:95], v[44:47], v[72:75]\n" \
  "v_cvt_pk_bf16_f32 v26, v120, v121\n" \
  "v_mfma_f32_16x16x32_bf16 v[64:67], a[172:175], v[40:43], v[64:67]\n" \
  "v_cvt_pk_bf16_f32 v27, v122, v123\n" \
  "v_mfma_f32_16x16x32_bf16 v[76:79], a[172:175], v[44:47], v[76:79]\n" \
  "buffer_store_dwordx4 v[24:27], v3, s[40:43], s69 offen sc1\n" \
  "s_waitcnt lgkmcnt(2)\n" \
  "v_mfma_f32_16x16x32_bf16 v[56:59], a[16:19], v[48:51], v[56:59]\n" \
  "ds_read_b128 v[40:43], v10 offset:6144\n" \
  "v_mfma_f32_16x16x32_bf16 v[68:71], a[16:19], v[52:55], v[68:71]\n" \
  "ds_read_b128 v[44:47], v10 offset:26624\n" \
  "v_mfma_f32_16x16x32_bf16 v[60:63], a[96:99], v[48:51], v[60:63]\n" \
  "s_add_u32 s72, s68, 4096\n" \
  "v_mfma_f32_16x16x32_bf16 v[72:75], a[96:99], v[52:55], v[72:75]\n" \
  "s_add_u32 s73, s68, 8192\n" \
  "v_mfma_f32_16x16x32_bf16 v[64:67], a[176:179], v[48:51], v[64:67]\n" \
  "s_add_u32 m0, s54, 40960\n" \
  "v_mfma_f32_16x16x32_bf16 v[76:79], a[176:179], v[52:55], v[76:79]\n" \
  "buffer_load_dwordx4 v1, s[36:39], s68 offen offset:0 sc1 lds\n" \
  "s_waitcnt lgkmcnt(2)\n" \
  "v_mfma_f32_16x16x32_bf16 v[56:59], a[20:23], v[32:35], v[56:59]\n" \
  "ds_read_b128 v[48:51], v10 offset:7168\n" \
  "v_mfma_f32_16x16x32_bf16 v[68:71], a[20:23], v[36:39], v[68:71]\n" \
  "ds_read_b128 v[52:55], v10 offset:27648\n" \
  "v_mfma_f32_16x16x32_bf16 v[60:63], a[100:103], v[32:35], v[60:63]\n" \
  "buffer_load_dwordx4 v1, s[36:39], s68 offen offset:1024 sc1 lds\n" \
  "v_mfma_f32_16x16x32_bf16 v[72:75], a[100:103], v[36:39], v[72:75]\n" \
  "v_mfma_f32_16x16x32_bf16 v[64:67], a[180:183], v[32:35], v[64:67]\n" \
  "v_mfma_f32_16x16x32_bf16 v[76:79], a[180:183], v[36:39], v[76:79]\n" \
  "s_waitcnt lgkmcnt(2)\n" \
  "v_mfma_f32_16x16x32_bf16 v[56:59], a[24:27], v[40:43], v[56:59]\n" \
  "ds_read_b128 v[32:35], v10 offset:8192\n" \
  "v_mfma_f32_16x16x32_bf16 v[68:71], a[24:27], v[44:47], v[68:71]\n" \
  "ds_read_b128 v[36:39], v10 offset:28672\n" \
  "v_mfma_f32_16x16x32_bf16 v[60:63], a[104:107], v[40:43], v[60:63]\n" \
  "buffer_load_dwordx4 v1, s[36:39], s68 offen offset:2048 sc1 lds\n" \
  "v_mfma_f32_16x16x32_bf16 v[72:75], a[104:107], v[44:47], v[72:75]\n" \
  "v_mfma_f32_16x16x32_bf16 v[64:67], a[184:187], v[40:43], v[64:67]\n" \
  "v_mfma_f32_16x16x32_bf16 v[76:79], a[184:187], v[44:47], v[76:79]\n" \
  "s_waitcnt lgkmcnt(2)\n" \
  "v_mfma_f32_16x16x32_bf16 v[56:59], a[28:31], v[48:51], v[56:59]\n" \
  "ds_read_b128 v[40:43], v10 offset:9216\n" \
  "v_mfma_f32_16x16x32_bf16 v[68:71], a[28:31], v[52:55], v[68:71]\n" \
  "ds_read_b128 v[44:47], v10 offset:29696\n" \
  "v_mfma_f32_16x16x32_bf16 v[60:63], a[108:111], v[48:51], v[60:63]\n" \
  "buffer_load_dwordx4 v1, s[36:39], s68 offen offset:3072 sc1 lds\n" \
  "v_mfma_f32_16x16x32_bf16 v[72:75], a[108:111], v[52:55], v[72:75]\n" \
  "v_mfma_f32_16x16x32_bf16 v[64:67], a[188:191], v[48:51], v[64:67]\n" \
  "v_mfma_f32_16x16x32_bf16 v[76:79], a[188:191], v[52:55], v[76:79]\n" \
  "s_waitcnt lgkmcnt(2)\n" \
  "v_mfma_f32_16x16x32_bf16 v[56:59], a[32:35], v[32:35], v[56:59]\n" \
  "ds_read_b128 v[48:51], v10 offset:10240\n" \
  "v_mfma_f32_16x16x32_bf16 v[68:71], a[32:35], v[36:39], v[68:71]\n" \
  "ds_read_b128 v[52:55], v10 offset:30720\n" \
  "v_mfma_f32_16x16x32_bf16 v[60:63], a[112:115], v[32:35], v[60:63]\n" \
  "s_add_u32 m0, s54, 45056\n" \
  "v_mfma_f32_16x16x32_bf16 v[72:75], a[112:115], v[36:39], v[72:75]\n" \
  "buffer_load_dwordx4 v1, s[36:39], s72 offen offset:0 sc1 lds\n" \
  "v_mfma_f32_16x16x32_bf16 v[64:67], a[192:195], v[32:35], v[64:67]\n" \
  "v_mfma_f32_16x16x32_bf16 v[76:79], a[192:195], v[36:39], v[76:79]\n" \
  "s_waitcnt lgkmcnt(2)\n" \
  "v_mfma_f32_16x16x32_bf16 v[56:59], a[36:39], v[40:43], v[56:59]\n" \
  "ds_read_b128 v[32:35], v10 offset:11264\n" \
  "v_mfma_f32_16x16x32_bf16 v[68:71], a[36:39], v[44:47], v[68:71]\n" \
  "ds_read_b128 v[36:39], v10 offset:31744\n" \
  "v_mfma_f32_16x16x32_bf16 v[60:63], a[116:119], v[40:43], v[60:63]\n" \
  "buffer_load_dwordx4 v1, s[36:39], s72 offen offset:1024 sc1 lds\n" \
  "v_mfma_f32_16x16x32_bf16 v[72:75], a[116:119], v[44:47], v[72:75]\n" \
  "v_mfma_f32_16x16x32_bf16 v[64:67], a[196:199], v[40:43], v[64:67]\n" \
  "v_mfma_f32_16x16x32_bf16 v[76:79], a[196:199], v[44:47], v[76:79]\n" \
  "s_waitcnt lgkmcnt(2)\n" \
  "v_mfma_f32_16x16x32_bf16 v[56:59], a[40:43], v[48:51], v[56:59]\n" \
  "ds_read_b128 v[40:43], v10 offset:12288\n" \
  "v_mfma_f32_16x16x32_bf16 v[68:71], a[40:43], v[52:55], v[68:71]\n" \
  "ds_read_b128 v[44:47], v10 offset:32768\n" \
  "v_mfma_f32_16x16x32_bf16 v[60:63], a[120:123], v[48:51], v[60:63]\n" \
  "buffer_load_dwordx4 v1, s[36:39], s72 offen offset:2048 sc1 lds\n" \
  "v_mfma_f32_16x16x32_bf16 v[72:75], a[120:123], v[52:55], v[72:75]\n" \
  "v_mfma_f32_16x16x32_bf16 v[64:67], a[200:203], v[48:51], v[64:67]\n" \
  "v_mfma_f32_16x16x32_bf16 v[76:79], a[200:203], v[52:55], v[76:79]\n" \
  "s_waitcnt lgkmcnt(2)\n" \
  "v_mfma_f32_16x16x32_bf16 v[56:59], a[44:47], v[32:35], v[56:59]\n" \
  "ds_read_b128 v[48:51], v10 offset:13312\n" \
  "v_mfma_f32_16x16x32_bf16 v[68:71], a[44:47], v[36:39], v[68:71]\n" \
  "ds_read_b128 v[52:55], v10 offset:33792\n" \
  "v_mfma_f32_16x16x32_bf16 v[60:63], a[124:127], v[32:35], v[60:63]\n" \
  "buffer_load_dwordx4 v1, s[36:39], s72 offen offset:3072 sc1 lds\n" \
  "v_mfma_f32_16x16x32_bf16 v[72:75], a[124:127], v[36:39], v[72:75]\n" \
  "v_mfma_f32_16x16x32_bf16 v[64:67], a[204:207], v[32:35], v[64:67]\n" \
  "v_mfma_f32_16x16x32_bf16 v[76:79], a[204:207], v[36:39], v[76:79]\n" \
  "s_waitcnt lgkmcnt(2)\n" \
  "v_mfma_f32_16x16x32_bf16 v[56:59], a[48:51], v[40:43], v[56:59]\n" \
  "ds_read_b128 v[32:35], v10 offset:14336\n" \
  "v_mfma_f32_16x16x32_bf16 v[68:71], a[48:51], v[44:47], v[68:71]\n" \
  "ds_read_b128 v[36:39], v10 offset:34816\n" \
  "v_mfma_f32_16x16x32_bf16 v[60:63], a[128:131], v[40:43], v[60:63]\n" \
  "s_add_u32 m0, s54, 49152\n" \
  "v_mfma_f32_16x16x32_bf16 v[72:75], a[128:131], v[44:47], v[72:75]\n" \
  "buffer_load_dwordx4 v1, s[36:39], s73 offen offset:0 sc1 lds\n" \
  "v_mfma_f32_16x16x32_bf16 v[64:67], a[208:211], v[40:43], v[64:67]\n" \
  "v_mfma_f32_16x16x32_bf16 v[76:79], a[208:211], v[44:47], v[76:79]\n" \
  "s_waitcnt lgkmcnt(2)\n" \
  "v_mfma_f32_16x16x32_bf16 v[56:59], a[52:55], v[48:51], v[56:59]\n" \
  "ds_read_b128 v[40:43], v10 offset:15360\n" \
  "v_mfma_f32_16x16x32_bf16 v[68:71], a[52:55], v[52:55], v[68:71]\n" \
  "ds_read_b128 v[44:47], v10 offset:35840\n" \
  "v_mfma_f32_16x16x32_bf16 v[60:63], a[132:135], v[48:51], v[60:63]\n" \
  "buffer_load_dwordx4 v1, s[36:39], s73 offen offset:1024 sc1 lds\n" \
  "v_mfma_f32_16x16x32_bf16 v[72:75], a[132:135], v[52:55], v[72:75]\n" \
  "v_mfma_f32_16x16x32_bf16 v[64:67], a[212:215], v[48:51], v[64:67]\n" \
  "v_mfma_f32_16x16x32_bf16 v[76:79], a[212:215], v[52:55], v[76:79]\n" \
  "s_waitcnt lgkmcnt(2)\n" \
  "v_mfma_f32_16x16x32_bf16 v[56:59], a[56:59], v[32:35], v[56:59]\n" \
  "ds_read_b128 v[48:51], v10 offset:16384\n" \
  "v_mfma_f32_16x16x32_bf16 v[68:71], a[56:59], v[36:39], v[68:71]\n" \
  "ds_read_b128 v[52:55], v10 offset:36864\n" \
  "v_mfma_f32_16x16x32_bf16 v[60:63], a[136:139], v[32:35], v[60:63]\n" \
  "s_add_u32 s68, s68, 40960\n" \
  "v_mfma_f32_16x16x32_bf16 v[72:75], a[136:139], v[36:39], v[72:75]\n" \
  "v_mfma_f32_16x16x32_bf16 v[64:67], a[216:219], v[32:35], v[64:67]\n" \
  "v_mfma_f32_16x16x32_bf16 v[76:79], a[216:219], v[36:39], v[76:79]\n" \
  "s_waitcnt lgkmcnt(2)\n" \
  "v_mfma_f32_16x16x32_bf16 v[56:59], a[60:63], v[40:43], v[56:59]\n" \
  "ds_read_b128 v[32:35], v10 offset:17408\n" \
  "v_mfma_f32_16x16x32_bf16 v[68:71], a[60:63], v[44:47], v[68:71]\n" \
  "ds_read_b128 v[36:39], v10 offset:37888\n" \
  "v_mfma_f32_16x16x32_bf16 v[60:63], a[140:143], v[40:43], v[60:63]\n" \
  "v_mov_b32 v11, s52\n" \
  "v_mfma_f32_16x16x32_bf16 v[72:75], a[140:143], v[44:47], v[72:75]\n" \
  "s_waitcnt vmcnt(10)\n" \
  "v_mfma_f32_16x16x32_bf16 v[64:67], a[220:223], v[40:43], v[64:67]\n" \
  "buffer_store_dword v11, v5, s[40:43], 0 offen sc1\n" \
  "v_mfma_f32_16x16x32_bf16 v[76:79], a[220:223], v[44:47], v[76:79]\n" \
  "s_waitcnt lgkmcnt(2)\n" \
  "v_mfma_f32_16x16x32_bf16 v[56:59], a[64:67], v[48:51], v[56:59]\n" \
  "ds_read_b128 v[40:43], v10 offset:18432\n" \
  "v_mfma_f32_16x16x32_bf16 v[68:71], a[64:67], v[52:55], v[68:71]\n" \
  "ds_read_b128 v[44:47], v10 offset:38912\n" \
  "v_mfma_f32_16x16x32_bf16 v[60:63], a[144:147], v[48:51], v[60:63]\n" \
  "v_mfma_f32_16x16x32_bf16 v[72:75], a[144:147], v[52:55], v[72:75]\n" \
  "v_mfma_f32_16x16x32_bf16 v[64:67], a[224:227], v[48:51], v[64:67]\n" \
  "v_mfma_f32_16x16x32_bf16 v[76:79], a[224:227], v[52:55], v[76:79]\n" \
  "s_waitcnt lgkmcnt(2)\n" \
  "v_mfma_f32_16x16x32_bf16 v[56:59], a[68:71], v[32:35], v[56:59]\n" \
  "ds_read_b128 v[48:51], v10 offset:19456\n" \
  "v_mfma_f32_16x16x32_bf16 v[68:71], a[68:71], v[36:39], v[68:71]\n" \
  "ds_read_b128 v[52:55], v10 offset:39936\n" \
  "v_mfma_f32_16x16x32_bf16 v[60:63], a[148:151], v[32:35], v[60:63]\n" \
  "v_readlane_b32 s69, v4, s52\n" \
  "v_mfma_f32_16x16x32_bf16 v[72:75], a[148:151], v[36:39], v[72:75]\n" \
  "v_readlane_b32 s71, v8, s52\n" \
  "v_mfma_f32_16x16x32_bf16 v[64:67], a[228:231], v[32:35], v[64:67]\n" \
  "s_add_u32 s52, s52, 1\n" \
  "v_mfma_f32_16x16x32_bf16 v[76:79], a[228:231], v[36:39], v[76:79]\n" \
  "s_waitcnt lgkmcnt(0)\n" \
  "s_barrier\n" \
  "v_mfma_f32_16x16x32_bf16 v[56:59], a[72:75], v[40:43], v[56:59]\n" \
  "ds_read_b128 v[32:35], v0 offset:0\n" \
  "v_mfma_f32_16x16x32_bf16 v[68:71], a[72:75], v[44:47], v[68:71]\n" \
  "ds_read_b128 v[36:39], v0 offset:20480\n" \
  "v_mfma_f32_16x16x32_bf16 v[60:63], a[152:155], v[40:43], v[60:63]\n" \
  "v_mfma_f32_16x16x32_bf16 v[72:75], a[152:155], v[44:47], v[72:75]\n" \
  "v_mfma_f32_16x16x32_bf16 v[64:67], a[232:235], v[40:43], v[64:67]\n" \
  "v_mfma_f32_16x16x32_bf16 v[76:79], a[232:235], v[44:47], v[76:79]\n" \
  "v_mfma_f32_16x16x32_bf16 v[56:59], a[76:79], v[48:51], v[56:59]\n" \
  "ds_read_b128 v[40:43], v0 offset:1024\n" \
  "v_mfma_f32_16x16x32_bf16 v[68:71], a[76:79], v[52:55], v[68:71]\n" \
  "ds_read_b128 v[44:47], v0 offset:21504\n" \
  "v_mfma_f32_16x16x32_bf16 v[60:63], a[156:159], v[48:51], v[60:63]\n" \
  "s_cmp_eq_u32 s52, s53\n" \
  "v_mfma_f32_16x16x32_bf16 v[72:75], a[156:159], v[52:55], v[72:75]\n" \
  "v_mfma_f32_16x16x32_bf16 v[64:67], a[236:239], v[48:51], v[64:67]\n" \
  "v_mfma_f32_16x16x32_bf16 v[76:79], a[236:239], v[52:55], v[76:79]\n" \
  "s_waitcnt lgkmcnt(2)\n" \
  "v_mfma_f32_16x16x32_bf16 v[100:103], a[0:3], v[32:35], 0\n" \
  "ds_read_b128 v[48:51], v0 offset:2048\n" \
  "v_mfma_f32_16x16x32_bf16 v[112:115], a[0:3], v[36:39], 0\n" \
  "ds_read_b128 v[52:55], v0 offset:22528\n" \
  "v_mfma_f32_16x16x32_bf16 v[104:107], a[80:83], v[32:35], 0\n" \
  "buffer_store_dwordx4 v[12:15], v7, s[40:43], s71 offen sc1\n" \
  "v_mfma_f32_16x16x32_bf16 v[116:119], a[80:83], v[36:39], 0\n" \
  "v_cvt_pk_bf16_f32 v16, v56, v57\n" \
  "v_mfma_f32_16x16x32_bf16 v[108:111], a[160:163], v[32:35], 0\n" \
  "v_cvt_pk_bf16_f32 v17, v58, v59\n" \
  "v_mfma_f32_16x16x32_bf16 v[120:123], a[160:163], v[36:39], 0\n" \
  "v_cvt_pk_bf16_f32 v18, v60, v61\n" \
  "s_waitcnt lgkmcnt(2)\n" \
  "v_mfma_f32_16x16x32_bf16 v[100:103], a[4:7], v[40:43], v[100:103]\n" \
  "ds_read_b128 v[32:35], v0 offset:3072\n" \
  "v_mfma_f32_16x16x32_bf16 v[112:115], a[4:7], v[44:47], v[112:115]\n" \
  "ds_read_b128 v[36:39], v0 offset:23552\n" \
  "v_mfma_f32_16x16x32_bf16 v[104:107], a[84:87], v[40:43], v[104:107]\n" \
  "v_cvt_pk_bf16_f32 v19, v62, v63\n" \
  "v_mfma_f32_16x16x32_bf16 v[116:119], a[84:87], v[44:47], v[116:119]\n" \
  "buffer_store_dwordx4 v[16:19], v2, s[40:43], s69 offen sc1\n" \
  "v_mfma_f32_16x16x32_bf16 v[108:111], a[164:167], v[40:43], v[108:111]\n" \
  "v_cvt_pk_bf16_f32 v20, v68, v69\n" \
  "v_mfma_f32_16x16x32_bf16 v[120:123], a[164:167], v[44:47], v[120:123]\n" \
  "v_cvt_pk_bf16_f32 v21, v70, v71\n" \
  "s_waitcnt lgkmcnt(2)\n" \
  "v_mfma_f32_16x16x32_bf16 v[100:103], a[8:11], v[48:51], v[100:103]\n" \
  "ds_read_b128 v[40:43], v0 offset:4096\n" \
  "v_mfma_f32_16x16x32_bf16 v[112:115], a[8:11], v[52:55], v[112:115]\n" \
  "ds_read_b128 v[44:47], v0 offset:24576\n" \
  "v_mfma_f32_16x16x32_bf16 v[104:107], a[88:91], v[48:51], v[104:107]\n" \
  "v_cvt_pk_bf16_f32 v22, v72, v73\n" \
  "v_mfma_f32_16x16x32_bf16 v[116:119], a[88:91], v[52:55], v[116:119]\n" \
  "v_cvt_pk_bf16_f32 v23, v74, v75\n" \
  "v_mfma_f32_16x16x32_bf16 v[108:111], a[168:171], v[48:51], v[108:111]\n" \
  "buffer_store_dwordx4 v[20:23], v2, s[40:43], s69 offen offset:1024 sc1\n" \
  "v_mfma_f32_16x16x32_bf16 v[120:123], a[168:171], v[52:55], v[120:123]\n" \
  "v_cvt_pk_bf16_f32 v24, v64, v65\n" \
  "s_waitcnt lgkmcnt(2)\n" \
  "v_mfma_f32_16x16x32_bf16 v[100:103], a[12:15], v[32:35], v[100:103]\n" \
  "ds_read_b128 v[48:51], v0 offset:5120\n" \
  "v_mfma_f32_16x16x32_bf16 v[112:115], a[12:15], v[36:39], v[112:115]\n" \
  "ds_read_b128 v[52:55], v0 offset:25600\n" \
  "v_mfma_f32_16x16x32_bf16 v[104:107], a[92:95], v[32:35], v[104:107]\n" \
  "v_cvt_pk_bf16_f32 v25, v66, v67\n" \
  "v_mfma_f32_16x16x32_bf16 v[116:119], a[92:95], v[36:39], v[116:119]\n" \
  "v_cvt_pk_bf16_f32 v26, v76, v77\n" \
  "v_mfma_f32_16x16x32_bf16 v[108:111], a[172:175], v[32:35], v[108:111]\n" \
  "v_cvt_pk_bf16_f32 v27, v78, v79\n" \
  "v_mfma_f32_16x16x32_bf16 v[120:123], a[172:175], v[36:39], v[120:123]\n" \
  "buffer_store_dwordx4 v[24:27], v3, s[40:43], s69 offen sc1\n" \
  "s_waitcnt lgkmcnt(2)\n" \
  "v_mfma_f32_16x16x32_bf16 v[100:103], a[16:19], v[40:43], v[100:103]\n" \
  "ds_read_b128 v[32:35], v0 offset:6144\n" \
  "v_mfma_f32_16x16x32_bf16 v[112:115], a[16:19], v[44:47], v[112:115]\n" \
  "ds_read_b128 v[36:39], v0 offset:26624\n" \
  "v_mfma_f32_16x16x32_bf16 v[104:107], a[96:99], v[40:43], v[104:107]\n" \
  "s_add_u32 s72, s68, 4096\n" \
  "v_mfma_f32_16x16x32_bf16 v[116:119], a[96:99], v[44:47], v[116:119]\n" \
  "s_add_u32 s73, s68, 8192\n" \
  "v_mfma_f32_16x16x32_bf16 v[108:111], a[176:179], v[40:43], v[108:111]\n" \
  "s_add_u32 m0, s54, 81920\n" \
  "v_mfma_f32_16x16x32_bf16 v[120:123], a[176:179], v[44:47], v[120:123]\n" \
  "buffer_load_dwordx4 v1, s[36:39], s68 offen offset:0 sc1 lds\n" \
  "s_waitcnt lgkmcnt(2)\n" \
  "v_mfma_f32_16x16x32_bf16 v[100:103], a[20:23], v[48:51], v[100:103]\n" \
  "ds_read_b128 v[40:43], v0 offset:7168\n" \
  "v_mfma_f32_16x16x32_bf16 v[112:115], a[20:23], v[52:55], v[112:115]\n" \
  "ds_read_b128 v[44:47], v0 offset:27648\n" \
  "v_mfma_f32_16x16x32_bf16 v[104:107], a[100:103], v[48:51], v[104:107]\n" \
  "buffer_load_dwordx4 v1, s[36:39], s68 offen offset:1024 sc1 lds\n" \
  "v_mfma_f32_16x16x32_bf16 v[116:119], a[100:103], v[52:55], v[116:119]\n" \
  "v_mfma_f32_16x16x32_bf16 v[108:111], a[180:183], v[48:51], v[108:111]\n" \
  "v_mfma_f32_16x16x32_bf16 v[120:123], a[180:183], v[52:55], v[120:123]\n" \
  "s_waitcnt lgkmcnt(2)\n" \
  "v_mfma_f32_16x16x32_bf16 v[100:103], a[24:27], v[32:35], v[100:103]\n" \
  "ds_read_b128 v[48:51], v0 offset:8192\n" \
  "v_mfma_f32_16x16x32_bf16 v[112:115], a[24:27], v[36:39], v[112:115]\n" \
  "ds_read_b128 v[52:55], v0 offset:28672\n" \
  "v_mfma_f32_16x16x32_bf16 v[104:107], a[104:107], v[32:35], v[104:107]\n" \
  "buffer_load_dwordx4 v1, s[36:39], s68 offen offset:2048 sc1 lds\n" \
  "v_mfma_f32_16x16x32_bf16 v[116:119], a[104:107], v[36:39], v[116:119]\n" \
  "v_mfma_f32_16x16x32_bf16 v[108:111], a[184:187], v[32:35], v[108:111]\n" \
  "v_mfma_f32_16x16x32_bf16 v[120:123], a[184:187], v[36:39], v[120:123]\n" \
  "s_waitcnt lgkmcnt(2)\n" \
  "v_mfma_f32_16x16x32_bf16 v[100:103], a[28:31], v[40:43], v[100:103]\n" \
  "ds_read_b128 v[32:35], v0 offset:9216\n" \
  "v_mfma_f32_16x16x32_bf16 v[112:115], a[28:31], v[44:47], v[112:115]\n" \
  "ds_read_b128 v[36:39], v0 offset:29696\n" \
  "v_mfma_f32_16x16x32_bf16 v[104:107], a[108:111], v[40:43], v[104:107]\n" \
  "buffer_load_dwordx4 v1, s[36:39], s68 offen offset:3072 sc1 lds\n" \
  "v_mfma_f32_16x16x32_bf16 v[116:119], a[108:111], v[44:47], v[116:119]\n" \
  "v_mfma_f32_16x16x32_bf16 v[108:111], a[188:191], v[40:43], v[108:111]\n" \
  "v_mfma_f32_16x16x32_bf16 v[120:123], a[188:191], v[44:47], v[120:123]\n" \
  "s_waitcnt lgkmcnt(2)\n" \
  "v_mfma_f32_16x16x32_bf16 v[100:103], a[32:35], v[48:51], v[100:103]\n" \
  "ds_read_b128 v[40:43], v0 offset:10240\n" \
  "v_mfma_f32_16x16x32_bf16 v[112:115], a[32:35], v[52:55], v[112:115]\n" \
  "ds_read_b128 v[44:47], v0 offset:30720\n" \
  "v_mfma_f32_16x16x32_bf16 v[104:107], a[112:115], v[48:51], v[104:107]\n" \
  "s_add_u32 m0, s54, 86016\n" \
  "v_mfma_f32_16x16x32_bf16 v[116:119], a[112:115], v[52:55], v[116:119]\n" \
  "buffer_load_dwordx4 v1, s[36:39], s72 offen offset:0 sc1 lds\n" \
  "v_mfma_f32_16x16x32_bf16 v[108:111], a[192:195], v[48:51], v[108:111]\n" \
  "v_mfma_f32_16x16x32_bf16 v[120:123], a[192:195], v[52:55], v[120:123]\n" \
  "s_waitcnt lgkmcnt(2)\n" \
  "v_mfma_f32_16x16x32_bf16 v[100:103], a[36:39], v[32:35], v[100:103]\n" \
  "ds_read_b128 v[48:51], v0 offset:11264\n" \
  "v_mfma_f32_16x16x32_bf16 v[112:115], a[36:39], v[36:39], v[112:115]\n" \
  "ds_read_b128 v[52:55], v0 offset:31744\n" \
  "v_mfma_f32_16x16x32_bf16 v[104:107], a[116:119], v[32:35], v[104:107]\n" \
  "buffer_load_dwordx4 v1, s[36:39], s72 offen offset:1024 sc1 lds\n" \
  "v_mfma_f32_16x16x32_bf16 v[116:119], a[116:119], v[36:39], v[116:119]\n" \
  "v_mfma_f32_16x16x32_bf16 v[108:111], a[196:199], v[32:35], v[108:111]\n" \
  "v_mfma_f32_16x16x32_bf16 v[120:123], a[196:199], v[36:39], v[120:123]\n" \
  "s_waitcnt lgkmcnt(2)\n" \
  "v_mfma_f32_16x16x32_bf16 v[100:103], a[40:43], v[40:43], v[100:103]\n" \
  "ds_read_b128 v[32:35], v0 offset:12288\n" \
  "v_mfma_f32_16x16x32_bf16 v[112:115], a[40:43], v[44:47], v[112:115]\n" \
  "ds_read_b128 v[36:39], v0 offset:32768\n" \
  "v_mfma_f32_16x16x32_bf16 v[104:107], a[120:123], v[40:43], v[104:107]\n" \
  "buffer_load_dwordx4 v1, s[36:39], s72 offen offset:2048 sc1 lds\n" \
  "v_mfma_f32_16x16x32_bf16 v[116:119], a[120:123], v[44:47], v[116:119]\n" \
  "v_mfma_f32_16x16x32_bf16 v[108:111], a[200:203], v[40:43], v[108:111]\n" \
  "v_mfma_f32_16x16x32_bf16 v[120:123], a[200:203], v[44:47], v[120:123]\n" \
  "s_waitcnt lgkmcnt(2)\n" \
  "v_mfma_f32_16x16x32_bf16 v[100:103], a[44:47], v[48:51], v[100:103]\n" \
  "ds_read_b128 v[40:43], v0 offset:13312\n" \
  "v_mfma_f32_16x16x32_bf16 v[112:115], a[44:47], v[52:55], v[112:115]\n" \
  "ds_read_b128 v[44:47], v0 offset:33792\n" \
  "v_mfma_f32_16x16x32_bf16 v[104:107], a[124:127], v[48:51], v[104:107]\n" \
  "buffer_load_dwordx4 v1, s[36:39], s72 offen offset:3072 sc1 lds\n" \
  "v_mfma_f32_16x16x32_bf16 v[116:119], a[124:127], v[52:55], v[116:119]\n" \
  "v_mfma_f32_16x16x32_bf16 v[108:111], a[204:207], v[48:51], v[108:111]\n" \
  "v_mfma_f32_16x16x32_bf16 v[120:123], a[204:207], v[52:55], v[120:123]\n" \
  "s_waitcnt lgkmcnt(2)\n" \
  "v_mfma_f32_16x16x32_bf16 v[100:103], a[48:51], v[32:35], v[100:103]\n" \
  "ds_read_b128 v[48:51], v0 offset:14336\n" \
  "v_mfma_f32_16x16x32_bf16 v[112:115], a[48:51], v[36:39], v[112:115]\n" \
  "ds_read_b128 v[52:55], v0 offset:34816\n" \
  "v_mfma_f32_16x16x32_bf16 v[104:107], a[128:131], v[32:35], v[104:107]\n" \
  "s_add_u32 m0, s54, 90112\n" \
  "v_mfma_f32_16x16x32_bf16 v[116:119], a[128:131], v[36:39], v[116:119]\n" \
  "buffer_load_dwordx4 v1, s[36:39], s73 offen offset:0 sc1 lds\n" \
  "v_mfma_f32_16x16x32_bf16 v[108:111], a[208:211], v[32:35], v[108:111]\n" \
  "v_mfma_f32_16x16x32_bf16 v[120:123], a[208:211], v[36:39], v[120:123]\n" \
  "s_waitcnt lgkmcnt(2)\n" \
  "v_mfma_f32_16x16x32_bf16 v[100:103], a[52:55], v[40:43], v[100:103]\n" \
  "ds_read_b128 v[32:35], v0 offset:15360\n" \
  "v_mfma_f32_16x16x32_bf16 v[112:115], a[52:55], v[44:47], v[112:115]\n" \
  "ds_read_b128 v[36:39], v0 offset:35840\n" \
  "v_mfma_f32_16x16x32_bf16 v[104:107], a[132:135], v[40:43], v[104:107]\n" \
  "buffer_load_dwordx4 v1, s[36:39], s73 offen offset:1024 sc1 lds\n" \
  "v_mfma_f32_16x16x32_bf16 v[116:119], a[132:135], v[44:47], v[116:119]\n" \
  "v_mfma_f32_16x16x32_bf16 v[108:111], a[212:215], v[40:43], v[108:111]\n" \
  "v_mfma_f32_16x16x32_bf16 v[120:123], a[212:215], v[44:47], v[120:123]\n" \
  "s_waitcnt lgkmcnt(2)\n" \
  "v_mfma_f32_16x16x32_bf16 v[100:103], a[56:59], v[48:51], v[100:103]\n" \
  "ds_read_b128 v[40:43], v0 offset:16384\n" \
  "v_mfma_f32_16x16x32_bf16 v[112:115], a[56:59], v[52:55], v[112:115]\n" \
  "ds_read_b128 v[44:47], v0 offset:36864\n" \
  "v_mfma_f32_16x16x32_bf16 v[104:107], a[136:139], v[48:51], v[104:107]\n" \
  "s_add_u32 s68, s68, 40960\n" \
  "v_mfma_f32_16x16x32_bf16 v[116:119], a[136:139], v[52:55], v[116:119]\n" \
  "v_mfma_f32_16x16x32_bf16 v[108:111], a[216:219], v[48:51], v[108:111]\n" \
  "v_mfma_f32_16x16x32_bf16 v[120:123], a[216:219], v[52:55], v[120:123]\n" \
  "s_waitcnt lgkmcnt(2)\n" \
  "v_mfma_f32_16x16x32_bf16 v[100:103], a[60:63], v[32:35], v[100:103]\n" \
  "ds_read_b128 v[48:51], v0 offset:17408\n" \
  "v_mfma_f32_16x16x32_bf16 v[112:115], a[60:63], v[36:39], v[112:115]\n" \
  "ds_read_b128 v[52:55], v0 offset:37888\n" \
  "v_mfma_f32_16x16x32_bf16 v[104:107], a[140:143], v[32:35], v[104:107]\n" \
  "v_mov_b32 v11, s52\n" \
  "v_mfma_f32_16x16x32_bf16 v[116:119], a[140:143], v[36:39], v[116:119]\n" \
  "s_waitcnt vmcnt(10)\n" \
  "v_mfma_f32_16x16x32_bf16 v[108:111], a[220:223], v[32:35], v[108:111]\n" \
  "buffer_store_dword v11, v5, s[40:43], 0 offen sc1\n" \
  "v_mfma_f32_16x16x32_bf16 v[120:123], a[220:223], v[36:39], v[120:123]\n" \
  "s_waitcnt lgkmcnt(2)\n" \
  "v_mfma_f32_16x16x32_bf16 v[100:103], a[64:67], v[40:43], v[100:103]\n" \
  "ds_read_b128 v[32:35], v0 offset:18432\n" \
  "v_mfma_f32_16x16x32_bf16 v[112:115], a[64:67], v[44:47], v[112:115]\n" \
  "ds_read_b128 v[36:39], v0 offset:38912\n" \
  "v_mfma_f32_16x16x32_bf16 v[104:107], a[144:147], v[40:43], v[104:107]\n" \
  "v_mfma_f32_16x16x32_bf16 v[116:119], a[144:147], v[44:47], v[116:119]\n" \
  "v_mfma_f32_16x16x32_bf16 v[108:111], a[224:227], v[40:43], v[108:111]\n" \
  "v_mfma_f32_16x16x32_bf16 v[120:123], a[224:227], v[44:47], v[120:123]\n" \
  "s_waitcnt lgkmcnt(2)\n" \
  "v_mfma_f32_16x16x32_bf16 v[100:103], a[68:71], v[48:51], v[100:103]\n" \
  "ds_read_b128 v[40:43], v0 offset:19456\n" \
  "v_mfma_f32_16x16x32_bf16 v[112:115], a[68:71], v[52:55], v[112:115]\n" \
  "ds_read_b128 v[44:47], v0 offset:39936\n" \
  "v_mfma_f32_16x16x32_bf16 v[104:107], a[148:151], v[48:51], v[104:107]\n" \
  "v_readlane_b32 s69, v4, s52\n" \
  "v_mfma_f32_16x16x32_bf16 v[116:119], a[148:151], v[52:55], v[116:119]\n" \
  "v_readlane_b32 s71, v8, s52\n" \
  "v_mfma_f32_16x16x32_bf16 v[108:111], a[228:231], v[48:51], v[108:111]\n" \
  "s_add_u32 s52, s52, 1\n" \
  "v_mfma_f32_16x16x32_bf16 v[120:123], a[228:231], v[52:55], v[120:123]\n" \
  "s_waitcnt lgkmcnt(0)\n" \
  "s_barrier\n" \
  "v_mfma_f32_16x16x32_bf16 v[100:103], a[72:75], v[32:35], v[100:103]\n" \
  "ds_read_b128 v[48:51], v9 offset:0\n" \
  "v_mfma_f32_16x16x32_bf16 v[112:115], a[72:75], v[36:39], v[112:115]\n" \
  "ds_read_b128 v[52:55], v9 offset:20480\n" \
  "v_mfma_f32_16x16x32_bf16 v[104:107], a[152:155], v[32:35], v[104:107]\n" \
  "v_mfma_f32_16x16x32_bf16 v[116:119], a[152:155], v[36:39], v[116:119]\n" \
  "v_mfma_f32_16x16x32_bf16 v[108:111], a[232:235], v[32:35], v[108:111]\n" \
  "v_mfma_f32_16x16x32_bf16 v[120:123], a[232:235], v[36:39], v[120:123]\n" \
  "v_mfma_f32_16x16x32_bf16 v[100:103], a[76:79], v[40:43], v[100:103]\n" \
  "ds_read_b128 v[32:35], v9 offset:1024\n" \
  "v_mfma_f32_16x16x32_bf16 v[112:115], a[76:79], v[44:47], v[112:115]\n" \
  "ds_read_b128 v[36:39], v9 offset:21504\n" \
  "v_mfma_f32_16x16x32_bf16 v[104:107], a[156:159], v[40:43], v[104:107]\n" \
  "s_cmp_eq_u32 s52, s53\n" \
  "v_mfma_f32_16x16x32_bf16 v[116:119], a[156:159], v[44:47], v[116:119]\n" \
  "v_mfma_f32_16x16x32_bf16 v[108:111], a[236:239], v[40:43], v[108:111]\n" \
  "v_mfma_f32_16x16x32_bf16 v[120:123], a[236:239], v[44:47], v[120:123]\n" \
  "s_cbranch_scc1 LPE_%=\n" \
  "s_waitcnt lgkmcnt(2)\n" \
  "v_mfma_f32_16x16x32_bf16 v[56:59], a[0:3], v[48:51], 0\n" \
  "ds_read_b128 v[40:43], v9 offset:2048\n" \
  "v_mfma_f32_16x16x32_bf16 v[68:71], a[0:3], v[52:55], 0\n" \
  "ds_read_b128 v[44:47], v9 offset:22528\n" \
  "v_mfma_f32_16x16x32_bf16 v[60:63], a[80:83], v[48:51], 0\n" \
  "buffer_store_dwordx4 v[12:15], v7, s[40:43], s71 offen sc1\n" \
  "v_mfma_f32_16x16x32_bf16 v[72:75], a[80:83], v[52:55], 0\n" \
  "v_cvt_pk_bf16_f32 v16, v100, v101\n" \
  "v_mfma_f32_16x16x32_bf16 v[64:67], a[160:163], v[48:51], 0\n" \
  "v_cvt_pk_bf16_f32 v17, v102, v103\n" \
  "v_mfma_f32_16x16x32_bf16 v[76:79], a[160:163], v[52:55], 0\n" \
  "v_cvt_pk_bf16_f32 v18, v104, v105\n" \
  "s_waitcnt lgkmcnt(2)\n" \
  "v_mfma_f32_16x16x32_bf16 v[56:59], a[4:7], v[32:35], v[56:59]\n" \
  "ds_read_b128 v[48:51], v9 offset:3072\n" \
  "v_mfma_f32_16x16x32_bf16 v[68:71], a[4:7], v[36:39], v[68:71]\n" \
  "ds_read_b128 v[52:55], v9 offset:23552\n" \
  "v_mfma_f32_16x16x32_bf16 v[60:63], a[84:87], v[32:35], v[60:63]\n" \
  "v_cvt_pk_bf16_f32 v19, v106, v107\n" \
  "v_mfma_f32_16x16x32_bf16 v[72:75], a[84:87], v[36:39], v[72:75]\n" \
  "buffer_store_dwordx4 v[16:19], v2, s[40:43], s69 offen sc1\n" \
  "v_mfma_f32_16x16x32_bf16 v[64:67], a[164:167], v[32:35], v[64:67]\n" \
  "v_cvt_pk_bf16_f32 v20, v112, v113\n" \
  "v_mfma_f32_16x16x32_bf16 v[76:79], a[164:167], v[36:39], v[76:79]\n" \
  "v_cvt_pk_bf16_f32 v21, v114, v115\n" \
  "s_waitcnt lgkmcnt(2)\n" \
  "v_mfma_f32_16x16x32_bf16 v[56:59], a[8:11], v[40:43], v[56:59]\n" \
  "ds_read_b128 v[32:35], v9 offset:4096\n" \
  "v_mfma_f32_16x16x32_bf16 v[68:71], a[8:11], v[44:47], v[68:71]\n" \
  "ds_read_b128 v[36:39], v9 offset:24576\n" \
  "v_mfma_f32_16x16x32_bf16 v[60:63], a[88:91], v[40:43], v[60:63]\n" \
  "v_cvt_pk_bf16_f32 v22, v116, v117\n" \
  "v_mfma_f32_16x16x32_bf16 v[72:75], a[88:91], v[44:47], v[72:75]\n" \
  "v_cvt_pk_bf16_f32 v23, v118, v119\n" \
  "v_mfma_f32_16x16x32_bf16 v[64:67], a[168:171], v[40:43], v[64:67]\n" \
  "buffer_store_dwordx4 v[20:23], v2, s[40:43], s69 offen offset:1024 sc1\n" \
  "v_mfma_f32_16x16x32_bf16 v[76:79], a[168:171], v[44:47], v[76:79]\n" \
  "v_cvt_pk_bf16_f32 v24, v108, v109\n" \
  "s_waitcnt lgkmcnt(2)\n" \
  "v_mfma_f32_16x16x32_bf16 v[56:59], a[12:15], v[48:51], v[56:59]\n" \
  "ds_read_b128 v[40:43], v9 offset:5120\n" \
  "v_mfma_f32_16x16x32_bf16 v[68:71], a[12:15], v[52:55], v[68:71]\n" \
  "ds_read_b128 v[44:47], v9 offset:25600\n" \
  "v_mfma_f32_16x16x32_bf16 v[60:63], a[92:95], v[48:51], v[60:63]\n" \
  "v_cvt_pk_bf16_f32 v25, v110, v111\n" \
  "v_mfma_f32_16x16x32_bf16 v[72:75], a[92:95], v[52:55], v[72:75]\n" \
  "v_cvt_pk_bf16_f32 v26, v120, v121\n" \
  "v_mfma_f32_16x16x32_bf16 v[64:67], a[172:175], v[48:51], v[64:67]\n" \
  "v_cvt_pk_bf16_f32 v27, v122, v123\n" \
  "v_mfma_f32_16x16x32_bf16 v[76:79], a[172:175], v[52:55], v[76:79]\n" \
  "buffer_store_dwordx4 v[24:27], v3, s[40:43], s69 offen sc1\n" \
  "s_waitcnt lgkmcnt(2)\n" \
  "v_mfma_f32_16x16x32_bf16 v[56:59], a[16:19], v[32:35], v[56:59]\n" \
  "ds_read_b128 v[48:51], v9 offset:6144\n" \
  "v_mfma_f32_16x16x32_bf16 v[68:71], a[16:19], v[36:39], v[68:71]\n" \
  "ds_read_b128 v[52:55], v9 offset:26624\n" \
  "v_mfma_f32_16x16x32_bf16 v[60:63], a[96:99], v[32:35], v[60:63]\n" \
  "s_add_u32 s72, s68, 4096\n" \
  "v_mfma_f32_16x16x32_bf16 v[72:75], a[96:99], v[36:39], v[72:75]\n" \
  "s_add_u32 s73, s68, 8192\n" \
  "v_mfma_f32_16x16x32_bf16 v[64:67], a[176:179], v[32:35], v[64:67]\n" \
  "s_add_u32 m0, s54, 0\n" \
  "v_mfma_f32_16x16x32_bf16 v[76:79], a[176:179], v[36:39], v[76:79]\n" \
  "buffer_load_dwordx4 v1, s[36:39], s68 offen offset:0 sc1 lds\n" \
  "s_waitcnt lgkmcnt(2)\n" \
  "v_mfma_f32_16x16x32_bf16 v[56:59], a[20:23], v[40:43], v[56:59]\n" \
  "ds_read_b128 v[32:35], v9 offset:7168\n" \
  "v_mfma_f32_16x16x32_bf16 v[68:71], a[20:23], v[44:47], v[68:71]\n" \
  "ds_read_b128 v[36:39], v9 offset:27648\n" \
  "v_mfma_f32_16x16x32_bf16 v[60:63], a[100:103], v[40:43], v[60:63]\n" \
  "buffer_load_dwordx4 v1, s[36:39], s68 offen offset:1024 sc1 lds\n" \
  "v_mfma_f32_16x16x32_bf16 v[72:75], a[100:103], v[44:47], v[72:75]\n" \
  "v_mfma_f32_16x16x32_bf16 v[64:67], a[180:183], v[40:43], v[64:67]\n" \
  "v_mfma_f32_16x16x32_bf16 v[76:79], a[180:183], v[44:47], v[76:79]\n" \
  "s_waitcnt lgkmcnt(2)\n" \
  "v_mfma_f32_16x16x32_bf16 v[56:59], a[24:27], v[48:51], v[56:59]\n" \
  "ds_read_b128 v[40:43], v9 offset:8192\n" \
  "v_mfma_f32_16x16x32_bf16 v[68:71], a[24:27], v[52:55], v[68:71]\n" \
  "ds_read_b128 v[44:47], v9 offset:28672\n" \
  "v_mfma_f32_16x16x32_bf16 v[60:63], a[104:107], v[48:51], v[60:63]\n" \
  "buffer_load_dwordx4 v1, s[36:39], s68 offen offset:2048 sc1 lds\n" \
  "v_mfma_f32_16x16x32_bf16 v[72:75], a[104:107], v[52:55], v[72:75]\n" \
  "v_mfma_f32_16x16x32_bf16 v[64:67], a[184:187], v[48:51], v[64:67]\n" \
  "v_mfma_f32_16x16x32_bf16 v[76:79], a[184:187], v[52:55], v[76:79]\n" \
  "s_waitcnt lgkmcnt(2)\n" \
  "v_mfma_f32_16x16x32_bf16 v[56:59], a[28:31], v[32:35], v[56:59]\n" \
  "ds_read_b128 v[48:51], v9 offset:9216\n" \
  "v_mfma_f32_16x16x32_bf16 v[68:71], a[28:31], v[36:39], v[68:71]\n" \
  "ds_read_b128 v[52:55], v9 offset:29696\n" \
  "v_mfma_f32_16x16x32_bf16 v[60:63], a[108:111], v[32:35], v[60:63]\n" \
  "buffer_load_dwordx4 v1, s[36:39], s68 offen offset:3072 sc1 lds\n" \
  "v_mfma_f32_16x16x32_bf16 v[72:75], a[108:111], v[36:39], v[72:75]\n" \
  "v_mfma_f32_16x16x32_bf16 v[64:67], a[188:191], v[32:35], v[64:67]\n" \
  "v_mfma_f32_16x16x32_bf16 v[76:79], a[188:191], v[36:39], v[76:79]\n" \
  "s_waitcnt lgkmcnt(2)\n" \
  "v_mfma_f32_16x16x32_bf16 v[56:59], a[32:35], v[40:43], v[56:59]\n" \
  "ds_read_b128 v[32:35], v9 offset:10240\n" \
  "v_mfma_f32_16x16x32_bf16 v[68:71], a[32:35], v[44:47], v[68:71]\n" \
  "ds_read_b128 v[36:39], v9 offset:30720\n" \
  "v_mfma_f32_16x16x32_bf16 v[60:63], a[112:115], v[40:43], v[60:63]\n" \
  "s_add_u32 m0, s54, 4096\n" \
  "v_mfma_f32_16x16x32_bf16 v[72:75], a[112:115], v[44:47], v[72:75]\n" \
  "buffer_load_dwordx4 v1, s[36:39], s72 offen offset:0 sc1 lds\n" \
  "v_mfma_f32_16x16x32_bf16 v[64:67], a[192:195], v[40:43], v[64:67]\n" \
  "v_mfma_f32_16x16x32_bf16 v[76:79], a[192:195], v[44:47], v[76:79]\n" \
  "s_waitcnt lgkmcnt(2)\n" \
  "v_mfma_f32_16x16x32_bf16 v[56:59], a[36:39], v[48:51], v[56:59]\n" \
  "ds_read_b128 v[40:43], v9 offset:11264\n" \
  "v_mfma_f32_16x16x32_bf16 v[68:71], a[36:39], v[52:55], v[68:71]\n" \
  "ds_read_b128 v[44:47], v9 offset:31744\n" \
  "v_mfma_f32_16x16x32_bf16 v[60:63], a[116:119], v[48:51], v[60:63]\n" \
  "buffer_load_dwordx4 v1, s[36:39], s72 offen offset:1024 sc1 lds\n" \
  "v_mfma_f32_16x16x32_bf16 v[72:75], a[116:119], v[52:55], v[72:75]\n" \
  "v_mfma_f32_16x16x32_bf16 v[64:67], a[196:199], v[48:51], v[64:67]\n" \
  "v_mfma_f32_16x16x32_bf16 v[76:79], a[196:199], v[52:55], v[76:79]\n" \
  "s_waitcnt lgkmcnt(2)\n" \
  "v_mfma_f32_16x16x32_bf16 v[56:59], a[40:43], v[32:35], v[56:59]\n" \
  "ds_read_b128 v[48:51], v9 offset:12288\n" \
  "v_mfma_f32_16x16x32_bf16 v[68:71], a[40:43], v[36:39], v[68:71]\n" \
  "ds_read_b128 v[52:55], v9 offset:32768\n" \
  "v_mfma_f32_16x16x32_bf16 v[60:63], a[120:123], v[32:35], v[60:63]\n" \
  "buffer_load_dwordx4 v1, s[36:39], s72 offen offset:2048 sc1 lds\n" \
  "v_mfma_f32_16x16x32_bf16 v[72:75], a[120:123], v[36:39], v[72:75]\n" \
  "v_mfma_f32_16x16x32_bf16 v[64:67], a[200:203], v[32:35], v[64:67]\n" \
  "v_mfma_f32_16x16x32_bf16 v[76:79], a[200:203], v[36:39], v[76:79]\n" \
  "s_waitcnt lgkmcnt(2)\n" \
  "v_mfma_f32_16x16x32_bf16 v[56:59], a[44:47], v[40:43], v[56:59]\n" \
  "ds_read_b128 v[32:35], v9 offset:13312\n" \
  "v_mfma_f32_16x16x32_bf16 v[68:71], a[44:47], v[44:47], v[68:71]\n" \
  "ds_read_b128 v[36:39], v9 offset:33792\n" \
  "v_mfma_f32_16x16x32_bf16 v[60:63], a[124:127], v[40:43], v[60:63]\n" \
  "buffer_load_dwordx4 v1, s[36:39], s72 offen offset:3072 sc1 lds\n" \
  "v_mfma_f32_16x16x32_bf16 v[72:75], a[124:127], v[44:47], v[72:75]\n" \
  "v_mfma_f32_16x16x32_bf16 v[64:67], a[204:207], v[40:43], v[64:67]\n" \
  "v_mfma_f32_16x16x32_bf16 v[76:79], a[204:207], v[44:47], v[76:79]\n" \
  "s_waitcnt lgkmcnt(2)\n" \
  "v_mfma_f32_16x16x32_bf16 v[56:59], a[48:51], v[48:51], v[56:59]\n" \
  "ds_read_b128 v[40:43], v9 offset:14336\n" \
  "v_mfma_f32_16x16x32_bf16 v[68:71], a[48:51], v[52:55], v[68:71]\n" \
  "ds_read_b128 v[44:47], v9 offset:34816\n" \
  "v_mfma_f32_16x16x32_bf16 v[60:63], a[128:131], v[48:51], v[60:63]\n" \
  "s_add_u32 m0, s54, 8192\n" \
  "v_mfma_f32_16x16x32_bf16 v[72:75], a[128:131], v[52:55], v[72:75]\n" \
  "buffer_load_dwordx4 v1, s[36:39], s73 offen offset:0 sc1 lds\n" \
  "v_mfma_f32_16x16x32_bf16 v[64:67], a[208:211], v[48:51], v[64:67]\n" \
  "v_mfma_f32_16x16x32_bf16 v[76:79], a[208:211], v[52:55], v[76:79]\n" \
  "s_waitcnt lgkmcnt(2)\n" \
  "v_mfma_f32_16x16x32_bf16 v[56:59], a[52:55], v[32:35], v[56:59]\n" \
  "ds_read_b128 v[48:51], v9 offset:15360\n" \
  "v_mfma_f32_16x16x32_bf16 v[68:71], a[52:55], v[36:39], v[68:71]\n" \
  "ds_read_b128 v[52:55], v9 offset:35840\n" \
  "v_mfma_f32_16x16x32_bf16 v[60:63], a[132:135], v[32:35], v[60:63]\n" \
  "buffer_load_dwordx4 v1, s[36:39], s73 offen offset:1024 sc1 lds\n" \
  "v_mfma_f32_16x16x32_bf16 v[72:75], a[132:135], v[36:39], v[72:75]\n" \
  "v_mfma_f32_16x16x32_bf16 v[64:67], a[212:215], v[32:35], v[64:67]\n" \
  "v_mfma_f32_16x16x32_bf16 v[76:79], a[212:215], v[36:39], v[76:79]\n" \
  "s_waitcnt lgkmcnt(2)\n" \
  "v_mfma_f32_16x16x32_bf16 v[56:59], a[56:59], v[40:43], v[56:59]\n" \
  "ds_read_b128 v[32:35], v9 offset:16384\n" \
  "v_mfma_f32_16x16x32_bf16 v[68:71], a[56:59], v[44:47], v[68:71]\n" \
  "ds_read_b128 v[36:39], v9 offset:36864\n" \
  "v_mfma_f32_16x16x32_bf16 v[60:63], a[136:139], v[40:43], v[60:63]\n" \
  "s_add_u32 s68, s68, 40960\n" \
  "v_mfma_f32_16x16x32_bf16 v[72:75], a[136:139], v[44:47], v[72:75]\n" \
  "v_mfma_f32_16x16x32_bf16 v[64:67], a[216:219], v[40:43], v[64:67]\n" \
  "v_mfma_f32_16x16x32_bf16 v[76:79], a[216:219], v[44:47], v[76:79]\n" \
  "s_waitcnt lgkmcnt(2)\n" \
  "v_mfma_f32_16x16x32_bf16 v[56:59], a[60:63], v[48:51], v[56:59]\n" \
  "ds_read_b128 v[40:43], v9 offset:17408\n" \
  "v_mfma_f32_16x16x32_bf16 v[68:71], a[60:63], v[52:55], v[68:71]\n" \
  "ds_read_b128 v[44:47], v9 offset:37888\n" \
  "v_mfma_f32_16x16x32_bf16 v[60:63], a[140:143], v[48:51], v[60:63]\n" \
  "v_mov_b32 v11, s52\n" \
  "v_mfma_f32_16x16x32_bf16 v[72:75], a[140:143], v[52:55], v[72:75]\n" \
  "s_waitcnt vmcnt(10)\n" \
  "v_mfma_f32_16x16x32_bf16 v[64:67], a[220:223], v[48:51], v[64:67]\n" \
  "buffer_store_dword v11, v5, s[40:43], 0 offen sc1\n" \
  "v_mfma_f32_16x16x32_bf16 v[76:79], a[220:223], v[52:55], v[76:79]\n" \
  "s_waitcnt lgkmcnt(2)\n" \
  "v_mfma_f32_16x16x32_bf16 v[56:59], a[64:67], v[32:35], v[56:59]\n" \
  "ds_read_b128 v[48:51], v9 offset:18432\n" \
  "v_mfma_f32_16x16x32_bf16 v[68:71], a[64:67], v[36:39], v[68:71]\n" \
  "ds_read_b128 v[52:55], v9 offset:38912\n" \
  "v_mfma_f32_16x16x32_bf16 v[60:63], a[144:147], v[32:35], v[60:63]\n" \
  "v_mfma_f32_16x16x32_bf16 v[72:75], a[144:147], v[36:39], v[72:75]\n" \
  "v_mfma_f32_16x16x32_bf16 v[64:67], a[224:227], v[32:35], v[64:67]\n" \
  "v_mfma_f32_16x16x32_bf16 v[76:79], a[224:227], v[36:39], v[76:79]\n" \
  "s_waitcnt lgkmcnt(2)\n" \
  "v_mfma_f32_16x16x32_bf16 v[56:59], a[68:71], v[40:43], v[56:59]\n" \
  "ds_read_b128 v[32:35], v9 offset:19456\n" \
  "v_mfma_f32_16x16x32_bf16 v[68:71], a[68:71], v[44:47], v[68:71]\n" \
  "ds_read_b128 v[36:39], v9 offset:39936\n" \
  "v_mfma_f32_16x16x32_bf16 v[60:63], a[148:151], v[40:43], v[60:63]\n" \
  "v_readlane_b32 s69, v4, s52\n" \
  "v_mfma_f32_16x16x32_bf16 v[72:75], a[148:151], v[44:47], v[72:75]\n" \
  "v_readlane_b32 s71, v8, s52\n" \
  "v_mfma_f32_16x16x32_bf16 v[64:67], a[228:231], v[40:43], v[64:67]\n" \
  "s_add_u32 s52, s52, 1\n" \
  "v_mfma_f32_16x16x32_bf16 v[76:79], a[228:231], v[44:47], v[76:79]\n" \
  "s_waitcnt lgkmcnt(0)\n" \
  "s_barrier\n" \
  "v_mfma_f32_16x16x32_bf16 v[56:59], a[72:75], v[48:51], v[56:59]\n" \
  "ds_read_b128 v[40:43], v10 offset:0\n" \
  "v_mfma_f32_16x16x32_bf16 v[68:71], a[72:75], v[52:55], v[68:71]\n" \
  "ds_read_b128 v[44:47], v10 offset:20480\n" \
  "v_mfma_f32_16x16x32_bf16 v[60:63], a[152:155], v[48:51], v[60:63]\n" \
  "v_mfma_f32_16x16x32_bf16 v[72:75], a[152:155], v[52:55], v[72:75]\n" \
  "v_mfma_f32_16x16x32_bf16 v[64:67], a[232:235], v[48:51], v[64:67]\n" \
  "v_mfma_f32_16x16x32_bf16 v[76:79], a[232:235], v[52:55], v[76:79]\n" \
  "v_mfma_f32_16x16x32_bf16 v[56:59], a[76:79], v[32:35], v[56:59]\n" \
  "ds_read_b128 v[48:51], v10 offset:1024\n" \
  "v_mfma_f32_16x16x32_bf16 v[68:71], a[76:79], v[36:39], v[68:71]\n" \
  "ds_read_b128 v[52:55], v10 offset:21504\n" \
  "v_mfma_f32_16x16x32_bf16 v[60:63], a[156:159], v[32:35], v[60:63]\n" \
  "s_cmp_eq_u32 s52, s53\n" \
  "v_mfma_f32_16x16x32_bf16 v[72:75], a[156:159], v[36:39], v[72:75]\n" \
  "v_mfma_f32_16x16x32_bf16 v[64:67], a[236:239], v[32:35], v[64:67]\n" \
  "v_mfma_f32_16x16x32_bf16 v[76:79], a[236:239], v[36:39], v[76:79]\n" \
  "s_waitcnt lgkmcnt(2)\n" \
  "v_mfma_f32_16x16x32_bf16 v[100:103], a[0:3], v[40:43], 0\n" \
  "ds_read_b128 v[32:35], v10 offset:2048\n" \
  "v_mfma_f32_16x16x32_bf16 v[112:115], a[0:3], v[44:47], 0\n" \
  "ds_read_b128 v[36:39], v10 offset:22528\n" \
  "v_mfma_f32_16x16x32_bf16 v[104:107], a[80:83], v[40:43], 0\n" \
  "buffer_store_dwordx4 v[12:15], v7, s[40:43], s71 offen sc1\n" \
  "v_mfma_f32_16x16x32_bf16 v[116:119], a[80:83], v[44:47], 0\n" \
  "v_cvt_pk_bf16_f32 v16, v56, v57\n" \
  "v_mfma_f32_16x16x32_bf16 v[108:111], a[160:163], v[40:43], 0\n" \
  "v_cvt_pk_bf16_f32 v17, v58, v59\n" \
  "v_mfma_f32_16x16x32_bf16 v[120:123], a[160:163], v[44:47], 0\n" \
  "v_cvt_pk_bf16_f32 v18, v60, v61\n" \
  "s_waitcnt lgkmcnt(2)\n" \
  "v_mfma_f32_16x16x32_bf16 v[100:103], a[4:7], v[48:51], v[100:103]\n" \
  "ds_read_b128 v[40:43], v10 offset:3072\n" \
  "v_mfma_f32_16x16x32_bf16 v[112:115], a[4:7], v[52:55], v[112:115]\n" \
  "ds_read_b128 v[44:47], v10 offset:23552\n" \
  "v_mfma_f32_16x16x32_bf16 v[104:107], a[84:87], v[48:51], v[104:107]\n" \
  "v_cvt_pk_bf16_f32 v19, v62, v63\n" \
  "v_mfma_f32_16x16x32_bf16 v[116:119], a[84:87], v[52:55], v[116:119]\n" \
  "buffer_store_dwordx4 v[16:19], v2, s[40:43], s69 offen sc1\n" \
  "v_mfma_f32_16x16x32_bf16 v[108:111], a[164:167], v[48:51], v[108:111]\n" \
  "v_cvt_pk_bf16_f32 v20, v68, v69\n" \
  "v_mfma_f32_16x16x32_bf16 v[120:123], a[164:167], v[52:55], v[120:123]\n" \
  "v_cvt_pk_bf16_f32 v21, v70, v71\n" \
  "s_waitcnt lgkmcnt(2)\n" \
  "v_mfma_f32_16x16x32_bf16 v[100:103], a[8:11], v[32:35], v[100:103]\n" \
  "ds_read_b128 v[48:51], v10 offset:4096\n" \
  "v_mfma_f32_16x16x32_bf16 v[112:115], a[8:11], v[36:39], v[112:115]\n" \
  "ds_read_b128 v[52:55], v10 offset:24576\n" \
  "v_mfma_f32_16x16x32_bf16 v[104:107], a[88:91], v[32:35], v[104:107]\n" \
  "v_cvt_pk_bf16_f32 v22, v72, v73\n" \
  "v_mfma_f32_16x16x32_bf16 v[116:119], a[88:91], v[36:39], v[116:119]\n" \
  "v_cvt_pk_bf16_f32 v23, v74, v75\n" \
  "v_mfma_f32_16x16x32_bf16 v[108:111], a[168:171], v[32:35], v[108:111]\n" \
  "buffer_store_dwordx4 v[20:23], v2, s[40:43], s69 offen offset:1024 sc1\n" \
  "v_mfma_f32_16x16x32_bf16 v[120:123], a[168:171], v[36:39], v[120:123]\n" \
  "v_cvt_pk_bf16_f32 v24, v64, v65\n" \
  "s_waitcnt lgkmcnt(2)\n" \
  "v_mfma_f32_16x16x32_bf16 v[100:103], a[12:15], v[40:43], v[100:103]\n" \
  "ds_read_b128 v[32:35], v10 offset:5120\n" \
  "v_mfma_f32_16x16x32_bf16 v[112:115], a[12:15], v[44:47], v[112:115]\n" \
  "ds_read_b128 v[36:39], v10 offset:25600\n" \
  "v_mfma_f32_16x16x32_bf16 v[104:107], a[92:95], v[40:43], v[104:107]\n" \
  "v_cvt_pk_bf16_f32 v25, v66, v67\n" \
  "v_mfma_f32_16x16x32_bf16 v[116:119], a[92:95], v[44:47], v[116:119]\n" \
  "v_cvt_pk_bf16_f32 v26, v76, v77\n" \
  "v_mfma_f32_16x16x32_bf16 v[108:111], a[172:175], v[40:43], v[108:111]\n" \
  "v_cvt_pk_bf16_f32 v27, v78, v79\n" \
  "v_mfma_f32_16x16x32_bf16 v[120:123], a[172:175], v[44:47], v[120:123]\n" \
  "buffer_store_dwordx4 v[24:27], v3, s[40:43], s69 offen sc1\n" \
  "s_waitcnt lgkmcnt(2)\n" \
  "v_mfma_f32_16x16x32_bf16 v[100:103], a[16:19], v[48:51], v[100:103]\n" \
  "ds_read_b128 v[40:43], v10 offset:6144\n" \
  "v_mfma_f32_16x16x32_bf16 v[112:115], a[16:19], v[52:55], v[112:115]\n" \
  "ds_read_b128 v[44:47], v10 offset:26624\n" \
  "v_mfma_f32_16x16x32_bf16 v[104:107], a[96:99], v[48:51], v[104:107]\n" \
  "s_add_u32 s72, s68, 4096\n" \
  "v_mfma_f32_16x16x32_bf16 v[116:119], a[96:99], v[52:55], v[116:119]\n" \
  "s_add_u32 s73, s68, 8192\n" \
  "v_mfma_f32_16x16x32_bf16 v[108:111], a[176:179], v[48:51], v[108:111]\n" \
  "s_add_u32 m0, s54, 40960\n" \
  "v_mfma_f32_16x16x32_bf16 v[120:123], a[176:179], v[52:55], v[120:123]\n" \
  "buffer_load_dwordx4 v1, s[36:39], s68 offen offset:0 sc1 lds\n" \
  "s_waitcnt lgkmcnt(2)\n" \
  "v_mfma_f32_16x16x32_bf16 v[100:103], a[20:23], v[32:35], v[100:103]\n" \
  "ds_read_b128 v[48:51], v10 offset:7168\n" \
  "v_mfma_f32_16x16x32_bf16 v[112:115], a[20:23], v[36:39], v[112:115]\n" \
  "ds_read_b128 v[52:55], v10 offset:27648\n" \
  "v_mfma_f32_16x16x32_bf16 v[104:107], a[100:103], v[32:35], v[104:107]\n" \
  "buffer_load_dwordx4 v1, s[36:39], s68 offen offset:1024 sc1 lds\n" \
  "v_mfma_f32_16x16x32_bf16 v[116:119], a[100:103], v[36:39], v[116:119]\n" \
  "v_mfma_f32_16x16x32_bf16 v[108:111], a[180:183], v[32:35], v[108:111]\n" \
  "v_mfma_f32_16x16x32_bf16 v[120:123], a[180:183], v[36:39], v[120:123]\n" \
  "s_waitcnt lgkmcnt(2)\n" \
  "v_mfma_f32_16x16x32_bf16 v[100:103], a[24:27], v[40:43], v[100:103]\n" \
  "ds_read_b128 v[32:35], v10 offset:8192\n" \
  "v_mfma_f32_16x16x32_bf16 v[112:115], a[24:27], v[44:47], v[112:115]\n" \
  "ds_read_b128 v[36:39], v10 offset:28672\n" \
  "v_mfma_f32_16x16x32_bf16 v[104:107], a[104:107], v[40:43], v[104:107]\n" \
  "buffer_load_dwordx4 v1, s[36:39], s68 offen offset:2048 sc1 lds\n" \
  "v_mfma_f32_16x16x32_bf16 v[116:119], a[104:107], v[44:47], v[116:119]\n" \
  "v_mfma_f32_16x16x32_bf16 v[108:111], a[184:187], v[40:43], v[108:111]\n" \
  "v_mfma_f32_16x16x32_bf16 v[120:123], a[184:187], v[44:47], v[120:123]\n" \
  "s_waitcnt lgkmcnt(2)\n" \
  "v_mfma_f32_16x16x32_bf16 v[100:103], a[28:31], v[48:51], v[100:103]\n" \
  "ds_read_b128 v[40:43], v10 offset:9216\n" \
  "v_mfma_f32_16x16x32_bf16 v[112:115], a[28:31], v[52:55], v[112:115]\n" \
  "ds_read_b128 v[44:47], v10 offset:29696\n" \
  "v_mfma_f32_16x16x32_bf16 v[104:107], a[108:111], v[48:51], v[104:107]\n" \
  "buffer_load_dwordx4 v1, s[36:39], s68 offen offset:3072 sc1 lds\n" \
  "v_mfma_f32_16x16x32_bf16 v[116:119], a[108:111], v[52:55], v[116:119]\n" \
  "v_mfma_f32_16x16x32_bf16 v[108:111], a[188:191], v[48:51], v[108:111]\n" \
  "v_mfma_f32_16x16x32_bf16 v[120:123], a[188:191], v[52:55], v[120:123]\n" \
  "s_waitcnt lgkmcnt(2)\n" \
  "v_mfma_f32_16x16x32_bf16 v[100:103], a[32:35], v[32:35], v[100:103]\n" \
  "ds_read_b128 v[48:51], v10 offset:10240\n" \
  "v_mfma_f32_16x16x32_bf16 v[112:115], a[32:35], v[36:39], v[112:115]\n" \
  "ds_read_b128 v[52:55], v10 offset:30720\n" \
  "v_mfma_f32_16x16x32_bf16 v[104:107], a[112:115], v[32:35], v[104:107]\n" \
  "s_add_u32 m0, s54, 45056\n" \
  "v_mfma_f32_16x16x32_bf16 v[116:119], a[112:115], v[36:39], v[116:119]\n" \
  "buffer_load_dwordx4 v1, s[36:39], s72 offen offset:0 sc1 lds\n" \
  "v_mfma_f32_16x16x32_bf16 v[108:111], a[192:195], v[32:35], v[108:111]\n" \
  "v_mfma_f32_16x16x32_bf16 v[120:123], a[192:195], v[36:39], v[120:123]\n" \
  "s_waitcnt lgkmcnt(2)\n" \
  "v_mfma_f32_16x16x32_bf16 v[100:103], a[36:39], v[40:43], v[100:103]\n" \
  "ds_read_b128 v[32:35], v10 offset:11264\n" \
  "v_mfma_f32_16x16x32_bf16 v[112:115], a[36:39], v[44:47], v[112:115]\n" \
  "ds_read_b128 v[36:39], v10 offset:31744\n" \
  "v_mfma_f32_16x16x32_bf16 v[104:107], a[116:119], v[40:43], v[104:107]\n" \
  "buffer_load_dwordx4 v1, s[36:39], s72 offen offset:1024 sc1 lds\n" \
  "v_mfma_f32_16x16x32_bf16 v[116:119], a[116:119], v[44:47], v[116:119]\n" \
  "v_mfma_f32_16x16x32_bf16 v[108:111], a[196:199], v[40:43], v[108:111]\n" \
  "v_mfma_f32_16x16x32_bf16 v[120:123], a[196:199], v[44:47], v[120:123]\n" \
  "s_waitcnt lgkmcnt(2)\n" \
  "v_mfma_f32_16x16x32_bf16 v[100:103], a[40:43], v[48:51], v[100:103]\n" \
  "ds_read_b128 v[40:43], v10 offset:12288\n" \
  "v_mfma_f32_16x16x32_bf16 v[112:115], a[40:43], v[52:55], v[112:115]\n" \
  "ds_read_b128 v[44:47], v10 offset:32768\n" \
  "v_mfma_f32_16x16x32_bf16 v[104:107], a[120:123], v[48:51], v[104:107]\n" \
  "buffer_load_dwordx4 v1, s[36:39], s72 offen offset:2048 sc1 lds\n" \
  "v_mfma_f32_16x16x32_bf16 v[116:119], a[120:123], v[52:55], v[116:119]\n" \
  "v_mfma_f32_16x16x32_bf16 v[108:111], a[200:203], v[48:51], v[108:111]\n" \
  "v_mfma_f32_16x16x32_bf16 v[120:123], a[200:203], v[52:55], v[120:123]\n" \
  "s_waitcnt lgkmcnt(2)\n" \
  "v_mfma_f32_16x16x32_bf16 v[100:103], a[44:47], v[32:35], v[100:103]\n" \
  "ds_read_b128 v[48:51], v10 offset:13312\n" \
  "v_mfma_f32_16x16x32_bf16 v[112:115], a[44:47], v[36:39], v[112:115]\n" \
  "ds_read_b128 v[52:55], v10 offset:33792\n" \
  "v_mfma_f32_16x16x32_bf16 v[104:107], a[124:127], v[32:35], v[104:107]\n" \
  "buffer_load_dwordx4 v1, s[36:39], s72 offen offset:3072 sc1 lds\n" \
  "v_mfma_f32_16x16x32_bf16 v[116:119], a[124:127], v[36:39], v[116:119]\n" \
  "v_mfma_f32_16x16x32_bf16 v[108:111], a[204:207], v[32:35], v[108:111]\n" \
  "v_mfma_f32_16x16x32_bf16 v[120:123], a[204:207], v[36:39], v[120:123]\n" \
  "s_waitcnt lgkmcnt(2)\n" \
  "v_mfma_f32_16x16x32_bf16 v[100:103], a[48:51], v[40:43], v[100:103]\n" \
  "ds_read_b128 v[32:35], v10 offset:14336\n" \
  "v_mfma_f32_16x16x32_bf16 v[112:115], a[48:51], v[44:47], v[112:115]\n" \
  "ds_read_b128 v[36:39], v10 offset:34816\n" \
  "v_mfma_f32_16x16x32_bf16 v[104:107], a[128:131], v[40:43], v[104:107]\n" \
  "s_add_u32 m0, s54, 49152\n" \
  "v_mfma_f32_16x16x32_bf16 v[116:119], a[128:131], v[44:47], v[116:119]\n" \
  "buffer_load_dwordx4 v1, s[36:39], s73 offen offset:0 sc1 lds\n" \
  "v_mfma_f32_16x16x32_bf16 v[108:111], a[208:211], v[40:43], v[108:111]\n" \
  "v_mfma_f32_16x16x32_bf16 v[120:123], a[208:211], v[44:47], v[120:123]\n" \
  "s_waitcnt lgkmcnt(2)\n" \
  "v_mfma_f32_16x16x32_bf16 v[100:103], a[52:55], v[48:51], v[100:103]\n" \
  "ds_read_b128 v[40:43], v10 offset:15360\n" \
  "v_mfma_f32_16x16x32_bf16 v[112:115], a[52:55], v[52:55], v[112:115]\n" \
  "ds_read_b128 v[44:47], v10 offset:35840\n" \
  "v_mfma_f32_16x16x32_bf16 v[104:107], a[132:135], v[48:51], v[104:107]\n" \
  "buffer_load_dwordx4 v1, s[36:39], s73 offen offset:1024 sc1 lds\n" \
  "v_mfma_f32_16x16x32_bf16 v[116:119], a[132:135], v[52:55], v[116:119]\n" \
  "v_mfma_f32_16x16x32_bf16 v[108:111], a[212:215], v[48:51], v[108:111]\n" \
  "v_mfma_f32_16x16x32_bf16 v[120:123], a[212:215], v[52:55], v[120:123]\n" \
  "s_waitcnt lgkmcnt(2)\n" \
  "v_mfma_f32_16x16x32_bf16 v[100:103], a[56:59], v[32:35], v[100:103]\n" \
  "ds_read_b128 v[48:51], v10 offset:16384\n" \
  "v_mfma_f32_16x16x32_bf16 v[112:115], a[56:59], v[36:39], v[112:115]\n" \
  "ds_read_b128 v[52:55], v10 offset:36864\n" \
  "v_mfma_f32_16x16x32_bf16 v[104:107], a[136:139], v[32:35], v[104:107]\n" \
  "s_add_u32 s68, s68, 40960\n" \
  "v_mfma_f32_16x16x32_bf16 v[116:119], a[136:139], v[36:39], v[116:119]\n" \
  "v_mfma_f32_16x16x32_bf16 v[108:111], a[216:219], v[32:35], v[108:111]\n" \
  "v_mfma_f32_16x16x32_bf16 v[120:123], a[216:219], v[36:39], v[120:123]\n" \
  "s_waitcnt lgkmcnt(2)\n" \
  "v_mfma_f32_16x16x32_bf16 v[100:103], a[60:63], v[40:43], v[100:103]\n" \
  "ds_read_b128 v[32:35], v10 offset:17408\n" \
  "v_mfma_f32_16x16x32_bf16 v[112:115], a[60:63], v[44:47], v[112:115]\n" \
  "ds_read_b128 v[36:39], v10 offset:37888\n" \
  "v_mfma_f32_16x16x32_bf16 v[104:107], a[140:143], v[40:43], v[104:107]\n" \
  "v_mov_b32 v11, s52\n" \
  "v_mfma_f32_16x16x32_bf16 v[116:119], a[140:143], v[44:47], v[116:119]\n" \
  "s_waitcnt vmcnt(10)\n" \
  "v_mfma_f32_16x16x32_bf16 v[108:111], a[220:223], v[40:43], v[108:111]\n" \
  "buffer_store_dword v11, v5, s[40:43], 0 offen sc1\n" \
  "v_mfma_f32_16x16x32_bf16 v[120:123], a[220:223], v[44:47], v[120:123]\n" \
  "s_waitcnt lgkmcnt(2)\n" \
  "v_mfma_f32_16x16x32_bf16 v[100:103], a[64:67], v[48:51], v[100:103]\n" \
  "ds_read_b128 v[40:43], v10 offset:18432\n" \
  "v_mfma_f32_16x16x32_bf16 v[112:115], a[64:67], v[52:55], v[112:115]\n" \
  "ds_read_b128 v[44:47], v10 offset:38912\n" \
  "v_mfma_f32_16x16x32_bf16 v[104:107], a[144:147], v[48:51], v[104:107]\n" \
  "v_mfma_f32_16x16x32_bf16 v[116:119], a[144:147], v[52:55], v[116:119]\n" \
  "v_mfma_f32_16x16x32_bf16 v[108:111], a[224:227], v[48:51], v[108:111]\n" \
  "v_mfma_f32_16x16x32_bf16 v[120:123], a[224:227], v[52:55], v[120:123]\n" \
  "s_waitcnt lgkmcnt(2)\n" \
  "v_mfma_f32_16x16x32_bf16 v[100:103], a[68:71], v[32:35], v[100:103]\n" \
  "ds_read_b128 v[48:51], v10 offset:19456\n" \
  "v_mfma_f32_16x16x32_bf16 v[112:115], a[68:71], v[36:39], v[112:115]\n" \
  "ds_read_b128 v[52:55], v10 offset:39936\n" \
  "v_mfma_f32_16x16x32_bf16 v[104:107], a[148:151], v[32:35], v[104:107]\n" \
  "v_readlane_b32 s69, v4, s52\n" \
  "v_mfma_f32_16x16x32_bf16 v[116:119], a[148:151], v[36:39], v[116:119]\n" \
  "v_readlane_b32 s71, v8, s52\n" \
  "v_mfma_f32_16x16x32_bf16 v[108:111], a[228:231], v[32:35], v[108:111]\n" \
  "s_add_u32 s52, s52, 1\n" \
  "v_mfma_f32_16x16x32_bf16 v[120:123], a[228:231], v[36:39], v[120:123]\n" \
  "s_waitcnt lgkmcnt(0)\n" \
  "s_barrier\n" \
  "v_mfma_f32_16x16x32_bf16 v[100:103], a[72:75], v[40:43], v[100:103]\n" \
  "ds_read_b128 v[32:35], v0 offset:0\n" \
  "v_mfma_f32_16x16x32_bf16 v[112:115], a[72:75], v[44:47], v[112:115]\n" \
  "ds_read_b128 v[36:39], v0 offset:20480\n" \
  "v_mfma_f32_16x16x32_bf16 v[104:107], a[152:155], v[40:43], v[104:107]\n" \
  "v_mfma_f32_16x16x32_bf16 v[116:119], a[152:155], v[44:47], v[116:119]\n" \
  "v_mfma_f32_16x16x32_bf16 v[108:111], a[232:235], v[40:43], v[108:111]\n" \
  "v_mfma_f32_16x16x32_bf16 v[120:123], a[232:235], v[44:47], v[120:123]\n" \
  "v_mfma_f32_16x16x32_bf16 v[100:103], a[76:79], v[48:51], v[100:103]\n" \
  "ds_read_b128 v[40:43], v0 offset:1024\n" \
  "v_mfma_f32_16x16x32_bf16 v[112:115], a[76:79], v[52:55], v[112:115]\n" \
  "ds_read_b128 v[44:47], v0 offset:21504\n" \
  "v_mfma_f32_16x16x32_bf16 v[104:107], a[156:159], v[48:51], v[104:107]\n" \
  "s_cmp_eq_u32 s52, s53\n" \
  "v_mfma_f32_16x16x32_bf16 v[116:119], a[156:159], v[52:55], v[116:119]\n" \
  "v_mfma_f32_16x16x32_bf16 v[108:111], a[236:239], v[48:51], v[108:111]\n" \
  "v_mfma_f32_16x16x32_bf16 v[120:123], a[236:239], v[52:55], v[120:123]\n" \
  "s_cbranch_scc0 LPL_%=\n" \
  "LPE_%=:\n" \
  "buffer_store_dwordx4 v[12:15], v7, s[40:43], s71 offen sc1\n" \
  "s_nop 6\n" \
  "v_cvt_pk_bf16_f32 v16, v100, v101\n" \
  "v_cvt_pk_bf16_f32 v17, v102, v103\n" \
  "v_cvt_pk_bf16_f32 v18, v104, v105\n" \
  "v_cvt_pk_bf16_f32 v19, v106, v107\n" \
  "v_cvt_pk_bf16_f32 v20, v112, v113\n" \
  "v_cvt_pk_bf16_f32 v21, v114, v115\n" \
  "v_cvt_pk_bf16_f32 v22, v116, v117\n" \
  "v_cvt_pk_bf16_f32 v23, v118, v119\n" \
  "v_cvt_pk_bf16_f32 v24, v108, v109\n" \
  "v_cvt_pk_bf16_f32 v25, v110, v111\n" \
  "v_cvt_pk_bf16_f32 v26, v120, v121\n" \
  "v_cvt_pk_bf16_f32 v27, v122, v123\n" \
  "buffer_store_dwordx4 v[16:19], v2, s[40:43], s69 offen sc1\n" \
  "buffer_store_dwordx4 v[20:23], v2, s[40:43], s69 offen offset:1024 sc1\n" \
  "buffer_store_dwordx4 v[24:27], v3, s[40:43], s69 offen sc1\n" \
  "v_mov_b32 v11, s52\n" \
  "s_waitcnt vmcnt(0) lgkmcnt(0)\n" \
  "buffer_store_dword v11, v5, s[40:43], 0 offen sc1\n"
#define VO_CLOBBERS \
  "v16", "v17", "v18", "v19", "v20", "v21", "v22", "v23", "v24", "v25", "v26", "v27", "v28", "v29", "v30", "v31", "v32", "v33", "v34", "v35", "v36", \
  "v37", "v38", "v39", "v40", "v41", "v42", "v43", "v44", "v45", "v46", "v47", "v48", "v49", "v50", "v51", "v52", "v53", "v54", "v55", "v56", "v57", \
  "v58", "v59", "v60", "v61", "v62", "v63", "v64", "v65", "v66", "v67", "v68", "v69", "v70", "v71", "v72", "v73", "v74", "v75", "v76", "v77", "v78", \
  "v79", "v80", "v81", "v82", "v83", "v84", "v85", "v86", "v87", "v88", "v89", "v90", "v91", "v92", "v93", "v94", "v95", "v96", "v97", "v98", "v99", \
  "v100", "v101", "v102", "v103", "v104", "v105", "v106", "v107", "v108", "v109", "v110", "v111", "v112", "v113", "v114", "v115", "v116", "v117", \
  "v118", "v119", "v120", "v121", "v122", "v123", "v124", "v125", "v126", "v127", "v224", "v225", "v226", "v227", "v228", "v229", "v230", "v231", \
  "v232", "v233", "v234", "v235", "v236", "v237", "v238", "v239", "v240", "v241", "v242", "v243", "v244", "v245", "v246", "v247", "v248", "v249", \
  "v250", "v251", "v252", "v253", "v254", "v255", "s68", "s69", "s70", "s71", "s72", "s73", "s74", "s75", "s76", "s77"
#define VO_ASM \
  "s_getpc_b64 s[70:71]\n" \
  "LCF5_%=:\n" \
  "s_add_u32 s70, s70, LVL_%=-LCF5_%=\n" \
  "s_addc_u32 s71, s71, 0\n" \
  "v_mbcnt_lo_u32_b32 v16, -1, 0\n" \
  "v_mbcnt_hi_u32_b32 v16, -1, v16\n" \
  "v_lshlrev_b32 v16, 6, v16\n" \
  "global_load_dword v17, v16, s[70:71]\n" \
  "s_waitcnt vmcnt(0)\n" \
  "s_mov_b32 s70, s38\n" \
  "s_mov_b32 s38, 0\n" \
  "s_mov_b32 s75, 200001\n" \
  "s_sub_u32 s52, s53, 2\n" \
  "LVA_%=:\n" \
  "s_lshl_b32 s73, s54, 11\n" \
  "s_lshl_b32 s74, s54, 10\n" \
  "s_add_u32 s74, s74, 8192\n" \
  "s_mov_b32 s76, s52\n" \
  "s_mov_b32 s69, 0x40000000\n" \
  "v_mov_b32 v12, v0\n" \
  "s_barrier\n" \
  "LVPS0_%=:\n" \
  "buffer_load_dword v11, v9, s[36:39], 0 offen sc1\n" \
  "s_waitcnt vmcnt(0)\n" \
  "v_cmp_ge_u32 vcc, s76, v11\n" \
  "s_nop 3\n" \
  "s_cbranch_vccz LVS0_%=\n" \
  "s_add_u32 s75, s75, 1\n" \
  "s_cmp_gt_u32 s75, 200000\n" \
  "s_cbranch_scc1 LVS0_%=\n" \
  "s_sleep 2\n" \
  "s_branch LVPS0_%=\n" \
  "LVS0_%=:\n" \
  "v_readlane_b32 s71, v6, s76\n" \
  "s_add_u32 m0, s73, 0\n" \
  "s_nop 4\n" \
  "buffer_load_dwordx4 v1, s[36:39], s71 offen offset:0 sc1 lds\n" \
  "buffer_load_dwordx4 v1, s[36:39], s71 offen offset:1024 sc1 lds\n" \
  "s_add_u32 m0, s74, 0\n" \
  "s_add_u32 s76, s76, 1\n" \
  "buffer_load_dwordx4 v2, s[36:39], s71 offen sc1 lds\n" \
  "s_min_u32 s76, s76, 63\n" \
  "LVPS1_%=:\n" \
  "buffer_load_dword v11, v9, s[36:39], 0 offen sc1\n" \
  "s_waitcnt vmcnt(0)\n" \
  "v_cmp_ge_u32 vcc, s76, v11\n" \
  "s_nop 3\n" \
  "s_cbranch_vccz LVS1_%=\n" \
  "s_add_u32 s75, s75, 1\n" \
  "s_cmp_gt_u32 s75, 200000\n" \
  "s_cbranch_scc1 LVS1_%=\n" \
  "s_sleep 2\n" \
  "s_branch LVPS1_%=\n" \
  "LVS1_%=:\n" \
  "v_readlane_b32 s71, v6, s76\n" \
  "s_add_u32 m0, s73, 16384\n" \
  "s_nop 4\n" \
  "buffer_load_dwordx4 v1, s[36:39], s71 offen offset:0 sc1 lds\n" \
  "buffer_load_dwordx4 v1, s[36:39], s71 offen offset:1024 sc1 lds\n" \
  "s_add_u32 m0, s74, 16384\n" \
  "s_add_u32 s76, s76, 1\n" \
  "buffer_load_dwordx4 v2, s[36:39], s71 offen sc1 lds\n" \
  "s_min_u32 s76, s76, 63\n" \
  "s_xor_b32 s73, s73, 0x8000\n" \
  "s_xor_b32 s74, s74, 0x8000\n" \
  "buffer_load_dword v11, v9, s[36:39], 0 offen sc1\n" \
  "s_waitcnt vmcnt(0)\n" \
  "s_barrier\n" \
  "ds_read_b128 v[32:35], v12 offset:0\n" \
  "ds_read_b128 v[56:59], v12 offset:1024\n" \
  "ds_read_b128 v[36:39], v12 offset:2048\n" \
  "ds_read_b128 v[60:63], v12 offset:3072\n" \
  "ds_read_b128 v[40:43], v12 offset:4096\n" \
  "ds_read_b128 v[64:67], v12 offset:5120\n" \
  "ds_read_b128 v[44:47], v12 offset:6144\n" \
  "ds_read_b128 v[68:71], v12 offset:7168\n" \
  "s_waitcnt lgkmcnt(4)\n" \
  "ds_read_b64 v[48:49], v12 offset:8192\n" \
  "ds_read_b64 v[72:73], v12 offset:8200\n" \
  "ds_read_b64 v[50:51], v12 offset:9216\n" \
  "ds_read_b64 v[74:75], v12 offset:9224\n" \
  "ds_read_b64 v[52:53], v12 offset:10240\n" \
  "ds_read_b64 v[76:77], v12 offset:10248\n" \
  "ds_read_b64 v[54:55], v12 offset:11264\n" \
  "ds_read_b64 v[78:79], v12 offset:11272\n" \
  "s_waitcnt lgkmcnt(0)\n" \
  "LVL_%=:\n" \
  "s_mov_b32 s77, s69\n" \
  "v_readlane_b32 s68, v4, s52\n" \
  "v_readlane_b32 s69, v5, s52\n" \
  "v_mfma_f32_16x16x32_bf16 v[224:227], v[32:35], a[0:3], 0\n" \
  "v_mfma_f32_16x16x32_bf16 v[228:231], v[56:59], a[0:3], 0\n" \
  "v_mfma_f32_16x16x32_bf16 v[224:227], v[36:39], a[4:7], v[224:227]\n" \
  "v_cvt_pk_bf16_f32 v252, v232, v233\n" \
  "v_mfma_f32_16x16x32_bf16 v[228:231], v[60:63], a[4:7], v[228:231]\n" \
  "v_cvt_pk_bf16_f32 v253, v234, v235\n" \
  "v_mfma_f32_16x16x32_bf16 v[224:227], v[40:43], a[8:11], v[224:227]\n" \
  "v_cvt_pk_bf16_f32 v254, v236, v237\n" \
  "v_mfma_f32_16x16x32_bf16 v[228:231], v[64:67], a[8:11], v[228:231]\n" \
  "v_cvt_pk_bf16_f32 v255, v238, v239\n" \
  "v_mfma_f32_16x16x32_bf16 v[224:227], v[44:47], a[12:15], v[224:227]\n" \
  "s_add_u32 s72, s77, 589824\n" \
  "v_mfma_f32_16x16x32_bf16 v[228:231], v[68:71], a[12:15], v[228:231]\n" \
  "buffer_store_dwordx4 v[252:255], v3, s[36:39], s72 offen sc1\n" \
  "v_mfma_f32_16x16x32_bf16 v[224:227], v[48:51], a[16:19], v[224:227]\n" \
  "s_waitcnt vmcnt(7)\n" \
  "v_mfma_f32_16x16x32_bf16 v[228:231], v[72:75], a[16:19], v[228:231]\n" \
  "v_cmp_ge_u32 vcc, s76, v11\n" \
  "v_mfma_f32_16x16x32_bf16 v[224:227], v[52:55], a[20:23], v[224:227]\n" \
  "v_mfma_f32_16x16x32_bf16 v[228:231], v[76:79], a[20:23], v[228:231]\n" \
  "s_cbranch_vccnz LVP0_%=\n" \
  "LVK0_%=:\n" \
  "v_mfma_f32_16x16x32_bf16 v[232:235], v[32:35], a[24:27], 0\n" \
  "v_mfma_f32_16x16x32_bf16 v[236:239], v[56:59], a[24:27], 0\n" \
  "v_cvt_pk_bf16_f32 v248, v224, v225\n" \
  "v_mfma_f32_16x16x32_bf16 v[232:235], v[36:39], a[28:31], v[232:235]\n" \
  "v_cvt_pk_bf16_f32 v249, v226, v227\n" \
  "v_mfma_f32_16x16x32_bf16 v[236:239], v[60:63], a[28:31], v[236:239]\n" \
  "v_cvt_pk_bf16_f32 v250, v228, v229\n" \
  "v_mfma_f32_16x16x32_bf16 v[232:235], v[40:43], a[32:35], v[232:235]\n" \
  "v_cvt_pk_bf16_f32 v251, v230, v231\n" \
  "v_mfma_f32_16x16x32_bf16 v[236:239], v[64:67], a[32:35], v[236:239]\n" \
  "s_add_u32 s72, s68, 0\n" \
  "v_mfma_f32_16x16x32_bf16 v[232:235], v[44:47], a[36:39], v[232:235]\n" \
  "buffer_store_dwordx4 v[248:251], v3, s[36:39], s72 offen sc1\n" \
  "v_mfma_f32_16x16x32_bf16 v[236:239], v[68:71], a[36:39], v[236:239]\n" \
  "v_readlane_b32 s71, v6, s76\n" \
  "v_mfma_f32_16x16x32_bf16 v[232:235], v[48:51], a[40:43], v[232:235]\n" \
  "s_add_u32 m0, s73, 0\n" \
  "v_mfma_f32_16x16x32_bf16 v[236:239], v[72:75], a[40:43], v[236:239]\n" \
  "s_nop 2\n" \
  "v_mfma_f32_16x16x32_bf16 v[232:235], v[52:55], a[44:47], v[232:235]\n" \
  "buffer_load_dwordx4 v1, s[36:39], s71 offen offset:0 sc1 lds\n" \
  "v_mfma_f32_16x16x32_bf16 v[236:239], v[76:79], a[44:47], v[236:239]\n" \
  "buffer_load_dwordx4 v1, s[36:39], s71 offen offset:1024 sc1 lds\n" \
  "v_mfma_f32_16x16x32_bf16 v[224:227], v[32:35], a[48:51], 0\n" \
  "v_mfma_f32_16x16x32_bf16 v[228:231], v[56:59], a[48:51], 0\n" \
  "v_mfma_f32_16x16x32_bf16 v[224:227], v[36:39], a[52:55], v[224:227]\n" \
  "v_cvt_pk_bf16_f32 v252, v232, v233\n" \
  "v_mfma_f32_16x16x32_bf16 v[228:231], v[60:63], a[52:55], v[228:231]\n" \
  "v_cvt_pk_bf16_f32 v253, v234, v235\n" \
  "v_mfma_f32_16x16x32_bf16 v[224:227], v[40:43], a[56:59], v[224:227]\n" \
  "v_cvt_pk_bf16_f32 v254, v236, v237\n" \
  "v_mfma_f32_16x16x32_bf16 v[228:231], v[64:67], a[56:59], v[228:231]\n" \
  "v_cvt_pk_bf16_f32 v255, v238, v239\n" \
  "v_mfma_f32_16x16x32_bf16 v[224:227], v[44:47], a[60:63], v[224:227]\n" \
  "s_add_u32 s72, s68, 65536\n" \
  "v_mfma_f32_16x16x32_bf16 v[228:231], v[68:71], a[60:63], v[228:231]\n" \
  "buffer_store_dwordx4 v[252:255], v3, s[36:39], s72 offen sc1\n" \
  "v_mfma_f32_16x16x32_bf16 v[224:227], v[48:51], a[64:67], v[224:227]\n" \
  "s_add_u32 m0, s74, 0\n" \
  "v_mfma_f32_16x16x32_bf16 v[228:231], v[72:75], a[64:67], v[228:231]\n" \
  "s_add_u32 s76, s76, 1\n" \
  "v_mfma_f32_16x16x32_bf16 v[224:227], v[52:55], a[68:71], v[224:227]\n" \
  "buffer_load_dwordx4 v2, s[36:39], s71 offen sc1 lds\n" \
  "v_mfma_f32_16x16x32_bf16 v[228:231], v[76:79], a[68:71], v[228:231]\n" \
  "s_min_u32 s76, s76, 63\n" \
  "v_mfma_f32_16x16x32_bf16 v[232:235], v[32:35], a[72:75], 0\n" \
  "v_mfma_f32_16x16x32_bf16 v[236:239], v[56:59], a[72:75], 0\n" \
  "v_mfma_f32_16x16x32_bf16 v[232:235], v[36:39], a[76:79], v[232:235]\n" \
  "v_cvt_pk_bf16_f32 v248, v224, v225\n" \
  "v_mfma_f32_16x16x32_bf16 v[236:239], v[60:63], a[76:79], v[236:239]\n" \
  "v_cvt_pk_bf16_f32 v249, v226, v227\n" \
  "v_mfma_f32_16x16x32_bf16 v[232:235], v[40:43], a[80:83], v[232:235]\n" \
  "v_cvt_pk_bf16_f32 v250, v228, v229\n" \
  "v_mfma_f32_16x16x32_bf16 v[236:239], v[64:67], a[80:83], v[236:239]\n" \
  "v_cvt_pk_bf16_f32 v251, v230, v231\n" \
  "v_mfma_f32_16x16x32_bf16 v[232:235], v[44:47], a[84:87], v[232:235]\n" \
  "s_add_u32 s72, s69, 131072\n" \
  "v_mfma_f32_16x16x32_bf16 v[236:239], v[68:71], a[84:87], v[236:239]\n" \
  "buffer_store_dwordx4 v[248:251], v3, s[36:39], s72 offen sc1\n" \
  "v_mfma_f32_16x16x32_bf16 v[232:235], v[48:51], a[88:91], v[232:235]\n" \
  "v_mfma_f32_16x16x32_bf16 v[236:239], v[72:75], a[88:91], v[236:239]\n" \
  "v_mfma_f32_16x16x32_bf16 v[232:235], v[52:55], a[92:95], v[232:235]\n" \
  "v_mfma_f32_16x16x32_bf16 v[236:239], v[76:79], a[92:95], v[236:239]\n" \
  "v_mfma_f32_16x16x32_bf16 v[224:227], v[32:35], a[96:99], 0\n" \
  "v_mfma_f32_16x16x32_bf16 v[228:231], v[56:59], a[96:99], 0\n" \
  "v_mfma_f32_16x16x32_bf16 v[224:227], v[36:39], a[100:103], v[224:227]\n" \
  "v_cvt_pk_bf16_f32 v252, v232, v233\n" \
  "v_mfma_f32_16x16x32_bf16 v[228:231], v[60:63], a[100:103], v[228:231]\n" \
  "v_cvt_pk_bf16_f32 v253, v234, v235\n" \
  "v_mfma_f32_16x16x32_bf16 v[224:227], v[40:43], a[104:107], v[224:227]\n" \
  "v_cvt_pk_bf16_f32 v254, v236, v237\n" \
  "v_mfma_f32_16x16x32_bf16 v[228:231], v[64:67], a[104:107], v[228:231]\n" \
  "v_cvt_pk_bf16_f32 v255, v238, v239\n" \
  "v_mfma_f32_16x16x32_bf16 v[224:227], v[44:47], a[108:111], v[224:227]\n" \
  "s_add_u32 s72, s69, 196608\n" \
  "v_mfma_f32_16x16x32_bf16 v[228:231], v[68:71], a[108:111], v[228:231]\n" \
  "buffer_store_dwordx4 v[252:255], v3, s[36:39], s72 offen sc1\n" \
  "v_mfma_f32_16x16x32_bf16 v[224:227], v[48:51], a[112:115], v[224:227]\n" \
  "buffer_load_dword v11, v9, s[36:39], 0 offen sc1\n" \
  "v_mfma_f32_16x16x32_bf16 v[228:231], v[72:75], a[112:115], v[228:231]\n" \
  "v_mfma_f32_16x16x32_bf16 v[224:227], v[52:55], a[116:119], v[224:227]\n" \
  "v_mfma_f32_16x16x32_bf16 v[228:231], v[76:79], a[116:119], v[228:231]\n" \
  "v_mfma_f32_16x16x32_bf16 v[232:235], v[32:35], a[120:123], 0\n" \
  "v_mfma_f32_16x16x32_bf16 v[236:239], v[56:59], a[120:123], 0\n" \
  "v_mfma_f32_16x16x32_bf16 v[232:235], v[36:39], a[124:127], v[232:235]\n" \
  "v_cvt_pk_bf16_f32 v248, v224, v225\n" \
  "v_mfma_f32_16x16x32_bf16 v[236:239], v[60:63], a[124:127], v[236:239]\n" \
  "v_cvt_pk_bf16_f32 v249, v226, v227\n" \
  "v_mfma_f32_16x16x32_bf16 v[232:235], v[40:43], a[128:131], v[232:235]\n" \
  "v_cvt_pk_bf16_f32 v250, v228, v229\n" \
  "v_mfma_f32_16x16x32_bf16 v[236:239], v[64:67], a[128:131], v[236:239]\n" \
  "v_cvt_pk_bf16_f32 v251, v230, v231\n" \
  "v_mfma_f32_16x16x32_bf16 v[232:235], v[44:47], a[132:135], v[232:235]\n" \
  "s_add_u32 s72, s69, 262144\n" \
  "v_mfma_f32_16x16x32_bf16 v[236:239], v[68:71], a[132:135], v[236:239]\n" \
  "buffer_store_dwordx4 v[248:251], v3, s[36:39], s72 offen sc1\n" \
  "v_mfma_f32_16x16x32_bf16 v[232:235], v[48:51], a[136:139], v[232:235]\n" \
  "v_mfma_f32_16x16x32_bf16 v[236:239], v[72:75], a[136:139], v[236:239]\n" \
  "v_mfma_f32_16x16x32_bf16 v[232:235], v[52:55], a[140:143], v[232:235]\n" \
  "v_mfma_f32_16x16x32_bf16 v[236:239], v[76:79], a[140:143], v[236:239]\n" \
  "v_mfma_f32_16x16x32_bf16 v[224:227], v[32:35], a[144:147], 0\n" \
  "v_mfma_f32_16x16x32_bf16 v[228:231], v[56:59], a[144:147], 0\n" \
  "v_mfma_f32_16x16x32_bf16 v[224:227], v[36:39], a[148:151], v[224:227]\n" \
  "v_cvt_pk_bf16_f32 v252, v232, v233\n" \
  "v_mfma_f32_16x16x32_bf16 v[228:231], v[60:63], a[148:151], v[228:231]\n" \
  "v_cvt_pk_bf16_f32 v253, v234, v235\n" \
  "v_mfma_f32_16x16x32_bf16 v[224:227], v[40:43], a[152:155], v[224:227]\n" \
  "v_cvt_pk_bf16_f32 v254, v236, v237\n" \
  "v_mfma_f32_16x16x32_bf16 v[228:231], v[64:67], a[152:155], v[228:231]\n" \
  "v_cvt_pk_bf16_f32 v255, v238, v239\n" \
  "v_mfma_f32_16x16x32_bf16 v[224:227], v[44:47], a[156:159], v[224:227]\n" \
  "s_add_u32 s72, s69, 327680\n" \
  "v_mfma_f32_16x16x32_bf16 v[228:231], v[68:71], a[156:159], v[228:231]\n" \
  "buffer_store_dwordx4 v[252:255], v3, s[36:39], s72 offen sc1\n" \
  "v_mfma_f32_16x16x32_bf16 v[224:227], v[48:51], a[160:163], v[224:227]\n" \
  "v_mov_b32 v10, s52\n" \
  "v_mfma_f32_16x16x32_bf16 v[228:231], v[72:75], a[160:163], v[228:231]\n" \
  "s_waitcnt vmcnt(10)\n" \
  "v_mfma_f32_16x16x32_bf16 v[224:227], v[52:55], a[164:167], v[224:227]\n" \
  "buffer_store_dword v10, v7, s[36:39], 0 offen sc1\n" \
  "v_mfma_f32_16x16x32_bf16 v[228:231], v[76:79], a[164:167], v[228:231]\n" \
  "s_barrier\n" \
  "v_mfma_f32_16x16x32_bf16 v[232:235], v[32:35], a[168:171], 0\n" \
  "v_mfma_f32_16x16x32_bf16 v[236:239], v[56:59], a[168:171], 0\n" \
  "v_cvt_pk_bf16_f32 v248, v224, v225\n" \
  "v_cvt_pk_bf16_f32 v249, v226, v227\n" \
  "v_mfma_f32_16x16x32_bf16 v[232:235], v[36:39], a[172:175], v[232:235]\n" \
  "v_cvt_pk_bf16_f32 v250, v228, v229\n" \
  "v_cvt_pk_bf16_f32 v251, v230, v231\n" \
  "v_mfma_f32_16x16x32_bf16 v[236:239], v[60:63], a[172:175], v[236:239]\n" \
  "s_add_u32 s72, s69, 393216\n" \
  "v_mfma_f32_16x16x32_bf16 v[232:235], v[40:43], a[176:179], v[232:235]\n" \
  "buffer_store_dwordx4 v[248:251], v3, s[36:39], s72 offen sc1\n" \
  "v_mfma_f32_16x16x32_bf16 v[236:239], v[64:67], a[176:179], v[236:239]\n" \
  "ds_read_b128 v[80:83], v12 offset:16384\n" \
  "v_mfma_f32_16x16x32_bf16 v[232:235], v[44:47], a[180:183], v[232:235]\n" \
  "ds_read_b128 v[104:107], v12 offset:17408\n" \
  "v_mfma_f32_16x16x32_bf16 v[236:239], v[68:71], a[180:183], v[236:239]\n" \
  "ds_read_b128 v[84:87], v12 offset:18432\n" \
  "v_mfma_f32_16x16x32_bf16 v[232:235], v[48:51], a[184:187], v[232:235]\n" \
  "ds_read_b128 v[108:111], v12 offset:19456\n" \
  "v_mfma_f32_16x16x32_bf16 v[236:239], v[72:75], a[184:187], v[236:239]\n" \
  "ds_read_b128 v[88:91], v12 offset:20480\n" \
  "v_mfma_f32_16x16x32_bf16 v[232:235], v[52:55], a[188:191], v[232:235]\n" \
  "ds_read_b128 v[112:115], v12 offset:21504\n" \
  "v_mfma_f32_16x16x32_bf16 v[236:239], v[76:79], a[188:191], v[236:239]\n" \
  "ds_read_b128 v[92:95], v12 offset:22528\n" \
  "v_mfma_f32_16x16x32_bf16 v[224:227], v[32:35], a[192:195], 0\n" \
  "v_mfma_f32_16x16x32_bf16 v[228:231], v[56:59], a[192:195], 0\n" \
  "v_cvt_pk_bf16_f32 v252, v232, v233\n" \
  "v_cvt_pk_bf16_f32 v253, v234, v235\n" \
  "v_mfma_f32_16x16x32_bf16 v[224:227], v[36:39], a[196:199], v[224:227]\n" \
  "v_cvt_pk_bf16_f32 v254, v236, v237\n" \
  "v_cvt_pk_bf16_f32 v255, v238, v239\n" \
  "v_mfma_f32_16x16x32_bf16 v[228:231], v[60:63], a[196:199], v[228:231]\n" \
  "s_add_u32 s72, s69, 458752\n" \
  "v_mfma_f32_16x16x32_bf16 v[224:227], v[40:43], a[200:203], v[224:227]\n" \
  "buffer_store_dwordx4 v[252:255], v3, s[36:39], s72 offen sc1\n" \
  "v_mfma_f32_16x16x32_bf16 v[228:231], v[64:67], a[200:203], v[228:231]\n" \
  "ds_read_b128 v[116:119], v12 offset:23552\n" \
  "v_mfma_f32_16x16x32_bf16 v[224:227], v[44:47], a[204:207], v[224:227]\n" \
  "ds_read_b64 v[96:97], v12 offset:24576\n" \
  "v_mfma_f32_16x16x32_bf16 v[228:231], v[68:71], a[204:207], v[228:231]\n" \
  "ds_read_b64 v[120:121], v12 offset:24584\n" \
  "v_mfma_f32_16x16x32_bf16 v[224:227], v[48:51], a[208:211], v[224:227]\n" \
  "ds_read_b64 v[98:99], v12 offset:25600\n" \
  "v_mfma_f32_16x16x32_bf16 v[228:231], v[72:75], a[208:211], v[228:231]\n" \
  "ds_read_b64 v[122:123], v12 offset:25608\n" \
  "v_mfma_f32_16x16x32_bf16 v[224:227], v[52:55], a[212:215], v[224:227]\n" \
  "ds_read_b64 v[100:101], v12 offset:26624\n" \
  "v_mfma_f32_16x16x32_bf16 v[228:231], v[76:79], a[212:215], v[228:231]\n" \
  "ds_read_b64 v[124:125], v12 offset:26632\n" \
  "v_mfma_f32_16x16x32_bf16 v[232:235], v[32:35], a[216:219], 0\n" \
  "v_mfma_f32_16x16x32_bf16 v[236:239], v[56:59], a[216:219], 0\n" \
  "v_mfma_f32_16x16x32_bf16 v[232:235], v[36:39], a[220:223], v[232:235]\n" \
  "v_cvt_pk_bf16_f32 v248, v224, v225\n" \
  "v_mfma_f32_16x16x32_bf16 v[236:239], v[60:63], a[220:223], v[236:239]\n" \
  "v_cvt_pk_bf16_f32 v249, v226, v227\n" \
  "v_mfma_f32_16x16x32_bf16 v[232:235], v[40:43], a[224:227], v[232:235]\n" \
  "v_cvt_pk_bf16_f32 v250, v228, v229\n" \
  "v_mfma_f32_16x16x32_bf16 v[236:239], v[64:67], a[224:227], v[236:239]\n" \
  "v_cvt_pk_bf16_f32 v251, v230, v231\n" \
  "v_mfma_f32_16x16x32_bf16 v[232:235], v[44:47], a[228:231], v[232:235]\n" \
  "s_add_u32 s72, s69, 524288\n" \
  "v_mfma_f32_16x16x32_bf16 v[236:239], v[68:71], a[228:231], v[236:239]\n" \
  "buffer_store_dwordx4 v[248:251], v3, s[36:39], s72 offen sc1\n" \
  "v_mfma_f32_16x16x32_bf16 v[232:235], v[48:51], a[232:235], v[232:235]\n" \
  "ds_read_b64 v[102:103], v12 offset:27648\n" \
  "v_mfma_f32_16x16x32_bf16 v[236:239], v[72:75], a[232:235], v[236:239]\n" \
  "ds_read_b64 v[126:127], v12 offset:27656\n" \
  "v_mfma_f32_16x16x32_bf16 v[232:235], v[52:55], a[236:239], v[232:235]\n" \
  "s_add_u32 s52, s52, 1\n" \
  "v_mfma_f32_16x16x32_bf16 v[236:239], v[76:79], a[236:239], v[236:239]\n" \
  "s_cmp_eq_u32 s52, s53\n" \
  "s_waitcnt lgkmcnt(0)\n" \
  "s_mov_b32 s77, s69\n" \
  "v_readlane_b32 s68, v4, s52\n" \
  "v_readlane_b32 s69, v5, s52\n" \
  "v_mfma_f32_16x16x32_bf16 v[224:227], v[80:83], a[0:3], 0\n" \
  "v_mfma_f32_16x16x32_bf16 v[228:231], v[104:107], a[0:3], 0\n" \
  "v_mfma_f32_16x16x32_bf16 v[224:227], v[84:87], a[4:7], v[224:227]\n" \
  "v_cvt_pk_bf16_f32 v252, v232, v233\n" \
  "v_mfma_f32_16x16x32_bf16 v[228:231], v[108:111], a[4:7], v[228:231]\n" \
  "v_cvt_pk_bf16_f32 v253, v234, v235\n" \
  "v_mfma_f32_16x16x32_bf16 v[224:227], v[88:91], a[8:11], v[224:227]\n" \
  "v_cvt_pk_bf16_f32 v254, v236, v237\n" \
  "v_mfma_f32_16x16x32_bf16 v[228:231], v[112:115], a[8:11], v[228:231]\n" \
  "v_cvt_pk_bf16_f32 v255, v238, v239\n" \
  "v_mfma_f32_16x16x32_bf16 v[224:227], v[92:95], a[12:15], v[224:227]\n" \
  "s_add_u32 s72, s77, 589824\n" \
  "v_mfma_f32_16x16x32_bf16 v[228:231], v[116:119], a[12:15], v[228:231]\n" \
  "buffer_store_dwordx4 v[252:255], v3, s[36:39], s72 offen sc1\n" \
  "v_mfma_f32_16x16x32_bf16 v[224:227], v[96:99], a[16:19], v[224:227]\n" \
  "s_waitcnt vmcnt(7)\n" \
  "v_mfma_f32_16x16x32_bf16 v[228:231], v[120:123], a[16:19], v[228:231]\n" \
  "v_cmp_ge_u32 vcc, s76, v11\n" \
  "v_mfma_f32_16x16x32_bf16 v[224:227], v[100:103], a[20:23], v[224:227]\n" \
  "v_mfma_f32_16x16x32_bf16 v[228:231], v[124:127], a[20:23], v[228:231]\n" \
  "s_cbranch_vccnz LVP1_%=\n" \
  "LVK1_%=:\n" \
  "v_mfma_f32_16x16x32_bf16 v[232:235], v[80:83], a[24:27], 0\n" \
  "v_mfma_f32_16x16x32_bf16 v[236:239], v[104:107], a[24:27], 0\n" \
  "v_cvt_pk_bf16_f32 v248, v224, v225\n" \
  "v_mfma_f32_16x16x32_bf16 v[232:235], v[84:87], a[28:31], v[232:235]\n" \
  "v_cvt_pk_bf16_f32 v249, v226, v227\n" \
  "v_mfma_f32_16x16x32_bf16 v[236:239], v[108:111], a[28:31], v[236:239]\n" \
  "v_cvt_pk_bf16_f32 v250, v228, v229\n" \
  "v_mfma_f32_16x16x32_bf16 v[232:235], v[88:91], a[32:35], v[232:235]\n" \
  "v_cvt_pk_bf16_f32 v251, v230, v231\n" \
  "v_mfma_f32_16x16x32_bf16 v[236:239], v[112:115], a[32:35], v[236:239]\n" \
  "s_add_u32 s72, s68, 0\n" \
  "v_mfma_f32_16x16x32_bf16 v[232:235], v[92:95], a[36:39], v[232:235]\n" \
  "buffer_store_dwordx4 v[248:251], v3, s[36:39], s72 offen sc1\n" \
  "v_mfma_f32_16x16x32_bf16 v[236:239], v[116:119], a[36:39], v[236:239]\n" \
  "v_readlane_b32 s71, v6, s76\n" \
  "v_mfma_f32_16x16x32_bf16 v[232:235], v[96:99], a[40:43], v[232:235]\n" \
  "s_add_u32 m0, s73, 16384\n" \
  "v_mfma_f32_16x16x32_bf16 v[236:239], v[120:123], a[40:43], v[236:239]\n" \
  "s_nop 2\n" \
  "v_mfma_f32_16x16x32_bf16 v[232:235], v[100:103], a[44:47], v[232:235]\n" \
  "buffer_load_dwordx4 v1, s[36:39], s71 offen offset:0 sc1 lds\n" \
  "v_mfma_f32_16x16x32_bf16 v[236:239], v[124:127], a[44:47], v[236:239]\n" \
  "buffer_load_dwordx4 v1, s[36:39], s71 offen offset:1024 sc1 lds\n" \
  "v_mfma_f32_16x16x32_bf16 v[224:227], v[80:83], a[48:51], 0\n" \
  "v_mfma_f32_16x16x32_bf16 v[228:231], v[104:107], a[48:51], 0\n" \
  "v_mfma_f32_16x16x32_bf16 v[224:227], v[84:87], a[52:55], v[224:227]\n" \
  "v_cvt_pk_bf16_f32 v252, v232, v233\n" \
  "v_mfma_f32_16x16x32_bf16 v[228:231], v[108:111], a[52:55], v[228:231]\n" \
  "v_cvt_pk_bf16_f32 v253, v234, v235\n" \
  "v_mfma_f32_16x16x32_bf16 v[224:227], v[88:91], a[56:59], v[224:227]\n" \
  "v_cvt_pk_bf16_f32 v254, v236, v237\n" \
  "v_mfma_f32_16x16x32_bf16 v[228:231], v[112:115], a[56:59], v[228:231]\n" \
  "v_cvt_pk_bf16_f32 v255, v238, v239\n" \
  "v_mfma_f32_16x16x32_bf16 v[224:227], v[92:95], a[60:63], v[224:227]\n" \
  "s_add_u32 s72, s68, 65536\n" \
  "v_mfma_f32_16x16x32_bf16 v[228:231], v[116:119], a[60:63], v[228:231]\n" \
  "buffer_store_dwordx4 v[252:255], v3, s[36:39], s72 offen sc1\n" \
  "v_mfma_f32_16x16x32_bf16 v[224:227], v[96:99], a[64:67], v[224:227]\n" \
  "s_add_u32 m0, s74, 16384\n" \
  "v_mfma_f32_16x16x32_bf16 v[228:231], v[120:123], a[64:67], v[228:231]\n" \
  "s_add_u32 s76, s76, 1\n" \
  "v_mfma_f32_16x16x32_bf16 v[224:227], v[100:103], a[68:71], v[224:227]\n" \
  "buffer_load_dwordx4 v2, s[36:39], s71 offen sc1 lds\n" \
  "v_mfma_f32_16x16x32_bf16 v[228:231], v[124:127], a[68:71], v[228:231]\n" \
  "s_min_u32 s76, s76, 63\n" \
  "v_mfma_f32_16x16x32_bf16 v[232:235], v[80:83], a[72:75], 0\n" \
  "v_mfma_f32_16x16x32_bf16 v[236:239], v[104:107], a[72:75], 0\n" \
  "v_mfma_f32_16x16x32_bf16 v[232:235], v[84:87], a[76:79], v[232:235]\n" \
  "v_cvt_pk_bf16_f32 v248, v224, v225\n" \
  "v_mfma_f32_16x16x32_bf16 v[236:239], v[108:111], a[76:79], v[236:239]\n" \
  "v_cvt_pk_bf16_f32 v249, v226, v227\n" \
  "v_mfma_f32_16x16x32_bf16 v[232:235], v[88:91], a[80:83], v[232:235]\n" \
  "v_cvt_pk_bf16_f32 v250, v228, v229\n" \
  "v_mfma_f32_16x16x32_bf16 v[236:239], v[112:115], a[80:83], v[236:239]\n" \
  "v_cvt_pk_bf16_f32 v251, v230, v231\n" \
  "v_mfma_f32_16x16x32_bf16 v[232:235], v[92:95], a[84:87], v[232:235]\n" \
  "s_add_u32 s72, s69, 131072\n" \
  "v_mfma_f32_16x16x32_bf16 v[236:239], v[116:119], a[84:87], v[236:239]\n" \
  "buffer_store_dwordx4 v[248:251], v3, s[36:39], s72 offen sc1\n" \
  "v_mfma_f32_16x16x32_bf16 v[232:235], v[96:99], a[88:91], v[232:235]\n" \
  "v_mfma_f32_16x16x32_bf16 v[236:239], v[120:123], a[88:91], v[236:239]\n" \
  "v_mfma_f32_16x16x32_bf16 v[232:235], v[100:103], a[92:95], v[232:235]\n" \
  "v_mfma_f32_16x16x32_bf16 v[236:239], v[124:127], a[92:95], v[236:239]\n" \
  "v_mfma_f32_16x16x32_bf16 v[224:227], v[80:83], a[96:99], 0\n" \
  "v_mfma_f32_16x16x32_bf16 v[228:231], v[104:107], a[96:99], 0\n" \
  "v_mfma_f32_16x16x32_bf16 v[224:227], v[84:87], a[100:103], v[224:227]\n" \
  "v_cvt_pk_bf16_f32 v252, v232, v233\n" \
  "v_mfma_f32_16x16x32_bf16 v[228:231], v[108:111], a[100:103], v[228:231]\n" \
  "v_cvt_pk_bf16_f32 v253, v234, v235\n" \
  "v_mfma_f32_16x16x32_bf16 v[224:227], v[88:91], a[104:107], v[224:227]\n" \
  "v_cvt_pk_bf16_f32 v254, v236, v237\n" \
  "v_mfma_f32_16x16x32_bf16 v[228:231], v[112:115], a[104:107], v[228:231]\n" \
  "v_cvt_pk_bf16_f32 v255, v238, v239\n" \
  "v_mfma_f32_16x16x32_bf16 v[224:227], v[92:95], a[108:111], v[224:227]\n" \
  "s_add_u32 s72, s69, 196608\n" \
  "v_mfma_f32_16x16x32_bf16 v[228:231], v[116:119], a[108:111], v[228:231]\n" \
  "buffer_store_dwordx4 v[252:255], v3, s[36:39], s72 offen sc1\n" \
  "v_mfma_f32_16x16x32_bf16 v[224:227], v[96:99], a[112:115], v[224:227]\n" \
  "buffer_load_dword v11, v9, s[36:39], 0 offen sc1\n" \
  "v_mfma_f32_16x16x32_bf16 v[228:231], v[120:123], a[112:115], v[228:231]\n" \
  "v_mfma_f32_16x16x32_bf16 v[224:227], v[100:103], a[116:119], v[224:227]\n" \
  "v_mfma_f32_16x16x32_bf16 v[228:231], v[124:127], a[116:119], v[228:231]\n" \
  "v_mfma_f32_16x16x32_bf16 v[232:235], v[80:83], a[120:123], 0\n" \
  "v_mfma_f32_16x16x32_bf16 v[236:239], v[104:107], a[120:123], 0\n" \
  "v_mfma_f32_16x16x32_bf16 v[232:235], v[84:87], a[124:127], v[232:235]\n" \
  "v_cvt_pk_bf16_f32 v248, v224, v225\n" \
  "v_mfma_f32_16x16x32_bf16 v[236:239], v[108:111], a[124:127], v[236:239]\n" \
  "v_cvt_pk_bf16_f32 v249, v226, v227\n" \
  "v_mfma_f32_16x16x32_bf16 v[232:235], v[88:91], a[128:131], v[232:235]\n" \
  "v_cvt_pk_bf16_f32 v250, v228, v229\n" \
  "v_mfma_f32_16x16x32_bf16 v[236:239], v[112:115], a[128:131], v[236:239]\n" \
  "v_cvt_pk_bf16_f32 v251, v230, v231\n" \
  "v_mfma_f32_16x16x32_bf16 v[232:235], v[92:95], a[132:135], v[232:235]\n" \
  "s_add_u32 s72, s69, 262144\n" \
  "v_mfma_f32_16x16x32_bf16 v[236:239], v[116:119], a[132:135], v[236:239]\n" \
  "buffer_store_dwordx4 v[248:251], v3, s[36:39], s72 offen sc1\n" \
  "v_mfma_f32_16x16x32_bf16 v[232:235], v[96:99], a[136:139], v[232:235]\n" \
  "s_xor_b32 s73, s73, 0x8000\n" \
  "v_mfma_f32_16x16x32_bf16 v[236:239], v[120:123], a[136:139], v[236:239]\n" \
  "s_xor_b32 s74, s74, 0x8000\n" \
  "v_mfma_f32_16x16x32_bf16 v[232:235], v[100:103], a[140:143], v[232:235]\n" \
  "v_xor_b32 v12, 0x8000, v12\n" \
  "v_mfma_f32_16x16x32_bf16 v[236:239], v[124:127], a[140:143], v[236:239]\n" \
  "v_mfma_f32_16x16x32_bf16 v[224:227], v[80:83], a[144:147], 0\n" \
  "v_mfma_f32_16x16x32_bf16 v[228:231], v[104:107], a[144:147], 0\n" \
  "v_mfma_f32_16x16x32_bf16 v[224:227], v[84:87], a[148:151], v[224:227]\n" \
  "v_cvt_pk_bf16_f32 v252, v232, v233\n" \
  "v_mfma_f32_16x16x32_bf16 v[228:231], v[108:111], a[148:151], v[228:231]\n" \
  "v_cvt_pk_bf16_f32 v253, v234, v235\n" \
  "v_mfma_f32_16x16x32_bf16 v[224:227], v[88:91], a[152:155], v[224:227]\n" \
  "v_cvt_pk_bf16_f32 v254, v236, v237\n" \
  "v_mfma_f32_16x16x32_bf16 v[228:231], v[112:115], a[152:155], v[228:231]\n" \
  "v_cvt_pk_bf16_f32 v255, v238, v239\n" \
  "v_mfma_f32_16x16x32_bf16 v[224:227], v[92:95], a[156:159], v[224:227]\n" \
  "s_add_u32 s72, s69, 327680\n" \
  "v_mfma_f32_16x16x32_bf16 v[228:231], v[116:119], a[156:159], v[228:231]\n" \
  "buffer_store_dwordx4 v[252:255], v3, s[36:39], s72 offen sc1\n" \
  "v_mfma_f32_16x16x32_bf16 v[224:227], v[96:99], a[160:163], v[224:227]\n" \
  "v_mov_b32 v10, s52\n" \
  "v_mfma_f32_16x16x32_bf16 v[228:231], v[120:123], a[160:163], v[228:231]\n" \
  "s_waitcnt vmcnt(10)\n" \
  "v_mfma_f32_16x16x32_bf16 v[224:227], v[100:103], a[164:167], v[224:227]\n" \
  "buffer_store_dword v10, v7, s[36:39], 0 offen sc1\n" \
  "v_mfma_f32_16x16x32_bf16 v[228:231], v[124:127], a[164:167], v[228:231]\n" \
  "s_barrier\n" \
  "v_mfma_f32_16x16x32_bf16 v[232:235], v[80:83], a[168:171], 0\n" \
  "v_mfma_f32_16x16x32_bf16 v[236:239], v[104:107], a[168:171], 0\n" \
  "v_cvt_pk_bf16_f32 v248, v224, v225\n" \
  "v_cvt_pk_bf16_f32 v249, v226, v227\n" \
  "v_mfma_f32_16x16x32_bf16 v[232:235], v[84:87], a[172:175], v[232:235]\n" \
  "v_cvt_pk_bf16_f32 v250, v228, v229\n" \
  "v_cvt_pk_bf16_f32 v251, v230, v231\n" \
  "v_mfma_f32_16x16x32_bf16 v[236:239], v[108:111], a[172:175], v[236:239]\n" \
  "s_add_u32 s72, s69, 393216\n" \
  "v_mfma_f32_16x16x32_bf16 v[232:235], v[88:91], a[176:179], v[232:235]\n" \
  "buffer_store_dwordx4 v[248:251], v3, s[36:39], s72 offen sc1\n" \
  "v_mfma_f32_16x16x32_bf16 v[236:239], v[112:115], a[176:179], v[236:239]\n" \
  "ds_read_b128 v[32:35], v12 offset:0\n" \
  "v_mfma_f32_16x16x32_bf16 v[232:235], v[92:95], a[180:183], v[232:235]\n" \
  "ds_read_b128 v[56:59], v12 offset:1024\n" \
  "v_mfma_f32_16x16x32_bf16 v[236:239], v[116:119], a[180:183], v[236:239]\n" \
  "ds_read_b128 v[36:39], v12 offset:2048\n" \
  "v_mfma_f32_16x16x32_bf16 v[232:235], v[96:99], a[184:187], v[232:235]\n" \
  "ds_read_b128 v[60:63], v12 offset:3072\n" \
  "v_mfma_f32_16x16x32_bf16 v[236:239], v[120:123], a[184:187], v[236:239]\n" \
  "ds_read_b128 v[40:43], v12 offset:4096\n" \
  "v_mfma_f32_16x16x32_bf16 v[232:235], v[100:103], a[188:191], v[232:235]\n" \
  "ds_read_b128 v[64:67], v12 offset:5120\n" \
  "v_mfma_f32_16x16x32_bf16 v[236:239], v[124:127], a[188:191], v[236:239]\n" \
  "ds_read_b128 v[44:47], v12 offset:6144\n" \
  "v_mfma_f32_16x16x32_bf16 v[224:227], v[80:83], a[192:195], 0\n" \
  "v_mfma_f32_16x16x32_bf16 v[228:231], v[104:107], a[192:195], 0\n" \
  "v_cvt_pk_bf16_f32 v252, v232, v233\n" \
  "v_cvt_pk_bf16_f32 v253, v234, v235\n" \
  "v_mfma_f32_16x16x32_bf16 v[224:227], v[84:87], a[196:199], v[224:227]\n" \
  "v_cvt_pk_bf16_f32 v254, v236, v237\n" \
  "v_cvt_pk_bf16_f32 v255, v238, v239\n" \
  "v_mfma_f32_16x16x32_bf16 v[228:231], v[108:111], a[196:199], v[228:231]\n" \
  "s_add_u32 s72, s69, 458752\n" \
  "v_mfma_f32_16x16x32_bf16 v[224:227], v[88:91], a[200:203], v[224:227]\n" \
  "buffer_store_dwordx4 v[252:255], v3, s[36:39], s72 offen sc1\n" \
  "v_mfma_f32_16x16x32_bf16 v[228:231], v[112:115], a[200:203], v[228:231]\n" \
  "ds_read_b128 v[68:71], v12 offset:7168\n" \
  "v_mfma_f32_16x16x32_bf16 v[224:227], v[92:95], a[204:207], v[224:227]\n" \
  "ds_read_b64 v[48:49], v12 offset:8192\n" \
  "v_mfma_f32_16x16x32_bf16 v[228:231], v[116:119], a[204:207], v[228:231]\n" \
  "ds_read_b64 v[72:73], v12 offset:8200\n" \
  "v_mfma_f32_16x16x32_bf16 v[224:227], v[96:99], a[208:211], v[224:227]\n" \
  "ds_read_b64 v[50:51], v12 offset:9216\n" \
  "v_mfma_f32_16x16x32_bf16 v[228:231], v[120:123], a[208:211], v[228:231]\n" \
  "ds_read_b64 v[74:75], v12 offset:9224\n" \
  "v_mfma_f32_16x16x32_bf16 v[224:227], v[100:103], a[212:215], v[224:227]\n" \
  "ds_read_b64 v[52:53], v12 offset:10240\n" \
  "v_mfma_f32_16x16x32_bf16 v[228:231], v[124:127], a[212:215], v[228:231]\n" \
  "ds_read_b64 v[76:77], v12 offset:10248\n" \
  "v_mfma_f32_16x16x32_bf16 v[232:235], v[80:83], a[216:219], 0\n" \
  "v_mfma_f32_16x16x32_bf16 v[236:239], v[104:107], a[216:219], 0\n" \
  "v_mfma_f32_16x16x32_bf16 v[232:235], v[84:87], a[220:223], v[232:235]\n" \
  "v_cvt_pk_bf16_f32 v248, v224, v225\n" \
  "v_mfma_f32_16x16x32_bf16 v[236:239], v[108:111], a[220:223], v[236:239]\n" \
  "v_cvt_pk_bf16_f32 v249, v226, v227\n" \
  "v_mfma_f32_16x16x32_bf16 v[232:235], v[88:91], a[224:227], v[232:235]\n" \
  "v_cvt_pk_bf16_f32 v250, v228, v229\n" \
  "v_mfma_f32_16x16x32_bf16 v[236:239], v[112:115], a[224:227], v[236:239]\n" \
  "v_cvt_pk_bf16_f32 v251, v230, v231\n" \
  "v_mfma_f32_16x16x32_bf16 v[232:235], v[92:95], a[228:231], v[232:235]\n" \
  "s_add_u32 s72, s69, 524288\n" \
  "v_mfma_f32_16x16x32_bf16 v[236:239], v[116:119], a[228:231], v[236:239]\n" \
  "buffer_store_dwordx4 v[248:251], v3, s[36:39], s72 offen sc1\n" \
  "v_mfma_f32_16x16x32_bf16 v[232:235], v[96:99], a[232:235], v[232:235]\n" \
  "ds_read_b64 v[54:55], v12 offset:11264\n" \
  "v_mfma_f32_16x16x32_bf16 v[236:239], v[120:123], a[232:235], v[236:239]\n" \
  "ds_read_b64 v[78:79], v12 offset:11272\n" \
  "v_mfma_f32_16x16x32_bf16 v[232:235], v[100:103], a[236:239], v[232:235]\n" \
  "s_add_u32 s52, s52, 1\n" \
  "v_mfma_f32_16x16x32_bf16 v[236:239], v[124:127], a[236:239], v[236:239]\n" \
  "s_cmp_eq_u32 s52, s53\n" \
  "s_waitcnt lgkmcnt(0)\n" \
  "s_cbranch_scc0 LVL_%=\n" \
  "s_cmp_lg_u32 s38, 0\n" \
  "s_cbranch_scc1 LVB_%=\n" \
  "s_waitcnt vmcnt(0)\n" \
  "s_mov_b32 s38, s70\n" \
  "s_mov_b32 s75, 0\n" \
  "s_mov_b32 s52, 0\n" \
  "s_branch LVA_%=\n" \
  "LVB_%=:\n" \
  "s_mov_b32 s77, s69\n" \
  "s_nop 7\n" \
  "v_cvt_pk_bf16_f32 v252, v232, v233\n" \
  "v_cvt_pk_bf16_f32 v253, v234, v235\n" \
  "v_cvt_pk_bf16_f32 v254, v236, v237\n" \
  "v_cvt_pk_bf16_f32 v255, v238, v239\n" \
  "s_add_u32 s72, s77, 589824\n" \
  "buffer_store_dwordx4 v[252:255], v3, s[36:39], s72 offen sc1\n" \
  "v_mov_b32 v10, s52\n" \
  "s_waitcnt vmcnt(0)\n" \
  "buffer_store_dword v10, v7, s[36:39], 0 offen sc1\n" \
  "s_branch LVD_%=\n" \
  "LVP0_%=:\n" \
  "buffer_load_dword v11, v9, s[36:39], 0 offen sc1\n" \
  "s_waitcnt vmcnt(0)\n" \
  "v_cmp_ge_u32 vcc, s76, v11\n" \
  "s_nop 3\n" \
  "s_cbranch_vccz LVK0_%=\n" \
  "s_add_u32 s75, s75, 1\n" \
  "s_cmp_gt_u32 s75, 200000\n" \
  "s_cbranch_scc1 LVK0_%=\n" \
  "s_sleep 2\n" \
  "s_branch LVP0_%=\n" \
  "LVP1_%=:\n" \
  "buffer_load_dword v11, v9, s[36:39], 0 offen sc1\n" \
  "s_waitcnt vmcnt(0)\n" \
  "v_cmp_ge_u32 vcc, s76, v11\n" \
  "s_nop 3\n" \
  "s_cbranch_vccz LVK1_%=\n" \
  "s_add_u32 s75, s75, 1\n" \
  "s_cmp_gt_u32 s75, 200000\n" \
  "s_cbranch_scc1 LVK1_%=\n" \
  "s_sleep 2\n" \
  "s_branch LVP1_%=\n" \
  "LVD_%=:\n"
#define UG_CLOBBERS \
  "v32", "v33", "v34", "v35", "v36", "v37", "v38", "v39", "v40", "v41", "v42", "v43", "v44", "v45", "v46", "v47", "v48", "v49", "v50", "v51", "v52", \
  "v53", "v54", "v55", "v56", "v57", "v58", "v59", "v60", "v61", "v62", "v63", "v64", "v65", "v66", "v67", "v68", "v69", "v70", "v71", "v72", "v73", \
  "v74", "v75", "v76", "v77", "v78", "v79", "v80", "v81", "v82", "v83", "v84", "v85", "v86", "v87", "v88", "v89", "v90", "v91", "v92", "v93", "v94", \
  "v95", "v96", "v97", "v98", "v99", "v100", "v101", "v102", "v103", "v104", "v105", "v106", "v107", "v108", "v109", "v110", "v111", "v112", "v113", \
  "v114", "v115", "v116", "v117", "v118", "v119", "v120", "v121", "v122", "v123", "v124", "v125", "v126", "v127", "v128", "v129", "v130", "v131", \
  "v132", "v133", "v134", "v135", "v136", "v137", "v138", "v139", "v140", "v141", "v142", "v143", "v144", "v145", "v146", "v147", "v148", "v149", \
  "v150", "v151", "v152", "v153", "v154", "v155", "v156", "v157", "v158", "v159", "v160", "v161", "v162", "v163", "v164", "v165", "v166", "v167", \
  "v168", "v169", "v170", "v171", "v172", "v173", "v174", "v175", "v176", "v177", "v178", "v179", "v180", "v181", "v182", "v183", "v184", "v185", \
  "v186", "v187", "v188", "v189", "v190", "v191", "v192", "v193", "v194", "v195", "v196", "v197", "v198", "v199", "v200", "v201", "v202", "v203", \
  "v204", "v205", "v206", "v207", "s68", "s69", "s70", "s71", "s72", "s73", "s74", "s75", "s76", "s77", "s78", "s79", "s80", "s81"
#define UG_ASM \
  "s_mul_i32 s74, s52, 21504\n" \
  "s_mul_i32 s73, s52, 61440\n" \
  "s_add_u32 s77, s73, 61440\n" \
  "LUW_%=:\n" \
  "s_add_u32 m0, s74, 0\n" \
  "s_nop 0\n" \
  "buffer_load_dwordx4 v0, s[44:47], s73 offen offset:0 lds\n" \
  "buffer_load_dwordx4 v1, s[44:47], s73 offen offset:1024 lds\n" \
  "buffer_load_dwordx4 v2, s[44:47], s73 offen offset:2048 lds\n" \
  "buffer_load_dwordx4 v3, s[44:47], s73 offen offset:3072 lds\n" \
  "s_add_u32 m0, s74, 4096\n" \
  "s_nop 0\n" \
  "buffer_load_dwordx4 v4, s[44:47], s73 offen offset:0 lds\n" \
  "buffer_load_dwordx4 v5, s[44:47], s73 offen offset:1024 lds\n" \
  "buffer_load_dwordx4 v6, s[44:47], s73 offen offset:2048 lds\n" \
  "s_add_u32 s74, s74, 7168\n" \
  "s_add_u32 s73, s73, 20480\n" \
  "s_cmp_lg_u32 s73, s77\n" \
  "s_cbranch_scc1 LUW_%=\n" \
  "s_and_b32 s73, s52, 1\n" \
  "s_lshr_b32 s74, s52, 1\n" \
  "s_mul_i32 s68, s73, 10\n" \
  "s_add_u32 s77, s68, 10\n" \
  "s_add_u32 s75, s68, s74\n" \
  "s_mul_i32 s75, s75, 393216\n" \
  "s_lshl_b32 s73, s73, 1\n" \
  "s_add_u32 s73, s73, s74\n" \
  "s_mul_i32 s76, s73, 12288\n" \
  "s_add_u32 s76, s76, 86016\n" \
  "s_add_u32 m0, s76, 0\n" \
  "s_nop 0\n" \
  "buffer_load_dwordx4 v8, s[40:43], s75 offen offset:0 lds\n" \
  "buffer_load_dwordx4 v9, s[40:43], s75 offen offset:1024 lds\n" \
  "buffer_load_dwordx4 v10, s[40:43], s75 offen offset:2048 lds\n" \
  "buffer_load_dwordx4 v11, s[40:43], s75 offen offset:3072 lds\n" \
  "s_add_u32 m0, s76, 4096\n" \
  "s_nop 0\n" \
  "buffer_load_dwordx4 v12, s[40:43], s75 offen offset:0 lds\n" \
  "buffer_load_dwordx4 v13, s[40:43], s75 offen offset:1024 lds\n" \
  "buffer_load_dwordx4 v14, s[40:43], s75 offen offset:2048 lds\n" \
  "buffer_load_dwordx4 v15, s[40:43], s75 offen offset:3072 lds\n" \
  "s_add_u32 m0, s76, 8192\n" \
  "s_nop 0\n" \
  "buffer_load_dwordx4 v16, s[40:43], s75 offen offset:0 lds\n" \
  "buffer_load_dwordx4 v17, s[40:43], s75 offen offset:1024 lds\n" \
  "buffer_load_dwordx4 v18, s[40:43], s75 offen offset:2048 lds\n" \
  "buffer_load_dwordx4 v19, s[40:43], s75 offen offset:3072 lds\n" \
  "s_add_u32 s75, s75, 786432\n" \
  "v_mov_b32 v1, v231\n" \
  "v_mov_b32 v2, v232\n" \
  "v_mov_b32 v3, v233\n" \
  "v_mov_b32 v4, v234\n" \
  "v_mov_b32 v5, v235\n" \
  "v_mov_b32 v6, v236\n" \
  "v_mov_b32 v0, 1\n" \
  "s_waitcnt vmcnt(0)\n" \
  "s_barrier\n" \
  "buffer_load_dword v241, v27, s[40:43], 0 offen\n" \
  "buffer_load_dword v241, v28, s[40:43], 0 offen\n" \
  "buffer_load_dword v241, v29, s[40:43], 0 offen\n" \
  "v_add_u32 v27, 43008, v7\n" \
  "v_add_u32 v28, 43008, v26\n" \
  "ds_read_b128 v[32:35], v20 offset:0\n" \
  "ds_read_b128 v[56:59], v20 offset:768\n" \
  "ds_read_b128 v[80:83], v20 offset:12288\n" \
  "ds_read_b128 v[104:107], v20 offset:13056\n" \
  "ds_read_b64_tr_b16 v[128:129], v7 offset:0\n" \
  "ds_read_b64_tr_b16 v[130:131], v7 offset:1792\n" \
  "ds_read_b128 v[36:39], v21 offset:0\n" \
  "ds_read_b128 v[60:63], v21 offset:768\n" \
  "ds_read_b128 v[84:87], v21 offset:12288\n" \
  "ds_read_b128 v[108:111], v21 offset:13056\n" \
  "ds_read_b64_tr_b16 v[132:133], v7 offset:14336\n" \
  "ds_read_b64_tr_b16 v[134:135], v7 offset:16128\n" \
  "s_waitcnt lgkmcnt(2)\n" \
  "ds_read_b128 v[40:43], v20 offset:128\n" \
  "ds_read_b128 v[64:67], v20 offset:896\n" \
  "ds_read_b128 v[88:91], v20 offset:12416\n" \
  "ds_read_b128 v[112:115], v20 offset:13184\n" \
  "ds_read_b64_tr_b16 v[136:137], v7 offset:28672\n" \
  "ds_read_b64_tr_b16 v[138:139], v7 offset:30464\n" \
  "ds_read_b128 v[44:47], v21 offset:128\n" \
  "ds_read_b128 v[68:71], v21 offset:896\n" \
  "ds_read_b128 v[92:95], v21 offset:12416\n" \
  "ds_read_b128 v[116:119], v21 offset:13184\n" \
  "ds_read_b64_tr_b16 v[140:141], v27 offset:0\n" \
  "ds_read_b64_tr_b16 v[142:143], v27 offset:1792\n" \
  "s_waitcnt lgkmcnt(2)\n" \
  "ds_read_b128 v[48:51], v20 offset:256\n" \
  "ds_read_b128 v[72:75], v20 offset:1024\n" \
  "ds_read_b128 v[96:99], v20 offset:12544\n" \
  "ds_read_b128 v[120:123], v20 offset:13312\n" \
  "ds_read_b64_tr_b16 v[144:145], v27 offset:14336\n" \
  "ds_read_b64_tr_b16 v[146:147], v27 offset:16128\n" \
  "ds_read_b128 v[52:55], v21 offset:256\n" \
  "ds_read_b128 v[76:79], v21 offset:1024\n" \
  "ds_read_b128 v[100:103], v21 offset:12544\n" \
  "ds_read_b128 v[124:127], v21 offset:13312\n" \
  "s_waitcnt lgkmcnt(2)\n" \
  "s_mov_b32 s71, 0x40000000\n" \
  "s_mov_b32 s72, 0x40000000\n" \
  "s_mov_b32 s88, 0x7ffffa00\n" \
  "s_mov_b32 s81, 0\n" \
  "s_waitcnt lgkmcnt(0)\n" \
  "LUR_%=:\n" \
  "v_mfma_f32_16x16x32_bf16 v[152:155], v[32:35], v[128:131], 0\n" \
  "ds_read_b64_tr_b16 v[148:149], v27 offset:28672\n" \
  "v_mfma_f32_16x16x32_bf16 v[156:159], v[56:59], v[128:131], 0\n" \
  "s_waitcnt lgkmcnt(8)\n" \
  "ds_read_b64_tr_b16 v[150:151], v27 offset:30464\n" \
  "v_mfma_f32_16x16x32_bf16 v[160:163], v[80:83], v[128:131], 0\n" \
  "v_cvt_pk_bf16_f32 v200, v184, v185\n" \
  "v_cvt_pk_bf16_f32 v201, v186, v187\n" \
  "v_mfma_f32_16x16x32_bf16 v[164:167], v[104:107], v[128:131], 0\n" \
  "v_cvt_pk_bf16_f32 v202, v188, v189\n" \
  "v_cvt_pk_bf16_f32 v203, v190, v191\n" \
  "v_mfma_f32_16x16x32_bf16 v[152:155], v[36:39], v[132:135], v[152:155]\n" \
  "ds_read_b64_tr_b16 v[128:129], v26 offset:0\n" \
  "v_mfma_f32_16x16x32_bf16 v[156:159], v[60:63], v[132:135], v[156:159]\n" \
  "ds_read_b64_tr_b16 v[130:131], v26 offset:1792\n" \
  "v_mfma_f32_16x16x32_bf16 v[160:163], v[84:87], v[132:135], v[160:163]\n" \
  "s_add_u32 s73, s71, 393216\n" \
  "v_mfma_f32_16x16x32_bf16 v[164:167], v[108:111], v[132:135], v[164:167]\n" \
  "buffer_store_dwordx4 v[200:203], v22, s[36:39], s73 offen sc1\n" \
  "v_mfma_f32_16x16x32_bf16 v[152:155], v[40:43], v[136:139], v[152:155]\n" \
  "ds_read_b64_tr_b16 v[132:133], v26 offset:14336\n" \
  "v_mfma_f32_16x16x32_bf16 v[156:159], v[64:67], v[136:139], v[156:159]\n" \
  "ds_read_b64_tr_b16 v[134:135], v26 offset:16128\n" \
  "v_mfma_f32_16x16x32_bf16 v[160:163], v[88:91], v[136:139], v[160:163]\n" \
  "v_cvt_pk_bf16_f32 v204, v192, v193\n" \
  "v_cvt_pk_bf16_f32 v205, v194, v195\n" \
  "v_mfma_f32_16x16x32_bf16 v[164:167], v[112:115], v[136:139], v[164:167]\n" \
  "v_cvt_pk_bf16_f32 v206, v196, v197\n" \
  "v_cvt_pk_bf16_f32 v207, v198, v199\n" \
  "v_mfma_f32_16x16x32_bf16 v[152:155], v[44:47], v[140:143], v[152:155]\n" \
  "ds_read_b64_tr_b16 v[136:137], v26 offset:28672\n" \
  "v_mfma_f32_16x16x32_bf16 v[156:159], v[68:71], v[140:143], v[156:159]\n" \
  "ds_read_b64_tr_b16 v[138:139], v26 offset:30464\n" \
  "v_mfma_f32_16x16x32_bf16 v[160:163], v[92:95], v[140:143], v[160:163]\n" \
  "s_add_u32 s73, s72, 393216\n" \
  "v_mfma_f32_16x16x32_bf16 v[164:167], v[116:119], v[140:143], v[164:167]\n" \
  "buffer_store_dwordx4 v[204:207], v22, s[36:39], s73 offen sc1\n" \
  "s_waitcnt lgkmcnt(10)\n" \
  "v_mfma_f32_16x16x32_bf16 v[152:155], v[48:51], v[144:147], v[152:155]\n" \
  "ds_read_b64_tr_b16 v[140:141], v28 offset:0\n" \
  "v_mfma_f32_16x16x32_bf16 v[156:159], v[72:75], v[144:147], v[156:159]\n" \
  "ds_read_b64_tr_b16 v[142:143], v28 offset:1792\n" \
  "v_mfma_f32_16x16x32_bf16 v[160:163], v[96:99], v[144:147], v[160:163]\n" \
  "v_readlane_b32 s69, v23, s68\n" \
  "v_readlane_b32 s71, v24, s68\n" \
  "s_sub_u32 s69, s69, s62\n" \
  "s_sub_u32 s71, s71, s63\n" \
  "s_add_u32 s68, s68, 1\n" \
  "v_mfma_f32_16x16x32_bf16 v[164:167], v[120:123], v[144:147], v[164:167]\n" \
  "v_readlane_b32 s70, v23, s68\n" \
  "v_readlane_b32 s72, v24, s68\n" \
  "s_sub_u32 s70, s70, s62\n" \
  "s_sub_u32 s72, s72, s63\n" \
  "s_add_u32 s68, s68, 1\n" \
  "s_waitcnt lgkmcnt(8)\n" \
  "v_mfma_f32_16x16x32_bf16 v[152:155], v[52:55], v[148:151], v[152:155]\n" \
  "ds_read_b64_tr_b16 v[144:145], v28 offset:14336\n" \
  "v_mfma_f32_16x16x32_bf16 v[156:159], v[76:79], v[148:151], v[156:159]\n" \
  "ds_read_b64_tr_b16 v[146:147], v28 offset:16128\n" \
  "v_mfma_f32_16x16x32_bf16 v[160:163], v[100:103], v[148:151], v[160:163]\n" \
  "s_cmp_eq_u32 s68, s77\n" \
  "s_cselect_b32 s75, 0x40000000, s75\n" \
  "v_mfma_f32_16x16x32_bf16 v[164:167], v[124:127], v[148:151], v[164:167]\n" \
  "s_barrier\n" \
  "s_waitcnt lgkmcnt(8)\n" \
  "v_mfma_f32_16x16x32_bf16 v[168:171], v[32:35], v[128:131], 0\n" \
  "ds_read_b64_tr_b16 v[148:149], v28 offset:28672\n" \
  "v_mfma_f32_16x16x32_bf16 v[172:175], v[56:59], v[128:131], 0\n" \
  "ds_read_b64_tr_b16 v[150:151], v28 offset:30464\n" \
  "v_mfma_f32_16x16x32_bf16 v[176:179], v[80:83], v[128:131], 0\n" \
  "v_cvt_pk_bf16_f32 v200, v152, v153\n" \
  "v_cvt_pk_bf16_f32 v201, v154, v155\n" \
  "v_mfma_f32_16x16x32_bf16 v[180:183], v[104:107], v[128:131], 0\n" \
  "s_add_u32 m0, s76, 0\n" \
  "s_nop 0\n" \
  "buffer_load_dwordx4 v8, s[40:43], s75 offen offset:0 lds\n" \
  "s_waitcnt lgkmcnt(8)\n" \
  "v_mfma_f32_16x16x32_bf16 v[168:171], v[36:39], v[132:135], v[168:171]\n" \
  "ds_read_b64_tr_b16 v[128:129], v7 offset:64\n" \
  "v_mfma_f32_16x16x32_bf16 v[172:175], v[60:63], v[132:135], v[172:175]\n" \
  "ds_read_b64_tr_b16 v[130:131], v7 offset:1856\n" \
  "v_mfma_f32_16x16x32_bf16 v[176:179], v[84:87], v[132:135], v[176:179]\n" \
  "v_cvt_pk_bf16_f32 v202, v156, v157\n" \
  "v_cvt_pk_bf16_f32 v203, v158, v159\n" \
  "v_mfma_f32_16x16x32_bf16 v[180:183], v[108:111], v[132:135], v[180:183]\n" \
  "s_add_u32 s73, s69, 0\n" \
  "s_waitcnt lgkmcnt(8)\n" \
  "v_mfma_f32_16x16x32_bf16 v[168:171], v[40:43], v[136:139], v[168:171]\n" \
  "ds_read_b64_tr_b16 v[132:133], v7 offset:14400\n" \
  "v_mfma_f32_16x16x32_bf16 v[172:175], v[64:67], v[136:139], v[172:175]\n" \
  "ds_read_b64_tr_b16 v[134:135], v7 offset:16192\n" \
  "v_mfma_f32_16x16x32_bf16 v[176:179], v[88:91], v[136:139], v[176:179]\n" \
  "buffer_load_dwordx4 v9, s[40:43], s75 offen offset:1024 lds\n" \
  "v_mfma_f32_16x16x32_bf16 v[180:183], v[112:115], v[136:139], v[180:183]\n" \
  "buffer_store_dwordx4 v[200:203], v22, s[36:39], s73 offen sc1\n" \
  "s_waitcnt lgkmcnt(8)\n" \
  "v_mfma_f32_16x16x32_bf16 v[168:171], v[44:47], v[140:143], v[168:171]\n" \
  "ds_read_b64_tr_b16 v[136:137], v7 offset:28736\n" \
  "v_mfma_f32_16x16x32_bf16 v[172:175], v[68:71], v[140:143], v[172:175]\n" \
  "ds_read_b64_tr_b16 v[138:139], v7 offset:30528\n" \
  "v_mfma_f32_16x16x32_bf16 v[176:179], v[92:95], v[140:143], v[176:179]\n" \
  "v_cvt_pk_bf16_f32 v204, v160, v161\n" \
  "v_cvt_pk_bf16_f32 v205, v162, v163\n" \
  "v_mfma_f32_16x16x32_bf16 v[180:183], v[116:119], v[140:143], v[180:183]\n" \
  "buffer_load_dwordx4 v10, s[40:43], s75 offen offset:2048 lds\n" \
  "s_waitcnt lgkmcnt(8)\n" \
  "v_mfma_f32_16x16x32_bf16 v[168:171], v[48:51], v[144:147], v[168:171]\n" \
  "ds_read_b64_tr_b16 v[140:141], v27 offset:64\n" \
  "v_mfma_f32_16x16x32_bf16 v[172:175], v[72:75], v[144:147], v[172:175]\n" \
  "ds_read_b64_tr_b16 v[142:143], v27 offset:1856\n" \
  "v_mfma_f32_16x16x32_bf16 v[176:179], v[96:99], v[144:147], v[176:179]\n" \
  "v_cvt_pk_bf16_f32 v206, v164, v165\n" \
  "v_cvt_pk_bf16_f32 v207, v166, v167\n" \
  "v_mfma_f32_16x16x32_bf16 v[180:183], v[120:123], v[144:147], v[180:183]\n" \
  "s_add_u32 s73, s70, 0\n" \
  "s_waitcnt lgkmcnt(8)\n" \
  "v_mfma_f32_16x16x32_bf16 v[168:171], v[52:55], v[148:151], v[168:171]\n" \
  "ds_read_b64_tr_b16 v[144:145], v27 offset:14400\n" \
  "v_mfma_f32_16x16x32_bf16 v[172:175], v[76:79], v[148:151], v[172:175]\n" \
  "ds_read_b64_tr_b16 v[146:147], v27 offset:16192\n" \
  "v_mfma_f32_16x16x32_bf16 v[176:179], v[100:103], v[148:151], v[176:179]\n" \
  "buffer_load_dwordx4 v11, s[40:43], s75 offen offset:3072 lds\n" \
  "v_mfma_f32_16x16x32_bf16 v[180:183], v[124:127], v[148:151], v[180:183]\n" \
  "buffer_store_dwordx4 v[204:207], v22, s[36:39], s73 offen sc1\n" \
  "s_waitcnt lgkmcnt(8)\n" \
  "v_mfma_f32_16x16x32_bf16 v[152:155], v[32:35], v[128:131], 0\n" \
  "ds_read_b64_tr_b16 v[148:149], v27 offset:28736\n" \
  "v_mfma_f32_16x16x32_bf16 v[156:159], v[56:59], v[128:131], 0\n" \
  "ds_read_b64_tr_b16 v[150:151], v27 offset:30528\n" \
  "v_mfma_f32_16x16x32_bf16 v[160:163], v[80:83], v[128:131], 0\n" \
  "v_cvt_pk_bf16_f32 v200, v168, v169\n" \
  "v_cvt_pk_bf16_f32 v201, v170, v171\n" \
  "v_mfma_f32_16x16x32_bf16 v[164:167], v[104:107], v[128:131], 0\n" \
  "s_add_u32 m0, s76, 4096\n" \
  "s_nop 0\n" \
  "buffer_load_dwordx4 v12, s[40:43], s75 offen offset:0 lds\n" \
  "s_waitcnt lgkmcnt(8)\n" \
  "v_mfma_f32_16x16x32_bf16 v[152:155], v[36:39], v[132:135], v[152:155]\n" \
  "ds_read_b64_tr_b16 v[128:129], v26 offset:64\n" \
  "v_mfma_f32_16x16x32_bf16 v[156:159], v[60:63], v[132:135], v[156:159]\n" \
  "ds_read_b64_tr_b16 v[130:131], v26 offset:1856\n" \
  "v_mfma_f32_16x16x32_bf16 v[160:163], v[84:87], v[132:135], v[160:163]\n" \
  "v_cvt_pk_bf16_f32 v202, v172, v173\n" \
  "v_cvt_pk_bf16_f32 v203, v174, v175\n" \
  "v_mfma_f32_16x16x32_bf16 v[164:167], v[108:111], v[132:135], v[164:167]\n" \
  "s_add_u32 s73, s69, 65536\n" \
  "s_waitcnt lgkmcnt(8)\n" \
  "v_mfma_f32_16x16x32_bf16 v[152:155], v[40:43], v[136:139], v[152:155]\n" \
  "ds_read_b64_tr_b16 v[132:133], v26 offset:14400\n" \
  "v_mfma_f32_16x16x32_bf16 v[156:159], v[64:67], v[136:139], v[156:159]\n" \
  "ds_read_b64_tr_b16 v[134:135], v26 offset:16192\n" \
  "v_mfma_f32_16x16x32_bf16 v[160:163], v[88:91], v[136:139], v[160:163]\n" \
  "buffer_load_dwordx4 v13, s[40:43], s75 offen offset:1024 lds\n" \
  "v_mfma_f32_16x16x32_bf16 v[164:167], v[112:115], v[136:139], v[164:167]\n" \
  "buffer_store_dwordx4 v[200:203], v22, s[36:39], s73 offen sc1\n" \
  "s_waitcnt lgkmcnt(8)\n" \
  "v_mfma_f32_16x16x32_bf16 v[152:155], v[44:47], v[140:143], v[152:155]\n" \
  "ds_read_b64_tr_b16 v[136:137], v26 offset:28736\n" \
  "v_mfma_f32_16x16x32_bf16 v[156:159], v[68:71], v[140:143], v[156:159]\n" \
  "ds_read_b64_tr_b16 v[138:139], v26 offset:30528\n" \
  "v_mfma_f32_16x16x32_bf16 v[160:163], v[92:95], v[140:143], v[160:163]\n" \
  "v_cvt_pk_bf16_f32 v204, v176, v177\n" \
  "v_cvt_pk_bf16_f32 v205, v178, v179\n" \
  "v_mfma_f32_16x16x32_bf16 v[164:167], v[116:119], v[140:143], v[164:167]\n" \
  "buffer_load_dwordx4 v14, s[40:43], s75 offen offset:2048 lds\n" \
  "s_waitcnt lgkmcnt(8)\n" \
  "v_mfma_f32_16x16x32_bf16 v[152:155], v[48:51], v[144:147], v[152:155]\n" \
  "ds_read_b64_tr_b16 v[140:141], v28 offset:64\n" \
  "v_mfma_f32_16x16x32_bf16 v[156:159], v[72:75], v[144:147], v[156:159]\n" \
  "ds_read_b64_tr_b16 v[142:143], v28 offset:1856\n" \
  "v_mfma_f32_16x16x32_bf16 v[160:163], v[96:99], v[144:147], v[160:163]\n" \
  "v_cvt_pk_bf16_f32 v206, v180, v181\n" \
  "v_cvt_pk_bf16_f32 v207, v182, v183\n" \
  "v_mfma_f32_16x16x32_bf16 v[164:167], v[120:123], v[144:147], v[164:167]\n" \
  "s_add_u32 s73, s70, 65536\n" \
  "s_waitcnt lgkmcnt(8)\n" \
  "v_mfma_f32_16x16x32_bf16 v[152:155], v[52:55], v[148:151], v[152:155]\n" \
  "ds_read_b64_tr_b16 v[144:145], v28 offset:14400\n" \
  "v_mfma_f32_16x16x32_bf16 v[156:159], v[76:79], v[148:151], v[156:159]\n" \
  "ds_read_b64_tr_b16 v[146:147], v28 offset:16192\n" \
  "v_mfma_f32_16x16x32_bf16 v[160:163], v[100:103], v[148:151], v[160:163]\n" \
  "buffer_load_dwordx4 v15, s[40:43], s75 offen offset:3072 lds\n" \
  "v_mfma_f32_16x16x32_bf16 v[164:167], v[124:127], v[148:151], v[164:167]\n" \
  "buffer_store_dwordx4 v[204:207], v22, s[36:39], s73 offen sc1\n" \
  "s_waitcnt lgkmcnt(8)\n" \
  "v_mfma_f32_16x16x32_bf16 v[168:171], v[32:35], v[128:131], 0\n" \
  "ds_read_b64_tr_b16 v[148:149], v28 offset:28736\n" \
  "v_mfma_f32_16x16x32_bf16 v[172:175], v[56:59], v[128:131], 0\n" \
  "ds_read_b64_tr_b16 v[150:151], v28 offset:30528\n" \
  "v_mfma_f32_16x16x32_bf16 v[176:179], v[80:83], v[128:131], 0\n" \
  "v_cvt_pk_bf16_f32 v200, v152, v153\n" \
  "v_cvt_pk_bf16_f32 v201, v154, v155\n" \
  "s_waitcnt vmcnt(12)\n" \
  "s_barrier\n" \
  "s_mov_b32 s78, s88\n" \
  "buffer_atomic_add v0, v25, s[36:39], s78 offen sc0 sc1\n" \
  "s_add_u32 s88, s88, 1536\n" \
  "s_and_b32 s88, s88, 0x3fffffff\n" \
  "v_mfma_f32_16x16x32_bf16 v[180:183], v[104:107], v[128:131], 0\n" \
  "s_add_u32 m0, s76, 8192\n" \
  "s_nop 0\n" \
  "buffer_load_dwordx4 v16, s[40:43], s75 offen offset:0 lds\n" \
  "s_waitcnt lgkmcnt(8)\n" \
  "v_mfma_f32_16x16x32_bf16 v[168:171], v[36:39], v[132:135], v[168:171]\n" \
  "ds_read_b64_tr_b16 v[128:129], v7 offset:128\n" \
  "v_mfma_f32_16x16x32_bf16 v[172:175], v[60:63], v[132:135], v[172:175]\n" \
  "ds_read_b64_tr_b16 v[130:131], v7 offset:1920\n" \
  "v_mfma_f32_16x16x32_bf16 v[176:179], v[84:87], v[132:135], v[176:179]\n" \
  "v_cvt_pk_bf16_f32 v202, v156, v157\n" \
  "v_cvt_pk_bf16_f32 v203, v158, v159\n" \
  "v_mfma_f32_16x16x32_bf16 v[180:183], v[108:111], v[132:135], v[180:183]\n" \
  "s_add_u32 s73, s69, 131072\n" \
  "s_waitcnt lgkmcnt(8)\n" \
  "v_mfma_f32_16x16x32_bf16 v[168:171], v[40:43], v[136:139], v[168:171]\n" \
  "ds_read_b64_tr_b16 v[132:133], v7 offset:14464\n" \
  "v_mfma_f32_16x16x32_bf16 v[172:175], v[64:67], v[136:139], v[172:175]\n" \
  "ds_read_b64_tr_b16 v[134:135], v7 offset:16256\n" \
  "v_mfma_f32_16x16x32_bf16 v[176:179], v[88:91], v[136:139], v[176:179]\n" \
  "buffer_load_dwordx4 v17, s[40:43], s75 offen offset:1024 lds\n" \
  "v_mfma_f32_16x16x32_bf16 v[180:183], v[112:115], v[136:139], v[180:183]\n" \
  "buffer_store_dwordx4 v[200:203], v22, s[36:39], s73 offen sc1\n" \
  "s_waitcnt lgkmcnt(8)\n" \
  "v_mfma_f32_16x16x32_bf16 v[168:171], v[44:47], v[140:143], v[168:171]\n" \
  "ds_read_b64_tr_b16 v[136:137], v7 offset:28800\n" \
  "v_mfma_f32_16x16x32_bf16 v[172:175], v[68:71], v[140:143], v[172:175]\n" \
  "ds_read_b64_tr_b16 v[138:139], v7 offset:30592\n" \
  "v_mfma_f32_16x16x32_bf16 v[176:179], v[92:95], v[140:143], v[176:179]\n" \
  "v_cvt_pk_bf16_f32 v204, v160, v161\n" \
  "v_cvt_pk_bf16_f32 v205, v162, v163\n" \
  "v_mfma_f32_16x16x32_bf16 v[180:183], v[116:119], v[140:143], v[180:183]\n" \
  "buffer_load_dwordx4 v18, s[40:43], s75 offen offset:2048 lds\n" \
  "s_waitcnt lgkmcnt(8)\n" \
  "v_mfma_f32_16x16x32_bf16 v[168:171], v[48:51], v[144:147], v[168:171]\n" \
  "ds_read_b64_tr_b16 v[140:141], v27 offset:128\n" \
  "v_mfma_f32_16x16x32_bf16 v[172:175], v[72:75], v[144:147], v[172:175]\n" \
  "ds_read_b64_tr_b16 v[142:143], v27 offset:1920\n" \
  "v_mfma_f32_16x16x32_bf16 v[176:179], v[96:99], v[144:147], v[176:179]\n" \
  "v_cvt_pk_bf16_f32 v206, v164, v165\n" \
  "v_cvt_pk_bf16_f32 v207, v166, v167\n" \
  "v_mfma_f32_16x16x32_bf16 v[180:183], v[120:123], v[144:147], v[180:183]\n" \
  "s_add_u32 s73, s70, 131072\n" \
  "s_waitcnt lgkmcnt(8)\n" \
  "v_mfma_f32_16x16x32_bf16 v[168:171], v[52:55], v[148:151], v[168:171]\n" \
  "ds_read_b64_tr_b16 v[144:145], v27 offset:14464\n" \
  "v_mfma_f32_16x16x32_bf16 v[172:175], v[76:79], v[148:151], v[172:175]\n" \
  "ds_read_b64_tr_b16 v[146:147], v27 offset:16256\n" \
  "v_mfma_f32_16x16x32_bf16 v[176:179], v[100:103], v[148:151], v[176:179]\n" \
  "buffer_load_dwordx4 v19, s[40:43], s75 offen offset:3072 lds\n" \
  "s_add_u32 s75, s75, 786432\n" \
  "v_mfma_f32_16x16x32_bf16 v[180:183], v[124:127], v[148:151], v[180:183]\n" \
  "buffer_store_dwordx4 v[204:207], v22, s[36:39], s73 offen sc1\n" \
  "s_waitcnt lgkmcnt(8)\n" \
  "v_mfma_f32_16x16x32_bf16 v[152:155], v[32:35], v[128:131], 0\n" \
  "ds_read_b64_tr_b16 v[148:149], v27 offset:28800\n" \
  "v_mfma_f32_16x16x32_bf16 v[156:159], v[56:59], v[128:131], 0\n" \
  "ds_read_b64_tr_b16 v[150:151], v27 offset:30592\n" \
  "v_mfma_f32_16x16x32_bf16 v[160:163], v[80:83], v[128:131], 0\n" \
  "v_cvt_pk_bf16_f32 v200, v168, v169\n" \
  "v_cvt_pk_bf16_f32 v201, v170, v171\n" \
  "v_mfma_f32_16x16x32_bf16 v[164:167], v[104:107], v[128:131], 0\n" \
  "v_cvt_pk_bf16_f32 v202, v172, v173\n" \
  "v_cvt_pk_bf16_f32 v203, v174, v175\n" \
  "s_waitcnt lgkmcnt(8)\n" \
  "v_mfma_f32_16x16x32_bf16 v[152:155], v[36:39], v[132:135], v[152:155]\n" \
  "ds_read_b64_tr_b16 v[128:129], v26 offset:128\n" \
  "v_mfma_f32_16x16x32_bf16 v[156:159], v[60:63], v[132:135], v[156:159]\n" \
  "ds_read_b64_tr_b16 v[130:131], v26 offset:1920\n" \
  "v_mfma_f32_16x16x32_bf16 v[160:163], v[84:87], v[132:135], v[160:163]\n" \
  "s_add_u32 s73, s69, 196608\n" \
  "v_mfma_f32_16x16x32_bf16 v[164:167], v[108:111], v[132:135], v[164:167]\n" \
  "buffer_store_dwordx4 v[200:203], v22, s[36:39], s73 offen sc1\n" \
  "s_waitcnt lgkmcnt(8)\n" \
  "v_mfma_f32_16x16x32_bf16 v[152:155], v[40:43], v[136:139], v[152:155]\n" \
  "ds_read_b64_tr_b16 v[132:133], v26 offset:14464\n" \
  "v_mfma_f32_16x16x32_bf16 v[156:159], v[64:67], v[136:139], v[156:159]\n" \
  "ds_read_b64_tr_b16 v[134:135], v26 offset:16256\n" \
  "v_mfma_f32_16x16x32_bf16 v[160:163], v[88:91], v[136:139], v[160:163]\n" \
  "v_cvt_pk_bf16_f32 v204, v176, v177\n" \
  "v_cvt_pk_bf16_f32 v205, v178, v179\n" \
  "v_mfma_f32_16x16x32_bf16 v[164:167], v[112:115], v[136:139], v[164:167]\n" \
  "v_cvt_pk_bf16_f32 v206, v180, v181\n" \
  "v_cvt_pk_bf16_f32 v207, v182, v183\n" \
  "s_waitcnt lgkmcnt(8)\n" \
  "v_mfma_f32_16x16x32_bf16 v[152:155], v[44:47], v[140:143], v[152:155]\n" \
  "ds_read_b64_tr_b16 v[136:137], v26 offset:28800\n" \
  "v_mfma_f32_16x16x32_bf16 v[156:159], v[68:71], v[140:143], v[156:159]\n" \
  "ds_read_b64_tr_b16 v[138:139], v26 offset:30592\n" \
  "v_mfma_f32_16x16x32_bf16 v[160:163], v[92:95], v[140:143], v[160:163]\n" \
  "s_add_u32 s73, s70, 196608\n" \
  "v_mfma_f32_16x16x32_bf16 v[164:167], v[116:119], v[140:143], v[164:167]\n" \
  "buffer_store_dwordx4 v[204:207], v22, s[36:39], s73 offen sc1\n" \
  "s_waitcnt lgkmcnt(8)\n" \
  "v_mfma_f32_16x16x32_bf16 v[152:155], v[48:51], v[144:147], v[152:155]\n" \
  "ds_read_b64_tr_b16 v[140:141], v28 offset:128\n" \
  "v_mfma_f32_16x16x32_bf16 v[156:159], v[72:75], v[144:147], v[156:159]\n" \
  "ds_read_b64_tr_b16 v[142:143], v28 offset:1920\n" \
  "v_mfma_f32_16x16x32_bf16 v[160:163], v[96:99], v[144:147], v[160:163]\n" \
  "s_getpc_b64 s[82:83]\n" \
  "LCF6_%=:\n" \
  "s_add_u32 s82, s82, LUN2_%=-LCF6_%=\n" \
  "s_addc_u32 s83, s83, 0\n" \
  "v_mbcnt_lo_u32_b32 v240, -1, 0\n" \
  "v_mbcnt_hi_u32_b32 v240, -1, v240\n" \
  "v_lshlrev_b32 v240, 6, v240\n" \
  "global_load_dword v241, v240, s[82:83]\n" \
  "v_mfma_f32_16x16x32_bf16 v[164:167], v[120:123], v[144:147], v[164:167]\n" \
  "s_waitcnt lgkmcnt(8)\n" \
  "v_mfma_f32_16x16x32_bf16 v[152:155], v[52:55], v[148:151], v[152:155]\n" \
  "ds_read_b64_tr_b16 v[144:145], v28 offset:14464\n" \
  "v_mfma_f32_16x16x32_bf16 v[156:159], v[76:79], v[148:151], v[156:159]\n" \
  "ds_read_b64_tr_b16 v[146:147], v28 offset:16256\n" \
  "v_mfma_f32_16x16x32_bf16 v[160:163], v[100:103], v[148:151], v[160:163]\n" \
  "v_mfma_f32_16x16x32_bf16 v[164:167], v[124:127], v[148:151], v[164:167]\n" \
  "s_waitcnt lgkmcnt(8)\n" \
  "v_mfma_f32_16x16x32_bf16 v[168:171], v[32:35], v[128:131], 0\n" \
  "ds_read_b64_tr_b16 v[148:149], v28 offset:28800\n" \
  "v_mfma_f32_16x16x32_bf16 v[172:175], v[56:59], v[128:131], 0\n" \
  "ds_read_b64_tr_b16 v[150:151], v28 offset:30592\n" \
  "v_mfma_f32_16x16x32_bf16 v[176:179], v[80:83], v[128:131], 0\n" \
  "v_cvt_pk_bf16_f32 v200, v152, v153\n" \
  "v_cvt_pk_bf16_f32 v201, v154, v155\n" \
  "v_mfma_f32_16x16x32_bf16 v[180:183], v[104:107], v[128:131], 0\n" \
  "v_cvt_pk_bf16_f32 v202, v156, v157\n" \
  "v_cvt_pk_bf16_f32 v203, v158, v159\n" \
  "s_waitcnt lgkmcnt(8)\n" \
  "v_mfma_f32_16x16x32_bf16 v[168:171], v[36:39], v[132:135], v[168:171]\n" \
  "ds_read_b64_tr_b16 v[128:129], v7 offset:192\n" \
  "v_mfma_f32_16x16x32_bf16 v[172:175], v[60:63], v[132:135], v[172:175]\n" \
  "ds_read_b64_tr_b16 v[130:131], v7 offset:1984\n" \
  "v_mfma_f32_16x16x32_bf16 v[176:179], v[84:87], v[132:135], v[176:179]\n" \
  "s_add_u32 s73, s69, 262144\n" \
  "v_mfma_f32_16x16x32_bf16 v[180:183], v[108:111], v[132:135], v[180:183]\n" \
  "buffer_store_dwordx4 v[200:203], v22, s[36:39], s73 offen sc1\n" \
  "s_waitcnt lgkmcnt(8)\n" \
  "v_mfma_f32_16x16x32_bf16 v[168:171], v[40:43], v[136:139], v[168:171]\n" \
  "ds_read_b64_tr_b16 v[132:133], v7 offset:14528\n" \
  "v_mfma_f32_16x16x32_bf16 v[172:175], v[64:67], v[136:139], v[172:175]\n" \
  "ds_read_b64_tr_b16 v[134:135], v7 offset:16320\n" \
  "v_mfma_f32_16x16x32_bf16 v[176:179], v[88:91], v[136:139], v[176:179]\n" \
  "v_cvt_pk_bf16_f32 v204, v160, v161\n" \
  "v_cvt_pk_bf16_f32 v205, v162, v163\n" \
  "v_mfma_f32_16x16x32_bf16 v[180:183], v[112:115], v[136:139], v[180:183]\n" \
  "v_cvt_pk_bf16_f32 v206, v164, v165\n" \
  "v_cvt_pk_bf16_f32 v207, v166, v167\n" \
  "s_waitcnt lgkmcnt(8)\n" \
  "v_mfma_f32_16x16x32_bf16 v[168:171], v[44:47], v[140:143], v[168:171]\n" \
  "ds_read_b64_tr_b16 v[136:137], v7 offset:28864\n" \
  "v_mfma_f32_16x16x32_bf16 v[172:175], v[68:71], v[140:143], v[172:175]\n" \
  "ds_read_b64_tr_b16 v[138:139], v7 offset:30656\n" \
  "v_mfma_f32_16x16x32_bf16 v[176:179], v[92:95], v[140:143], v[176:179]\n" \
  "s_add_u32 s73, s70, 262144\n" \
  "v_mfma_f32_16x16x32_bf16 v[180:183], v[116:119], v[140:143], v[180:183]\n" \
  "buffer_store_dwordx4 v[204:207], v22, s[36:39], s73 offen sc1\n" \
  "s_waitcnt lgkmcnt(8)\n" \
  "v_mfma_f32_16x16x32_bf16 v[168:171], v[48:51], v[144:147], v[168:171]\n" \
  "ds_read_b64_tr_b16 v[140:141], v27 offset:192\n" \
  "v_mfma_f32_16x16x32_bf16 v[172:175], v[72:75], v[144:147], v[172:175]\n" \
  "ds_read_b64_tr_b16 v[142:143], v27 offset:1984\n" \
  "v_mfma_f32_16x16x32_bf16 v[176:179], v[96:99], v[144:147], v[176:179]\n" \
  "v_mfma_f32_16x16x32_bf16 v[180:183], v[120:123], v[144:147], v[180:183]\n" \
  "s_waitcnt lgkmcnt(8)\n" \
  "v_mfma_f32_16x16x32_bf16 v[168:171], v[52:55], v[148:151], v[168:171]\n" \
  "ds_read_b64_tr_b16 v[144:145], v27 offset:14528\n" \
  "v_mfma_f32_16x16x32_bf16 v[172:175], v[76:79], v[148:151], v[172:175]\n" \
  "ds_read_b64_tr_b16 v[146:147], v27 offset:16320\n" \
  "v_mfma_f32_16x16x32_bf16 v[176:179], v[100:103], v[148:151], v[176:179]\n" \
  "v_mfma_f32_16x16x32_bf16 v[180:183], v[124:127], v[148:151], v[180:183]\n" \
  "s_waitcnt vmcnt(6)\n" \
  "s_barrier\n" \
  "v_readfirstlane_b32 s74, v0\n" \
  "v_mov_b32 v0, 1\n" \
  "s_cmp_eq_u32 s74, 11\n" \
  "s_cselect_b32 s73, s78, 0x40000000\n" \
  "buffer_store_dword v0, v31, s[36:39], s73 offen sc1\n" \
  "s_waitcnt lgkmcnt(8)\n" \
  "v_mfma_f32_16x16x32_bf16 v[184:187], v[32:35], v[128:131], 0\n" \
  "ds_read_b128 v[32:35], v20 offset:0\n" \
  "ds_read_b64_tr_b16 v[148:149], v27 offset:28864\n" \
  "v_mfma_f32_16x16x32_bf16 v[188:191], v[56:59], v[128:131], 0\n" \
  "ds_read_b128 v[56:59], v20 offset:768\n" \
  "ds_read_b64_tr_b16 v[150:151], v27 offset:30656\n" \
  "v_mfma_f32_16x16x32_bf16 v[192:195], v[80:83], v[128:131], 0\n" \
  "ds_read_b128 v[80:83], v20 offset:12288\n" \
  "v_cvt_pk_bf16_f32 v200, v168, v169\n" \
  "v_cvt_pk_bf16_f32 v201, v170, v171\n" \
  "v_mfma_f32_16x16x32_bf16 v[196:199], v[104:107], v[128:131], 0\n" \
  "ds_read_b128 v[104:107], v20 offset:13056\n" \
  "v_cvt_pk_bf16_f32 v202, v172, v173\n" \
  "v_cvt_pk_bf16_f32 v203, v174, v175\n" \
  "s_waitcnt lgkmcnt(12)\n" \
  "v_mfma_f32_16x16x32_bf16 v[184:187], v[36:39], v[132:135], v[184:187]\n" \
  "ds_read_b128 v[36:39], v21 offset:0\n" \
  "ds_read_b64_tr_b16 v[128:129], v7 offset:0\n" \
  "v_mfma_f32_16x16x32_bf16 v[188:191], v[60:63], v[132:135], v[188:191]\n" \
  "ds_read_b128 v[60:63], v21 offset:768\n" \
  "s_waitcnt lgkmcnt(8)\n" \
  "ds_read_b64_tr_b16 v[130:131], v7 offset:1792\n" \
  "v_mfma_f32_16x16x32_bf16 v[192:195], v[84:87], v[132:135], v[192:195]\n" \
  "ds_read_b128 v[84:87], v21 offset:12288\n" \
  "s_add_u32 s73, s69, 327680\n" \
  "v_mfma_f32_16x16x32_bf16 v[196:199], v[108:111], v[132:135], v[196:199]\n" \
  "ds_read_b128 v[108:111], v21 offset:13056\n" \
  "buffer_store_dwordx4 v[200:203], v22, s[36:39], s73 offen sc1\n" \
  "v_mfma_f32_16x16x32_bf16 v[184:187], v[40:43], v[136:139], v[184:187]\n" \
  "ds_read_b128 v[40:43], v20 offset:128\n" \
  "ds_read_b64_tr_b16 v[132:133], v7 offset:14336\n" \
  "v_mfma_f32_16x16x32_bf16 v[188:191], v[64:67], v[136:139], v[188:191]\n" \
  "ds_read_b128 v[64:67], v20 offset:896\n" \
  "ds_read_b64_tr_b16 v[134:135], v7 offset:16128\n" \
  "v_mfma_f32_16x16x32_bf16 v[192:195], v[88:91], v[136:139], v[192:195]\n" \
  "s_waitcnt lgkmcnt(8)\n" \
  "ds_read_b128 v[88:91], v20 offset:12416\n" \
  "v_cvt_pk_bf16_f32 v204, v176, v177\n" \
  "v_cvt_pk_bf16_f32 v205, v178, v179\n" \
  "v_mfma_f32_16x16x32_bf16 v[196:199], v[112:115], v[136:139], v[196:199]\n" \
  "ds_read_b128 v[112:115], v20 offset:13184\n" \
  "v_cvt_pk_bf16_f32 v206, v180, v181\n" \
  "v_cvt_pk_bf16_f32 v207, v182, v183\n" \
  "v_mfma_f32_16x16x32_bf16 v[184:187], v[44:47], v[140:143], v[184:187]\n" \
  "ds_read_b128 v[44:47], v21 offset:128\n" \
  "ds_read_b64_tr_b16 v[136:137], v7 offset:28672\n" \
  "v_mfma_f32_16x16x32_bf16 v[188:191], v[68:71], v[140:143], v[188:191]\n" \
  "ds_read_b128 v[68:71], v21 offset:896\n" \
  "ds_read_b64_tr_b16 v[138:139], v7 offset:30464\n" \
  "v_mfma_f32_16x16x32_bf16 v[192:195], v[92:95], v[140:143], v[192:195]\n" \
  "ds_read_b128 v[92:95], v21 offset:12416\n" \
  "s_add_u32 s73, s70, 327680\n" \
  "v_mfma_f32_16x16x32_bf16 v[196:199], v[116:119], v[140:143], v[196:199]\n" \
  "s_waitcnt lgkmcnt(8)\n" \
  "ds_read_b128 v[116:119], v21 offset:13184\n" \
  "buffer_store_dwordx4 v[204:207], v22, s[36:39], s73 offen sc1\n" \
  "v_mfma_f32_16x16x32_bf16 v[184:187], v[48:51], v[144:147], v[184:187]\n" \
  "ds_read_b128 v[48:51], v20 offset:256\n" \
  "ds_read_b64_tr_b16 v[140:141], v27 offset:0\n" \
  "v_mfma_f32_16x16x32_bf16 v[188:191], v[72:75], v[144:147], v[188:191]\n" \
  "ds_read_b128 v[72:75], v20 offset:1024\n" \
  "ds_read_b64_tr_b16 v[142:143], v27 offset:1792\n" \
  "v_mfma_f32_16x16x32_bf16 v[192:195], v[96:99], v[144:147], v[192:195]\n" \
  "ds_read_b128 v[96:99], v20 offset:12544\n" \
  "s_cmp_lg_u32 s78, 0\n" \
  "v_mfma_f32_16x16x32_bf16 v[196:199], v[120:123], v[144:147], v[196:199]\n" \
  "ds_read_b128 v[120:123], v20 offset:13312\n" \
  "v_mfma_f32_16x16x32_bf16 v[184:187], v[52:55], v[148:151], v[184:187]\n" \
  "s_waitcnt lgkmcnt(8)\n" \
  "ds_read_b128 v[52:55], v21 offset:256\n" \
  "ds_read_b64_tr_b16 v[144:145], v27 offset:14336\n" \
  "v_mfma_f32_16x16x32_bf16 v[188:191], v[76:79], v[148:151], v[188:191]\n" \
  "ds_read_b128 v[76:79], v21 offset:1024\n" \
  "ds_read_b64_tr_b16 v[146:147], v27 offset:16128\n" \
  "v_mfma_f32_16x16x32_bf16 v[192:195], v[100:103], v[148:151], v[192:195]\n" \
  "ds_read_b128 v[100:103], v21 offset:12544\n" \
  "v_mfma_f32_16x16x32_bf16 v[196:199], v[124:127], v[148:151], v[196:199]\n" \
  "ds_read_b128 v[124:127], v21 offset:13312\n" \
  "s_cbranch_scc0 LUN2_%=\n" \
  "s_mov_b32 s85, 0\n" \
  "LSDu_%=:\n" \
  "buffer_load_dwordx2 v[244:245], v6, s[36:39], 0 offen sc1\n" \
  "s_waitcnt vmcnt(0)\n" \
  "v_readlane_b32 s82, v244, 3\n" \
  "s_nop 0\n" \
  "s_add_u32 s82, s82, 1\n" \
  "v_cmp_ne_u32 vcc, s82, v244\n" \
  "s_nop 3\n" \
  "s_and_b32 s82, vcc_lo, 7\n" \
  "s_cbranch_scc0 LSFu_%=\n" \
  "s_add_u32 s85, s85, 1\n" \
  "s_cmp_gt_u32 s85, 200000\n" \
  "s_cbranch_scc1 LSFu_%=\n" \
  "s_sleep 4\n" \
  "s_branch LSDu_%=\n" \
  "LSFu_%=:\n" \
  "v_readlane_b32 s82, v245, 0\n" \
  "v_readlane_b32 s83, v245, 1\n" \
  "v_readlane_b32 s84, v245, 2\n" \
  "s_nop 1\n" \
  "v_lshrrev_b32 v244, 21, v212\n" \
  "v_lshrrev_b64 v[246:247], v244, s[82:83]\n" \
  "v_lshrrev_b32_e64 v248, v244, s84\n" \
  "v_cmp_gt_u32 vcc, 64, v244\n" \
  "s_nop 1\n" \
  "v_cndmask_b32 v246, v248, v246, vcc\n" \
  "v_and_b32 v246, 1, v246\n" \
  "v_lshlrev_b32 v246, 13, v246\n" \
  "v_xor_b32 v212, v212, v246\n" \
  "v_lshrrev_b32 v244, 21, v215\n" \
  "v_lshrrev_b64 v[246:247], v244, s[82:83]\n" \
  "v_lshrrev_b32_e64 v248, v244, s84\n" \
  "v_cmp_gt_u32 vcc, 64, v244\n" \
  "s_nop 1\n" \
  "v_cndmask_b32 v246, v248, v246, vcc\n" \
  "v_and_b32 v246, 1, v246\n" \
  "v_lshlrev_b32 v246, 13, v246\n" \
  "v_xor_b32 v215, v215, v246\n" \
  "v_lshrrev_b32 v244, 21, v216\n" \
  "v_lshrrev_b64 v[246:247], v244, s[82:83]\n" \
  "v_lshrrev_b32_e64 v248, v244, s84\n" \
  "v_cmp_gt_u32 vcc, 64, v244\n" \
  "s_nop 1\n" \
  "v_cndmask_b32 v246, v248, v246, vcc\n" \
  "v_and_b32 v246, 1, v246\n" \
  "v_lshlrev_b32 v246, 13, v246\n" \
  "v_xor_b32 v216, v216, v246\n" \
  "v_lshrrev_b32 v244, 21, v217\n" \
  "v_lshrrev_b64 v[246:247], v244, s[82:83]\n" \
  "v_lshrrev_b32_e64 v248, v244, s84\n" \
  "v_cmp_gt_u32 vcc, 64, v244\n" \
  "s_nop 1\n" \
  "v_cndmask_b32 v246, v248, v246, vcc\n" \
  "v_and_b32 v246, 1, v246\n" \
  "v_lshlrev_b32 v246, 13, v246\n" \
  "v_xor_b32 v217, v217, v246\n" \
  "v_lshrrev_b32 v244, 21, v219\n" \
  "v_lshrrev_b64 v[246:247], v244, s[82:83]\n" \
  "v_lshrrev_b32_e64 v248, v244, s84\n" \
  "v_cmp_gt_u32 vcc, 64, v244\n" \
  "s_nop 1\n" \
  "v_cndmask_b32 v246, v248, v246, vcc\n" \
  "v_and_b32 v246, 1, v246\n" \
  "v_lshlrev_b32 v246, 13, v246\n" \
  "v_xor_b32 v219, v219, v246\n" \
  "s_mov_b32 exec_lo, 0xfffff3fc\n" \
  "v_lshrrev_b32 v244, 21, v23\n" \
  "v_lshrrev_b64 v[246:247], v244, s[82:83]\n" \
  "v_lshrrev_b32_e64 v248, v244, s84\n" \
  "v_cmp_gt_u32 vcc, 64, v244\n" \
  "s_nop 1\n" \
  "v_cndmask_b32 v246, v248, v246, vcc\n" \
  "v_and_b32 v246, 1, v246\n" \
  "v_lshlrev_b32 v246, 13, v246\n" \
  "v_xor_b32 v23, v23, v246\n" \
  "v_lshrrev_b32 v244, 21, v24\n" \
  "v_lshrrev_b64 v[246:247], v244, s[82:83]\n" \
  "v_lshrrev_b32_e64 v248, v244, s84\n" \
  "v_cmp_gt_u32 vcc, 64, v244\n" \
  "s_nop 1\n" \
  "v_cndmask_b32 v246, v248, v246, vcc\n" \
  "v_and_b32 v246, 1, v246\n" \
  "v_lshlrev_b32 v246, 13, v246\n" \
  "v_xor_b32 v24, v24, v246\n" \
  "v_lshrrev_b32 v244, 21, v224\n" \
  "v_lshrrev_b64 v[246:247], v244, s[82:83]\n" \
  "v_lshrrev_b32_e64 v248, v244, s84\n" \
  "v_cmp_gt_u32 vcc, 64, v244\n" \
  "s_nop 1\n" \
  "v_cndmask_b32 v246, v248, v246, vcc\n" \
  "v_and_b32 v246, 1, v246\n" \
  "v_lshlrev_b32 v246, 13, v246\n" \
  "v_xor_b32 v224, v224, v246\n" \
  "s_mov_b32 exec_lo, -1\n" \
  "s_branch LUR_%=\n" \
  "LUN2_%=:\n" \
  "v_mfma_f32_16x16x32_bf16 v[152:155], v[32:35], v[128:131], 0\n" \
  "ds_read_b64_tr_b16 v[148:149], v27 offset:28672\n" \
  "v_mfma_f32_16x16x32_bf16 v[156:159], v[56:59], v[128:131], 0\n" \
  "s_waitcnt lgkmcnt(8)\n" \
  "ds_read_b64_tr_b16 v[150:151], v27 offset:30464\n" \
  "v_mfma_f32_16x16x32_bf16 v[160:163], v[80:83], v[128:131], 0\n" \
  "v_cvt_pk_bf16_f32 v200, v184, v185\n" \
  "v_cvt_pk_bf16_f32 v201, v186, v187\n" \
  "v_mfma_f32_16x16x32_bf16 v[164:167], v[104:107], v[128:131], 0\n" \
  "v_cvt_pk_bf16_f32 v202, v188, v189\n" \
  "v_cvt_pk_bf16_f32 v203, v190, v191\n" \
  "v_mfma_f32_16x16x32_bf16 v[152:155], v[36:39], v[132:135], v[152:155]\n" \
  "ds_read_b64_tr_b16 v[128:129], v26 offset:0\n" \
  "v_mfma_f32_16x16x32_bf16 v[156:159], v[60:63], v[132:135], v[156:159]\n" \
  "ds_read_b64_tr_b16 v[130:131], v26 offset:1792\n" \
  "v_mfma_f32_16x16x32_bf16 v[160:163], v[84:87], v[132:135], v[160:163]\n" \
  "s_add_u32 s73, s71, 393216\n" \
  "v_mfma_f32_16x16x32_bf16 v[164:167], v[108:111], v[132:135], v[164:167]\n" \
  "buffer_store_dwordx4 v[200:203], v22, s[36:39], s73 offen sc1\n" \
  "v_mfma_f32_16x16x32_bf16 v[152:155], v[40:43], v[136:139], v[152:155]\n" \
  "ds_read_b64_tr_b16 v[132:133], v26 offset:14336\n" \
  "v_mfma_f32_16x16x32_bf16 v[156:159], v[64:67], v[136:139], v[156:159]\n" \
  "ds_read_b64_tr_b16 v[134:135], v26 offset:16128\n" \
  "v_mfma_f32_16x16x32_bf16 v[160:163], v[88:91], v[136:139], v[160:163]\n" \
  "v_cvt_pk_bf16_f32 v204, v192, v193\n" \
  "v_cvt_pk_bf16_f32 v205, v194, v195\n" \
  "v_mfma_f32_16x16x32_bf16 v[164:167], v[112:115], v[136:139], v[164:167]\n" \
  "v_cvt_pk_bf16_f32 v206, v196, v197\n" \
  "v_cvt_pk_bf16_f32 v207, v198, v199\n" \
  "v_mfma_f32_16x16x32_bf16 v[152:155], v[44:47], v[140:143], v[152:155]\n" \
  "ds_read_b64_tr_b16 v[136:137], v26 offset:28672\n" \
  "v_mfma_f32_16x16x32_bf16 v[156:159], v[68:71], v[140:143], v[156:159]\n" \
  "ds_read_b64_tr_b16 v[138:139], v26 offset:30464\n" \
  "v_mfma_f32_16x16x32_bf16 v[160:163], v[92:95], v[140:143], v[160:163]\n" \
  "s_add_u32 s73, s72, 393216\n" \
  "v_mfma_f32_16x16x32_bf16 v[164:167], v[116:119], v[140:143], v[164:167]\n" \
  "buffer_store_dwordx4 v[204:207], v22, s[36:39], s73 offen sc1\n" \
  "s_waitcnt lgkmcnt(10)\n" \
  "v_mfma_f32_16x16x32_bf16 v[152:155], v[48:51], v[144:147], v[152:155]\n" \
  "ds_read_b64_tr_b16 v[140:141], v28 offset:0\n" \
  "v_mfma_f32_16x16x32_bf16 v[156:159], v[72:75], v[144:147], v[156:159]\n" \
  "ds_read_b64_tr_b16 v[142:143], v28 offset:1792\n" \
  "v_mfma_f32_16x16x32_bf16 v[160:163], v[96:99], v[144:147], v[160:163]\n" \
  "v_readlane_b32 s69, v23, s68\n" \
  "v_readlane_b32 s71, v24, s68\n" \
  "s_sub_u32 s69, s69, s62\n" \
  "s_sub_u32 s71, s71, s63\n" \
  "s_add_u32 s68, s68, 1\n" \
  "v_mfma_f32_16x16x32_bf16 v[164:167], v[120:123], v[144:147], v[164:167]\n" \
  "v_readlane_b32 s70, v23, s68\n" \
  "v_readlane_b32 s72, v24, s68\n" \
  "s_sub_u32 s70, s70, s62\n" \
  "s_sub_u32 s72, s72, s63\n" \
  "s_add_u32 s68, s68, 1\n" \
  "s_waitcnt lgkmcnt(8)\n" \
  "v_mfma_f32_16x16x32_bf16 v[152:155], v[52:55], v[148:151], v[152:155]\n" \
  "ds_read_b64_tr_b16 v[144:145], v28 offset:14336\n" \
  "v_mfma_f32_16x16x32_bf16 v[156:159], v[76:79], v[148:151], v[156:159]\n" \
  "ds_read_b64_tr_b16 v[146:147], v28 offset:16128\n" \
  "v_mfma_f32_16x16x32_bf16 v[160:163], v[100:103], v[148:151], v[160:163]\n" \
  "s_cmp_eq_u32 s68, s77\n" \
  "s_cselect_b32 s75, 0x40000000, s75\n" \
  "v_mfma_f32_16x16x32_bf16 v[164:167], v[124:127], v[148:151], v[164:167]\n" \
  "s_barrier\n" \
  "s_waitcnt lgkmcnt(8)\n" \
  "v_mfma_f32_16x16x32_bf16 v[168:171], v[32:35], v[128:131], 0\n" \
  "ds_read_b64_tr_b16 v[148:149], v28 offset:28672\n" \
  "v_mfma_f32_16x16x32_bf16 v[172:175], v[56:59], v[128:131], 0\n" \
  "ds_read_b64_tr_b16 v[150:151], v28 offset:30464\n" \
  "v_mfma_f32_16x16x32_bf16 v[176:179], v[80:83], v[128:131], 0\n" \
  "v_cvt_pk_bf16_f32 v200, v152, v153\n" \
  "v_cvt_pk_bf16_f32 v201, v154, v155\n" \
  "v_mfma_f32_16x16x32_bf16 v[180:183], v[104:107], v[128:131], 0\n" \
  "s_add_u32 m0, s76, 0\n" \
  "s_nop 0\n" \
  "buffer_load_dwordx4 v8, s[40:43], s75 offen offset:0 lds\n" \
  "s_waitcnt lgkmcnt(8)\n" \
  "v_mfma_f32_16x16x32_bf16 v[168:171], v[36:39], v[132:135], v[168:171]\n" \
  "ds_read_b64_tr_b16 v[128:129], v7 offset:64\n" \
  "v_mfma_f32_16x16x32_bf16 v[172:175], v[60:63], v[132:135], v[172:175]\n" \
  "ds_read_b64_tr_b16 v[130:131], v7 offset:1856\n" \
  "v_mfma_f32_16x16x32_bf16 v[176:179], v[84:87], v[132:135], v[176:179]\n" \
  "v_cvt_pk_bf16_f32 v202, v156, v157\n" \
  "v_cvt_pk_bf16_f32 v203, v158, v159\n" \
  "v_mfma_f32_16x16x32_bf16 v[180:183], v[108:111], v[132:135], v[180:183]\n" \
  "s_add_u32 s73, s69, 0\n" \
  "buffer_load_dword v242, v30, s[36:39], s78 offen sc1\n" \
  "s_waitcnt lgkmcnt(8)\n" \
  "v_mfma_f32_16x16x32_bf16 v[168:171], v[40:43], v[136:139], v[168:171]\n" \
  "ds_read_b64_tr_b16 v[132:133], v7 offset:14400\n" \
  "v_mfma_f32_16x16x32_bf16 v[172:175], v[64:67], v[136:139], v[172:175]\n" \
  "ds_read_b64_tr_b16 v[134:135], v7 offset:16192\n" \
  "v_mfma_f32_16x16x32_bf16 v[176:179], v[88:91], v[136:139], v[176:179]\n" \
  "buffer_load_dwordx4 v9, s[40:43], s75 offen offset:1024 lds\n" \
  "v_mfma_f32_16x16x32_bf16 v[180:183], v[112:115], v[136:139], v[180:183]\n" \
  "buffer_store_dwordx4 v[200:203], v22, s[36:39], s73 offen sc1\n" \
  "s_waitcnt lgkmcnt(8)\n" \
  "v_mfma_f32_16x16x32_bf16 v[168:171], v[44:47], v[140:143], v[168:171]\n" \
  "ds_read_b64_tr_b16 v[136:137], v7 offset:28736\n" \
  "v_mfma_f32_16x16x32_bf16 v[172:175], v[68:71], v[140:143], v[172:175]\n" \
  "ds_read_b64_tr_b16 v[138:139], v7 offset:30528\n" \
  "v_mfma_f32_16x16x32_bf16 v[176:179], v[92:95], v[140:143], v[176:179]\n" \
  "v_cvt_pk_bf16_f32 v204, v160, v161\n" \
  "v_cvt_pk_bf16_f32 v205, v162, v163\n" \
  "v_mfma_f32_16x16x32_bf16 v[180:183], v[116:119], v[140:143], v[180:183]\n" \
  "buffer_load_dwordx4 v10, s[40:43], s75 offen offset:2048 lds\n" \
  "s_waitcnt lgkmcnt(8)\n" \
  "v_mfma_f32_16x16x32_bf16 v[168:171], v[48:51], v[144:147], v[168:171]\n" \
  "ds_read_b64_tr_b16 v[140:141], v27 offset:64\n" \
  "v_mfma_f32_16x16x32_bf16 v[172:175], v[72:75], v[144:147], v[172:175]\n" \
  "ds_read_b64_tr_b16 v[142:143], v27 offset:1856\n" \
  "v_mfma_f32_16x16x32_bf16 v[176:179], v[96:99], v[144:147], v[176:179]\n" \
  "v_cvt_pk_bf16_f32 v206, v164, v165\n" \
  "v_cvt_pk_bf16_f32 v207, v166, v167\n" \
  "v_mfma_f32_16x16x32_bf16 v[180:183], v[120:123], v[144:147], v[180:183]\n" \
  "s_add_u32 s73, s70, 0\n" \
  "s_waitcnt lgkmcnt(8)\n" \
  "v_mfma_f32_16x16x32_bf16 v[168:171], v[52:55], v[148:151], v[168:171]\n" \
  "ds_read_b64_tr_b16 v[144:145], v27 offset:14400\n" \
  "v_mfma_f32_16x16x32_bf16 v[172:175], v[76:79], v[148:151], v[172:175]\n" \
  "ds_read_b64_tr_b16 v[146:147], v27 offset:16192\n" \
  "v_mfma_f32_16x16x32_bf16 v[176:179], v[100:103], v[148:151], v[176:179]\n" \
  "buffer_load_dwordx4 v11, s[40:43], s75 offen offset:3072 lds\n" \
  "v_mfma_f32_16x16x32_bf16 v[180:183], v[124:127], v[148:151], v[180:183]\n" \
  "buffer_store_dwordx4 v[204:207], v22, s[36:39], s73 offen sc1\n" \
  "s_waitcnt lgkmcnt(8)\n" \
  "v_mfma_f32_16x16x32_bf16 v[152:155], v[32:35], v[128:131], 0\n" \
  "ds_read_b64_tr_b16 v[148:149], v27 offset:28736\n" \
  "v_mfma_f32_16x16x32_bf16 v[156:159], v[56:59], v[128:131], 0\n" \
  "ds_read_b64_tr_b16 v[150:151], v27 offset:30528\n" \
  "v_mfma_f32_16x16x32_bf16 v[160:163], v[80:83], v[128:131], 0\n" \
  "v_cvt_pk_bf16_f32 v200, v168, v169\n" \
  "v_cvt_pk_bf16_f32 v201, v170, v171\n" \
  "v_mfma_f32_16x16x32_bf16 v[164:167], v[104:107], v[128:131], 0\n" \
  "s_add_u32 m0, s76, 4096\n" \
  "s_nop 0\n" \
  "buffer_load_dwordx4 v12, s[40:43], s75 offen offset:0 lds\n" \
  "s_waitcnt lgkmcnt(8)\n" \
  "v_mfma_f32_16x16x32_bf16 v[152:155], v[36:39], v[132:135], v[152:155]\n" \
  "ds_read_b64_tr_b16 v[128:129], v26 offset:64\n" \
  "v_mfma_f32_16x16x32_bf16 v[156:159], v[60:63], v[132:135], v[156:159]\n" \
  "ds_read_b64_tr_b16 v[130:131], v26 offset:1856\n" \
  "v_mfma_f32_16x16x32_bf16 v[160:163], v[84:87], v[132:135], v[160:163]\n" \
  "v_cvt_pk_bf16_f32 v202, v172, v173\n" \
  "v_cvt_pk_bf16_f32 v203, v174, v175\n" \
  "v_mfma_f32_16x16x32_bf16 v[164:167], v[108:111], v[132:135], v[164:167]\n" \
  "s_add_u32 s73, s69, 65536\n" \
  "s_waitcnt lgkmcnt(8)\n" \
  "v_mfma_f32_16x16x32_bf16 v[152:155], v[40:43], v[136:139], v[152:155]\n" \
  "ds_read_b64_tr_b16 v[132:133], v26 offset:14400\n" \
  "v_mfma_f32_16x16x32_bf16 v[156:159], v[64:67], v[136:139], v[156:159]\n" \
  "ds_read_b64_tr_b16 v[134:135], v26 offset:16192\n" \
  "v_mfma_f32_16x16x32_bf16 v[160:163], v[88:91], v[136:139], v[160:163]\n" \
  "buffer_load_dwordx4 v13, s[40:43], s75 offen offset:1024 lds\n" \
  "v_mfma_f32_16x16x32_bf16 v[164:167], v[112:115], v[136:139], v[164:167]\n" \
  "buffer_store_dwordx4 v[200:203], v22, s[36:39], s73 offen sc1\n" \
  "s_waitcnt lgkmcnt(8)\n" \
  "v_mfma_f32_16x16x32_bf16 v[152:155], v[44:47], v[140:143], v[152:155]\n" \
  "ds_read_b64_tr_b16 v[136:137], v26 offset:28736\n" \
  "v_mfma_f32_16x16x32_bf16 v[156:159], v[68:71], v[140:143], v[156:159]\n" \
  "ds_read_b64_tr_b16 v[138:139], v26 offset:30528\n" \
  "v_mfma_f32_16x16x32_bf16 v[160:163], v[92:95], v[140:143], v[160:163]\n" \
  "v_cvt_pk_bf16_f32 v204, v176, v177\n" \
  "v_cvt_pk_bf16_f32 v205, v178, v179\n" \
  "v_mfma_f32_16x16x32_bf16 v[164:167], v[116:119], v[140:143], v[164:167]\n" \
  "buffer_load_dwordx4 v14, s[40:43], s75 offen offset:2048 lds\n" \
  "s_waitcnt lgkmcnt(8)\n" \
  "v_mfma_f32_16x16x32_bf16 v[152:155], v[48:51], v[144:147], v[152:155]\n" \
  "ds_read_b64_tr_b16 v[140:141], v28 offset:64\n" \
  "v_mfma_f32_16x16x32_bf16 v[156:159], v[72:75], v[144:147], v[156:159]\n" \
  "ds_read_b64_tr_b16 v[142:143], v28 offset:1856\n" \
  "v_mfma_f32_16x16x32_bf16 v[160:163], v[96:99], v[144:147], v[160:163]\n" \
  "v_cvt_pk_bf16_f32 v206, v180, v181\n" \
  "v_cvt_pk_bf16_f32 v207, v182, v183\n" \
  "v_mfma_f32_16x16x32_bf16 v[164:167], v[120:123], v[144:147], v[164:167]\n" \
  "s_add_u32 s73, s70, 65536\n" \
  "s_waitcnt vmcnt(9)\n" \
  "v_cmp_ne_u32 vcc, 1, v242\n" \
  "v_readlane_b32 s79, v224, 0\n" \
  "s_waitcnt lgkmcnt(8)\n" \
  "v_mfma_f32_16x16x32_bf16 v[152:155], v[52:55], v[148:151], v[152:155]\n" \
  "ds_read_b64_tr_b16 v[144:145], v28 offset:14400\n" \
  "v_mfma_f32_16x16x32_bf16 v[156:159], v[76:79], v[148:151], v[156:159]\n" \
  "ds_read_b64_tr_b16 v[146:147], v28 offset:16192\n" \
  "v_mfma_f32_16x16x32_bf16 v[160:163], v[100:103], v[148:151], v[160:163]\n" \
  "buffer_load_dwordx4 v15, s[40:43], s75 offen offset:3072 lds\n" \
  "v_mfma_f32_16x16x32_bf16 v[164:167], v[124:127], v[148:151], v[164:167]\n" \
  "buffer_store_dwordx4 v[204:207], v22, s[36:39], s73 offen sc1\n" \
  "s_cbranch_vccnz LUP2_%=\n" \
  "LUK2_%=:\n" \
  "s_waitcnt lgkmcnt(8)\n" \
  "v_mfma_f32_16x16x32_bf16 v[168:171], v[32:35], v[128:131], 0\n" \
  "ds_read_b64_tr_b16 v[148:149], v28 offset:28736\n" \
  "v_mfma_f32_16x16x32_bf16 v[172:175], v[56:59], v[128:131], 0\n" \
  "ds_read_b64_tr_b16 v[150:151], v28 offset:30528\n" \
  "buffer_load_dwordx4 a[0:3], v1, s[36:39], s79 offen sc1\n" \
  "v_readlane_b32 s80, v224, 1\n" \
  "v_mfma_f32_16x16x32_bf16 v[176:179], v[80:83], v[128:131], 0\n" \
  "v_cvt_pk_bf16_f32 v200, v152, v153\n" \
  "v_cvt_pk_bf16_f32 v201, v154, v155\n" \
  "s_waitcnt vmcnt(14)\n" \
  "s_barrier\n" \
  "s_mov_b32 s78, s88\n" \
  "buffer_atomic_add v0, v25, s[36:39], s78 offen sc0 sc1\n" \
  "s_add_u32 s88, s88, 1536\n" \
  "s_and_b32 s88, s88, 0x3fffffff\n" \
  "v_mfma_f32_16x16x32_bf16 v[180:183], v[104:107], v[128:131], 0\n" \
  "s_add_u32 m0, s76, 8192\n" \
  "s_nop 0\n" \
  "buffer_load_dwordx4 v16, s[40:43], s75 offen offset:0 lds\n" \
  "s_waitcnt lgkmcnt(8)\n" \
  "v_mfma_f32_16x16x32_bf16 v[168:171], v[36:39], v[132:135], v[168:171]\n" \
  "ds_read_b64_tr_b16 v[128:129], v7 offset:128\n" \
  "v_mfma_f32_16x16x32_bf16 v[172:175], v[60:63], v[132:135], v[172:175]\n" \
  "ds_read_b64_tr_b16 v[130:131], v7 offset:1920\n" \
  "buffer_load_dwordx4 a[8:11], v2, s[36:39], s79 offen sc1\n" \
  "v_mfma_f32_16x16x32_bf16 v[176:179], v[84:87], v[132:135], v[176:179]\n" \
  "v_cvt_pk_bf16_f32 v202, v156, v157\n" \
  "v_cvt_pk_bf16_f32 v203, v158, v159\n" \
  "v_mfma_f32_16x16x32_bf16 v[180:183], v[108:111], v[132:135], v[180:183]\n" \
  "s_add_u32 s73, s69, 131072\n" \
  "s_waitcnt lgkmcnt(8)\n" \
  "v_mfma_f32_16x16x32_bf16 v[168:171], v[40:43], v[136:139], v[168:171]\n" \
  "ds_read_b64_tr_b16 v[132:133], v7 offset:14464\n" \
  "v_mfma_f32_16x16x32_bf16 v[172:175], v[64:67], v[136:139], v[172:175]\n" \
  "ds_read_b64_tr_b16 v[134:135], v7 offset:16256\n" \
  "buffer_load_dwordx4 a[16:19], v3, s[36:39], s79 offen sc1\n" \
  "v_mfma_f32_16x16x32_bf16 v[176:179], v[88:91], v[136:139], v[176:179]\n" \
  "buffer_load_dwordx4 v17, s[40:43], s75 offen offset:1024 lds\n" \
  "v_mfma_f32_16x16x32_bf16 v[180:183], v[112:115], v[136:139], v[180:183]\n" \
  "buffer_store_dwordx4 v[200:203], v22, s[36:39], s73 offen sc1\n" \
  "s_waitcnt lgkmcnt(8)\n" \
  "v_mfma_f32_16x16x32_bf16 v[168:171], v[44:47], v[140:143], v[168:171]\n" \
  "ds_read_b64_tr_b16 v[136:137], v7 offset:28800\n" \
  "v_mfma_f32_16x16x32_bf16 v[172:175], v[68:71], v[140:143], v[172:175]\n" \
  "ds_read_b64_tr_b16 v[138:139], v7 offset:30592\n" \
  "buffer_load_dwordx4 a[24:27], v4, s[36:39], s79 offen sc1\n" \
  "v_mfma_f32_16x16x32_bf16 v[176:179], v[92:95], v[140:143], v[176:179]\n" \
  "v_cvt_pk_bf16_f32 v204, v160, v161\n" \
  "v_cvt_pk_bf16_f32 v205, v162, v163\n" \
  "v_mfma_f32_16x16x32_bf16 v[180:183], v[116:119], v[140:143], v[180:183]\n" \
  "buffer_load_dwordx4 v18, s[40:43], s75 offen offset:2048 lds\n" \
  "s_waitcnt lgkmcnt(8)\n" \
  "v_mfma_f32_16x16x32_bf16 v[168:171], v[48:51], v[144:147], v[168:171]\n" \
  "ds_read_b64_tr_b16 v[140:141], v27 offset:128\n" \
  "v_mfma_f32_16x16x32_bf16 v[172:175], v[72:75], v[144:147], v[172:175]\n" \
  "ds_read_b64_tr_b16 v[142:143], v27 offset:1920\n" \
  "buffer_load_dwordx4 a[32:35], v5, s[36:39], s79 offen sc1\n" \
  "v_mfma_f32_16x16x32_bf16 v[176:179], v[96:99], v[144:147], v[176:179]\n" \
  "v_cvt_pk_bf16_f32 v206, v164, v165\n" \
  "v_cvt_pk_bf16_f32 v207, v166, v167\n" \
  "v_mfma_f32_16x16x32_bf16 v[180:183], v[120:123], v[144:147], v[180:183]\n" \
  "s_add_u32 s73, s70, 131072\n" \
  "s_waitcnt lgkmcnt(8)\n" \
  "v_mfma_f32_16x16x32_bf16 v[168:171], v[52:55], v[148:151], v[168:171]\n" \
  "ds_read_b64_tr_b16 v[144:145], v27 offset:14464\n" \
  "v_mfma_f32_16x16x32_bf16 v[172:175], v[76:79], v[148:151], v[172:175]\n" \
  "ds_read_b64_tr_b16 v[146:147], v27 offset:16256\n" \
  "buffer_load_dwordx4 a[4:7], v1, s[36:39], s80 offen sc1\n" \
  "v_readlane_b32 s79, v224, 10\n" \
  "v_mfma_f32_16x16x32_bf16 v[176:179], v[100:103], v[148:151], v[176:179]\n" \
  "buffer_load_dwordx4 v19, s[40:43], s75 offen offset:3072 lds\n" \
  "s_add_u32 s75, s75, 786432\n" \
  "v_mfma_f32_16x16x32_bf16 v[180:183], v[124:127], v[148:151], v[180:183]\n" \
  "buffer_store_dwordx4 v[204:207], v22, s[36:39], s73 offen sc1\n" \
  "s_waitcnt lgkmcnt(8)\n" \
  "v_mfma_f32_16x16x32_bf16 v[152:155], v[32:35], v[128:131], 0\n" \
  "ds_read_b64_tr_b16 v[148:149], v27 offset:28800\n" \
  "v_mfma_f32_16x16x32_bf16 v[156:159], v[56:59], v[128:131], 0\n" \
  "ds_read_b64_tr_b16 v[150:151], v27 offset:30592\n" \
  "buffer_load_dwordx4 a[12:15], v2, s[36:39], s80 offen sc1\n" \
  "v_mfma_f32_16x16x32_bf16 v[160:163], v[80:83], v[128:131], 0\n" \
  "v_cvt_pk_bf16_f32 v200, v168, v169\n" \
  "v_cvt_pk_bf16_f32 v201, v170, v171\n" \
  "v_mfma_f32_16x16x32_bf16 v[164:167], v[104:107], v[128:131], 0\n" \
  "v_cvt_pk_bf16_f32 v202, v172, v173\n" \
  "v_cvt_pk_bf16_f32 v203, v174, v175\n" \
  "s_waitcnt lgkmcnt(8)\n" \
  "v_mfma_f32_16x16x32_bf16 v[152:155], v[36:39], v[132:135], v[152:155]\n" \
  "ds_read_b64_tr_b16 v[128:129], v26 offset:128\n" \
  "v_mfma_f32_16x16x32_bf16 v[156:159], v[60:63], v[132:135], v[156:159]\n" \
  "ds_read_b64_tr_b16 v[130:131], v26 offset:1920\n" \
  "buffer_load_dwordx4 a[20:23], v3, s[36:39], s80 offen sc1\n" \
  "v_mfma_f32_16x16x32_bf16 v[160:163], v[84:87], v[132:135], v[160:163]\n" \
  "s_add_u32 s73, s69, 196608\n" \
  "v_mfma_f32_16x16x32_bf16 v[164:167], v[108:111], v[132:135], v[164:167]\n" \
  "buffer_store_dwordx4 v[200:203], v22, s[36:39], s73 offen sc1\n" \
  "s_waitcnt lgkmcnt(8)\n" \
  "v_mfma_f32_16x16x32_bf16 v[152:155], v[40:43], v[136:139], v[152:155]\n" \
  "ds_read_b64_tr_b16 v[132:133], v26 offset:14464\n" \
  "v_mfma_f32_16x16x32_bf16 v[156:159], v[64:67], v[136:139], v[156:159]\n" \
  "ds_read_b64_tr_b16 v[134:135], v26 offset:16256\n" \
  "buffer_load_dwordx4 a[28:31], v4, s[36:39], s80 offen sc1\n" \
  "v_mfma_f32_16x16x32_bf16 v[160:163], v[88:91], v[136:139], v[160:163]\n" \
  "v_cvt_pk_bf16_f32 v204, v176, v177\n" \
  "v_cvt_pk_bf16_f32 v205, v178, v179\n" \
  "v_mfma_f32_16x16x32_bf16 v[164:167], v[112:115], v[136:139], v[164:167]\n" \
  "v_cvt_pk_bf16_f32 v206, v180, v181\n" \
  "v_cvt_pk_bf16_f32 v207, v182, v183\n" \
  "s_waitcnt lgkmcnt(8)\n" \
  "v_mfma_f32_16x16x32_bf16 v[152:155], v[44:47], v[140:143], v[152:155]\n" \
  "ds_read_b64_tr_b16 v[136:137], v26 offset:28800\n" \
  "v_mfma_f32_16x16x32_bf16 v[156:159], v[68:71], v[140:143], v[156:159]\n" \
  "ds_read_b64_tr_b16 v[138:139], v26 offset:30592\n" \
  "buffer_load_dwordx4 a[36:39], v5, s[36:39], s80 offen sc1\n" \
  "v_mfma_f32_16x16x32_bf16 v[160:163], v[92:95], v[140:143], v[160:163]\n" \
  "s_add_u32 s73, s70, 196608\n" \
  "v_mfma_f32_16x16x32_bf16 v[164:167], v[116:119], v[140:143], v[164:167]\n" \
  "buffer_store_dwordx4 v[204:207], v22, s[36:39], s73 offen sc1\n" \
  "s_waitcnt lgkmcnt(8)\n" \
  "v_mfma_f32_16x16x32_bf16 v[152:155], v[48:51], v[144:147], v[152:155]\n" \
  "ds_read_b64_tr_b16 v[140:141], v28 offset:128\n" \
  "v_mfma_f32_16x16x32_bf16 v[156:159], v[72:75], v[144:147], v[156:159]\n" \
  "ds_read_b64_tr_b16 v[142:143], v28 offset:1920\n" \
  "buffer_load_dwordx4 a[40:43], v1, s[36:39], s79 offen sc1\n" \
  "v_readlane_b32 s80, v224, 11\n" \
  "v_mfma_f32_16x16x32_bf16 v[160:163], v[96:99], v[144:147], v[160:163]\n" \
  "s_getpc_b64 s[82:83]\n" \
  "LCF7_%=:\n" \
  "s_add_u32 s82, s82, LUN3_%=-LCF7_%=\n" \
  "s_addc_u32 s83, s83, 0\n" \
  "v_mbcnt_lo_u32_b32 v240, -1, 0\n" \
  "v_mbcnt_hi_u32_b32 v240, -1, v240\n" \
  "v_lshlrev_b32 v240, 6, v240\n" \
  "global_load_dword v241, v240, s[82:83]\n" \
  "v_mfma_f32_16x16x32_bf16 v[164:167], v[120:123], v[144:147], v[164:167]\n" \
  "s_waitcnt lgkmcnt(8)\n" \
  "v_mfma_f32_16x16x32_bf16 v[152:155], v[52:55], v[148:151], v[152:155]\n" \
  "ds_read_b64_tr_b16 v[144:145], v28 offset:14464\n" \
  "v_mfma_f32_16x16x32_bf16 v[156:159], v[76:79], v[148:151], v[156:159]\n" \
  "ds_read_b64_tr_b16 v[146:147], v28 offset:16256\n" \
  "buffer_load_dwordx4 a[48:51], v2, s[36:39], s79 offen sc1\n" \
  "v_mfma_f32_16x16x32_bf16 v[160:163], v[100:103], v[148:151], v[160:163]\n" \
  "v_mfma_f32_16x16x32_bf16 v[164:167], v[124:127], v[148:151], v[164:167]\n" \
  "s_waitcnt lgkmcnt(8)\n" \
  "v_mfma_f32_16x16x32_bf16 v[168:171], v[32:35], v[128:131], 0\n" \
  "ds_read_b64_tr_b16 v[148:149], v28 offset:28800\n" \
  "v_mfma_f32_16x16x32_bf16 v[172:175], v[56:59], v[128:131], 0\n" \
  "ds_read_b64_tr_b16 v[150:151], v28 offset:30592\n" \
  "buffer_load_dwordx4 a[56:59], v3, s[36:39], s79 offen sc1\n" \
  "v_mfma_f32_16x16x32_bf16 v[176:179], v[80:83], v[128:131], 0\n" \
  "v_cvt_pk_bf16_f32 v200, v152, v153\n" \
  "v_cvt_pk_bf16_f32 v201, v154, v155\n" \
  "v_mfma_f32_16x16x32_bf16 v[180:183], v[104:107], v[128:131], 0\n" \
  "v_cvt_pk_bf16_f32 v202, v156, v157\n" \
  "v_cvt_pk_bf16_f32 v203, v158, v159\n" \
  "s_waitcnt lgkmcnt(8)\n" \
  "v_mfma_f32_16x16x32_bf16 v[168:171], v[36:39], v[132:135], v[168:171]\n" \
  "ds_read_b64_tr_b16 v[128:129], v7 offset:192\n" \
  "v_mfma_f32_16x16x32_bf16 v[172:175], v[60:63], v[132:135], v[172:175]\n" \
  "ds_read_b64_tr_b16 v[130:131], v7 offset:1984\n" \
  "buffer_load_dwordx4 a[64:67], v4, s[36:39], s79 offen sc1\n" \
  "v_mfma_f32_16x16x32_bf16 v[176:179], v[84:87], v[132:135], v[176:179]\n" \
  "s_add_u32 s73, s69, 262144\n" \
  "v_mfma_f32_16x16x32_bf16 v[180:183], v[108:111], v[132:135], v[180:183]\n" \
  "buffer_store_dwordx4 v[200:203], v22, s[36:39], s73 offen sc1\n" \
  "s_waitcnt lgkmcnt(8)\n" \
  "v_mfma_f32_16x16x32_bf16 v[168:171], v[40:43], v[136:139], v[168:171]\n" \
  "ds_read_b64_tr_b16 v[132:133], v7 offset:14528\n" \
  "v_mfma_f32_16x16x32_bf16 v[172:175], v[64:67], v[136:139], v[172:175]\n" \
  "ds_read_b64_tr_b16 v[134:135], v7 offset:16320\n" \
  "buffer_load_dwordx4 a[72:75], v5, s[36:39], s79 offen sc1\n" \
  "v_mfma_f32_16x16x32_bf16 v[176:179], v[88:91], v[136:139], v[176:179]\n" \
  "v_cvt_pk_bf16_f32 v204, v160, v161\n" \
  "v_cvt_pk_bf16_f32 v205, v162, v163\n" \
  "v_mfma_f32_16x16x32_bf16 v[180:183], v[112:115], v[136:139], v[180:183]\n" \
  "v_cvt_pk_bf16_f32 v206, v164, v165\n" \
  "v_cvt_pk_bf16_f32 v207, v166, v167\n" \
  "s_waitcnt lgkmcnt(8)\n" \
  "v_mfma_f32_16x16x32_bf16 v[168:171], v[44:47], v[140:143], v[168:171]\n" \
  "ds_read_b64_tr_b16 v[136:137], v7 offset:28864\n" \
  "v_mfma_f32_16x16x32_bf16 v[172:175], v[68:71], v[140:143], v[172:175]\n" \
  "ds_read_b64_tr_b16 v[138:139], v7 offset:30656\n" \
  "buffer_load_dwordx4 a[44:47], v1, s[36:39], s80 offen sc1\n" \
  "v_mfma_f32_16x16x32_bf16 v[176:179], v[92:95], v[140:143], v[176:179]\n" \
  "s_add_u32 s73, s70, 262144\n" \
  "v_mfma_f32_16x16x32_bf16 v[180:183], v[116:119], v[140:143], v[180:183]\n" \
  "buffer_store_dwordx4 v[204:207], v22, s[36:39], s73 offen sc1\n" \
  "s_waitcnt lgkmcnt(8)\n" \
  "v_mfma_f32_16x16x32_bf16 v[168:171], v[48:51], v[144:147], v[168:171]\n" \
  "ds_read_b64_tr_b16 v[140:141], v27 offset:192\n" \
  "v_mfma_f32_16x16x32_bf16 v[172:175], v[72:75], v[144:147], v[172:175]\n" \
  "ds_read_b64_tr_b16 v[142:143], v27 offset:1984\n" \
  "buffer_load_dwordx4 a[52:55], v2, s[36:39], s80 offen sc1\n" \
  "v_mfma_f32_16x16x32_bf16 v[176:179], v[96:99], v[144:147], v[176:179]\n" \
  "v_mfma_f32_16x16x32_bf16 v[180:183], v[120:123], v[144:147], v[180:183]\n" \
  "s_waitcnt lgkmcnt(8)\n" \
  "v_mfma_f32_16x16x32_bf16 v[168:171], v[52:55], v[148:151], v[168:171]\n" \
  "ds_read_b64_tr_b16 v[144:145], v27 offset:14528\n" \
  "v_mfma_f32_16x16x32_bf16 v[172:175], v[76:79], v[148:151], v[172:175]\n" \
  "ds_read_b64_tr_b16 v[146:147], v27 offset:16320\n" \
  "buffer_load_dwordx4 a[60:63], v3, s[36:39], s80 offen sc1\n" \
  "v_mfma_f32_16x16x32_bf16 v[176:179], v[100:103], v[148:151], v[176:179]\n" \
  "v_mfma_f32_16x16x32_bf16 v[180:183], v[124:127], v[148:151], v[180:183]\n" \
  "s_waitcnt vmcnt(18)\n" \
  "s_barrier\n" \
  "v_readfirstlane_b32 s74, v0\n" \
  "v_mov_b32 v0, 1\n" \
  "s_cmp_eq_u32 s74, 11\n" \
  "s_cselect_b32 s73, s78, 0x40000000\n" \
  "buffer_store_dword v0, v31, s[36:39], s73 offen sc1\n" \
  "s_waitcnt lgkmcnt(8)\n" \
  "v_mfma_f32_16x16x32_bf16 v[184:187], v[32:35], v[128:131], 0\n" \
  "ds_read_b128 v[32:35], v20 offset:0\n" \
  "ds_read_b64_tr_b16 v[148:149], v27 offset:28864\n" \
  "v_mfma_f32_16x16x32_bf16 v[188:191], v[56:59], v[128:131], 0\n" \
  "ds_read_b128 v[56:59], v20 offset:768\n" \
  "ds_read_b64_tr_b16 v[150:151], v27 offset:30656\n" \
  "buffer_load_dwordx4 a[68:71], v4, s[36:39], s80 offen sc1\n" \
  "v_mfma_f32_16x16x32_bf16 v[192:195], v[80:83], v[128:131], 0\n" \
  "ds_read_b128 v[80:83], v20 offset:12288\n" \
  "v_cvt_pk_bf16_f32 v200, v168, v169\n" \
  "v_cvt_pk_bf16_f32 v201, v170, v171\n" \
  "v_mfma_f32_16x16x32_bf16 v[196:199], v[104:107], v[128:131], 0\n" \
  "ds_read_b128 v[104:107], v20 offset:13056\n" \
  "v_cvt_pk_bf16_f32 v202, v172, v173\n" \
  "v_cvt_pk_bf16_f32 v203, v174, v175\n" \
  "s_waitcnt lgkmcnt(12)\n" \
  "v_mfma_f32_16x16x32_bf16 v[184:187], v[36:39], v[132:135], v[184:187]\n" \
  "ds_read_b128 v[36:39], v21 offset:0\n" \
  "ds_read_b64_tr_b16 v[128:129], v7 offset:0\n" \
  "v_mfma_f32_16x16x32_bf16 v[188:191], v[60:63], v[132:135], v[188:191]\n" \
  "ds_read_b128 v[60:63], v21 offset:768\n" \
  "s_waitcnt lgkmcnt(8)\n" \
  "ds_read_b64_tr_b16 v[130:131], v7 offset:1792\n" \
  "buffer_load_dwordx4 a[76:79], v5, s[36:39], s80 offen sc1\n" \
  "v_mfma_f32_16x16x32_bf16 v[192:195], v[84:87], v[132:135], v[192:195]\n" \
  "ds_read_b128 v[84:87], v21 offset:12288\n" \
  "s_add_u32 s73, s69, 327680\n" \
  "v_mfma_f32_16x16x32_bf16 v[196:199], v[108:111], v[132:135], v[196:199]\n" \
  "ds_read_b128 v[108:111], v21 offset:13056\n" \
  "buffer_store_dwordx4 v[200:203], v22, s[36:39], s73 offen sc1\n" \
  "v_mfma_f32_16x16x32_bf16 v[184:187], v[40:43], v[136:139], v[184:187]\n" \
  "ds_read_b128 v[40:43], v20 offset:128\n" \
  "ds_read_b64_tr_b16 v[132:133], v7 offset:14336\n" \
  "v_mfma_f32_16x16x32_bf16 v[188:191], v[64:67], v[136:139], v[188:191]\n" \
  "ds_read_b128 v[64:67], v20 offset:896\n" \
  "ds_read_b64_tr_b16 v[134:135], v7 offset:16128\n" \
  "v_mfma_f32_16x16x32_bf16 v[192:195], v[88:91], v[136:139], v[192:195]\n" \
  "s_waitcnt lgkmcnt(8)\n" \
  "ds_read_b128 v[88:91], v20 offset:12416\n" \
  "v_cvt_pk_bf16_f32 v204, v176, v177\n" \
  "v_cvt_pk_bf16_f32 v205, v178, v179\n" \
  "v_mfma_f32_16x16x32_bf16 v[196:199], v[112:115], v[136:139], v[196:199]\n" \
  "ds_read_b128 v[112:115], v20 offset:13184\n" \
  "v_cvt_pk_bf16_f32 v206, v180, v181\n" \
  "v_cvt_pk_bf16_f32 v207, v182, v183\n" \
  "v_mfma_f32_16x16x32_bf16 v[184:187], v[44:47], v[140:143], v[184:187]\n" \
  "ds_read_b128 v[44:47], v21 offset:128\n" \
  "ds_read_b64_tr_b16 v[136:137], v7 offset:28672\n" \
  "v_mfma_f32_16x16x32_bf16 v[188:191], v[68:71], v[140:143], v[188:191]\n" \
  "ds_read_b128 v[68:71], v21 offset:896\n" \
  "ds_read_b64_tr_b16 v[138:139], v7 offset:30464\n" \
  "v_mfma_f32_16x16x32_bf16 v[192:195], v[92:95], v[140:143], v[192:195]\n" \
  "ds_read_b128 v[92:95], v21 offset:12416\n" \
  "s_add_u32 s73, s70, 327680\n" \
  "v_mfma_f32_16x16x32_bf16 v[196:199], v[116:119], v[140:143], v[196:199]\n" \
  "s_waitcnt lgkmcnt(8)\n" \
  "ds_read_b128 v[116:119], v21 offset:13184\n" \
  "buffer_store_dwordx4 v[204:207], v22, s[36:39], s73 offen sc1\n" \
  "v_mfma_f32_16x16x32_bf16 v[184:187], v[48:51], v[144:147], v[184:187]\n" \
  "ds_read_b128 v[48:51], v20 offset:256\n" \
  "ds_read_b64_tr_b16 v[140:141], v27 offset:0\n" \
  "v_mfma_f32_16x16x32_bf16 v[188:191], v[72:75], v[144:147], v[188:191]\n" \
  "ds_read_b128 v[72:75], v20 offset:1024\n" \
  "ds_read_b64_tr_b16 v[142:143], v27 offset:1792\n" \
  "v_mfma_f32_16x16x32_bf16 v[192:195], v[96:99], v[144:147], v[192:195]\n" \
  "ds_read_b128 v[96:99], v20 offset:12544\n" \
  "s_cmp_lg_u32 s78, 0\n" \
  "v_mfma_f32_16x16x32_bf16 v[196:199], v[120:123], v[144:147], v[196:199]\n" \
  "ds_read_b128 v[120:123], v20 offset:13312\n" \
  "v_mfma_f32_16x16x32_bf16 v[184:187], v[52:55], v[148:151], v[184:187]\n" \
  "s_waitcnt lgkmcnt(8)\n" \
  "ds_read_b128 v[52:55], v21 offset:256\n" \
  "ds_read_b64_tr_b16 v[144:145], v27 offset:14336\n" \
  "v_mfma_f32_16x16x32_bf16 v[188:191], v[76:79], v[148:151], v[188:191]\n" \
  "ds_read_b128 v[76:79], v21 offset:1024\n" \
  "ds_read_b64_tr_b16 v[146:147], v27 offset:16128\n" \
  "v_mfma_f32_16x16x32_bf16 v[192:195], v[100:103], v[148:151], v[192:195]\n" \
  "ds_read_b128 v[100:103], v21 offset:12544\n" \
  "v_mfma_f32_16x16x32_bf16 v[196:199], v[124:127], v[148:151], v[196:199]\n" \
  "ds_read_b128 v[124:127], v21 offset:13312\n" \
  "LUN3_%=:\n" \
  "v_mfma_f32_16x16x32_bf16 v[152:155], v[32:35], v[128:131], 0\n" \
  "ds_read_b64_tr_b16 v[148:149], v27 offset:28672\n" \
  "v_mfma_f32_16x16x32_bf16 v[156:159], v[56:59], v[128:131], 0\n" \
  "s_waitcnt lgkmcnt(8)\n" \
  "ds_read_b64_tr_b16 v[150:151], v27 offset:30464\n" \
  "v_mfma_f32_16x16x32_bf16 v[160:163], v[80:83], v[128:131], 0\n" \
  "v_cvt_pk_bf16_f32 v200, v184, v185\n" \
  "v_cvt_pk_bf16_f32 v201, v186, v187\n" \
  "v_mfma_f32_16x16x32_bf16 v[164:167], v[104:107], v[128:131], 0\n" \
  "v_cvt_pk_bf16_f32 v202, v188, v189\n" \
  "v_cvt_pk_bf16_f32 v203, v190, v191\n" \
  "v_mfma_f32_16x16x32_bf16 v[152:155], v[36:39], v[132:135], v[152:155]\n" \
  "ds_read_b64_tr_b16 v[128:129], v26 offset:0\n" \
  "v_mfma_f32_16x16x32_bf16 v[156:159], v[60:63], v[132:135], v[156:159]\n" \
  "ds_read_b64_tr_b16 v[130:131], v26 offset:1792\n" \
  "v_mfma_f32_16x16x32_bf16 v[160:163], v[84:87], v[132:135], v[160:163]\n" \
  "s_add_u32 s73, s71, 393216\n" \
  "v_mfma_f32_16x16x32_bf16 v[164:167], v[108:111], v[132:135], v[164:167]\n" \
  "buffer_store_dwordx4 v[200:203], v22, s[36:39], s73 offen sc1\n" \
  "v_mfma_f32_16x16x32_bf16 v[152:155], v[40:43], v[136:139], v[152:155]\n" \
  "ds_read_b64_tr_b16 v[132:133], v26 offset:14336\n" \
  "v_mfma_f32_16x16x32_bf16 v[156:159], v[64:67], v[136:139], v[156:159]\n" \
  "ds_read_b64_tr_b16 v[134:135], v26 offset:16128\n" \
  "v_mfma_f32_16x16x32_bf16 v[160:163], v[88:91], v[136:139], v[160:163]\n" \
  "v_cvt_pk_bf16_f32 v204, v192, v193\n" \
  "v_cvt_pk_bf16_f32 v205, v194, v195\n" \
  "v_mfma_f32_16x16x32_bf16 v[164:167], v[112:115], v[136:139], v[164:167]\n" \
  "v_cvt_pk_bf16_f32 v206, v196, v197\n" \
  "v_cvt_pk_bf16_f32 v207, v198, v199\n" \
  "v_mfma_f32_16x16x32_bf16 v[152:155], v[44:47], v[140:143], v[152:155]\n" \
  "ds_read_b64_tr_b16 v[136:137], v26 offset:28672\n" \
  "v_mfma_f32_16x16x32_bf16 v[156:159], v[68:71], v[140:143], v[156:159]\n" \
  "ds_read_b64_tr_b16 v[138:139], v26 offset:30464\n" \
  "v_mfma_f32_16x16x32_bf16 v[160:163], v[92:95], v[140:143], v[160:163]\n" \
  "s_add_u32 s73, s72, 393216\n" \
  "v_mfma_f32_16x16x32_bf16 v[164:167], v[116:119], v[140:143], v[164:167]\n" \
  "buffer_store_dwordx4 v[204:207], v22, s[36:39], s73 offen sc1\n" \
  "s_waitcnt lgkmcnt(10)\n" \
  "v_mfma_f32_16x16x32_bf16 v[152:155], v[48:51], v[144:147], v[152:155]\n" \
  "ds_read_b64_tr_b16 v[140:141], v28 offset:0\n" \
  "v_mfma_f32_16x16x32_bf16 v[156:159], v[72:75], v[144:147], v[156:159]\n" \
  "ds_read_b64_tr_b16 v[142:143], v28 offset:1792\n" \
  "v_mfma_f32_16x16x32_bf16 v[160:163], v[96:99], v[144:147], v[160:163]\n" \
  "v_readlane_b32 s69, v23, s68\n" \
  "v_readlane_b32 s71, v24, s68\n" \
  "s_sub_u32 s69, s69, s62\n" \
  "s_sub_u32 s71, s71, s63\n" \
  "s_add_u32 s68, s68, 1\n" \
  "v_mfma_f32_16x16x32_bf16 v[164:167], v[120:123], v[144:147], v[164:167]\n" \
  "v_readlane_b32 s70, v23, s68\n" \
  "v_readlane_b32 s72, v24, s68\n" \
  "s_sub_u32 s70, s70, s62\n" \
  "s_sub_u32 s72, s72, s63\n" \
  "s_add_u32 s68, s68, 1\n" \
  "s_waitcnt lgkmcnt(8)\n" \
  "v_mfma_f32_16x16x32_bf16 v[152:155], v[52:55], v[148:151], v[152:155]\n" \
  "ds_read_b64_tr_b16 v[144:145], v28 offset:14336\n" \
  "v_mfma_f32_16x16x32_bf16 v[156:159], v[76:79], v[148:151], v[156:159]\n" \
  "ds_read_b64_tr_b16 v[146:147], v28 offset:16128\n" \
  "v_mfma_f32_16x16x32_bf16 v[160:163], v[100:103], v[148:151], v[160:163]\n" \
  "s_cmp_eq_u32 s68, s77\n" \
  "s_cselect_b32 s75, 0x40000000, s75\n" \
  "v_mfma_f32_16x16x32_bf16 v[164:167], v[124:127], v[148:151], v[164:167]\n" \
  "s_barrier\n" \
  "s_waitcnt lgkmcnt(8)\n" \
  "v_mfma_f32_16x16x32_bf16 v[168:171], v[32:35], v[128:131], 0\n" \
  "ds_read_b64_tr_b16 v[148:149], v28 offset:28672\n" \
  "v_mfma_f32_16x16x32_bf16 v[172:175], v[56:59], v[128:131], 0\n" \
  "ds_read_b64_tr_b16 v[150:151], v28 offset:30464\n" \
  "v_mfma_f32_16x16x32_bf16 v[176:179], v[80:83], v[128:131], 0\n" \
  "v_cvt_pk_bf16_f32 v200, v152, v153\n" \
  "v_cvt_pk_bf16_f32 v201, v154, v155\n" \
  "v_mfma_f32_16x16x32_bf16 v[180:183], v[104:107], v[128:131], 0\n" \
  "s_add_u32 m0, s76, 0\n" \
  "s_nop 0\n" \
  "buffer_load_dwordx4 v8, s[40:43], s75 offen offset:0 lds\n" \
  "s_waitcnt lgkmcnt(8)\n" \
  "v_mfma_f32_16x16x32_bf16 v[168:171], v[36:39], v[132:135], v[168:171]\n" \
  "ds_read_b64_tr_b16 v[128:129], v7 offset:64\n" \
  "v_mfma_f32_16x16x32_bf16 v[172:175], v[60:63], v[132:135], v[172:175]\n" \
  "ds_read_b64_tr_b16 v[130:131], v7 offset:1856\n" \
  "v_mfma_f32_16x16x32_bf16 v[176:179], v[84:87], v[132:135], v[176:179]\n" \
  "v_cvt_pk_bf16_f32 v202, v156, v157\n" \
  "v_cvt_pk_bf16_f32 v203, v158, v159\n" \
  "v_mfma_f32_16x16x32_bf16 v[180:183], v[108:111], v[132:135], v[180:183]\n" \
  "s_add_u32 s73, s69, 0\n" \
  "buffer_load_dword v242, v30, s[36:39], s78 offen sc1\n" \
  "s_waitcnt lgkmcnt(8)\n" \
  "v_mfma_f32_16x16x32_bf16 v[168:171], v[40:43], v[136:139], v[168:171]\n" \
  "ds_read_b64_tr_b16 v[132:133], v7 offset:14400\n" \
  "v_mfma_f32_16x16x32_bf16 v[172:175], v[64:67], v[136:139], v[172:175]\n" \
  "ds_read_b64_tr_b16 v[134:135], v7 offset:16192\n" \
  "v_mfma_f32_16x16x32_bf16 v[176:179], v[88:91], v[136:139], v[176:179]\n" \
  "buffer_load_dwordx4 v9, s[40:43], s75 offen offset:1024 lds\n" \
  "v_mfma_f32_16x16x32_bf16 v[180:183], v[112:115], v[136:139], v[180:183]\n" \
  "buffer_store_dwordx4 v[200:203], v22, s[36:39], s73 offen sc1\n" \
  "s_waitcnt lgkmcnt(8)\n" \
  "v_mfma_f32_16x16x32_bf16 v[168:171], v[44:47], v[140:143], v[168:171]\n" \
  "ds_read_b64_tr_b16 v[136:137], v7 offset:28736\n" \
  "v_mfma_f32_16x16x32_bf16 v[172:175], v[68:71], v[140:143], v[172:175]\n" \
  "ds_read_b64_tr_b16 v[138:139], v7 offset:30528\n" \
  "v_mfma_f32_16x16x32_bf16 v[176:179], v[92:95], v[140:143], v[176:179]\n" \
  "v_cvt_pk_bf16_f32 v204, v160, v161\n" \
  "v_cvt_pk_bf16_f32 v205, v162, v163\n" \
  "v_mfma_f32_16x16x32_bf16 v[180:183], v[116:119], v[140:143], v[180:183]\n" \
  "buffer_load_dwordx4 v10, s[40:43], s75 offen offset:2048 lds\n" \
  "s_waitcnt lgkmcnt(8)\n" \
  "v_mfma_f32_16x16x32_bf16 v[168:171], v[48:51], v[144:147], v[168:171]\n" \
  "ds_read_b64_tr_b16 v[140:141], v27 offset:64\n" \
  "v_mfma_f32_16x16x32_bf16 v[172:175], v[72:75], v[144:147], v[172:175]\n" \
  "ds_read_b64_tr_b16 v[142:143], v27 offset:1856\n" \
  "v_mfma_f32_16x16x32_bf16 v[176:179], v[96:99], v[144:147], v[176:179]\n" \
  "v_cvt_pk_bf16_f32 v206, v164, v165\n" \
  "v_cvt_pk_bf16_f32 v207, v166, v167\n" \
  "v_mfma_f32_16x16x32_bf16 v[180:183], v[120:123], v[144:147], v[180:183]\n" \
  "s_add_u32 s73, s70, 0\n" \
  "s_waitcnt lgkmcnt(8)\n" \
  "v_mfma_f32_16x16x32_bf16 v[168:171], v[52:55], v[148:151], v[168:171]\n" \
  "ds_read_b64_tr_b16 v[144:145], v27 offset:14400\n" \
  "v_mfma_f32_16x16x32_bf16 v[172:175], v[76:79], v[148:151], v[172:175]\n" \
  "ds_read_b64_tr_b16 v[146:147], v27 offset:16192\n" \
  "v_mfma_f32_16x16x32_bf16 v[176:179], v[100:103], v[148:151], v[176:179]\n" \
  "buffer_load_dwordx4 v11, s[40:43], s75 offen offset:3072 lds\n" \
  "v_mfma_f32_16x16x32_bf16 v[180:183], v[124:127], v[148:151], v[180:183]\n" \
  "buffer_store_dwordx4 v[204:207], v22, s[36:39], s73 offen sc1\n" \
  "s_waitcnt lgkmcnt(8)\n" \
  "v_mfma_f32_16x16x32_bf16 v[152:155], v[32:35], v[128:131], 0\n" \
  "ds_read_b64_tr_b16 v[148:149], v27 offset:28736\n" \
  "v_mfma_f32_16x16x32_bf16 v[156:159], v[56:59], v[128:131], 0\n" \
  "ds_read_b64_tr_b16 v[150:151], v27 offset:30528\n" \
  "v_mfma_f32_16x16x32_bf16 v[160:163], v[80:83], v[128:131], 0\n" \
  "v_cvt_pk_bf16_f32 v200, v168, v169\n" \
  "v_cvt_pk_bf16_f32 v201, v170, v171\n" \
  "v_mfma_f32_16x16x32_bf16 v[164:167], v[104:107], v[128:131], 0\n" \
  "s_add_u32 m0, s76, 4096\n" \
  "s_nop 0\n" \
  "buffer_load_dwordx4 v12, s[40:43], s75 offen offset:0 lds\n" \
  "s_waitcnt lgkmcnt(8)\n" \
  "v_mfma_f32_16x16x32_bf16 v[152:155], v[36:39], v[132:135], v[152:155]\n" \
  "ds_read_b64_tr_b16 v[128:129], v26 offset:64\n" \
  "v_mfma_f32_16x16x32_bf16 v[156:159], v[60:63], v[132:135], v[156:159]\n" \
  "ds_read_b64_tr_b16 v[130:131], v26 offset:1856\n" \
  "v_mfma_f32_16x16x32_bf16 v[160:163], v[84:87], v[132:135], v[160:163]\n" \
  "v_cvt_pk_bf16_f32 v202, v172, v173\n" \
  "v_cvt_pk_bf16_f32 v203, v174, v175\n" \
  "v_mfma_f32_16x16x32_bf16 v[164:167], v[108:111], v[132:135], v[164:167]\n" \
  "s_add_u32 s73, s69, 65536\n" \
  "s_waitcnt lgkmcnt(8)\n" \
  "v_mfma_f32_16x16x32_bf16 v[152:155], v[40:43], v[136:139], v[152:155]\n" \
  "ds_read_b64_tr_b16 v[132:133], v26 offset:14400\n" \
  "v_mfma_f32_16x16x32_bf16 v[156:159], v[64:67], v[136:139], v[156:159]\n" \
  "ds_read_b64_tr_b16 v[134:135], v26 offset:16192\n" \
  "v_mfma_f32_16x16x32_bf16 v[160:163], v[88:91], v[136:139], v[160:163]\n" \
  "buffer_load_dwordx4 v13, s[40:43], s75 offen offset:1024 lds\n" \
  "v_mfma_f32_16x16x32_bf16 v[164:167], v[112:115], v[136:139], v[164:167]\n" \
  "buffer_store_dwordx4 v[200:203], v22, s[36:39], s73 offen sc1\n" \
  "s_waitcnt lgkmcnt(8)\n" \
  "v_mfma_f32_16x16x32_bf16 v[152:155], v[44:47], v[140:143], v[152:155]\n" \
  "ds_read_b64_tr_b16 v[136:137], v26 offset:28736\n" \
  "v_mfma_f32_16x16x32_bf16 v[156:159], v[68:71], v[140:143], v[156:159]\n" \
  "ds_read_b64_tr_b16 v[138:139], v26 offset:30528\n" \
  "v_mfma_f32_16x16x32_bf16 v[160:163], v[92:95], v[140:143], v[160:163]\n" \
  "v_cvt_pk_bf16_f32 v204, v176, v177\n" \
  "v_cvt_pk_bf16_f32 v205, v178, v179\n" \
  "v_mfma_f32_16x16x32_bf16 v[164:167], v[116:119], v[140:143], v[164:167]\n" \
  "buffer_load_dwordx4 v14, s[40:43], s75 offen offset:2048 lds\n" \
  "s_waitcnt lgkmcnt(8)\n" \
  "v_mfma_f32_16x16x32_bf16 v[152:155], v[48:51], v[144:147], v[152:155]\n" \
  "ds_read_b64_tr_b16 v[140:141], v28 offset:64\n" \
  "v_mfma_f32_16x16x32_bf16 v[156:159], v[72:75], v[144:147], v[156:159]\n" \
  "ds_read_b64_tr_b16 v[142:143], v28 offset:1856\n" \
  "v_mfma_f32_16x16x32_bf16 v[160:163], v[96:99], v[144:147], v[160:163]\n" \
  "v_cvt_pk_bf16_f32 v206, v180, v181\n" \
  "v_cvt_pk_bf16_f32 v207, v182, v183\n" \
  "v_mfma_f32_16x16x32_bf16 v[164:167], v[120:123], v[144:147], v[164:167]\n" \
  "s_add_u32 s73, s70, 65536\n" \
  "s_waitcnt vmcnt(9)\n" \
  "v_cmp_ne_u32 vcc, 1, v242\n" \
  "v_readlane_b32 s79, v224, 2\n" \
  "s_waitcnt lgkmcnt(8)\n" \
  "v_mfma_f32_16x16x32_bf16 v[152:155], v[52:55], v[148:151], v[152:155]\n" \
  "ds_read_b64_tr_b16 v[144:145], v28 offset:14400\n" \
  "v_mfma_f32_16x16x32_bf16 v[156:159], v[76:79], v[148:151], v[156:159]\n" \
  "ds_read_b64_tr_b16 v[146:147], v28 offset:16192\n" \
  "v_mfma_f32_16x16x32_bf16 v[160:163], v[100:103], v[148:151], v[160:163]\n" \
  "buffer_load_dwordx4 v15, s[40:43], s75 offen offset:3072 lds\n" \
  "v_mfma_f32_16x16x32_bf16 v[164:167], v[124:127], v[148:151], v[164:167]\n" \
  "buffer_store_dwordx4 v[204:207], v22, s[36:39], s73 offen sc1\n" \
  "s_cbranch_vccnz LUP3_%=\n" \
  "LUK3_%=:\n" \
  "s_waitcnt lgkmcnt(8)\n" \
  "v_mfma_f32_16x16x32_bf16 v[168:171], v[32:35], v[128:131], 0\n" \
  "ds_read_b64_tr_b16 v[148:149], v28 offset:28736\n" \
  "v_mfma_f32_16x16x32_bf16 v[172:175], v[56:59], v[128:131], 0\n" \
  "ds_read_b64_tr_b16 v[150:151], v28 offset:30528\n" \
  "buffer_load_dwordx4 a[80:83], v1, s[36:39], s79 offen sc1\n" \
  "v_readlane_b32 s80, v224, 3\n" \
  "v_mfma_f32_16x16x32_bf16 v[176:179], v[80:83], v[128:131], 0\n" \
  "v_cvt_pk_bf16_f32 v200, v152, v153\n" \
  "v_cvt_pk_bf16_f32 v201, v154, v155\n" \
  "s_waitcnt vmcnt(14)\n" \
  "s_barrier\n" \
  "s_mov_b32 s78, s88\n" \
  "buffer_atomic_add v0, v25, s[36:39], s78 offen sc0 sc1\n" \
  "s_add_u32 s88, s88, 1536\n" \
  "s_and_b32 s88, s88, 0x3fffffff\n" \
  "v_mfma_f32_16x16x32_bf16 v[180:183], v[104:107], v[128:131], 0\n" \
  "s_add_u32 m0, s76, 8192\n" \
  "s_nop 0\n" \
  "buffer_load_dwordx4 v16, s[40:43], s75 offen offset:0 lds\n" \
  "s_waitcnt lgkmcnt(8)\n" \
  "v_mfma_f32_16x16x32_bf16 v[168:171], v[36:39], v[132:135], v[168:171]\n" \
  "ds_read_b64_tr_b16 v[128:129], v7 offset:128\n" \
  "v_mfma_f32_16x16x32_bf16 v[172:175], v[60:63], v[132:135], v[172:175]\n" \
  "ds_read_b64_tr_b16 v[130:131], v7 offset:1920\n" \
  "buffer_load_dwordx4 a[88:91], v2, s[36:39], s79 offen sc1\n" \
  "v_mfma_f32_16x16x32_bf16 v[176:179], v[84:87], v[132:135], v[176:179]\n" \
  "v_cvt_pk_bf16_f32 v202, v156, v157\n" \
  "v_cvt_pk_bf16_f32 v203, v158, v159\n" \
  "v_mfma_f32_16x16x32_bf16 v[180:183], v[108:111], v[132:135], v[180:183]\n" \
  "s_add_u32 s73, s69, 131072\n" \
  "s_waitcnt lgkmcnt(8)\n" \
  "v_mfma_f32_16x16x32_bf16 v[168:171], v[40:43], v[136:139], v[168:171]\n" \
  "ds_read_b64_tr_b16 v[132:133], v7 offset:14464\n" \
  "v_mfma_f32_16x16x32_bf16 v[172:175], v[64:67], v[136:139], v[172:175]\n" \
  "ds_read_b64_tr_b16 v[134:135], v7 offset:16256\n" \
  "buffer_load_dwordx4 a[96:99], v3, s[36:39], s79 offen sc1\n" \
  "v_mfma_f32_16x16x32_bf16 v[176:179], v[88:91], v[136:139], v[176:179]\n" \
  "buffer_load_dwordx4 v17, s[40:43], s75 offen offset:1024 lds\n" \
  "v_mfma_f32_16x16x32_bf16 v[180:183], v[112:115], v[136:139], v[180:183]\n" \
  "buffer_store_dwordx4 v[200:203], v22, s[36:39], s73 offen sc1\n" \
  "s_waitcnt lgkmcnt(8)\n" \
  "v_mfma_f32_16x16x32_bf16 v[168:171], v[44:47], v[140:143], v[168:171]\n" \
  "ds_read_b64_tr_b16 v[136:137], v7 offset:28800\n" \
  "v_mfma_f32_16x16x32_bf16 v[172:175], v[68:71], v[140:143], v[172:175]\n" \
  "ds_read_b64_tr_b16 v[138:139], v7 offset:30592\n" \
  "buffer_load_dwordx4 a[104:107], v4, s[36:39], s79 offen sc1\n" \
  "v_mfma_f32_16x16x32_bf16 v[176:179], v[92:95], v[140:143], v[176:179]\n" \
  "v_cvt_pk_bf16_f32 v204, v160, v161\n" \
  "v_cvt_pk_bf16_f32 v205, v162, v163\n" \
  "v_mfma_f32_16x16x32_bf16 v[180:183], v[116:119], v[140:143], v[180:183]\n" \
  "buffer_load_dwordx4 v18, s[40:43], s75 offen offset:2048 lds\n" \
  "s_waitcnt lgkmcnt(8)\n" \
  "v_mfma_f32_16x16x32_bf16 v[168:171], v[48:51], v[144:147], v[168:171]\n" \
  "ds_read_b64_tr_b16 v[140:141], v27 offset:128\n" \
  "v_mfma_f32_16x16x32_bf16 v[172:175], v[72:75], v[144:147], v[172:175]\n" \
  "ds_read_b64_tr_b16 v[142:143], v27 offset:1920\n" \
  "buffer_load_dwordx4 a[112:115], v5, s[36:39], s79 offen sc1\n" \
  "v_mfma_f32_16x16x32_bf16 v[176:179], v[96:99], v[144:147], v[176:179]\n" \
  "v_cvt_pk_bf16_f32 v206, v164, v165\n" \
  "v_cvt_pk_bf16_f32 v207, v166, v167\n" \
  "v_mfma_f32_16x16x32_bf16 v[180:183], v[120:123], v[144:147], v[180:183]\n" \
  "s_add_u32 s73, s70, 131072\n" \
  "s_waitcnt lgkmcnt(8)\n" \
  "v_mfma_f32_16x16x32_bf16 v[168:171], v[52:55], v[148:151], v[168:171]\n" \
  "ds_read_b64_tr_b16 v[144:145], v27 offset:14464\n" \
  "v_mfma_f32_16x16x32_bf16 v[172:175], v[76:79], v[148:151], v[172:175]\n" \
  "ds_read_b64_tr_b16 v[146:147], v27 offset:16256\n" \
  "buffer_load_dwordx4 a[84:87], v1, s[36:39], s80 offen sc1\n" \
  "v_readlane_b32 s79, v224, 12\n" \
  "v_mfma_f32_16x16x32_bf16 v[176:179], v[100:103], v[148:151], v[176:179]\n" \
  "buffer_load_dwordx4 v19, s[40:43], s75 offen offset:3072 lds\n" \
  "s_add_u32 s75, s75, 786432\n" \
  "v_mfma_f32_16x16x32_bf16 v[180:183], v[124:127], v[148:151], v[180:183]\n" \
  "buffer_store_dwordx4 v[204:207], v22, s[36:39], s73 offen sc1\n" \
  "s_waitcnt lgkmcnt(8)\n" \
  "v_mfma_f32_16x16x32_bf16 v[152:155], v[32:35], v[128:131], 0\n" \
  "ds_read_b64_tr_b16 v[148:149], v27 offset:28800\n" \
  "v_mfma_f32_16x16x32_bf16 v[156:159], v[56:59], v[128:131], 0\n" \
  "ds_read_b64_tr_b16 v[150:151], v27 offset:30592\n" \
  "buffer_load_dwordx4 a[92:95], v2, s[36:39], s80 offen sc1\n" \
  "v_mfma_f32_16x16x32_bf16 v[160:163], v[80:83], v[128:131], 0\n" \
  "v_cvt_pk_bf16_f32 v200, v168, v169\n" \
  "v_cvt_pk_bf16_f32 v201, v170, v171\n" \
  "v_mfma_f32_16x16x32_bf16 v[164:167], v[104:107], v[128:131], 0\n" \
  "v_cvt_pk_bf16_f32 v202, v172, v173\n" \
  "v_cvt_pk_bf16_f32 v203, v174, v175\n" \
  "s_waitcnt lgkmcnt(8)\n" \
  "v_mfma_f32_16x16x32_bf16 v[152:155], v[36:39], v[132:135], v[152:155]\n" \
  "ds_read_b64_tr_b16 v[128:129], v26 offset:128\n" \
  "v_mfma_f32_16x16x32_bf16 v[156:159], v[60:63], v[132:135], v[156:159]\n" \
  "ds_read_b64_tr_b16 v[130:131], v26 offset:1920\n" \
  "buffer_load_dwordx4 a[100:103], v3, s[36:39], s80 offen sc1\n" \
  "v_mfma_f32_16x16x32_bf16 v[160:163], v[84:87], v[132:135], v[160:163]\n" \
  "s_add_u32 s73, s69, 196608\n" \
  "v_mfma_f32_16x16x32_bf16 v[164:167], v[108:111], v[132:135], v[164:167]\n" \
  "buffer_store_dwordx4 v[200:203], v22, s[36:39], s73 offen sc1\n" \
  "s_waitcnt lgkmcnt(8)\n" \
  "v_mfma_f32_16x16x32_bf16 v[152:155], v[40:43], v[136:139], v[152:155]\n" \
  "ds_read_b64_tr_b16 v[132:133], v26 offset:14464\n" \
  "v_mfma_f32_16x16x32_bf16 v[156:159], v[64:67], v[136:139], v[156:159]\n" \
  "ds_read_b64_tr_b16 v[134:135], v26 offset:16256\n" \
  "buffer_load_dwordx4 a[108:111], v4, s[36:39], s80 offen sc1\n" \
  "v_mfma_f32_16x16x32_bf16 v[160:163], v[88:91], v[136:139], v[160:163]\n" \
  "v_cvt_pk_bf16_f32 v204, v176, v177\n" \
  "v_cvt_pk_bf16_f32 v205, v178, v179\n" \
  "v_mfma_f32_16x16x32_bf16 v[164:167], v[112:115], v[136:139], v[164:167]\n" \
  "v_cvt_pk_bf16_f32 v206, v180, v181\n" \
  "v_cvt_pk_bf16_f32 v207, v182, v183\n" \
  "s_waitcnt lgkmcnt(8)\n" \
  "v_mfma_f32_16x16x32_bf16 v[152:155], v[44:47], v[140:143], v[152:155]\n" \
  "ds_read_b64_tr_b16 v[136:137], v26 offset:28800\n" \
  "v_mfma_f32_16x16x32_bf16 v[156:159], v[68:71], v[140:143], v[156:159]\n" \
  "ds_read_b64_tr_b16 v[138:139], v26 offset:30592\n" \
  "buffer_load_dwordx4 a[116:119], v5, s[36:39], s80 offen sc1\n" \
  "v_mfma_f32_16x16x32_bf16 v[160:163], v[92:95], v[140:143], v[160:163]\n" \
  "s_add_u32 s73, s70, 196608\n" \
  "v_mfma_f32_16x16x32_bf16 v[164:167], v[116:119], v[140:143], v[164:167]\n" \
  "buffer_store_dwordx4 v[204:207], v22, s[36:39], s73 offen sc1\n" \
  "s_waitcnt lgkmcnt(8)\n" \
  "v_mfma_f32_16x16x32_bf16 v[152:155], v[48:51], v[144:147], v[152:155]\n" \
  "ds_read_b64_tr_b16 v[140:141], v28 offset:128\n" \
  "v_mfma_f32_16x16x32_bf16 v[156:159], v[72:75], v[144:147], v[156:159]\n" \
  "ds_read_b64_tr_b16 v[142:143], v28 offset:1920\n" \
  "buffer_load_dwordx4 a[120:123], v1, s[36:39], s79 offen sc1\n" \
  "v_readlane_b32 s80, v224, 13\n" \
  "v_mfma_f32_16x16x32_bf16 v[160:163], v[96:99], v[144:147], v[160:163]\n" \
  "s_getpc_b64 s[82:83]\n" \
  "LCF8_%=:\n" \
  "s_add_u32 s82, s82, LUN4_%=-LCF8_%=\n" \
  "s_addc_u32 s83, s83, 0\n" \
  "v_mbcnt_lo_u32_b32 v240, -1, 0\n" \
  "v_mbcnt_hi_u32_b32 v240, -1, v240\n" \
  "v_lshlrev_b32 v240, 6, v240\n" \
  "global_load_dword v241, v240, s[82:83]\n" \
  "v_mfma_f32_16x16x32_bf16 v[164:167], v[120:123], v[144:147], v[164:167]\n" \
  "s_waitcnt lgkmcnt(8)\n" \
  "v_mfma_f32_16x16x32_bf16 v[152:155], v[52:55], v[148:151], v[152:155]\n" \
  "ds_read_b64_tr_b16 v[144:145], v28 offset:14464\n" \
  "v_mfma_f32_16x16x32_bf16 v[156:159], v[76:79], v[148:151], v[156:159]\n" \
  "ds_read_b64_tr_b16 v[146:147], v28 offset:16256\n" \
  "buffer_load_dwordx4 a[128:131], v2, s[36:39], s79 offen sc1\n" \
  "v_mfma_f32_16x16x32_bf16 v[160:163], v[100:103], v[148:151], v[160:163]\n" \
  "v_mfma_f32_16x16x32_bf16 v[164:167], v[124:127], v[148:151], v[164:167]\n" \
  "s_waitcnt lgkmcnt(8)\n" \
  "v_mfma_f32_16x16x32_bf16 v[168:171], v[32:35], v[128:131], 0\n" \
  "ds_read_b64_tr_b16 v[148:149], v28 offset:28800\n" \
  "v_mfma_f32_16x16x32_bf16 v[172:175], v[56:59], v[128:131], 0\n" \
  "ds_read_b64_tr_b16 v[150:151], v28 offset:30592\n" \
  "buffer_load_dwordx4 a[136:139], v3, s[36:39], s79 offen sc1\n" \
  "v_mfma_f32_16x16x32_bf16 v[176:179], v[80:83], v[128:131], 0\n" \
  "v_cvt_pk_bf16_f32 v200, v152, v153\n" \
  "v_cvt_pk_bf16_f32 v201, v154, v155\n" \
  "v_mfma_f32_16x16x32_bf16 v[180:183], v[104:107], v[128:131], 0\n" \
  "v_cvt_pk_bf16_f32 v202, v156, v157\n" \
  "v_cvt_pk_bf16_f32 v203, v158, v159\n" \
  "s_waitcnt lgkmcnt(8)\n" \
  "v_mfma_f32_16x16x32_bf16 v[168:171], v[36:39], v[132:135], v[168:171]\n" \
  "ds_read_b64_tr_b16 v[128:129], v7 offset:192\n" \
  "v_mfma_f32_16x16x32_bf16 v[172:175], v[60:63], v[132:135], v[172:175]\n" \
  "ds_read_b64_tr_b16 v[130:131], v7 offset:1984\n" \
  "buffer_load_dwordx4 a[144:147], v4, s[36:39], s79 offen sc1\n" \
  "v_mfma_f32_16x16x32_bf16 v[176:179], v[84:87], v[132:135], v[176:179]\n" \
  "s_add_u32 s73, s69, 262144\n" \
  "v_mfma_f32_16x16x32_bf16 v[180:183], v[108:111], v[132:135], v[180:183]\n" \
  "buffer_store_dwordx4 v[200:203], v22, s[36:39], s73 offen sc1\n" \
  "s_waitcnt lgkmcnt(8)\n" \
  "v_mfma_f32_16x16x32_bf16 v[168:171], v[40:43], v[136:139], v[168:171]\n" \
  "ds_read_b64_tr_b16 v[132:133], v7 offset:14528\n" \
  "v_mfma_f32_16x16x32_bf16 v[172:175], v[64:67], v[136:139], v[172:175]\n" \
  "ds_read_b64_tr_b16 v[134:135], v7 offset:16320\n" \
  "buffer_load_dwordx4 a[152:155], v5, s[36:39], s79 offen sc1\n" \
  "v_mfma_f32_16x16x32_bf16 v[176:179], v[88:91], v[136:139], v[176:179]\n" \
  "v_cvt_pk_bf16_f32 v204, v160, v161\n" \
  "v_cvt_pk_bf16_f32 v205, v162, v163\n" \
  "v_mfma_f32_16x16x32_bf16 v[180:183], v[112:115], v[136:139], v[180:183]\n" \
  "v_cvt_pk_bf16_f32 v206, v164, v165\n" \
  "v_cvt_pk_bf16_f32 v207, v166, v167\n" \
  "s_waitcnt lgkmcnt(8)\n" \
  "v_mfma_f32_16x16x32_bf16 v[168:171], v[44:47], v[140:143], v[168:171]\n" \
  "ds_read_b64_tr_b16 v[136:137], v7 offset:28864\n" \
  "v_mfma_f32_16x16x32_bf16 v[172:175], v[68:71], v[140:143], v[172:175]\n" \
  "ds_read_b64_tr_b16 v[138:139], v7 offset:30656\n" \
  "buffer_load_dwordx4 a[124:127], v1, s[36:39], s80 offen sc1\n" \
  "v_mfma_f32_16x16x32_bf16 v[176:179], v[92:95], v[140:143], v[176:179]\n" \
  "s_add_u32 s73, s70, 262144\n" \
  "v_mfma_f32_16x16x32_bf16 v[180:183], v[116:119], v[140:143], v[180:183]\n" \
  "buffer_store_dwordx4 v[204:207], v22, s[36:39], s73 offen sc1\n" \
  "s_waitcnt lgkmcnt(8)\n" \
  "v_mfma_f32_16x16x32_bf16 v[168:171], v[48:51], v[144:147], v[168:171]\n" \
  "ds_read_b64_tr_b16 v[140:141], v27 offset:192\n" \
  "v_mfma_f32_16x16x32_bf16 v[172:175], v[72:75], v[144:147], v[172:175]\n" \
  "ds_read_b64_tr_b16 v[142:143], v27 offset:1984\n" \
  "buffer_load_dwordx4 a[132:135], v2, s[36:39], s80 offen sc1\n" \
  "v_mfma_f32_16x16x32_bf16 v[176:179], v[96:99], v[144:147], v[176:179]\n" \
  "v_mfma_f32_16x16x32_bf16 v[180:183], v[120:123], v[144:147], v[180:183]\n" \
  "s_waitcnt lgkmcnt(8)\n" \
  "v_mfma_f32_16x16x32_bf16 v[168:171], v[52:55], v[148:151], v[168:171]\n" \
  "ds_read_b64_tr_b16 v[144:145], v27 offset:14528\n" \
  "v_mfma_f32_16x16x32_bf16 v[172:175], v[76:79], v[148:151], v[172:175]\n" \
  "ds_read_b64_tr_b16 v[146:147], v27 offset:16320\n" \
  "buffer_load_dwordx4 a[140:143], v3, s[36:39], s80 offen sc1\n" \
  "v_mfma_f32_16x16x32_bf16 v[176:179], v[100:103], v[148:151], v[176:179]\n" \
  "v_mfma_f32_16x16x32_bf16 v[180:183], v[124:127], v[148:151], v[180:183]\n" \
  "s_waitcnt vmcnt(18)\n" \
  "s_barrier\n" \
  "v_readfirstlane_b32 s74, v0\n" \
  "v_mov_b32 v0, 1\n" \
  "s_cmp_eq_u32 s74, 11\n" \
  "s_cselect_b32 s73, s78, 0x40000000\n" \
  "buffer_store_dword v0, v31, s[36:39], s73 offen sc1\n" \
  "s_waitcnt lgkmcnt(8)\n" \
  "v_mfma_f32_16x16x32_bf16 v[184:187], v[32:35], v[128:131], 0\n" \
  "ds_read_b128 v[32:35], v20 offset:0\n" \
  "ds_read_b64_tr_b16 v[148:149], v27 offset:28864\n" \
  "v_mfma_f32_16x16x32_bf16 v[188:191], v[56:59], v[128:131], 0\n" \
  "ds_read_b128 v[56:59], v20 offset:768\n" \
  "ds_read_b64_tr_b16 v[150:151], v27 offset:30656\n" \
  "buffer_load_dwordx4 a[148:151], v4, s[36:39], s80 offen sc1\n" \
  "v_mfma_f32_16x16x32_bf16 v[192:195], v[80:83], v[128:131], 0\n" \
  "ds_read_b128 v[80:83], v20 offset:12288\n" \
  "v_cvt_pk_bf16_f32 v200, v168, v169\n" \
  "v_cvt_pk_bf16_f32 v201, v170, v171\n" \
  "v_mfma_f32_16x16x32_bf16 v[196:199], v[104:107], v[128:131], 0\n" \
  "ds_read_b128 v[104:107], v20 offset:13056\n" \
  "v_cvt_pk_bf16_f32 v202, v172, v173\n" \
  "v_cvt_pk_bf16_f32 v203, v174, v175\n" \
  "s_waitcnt lgkmcnt(12)\n" \
  "v_mfma_f32_16x16x32_bf16 v[184:187], v[36:39], v[132:135], v[184:187]\n" \
  "ds_read_b128 v[36:39], v21 offset:0\n" \
  "ds_read_b64_tr_b16 v[128:129], v7 offset:0\n" \
  "v_mfma_f32_16x16x32_bf16 v[188:191], v[60:63], v[132:135], v[188:191]\n" \
  "ds_read_b128 v[60:63], v21 offset:768\n" \
  "s_waitcnt lgkmcnt(8)\n" \
  "ds_read_b64_tr_b16 v[130:131], v7 offset:1792\n" \
  "buffer_load_dwordx4 a[156:159], v5, s[36:39], s80 offen sc1\n" \
  "v_mfma_f32_16x16x32_bf16 v[192:195], v[84:87], v[132:135], v[192:195]\n" \
  "ds_read_b128 v[84:87], v21 offset:12288\n" \
  "s_add_u32 s73, s69, 327680\n" \
  "v_mfma_f32_16x16x32_bf16 v[196:199], v[108:111], v[132:135], v[196:199]\n" \
  "ds_read_b128 v[108:111], v21 offset:13056\n" \
  "buffer_store_dwordx4 v[200:203], v22, s[36:39], s73 offen sc1\n" \
  "v_mfma_f32_16x16x32_bf16 v[184:187], v[40:43], v[136:139], v[184:187]\n" \
  "ds_read_b128 v[40:43], v20 offset:128\n" \
  "ds_read_b64_tr_b16 v[132:133], v7 offset:14336\n" \
  "v_mfma_f32_16x16x32_bf16 v[188:191], v[64:67], v[136:139], v[188:191]\n" \
  "ds_read_b128 v[64:67], v20 offset:896\n" \
  "ds_read_b64_tr_b16 v[134:135], v7 offset:16128\n" \
  "v_mfma_f32_16x16x32_bf16 v[192:195], v[88:91], v[136:139], v[192:195]\n" \
  "s_waitcnt lgkmcnt(8)\n" \
  "ds_read_b128 v[88:91], v20 offset:12416\n" \
  "v_cvt_pk_bf16_f32 v204, v176, v177\n" \
  "v_cvt_pk_bf16_f32 v205, v178, v179\n" \
  "v_mfma_f32_16x16x32_bf16 v[196:199], v[112:115], v[136:139], v[196:199]\n" \
  "ds_read_b128 v[112:115], v20 offset:13184\n" \
  "v_cvt_pk_bf16_f32 v206, v180, v181\n" \
  "v_cvt_pk_bf16_f32 v207, v182, v183\n" \
  "v_mfma_f32_16x16x32_bf16 v[184:187], v[44:47], v[140:143], v[184:187]\n" \
  "ds_read_b128 v[44:47], v21 offset:128\n" \
  "ds_read_b64_tr_b16 v[136:137], v7 offset:28672\n" \
  "v_mfma_f32_16x16x32_bf16 v[188:191], v[68:71], v[140:143], v[188:191]\n" \
  "ds_read_b128 v[68:71], v21 offset:896\n" \
  "ds_read_b64_tr_b16 v[138:139], v7 offset:30464\n" \
  "v_mfma_f32_16x16x32_bf16 v[192:195], v[92:95], v[140:143], v[192:195]\n" \
  "ds_read_b128 v[92:95], v21 offset:12416\n" \
  "s_add_u32 s73, s70, 327680\n" \
  "v_mfma_f32_16x16x32_bf16 v[196:199], v[116:119], v[140:143], v[196:199]\n" \
  "s_waitcnt lgkmcnt(8)\n" \
  "ds_read_b128 v[116:119], v21 offset:13184\n" \
  "buffer_store_dwordx4 v[204:207], v22, s[36:39], s73 offen sc1\n" \
  "v_mfma_f32_16x16x32_bf16 v[184:187], v[48:51], v[144:147], v[184:187]\n" \
  "ds_read_b128 v[48:51], v20 offset:256\n" \
  "ds_read_b64_tr_b16 v[140:141], v27 offset:0\n" \
  "v_mfma_f32_16x16x32_bf16 v[188:191], v[72:75], v[144:147], v[188:191]\n" \
  "ds_read_b128 v[72:75], v20 offset:1024\n" \
  "ds_read_b64_tr_b16 v[142:143], v27 offset:1792\n" \
  "v_mfma_f32_16x16x32_bf16 v[192:195], v[96:99], v[144:147], v[192:195]\n" \
  "ds_read_b128 v[96:99], v20 offset:12544\n" \
  "s_cmp_lg_u32 s78, 0\n" \
  "v_mfma_f32_16x16x32_bf16 v[196:199], v[120:123], v[144:147], v[196:199]\n" \
  "ds_read_b128 v[120:123], v20 offset:13312\n" \
  "v_mfma_f32_16x16x32_bf16 v[184:187], v[52:55], v[148:151], v[184:187]\n" \
  "s_waitcnt lgkmcnt(8)\n" \
  "ds_read_b128 v[52:55], v21 offset:256\n" \
  "ds_read_b64_tr_b16 v[144:145], v27 offset:14336\n" \
  "v_mfma_f32_16x16x32_bf16 v[188:191], v[76:79], v[148:151], v[188:191]\n" \
  "ds_read_b128 v[76:79], v21 offset:1024\n" \
  "ds_read_b64_tr_b16 v[146:147], v27 offset:16128\n" \
  "v_mfma_f32_16x16x32_bf16 v[192:195], v[100:103], v[148:151], v[192:195]\n" \
  "ds_read_b128 v[100:103], v21 offset:12544\n" \
  "v_mfma_f32_16x16x32_bf16 v[196:199], v[124:127], v[148:151], v[196:199]\n" \
  "ds_read_b128 v[124:127], v21 offset:13312\n" \
  "LUN4_%=:\n" \
  "v_mfma_f32_16x16x32_bf16 v[152:155], v[32:35], v[128:131], 0\n" \
  "ds_read_b64_tr_b16 v[148:149], v27 offset:28672\n" \
  "v_mfma_f32_16x16x32_bf16 v[156:159], v[56:59], v[128:131], 0\n" \
  "s_waitcnt lgkmcnt(8)\n" \
  "ds_read_b64_tr_b16 v[150:151], v27 offset:30464\n" \
  "v_mfma_f32_16x16x32_bf16 v[160:163], v[80:83], v[128:131], 0\n" \
  "v_cvt_pk_bf16_f32 v200, v184, v185\n" \
  "v_cvt_pk_bf16_f32 v201, v186, v187\n" \
  "v_mfma_f32_16x16x32_bf16 v[164:167], v[104:107], v[128:131], 0\n" \
  "v_cvt_pk_bf16_f32 v202, v188, v189\n" \
  "v_cvt_pk_bf16_f32 v203, v190, v191\n" \
  "v_mfma_f32_16x16x32_bf16 v[152:155], v[36:39], v[132:135], v[152:155]\n" \
  "ds_read_b64_tr_b16 v[128:129], v26 offset:0\n" \
  "v_mfma_f32_16x16x32_bf16 v[156:159], v[60:63], v[132:135], v[156:159]\n" \
  "ds_read_b64_tr_b16 v[130:131], v26 offset:1792\n" \
  "v_mfma_f32_16x16x32_bf16 v[160:163], v[84:87], v[132:135], v[160:163]\n" \
  "s_add_u32 s73, s71, 393216\n" \
  "v_mfma_f32_16x16x32_bf16 v[164:167], v[108:111], v[132:135], v[164:167]\n" \
  "buffer_store_dwordx4 v[200:203], v22, s[36:39], s73 offen sc1\n" \
  "v_mfma_f32_16x16x32_bf16 v[152:155], v[40:43], v[136:139], v[152:155]\n" \
  "ds_read_b64_tr_b16 v[132:133], v26 offset:14336\n" \
  "v_mfma_f32_16x16x32_bf16 v[156:159], v[64:67], v[136:139], v[156:159]\n" \
  "ds_read_b64_tr_b16 v[134:135], v26 offset:16128\n" \
  "v_mfma_f32_16x16x32_bf16 v[160:163], v[88:91], v[136:139], v[160:163]\n" \
  "v_cvt_pk_bf16_f32 v204, v192, v193\n" \
  "v_cvt_pk_bf16_f32 v205, v194, v195\n" \
  "v_mfma_f32_16x16x32_bf16 v[164:167], v[112:115], v[136:139], v[164:167]\n" \
  "v_cvt_pk_bf16_f32 v206, v196, v197\n" \
  "v_cvt_pk_bf16_f32 v207, v198, v199\n" \
  "v_mfma_f32_16x16x32_bf16 v[152:155], v[44:47], v[140:143], v[152:155]\n" \
  "ds_read_b64_tr_b16 v[136:137], v26 offset:28672\n" \
  "v_mfma_f32_16x16x32_bf16 v[156:159], v[68:71], v[140:143], v[156:159]\n" \
  "ds_read_b64_tr_b16 v[138:139], v26 offset:30464\n" \
  "v_mfma_f32_16x16x32_bf16 v[160:163], v[92:95], v[140:143], v[160:163]\n" \
  "s_add_u32 s73, s72, 393216\n" \
  "v_mfma_f32_16x16x32_bf16 v[164:167], v[116:119], v[140:143], v[164:167]\n" \
  "buffer_store_dwordx4 v[204:207], v22, s[36:39], s73 offen sc1\n" \
  "s_waitcnt lgkmcnt(10)\n" \
  "v_mfma_f32_16x16x32_bf16 v[152:155], v[48:51], v[144:147], v[152:155]\n" \
  "ds_read_b64_tr_b16 v[140:141], v28 offset:0\n" \
  "v_mfma_f32_16x16x32_bf16 v[156:159], v[72:75], v[144:147], v[156:159]\n" \
  "ds_read_b64_tr_b16 v[142:143], v28 offset:1792\n" \
  "v_mfma_f32_16x16x32_bf16 v[160:163], v[96:99], v[144:147], v[160:163]\n" \
  "v_readlane_b32 s69, v23, s68\n" \
  "v_readlane_b32 s71, v24, s68\n" \
  "s_sub_u32 s69, s69, s62\n" \
  "s_sub_u32 s71, s71, s63\n" \
  "s_add_u32 s68, s68, 1\n" \
  "v_mfma_f32_16x16x32_bf16 v[164:167], v[120:123], v[144:147], v[164:167]\n" \
  "v_readlane_b32 s70, v23, s68\n" \
  "v_readlane_b32 s72, v24, s68\n" \
  "s_sub_u32 s70, s70, s62\n" \
  "s_sub_u32 s72, s72, s63\n" \
  "s_add_u32 s68, s68, 1\n" \
  "s_waitcnt lgkmcnt(8)\n" \
  "v_mfma_f32_16x16x32_bf16 v[152:155], v[52:55], v[148:151], v[152:155]\n" \
  "ds_read_b64_tr_b16 v[144:145], v28 offset:14336\n" \
  "v_mfma_f32_16x16x32_bf16 v[156:159], v[76:79], v[148:151], v[156:159]\n" \
  "ds_read_b64_tr_b16 v[146:147], v28 offset:16128\n" \
  "v_mfma_f32_16x16x32_bf16 v[160:163], v[100:103], v[148:151], v[160:163]\n" \
  "s_cmp_eq_u32 s68, s77\n" \
  "s_cselect_b32 s75, 0x40000000, s75\n" \
  "v_mfma_f32_16x16x32_bf16 v[164:167], v[124:127], v[148:151], v[164:167]\n" \
  "s_barrier\n" \
  "s_waitcnt lgkmcnt(8)\n" \
  "v_mfma_f32_16x16x32_bf16 v[168:171], v[32:35], v[128:131], 0\n" \
  "ds_read_b64_tr_b16 v[148:149], v28 offset:28672\n" \
  "v_mfma_f32_16x16x32_bf16 v[172:175], v[56:59], v[128:131], 0\n" \
  "ds_read_b64_tr_b16 v[150:151], v28 offset:30464\n" \
  "v_mfma_f32_16x16x32_bf16 v[176:179], v[80:83], v[128:131], 0\n" \
  "v_cvt_pk_bf16_f32 v200, v152, v153\n" \
  "v_cvt_pk_bf16_f32 v201, v154, v155\n" \
  "v_mfma_f32_16x16x32_bf16 v[180:183], v[104:107], v[128:131], 0\n" \
  "s_add_u32 m0, s76, 0\n" \
  "s_nop 0\n" \
  "buffer_load_dwordx4 v8, s[40:43], s75 offen offset:0 lds\n" \
  "s_waitcnt lgkmcnt(8)\n" \
  "v_mfma_f32_16x16x32_bf16 v[168:171], v[36:39], v[132:135], v[168:171]\n" \
  "ds_read_b64_tr_b16 v[128:129], v7 offset:64\n" \
  "v_mfma_f32_16x16x32_bf16 v[172:175], v[60:63], v[132:135], v[172:175]\n" \
  "ds_read_b64_tr_b16 v[130:131], v7 offset:1856\n" \
  "v_mfma_f32_16x16x32_bf16 v[176:179], v[84:87], v[132:135], v[176:179]\n" \
  "v_cvt_pk_bf16_f32 v202, v156, v157\n" \
  "v_cvt_pk_bf16_f32 v203, v158, v159\n" \
  "v_mfma_f32_16x16x32_bf16 v[180:183], v[108:111], v[132:135], v[180:183]\n" \
  "s_add_u32 s73, s69, 0\n" \
  "buffer_load_dword v242, v30, s[36:39], s78 offen sc1\n" \
  "s_waitcnt lgkmcnt(8)\n" \
  "v_mfma_f32_16x16x32_bf16 v[168:171], v[40:43], v[136:139], v[168:171]\n" \
  "ds_read_b64_tr_b16 v[132:133], v7 offset:14400\n" \
  "v_mfma_f32_16x16x32_bf16 v[172:175], v[64:67], v[136:139], v[172:175]\n" \
  "ds_read_b64_tr_b16 v[134:135], v7 offset:16192\n" \
  "v_mfma_f32_16x16x32_bf16 v[176:179], v[88:91], v[136:139], v[176:179]\n" \
  "buffer_load_dwordx4 v9, s[40:43], s75 offen offset:1024 lds\n" \
  "v_mfma_f32_16x16x32_bf16 v[180:183], v[112:115], v[136:139], v[180:183]\n" \
  "buffer_store_dwordx4 v[200:203], v22, s[36:39], s73 offen sc1\n" \
  "s_waitcnt lgkmcnt(8)\n" \
  "v_mfma_f32_16x16x32_bf16 v[168:171], v[44:47], v[140:143], v[168:171]\n" \
  "ds_read_b64_tr_b16 v[136:137], v7 offset:28736\n" \
  "v_mfma_f32_16x16x32_bf16 v[172:175], v[68:71], v[140:143], v[172:175]\n" \
  "ds_read_b64_tr_b16 v[138:139], v7 offset:30528\n" \
  "v_mfma_f32_16x16x32_bf16 v[176:179], v[92:95], v[140:143], v[176:179]\n" \
  "v_cvt_pk_bf16_f32 v204, v160, v161\n" \
  "v_cvt_pk_bf16_f32 v205, v162, v163\n" \
  "v_mfma_f32_16x16x32_bf16 v[180:183], v[116:119], v[140:143], v[180:183]\n" \
  "buffer_load_dwordx4 v10, s[40:43], s75 offen offset:2048 lds\n" \
  "s_waitcnt lgkmcnt(8)\n" \
  "v_mfma_f32_16x16x32_bf16 v[168:171], v[48:51], v[144:147], v[168:171]\n" \
  "ds_read_b64_tr_b16 v[140:141], v27 offset:64\n" \
  "v_mfma_f32_16x16x32_bf16 v[172:175], v[72:75], v[144:147], v[172:175]\n" \
  "ds_read_b64_tr_b16 v[142:143], v27 offset:1856\n" \
  "v_mfma_f32_16x16x32_bf16 v[176:179], v[96:99], v[144:147], v[176:179]\n" \
  "v_cvt_pk_bf16_f32 v206, v164, v165\n" \
  "v_cvt_pk_bf16_f32 v207, v166, v167\n" \
  "v_mfma_f32_16x16x32_bf16 v[180:183], v[120:123], v[144:147], v[180:183]\n" \
  "s_add_u32 s73, s70, 0\n" \
  "s_waitcnt lgkmcnt(8)\n" \
  "v_mfma_f32_16x16x32_bf16 v[168:171], v[52:55], v[148:151], v[168:171]\n" \
  "ds_read_b64_tr_b16 v[144:145], v27 offset:14400\n" \
  "v_mfma_f32_16x16x32_bf16 v[172:175], v[76:79], v[148:151], v[172:175]\n" \
  "ds_read_b64_tr_b16 v[146:147], v27 offset:16192\n" \
  "v_mfma_f32_16x16x32_bf16 v[176:179], v[100:103], v[148:151], v[176:179]\n" \
  "buffer_load_dwordx4 v11, s[40:43], s75 offen offset:3072 lds\n" \
  "v_mfma_f32_16x16x32_bf16 v[180:183], v[124:127], v[148:151], v[180:183]\n" \
  "buffer_store_dwordx4 v[204:207], v22, s[36:39], s73 offen sc1\n" \
  "s_waitcnt lgkmcnt(8)\n" \
  "v_mfma_f32_16x16x32_bf16 v[152:155], v[32:35], v[128:131], 0\n" \
  "ds_read_b64_tr_b16 v[148:149], v27 offset:28736\n" \
  "v_mfma_f32_16x16x32_bf16 v[156:159], v[56:59], v[128:131], 0\n" \
  "ds_read_b64_tr_b16 v[150:151], v27 offset:30528\n" \
  "v_mfma_f32_16x16x32_bf16 v[160:163], v[80:83], v[128:131], 0\n" \
  "v_cvt_pk_bf16_f32 v200, v168, v169\n" \
  "v_cvt_pk_bf16_f32 v201, v170, v171\n" \
  "v_mfma_f32_16x16x32_bf16 v[164:167], v[104:107], v[128:131], 0\n" \
  "s_add_u32 m0, s76, 4096\n" \
  "s_nop 0\n" \
  "buffer_load_dwordx4 v12, s[40:43], s75 offen offset:0 lds\n" \
  "s_waitcnt lgkmcnt(8)\n" \
  "v_mfma_f32_16x16x32_bf16 v[152:155], v[36:39], v[132:135], v[152:155]\n" \
  "ds_read_b64_tr_b16 v[128:129], v26 offset:64\n" \
  "v_mfma_f32_16x16x32_bf16 v[156:159], v[60:63], v[132:135], v[156:159]\n" \
  "ds_read_b64_tr_b16 v[130:131], v26 offset:1856\n" \
  "v_mfma_f32_16x16x32_bf16 v[160:163], v[84:87], v[132:135], v[160:163]\n" \
  "v_cvt_pk_bf16_f32 v202, v172, v173\n" \
  "v_cvt_pk_bf16_f32 v203, v174, v175\n" \
  "v_mfma_f32_16x16x32_bf16 v[164:167], v[108:111], v[132:135], v[164:167]\n" \
  "s_add_u32 s73, s69, 65536\n" \
  "s_waitcnt lgkmcnt(8)\n" \
  "v_mfma_f32_16x16x32_bf16 v[152:155], v[40:43], v[136:139], v[152:155]\n" \
  "ds_read_b64_tr_b16 v[132:133], v26 offset:14400\n" \
  "v_mfma_f32_16x16x32_bf16 v[156:159], v[64:67], v[136:139], v[156:159]\n" \
  "ds_read_b64_tr_b16 v[134:135], v26 offset:16192\n" \
  "v_mfma_f32_16x16x32_bf16 v[160:163], v[88:91], v[136:139], v[160:163]\n" \
  "buffer_load_dwordx4 v13, s[40:43], s75 offen offset:1024 lds\n" \
  "v_mfma_f32_16x16x32_bf16 v[164:167], v[112:115], v[136:139], v[164:167]\n" \
  "buffer_store_dwordx4 v[200:203], v22, s[36:39], s73 offen sc1\n" \
  "s_waitcnt lgkmcnt(8)\n" \
  "v_mfma_f32_16x16x32_bf16 v[152:155], v[44:47], v[140:143], v[152:155]\n" \
  "ds_read_b64_tr_b16 v[136:137], v26 offset:28736\n" \
  "v_mfma_f32_16x16x32_bf16 v[156:159], v[68:71], v[140:143], v[156:159]\n" \
  "ds_read_b64_tr_b16 v[138:139], v26 offset:30528\n" \
  "v_mfma_f32_16x16x32_bf16 v[160:163], v[92:95], v[140:143], v[160:163]\n" \
  "v_cvt_pk_bf16_f32 v204, v176, v177\n" \
  "v_cvt_pk_bf16_f32 v205, v178, v179\n" \
  "v_mfma_f32_16x16x32_bf16 v[164:167], v[116:119], v[140:143], v[164:167]\n" \
  "buffer_load_dwordx4 v14, s[40:43], s75 offen offset:2048 lds\n" \
  "s_waitcnt lgkmcnt(8)\n" \
  "v_mfma_f32_16x16x32_bf16 v[152:155], v[48:51], v[144:147], v[152:155]\n" \
  "ds_read_b64_tr_b16 v[140:141], v28 offset:64\n" \
  "v_mfma_f32_16x16x32_bf16 v[156:159], v[72:75], v[144:147], v[156:159]\n" \
  "ds_read_b64_tr_b16 v[142:143], v28 offset:1856\n" \
  "v_mfma_f32_16x16x32_bf16 v[160:163], v[96:99], v[144:147], v[160:163]\n" \
  "v_cvt_pk_bf16_f32 v206, v180, v181\n" \
  "v_cvt_pk_bf16_f32 v207, v182, v183\n" \
  "v_mfma_f32_16x16x32_bf16 v[164:167], v[120:123], v[144:147], v[164:167]\n" \
  "s_add_u32 s73, s70, 65536\n" \
  "s_waitcnt vmcnt(9)\n" \
  "v_cmp_ne_u32 vcc, 1, v242\n" \
  "v_readlane_b32 s79, v224, 4\n" \
  "s_waitcnt lgkmcnt(8)\n" \
  "v_mfma_f32_16x16x32_bf16 v[152:155], v[52:55], v[148:151], v[152:155]\n" \
  "ds_read_b64_tr_b16 v[144:145], v28 offset:14400\n" \
  "v_mfma_f32_16x16x32_bf16 v[156:159], v[76:79], v[148:151], v[156:159]\n" \
  "ds_read_b64_tr_b16 v[146:147], v28 offset:16192\n" \
  "v_mfma_f32_16x16x32_bf16 v[160:163], v[100:103], v[148:151], v[160:163]\n" \
  "buffer_load_dwordx4 v15, s[40:43], s75 offen offset:3072 lds\n" \
  "v_mfma_f32_16x16x32_bf16 v[164:167], v[124:127], v[148:151], v[164:167]\n" \
  "buffer_store_dwordx4 v[204:207], v22, s[36:39], s73 offen sc1\n" \
  "s_cbranch_vccnz LUP4_%=\n" \
  "LUK4_%=:\n" \
  "s_waitcnt lgkmcnt(8)\n" \
  "v_mfma_f32_16x16x32_bf16 v[168:171], v[32:35], v[128:131], 0\n" \
  "ds_read_b64_tr_b16 v[148:149], v28 offset:28736\n" \
  "v_mfma_f32_16x16x32_bf16 v[172:175], v[56:59], v[128:131], 0\n" \
  "ds_read_b64_tr_b16 v[150:151], v28 offset:30528\n" \
  "buffer_load_dwordx4 a[160:163], v1, s[36:39], s79 offen sc1\n" \
  "v_readlane_b32 s80, v224, 5\n" \
  "v_mfma_f32_16x16x32_bf16 v[176:179], v[80:83], v[128:131], 0\n" \
  "v_cvt_pk_bf16_f32 v200, v152, v153\n" \
  "v_cvt_pk_bf16_f32 v201, v154, v155\n" \
  "s_waitcnt vmcnt(14)\n" \
  "s_barrier\n" \
  "s_mov_b32 s78, s88\n" \
  "buffer_atomic_add v0, v25, s[36:39], s78 offen sc0 sc1\n" \
  "s_add_u32 s88, s88, 1536\n" \
  "s_and_b32 s88, s88, 0x3fffffff\n" \
  "v_mfma_f32_16x16x32_bf16 v[180:183], v[104:107], v[128:131], 0\n" \
  "s_add_u32 m0, s76, 8192\n" \
  "s_nop 0\n" \
  "buffer_load_dwordx4 v16, s[40:43], s75 offen offset:0 lds\n" \
  "s_waitcnt lgkmcnt(8)\n" \
  "v_mfma_f32_16x16x32_bf16 v[168:171], v[36:39], v[132:135], v[168:171]\n" \
  "ds_read_b64_tr_b16 v[128:129], v7 offset:128\n" \
  "v_mfma_f32_16x16x32_bf16 v[172:175], v[60:63], v[132:135], v[172:175]\n" \
  "ds_read_b64_tr_b16 v[130:131], v7 offset:1920\n" \
  "buffer_load_dwordx4 a[168:171], v2, s[36:39], s79 offen sc1\n" \
  "v_mfma_f32_16x16x32_bf16 v[176:179], v[84:87], v[132:135], v[176:179]\n" \
  "v_cvt_pk_bf16_f32 v202, v156, v157\n" \
  "v_cvt_pk_bf16_f32 v203, v158, v159\n" \
  "v_mfma_f32_16x16x32_bf16 v[180:183], v[108:111], v[132:135], v[180:183]\n" \
  "s_add_u32 s73, s69, 131072\n" \
  "s_waitcnt lgkmcnt(8)\n" \
  "v_mfma_f32_16x16x32_bf16 v[168:171], v[40:43], v[136:139], v[168:171]\n" \
  "ds_read_b64_tr_b16 v[132:133], v7 offset:14464\n" \
  "v_mfma_f32_16x16x32_bf16 v[172:175], v[64:67], v[136:139], v[172:175]\n" \
  "ds_read_b64_tr_b16 v[134:135], v7 offset:16256\n" \
  "buffer_load_dwordx4 a[176:179], v3, s[36:39], s79 offen sc1\n" \
  "v_mfma_f32_16x16x32_bf16 v[176:179], v[88:91], v[136:139], v[176:179]\n" \
  "buffer_load_dwordx4 v17, s[40:43], s75 offen offset:1024 lds\n" \
  "v_mfma_f32_16x16x32_bf16 v[180:183], v[112:115], v[136:139], v[180:183]\n" \
  "buffer_store_dwordx4 v[200:203], v22, s[36:39], s73 offen sc1\n" \
  "s_waitcnt lgkmcnt(8)\n" \
  "v_mfma_f32_16x16x32_bf16 v[168:171], v[44:47], v[140:143], v[168:171]\n" \
  "ds_read_b64_tr_b16 v[136:137], v7 offset:28800\n" \
  "v_mfma_f32_16x16x32_bf16 v[172:175], v[68:71], v[140:143], v[172:175]\n" \
  "ds_read_b64_tr_b16 v[138:139], v7 offset:30592\n" \
  "buffer_load_dwordx4 a[184:187], v4, s[36:39], s79 offen sc1\n" \
  "v_mfma_f32_16x16x32_bf16 v[176:179], v[92:95], v[140:143], v[176:179]\n" \
  "v_cvt_pk_bf16_f32 v204, v160, v161\n" \
  "v_cvt_pk_bf16_f32 v205, v162, v163\n" \
  "v_mfma_f32_16x16x32_bf16 v[180:183], v[116:119], v[140:143], v[180:183]\n" \
  "buffer_load_dwordx4 v18, s[40:43], s75 offen offset:2048 lds\n" \
  "s_waitcnt lgkmcnt(8)\n" \
  "v_mfma_f32_16x16x32_bf16 v[168:171], v[48:51], v[144:147], v[168:171]\n" \
  "ds_read_b64_tr_b16 v[140:141], v27 offset:128\n" \
  "v_mfma_f32_16x16x32_bf16 v[172:175], v[72:75], v[144:147], v[172:175]\n" \
  "ds_read_b64_tr_b16 v[142:143], v27 offset:1920\n" \
  "buffer_load_dwordx4 a[192:195], v5, s[36:39], s79 offen sc1\n" \
  "v_mfma_f32_16x16x32_bf16 v[176:179], v[96:99], v[144:147], v[176:179]\n" \
  "v_cvt_pk_bf16_f32 v206, v164, v165\n" \
  "v_cvt_pk_bf16_f32 v207, v166, v167\n" \
  "v_mfma_f32_16x16x32_bf16 v[180:183], v[120:123], v[144:147], v[180:183]\n" \
  "s_add_u32 s73, s70, 131072\n" \
  "s_waitcnt lgkmcnt(8)\n" \
  "v_mfma_f32_16x16x32_bf16 v[168:171], v[52:55], v[148:151], v[168:171]\n" \
  "ds_read_b64_tr_b16 v[144:145], v27 offset:14464\n" \
  "v_mfma_f32_16x16x32_bf16 v[172:175], v[76:79], v[148:151], v[172:175]\n" \
  "ds_read_b64_tr_b16 v[146:147], v27 offset:16256\n" \
  "buffer_load_dwordx4 a[164:167], v1, s[36:39], s80 offen sc1\n" \
  "v_readlane_b32 s79, v224, 14\n" \
  "v_mfma_f32_16x16x32_bf16 v[176:179], v[100:103], v[148:151], v[176:179]\n" \
  "buffer_load_dwordx4 v19, s[40:43], s75 offen offset:3072 lds\n" \
  "s_add_u32 s75, s75, 786432\n" \
  "v_mfma_f32_16x16x32_bf16 v[180:183], v[124:127], v[148:151], v[180:183]\n" \
  "buffer_store_dwordx4 v[204:207], v22, s[36:39], s73 offen sc1\n" \
  "s_waitcnt lgkmcnt(8)\n" \
  "v_mfma_f32_16x16x32_bf16 v[152:155], v[32:35], v[128:131], 0\n" \
  "ds_read_b64_tr_b16 v[148:149], v27 offset:28800\n" \
  "v_mfma_f32_16x16x32_bf16 v[156:159], v[56:59], v[128:131], 0\n" \
  "ds_read_b64_tr_b16 v[150:151], v27 offset:30592\n" \
  "buffer_load_dwordx4 a[172:175], v2, s[36:39], s80 offen sc1\n" \
  "v_mfma_f32_16x16x32_bf16 v[160:163], v[80:83], v[128:131], 0\n" \
  "v_cvt_pk_bf16_f32 v200, v168, v169\n" \
  "v_cvt_pk_bf16_f32 v201, v170, v171\n" \
  "v_mfma_f32_16x16x32_bf16 v[164:167], v[104:107], v[128:131], 0\n" \
  "v_cvt_pk_bf16_f32 v202, v172, v173\n" \
  "v_cvt_pk_bf16_f32 v203, v174, v175\n" \
  "s_waitcnt lgkmcnt(8)\n" \
  "v_mfma_f32_16x16x32_bf16 v[152:155], v[36:39], v[132:135], v[152:155]\n" \
  "ds_read_b64_tr_b16 v[128:129], v26 offset:128\n" \
  "v_mfma_f32_16x16x32_bf16 v[156:159], v[60:63], v[132:135], v[156:159]\n" \
  "ds_read_b64_tr_b16 v[130:131], v26 offset:1920\n" \
  "buffer_load_dwordx4 a[180:183], v3, s[36:39], s80 offen sc1\n" \
  "v_mfma_f32_16x16x32_bf16 v[160:163], v[84:87], v[132:135], v[160:163]\n" \
  "s_add_u32 s73, s69, 196608\n" \
  "v_mfma_f32_16x16x32_bf16 v[164:167], v[108:111], v[132:135], v[164:167]\n" \
  "buffer_store_dwordx4 v[200:203], v22, s[36:39], s73 offen sc1\n" \
  "s_waitcnt lgkmcnt(8)\n" \
  "v_mfma_f32_16x16x32_bf16 v[152:155], v[40:43], v[136:139], v[152:155]\n" \
  "ds_read_b64_tr_b16 v[132:133], v26 offset:14464\n" \
  "v_mfma_f32_16x16x32_bf16 v[156:159], v[64:67], v[136:139], v[156:159]\n" \
  "ds_read_b64_tr_b16 v[134:135], v26 offset:16256\n" \
  "buffer_load_dwordx4 a[188:191], v4, s[36:39], s80 offen sc1\n" \
  "v_mfma_f32_16x16x32_bf16 v[160:163], v[88:91], v[136:139], v[160:163]\n" \
  "v_cvt_pk_bf16_f32 v204, v176, v177\n" \
  "v_cvt_pk_bf16_f32 v205, v178, v179\n" \
  "v_mfma_f32_16x16x32_bf16 v[164:167], v[112:115], v[136:139], v[164:167]\n" \
  "v_cvt_pk_bf16_f32 v206, v180, v181\n" \
  "v_cvt_pk_bf16_f32 v207, v182, v183\n" \
  "s_waitcnt lgkmcnt(8)\n" \
  "v_mfma_f32_16x16x32_bf16 v[152:155], v[44:47], v[140:143], v[152:155]\n" \
  "ds_read_b64_tr_b16 v[136:137], v26 offset:28800\n" \
  "v_mfma_f32_16x16x32_bf16 v[156:159], v[68:71], v[140:143], v[156:159]\n" \
  "ds_read_b64_tr_b16 v[138:139], v26 offset:30592\n" \
  "buffer_load_dwordx4 a[196:199], v5, s[36:39], s80 offen sc1\n" \
  "v_mfma_f32_16x16x32_bf16 v[160:163], v[92:95], v[140:143], v[160:163]\n" \
  "s_add_u32 s73, s70, 196608\n" \
  "v_mfma_f32_16x16x32_bf16 v[164:167], v[116:119], v[140:143], v[164:167]\n" \
  "buffer_store_dwordx4 v[204:207], v22, s[36:39], s73 offen sc1\n" \
  "s_waitcnt lgkmcnt(8)\n" \
  "v_mfma_f32_16x16x32_bf16 v[152:155], v[48:51], v[144:147], v[152:155]\n" \
  "ds_read_b64_tr_b16 v[140:141], v28 offset:128\n" \
  "v_mfma_f32_16x16x32_bf16 v[156:159], v[72:75], v[144:147], v[156:159]\n" \
  "ds_read_b64_tr_b16 v[142:143], v28 offset:1920\n" \
  "buffer_load_dwordx4 a[200:203], v1, s[36:39], s79 offen sc1\n" \
  "v_readlane_b32 s80, v224, 15\n" \
  "v_mfma_f32_16x16x32_bf16 v[160:163], v[96:99], v[144:147], v[160:163]\n" \
  "s_getpc_b64 s[82:83]\n" \
  "LCF9_%=:\n" \
  "s_add_u32 s82, s82, LUN5_%=-LCF9_%=\n" \
  "s_addc_u32 s83, s83, 0\n" \
  "v_mbcnt_lo_u32_b32 v240, -1, 0\n" \
  "v_mbcnt_hi_u32_b32 v240, -1, v240\n" \
  "v_lshlrev_b32 v240, 6, v240\n" \
  "global_load_dword v241, v240, s[82:83]\n" \
  "v_add_u32 v240, 4096, v240\n" \
  "global_load_dword v241, v240, s[82:83]\n" \
  "v_mfma_f32_16x16x32_bf16 v[164:167], v[120:123], v[144:147], v[164:167]\n" \
  "s_getpc_b64 s[82:83]\n" \
  "LCF10_%=:\n" \
  "s_add_u32 s82, s82, LXIT_%=-4096-LCF10_%=\n" \
  "s_addc_u32 s83, s83, 0\n" \
  "v_mbcnt_lo_u32_b32 v240, -1, 0\n" \
  "v_mbcnt_hi_u32_b32 v240, -1, v240\n" \
  "v_lshlrev_b32 v240, 6, v240\n" \
  "global_load_dword v241, v240, s[82:83]\n" \
  "s_waitcnt lgkmcnt(8)\n" \
  "v_mfma_f32_16x16x32_bf16 v[152:155], v[52:55], v[148:151], v[152:155]\n" \
  "ds_read_b64_tr_b16 v[144:145], v28 offset:14464\n" \
  "v_mfma_f32_16x16x32_bf16 v[156:159], v[76:79], v[148:151], v[156:159]\n" \
  "ds_read_b64_tr_b16 v[146:147], v28 offset:16256\n" \
  "buffer_load_dwordx4 a[208:211], v2, s[36:39], s79 offen sc1\n" \
  "v_mfma_f32_16x16x32_bf16 v[160:163], v[100:103], v[148:151], v[160:163]\n" \
  "v_mfma_f32_16x16x32_bf16 v[164:167], v[124:127], v[148:151], v[164:167]\n" \
  "s_waitcnt lgkmcnt(8)\n" \
  "v_mfma_f32_16x16x32_bf16 v[168:171], v[32:35], v[128:131], 0\n" \
  "ds_read_b64_tr_b16 v[148:149], v28 offset:28800\n" \
  "v_mfma_f32_16x16x32_bf16 v[172:175], v[56:59], v[128:131], 0\n" \
  "ds_read_b64_tr_b16 v[150:151], v28 offset:30592\n" \
  "buffer_load_dwordx4 a[216:219], v3, s[36:39], s79 offen sc1\n" \
  "v_mfma_f32_16x16x32_bf16 v[176:179], v[80:83], v[128:131], 0\n" \
  "v_cvt_pk_bf16_f32 v200, v152, v153\n" \
  "v_cvt_pk_bf16_f32 v201, v154, v155\n" \
  "v_mfma_f32_16x16x32_bf16 v[180:183], v[104:107], v[128:131], 0\n" \
  "v_cvt_pk_bf16_f32 v202, v156, v157\n" \
  "v_cvt_pk_bf16_f32 v203, v158, v159\n" \
  "s_waitcnt lgkmcnt(8)\n" \
  "v_mfma_f32_16x16x32_bf16 v[168:171], v[36:39], v[132:135], v[168:171]\n" \
  "ds_read_b64_tr_b16 v[128:129], v7 offset:192\n" \
  "v_mfma_f32_16x16x32_bf16 v[172:175], v[60:63], v[132:135], v[172:175]\n" \
  "ds_read_b64_tr_b16 v[130:131], v7 offset:1984\n" \
  "buffer_load_dwordx4 a[224:227], v4, s[36:39], s79 offen sc1\n" \
  "v_mfma_f32_16x16x32_bf16 v[176:179], v[84:87], v[132:135], v[176:179]\n" \
  "s_add_u32 s73, s69, 262144\n" \
  "v_mfma_f32_16x16x32_bf16 v[180:183], v[108:111], v[132:135], v[180:183]\n" \
  "buffer_store_dwordx4 v[200:203], v22, s[36:39], s73 offen sc1\n" \
  "s_waitcnt lgkmcnt(8)\n" \
  "v_mfma_f32_16x16x32_bf16 v[168:171], v[40:43], v[136:139], v[168:171]\n" \
  "ds_read_b64_tr_b16 v[132:133], v7 offset:14528\n" \
  "v_mfma_f32_16x16x32_bf16 v[172:175], v[64:67], v[136:139], v[172:175]\n" \
  "ds_read_b64_tr_b16 v[134:135], v7 offset:16320\n" \
  "buffer_load_dwordx4 a[232:235], v5, s[36:39], s79 offen sc1\n" \
  "v_mfma_f32_16x16x32_bf16 v[176:179], v[88:91], v[136:139], v[176:179]\n" \
  "v_cvt_pk_bf16_f32 v204, v160, v161\n" \
  "v_cvt_pk_bf16_f32 v205, v162, v163\n" \
  "v_mfma_f32_16x16x32_bf16 v[180:183], v[112:115], v[136:139], v[180:183]\n" \
  "v_cvt_pk_bf16_f32 v206, v164, v165\n" \
  "v_cvt_pk_bf16_f32 v207, v166, v167\n" \
  "s_waitcnt lgkmcnt(8)\n" \
  "v_mfma_f32_16x16x32_bf16 v[168:171], v[44:47], v[140:143], v[168:171]\n" \
  "ds_read_b64_tr_b16 v[136:137], v7 offset:28864\n" \
  "v_mfma_f32_16x16x32_bf16 v[172:175], v[68:71], v[140:143], v[172:175]\n" \
  "ds_read_b64_tr_b16 v[138:139], v7 offset:30656\n" \
  "buffer_load_dwordx4 a[204:207], v1, s[36:39], s80 offen sc1\n" \
  "v_mfma_f32_16x16x32_bf16 v[176:179], v[92:95], v[140:143], v[176:179]\n" \
  "s_add_u32 s73, s70, 262144\n" \
  "v_mfma_f32_16x16x32_bf16 v[180:183], v[116:119], v[140:143], v[180:183]\n" \
  "buffer_store_dwordx4 v[204:207], v22, s[36:39], s73 offen sc1\n" \
  "s_waitcnt lgkmcnt(8)\n" \
  "v_mfma_f32_16x16x32_bf16 v[168:171], v[48:51], v[144:147], v[168:171]\n" \
  "ds_read_b64_tr_b16 v[140:141], v27 offset:192\n" \
  "v_mfma_f32_16x16x32_bf16 v[172:175], v[72:75], v[144:147], v[172:175]\n" \
  "ds_read_b64_tr_b16 v[142:143], v27 offset:1984\n" \
  "buffer_load_dwordx4 a[212:215], v2, s[36:39], s80 offen sc1\n" \
  "v_mfma_f32_16x16x32_bf16 v[176:179], v[96:99], v[144:147], v[176:179]\n" \
  "v_mfma_f32_16x16x32_bf16 v[180:183], v[120:123], v[144:147], v[180:183]\n" \
  "s_waitcnt lgkmcnt(8)\n" \
  "v_mfma_f32_16x16x32_bf16 v[168:171], v[52:55], v[148:151], v[168:171]\n" \
  "ds_read_b64_tr_b16 v[144:145], v27 offset:14528\n" \
  "v_mfma_f32_16x16x32_bf16 v[172:175], v[76:79], v[148:151], v[172:175]\n" \
  "ds_read_b64_tr_b16 v[146:147], v27 offset:16320\n" \
  "buffer_load_dwordx4 a[220:223], v3, s[36:39], s80 offen sc1\n" \
  "v_mfma_f32_16x16x32_bf16 v[176:179], v[100:103], v[148:151], v[176:179]\n" \
  "v_mfma_f32_16x16x32_bf16 v[180:183], v[124:127], v[148:151], v[180:183]\n" \
  "s_waitcnt vmcnt(20)\n" \
  "s_barrier\n" \
  "v_readfirstlane_b32 s74, v0\n" \
  "v_mov_b32 v0, 1\n" \
  "s_cmp_eq_u32 s74, 11\n" \
  "s_cselect_b32 s73, s78, 0x40000000\n" \
  "buffer_store_dword v0, v31, s[36:39], s73 offen sc1\n" \
  "s_waitcnt lgkmcnt(8)\n" \
  "v_mfma_f32_16x16x32_bf16 v[184:187], v[32:35], v[128:131], 0\n" \
  "ds_read_b128 v[32:35], v20 offset:0\n" \
  "ds_read_b64_tr_b16 v[148:149], v27 offset:28864\n" \
  "v_mfma_f32_16x16x32_bf16 v[188:191], v[56:59], v[128:131], 0\n" \
  "ds_read_b128 v[56:59], v20 offset:768\n" \
  "ds_read_b64_tr_b16 v[150:151], v27 offset:30656\n" \
  "buffer_load_dwordx4 a[228:231], v4, s[36:39], s80 offen sc1\n" \
  "v_mfma_f32_16x16x32_bf16 v[192:195], v[80:83], v[128:131], 0\n" \
  "ds_read_b128 v[80:83], v20 offset:12288\n" \
  "v_cvt_pk_bf16_f32 v200, v168, v169\n" \
  "v_cvt_pk_bf16_f32 v201, v170, v171\n" \
  "v_mfma_f32_16x16x32_bf16 v[196:199], v[104:107], v[128:131], 0\n" \
  "ds_read_b128 v[104:107], v20 offset:13056\n" \
  "v_cvt_pk_bf16_f32 v202, v172, v173\n" \
  "v_cvt_pk_bf16_f32 v203, v174, v175\n" \
  "s_waitcnt lgkmcnt(12)\n" \
  "v_mfma_f32_16x16x32_bf16 v[184:187], v[36:39], v[132:135], v[184:187]\n" \
  "ds_read_b128 v[36:39], v21 offset:0\n" \
  "ds_read_b64_tr_b16 v[128:129], v7 offset:0\n" \
  "v_mfma_f32_16x16x32_bf16 v[188:191], v[60:63], v[132:135], v[188:191]\n" \
  "ds_read_b128 v[60:63], v21 offset:768\n" \
  "s_waitcnt lgkmcnt(8)\n" \
  "ds_read_b64_tr_b16 v[130:131], v7 offset:1792\n" \
  "buffer_load_dwordx4 a[236:239], v5, s[36:39], s80 offen sc1\n" \
  "v_mfma_f32_16x16x32_bf16 v[192:195], v[84:87], v[132:135], v[192:195]\n" \
  "ds_read_b128 v[84:87], v21 offset:12288\n" \
  "s_add_u32 s73, s69, 327680\n" \
  "v_mfma_f32_16x16x32_bf16 v[196:199], v[108:111], v[132:135], v[196:199]\n" \
  "ds_read_b128 v[108:111], v21 offset:13056\n" \
  "buffer_store_dwordx4 v[200:203], v22, s[36:39], s73 offen sc1\n" \
  "v_mfma_f32_16x16x32_bf16 v[184:187], v[40:43], v[136:139], v[184:187]\n" \
  "ds_read_b128 v[40:43], v20 offset:128\n" \
  "ds_read_b64_tr_b16 v[132:133], v7 offset:14336\n" \
  "v_mfma_f32_16x16x32_bf16 v[188:191], v[64:67], v[136:139], v[188:191]\n" \
  "ds_read_b128 v[64:67], v20 offset:896\n" \
  "ds_read_b64_tr_b16 v[134:135], v7 offset:16128\n" \
  "v_mfma_f32_16x16x32_bf16 v[192:195], v[88:91], v[136:139], v[192:195]\n" \
  "s_waitcnt lgkmcnt(8)\n" \
  "ds_read_b128 v[88:91], v20 offset:12416\n" \
  "v_cvt_pk_bf16_f32 v204, v176, v177\n" \
  "v_cvt_pk_bf16_f32 v205, v178, v179\n" \
  "v_mfma_f32_16x16x32_bf16 v[196:199], v[112:115], v[136:139], v[196:199]\n" \
  "ds_read_b128 v[112:115], v20 offset:13184\n" \
  "v_cvt_pk_bf16_f32 v206, v180, v181\n" \
  "v_cvt_pk_bf16_f32 v207, v182, v183\n" \
  "v_mfma_f32_16x16x32_bf16 v[184:187], v[44:47], v[140:143], v[184:187]\n" \
  "ds_read_b128 v[44:47], v21 offset:128\n" \
  "ds_read_b64_tr_b16 v[136:137], v7 offset:28672\n" \
  "v_mfma_f32_16x16x32_bf16 v[188:191], v[68:71], v[140:143], v[188:191]\n" \
  "ds_read_b128 v[68:71], v21 offset:896\n" \
  "ds_read_b64_tr_b16 v[138:139], v7 offset:30464\n" \
  "v_mfma_f32_16x16x32_bf16 v[192:195], v[92:95], v[140:143], v[192:195]\n" \
  "ds_read_b128 v[92:95], v21 offset:12416\n" \
  "s_add_u32 s73, s70, 327680\n" \
  "v_mfma_f32_16x16x32_bf16 v[196:199], v[116:119], v[140:143], v[196:199]\n" \
  "s_waitcnt lgkmcnt(8)\n" \
  "ds_read_b128 v[116:119], v21 offset:13184\n" \
  "buffer_store_dwordx4 v[204:207], v22, s[36:39], s73 offen sc1\n" \
  "v_mfma_f32_16x16x32_bf16 v[184:187], v[48:51], v[144:147], v[184:187]\n" \
  "ds_read_b128 v[48:51], v20 offset:256\n" \
  "ds_read_b64_tr_b16 v[140:141], v27 offset:0\n" \
  "v_mfma_f32_16x16x32_bf16 v[188:191], v[72:75], v[144:147], v[188:191]\n" \
  "ds_read_b128 v[72:75], v20 offset:1024\n" \
  "ds_read_b64_tr_b16 v[142:143], v27 offset:1792\n" \
  "v_mfma_f32_16x16x32_bf16 v[192:195], v[96:99], v[144:147], v[192:195]\n" \
  "ds_read_b128 v[96:99], v20 offset:12544\n" \
  "s_cmp_lg_u32 s78, 0\n" \
  "v_mfma_f32_16x16x32_bf16 v[196:199], v[120:123], v[144:147], v[196:199]\n" \
  "ds_read_b128 v[120:123], v20 offset:13312\n" \
  "v_mfma_f32_16x16x32_bf16 v[184:187], v[52:55], v[148:151], v[184:187]\n" \
  "s_waitcnt lgkmcnt(8)\n" \
  "ds_read_b128 v[52:55], v21 offset:256\n" \
  "ds_read_b64_tr_b16 v[144:145], v27 offset:14336\n" \
  "v_mfma_f32_16x16x32_bf16 v[188:191], v[76:79], v[148:151], v[188:191]\n" \
  "ds_read_b128 v[76:79], v21 offset:1024\n" \
  "ds_read_b64_tr_b16 v[146:147], v27 offset:16128\n" \
  "v_mfma_f32_16x16x32_bf16 v[192:195], v[100:103], v[148:151], v[192:195]\n" \
  "ds_read_b128 v[100:103], v21 offset:12544\n" \
  "v_mfma_f32_16x16x32_bf16 v[196:199], v[124:127], v[148:151], v[196:199]\n" \
  "ds_read_b128 v[124:127], v21 offset:13312\n" \
  "LUN5_%=:\n" \
  "s_nop 7\n" \
  "v_cvt_pk_bf16_f32 v200, v184, v185\n" \
  "v_cvt_pk_bf16_f32 v201, v186, v187\n" \
  "v_cvt_pk_bf16_f32 v202, v188, v189\n" \
  "v_cvt_pk_bf16_f32 v203, v190, v191\n" \
  "s_add_u32 s73, s71, 393216\n" \
  "buffer_store_dwordx4 v[200:203], v22, s[36:39], s73 offen sc1\n" \
  "v_cvt_pk_bf16_f32 v204, v192, v193\n" \
  "v_cvt_pk_bf16_f32 v205, v194, v195\n" \
  "v_cvt_pk_bf16_f32 v206, v196, v197\n" \
  "v_cvt_pk_bf16_f32 v207, v198, v199\n" \
  "s_add_u32 s73, s72, 393216\n" \
  "buffer_store_dwordx4 v[204:207], v22, s[36:39], s73 offen sc1\n" \
  "s_waitcnt vmcnt(0) lgkmcnt(0)\n" \
  "s_barrier\n" \
  "s_add_u32 s78, s78, 1536\n" \
  "buffer_atomic_add v0, v25, s[36:39], s78 offen sc0 sc1\n" \
  "s_waitcnt vmcnt(0)\n" \
  "v_readfirstlane_b32 s74, v0\n" \
  "v_mov_b32 v0, 1\n" \
  "s_cmp_eq_u32 s74, 11\n" \
  "s_cselect_b32 s73, s78, 0x40000000\n" \
  "buffer_store_dword v0, v31, s[36:39], s73 offen sc1\n" \
  "s_branch LUE_%=\n" \
  "LUP2_%=:\n" \
  "buffer_load_dword v242, v30, s[36:39], s78 offen sc1\n" \
  "s_waitcnt vmcnt(0)\n" \
  "v_cmp_ne_u32 vcc, 1, v242\n" \
  "s_nop 3\n" \
  "s_cbranch_vccz LUK2_%=\n" \
  "s_add_u32 s81, s81, 1\n" \
  "s_cmp_gt_u32 s81, 200000\n" \
  "s_cbranch_scc1 LUK2_%=\n" \
  "s_sleep 2\n" \
  "s_branch LUP2_%=\n" \
  "LUP3_%=:\n" \
  "buffer_load_dword v242, v30, s[36:39], s78 offen sc1\n" \
  "s_waitcnt vmcnt(0)\n" \
  "v_cmp_ne_u32 vcc, 1, v242\n" \
  "s_nop 3\n" \
  "s_cbranch_vccz LUK3_%=\n" \
  "s_add_u32 s81, s81, 1\n" \
  "s_cmp_gt_u32 s81, 200000\n" \
  "s_cbranch_scc1 LUK3_%=\n" \
  "s_sleep 2\n" \
  "s_branch LUP3_%=\n" \
  "LUP4_%=:\n" \
  "buffer_load_dword v242, v30, s[36:39], s78 offen sc1\n" \
  "s_waitcnt vmcnt(0)\n" \
  "v_cmp_ne_u32 vcc, 1, v242\n" \
  "s_nop 3\n" \
  "s_cbranch_vccz LUK4_%=\n" \
  "s_add_u32 s81, s81, 1\n" \
  "s_cmp_gt_u32 s81, 200000\n" \
  "s_cbranch_scc1 LUK4_%=\n" \
  "s_sleep 2\n" \
  "s_branch LUP4_%=\n" \
  "LUE_%=:\n"
#define TAKE0_CLOBBERS \
  "v2", "v3"
#define TAKE0_ASM \
  "v_add_u32 v2, 61440, v0\n" \
  "v_add_u32 v3, 61440, v1\n" \
  "s_cmp_eq_u32 s52, 3\n" \
  "s_cbranch_scc1 LTV_%=\n" \
  "ds_read_b64_tr_b16 a[0:1], v0 offset:0\n" \
  "ds_read_b64_tr_b16 a[2:3], v0 offset:1536\n" \
  "ds_read_b64_tr_b16 a[80:81], v0 offset:8\n" \
  "ds_read_b64_tr_b16 a[82:83], v0 offset:1544\n" \
  "ds_read_b64_tr_b16 a[160:161], v1 offset:0\n" \
  "ds_read_b64_tr_b16 a[162:163], v1 offset:1536\n" \
  "ds_read_b64_tr_b16 a[4:5], v0 offset:12288\n" \
  "ds_read_b64_tr_b16 a[6:7], v0 offset:13824\n" \
  "ds_read_b64_tr_b16 a[84:85], v0 offset:12296\n" \
  "ds_read_b64_tr_b16 a[86:87], v0 offset:13832\n" \
  "ds_read_b64_tr_b16 a[164:165], v1 offset:12288\n" \
  "ds_read_b64_tr_b16 a[166:167], v1 offset:13824\n" \
  "ds_read_b64_tr_b16 a[8:9], v0 offset:24576\n" \
  "ds_read_b64_tr_b16 a[10:11], v0 offset:26112\n" \
  "ds_read_b64_tr_b16 a[88:89], v0 offset:24584\n" \
  "s_waitcnt lgkmcnt(8)\n" \
  "ds_read_b64_tr_b16 a[90:91], v0 offset:26120\n" \
  "ds_read_b64_tr_b16 a[168:169], v1 offset:24576\n" \
  "ds_read_b64_tr_b16 a[170:171], v1 offset:26112\n" \
  "ds_read_b64_tr_b16 a[12:13], v0 offset:36864\n" \
  "ds_read_b64_tr_b16 a[14:15], v0 offset:38400\n" \
  "ds_read_b64_tr_b16 a[92:93], v0 offset:36872\n" \
  "ds_read_b64_tr_b16 a[94:95], v0 offset:38408\n" \
  "s_waitcnt lgkmcnt(8)\n" \
  "ds_read_b64_tr_b16 a[172:173], v1 offset:36864\n" \
  "ds_read_b64_tr_b16 a[174:175], v1 offset:38400\n" \
  "ds_read_b64_tr_b16 a[16:17], v0 offset:49152\n" \
  "ds_read_b64_tr_b16 a[18:19], v0 offset:50688\n" \
  "ds_read_b64_tr_b16 a[96:97], v0 offset:49160\n" \
  "ds_read_b64_tr_b16 a[98:99], v0 offset:50696\n" \
  "ds_read_b64_tr_b16 a[176:177], v1 offset:49152\n" \
  "s_waitcnt lgkmcnt(8)\n" \
  "ds_read_b64_tr_b16 a[178:179], v1 offset:50688\n" \
  "ds_read_b64_tr_b16 a[20:21], v2 offset:0\n" \
  "ds_read_b64_tr_b16 a[22:23], v2 offset:1536\n" \
  "ds_read_b64_tr_b16 a[100:101], v2 offset:8\n" \
  "ds_read_b64_tr_b16 a[102:103], v2 offset:1544\n" \
  "ds_read_b64_tr_b16 a[180:181], v3 offset:0\n" \
  "ds_read_b64_tr_b16 a[182:183], v3 offset:1536\n" \
  "s_waitcnt lgkmcnt(8)\n" \
  "ds_read_b64_tr_b16 a[24:25], v2 offset:12288\n" \
  "ds_read_b64_tr_b16 a[26:27], v2 offset:13824\n" \
  "ds_read_b64_tr_b16 a[104:105], v2 offset:12296\n" \
  "ds_read_b64_tr_b16 a[106:107], v2 offset:13832\n" \
  "ds_read_b64_tr_b16 a[184:185], v3 offset:12288\n" \
  "ds_read_b64_tr_b16 a[186:187], v3 offset:13824\n" \
  "ds_read_b64_tr_b16 a[28:29], v2 offset:24576\n" \
  "s_waitcnt lgkmcnt(8)\n" \
  "ds_read_b64_tr_b16 a[30:31], v2 offset:26112\n" \
  "ds_read_b64_tr_b16 a[108:109], v2 offset:24584\n" \
  "ds_read_b64_tr_b16 a[110:111], v2 offset:26120\n" \
  "ds_read_b64_tr_b16 a[188:189], v3 offset:24576\n" \
  "ds_read_b64_tr_b16 a[190:191], v3 offset:26112\n" \
  "ds_read_b64_tr_b16 a[32:33], v2 offset:36864\n" \
  "ds_read_b64_tr_b16 a[34:35], v2 offset:38400\n" \
  "s_waitcnt lgkmcnt(8)\n" \
  "ds_read_b64_tr_b16 a[112:113], v2 offset:36872\n" \
  "ds_read_b64_tr_b16 a[114:115], v2 offset:38408\n" \
  "ds_read_b64_tr_b16 a[192:193], v3 offset:36864\n" \
  "ds_read_b64_tr_b16 a[194:195], v3 offset:38400\n" \
  "ds_read_b64_tr_b16 a[36:37], v2 offset:49152\n" \
  "ds_read_b64_tr_b16 a[38:39], v2 offset:50688\n" \
  "ds_read_b64_tr_b16 a[116:117], v2 offset:49160\n" \
  "s_waitcnt lgkmcnt(8)\n" \
  "ds_read_b64_tr_b16 a[118:119], v2 offset:50696\n" \
  "ds_read_b64_tr_b16 a[196:197], v3 offset:49152\n" \
  "ds_read_b64_tr_b16 a[198:199], v3 offset:50688\n" \
  "s_branch LTE_%=\n" \
  "LTV_%=:\n" \
  "ds_read_b64_tr_b16 a[0:1], v0 offset:0\n" \
  "ds_read_b64_tr_b16 a[2:3], v0 offset:5120\n" \
  "ds_read_b64_tr_b16 a[16:17], v1 offset:0\n" \
  "ds_read_b64_tr_b16 a[24:25], v0 offset:32\n" \
  "ds_read_b64_tr_b16 a[26:27], v0 offset:5152\n" \
  "ds_read_b64_tr_b16 a[40:41], v1 offset:32\n" \
  "ds_read_b64_tr_b16 a[48:49], v0 offset:64\n" \
  "ds_read_b64_tr_b16 a[50:51], v0 offset:5184\n" \
  "ds_read_b64_tr_b16 a[64:65], v1 offset:64\n" \
  "ds_read_b64_tr_b16 a[72:73], v0 offset:96\n" \
  "ds_read_b64_tr_b16 a[74:75], v0 offset:5216\n" \
  "ds_read_b64_tr_b16 a[88:89], v1 offset:96\n" \
  "ds_read_b64_tr_b16 a[96:97], v0 offset:128\n" \
  "ds_read_b64_tr_b16 a[98:99], v0 offset:5248\n" \
  "ds_read_b64_tr_b16 a[112:113], v1 offset:128\n" \
  "s_waitcnt lgkmcnt(8)\n" \
  "ds_read_b64_tr_b16 a[120:121], v0 offset:160\n" \
  "ds_read_b64_tr_b16 a[122:123], v0 offset:5280\n" \
  "ds_read_b64_tr_b16 a[136:137], v1 offset:160\n" \
  "ds_read_b64_tr_b16 a[144:145], v0 offset:192\n" \
  "ds_read_b64_tr_b16 a[146:147], v0 offset:5312\n" \
  "ds_read_b64_tr_b16 a[160:161], v1 offset:192\n" \
  "ds_read_b64_tr_b16 a[168:169], v0 offset:224\n" \
  "s_waitcnt lgkmcnt(8)\n" \
  "ds_read_b64_tr_b16 a[170:171], v0 offset:5344\n" \
  "ds_read_b64_tr_b16 a[184:185], v1 offset:224\n" \
  "ds_read_b64_tr_b16 a[192:193], v0 offset:256\n" \
  "ds_read_b64_tr_b16 a[194:195], v0 offset:5376\n" \
  "ds_read_b64_tr_b16 a[208:209], v1 offset:256\n" \
  "ds_read_b64_tr_b16 a[216:217], v0 offset:288\n" \
  "ds_read_b64_tr_b16 a[218:219], v0 offset:5408\n" \
  "s_waitcnt lgkmcnt(8)\n" \
  "ds_read_b64_tr_b16 a[232:233], v1 offset:288\n" \
  "ds_read_b64_tr_b16 a[4:5], v2 offset:0\n" \
  "ds_read_b64_tr_b16 a[6:7], v2 offset:5120\n" \
  "ds_read_b64_tr_b16 a[18:19], v3 offset:0\n" \
  "ds_read_b64_tr_b16 a[28:29], v2 offset:32\n" \
  "ds_read_b64_tr_b16 a[30:31], v2 offset:5152\n" \
  "ds_read_b64_tr_b16 a[42:43], v3 offset:32\n" \
  "s_waitcnt lgkmcnt(8)\n" \
  "ds_read_b64_tr_b16 a[52:53], v2 offset:64\n" \
  "ds_read_b64_tr_b16 a[54:55], v2 offset:5184\n" \
  "ds_read_b64_tr_b16 a[66:67], v3 offset:64\n" \
  "ds_read_b64_tr_b16 a[76:77], v2 offset:96\n" \
  "ds_read_b64_tr_b16 a[78:79], v2 offset:5216\n" \
  "ds_read_b64_tr_b16 a[90:91], v3 offset:96\n" \
  "ds_read_b64_tr_b16 a[100:101], v2 offset:128\n" \
  "s_waitcnt lgkmcnt(8)\n" \
  "ds_read_b64_tr_b16 a[102:103], v2 offset:5248\n" \
  "ds_read_b64_tr_b16 a[114:115], v3 offset:128\n" \
  "ds_read_b64_tr_b16 a[124:125], v2 offset:160\n" \
  "ds_read_b64_tr_b16 a[126:127], v2 offset:5280\n" \
  "ds_read_b64_tr_b16 a[138:139], v3 offset:160\n" \
  "ds_read_b64_tr_b16 a[148:149], v2 offset:192\n" \
  "ds_read_b64_tr_b16 a[150:151], v2 offset:5312\n" \
  "s_waitcnt lgkmcnt(8)\n" \
  "ds_read_b64_tr_b16 a[162:163], v3 offset:192\n" \
  "ds_read_b64_tr_b16 a[172:173], v2 offset:224\n" \
  "ds_read_b64_tr_b16 a[174:175], v2 offset:5344\n" \
  "ds_read_b64_tr_b16 a[186:187], v3 offset:224\n" \
  "ds_read_b64_tr_b16 a[196:197], v2 offset:256\n" \
  "ds_read_b64_tr_b16 a[198:199], v2 offset:5376\n" \
  "ds_read_b64_tr_b16 a[210:211], v3 offset:256\n" \
  "s_waitcnt lgkmcnt(8)\n" \
  "ds_read_b64_tr_b16 a[220:221], v2 offset:288\n" \
  "ds_read_b64_tr_b16 a[222:223], v2 offset:5408\n" \
  "ds_read_b64_tr_b16 a[234:235], v3 offset:288\n" \
  "LTE_%=:\n" \
  "s_waitcnt lgkmcnt(0)\n"
#define TAKE1_CLOBBERS \
  "v2", "v3"
#define TAKE1_ASM \
  "v_add_u32 v2, 61440, v0\n" \
  "v_add_u32 v3, 61440, v1\n" \
  "s_cmp_eq_u32 s52, 3\n" \
  "s_cbranch_scc1 LTV_%=\n" \
  "ds_read_b64_tr_b16 a[40:41], v0 offset:0\n" \
  "ds_read_b64_tr_b16 a[42:43], v0 offset:1536\n" \
  "ds_read_b64_tr_b16 a[120:121], v0 offset:8\n" \
  "ds_read_b64_tr_b16 a[122:123], v0 offset:1544\n" \
  "ds_read_b64_tr_b16 a[200:201], v1 offset:0\n" \
  "ds_read_b64_tr_b16 a[202:203], v1 offset:1536\n" \
  "ds_read_b64_tr_b16 a[44:45], v0 offset:12288\n" \
  "ds_read_b64_tr_b16 a[46:47], v0 offset:13824\n" \
  "ds_read_b64_tr_b16 a[124:125], v0 offset:12296\n" \
  "ds_read_b64_tr_b16 a[126:127], v0 offset:13832\n" \
  "ds_read_b64_tr_b16 a[204:205], v1 offset:12288\n" \
  "ds_read_b64_tr_b16 a[206:207], v1 offset:13824\n" \
  "ds_read_b64_tr_b16 a[48:49], v0 offset:24576\n" \
  "ds_read_b64_tr_b16 a[50:51], v0 offset:26112\n" \
  "ds_read_b64_tr_b16 a[128:129], v0 offset:24584\n" \
  "s_waitcnt lgkmcnt(8)\n" \
  "ds_read_b64_tr_b16 a[130:131], v0 offset:26120\n" \
  "ds_read_b64_tr_b16 a[208:209], v1 offset:24576\n" \
  "ds_read_b64_tr_b16 a[210:211], v1 offset:26112\n" \
  "ds_read_b64_tr_b16 a[52:53], v0 offset:36864\n" \
  "ds_read_b64_tr_b16 a[54:55], v0 offset:38400\n" \
  "ds_read_b64_tr_b16 a[132:133], v0 offset:36872\n" \
  "ds_read_b64_tr_b16 a[134:135], v0 offset:38408\n" \
  "s_waitcnt lgkmcnt(8)\n" \
  "ds_read_b64_tr_b16 a[212:213], v1 offset:36864\n" \
  "ds_read_b64_tr_b16 a[214:215], v1 offset:38400\n" \
  "ds_read_b64_tr_b16 a[56:57], v0 offset:49152\n" \
  "ds_read_b64_tr_b16 a[58:59], v0 offset:50688\n" \
  "ds_read_b64_tr_b16 a[136:137], v0 offset:49160\n" \
  "ds_read_b64_tr_b16 a[138:139], v0 offset:50696\n" \
  "ds_read_b64_tr_b16 a[216:217], v1 offset:49152\n" \
  "s_waitcnt lgkmcnt(8)\n" \
  "ds_read_b64_tr_b16 a[218:219], v1 offset:50688\n" \
  "ds_read_b64_tr_b16 a[60:61], v2 offset:0\n" \
  "ds_read_b64_tr_b16 a[62:63], v2 offset:1536\n" \
  "ds_read_b64_tr_b16 a[140:141], v2 offset:8\n" \
  "ds_read_b64_tr_b16 a[142:143], v2 offset:1544\n" \
  "ds_read_b64_tr_b16 a[220:221], v3 offset:0\n" \
  "ds_read_b64_tr_b16 a[222:223], v3 offset:1536\n" \
  "s_waitcnt lgkmcnt(8)\n" \
  "ds_read_b64_tr_b16 a[64:65], v2 offset:12288\n" \
  "ds_read_b64_tr_b16 a[66:67], v2 offset:13824\n" \
  "ds_read_b64_tr_b16 a[144:145], v2 offset:12296\n" \
  "ds_read_b64_tr_b16 a[146:147], v2 offset:13832\n" \
  "ds_read_b64_tr_b16 a[224:225], v3 offset:12288\n" \
  "ds_read_b64_tr_b16 a[226:227], v3 offset:13824\n" \
  "ds_read_b64_tr_b16 a[68:69], v2 offset:24576\n" \
  "s_waitcnt lgkmcnt(8)\n" \
  "ds_read_b64_tr_b16 a[70:71], v2 offset:26112\n" \
  "ds_read_b64_tr_b16 a[148:149], v2 offset:24584\n" \
  "ds_read_b64_tr_b16 a[150:151], v2 offset:26120\n" \
  "ds_read_b64_tr_b16 a[228:229], v3 offset:24576\n" \
  "ds_read_b64_tr_b16 a[230:231], v3 offset:26112\n" \
  "ds_read_b64_tr_b16 a[72:73], v2 offset:36864\n" \
  "ds_read_b64_tr_b16 a[74:75], v2 offset:38400\n" \
  "s_waitcnt lgkmcnt(8)\n" \
  "ds_read_b64_tr_b16 a[152:153], v2 offset:36872\n" \
  "ds_read_b64_tr_b16 a[154:155], v2 offset:38408\n" \
  "ds_read_b64_tr_b16 a[232:233], v3 offset:36864\n" \
  "ds_read_b64_tr_b16 a[234:235], v3 offset:38400\n" \
  "ds_read_b64_tr_b16 a[76:77], v2 offset:49152\n" \
  "ds_read_b64_tr_b16 a[78:79], v2 offset:50688\n" \
  "ds_read_b64_tr_b16 a[156:157], v2 offset:49160\n" \
  "s_waitcnt lgkmcnt(8)\n" \
  "ds_read_b64_tr_b16 a[158:159], v2 offset:50696\n" \
  "ds_read_b64_tr_b16 a[236:237], v3 offset:49152\n" \
  "ds_read_b64_tr_b16 a[238:239], v3 offset:50688\n" \
  "s_branch LTE_%=\n" \
  "LTV_%=:\n" \
  "ds_read_b64_tr_b16 a[8:9], v0 offset:0\n" \
  "ds_read_b64_tr_b16 a[10:11], v0 offset:5120\n" \
  "ds_read_b64_tr_b16 a[20:21], v1 offset:0\n" \
  "ds_read_b64_tr_b16 a[32:33], v0 offset:32\n" \
  "ds_read_b64_tr_b16 a[34:35], v0 offset:5152\n" \
  "ds_read_b64_tr_b16 a[44:45], v1 offset:32\n" \
  "ds_read_b64_tr_b16 a[56:57], v0 offset:64\n" \
  "ds_read_b64_tr_b16 a[58:59], v0 offset:5184\n" \
  "ds_read_b64_tr_b16 a[68:69], v1 offset:64\n" \
  "ds_read_b64_tr_b16 a[80:81], v0 offset:96\n" \
  "ds_read_b64_tr_b16 a[82:83], v0 offset:5216\n" \
  "ds_read_b64_tr_b16 a[92:93], v1 offset:96\n" \
  "ds_read_b64_tr_b16 a[104:105], v0 offset:128\n" \
  "ds_read_b64_tr_b16 a[106:107], v0 offset:5248\n" \
  "ds_read_b64_tr_b16 a[116:117], v1 offset:128\n" \
  "s_waitcnt lgkmcnt(8)\n" \
  "ds_read_b64_tr_b16 a[128:129], v0 offset:160\n" \
  "ds_read_b64_tr_b16 a[130:131], v0 offset:5280\n" \
  "ds_read_b64_tr_b16 a[140:141], v1 offset:160\n" \
  "ds_read_b64_tr_b16 a[152:153], v0 offset:192\n" \
  "ds_read_b64_tr_b16 a[154:155], v0 offset:5312\n" \
  "ds_read_b64_tr_b16 a[164:165], v1 offset:192\n" \
  "ds_read_b64_tr_b16 a[176:177], v0 offset:224\n" \
  "s_waitcnt lgkmcnt(8)\n" \
  "ds_read_b64_tr_b16 a[178:179], v0 offset:5344\n" \
  "ds_read_b64_tr_b16 a[188:189], v1 offset:224\n" \
  "ds_read_b64_tr_b16 a[200:201], v0 offset:256\n" \
  "ds_read_b64_tr_b16 a[202:203], v0 offset:5376\n" \
  "ds_read_b64_tr_b16 a[212:213], v1 offset:256\n" \
  "ds_read_b64_tr_b16 a[224:225], v0 offset:288\n" \
  "ds_read_b64_tr_b16 a[226:227], v0 offset:5408\n" \
  "s_waitcnt lgkmcnt(8)\n" \
  "ds_read_b64_tr_b16 a[236:237], v1 offset:288\n" \
  "ds_read_b64_tr_b16 a[12:13], v2 offset:0\n" \
  "ds_read_b64_tr_b16 a[14:15], v2 offset:5120\n" \
  "ds_read_b64_tr_b16 a[22:23], v3 offset:0\n" \
  "ds_read_b64_tr_b16 a[36:37], v2 offset:32\n" \
  "ds_read_b64_tr_b16 a[38:39], v2 offset:5152\n" \
  "ds_read_b64_tr_b16 a[46:47], v3 offset:32\n" \
  "s_waitcnt lgkmcnt(8)\n" \
  "ds_read_b64_tr_b16 a[60:61], v2 offset:64\n" \
  "ds_read_b64_tr_b16 a[62:63], v2 offset:5184\n" \
  "ds_read_b64_tr_b16 a[70:71], v3 offset:64\n" \
  "ds_read_b64_tr_b16 a[84:85], v2 offset:96\n" \
  "ds_read_b64_tr_b16 a[86:87], v2 offset:5216\n" \
  "ds_read_b64_tr_b16 a[94:95], v3 offset:96\n" \
  "ds_read_b64_tr_b16 a[108:109], v2 offset:128\n" \
  "s_waitcnt lgkmcnt(8)\n" \
  "ds_read_b64_tr_b16 a[110:111], v2 offset:5248\n" \
  "ds_read_b64_tr_b16 a[118:119], v3 offset:128\n" \
  "ds_read_b64_tr_b16 a[132:133], v2 offset:160\n" \
  "ds_read_b64_tr_b16 a[134:135], v2 offset:5280\n" \
  "ds_read_b64_tr_b16 a[142:143], v3 offset:160\n" \
  "ds_read_b64_tr_b16 a[156:157], v2 offset:192\n" \
  "ds_read_b64_tr_b16 a[158:159], v2 offset:5312\n" \
  "s_waitcnt lgkmcnt(8)\n" \
  "ds_read_b64_tr_b16 a[166:167], v3 offset:192\n" \
  "ds_read_b64_tr_b16 a[180:181], v2 offset:224\n" \
  "ds_read_b64_tr_b16 a[182:183], v2 offset:5344\n" \
  "ds_read_b64_tr_b16 a[190:191], v3 offset:224\n" \
  "ds_read_b64_tr_b16 a[204:205], v2 offset:256\n" \
  "ds_read_b64_tr_b16 a[206:207], v2 offset:5376\n" \
  "ds_read_b64_tr_b16 a[214:215], v3 offset:256\n" \
  "s_waitcnt lgkmcnt(8)\n" \
  "ds_read_b64_tr_b16 a[228:229], v2 offset:288\n" \
  "ds_read_b64_tr_b16 a[230:231], v2 offset:5408\n" \
  "ds_read_b64_tr_b16 a[238:239], v3 offset:288\n" \
  "LTE_%=:\n" \
  "s_waitcnt lgkmcnt(0)\n"

__device__ __forceinline__ int fresh_lane() {
  int ln;
  asm volatile("v_mbcnt_lo_u32_b32 %0, -1, 0\n\tv_mbcnt_hi_u32_b32 %0, -1, %0" : "=v"(ln));
  __builtin_assume(ln >= 0 && ln < 64);
  return ln;
}
__device__ __forceinline__ unsigned now_ticks() { return (unsigned)__builtin_amdgcn_s_memrealtime(); }
template <int SLEEP>
__device__ __forceinline__ void wait_ge(gu32* ctrl, long word, int need) {
#pragma unroll 1
  for (int spin = 0;; ++spin) {
    const int x = (int)__hip_atomic_load(ctrl + word, RLX_AGENT);
    if (__builtin_amdgcn_ballot_w64(x < need) == 0) return;
    if (spin > SPIN_CAP || ((spin & 1023) == 1023 && rfl(__hip_atomic_load(ctrl + C_ERR, RLX_AGENT)))) { __hip_atomic_store(ctrl + C_ERR, 1u, RLX_AGENT); return; }
    __builtin_amdgcn_s_sleep(SLEEP);
  }
}
template <typename VT>
__device__ __forceinline__ void set_rsrc(VT& sr, int i, const void* ptr, unsigned bytes) {
  const unsigned long long a = uni64((unsigned long long)ptr);
  sr[4 * i] = (unsigned)a; sr[4 * i + 1] = (unsigned)(a >> 32) & 0xffffu; sr[4 * i + 2] = bytes; sr[4 * i + 3] = 0x00020000u;
}
__device__ __forceinline__ int side_a(const Params& p, int blk) {
  const unsigned w = (p.m0 & -(unsigned)(blk < 32)) | (p.m1 & -(unsigned)(blk >> 5 == 1)) | (p.m2 & -(unsigned)(blk >= 64));
  return (blk << 21) | (int)((w >> (blk & 31) & 1) << 13);
}
__device__ __forceinline__ int hand_off(const Params& p, int s) { return side_a(p, HB + s % HRING / 6) + (s % 6) * 2 * H_STEP; }
__device__ __forceinline__ int pt_off(const Params& p, int s) { return side_a(p, PB + s / 12) + (s % 12) * 2 * PT_SEG; }
__device__ __forceinline__ int z_off(const Params& p, int s) { return (side_a(p, slot_blk(s) + 1) ^ 8192) + 524288 + s / RING * 655360; }
template <typename VT> __device__ __forceinline__ void pin(VT& v) {
#pragma unroll
  for (unsigned i = 0; i < __builtin_vectorelements(VT); ++i) { unsigned r = rfl(v[i]); asm volatile("" : "+s"(r)); v[i] = r; }
}
__device__ __forceinline__ void pause(unsigned ticks) {
  const unsigned t0 = now_ticks();
#pragma unroll 1
  while (now_ticks() - t0 < ticks) __builtin_amdgcn_s_sleep(8);
}
#define OPAQUE(v) asm volatile("" : "=v"(v))
__device__ __forceinline__ const __attribute__((address_space(4))) Params* fresh_params() {
  const __attribute__((address_space(4))) Params* pk = (const __attribute__((address_space(4))) Params*)__builtin_amdgcn_kernarg_segment_ptr();
  asm volatile("" : "+s"(pk) :: "memory");
  return pk;
}

__device__ __forceinline__ v4u ld16s(rsrc_t r, int vo, int so) { return __builtin_amdgcn_raw_buffer_load_b128(r, vo, so, 16); }
__device__ __forceinline__ v2u ld8s(rsrc_t r, int vo, int so) { return __builtin_amdgcn_raw_buffer_load_b64(r, vo, so, 16); }
#define TS(cond, i)
#define W240_IN(w) "{a[0:31]}"(w[0]), "{a[32:63]}"(w[1]), "{a[64:95]}"(w[2]), "{a[96:127]}"(w[3]), "{a[128:159]}"(w[4]), "{a[160:191]}"(w[5]), \
                   "{a[192:223]}"(w[6]), "{a[224:239]}"(w##7)
#define W240_IO(m, w) m "{a[0:31]}"(w[0]), m "{a[32:63]}"(w[1]), m "{a[64:95]}"(w[2]), m "{a[96:127]}"(w[3]), m "{a[128:159]}"(w[4]), m "{a[160:191]}"(w[5]), \
                      m "{a[192:223]}"(w[6]), m "{a[224:239]}"(w##7)
constexpr int L_MISC = 122880;
#define FFN_WAIT 100
typedef unsigned v3u __attribute__((ext_vector_type(3)));
__device__ __forceinline__ Params load_params(v3u par) {
  const __attribute__((address_space(4))) Params* k = fresh_params();
  Params p;
  p.x = k->x; p.mem0 = k->mem0; p.wq = k->wq; p.wk = k->wk; p.wv = k->wv; p.wo = k->wo; p.wr = k->wr; p.wg = k->wg; p.wu = k->wu; p.wd = k->wd; p.c = k->c; p.wmod = k->wmod;
  p.out = k->out; p.ws = k->ws; p.dE = k->dE; p.dL = k->dL; p.dM = k->dM; p.dZ = k->dZ; p.dS = k->dS; p.lead = k->lead;
  p.m0 = par[0]; p.m1 = par[1]; p.m2 = par[2]; p.co = side_a(p, LB) ^ 8192;
  return p;
}
__device__ __forceinline__ int look_off(int lane) { return lmap(H_PLAN) + (lane & 3) * 4; }
__device__ __forceinline__ int sum_off(int lane) { return (lane & 3) == 3 ? EPOCH_OFF : SUM_OFF + (blockIdx.x & 7) * 128 + (lane & 3) * 8; }
__device__ __forceinline__ int u_off(int lane, int hi) { return (2 * (USLOT + lane % 20) + hi) << 21 | (lane % 10 == 1) << 13; }
__device__ __forceinline__ int end_job(int type, int h) { const int i = type * 8 + (h & 7); return i < 20 ? ((i >= 10) * 4 + (h >> 3)) * 10 + i % 10 : 80 + (h >> 3) * 12 + i - 20; }
__device__ __forceinline__ int flag_off(const Params& p, int h) { return side_a(p, HB) + lmap(H_FLAG + h * 128); }

#define FPACE 12
#define WARMQ 1
#define FDELAY 0
#define ODELAY 0
#define SEL_AT 60
__device__ __forceinline__ unsigned wave_max_u32(unsigned m) {
  m = max(m, (unsigned)__builtin_amdgcn_update_dpp(0, (int)m, 0xb1, 0xf, 0xf, true)); m = max(m, (unsigned)__builtin_amdgcn_update_dpp(0, (int)m, 0x4e, 0xf, 0xf, true));
  m = max(m, (unsigned)__builtin_amdgcn_update_dpp(0, (int)m, 0x124, 0xf, 0xf, true)); m = max(m, ror8u(m));
  auto a = __builtin_amdgcn_permlane16_swap(m, m, false, false); m = max(a[0], a[1]);
  auto b = __builtin_amdgcn_permlane32_swap(m, m, false, false); return max(b[0], b[1]);
}
__device__ __forceinline__ unsigned score_key(unsigned r, int row) {
  unsigned u = f2u(u2f(r) + 0.f);
  u = (u & 0x80000000u) ? ~u : (u | 0x80000000u);
  return (u & 0xffff0000u) | (unsigned)(S - 1 - row);
}
template <int N>
__device__ __forceinline__ unsigned top8(const unsigned* key, int lane) {
  unsigned mine = 0, bound = 0;
#pragma unroll 1
  for (int it = 0; it < TOPK; ++it) {
    unsigned m = 0;
#pragma unroll
    for (int i = 0; i < N; ++i) m = max(m, key[i] - bound);
    bound += wave_max_u32(m);
    if ((lane & 7) == it) mine = bound;
  }
  return mine;
}
__device__ __forceinline__ void select_rows(v3u par, int lane) {
  const Params p = load_params(par);
  gu32* ctrl = (gu32*)(p.ws + p.co);
  const rsrc_t rR = mkrsrc(p.ws + p.co + C_R * 4, S * 4);
  unsigned key[28], mine;
  wait_ge<32>(ctrl, C_G, SEL_AT);
  if (lane == 0) __hip_atomic_fetch_add((gu32*)(p.ws + EPOCH_OFF), 1u, RLX_AGENT);
#pragma unroll 1
  for (int spin = 0;; ++spin) {
    asm volatile("" ::: "memory");
    v4u rr[7];
#pragma unroll
    for (int i = 0; i < 7; ++i) rr[i] = ld16s(rR, lane * 16 + i * 1024, 0);
    __builtin_amdgcn_sched_barrier(0);
    unsigned m = 0;
#pragma unroll
    for (int i = 0; i < 28; ++i) { m = max(m, rr[i >> 2][i & 3]); key[i] = score_key(rr[i >> 2][i & 3], (i >> 2) * 256 + lane * 4 + (i & 3)); }
    if (__builtin_amdgcn_ballot_w64(m == ~0u) == 0) break;
    if (spin > SPIN_CAP / 4) { __hip_atomic_store(ctrl + C_ERR, 1u, RLX_AGENT); break; }
    __builtin_amdgcn_s_sleep(16);
  }
  mine = top8<28>(key, lane);
  __hip_atomic_store(ctrl + C_IDX + (lane >> 3) * 32 + (lane & 7), S - (mine & 0xffffu), RLX_AGENT);
  TS(lane == 0, 16)
  key[4] = lane < 8 ? mine : 0u;
#pragma unroll 1
  for (int spin = -1;; ++spin) {
    asm volatile("" ::: "memory");
    const v4u r = ld16s(rR, lane * 16 + 7 * 1024, 0);
    const bool all = __builtin_amdgcn_ballot_w64(max(max(r[0], r[1]), max(r[2], r[3])) == ~0u) == 0 || spin > SPIN_CAP;
    if (!all && spin >= 0) { __builtin_amdgcn_s_sleep(1); continue; }
    TS(lane == 0, 17)
#pragma unroll
    for (int i = 0; i < 4; ++i) key[i] = score_key(r[i], S - 256 + lane * 4 + i);
    unsigned fin = mine;
    if (spin < 0 || __builtin_amdgcn_ballot_w64(max(max(key[0], key[1]), max(key[2], key[3])) > (unsigned)__builtin_amdgcn_readlane(mine, 7))) fin = top8<5>(key, lane);
    if (!all) continue;
    __hip_atomic_store(ctrl + C_IDX + 256 + (lane >> 3) * 32 + (lane & 7), S - (fin & 0xffffu), RLX_AGENT);
    break;
  }
  TS(lane == 0, 18)
}

__device__ __forceinline__ void ffn_warm(int m, int i, int lane) {
  const auto* k = fresh_params();
  asm volatile("s_mov_b32 m0, %0\n\ts_nop 3\n\tbuffer_load_dword %1, %2, %3 offen lds" ::"s"(MAIN_LDS - 256), "v"(lane * 128), "s"(mkrsrc(m == 0 ? k->wg : m == 1 ? k->wu : k->wd, DM * FF * 2)), "s"(i * 8192) : "memory");
}
__device__ __forceinline__ void ffn_role(const Params& p, int role, int lane, int wave) {
  gu32* ctrl = (gu32*)(p.ws + p.co);
  const int g = lane >> 4, n = lane & 15, q = (lane >> 2) & 3, pp = lane & 3;
  const bool down = role >= 8;
  const int c = role - 8;
  v4u wa[20], wd[12];
  {
    const rsrc_t rg = mkrsrc(p.wg, DM * FF * 2), ru = mkrsrc(p.wu, DM * FF * 2), rd = mkrsrc(p.wd, FF * DM * 2);
    BAR();
    pause(FDELAY + ((role * 4 + wave) * 37 & 63) * FPACE / 24);
#pragma unroll 1
    for (int i = 0; i < 10; ++i) {
      const int vo = ((wave * 160 + i * 16 + (lane >> 2)) * FF + role * 32) * 2 + (lane & 3) * 16;
      dma16(rg, vo, 0, wave * 32768 + i * 1024); __builtin_amdgcn_s_sleep(FPACE); dma16(ru, vo, 0, wave * 32768 + 10240 + i * 1024);
      __builtin_amdgcn_s_sleep(FPACE);
    }
#pragma unroll 1
    for (int i = 0; i < 12; ++i) { dma16(rd, down ? ((wave * 384 + i * 32 + (lane >> 1)) * DM + c * 16) * 2 + (lane & 1) * 16 : OOB, 0, wave * 32768 + 20480 + i * 1024); __builtin_amdgcn_s_sleep(FPACE); }
    VMCNT(0);
#pragma unroll
    for (int i = 0; i < 32; ++i) {
      const unsigned a = wave * 32768 + (i < 20 ? ((i / 5) & 1) * 10240 + (i % 5) * 2048 + (8 * g + q) * 64 + (i / 10) * 32 : i * 1024 + (8 * g + q) * 32) + pp * 8;
      const v2u lo = trread(a), hi = trread(a + (i < 20 ? 256 : 128));
      const v4u x = {lo[0], lo[1], hi[0], hi[1]};
      if (i < 20) wa[i] = x; else wd[i - 20] = x;
    }
  }
  const rsrc_t rT = mkrsrc(p.ws + X1_OFF, (unsigned)(WS_END - X1_OFF));
  constexpr int SO_H2 = DM * 2, SO_ACT = (int)(ACT_OFF - X1_OFF), RED = 131072, LW = RED + 24576;
  int row = -1;
  v2u res = {};
#pragma unroll 1
  for (int rnd = 0; rnd < 2; ++rnd) {
    if (wave == 0) {
      const int w = C_IDX + rnd * 256 + (role & 7) * 32 + (lane & 7);
      if (rnd) wait_ge<1>(ctrl, w, 1); else wait_ge<16>(ctrl, w, 1);
      if (lane < 8) *(LAS unsigned*)(LW + lane * 4) = __hip_atomic_load(ctrl + w, RLX_AGENT);
    }
    __syncthreads();
    const unsigned t0 = now_ticks();
    const int nr = (*(LAS int*)(LW + (n & 7) * 4) - 1) & (S - 1);
    if (__builtin_amdgcn_ballot_w64(nr != row) == 0) break;
    row = nr;
    v2u x1, h2, gb; unsigned rv;
    if (down && wave == 0) { gb = ld8s(mkrsrc(p.ws + MODB_OFF, 7680), (5 * DM + c * 16 + 4 * g) * 2, 0); x1 = ld8s(rT, (row * 2 * DM + c * 16 + 4 * g) * 2, 0); h2 = ld8s(rT, (row * 2 * DM + c * 16 + 4 * g) * 2, SO_H2); rv = __hip_atomic_load(ctrl + C_R + row, RLX_AGENT); }
    {
      v4u xb[5];
#pragma unroll
      for (int i = 0; i < 5; ++i) xb[i] = ld16s(rT, (row * 2 * DM + wave * 160 + i * 32 + 8 * g) * 2, SO_H2);
      __builtin_amdgcn_sched_barrier(0);
      f4 ac[4] = {};
#pragma unroll
      for (int i = 0; i < 20; ++i) ac[i & 3] = mfma32(wa[(i & 3) * 5 + (i >> 2)], xb[i >> 2], ac[i & 3]);
#pragma unroll
      for (int k = 0; k < 4; ++k) *(LAS f4*)(RED + wave * 4096 + k * 1024 + lane * 16) = ac[k];
      __syncthreads();
      if (wave >= 2) {
        f4 ag = {}, au = {};
#pragma unroll
        for (int w = 0; w < 4; ++w) { ag += *(LAS f4*)(RED + w * 4096 + (wave - 2) * 2048 + lane * 16); au += *(LAS f4*)(RED + w * 4096 + (wave - 2) * 2048 + 1024 + lane * 16); }
        float a[4];
#pragma unroll
        for (int r = 0; r < 4; ++r) { const float gs = rbf(ag[r]); a[r] = rbf(gs / (1.f + __expf(-gs))) * rbf(au[r]); }
        __builtin_amdgcn_raw_buffer_store_b64(v2u{unsent(pack2(a[0], a[1])), unsent(pack2(a[2], a[3]))}, rT,
                                              n < 8 ? rnd * 24576 + (role * 2 + wave - 2) * 256 + (g >> 1) * 128 + n * 16 + (g & 1) * 8 : OOB, SO_ACT, 16);
      }
    }
    if (!down) continue;
    while ((int)(now_ticks() - t0) < FFN_WAIT) __builtin_amdgcn_s_sleep(1);
    v4u ab[12];
#pragma unroll 1
    for (int spin = 0;; ++spin) {
      asm volatile("" ::: "memory");
      unsigned m = 0;
#pragma unroll
      for (int i = 0; i < 12; ++i) ab[i] = ld16s(rT, rnd * 24576 + (wave * 12 + i) * 512 + g * 128 + (n & 7) * 16, SO_ACT);
      __builtin_amdgcn_sched_barrier(0);
#pragma unroll
      for (int i = 0; i < 12; ++i) m = max(max(m, max(ab[i][0], ab[i][1])), max(ab[i][2], ab[i][3]));
      if (__builtin_amdgcn_ballot_w64(m == ~0u) == 0) break;
      if (spin > SPIN_CAP) { __hip_atomic_store(ctrl + C_ERR, 1u, RLX_AGENT); break; }
      __builtin_amdgcn_s_sleep(2);
    }
    f4 y = {};
#pragma unroll
    for (int i = 0; i < 12; ++i) y = mfma32(wd[i], ab[i], y);
    *(LAS f4*)(RED + 16384 + wave * 1024 + lane * 16) = y;
    __syncthreads();
    if (wave == 0) {
#pragma unroll
      for (int w = 1; w < 4; ++w) y += *(LAS f4*)(RED + 16384 + w * 1024 + lane * 16);
      const float rs = rbf(1.f / (1.f + __expf(-u2f(rv))));
      float o[4];
#pragma unroll
      for (int r = 0; r < 4; ++r) {
        const float hv = (r & 1) ? hi2f(h2[r >> 1]) : lo2f(h2[r >> 1]), xv = (r & 1) ? hi2f(x1[r >> 1]) : lo2f(x1[r >> 1]);
        o[r] = xv + rbf(((r & 1) ? hi2f(gb[r >> 1]) : lo2f(gb[r >> 1])) * rbf(hv + rbf(rbf(y[r]) * rs)));
      }
      res = v2u{pack2(o[0], o[1]), pack2(o[2], o[3])};
    }
    TS(role == 8 && wave == 0 && lane == 0, 14 + rnd)
  }
  if (down && wave == 0) __builtin_amdgcn_raw_buffer_store_b64(res, mkrsrc(p.out, S * DM * 2), n < 8 ? (row * DM + c * 16 + 4 * g) * 2 : OOB, 0, 16);
}

constexpr int L_LN = 123904, L_XR = 139264;
__device__ __forceinline__ void ln1_fetch(const u16* x, int tile, int lane, int wave) {
  const rsrc_t rx = mkrsrc(x, S * DM * 2);
#pragma unroll
  for (int k = 0; k < 4; ++k) dma16(rx, lane * 16, (tile * 16 + wave * 4 + k) * 1280, L_LN + (wave * 4 + k) * 1040);
  dma16(rx, (lane >> 4) * 1280 + 1024 + (lane & 15) * 16, (tile * 16 + wave * 4) * 1280, L_LN + 16640 + wave * 1024);
}
__device__ __forceinline__ void ln1_rows(unsigned char* ws, unsigned epoch, int tile, int lane, int wave) {
  const rsrc_t rh = mkrsrc(ws + HF_OFF, (unsigned)HF_BYTES), rm = mkrsrc(ws + MODG_OFF, 5120);
  const int r = wave * 8 + (lane & 7);
  v4u a[10];
#pragma unroll
  for (int i = 0; i < 10; ++i) a[i] = lds128(L_LN + (i < 8 ? r * 1040 : 16640 - 1024 + r * 256) + (lane >> 3) * 16 + i * 128);
  float s = 0.f, q = 0.f;
  const bf2 one = {(__bf16)1.f, (__bf16)1.f};
#pragma unroll
  for (int i = 0; i < 10; ++i)
#pragma unroll
    for (int k = 0; k < 4; ++k) { const unsigned w = a[i][k]; s = __builtin_amdgcn_fdot2_f32_bf16(__builtin_bit_cast(bf2, w), one, s, false); }
  s += ror8f(s); s = swapsum16(s, s); s = swapsum32(s, s);
  const float mean = s * (1.f / DM);
#pragma unroll
  for (int i = 0; i < 10; ++i)
#pragma unroll
    for (int k = 0; k < 4; ++k) { const float d0 = lo2f(a[i][k]) - mean, d1 = hi2f(a[i][k]) - mean; q = fmaf(d0, d0, fmaf(d1, d1, q)); }
  q += ror8f(q); q = swapsum16(q, q); q = swapsum32(q, q);
  const float mb = rbf(mean), isd = __builtin_amdgcn_rcpf(rbf(__builtin_amdgcn_sqrtf(rbf(rbf(q * (1.f / DM)) + 1e-5f))));
#pragma unroll
  for (int i = 0; i < 10; ++i) {
#pragma unroll
    for (int k = 0; k < 4; ++k) { const unsigned d = pack2(lo2f(a[i][k]) - mb, hi2f(a[i][k]) - mb); a[i][k] = pack2(lo2f(d) * isd, hi2f(d) * isd); }
    asm volatile("" : "+v"(a[i]));
  }
  const unsigned lm = L_LN + 21504 + wave * 2560;
#pragma unroll 1
  for (int spin = 0;; ++spin) {
    asm volatile("" ::: "memory");
    v4u m[5];
    bool ok = true;
#pragma unroll
    for (int i = 0; i < 5; ++i) m[i] = ld16s(rm, lane * 16 + i * 1024, 0);
#pragma unroll
    for (int i = 0; i < 5; ++i) { ok &= m[i][0] == epoch + 1 && m[i][2] == epoch + 1; *(LAS v2u*)(lm + i * 512 + lane * 8) = v2u{m[i][1], m[i][3]}; }
    if (__builtin_amdgcn_ballot_w64(!ok) == 0 || spin > SPIN_CAP) break;
    __builtin_amdgcn_s_sleep(8);
  }
  LGKM0();
  { const auto& p = *fresh_params(); TS(wave == 0 && lane == 0 && tile == 15, 61) }
#pragma unroll
  for (int i = 0; i < 10; ++i) {
    const v4u sh = lds128(lm + (lane >> 3) * 16 + i * 128), sc = lds128(lm + 1280 + (lane >> 3) * 16 + i * 128);
    v4u o;
#pragma unroll
    for (int k = 0; k < 4; ++k) {
      const unsigned m = pack2(lo2f(a[i][k]) * lo2f(sc[k]), hi2f(a[i][k]) * hi2f(sc[k]));
      o[k] = pack2(lo2f(m) + lo2f(sh[k]), hi2f(m) + hi2f(sh[k]));
    }
    __builtin_amdgcn_raw_buffer_store_b128(o, rh, tile * 20480 + i * 2048 + (lane >> 3) * 256 + r * 16, 0, 16);
  }
}
__device__ __forceinline__ void w_issue(rsrc_t rw, int type, int h, int m, int slot, int i0, int cnt, int lane) {
#pragma unroll 1
  for (int i = i0; i < i0 + cnt; ++i) {
    const int c = i * 64 + lane;
    dma16(rw, type < 3 ? ((m * 160 + c / 24) * HD + h * DH) * 2 + (c % 24) * 16 : (h * DH + (i < 40 ? 32 * m : 96 + 16 * m)) * DM * 2 + c * 16, 0, slot * 61440 + i * 1024);
  }
}
__device__ __forceinline__ rsrc_t w_rsrc(int type) { const auto* k = fresh_params(); return mkrsrc(type == 0 ? k->wk : type == 1 ? k->wq : type == 2 ? k->wv : k->wo, DM * HD * 2); }
__device__ __forceinline__ void put_line(unsigned char* ws, int off, v4u v, int lanes, int lane) {
  __builtin_amdgcn_raw_buffer_store_b128(v, mkrsrc(ws, WS_BYTES), lane < lanes ? off + lane * 16 : OOB, 0, 16);
}
__device__ __forceinline__ void sum_table(unsigned char* ws, unsigned old, int lane) {
  const unsigned epoch = old + 1;
  typedef GAS unsigned long long gu64;
#pragma unroll 1
  for (int spin = 0;; ++spin) {
    const unsigned long long a = __hip_atomic_load((gu64*)(ws + TAB_OFF) + lane * 16, RLX_AGENT), b = __hip_atomic_load((gu64*)(ws + TAB_OFF) + min(64 + lane, NTAB - 1) * 16, RLX_AGENT);
    if (__builtin_amdgcn_ballot_w64((unsigned)a != epoch || (unsigned)b != epoch) == 0 || spin > SPIN_CAP) {
      LAS int* v = (LAS int*)L_MISC;
      v[lane] = 0; v[128 + lane] = 0;
      LGKM0();
      v[16 + lane] = (int)(a >> 32); v[80 + lane] = (int)(b >> 32);
      LGKM0();
      int sa = 0, sb = 0;
#pragma unroll
      for (int k = -CALWIN; k < CALWIN; ++k) { sa += v[16 + lane + k]; sb += v[80 + lane + k]; }
      const unsigned long long lo = __builtin_amdgcn_ballot_w64((sa > 0) ^ (lane & 1)), hi = __builtin_amdgcn_ballot_w64((sb > 0) ^ (lane & 1));
      put_line(ws, SUM_OFF, lane & 1 ? v4u{epoch, (unsigned)hi, epoch, 0u} : v4u{epoch, (unsigned)lo, epoch, (unsigned)(lo >> 32)}, 64, lane);
      return;
    }
    __builtin_amdgcn_s_sleep(2);
  }
}
__device__ __forceinline__ v3u wait_table(unsigned char* ws, unsigned old, int lane) {
  const unsigned epoch = old + 1;
#pragma unroll 1
  for (int spin = 0;; ++spin) {
    const unsigned long long a = __hip_atomic_load((GAS unsigned long long*)(ws + SUM_OFF) + (blockIdx.x & 7) * 16 + (lane & 3), RLX_AGENT);
    if (__builtin_amdgcn_ballot_w64((unsigned)a != epoch) == 0 || spin > SPIN_CAP)
      return v3u{(unsigned)__builtin_amdgcn_readlane(a >> 32, 0), (unsigned)__builtin_amdgcn_readlane(a >> 32, 1), (unsigned)__builtin_amdgcn_readlane(a >> 32, 2)};
    __builtin_amdgcn_s_sleep(4);
  }
}
__device__ __forceinline__ void calibrate(unsigned char* ws, unsigned epoch, int blk, int lane) {
  const rsrc_t r = mkrsrc(ws, WS_BYTES);
  unsigned best[2] = {1u << 30, 1u << 30};
  const bool busy = (unsigned)(blk - 2 * USLOT) % 20u < 4u && blk < 2 * USLOT + 40;
#pragma unroll 1
  for (int rep = -1; rep < (busy ? -1 : CALREP); ++rep) {
    const int off = lane < (rep < 0 ? 16 : 8) ? (blk << 21) + 0x1FC000 + (rep < 0 ? lane >> 3 : rep & 1) * 8192 + (lane & 7) * 16 : OOB;
    const unsigned long long t1 = __builtin_amdgcn_s_memtime();
    LGKM0();
    __builtin_amdgcn_raw_buffer_store_b128(v4u{1u, 2u, 3u, 4u}, r, off, 0, 16);
    VMCNT(0);
    const unsigned d = rep < 0 ? 1u << 30 : (unsigned)(__builtin_amdgcn_s_memtime() - t1);
    if (rep & 1) best[1] = min(best[1], d); else best[0] = min(best[0], d);
  }
  const int d = min(max((int)(best[1] - best[0]), -250), 250);
  put_line(ws, TAB_OFF + blk * 128, v4u{epoch + 1, (unsigned)(blk & 1 ? -d : d), 0u, 0u}, 8, lane);
}
__device__ __forceinline__ v3u token_start(v32u (&wa)[7], v16u& wa7, int type, int h, int lane, int wave) {
  const auto* k = fresh_params();
  const unsigned epoch = __hip_atomic_load((gu32*)(k->ws + EPOCH_OFF), RLX_AGENT);
  const int duty = (0x2130 >> 4 * type) & 3;
  const int tile = duty < 3 ? h * 3 + duty : 96 + h, cb = duty * 32 + h, g = lane >> 4, q = (lane >> 2) & 3, pp = lane & 3;
  const rsrc_t rw = w_rsrc(type);
  const int cal = wave * 32 + (h & 7) * 4 + (h >> 3);
  if (duty == 3 && cal < NCAL) calibrate(k->ws, epoch, cal, lane);
  if (duty == 3 && wave == 3) {
#pragma unroll
    for (int j = 0; j < 3; ++j) {
      const int i = (j * 32 + h) * 64 + lane;
      __builtin_amdgcn_raw_buffer_store_b128(v4u{0u, 0u, 0u, 0u}, mkrsrc(k->ws, WS_BYTES),
                                             i < ZERO_BYTES / 16 ? CTRL_OFF + i * 16 : i < ZERO_BYTES / 16 + 512 ? (HB << 21) + lmap(H_FLAG) + (i & 255) * 16 + (i & 256) * 32 : OOB, 0, 16);
    }
    VMCNT(0);
    put_line(k->ws, TAB_OFF + (NCAL + h) * 128, v4u{epoch + 1, 0u, 0u, 0u}, 8, lane);
  }
  if (duty == 3) { BAR(); if (wave < 3 && WARMAT == 0) ffn_warm(wave, blockIdx.x, lane); }
  if (cb == 127 && wave == 0) sum_table(k->ws, epoch, lane);
  LAS float* red = (LAS float*)(L_LN + 20736);
  if (wave < 2) {
    const rsrc_t rm = mkrsrc(k->wmod, DM * 6 * DM * 2), rc = mkrsrc(k->c, DM * 2);
    const int rg = wave * 320 + (lane >> 2);
    v4u w[20]; unsigned cv[5];
#pragma unroll
    for (int i = 0; i < 5; ++i) cv[i] = __builtin_amdgcn_raw_buffer_load_b16(rc, (wave * 320 + i * 64 + lane) * 2, 0, 0);
#pragma unroll
    for (int i = 0; i < 20; ++i) w[i] = __builtin_amdgcn_raw_buffer_load_b128(rm, cb < 120 ? ((rg + 16 * i) * 3840 + cb * 32 + pp * 8) * 2 : OOB, 0, MODAUX);
    ln1_fetch(k->x, tile, lane, 2 * wave); ln1_fetch(k->x, tile, lane, 2 * wave + 1);
    __builtin_amdgcn_sched_barrier(0);
    LAS float* sl = (LAS float*)(L_LN + 21504 + wave * 2560);
#pragma unroll
    for (int i = 0; i < 5; ++i) { const float c = bf2f((u16)cv[i]); sl[i * 64 + lane] = rbf(c * __builtin_amdgcn_rcpf(1.f + __expf(-c))); }
    LGKM0();
    float acc[8] = {};
#pragma unroll
    for (int i = 0; i < 20; ++i) {
      const float sv = sl[16 * i + (lane >> 2)];
#pragma unroll
      for (int j = 0; j < 4; ++j) { acc[2 * j] += sv * lo2f(w[i][j]); acc[2 * j + 1] += sv * hi2f(w[i][j]); }
    }
#pragma unroll
    for (int j = 0; j < 8; ++j) {
      float v = acc[j];
      v += u2f(__builtin_amdgcn_update_dpp(0, (int)f2u(v), 0x124, 0xf, 0xf, true)); v += ror8f(v); v = swapsum16(v, v); v = swapsum32(v, v);
      if (lane < 4) red[wave * 32 + lane * 8 + j] = v;
    }
    VMCNT(0); LGKM0();
    { const auto& p = *k; TS(wave == 0 && lane == 0 && cb == 5, 60) }
  } else if (cb >= 40) { __builtin_amdgcn_s_sleep(8); w_issue(rw, type, h, wave - 2, wave - 2, 0, 30, lane); }
  BAR();
  if (wave >= 2) w_issue(rw, type, h, wave - 2, wave - 2, cb >= 40 ? 30 : 0, cb >= 40 ? 30 : 60, lane);
  else {
    if (wave == 1) {
      const int c = 2 * (lane & 15);
      float s0 = red[c] + red[32 + c], s1 = red[c + 1] + red[33 + c];
      if ((cb / 20) % 3 == 1) { s0 = 1.f + rbf(s0); s1 = 1.f + rbf(s1); }
      if (cb < 40) __builtin_amdgcn_raw_buffer_store_b64(v2u{epoch + 1, pack2(s0, s1)}, mkrsrc(k->ws + MODG_OFF, 5120), lane < 16 ? cb * 128 + lane * 8 : OOB, 0, 16);
      else __builtin_amdgcn_raw_buffer_store_b32(pack2(s0, s1), mkrsrc(k->ws + MODB_OFF, 7680), lane < 16 ? cb * 64 + lane * 4 : OOB, 0, 16);
    }
    ln1_rows(k->ws, epoch, tile, lane, wave);
  }
  const v3u par = wait_table(k->ws, epoch, lane);
  const Params p = load_params(par);
  gu32* ctrl = (gu32*)(p.ws + p.co);
  VMCNT(0);
  if (wave < 2 && lane == 0) {
    __hip_atomic_fetch_add(ctrl + C_LN + (duty < 3 ? tile % 6 : 6 + (h & 1)) * 32, 1u, RLX_AGENT);
    if (wave == 1 && cb >= 40 && cb < 120) __hip_atomic_fetch_add(ctrl + C_MOD + (cb & 1) * 32, 1u, RLX_AGENT);
  }
  TS(wave == 0 && lane == 0 && type == 0, 640 + h)
  const v2u tb = type < 3 ? v2u{(unsigned)((8 * g + q) * 384 + (32 * wave + 8 * pp) * 2), (unsigned)((8 * g + q) * 384 + (128 + 16 * wave + 4 * pp) * 2)}
                          : v2u{(unsigned)((8 * g + q) * 1280 + (160 * wave + 4 * pp) * 2), (unsigned)((32 + 4 * g + q) * 1280 + (160 * wave + 4 * pp) * 2)};
  BAR();
  asm volatile(TAKE0_ASM : W240_IO("=", wa) : "{v[0:1]}"(tb), "{s52}"(type) : "memory", "scc", TAKE0_CLOBBERS);
  BAR();
  TS(wave == 0 && lane == 0 && cb == 5, 62)
  w_issue(rw, type, h, 2 + (wave >> 1), wave >> 1, (wave & 1) * 30, 30, lane);
  VMCNT(0); BAR();
  asm volatile(TAKE1_ASM : W240_IO("+", wa) : "{v[0:1]}"(tb), "{s52}"(type) : "memory", "scc", TAKE1_CLOBBERS);
  if (wave == 2 && type < 3) wait_ge<2>(ctrl, C_LN + (lane % 6) * 32, 32);
  BAR();
  TS(wave == 0 && lane == 0, 512 + type * 32 + h)
  return par;
}
__device__ __forceinline__ void c_args(v32u& vc, v16u& sc, const Params& p, int c8, int rep, int lane, int wave) {
  constexpr int L_Z = 10240;
  const int g = lane >> 4, n = lane & 15, j0 = 4 * (g & 1) + 2 * (g >> 1), hi = c8 >> 6;
  OPAQUE(vc);
  vc[0] = n < 8 ? 0x4444 : 0xEEEE;
  vc[1] = lane * 16;
  vc[2] = L_Z + (g >> 1) * 256 + n * 16 + (g & 1) * 8;
  vc[3] = L_Z + n * 16 + j0 * 2;
  vc[4] = hand_off(p, lane) + rep * 2 * H_COPY;
  vc[5] = (c8 & 63) * 32768 + (n >> 3) * 512 + g * 128 + (n & 7) * 16;
  vc[7] = lane == 0 && (wave == 0 || wave == 3) ? p.co + (C_ANCH + 128 + rep * 32) * 4 : OOB;
  vc[8] = p.co + (C_ANCH + rep * 32 + lane % 3) * 4;
  if (wave == 3) {
    vc[9] = side_a(p, slot_blk(lane) + hi);
    vc[10] = (n >> 3) * 512 + g * 128 + (n & 7) * 16;
    vc[11] = 0;
    vc[6] = vc[10] ^ 512;
  } else {
    vc[9] = z_off(p, lane);
    vc[10] = lmap(c8 * 4096) + lane * 16;
    vc[11] = lane == 0 ? p.co + (C_Z + (c8 & 7) * 32) * 4 : OOB;
  }
  vc[12] = ((n & 7) >> 1) - 2 * (g & 1);
  vc[13] = lmap(c8 * 128) + (n & 7) * 16 + j0 * 2 + ((rep + (n >> 3)) & 3) * 2 * H_COPY;
  if (wave == 3) vc[2] = lmap(c8 * 128) + (n & 7) * 16 + j0 * 2 + ((rep + 2 + (n >> 3)) & 3) * 2 * H_COPY;
  vc[14] = CTRL_OFF + (C_U + 3 * 384 + 256 + rep * 32 + (lane & 7)) * 4;
  vc[15] = 1;
#pragma unroll
  for (int i = 0; i < 5; ++i) {
    vc[18 + i] = vc[5] + lmap((wave * 5 + i) * 1024);
    vc[23 + i] = c8 >= 0 && wave * 5 + i < 16 ? vc[18 + i] : OOB;
  }
  vc[28] = sum_off(lane);
  vc[29] = (2 * ((g >> 1) ^ (n >> 3)) + (n >> 3)) * 128 + (n & 7) * 16 + (g & 1) * 8;
  vc[16] = u_off(lane, c8 >= 64); vc[17] = look_off(lane);
#pragma unroll
  for (int i = 4; i < 16; ++i) sc[i] = 0;
  sc[0] = wave; sc[1] = p.dL + p.dM; sc[2] = p.dS; sc[3] = p.dZ; sc[8] = rfl(rep * 2 * H_COPY);
}

__device__ __forceinline__ void u_role(const Params& p, int h, int th, int c8, int rep, int lane, int wave) {
  const int g = lane >> 4, n = lane & 15, q = (lane >> 2) & 3, pp = lane & 3;
  v32u vc; v12u sr; v16u sc;
  OPAQUE(vc);
#pragma unroll
  for (int k = 0; k < 7; ++k) { const int c = k * 64 + lane; vc[k] = ((h * DH + c / 28) * DM + 208 * th) * 2 + ((c % 28) ^ (c / 28 >> 2 & 2)) * 16 - (k & 3) * 1024; }
  const int ch = wave >> 1;
  vc[7] = (8 * g + q) * 448 + pp * 8 + ((7 * ch) ^ (g & 1)) * 32;
  vc[26] = (8 * g + q) * 448 + pp * 8 + ((7 * ch + 1) ^ (g & 1)) * 32;
#pragma unroll
  for (int k = 0; k < 12; ++k) { const int c = k * 64 + lane, r = c / 24; vc[8 + k] = (r * HD + h * DH) * 2 + (((c % 24) ^ (r >> 2)) * 16) - (k & 3) * 1024; }
  const int row = 16 * (n >> 3) + 8 * ((n >> 1) & 1) + 4 * ((n >> 2) & 1) + (n & 1);
  vc[20] = 86016 + (wave & 1) * 24576 + row * 384 + (g ^ (row >> 2)) * 16;
  vc[21] = vc[20] ^ 64;
  vc[22] = (26 * th + 14 * ch + (n >> 3)) * 32768 + lmap(h * 512) + g * 128 + (n & 7) * 16;
  vc[23] = u_off(lane, th == 2 && ch); vc[24] = u_off(lane, th == 2);
#pragma unroll
  for (int k = 0; k < 3; ++k) { const int c = (k * 12 + th * 4 + wave) * 64 + lane; vc[27 + k] = (c / 3 * HD + h * DH) * 2 + c % 3 * 128; }
  set_rsrc(sr, 0, p.ws, WS_BYTES); set_rsrc(sr, 1, p.wv, DM * HD * 2); set_rsrc(sr, 2, p.wo, DM * HD * 2);
  vc[25] = lane == 0 && wave == 0 ? CTRL_OFF + (C_U + (h & 7) * 32) * 4 : OOB;
  vc[30] = CTRL_OFF + (C_U + 256 + rep * 32 + (lane & 7)) * 4;
  vc[31] = lane < 4 ? CTRL_OFF + (C_U + 256 + lane * 32 + (h & 7)) * 4 : OOB;
  v32u vd;
  c_args(vd, sc, p, c8, rep, lane, wave);
  sc[9] = c8; sc[10] = (th == 2 && ch) << 21; sc[11] = (th == 2) << 21;
  pin(sc); pin(sr);
  asm volatile(UG_ASM CROLE_ASM : "+{v[0:31]}"(vc), "+{v[208:239]}"(vd), "+{s[52:67]}"(sc), "+{s[36:47]}"(sr) :: "memory", "vcc", "scc", CROLE_CLOBBERS);
  gu32* ctrl = (gu32*)(fresh_params()->ws + CTRL_OFF);
  if (c8 >= 0 && fresh_lane() == 0) {
    if (sc[1] > 20000u || sc[5] > 20000u) { __hip_atomic_store(ctrl + C_ERR, 1u, RLX_AGENT); __hip_atomic_store(ctrl + C_ERR + 2048, 1u, RLX_AGENT); }
  }
}

__device__ __forceinline__ void proj_role(const v32u (&wa)[7], const v16u& wa7, const Params& p, int type, int head, int lane, int wave) {
  gu32* ctrl = (gu32*)(p.ws + p.co);
  {
    const rsrc_t rh = mkrsrc(p.ws + HF_OFF, (unsigned)HF_BYTES);
#pragma unroll
    for (int k = 0; k < 20; ++k) dma16_sc1(rh, wave * 10240 + lane * 16, (k / 10) * 40960 + (k % 10) * 1024, (k / 10) * 40960 + wave * 10240 + (k % 10) * 1024);
  }
  const int ej = end_job(type, head);
  if (ej < 80)
#pragma unroll 1
    for (int j = 0; j < 4; ++j)
      dma16(mkrsrc(p.x, S * DM * 2), lane < 40 ? lane * 16 : OOB, ((OSPLIT + ej / 10 + (j >> 1) * 8) * 32 + ej % 10 * 4 + wave) * 1280 + (j & 1) * 640, L_XR + (wave * 4 + j) * 1024);
  v12u vc; v8u sr;
  vc[0] = lane * 16; vc[1] = wave * 10240 + lane * 16;
  if (type < 2) {
    vc[2] = lmap(KQ_OFF + head * 24576 + type * 8192) + wave * 2048 + lane * 16;
    vc[3] = lmap(KQ_OFF + head * 24576 + 16384) + type * 4096 + wave * 1024 + lane * 16;
    vc[4] = side_a(p, slot_blk(lane) + 1);
    vc[5] = lane == 0 ? flag_off(p, head) + (type * 4 + wave) * 4 : OOB;
  } else {
    vc[2] = head * 32768 + wave * 2048 + lane * 16;
    vc[3] = head * 32768 + 16384 + wave * 1024 + lane * 16;
    vc[4] = side_a(p, slot_blk(lane)) ^ 8192;
    vc[5] = lane == 0 ? p.co + (C_VF + head * 32 + wave) * 4 : OOB;
  }
  vc[6] = 0;
  {
    const int pc = wave * 64 + type * 32 + head, k = pc % 34, o = k < 10 ? k : k + 6;
    vc[7] = type < 2 && pc < 136 ? (pc / 34) * 2 * H_COPY + lmap(o * 1024) + lane * 16 : OOB;
    vc[8] = lane < 63 ? hand_off(p, lane + 1) : OOB;
  }
  vc[9] = 0; vc[10] = 0; vc[11] = 0;
  set_rsrc(sr, 0, p.ws + HF_OFF, (unsigned)HF_BYTES); set_rsrc(sr, 1, p.ws, WS_BYTES);
#pragma unroll 1
  for (int k = 0; k < 11; ++k) {
    if (wave == 0 && k > 1) {
      if (k == 2) wait_ge<8>(ctrl, C_LN + (6 + (lane & 1)) * 32, 32);
      wait_ge<8>(ctrl, C_G + (head & 15) * 32, max(6 * k - (k < 8 ? AHEAD : AHEAD2), 1));
      if (6 * k + 5 >= RING) wait_ge<8>(ctrl, C_AF + min(max(6 * k + lane % 6 - RING, 0), NSEG - 1 - RING) * 32, 2);
    }
    v4u sc = {6u * k, min(6u * k + 6u, 64u), (unsigned)wave * 10240u, 0u};
    v12u vt = vc;
    pin(sc);
    asm volatile(PROJ_ASM : "+{s[52:55]}"(sc), "+{v[0:11]}"(vt) : "{s[36:43]}"(sr), W240_IN(wa) : "memory", "vcc", "scc", PROJ_CLOBBERS);
    TS(wave == 0 && lane == 0 && head == 0 && (k == 4 || k == 7 || k == 10), 20 + type * 4 + (k - 4) / 3)
  }
}

__device__ __forceinline__ void first_markers(const Params& p, int i) {
  const rsrc_t r = mkrsrc(p.ws, WS_BYTES);
  const v4u m = {~0u, ~0u, ~0u, ~0u};
  const int h0 = side_a(p, HB);
#pragma unroll 1
  for (int j = i; j < 4 * 2560; j += 8192) {
    const int o = j % 2560;
    v4u val = m;
    if (o < 640) { const v4u x = *(const GAS v4u*)(p.mem0 + (o & 7) * DM + (o >> 3) * 8); val = v4u{unsent(x[0]), unsent(x[1]), unsent(x[2]), unsent(x[3])}; }
    __builtin_amdgcn_raw_buffer_store_b128(val, r, o < 640 || o >= 1024 ? h0 + lmap(j * 16) : OOB, 0, 16);
  }
  __builtin_amdgcn_raw_buffer_store_b128(m, r, i < 512 ? p.co + C_R * 4 + i * 16 : i < 512 + TOPK * FF / 4 ? ACT_OFF + (i - 512) * 16 : OOB, 0, 16);
#pragma unroll 1
  for (int j = i; j < (NSEG - OSPLIT) * 32 * 80; j += 8192) __builtin_amdgcn_raw_buffer_store_b128(m, r, X1_OFF + (OSPLIT * 32 + j / 80) * 2560 + (j % 80) * 16, 0, 16);
}
__device__ __forceinline__ void vo_role(const v32u (&wa)[7], const v16u& wa7, const Params& p, int h, int lane, int wave) {
  const int g = lane >> 4, n = lane & 15, q = (lane >> 2) & 3, pp = lane & 3;
  first_markers(p, h * 256 + wave * 64 + lane);
  v16u vc; v4u sr;
  vc[0] = lane * 16;
  vc[1] = h * 32768 + wave * 2048 + lane * 16;
  vc[2] = h * 32768 + 16384 + wave * 1024 + lane * 16;
  vc[3] = (20 * wave + (n >> 3)) * 32768 + lmap(h * 512) + g * 128 + (n & 7) * 16;
  vc[4] = side_a(p, slot_blk(lane));
  vc[5] = wave == 3 ? side_a(p, slot_blk(lane) + 1) - (1 << 21) : vc[4];
  vc[6] = vc[4] ^ 8192;
  vc[7] = lane == 0 ? flag_off(p, h) + (8 + wave) * 4 : OOB;
  vc[8] = 0;
  vc[9] = p.co + (C_VF + h * 32 + pp) * 4;
#pragma unroll
  for (int k = 10; k < 16; ++k) vc[k] = 0;
  set_rsrc(sr, 0, p.ws, WS_BYTES);
  v4u sc = {0u, 64u, (unsigned)wave, 0u};
  pin(sc); pin(sr);
  asm volatile(VO_ASM : "+{s[52:55]}"(sc), "+{v[0:15]}"(vc), "+{s[36:39]}"(sr) : W240_IN(wa) : "memory", "vcc", "scc", VO_CLOBBERS);
}

typedef float f2 __attribute__((ext_vector_type(2)));
__device__ __forceinline__ f2 up2(unsigned w) { return f2{lo2f(w), hi2f(w)}; }
constexpr int L_PAR = 123904;
__device__ __forceinline__ void row_params(v3u par, int wave) {
  const Params p = load_params(par);
  const int tid = wave * 64 + fresh_lane();
  const rsrc_t rm = mkrsrc(p.ws + MODB_OFF + 2560, 5120), rr = mkrsrc(p.wr, 1280);
  wait_ge<8>((gu32*)(p.ws + p.co), C_MOD + (tid & 1) * 32, 40);
  unsigned w[5];
#pragma unroll
  for (int k = 0; k < 5; ++k) w[k] = __builtin_amdgcn_raw_buffer_load_b32(rm, (k * 256 + tid) * 4, 0, 16);
  const v2u r = __builtin_amdgcn_raw_buffer_load_b64(rr, tid * 8, 0, 0);
  BAR();
#pragma unroll
  for (int k = 0; k < 5; ++k) *(LAS f2*)(L_PAR + (k * 256 + tid) * 8) = up2(w[k]);
  *(LAS v2u*)(L_PAR + 10240 + tid * 8) = r;
  LGKM0(); BAR();
}
__device__ __forceinline__ v3u o_start(int id, int lane, int wave) {
  const auto* k = fresh_params();
  const v3u par = wait_table(k->ws, __hip_atomic_load((gu32*)(k->ws + EPOCH_OFF), RLX_AGENT), lane);
  if (id == 15) return par;
  if (wave < 2) {
    const unsigned x = __builtin_amdgcn_raw_buffer_load_b32(mkrsrc(wave ? k->out : k->x, S * DM * 2), lane * 65536 + id * 128, 0, 0);
    asm volatile("" ::"v"(x));
  }
  row_params(par, wave);
  return par;
}
__device__ __forceinline__ float sum16(float v) {
  v += u2f(__builtin_amdgcn_update_dpp(0, (int)f2u(v), 0xb1, 0xf, 0xf, true)); v += u2f(__builtin_amdgcn_update_dpp(0, (int)f2u(v), 0x4e, 0xf, 0xf, true));
  v += u2f(__builtin_amdgcn_update_dpp(0, (int)f2u(v), 0x124, 0xf, 0xf, true));
  return v + ror8f(v);
}
struct RowIn { v4u x[5], y[5]; };
__device__ __forceinline__ void rows_load(RowIn& r, int row0) {
  const auto* k = fresh_params();
  const int lane = fresh_lane(), row = row0 + (lane >> 4), co = (lane & 15) * 16;
  const rsrc_t rx = mkrsrc(k->x, S * DM * 2), rT = mkrsrc(k->ws + X1_OFF, 2 * HF_BYTES);
#pragma unroll
  for (int i = 0; i < 5; ++i) { r.y[i] = ld16s(rT, row * 2560 + co + i * 256, 0); r.x[i] = __builtin_amdgcn_raw_buffer_load_b128(rx, row * 1280 + co + i * 256, 0, 0); }
}
__device__ __forceinline__ unsigned rows_do(const RowIn& r, int row0) {
  const auto* kp = fresh_params();
  const int lane = fresh_lane(), row = row0 + (lane >> 4), co = (lane & 15) * 16;
  const rsrc_t rT = mkrsrc(kp->ws + X1_OFF, (unsigned)(WS_END - X1_OFF)), rO = mkrsrc(kp->out, S * DM * 2);
  auto par = [&](int a, int i, int k) { const f4 v = *(LAS f4*)(L_PAR + a * 2560 + co * 2 + i * 512 + (k >> 1) * 16); return k & 1 ? f2{v[2], v[3]} : f2{v[0], v[1]}; };
  f2 x1[20], s = {}, q = {};
  float rs = 0.f;
#pragma unroll
  for (int i = 0; i < 5; ++i) {
    v4u w;
#pragma unroll
    for (int k = 0; k < 4; ++k) {
      const f2 t = __builtin_elementwise_fma(par(0, i, k), up2(r.y[i][k]), up2(r.x[i][k]));
      w[k] = pack2(t[0], t[1]); x1[4 * i + k] = up2(w[k]); s += x1[4 * i + k];
    }
    __builtin_amdgcn_raw_buffer_store_b128(w, rT, row * 2560 + co + i * 256, 0, 16);
  }
  const float mean = sum16(s[0] + s[1]) * (1.f / DM);
#pragma unroll
  for (int i = 0; i < 20; ++i) { const f2 d = x1[i] - mean; q = __builtin_elementwise_fma(d, d, q); }
  const float isd = __builtin_amdgcn_rcpf(rbf(__builtin_amdgcn_sqrtf(rbf(rbf(sum16(q[0] + q[1]) * (1.f / DM)) + 1e-5f)))), mi = -rbf(mean) * isd;
#pragma unroll
  for (int i = 0; i < 5; ++i) {
    const v4u wr = lds128(L_PAR + 10240 + co + i * 256);
    v4u h, o;
#pragma unroll
    for (int k = 0; k < 4; ++k) {
      const f2 hh = __builtin_elementwise_fma(__builtin_elementwise_fma(x1[4 * i + k], f2{isd, isd}, f2{mi, mi}), par(2, i, k), par(1, i, k));
      const f2 oo = __builtin_elementwise_fma(par(3, i, k), hh, x1[4 * i + k]);
      const unsigned hb = pack2(hh[0], hh[1]), wk = wr[k];
      h[k] = hb; o[k] = pack2(oo[0], oo[1]);
      rs = __builtin_amdgcn_fdot2_f32_bf16(__builtin_bit_cast(bf2, hb), __builtin_bit_cast(bf2, wk), rs, false);
    }
    __builtin_amdgcn_raw_buffer_store_b128(h, rT, row * 2560 + 1280 + co + i * 256, 0, 16);
    __builtin_amdgcn_raw_buffer_store_b128(o, rO, row * 1280 + co + i * 256, 0, 16);
  }
  return f2u(rbf(sum16(rs)));
}
__device__ __forceinline__ void row_one(v3u par, int row, unsigned xl) {
  const Params p = load_params(par);
  const int lane = fresh_lane(), ln = min(lane, 39), vo = lane < 40 ? lane * 16 : OOB;
  const rsrc_t rT = mkrsrc(p.ws + X1_OFF, (unsigned)(WS_END - X1_OFF)), rO = mkrsrc(p.out, S * DM * 2);
  f4 pr[4][4]; v4u wr[2], x[2], y[2];
#pragma unroll
  for (int i = 0; i < 2; ++i) {
#pragma unroll
    for (int a = 0; a < 8; ++a) pr[a >> 1][2 * i + (a & 1)] = *(LAS f4*)(L_PAR + (a >> 1) * 2560 + i * 1280 + ln * 32 + (a & 1) * 16);
    wr[i] = lds128(L_PAR + 10240 + i * 640 + ln * 16); x[i] = lds128(xl + i * 1024 + lane * 16);
  }
  auto par2 = [&](int a, int k) { const f4 v = pr[a][k >> 1]; return k & 1 ? f2{v[2], v[3]} : f2{v[0], v[1]}; };
#pragma unroll 1
  for (int spin = 0;; ++spin) {
    asm volatile("" ::: "memory");
#pragma unroll
    for (int i = 0; i < 2; ++i) y[i] = ld16s(rT, vo + row * 2560 + i * 640, 0);
    if (__builtin_amdgcn_ballot_w64(max(max(max(y[0][0], y[0][1]), max(y[0][2], y[0][3])), max(max(y[1][0], y[1][1]), max(y[1][2], y[1][3]))) == ~0u) == 0) break;
    if (spin > SPIN_CAP) { __hip_atomic_store((gu32*)(p.ws + p.co) + C_ERR, 1u, RLX_AGENT); break; }
    __builtin_amdgcn_s_sleep(1);
  }
  f2 x1[8], s = {}, q = {};
#pragma unroll
  for (int i = 0; i < 2; ++i) {
    v4u w;
#pragma unroll
    for (int k = 0; k < 4; ++k) {
      const f2 t = __builtin_elementwise_fma(par2(0, 4 * i + k), up2(y[i][k]), up2(x[i][k]));
      w[k] = pack2(t[0], t[1]); x1[4 * i + k] = up2(w[k]); s += x1[4 * i + k];
    }
    __builtin_amdgcn_raw_buffer_store_b128(w, rT, vo + row * 2560 + i * 640, 0, 16);
  }
  const float mean = wave_sum(lane < 40 ? s[0] + s[1] : 0.f) * (1.f / DM);
#pragma unroll
  for (int i = 0; i < 8; ++i) { const f2 d = x1[i] - mean; q = __builtin_elementwise_fma(d, d, q); }
  const float isd = __builtin_amdgcn_rcpf(rbf(__builtin_amdgcn_sqrtf(rbf(rbf(wave_sum(lane < 40 ? q[0] + q[1] : 0.f) * (1.f / DM)) + 1e-5f)))), mi = -rbf(mean) * isd;
  float rs = 0.f;
#pragma unroll
  for (int i = 0; i < 2; ++i) {
    v4u h, o;
#pragma unroll
    for (int k = 0; k < 4; ++k) {
      const f2 hh = __builtin_elementwise_fma(__builtin_elementwise_fma(x1[4 * i + k], f2{isd, isd}, f2{mi, mi}), par2(2, 4 * i + k), par2(1, 4 * i + k));
      const f2 oo = __builtin_elementwise_fma(par2(3, 4 * i + k), hh, x1[4 * i + k]);
      const unsigned hb = pack2(hh[0], hh[1]), wk = wr[i][k];
      h[k] = hb; o[k] = pack2(oo[0], oo[1]);
      rs = __builtin_amdgcn_fdot2_f32_bf16(__builtin_bit_cast(bf2, hb), __builtin_bit_cast(bf2, wk), rs, false);
    }
    __builtin_amdgcn_raw_buffer_store_b128(h, rT, vo + row * 2560 + 1280 + i * 640, 0, 16);
    __builtin_amdgcn_raw_buffer_store_b128(o, rO, vo + row * 1280 + i * 640, 0, 16);
  }
  const unsigned sc = f2u(rbf(wave_sum(lane < 40 ? rs : 0.f)));
  VMCNT(0);
  if (lane == 0) __hip_atomic_store((gu32*)(p.ws + p.co) + C_R + row, sc, RLX_AGENT);
}
#define OGATE min(seg + 1, 31 + seg / 2)
#define OSTAG 140
#define OPACE 0
template <int NT>
__device__ __forceinline__ void o_unit(const Params& p, int seg, int t0, int rep, int lane, int wave) {
  gu32* ctrl = (gu32*)(p.ws + p.co);
  constexpr int NB = NT < 4 ? NT : 4;
  const rsrc_t rH = mkrsrc(p.ws, WS_BYTES);
  const int g = lane >> 4, n = lane & 15;
  const int sa = rfl(side_a(p, slot_blk(seg))), sb = rfl(side_a(p, slot_blk(seg) + 1)), sp = rfl(pt_off(p, seg));
  if (wave == 0) {
    const long w = ((long)flag_off(p, lane & 31) - p.co) / 4 + 8 + (lane >> 5) * 2;
    wait_ge<16>(ctrl, C_G + rep * 32, OGATE);
    if (OGATE < seg + 1) { wait_ge<32>(ctrl, w, seg + 1); wait_ge<32>(ctrl, w + 1, seg + 1); }
  }
  BAR();
  const int vy = (n >> 3) * 32768 + lmap(wave * 4096) + g * 128 + (n & 7) * 16, vz = (n >> 3) * 4096 + wave * 1024 + g * 128 + (n & 7) * 16;
  v4u y[NB][8], z[NB][2], pt[2][8], pm[2][2];
  auto ldy = [&](int t) {
    const int T = t0 + t, so = T < 32 ? sa + T * 65536 : sb + (T - 32) * 65536;
#pragma unroll
    for (int j = 0; j < 8; ++j) y[t % NB][j] = ld16s(rH, vy + j * 512, so);
  };
  auto ldz = [&](int t) {
#pragma unroll
    for (int j = 0; j < 2; ++j) z[t % NB][j] = ld16s(rH, vz + j * 512, (sb ^ 8192) + 524288 + seg / RING * 655360 + (t0 + t) * 16384);
  };
#pragma unroll
  for (int t = 0; t < NB; ++t) { ldy(t); __builtin_amdgcn_sched_barrier(0); if (seg < 60) __builtin_amdgcn_s_sleep(OPACE); }
  __builtin_amdgcn_sched_barrier(0);
  if (wave == 0) {
    if (seg < NSEG - 2) wait_ge<4>(ctrl, C_G + rep * 32, seg + 3);
    else wait_ge<2>(ctrl, C_PT + (lane & 15) * 32, lane & 8 ? 10 : 8);
  }
  BAR();
#pragma unroll
  for (int j = 0; j < 16; ++j) pt[j & 1][j >> 1] = ld16s(rH, lane * 16 + (j & 3) * 1024, sp + wave * 32768 + (j >> 3) * 16384 + ((j >> 2) & 1) * 4096);
#pragma unroll
  for (int j = 0; j < 4; ++j) pm[j & 1][j >> 1] = ld16s(rH, g * 512 + n * 16 + (j & 1) * 256 + (j >> 1) * 2048, sp + lmap(65536 + wave * 4096));
#pragma unroll
  for (int t = 0; t < NB; ++t) ldz(t);
  __builtin_amdgcn_sched_barrier(0);
#pragma unroll
  for (int t = 0; t < NT; ++t) {
    f4 acc[2] = {};
#pragma unroll
    for (int j = 0; j < 8; ++j) { acc[0] = mfma32(y[t % NB][j], pt[0][j], acc[0]); acc[1] = mfma32(y[t % NB][j], pt[1][j], acc[1]); }
#pragma unroll
    for (int j = 0; j < 2; ++j) { acc[0] = mfma32(z[t % NB][j], pm[0][j], acc[0]); acc[1] = mfma32(z[t % NB][j], pm[1][j], acc[1]); }
    __builtin_amdgcn_sched_barrier(0);
    if (t + NB < NT) { ldy(t + NB); ldz(t + NB); }
#pragma unroll
    for (int mt = 0; mt < 2; ++mt) *(LAS f4*)(wave * (32 * (NT * 64 + 16)) + (mt * 16 + n) * (NT * 64 + 16) + t * 64 + g * 16) = acc[mt];
    __builtin_amdgcn_sched_barrier(0);
  }
  LGKM0(); BAR();
  const rsrc_t rT = mkrsrc(p.ws + X1_OFF, (unsigned)(WS_END - X1_OFF));
#pragma unroll
  for (int i = 0; i < (NT + 3) / 4; ++i) {
    const int it = i * 256 + wave * 64 + lane, tok = (it / (2 * NT)) & 31, ch = it % (2 * NT);
    f4 a = {}, b = {};
#pragma unroll
    for (int w = 0; w < 4; ++w) { const unsigned ad = w * (32 * (NT * 64 + 16)) + tok * (NT * 64 + 16) + ch * 32; a += *(LAS f4*)ad; b += *(LAS f4*)(ad + 16); }
    __builtin_amdgcn_raw_buffer_store_b128(v4u{unsent(pack2(a[0], a[1])), unsent(pack2(a[2], a[3])), unsent(pack2(b[0], b[1])), unsent(pack2(b[2], b[3]))}, rT,
                                           it < 64 * NT ? (seg * 32 + tok) * 2560 + t0 * 32 + ch * 16 : OOB, 0, 16);
  }
}
static_assert(OSPLIT <= 2 * RING && OSPLIT >= NSEG - RING, "Z offset, arrival counts");
__device__ __forceinline__ void o_stream(v3u par, int id, int nb, int rep, int wave) {
  const Params p = load_params(par);
  gu32* ctrl = (gu32*)(p.ws + p.co);
  const int lane = fresh_lane(), g = lane >> 4, n = lane & 15;
  const rsrc_t rH = mkrsrc(p.ws, WS_BYTES);
  const int ta = side_a(p, slot_blk(lane)), tb = side_a(p, slot_blk(lane) + 1), tp = pt_off(p, lane);
  const int vy = (n >> 3) * 32768 + g * 128 + (n & 7) * 16, vz = (n >> 3) * 4096 + g * 128 + (n & 7) * 16;
  v4u y[5][8];
  auto ld = [&](int u, int it, int t) {
    const int sa = __builtin_amdgcn_readlane(ta, u >> 1), sb = __builtin_amdgcn_readlane(tb, u >> 1), T = (u & 1) * 20 + wave * 5 + t;
    const int so = it < 4 ? (T < 32 ? sa : sb - (1 << 21)) + T * 65536 : (sb ^ 8192) + (u >= 2 * RING ? 1179648 : 524288) + T * 16384;
    const int vo = it < 4 ? vy + lmap(((it + wave) & 3) * 4096) : vz;
#pragma unroll
    for (int j = 0; j < 8; ++j) y[t][j] = ld16s(rH, vo + j * 512, so);
  };
  if (wave == 0) wait_ge<8>(ctrl, C_G + rep * 32, (id >> 1) + 1);
  BAR();
#pragma unroll
  for (int t = 0; t < 5; ++t) ld(id, 0, t);
  int up = -1, srow = OOB;
  unsigned score = 0;
  RowIn ri;
  auto put_score = [&]() { __builtin_amdgcn_raw_buffer_store_b32(score, rH, n == 0 ? srow : OOB, 0, 16); };
#pragma unroll 1
  for (int u = id; u < 2 * OSPLIT; u += nb) {
    const int seg = u >> 1, un = min(u + nb, 2 * OSPLIT - 1);
    TS(id == 3 && wave == 0 && lane == 0 && u == id + 2 * nb, 40)
    if (wave == 0) wait_ge<8>(ctrl, C_G + rep * 32, seg + 3);
    BAR();
    TS(id == 3 && wave == 0 && lane == 0 && u == id + 2 * nb, 41)
    const int sp = __builtin_amdgcn_readlane(tp, seg);
#pragma unroll 1
    for (int k = 0; k < 20; ++k) dma16_sc1(rH, lane * 16, sp + lmap((wave * 20 + k) * 1024), (wave * 20 + k) * 1024);
    VMCNT(0); BAR();
    put_score();
    if (up >= 0 && wave == 1 && lane == 0) __hip_atomic_fetch_add(ctrl + C_AF + (up >> 1) * 32, 1u, RLX_AGENT);
    TS(id == 3 && wave == 0 && lane == 0 && u == id + 2 * nb, 42)
    f4 acc[2][5] = {};
#pragma unroll
    for (int it = 0; it < 5; ++it) {
      if (it == 4) {
        if (wave == 0) wait_ge<8>(ctrl, lane & 1 ? C_G + rep * 32 : C_AF + (max(up, 0) >> 1) * 32, lane & 1 ? (un >> 1) + 1 : up >= 0 ? 2 : 0);
        BAR();
        rows_load(ri, up * 16 + wave * 4);
      }
      v4u pf[2][8];
      const unsigned lp = it < 4 ? ((it + wave) & 3) * 16384 + lane * 16 : 65536 + g * 512 + n * 16;
#pragma unroll
      for (int j = 0; j < 16; ++j) pf[j & 1][j >> 1] = lds128(lp + (j >> 1) * 2048 + (j & 1) * (it < 4 ? 1024 : 256));
#pragma unroll
      for (int t = 0; t < 5; ++t) {
#pragma unroll
        for (int j = 0; j < 8; ++j) { acc[0][t] = mfma32(y[t][j], pf[0][j], acc[0][t]); acc[1][t] = mfma32(y[t][j], pf[1][j], acc[1][t]); }
        __builtin_amdgcn_sched_barrier(0);
        if (it < 4) ld(u, it + 1, t); else ld(un, 0, t);
        __builtin_amdgcn_sched_barrier(0);
      }
    }
#pragma unroll
    for (int k = 0; k < 10; ++k) {
      const f4 a = acc[k & 1][k >> 1];
      *(LAS v2u*)(81920 + ((k & 1) * 16 + n) * 656 + (wave * 5 + (k >> 1)) * 32 + g * 8) = v2u{pack2(a[0], a[1]), pack2(a[2], a[3])};
    }
    LGKM0(); BAR();
#pragma unroll
    for (int k = 0; k < 5; ++k) {
      const int c = k * 64 + lane, r = wave * 8 + c / 40, o = (c % 40) * 16;
      __builtin_amdgcn_raw_buffer_store_b128(lds128(81920 + r * 656 + o), rH, X1_OFF + (seg * 32 + r) * 2560 + (u & 1) * 640 + o, 0, 16);
    }
    __builtin_amdgcn_sched_barrier(0);
    TS(id == 3 && wave == 0 && lane == 0 && u == id + 2 * nb, 43)
    if (up >= 0) { score = rows_do(ri, up * 16 + wave * 4); srow = p.co + (C_R + up * 16 + wave * 4 + g) * 4; }
    TS(id == 3 && wave == 0 && lane == 0 && u == id + 2 * nb, 44)
    TS(up >= 0 && wave == 0 && lane == 0, 704 + (up >> 1))
    up = u;
  }
  VMCNT(0); BAR();
  put_score();
  if (wave == 0) {
    if (lane == 0) __hip_atomic_fetch_add(ctrl + C_AF + (up >> 1) * 32, 1u, RLX_AGENT);
    wait_ge<4>(ctrl, C_AF + (up >> 1) * 32, 2);
  }
  BAR();
  rows_load(ri, up * 16 + wave * 4);
  score = rows_do(ri, up * 16 + wave * 4); srow = p.co + (C_R + up * 16 + wave * 4 + g) * 4;
  VMCNT(0);
  put_score();
  TS(wave == 0 && lane == 0, 704 + (up >> 1))
}
__device__ __forceinline__ void o_role(v3u par, int u, int rep, int wave) {
  pause(u / 10 * OSTAG + ODELAY);
#pragma unroll 1
  for (int seg = OSPLIT + u / 10; seg < NSEG; seg += 8) {
    const Params p = load_params(par);
    const int lane = fresh_lane(), row = seg * 32 + (u % 10) * 4 + wave;
    o_unit<4>(p, seg, (u % 10) * 4, rep, lane, wave);
    TS(u % 10 == 0 && wave == 0 && lane == 0, 1500 + seg)
    if (seg < NSEG - 4) pause(100);
    if (u % 10 < 8) row_one(par, row, L_XR + (wave * 4 + (seg >= OSPLIT + 8) * 2) * 1024);
    TS(u % 10 == 0 && wave == 0 && lane == 0, 704 + seg)
  }
}

__device__ __forceinline__ void s_role(const Params& p, int h, int rep, int lane, int wave) {
  constexpr int L_K = 24576, L_D = 30720, L_Q = 57344, L_MEM = 63488, L_RES = 73728;
  const int g = lane >> 4, n = lane & 15, q = (lane >> 2) & 3, pp = lane & 3, j0 = 4 * (g & 1) + 2 * (g >> 1);
  v32u vc; v12u sr; v16u sc;
  vc[0] = n < 8 ? 0x4444 : 0xEEEE;
  vc[1] = lane * 16;
  vc[2] = L_MEM + lane * 16;
  vc[3] = L_RES + (wave ^ wave >> 1) * 22528 + lane * 16;
  vc[4] = 0;
  vc[5] = L_Q + g * 1024 + q * 128 + (4 * g + q) * 8 + pp * 2;
  vc[6] = L_K + g * 128 + (n ^ (8 * (g >> 1))) * 8;
  vc[7] = lane < 63 ? side_a(p, slot_blk(lane + 1) + 1) : OOB;
  vc[8] = lmap(KQ_OFF + h * 24576) + lane * 16;
  vc[9] = L_K + pp * 128 + ((4 * g + q) ^ (8 * (pp >> 1))) * 8;
  vc[10] = 0;
  vc[11] = hand_off(p, lane) + rep * 2 * H_COPY;
  vc[12] = L_D + lane * 4;
  const int kt = wave == 2, tok = (g * 16 + 8 * kt + 4 * (q >> 1) + pp) * 16;
  if (wave == 1 || wave == 2) {
    vc[13] = n - 4 * g;
    vc[14] = lane == 0 ? p.co + (C_PT + (h & 7) * 32) * 4 : OOB;
    vc[15] = pt_off(p, lane);
    vc[16] = lmap(h * 2048 + kt * 1024 + lane * 16);
    vc[17] = lmap(65536 + h * 512 + (16 * kt + n) * 16 + j0 * 2);
    vc[18] = lane * 16;
  } else {
    vc[13] = lmap(H_P) + (n < 8 ? h * 128 + n * 16 + j0 * 2 : 4096 + (h >> 3) * 1024 + ((2 * (((h >> 1) ^ h) & 1) + (g & 1)) * 16 + (h & 1) * 8 + n - 8) * 16 + ((h >> 2) & 1) * 8 + (g >> 1) * 4);
    vc[14] = j0 == (n & 7) ? 0x3f800000u : 0u;
    vc[15] = j0 + 1 == (n & 7) ? 0x3f800000u : 0u;
    vc[16] = (h & 7) == 0 && lane < 8 ? p.co + (C_G0 + rep * 32) * 4 + lane * 16 : OOB; vc[17] = 0;
    vc[18] = flag_off(p, h) + (lane % 12) * 4;
  }
  if (wave == 3) { vc[13] = lane < 4 ? lane * 2 * H_COPY + lmap(H_NOTE + rep * 128) : OOB; vc[14] = look_off(lane); }
  vc[19] = sum_off(lane);
  vc[20] = (q & 1) * 1024 + tok;
  vc[21] = 16384 + (q & 1) * 8 + tok;
  vc[22] = lmap(H_E + h * 512 + (2 * kt + (g >> 1)) * 128 + (n & 7) * 16 + (g & 1) * 8 + (n >> 3) * 4);
  vc[23] = 0;
#pragma unroll
  for (int k = 0; k < 3; ++k) { const int c = k * 64 + lane; vc[24 + k] = ((16 * wave + c / 24) * HD + h * DH) * 2 + ((c % 24) ^ (c / 24 & 4)) * 16 - k * 1024; }
  vc[27] = (16 * (g >> 1) + 8 * (q >> 1) + 4 * (g & 1) + (q & 1)) * 384 + (((32 * wave + 8 * pp) * 2) ^ (64 * (g & 1)));
  vc[28] = lane == 0 ? p.co + (C_ANCH + 128 + rep * 32) * 4 : OOB;
  vc[29] = p.co + (C_ANCH + rep * 32 + lane % 3) * 4;
  vc[30] = (16 * (g >> 1) + 8 * (q >> 1) + 4 * (g & 1) + (q & 1)) * 384 + (((128 + 16 * wave + 4 * pp) * 2) ^ (64 * (g & 1))); vc[31] = 0;
  set_rsrc(sr, 0, p.ws, WS_BYTES); set_rsrc(sr, 1, p.wq, DM * HD * 2); set_rsrc(sr, 2, p.wk, DM * HD * 2);
#pragma unroll
  for (int i = 4; i < 16; ++i) sc[i] = 0;
  sc[0] = wave; sc[1] = p.dL + p.dM; sc[2] = p.dS; sc[3] = rfl(side_a(p, 1)); sc[9] = p.dL * 23 / 4 + KQ_DELAY - 250; sc[10] = rfl(rep * 2 * H_COPY);
#pragma unroll
  for (int k = 1; k < 4; ++k) sc[5 + k] = rfl((((rep + k) & 3) - rep) * 2 * H_COPY);
  pin(sc); pin(sr);
  asm volatile(SROLE_ASM : "+{s[52:67]}"(sc), "+{v[0:31]}"(vc), "+{s[36:47]}"(sr) :: "memory", "vcc", "scc", SROLE_CLOBBERS);
  gu32* ctrl = (gu32*)(fresh_params()->ws + CTRL_OFF);
  if (fresh_lane() == 0) {
    if (sc[1] > 20000u || sc[4] > 20000u) { __hip_atomic_store(ctrl + C_ERR, 1u, RLX_AGENT); __hip_atomic_store(ctrl + C_ERR + 2048, 1u, RLX_AGENT); }
  }
}

__device__ __forceinline__ void start_chain(const Params& p, int lane
) {
  gu32* ctrl = (gu32*)(p.ws + p.co);
  const int ho = hand_off(p, lane);
  const rsrc_t rH = mkrsrc(p.ws, WS_BYTES);
  wait_ge<1>(ctrl, C_ANCH + 128 + (lane & 3) * 32, 28);
  unsigned A = now_ticks() * 4 + p.lead;
  int x = XSTART, lev0 = 0;
  unsigned due = 0;
  float inv = 1.f / FSTART;
  auto plan = [&](int s) {
    const float sl = inv - 1.f / 23.f;
    const int lev = lev0 + (s >= 50 && s < 61 ? BUSY : 0);
    const int dE = p.dE + lev + (int)(4.f * SLOPE_E * sl), dL = p.dL + lev + (int)(4.f * SLOPE_L * sl), dM = p.dM + lev / 2 + (int)(4.f * SLOPE_M * sl);
    const v4u v = {A | (s == 0), (A + x + dE) | (s == 0), (A + x + dL) | (s == 0), 0u};
    const int hs = __builtin_amdgcn_readlane(ho, (s - 1) & 63);
    __builtin_amdgcn_raw_buffer_store_b128(v, rH, lane >= 32 ? OOB : (s ? hs + (lane >> 3) * 2 * H_COPY + lmap(H_PLAN) : p.co + (C_ANCH + (lane >> 3) * 32) * 4) + (lane & 7) * 16, 0, 16);
    if (lane == s) due = A + x + dL;
    A += dL + dM + 2 * x; x >>= 1;
  };
#pragma unroll 1
  for (int s = 0; s <= HORIZON; ++s) plan(s);
  unsigned e1 = 0, a1 = 0, e2 = 0, a2 = 0;
#pragma unroll 1
  for (int g = 1, n1 = 0, n2 = 0, n3 = 0, n4 = 0, calm = 0; g <= NSEG; ++g) {
    v4u w;
    {
      const unsigned t = __builtin_amdgcn_readlane(due, g - 1) + n1 * p.dS + GLOOK;
#pragma unroll 1
      while ((unsigned)(t - now_ticks() * 4) - 1u < 4000u) __builtin_amdgcn_s_sleep(1);
    }
#pragma unroll 1
    for (int spin = 0;; ++spin) {
      asm volatile("" ::: "memory");
      w = ld16s(rH, p.co + (C_G0 + (lane & 3) * 32) * 4, 0);
      if (__builtin_amdgcn_ballot_w64((int)w[0] < g << 16) == 0) break;
      if (spin > SPIN_CAP) { __hip_atomic_store(ctrl + C_ERR, 1u, RLX_AGENT); break; }
      __builtin_amdgcn_s_sleep(GPOLL);
    }
    if (lane < 16) __hip_atomic_store(ctrl + C_G + lane * 32, (unsigned)g, RLX_AGENT);
    const int n = rfl(w[0]) & 0xffff;
    if (n > n1 && n1 > n2 && n2 > n3 && n3 > n4 && g >= calm && g < NSEG) {
      const int hs = __builtin_amdgcn_readlane(ho, g);
      if (lane < 4) __hip_atomic_store(ctrl + ((long)hs + lane * 2 * H_COPY + lmap(H_PLAN) - p.co) / 4 + 3, (unsigned)(n + MEET), RLX_AGENT);
      calm = g + 4;
    }
    if (n > n1 && n1 == n2) lev0 += LEVUP;
    if (g > 2 && n == n2) {
      float r = 0.25f * (float)(w[2] - a2) / (float)(w[1] - e2);
      r = fmaxf(r, u2f(__builtin_amdgcn_update_dpp(0, (int)f2u(r), 0xb1, 0xf, 0xf, true))); r = fmaxf(r, u2f(__builtin_amdgcn_update_dpp(0, (int)f2u(r), 0x4e, 0xf, 0xf, true)));
      r = fminf(fmaxf(r, 1.f / 25.f), 1.f / 15.f);
      inv = fmaxf(r, inv + (r - inv) * (g < 6 ? 0.5f : FDECAY));
    }
    n4 = n3; n3 = n2; n2 = n1; n1 = n; e2 = e1; a2 = a1; e1 = w[1]; a1 = w[2];
    if (g + HORIZON < NSEG) plan(g + HORIZON);
  }
}

__global__ void __launch_bounds__(256, 1) k_main(Params p0) {
  extern __shared__ __attribute__((aligned(16))) unsigned char smem[];
  int job = (blockIdx.x & 7) * 32 + (blockIdx.x >> 3) | rfl(threadIdx.x >> 6) << 8;
  {
    unsigned long long pc;
    asm volatile("s_getpc_b64 s[40:41]\n\ts_add_u32 s40, s40, _Z6k_main6Params@rel32@lo+4\n\ts_addc_u32 s41, s41, _Z6k_main6Params@rel32@hi+12" : "={s[40:41]}"(pc) :: "scc");
    const int lane = fresh_lane(), wv = job >> 8;
    const rsrc_t r = wv == 3 ? mkrsrc((const void*)pc, CODE_BYTES) : mkrsrc(p0.ws, WS_BYTES);
    const int vo = wv == 3 ? (lane * 32 + (job & 31)) * 128 : wv < 2 ? ((wv * 64 + lane) << 21) + 0x1FF000 + (job & 31) * 128 : OOB;
    asm volatile("s_mov_b32 m0, %0\n\ts_nop 3\n\tbuffer_load_dword %1, %2, 0 offen lds" ::"s"(MAIN_LDS - 256), "v"(vo), "s"(r) : "memory");
    if (wv < 3 && (job & 0x83) != 0x81) ffn_warm(wv, blockIdx.x, lane);
  }
  v3u par;
#define JOB() job = rfl(job); asm volatile("" : "+s"(job)); const int wv = job >> 8, jb = job & 255, jx = jb >> 5, jt = jb & 31, type = jb & 3, head = (jb - 128) >> 2; (void)jx; (void)jt; (void)type; (void)head;
#define TSJ(i) { JOB() const Params p = load_params(par); TS(wv == 0 && fresh_lane() == 0, (i) + jb) }
  {
    JOB()
    if (jb >= 128) {
      v32u wa[7]; v16u wa7;
      par = token_start(wa, wa7, type, head, fresh_lane(), wv);
      pin(par);
      if (type < 3) proj_role(wa, wa7, load_params(par), type, head, fresh_lane(), wv);
      else { vo_role(wa, wa7, load_params(par), head, fresh_lane(), wv); }
    } else {
      par = v3u{0u, 0u, 0u};
      if (jt < 8) s_role(load_params(par), jx * 8 + jt, jx, fresh_lane(), wv);
      else {
        u_role(load_params(par), ((jx + (jt - 8) / 3) & 3) * 8 + (jt - 8) / 3, (jt - 8) % 3, jt < 28 ? jx * 20 + jt - 8 : -1, jx, fresh_lane(), wv);
        JOB()
        if (jt >= 28) par = o_start(jx * 4 + jt - 28, fresh_lane(), wv);
      }
    }
    pin(par);
    TSJ(256)
  }
  JOB()
  if (jb >= 128 || jt >= 28) {
    if (jb == 127) { if (wv == 0) start_chain(load_params(par), fresh_lane()
); else if (wv == 1) select_rows(par, fresh_lane()); }
    JOB()
    const int u = end_job(type, head);
    if (jb < 127 || (jb > 127 && u < 80)) {
      if (jb > 127) row_params(par, wv);
      if (jb < 127) o_stream(par, jx * 4 + jt - 28, 15, jb & 15, wv); else o_role(par, u, jb & 15, wv);
    } else if (jb > 127) ffn_role(load_params(par), u - 80, fresh_lane(), wv);
  }
  TSJ(1024)
}

#define SCHED_DE 368
#define SCHED_DL 436
#define SCHED_DM 174
#define SCHED_DZ 96
#define SCHED_DS 400
#define SCHED_LEAD 520

extern "C" void kernel_launch(void* const* d_in, const int* in_sizes, int n_in, void* d_out, int out_size, void* d_ws, size_t ws_size,
                              hipStream_t stream) {
  Params p;
  p.x = (const u16*)d_in[0]; p.mem0 = (const u16*)d_in[1]; p.wq = (const u16*)d_in[2]; p.wk = (const u16*)d_in[3];
  p.wv = (const u16*)d_in[4]; p.wo = (const u16*)d_in[5]; p.wr = (const u16*)d_in[8]; p.wg = (const u16*)d_in[9];
  p.wu = (const u16*)d_in[10]; p.wd = (const u16*)d_in[11]; p.c = (const u16*)d_in[12]; p.wmod = (const u16*)d_in[13];
  p.out = (u16*)d_out;
  p.ws = (unsigned char*)(((uintptr_t)d_ws + ws_size - WS_BYTES) & ~(uintptr_t)(2 * MB - 1));
  (void)hipFuncSetAttribute((const void*)k_main, hipFuncAttributeMaxDynamicSharedMemorySize, MAIN_LDS);
  p.dE = SCHED_DE; p.dL = SCHED_DL; p.dM = SCHED_DM; p.dZ = SCHED_DZ; p.dS = SCHED_DS; p.lead = SCHED_LEAD;
  p.m0 = 0; p.m1 = 0; p.m2 = 0; p.co = 0;
  k_main<<<256, 256, MAIN_LDS, stream>>>(p);
}
```
